```python
import torch

def _softmax(x, axis=-1):
    return torch.softmax(x, dim=axis)
def _gelu(x):
    return torch.nn.functional.gelu(x, approximate="tanh")
def _silu(x):
    return torch.nn.functional.silu(x)
def _sigmoid(x):
    return torch.sigmoid(x)
def _layernorm(x, axis=-1, eps=1e-5):
    m = x.mean(dim=axis, keepdim=True)
    v = x.var(dim=axis, keepdim=True, unbiased=False)
    return (x - m) / torch.sqrt(v + eps)
def _scatter_add(tgt, idx, src, axis=0):
    return tgt.clone().index_add_(axis, idx, src)
def _segment_sum(src, seg, n):
    out = torch.zeros((n,) + src.shape[1:], dtype=src.dtype)
    return out.index_add_(0, seg, src)
def _pad(x, pw):
    flat = tuple(v for pair in reversed(pw) for v in pair)
    return torch.nn.functional.pad(x, flat)

kernel_name = 'standalone__deformable_dcnv1__vision'

def setup_inputs():

    _w = (lambda a: (lambda t: t.to(torch.bfloat16) if t.is_floating_point() else t)(torch.as_tensor(a)))
    H = W = 224
    C = 96
    K = 3
    return {
        "x": _w((lambda *s: torch.randn(*s, dtype=torch.bfloat16) * 0.05)(1, H, W, C)),
        "offset": _w((lambda *s: torch.randn(*s, dtype=torch.bfloat16) * 0.05)(1, H, W, 2 * K * K) * 0.5),
        "w": _w((lambda *s: torch.randn(*s, dtype=torch.bfloat16) * 0.05)(K, K, C, C) * (K * K * C) ** -0.5),
    }

def reference(x, offset, w):
    B, H, W, C = x.shape
    K = 3
    hh = (lambda x, dt: x.to(dt))(torch.arange(H), x.dtype).reshape(1, H, 1)
    ww = (lambda x, dt: x.to(dt))(torch.arange(W), x.dtype).reshape(1, 1, W)
    xf = x.reshape(B, H * W, C)
    def _gather(yi, xi):
        lin = (yi * W + xi).reshape(B, H * W)
        idx = (lambda x, n, axis: torch.repeat_interleave(x, n, dim=axis))(lin[..., None], C, axis=-1)
        return (lambda x, i, axis: torch.take_along_dim(x, i, dim=axis))(xf, idx, axis=1).reshape(B, H, W, C)
    taps = []
    for ki in range(K):
        for kj in range(K):
            t = ki * K + kj
            dy = offset[..., 2 * t]
            dx = offset[..., 2 * t + 1]
            py = torch.clip(hh + (ki - 0.5 * (K - 1)) + dy, 0.0, H - 1.0)
            px = torch.clip(ww + (kj - 0.5 * (K - 1)) + dx, 0.0, W - 1.0)
            y0 = torch.floor(py)
            x0 = torch.floor(px)
            y1 = torch.clip(y0 + 1.0, 0.0, H - 1.0)
            x1 = torch.clip(x0 + 1.0, 0.0, W - 1.0)
            wy = (py - y0)[..., None]
            wx = (px - x0)[..., None]
            y0i = (lambda x, dt: x.to(dt))(y0, torch.int64)
            y1i = (lambda x, dt: x.to(dt))(y1, torch.int64)
            x0i = (lambda x, dt: x.to(dt))(x0, torch.int64)
            x1i = (lambda x, dt: x.to(dt))(x1, torch.int64)
            v = (1 - wy) * (1 - wx) * _gather(y0i, x0i) + (1 - wy) * wx * _gather(y0i, x1i) + wy * (1 - wx) * _gather(y1i, x0i) + wy * wx * _gather(y1i, x1i)
            taps.append(v)
    samp = torch.stack(taps, 3)
    return torch.einsum("bhwkc,kco->bhwo", samp, w.reshape(K * K, C, C))
```

```cpp
#define STAMPS
#define SPIN_A 0
#include <hip/hip_runtime.h>
#include <stdint.h>

#define IMG 224
#define CH 96
#define ROWB (IMG * CH * 2)
#define NTHREADS 512
#define WTAP 18432
#define TBL_OFF (2 * WTAP)
#define FLAG_OFF (TBL_OFF + 4 * 2304)
#define WIN_OFF (FLAG_OFF + 64)
#define PITCH 3968
#define LDS_BYTES 163840
#define XPOL ""
#define OPOL ""
#define PFC 4

typedef __attribute__((ext_vector_type(2))) __bf16 bf16x2;
typedef __attribute__((ext_vector_type(8))) __bf16 bf16x8;
typedef __attribute__((ext_vector_type(4))) float f32x4;
typedef __attribute__((ext_vector_type(4))) unsigned u32x4;
typedef __attribute__((ext_vector_type(8))) unsigned u32x8;
typedef __attribute__((address_space(1))) u32x4 glb_u32x4;
typedef __attribute__((ext_vector_type(2))) unsigned u32x2;
typedef __attribute__((ext_vector_type(4))) short s16x4;
typedef __attribute__((ext_vector_type(2))) unsigned short u16x2;
typedef __attribute__((address_space(3))) s16x4 lds_s16x4;
typedef __attribute__((address_space(3))) unsigned short lds_u16;
typedef __attribute__((address_space(3))) unsigned lds_u32;
typedef __attribute__((address_space(3))) u32x2 lds_u32x2;
typedef __attribute__((address_space(3))) u32x4 lds_u32x4;

#define COMPUTE_ASM_TEXT \
        "s_mov_b32 %[slot], -18432\n\t" \
        "s_mov_b32 %[cnt], 9\n\t" \
        "s_bitcmp1_b32 %[vm], 4\n\t" \
        "s_cselect_b64 vcc, -1, 0\n\t" \
        "v_mov_b32 v30, 46080\n\t" \
        "v_mov_b32 v116, 0\n\t" \
        "v_mov_b32 v117, 0\n\t" \
        "v_mov_b32 v118, 0\n\t" \
        "v_mov_b32 v119, 0\n\t" \
        "v_mov_b32 v120, 0\n\t" \
        "v_mov_b32 v121, 0\n\t" \
        "v_mov_b32 v122, 0\n\t" \
        "v_mov_b32 v123, 0\n\t" \
        "v_mov_b32 v124, 0\n\t" \
        "v_mov_b32 v125, 0\n\t" \
        "v_mov_b32 v126, 0\n\t" \
        "v_mov_b32 v127, 0\n\t" \
        "s_bitcmp1_b32 %[vm], 5\n\t" \
        "s_cbranch_scc1 .Lc_i1%=\n\t" \
        "v_mov_b32 v44, 0\n\t" \
        "v_mov_b32 v45, 0\n\t" \
        "v_mov_b32 v46, 0\n\t" \
        "v_mov_b32 v47, 0\n\t" \
        "v_mov_b32 v48, 0\n\t" \
        "v_mov_b32 v49, 0\n\t" \
        "v_mov_b32 v50, 0\n\t" \
        "v_mov_b32 v51, 0\n\t" \
        "v_mov_b32 v52, 0\n\t" \
        "v_mov_b32 v53, 0\n\t" \
        "v_mov_b32 v54, 0\n\t" \
        "v_mov_b32 v55, 0\n\t" \
        "v_mov_b32 v56, 0\n\t" \
        "v_mov_b32 v57, 0\n\t" \
        "v_mov_b32 v58, 0\n\t" \
        "v_mov_b32 v59, 0\n\t" \
        "v_mov_b32 v60, 0\n\t" \
        "v_mov_b32 v61, 0\n\t" \
        "v_mov_b32 v62, 0\n\t" \
        "v_mov_b32 v63, 0\n\t" \
        "v_mov_b32 v64, 0\n\t" \
        "v_mov_b32 v65, 0\n\t" \
        "v_mov_b32 v66, 0\n\t" \
        "v_mov_b32 v67, 0\n\t" \
        "v_mov_b32 v68, 0\n\t" \
        "v_mov_b32 v69, 0\n\t" \
        "v_mov_b32 v70, 0\n\t" \
        "v_mov_b32 v71, 0\n\t" \
        "v_mov_b32 v72, 0\n\t" \
        "v_mov_b32 v73, 0\n\t" \
        "v_mov_b32 v74, 0\n\t" \
        "v_mov_b32 v75, 0\n\t" \
        "v_mov_b32 v76, 0\n\t" \
        "v_mov_b32 v77, 0\n\t" \
        "v_mov_b32 v78, 0\n\t" \
        "v_mov_b32 v79, 0\n\t" \
        "v_mov_b32 v80, 0\n\t" \
        "v_mov_b32 v81, 0\n\t" \
        "v_mov_b32 v82, 0\n\t" \
        "v_mov_b32 v83, 0\n\t" \
        "v_mov_b32 v84, 0\n\t" \
        "v_mov_b32 v85, 0\n\t" \
        "v_mov_b32 v86, 0\n\t" \
        "v_mov_b32 v87, 0\n\t" \
        "v_mov_b32 v88, 0\n\t" \
        "v_mov_b32 v89, 0\n\t" \
        "v_mov_b32 v90, 0\n\t" \
        "v_mov_b32 v91, 0\n\t" \
        "v_mov_b32 v152, 0\n\t" \
        "v_mov_b32 v153, 0\n\t" \
        "v_mov_b32 v154, 0\n\t" \
        "v_mov_b32 v155, 0\n\t" \
        "v_mov_b32 v156, 0\n\t" \
        "v_mov_b32 v157, 0\n\t" \
        "v_mov_b32 v158, 0\n\t" \
        "v_mov_b32 v159, 0\n\t" \
        "v_mov_b32 v160, 0\n\t" \
        "v_mov_b32 v161, 0\n\t" \
        "v_mov_b32 v162, 0\n\t" \
        "v_mov_b32 v163, 0\n\t" \
        "v_mov_b32 v164, 0\n\t" \
        "v_mov_b32 v165, 0\n\t" \
        "v_mov_b32 v166, 0\n\t" \
        "v_mov_b32 v167, 0\n\t" \
        "v_mov_b32 v168, 0\n\t" \
        "v_mov_b32 v169, 0\n\t" \
        "v_mov_b32 v170, 0\n\t" \
        "v_mov_b32 v171, 0\n\t" \
        "v_mov_b32 v172, 0\n\t" \
        "v_mov_b32 v173, 0\n\t" \
        "v_mov_b32 v174, 0\n\t" \
        "v_mov_b32 v175, 0\n\t" \
        "v_mov_b32 v176, 0\n\t" \
        "v_mov_b32 v177, 0\n\t" \
        "v_mov_b32 v178, 0\n\t" \
        "v_mov_b32 v179, 0\n\t" \
        "v_mov_b32 v180, 0\n\t" \
        "v_mov_b32 v181, 0\n\t" \
        "v_mov_b32 v182, 0\n\t" \
        "v_mov_b32 v183, 0\n\t" \
        "v_mov_b32 v184, 0\n\t" \
        "v_mov_b32 v185, 0\n\t" \
        "v_mov_b32 v186, 0\n\t" \
        "v_mov_b32 v187, 0\n\t" \
        "v_mov_b32 v188, 0\n\t" \
        "v_mov_b32 v189, 0\n\t" \
        "v_mov_b32 v190, 0\n\t" \
        "v_mov_b32 v191, 0\n\t" \
        "v_mov_b32 v192, 0\n\t" \
        "v_mov_b32 v193, 0\n\t" \
        "v_mov_b32 v194, 0\n\t" \
        "v_mov_b32 v195, 0\n\t" \
        "v_mov_b32 v196, 0\n\t" \
        "v_mov_b32 v197, 0\n\t" \
        "v_mov_b32 v198, 0\n\t" \
        "v_mov_b32 v199, 0\n\t" \
        "v_mov_b32 v232, 0\n\t" \
        "v_mov_b32 v233, 0\n\t" \
        "v_mov_b32 v234, 0\n\t" \
        "v_mov_b32 v235, 0\n\t" \
        "v_mov_b32 v236, 0\n\t" \
        "v_mov_b32 v237, 0\n\t" \
        "v_mov_b32 v238, 0\n\t" \
        "v_mov_b32 v239, 0\n\t" \
        "v_mov_b32 v240, 0\n\t" \
        "v_mov_b32 v241, 0\n\t" \
        "v_mov_b32 v242, 0\n\t" \
        "v_mov_b32 v243, 0\n\t" \
        "v_mov_b32 v244, 0\n\t" \
        "v_mov_b32 v245, 0\n\t" \
        "v_mov_b32 v246, 0\n\t" \
        "v_mov_b32 v247, 0\n\t" \
        ".Lc_i1%=:\n\t" \
        "s_mov_b64 s[90:91], %[wsrc0]\n\t" \
        "s_mov_b32 m0, %[wdst]\n\t" \
        "s_bitcmp1_b32 %[vm], 7\n\t" \
        "s_waitcnt vmcnt(10)\n\t" \
        "s_cbranch_scc1 .Lc_i2%=\n\t" \
        "s_waitcnt vmcnt(0)\n\t" \
        ".Lc_i2%=:\n\t" \
        "s_memrealtime %[st0]\n\t" \
        "s_waitcnt lgkmcnt(0)\n\t" \
        "s_barrier\n\t" \
        "s_memrealtime %[st1]\n\t" \
        "s_memtime %[cy1]\n\t" \
        "s_waitcnt lgkmcnt(0)\n\t" \
        "ds_read_b128 v[40:43], v30\n\t" \
        "s_bitcmp1_b32 %[vm], 5\n\t" \
        "s_cbranch_scc0 .Lb_start%=\n\t" \
        "ds_read_b128 v[36:39], %[taddr]\n\t" \
        "ds_read_b64_tr_b16 v[128:129], %[wb0] offset:0\n\t" \
        "ds_read_b64_tr_b16 v[130:131], %[wb0] offset:768\n\t" \
        "ds_read_b64_tr_b16 v[132:133], %[wb1] offset:0\n\t" \
        "ds_read_b64_tr_b16 v[134:135], %[wb1] offset:768\n\t" \
        "ds_read_b64_tr_b16 v[136:137], %[wb2] offset:0\n\t" \
        "ds_read_b64_tr_b16 v[138:139], %[wb2] offset:768\n\t" \
        "s_waitcnt lgkmcnt(6)\n\t" \
        "v_or3_b32 v40, v40, v41, v42\n\t" \
        "v_or_b32 v40, v40, v43\n\t" \
        "v_xor_b32 v30, v36, %[kq]\n\t" \
        "v_xor_b32 v31, v37, %[kq]\n\t" \
        "v_xor_b32 v32, v38, %[kq]\n\t" \
        "v_xor_b32 v33, v39, %[kq]\n\t" \
        "v_readfirstlane_b32 %[ab], v40\n\t" \
        "s_nop 0\n\t" \
        "s_cmp_eq_u32 %[ab], 0\n\t" \
        "s_cbranch_scc1 .La_go%=\n\t" \
        "s_and_b32 %[ab], %[ab], 2\n\t" \
        "s_cbranch_scc1 .Lc_end%=\n\t" \
        "s_barrier\n\t" \
        ".La_go%=:\n\t" \
        "ds_read_b128 v[200:203], v30 offset:0\n\t" \
        "ds_read_b128 v[204:207], v31 offset:0\n\t" \
        "ds_read_b64_tr_b16 v[140:141], %[wb3] offset:0\n\t" \
        "ds_read_b64_tr_b16 v[142:143], %[wb3] offset:768\n\t" \
        "ds_read_b64_tr_b16 v[144:145], %[wb4] offset:0\n\t" \
        "ds_read_b64_tr_b16 v[146:147], %[wb4] offset:768\n\t" \
        "ds_read_b64_tr_b16 v[148:149], %[wb5] offset:0\n\t" \
        "ds_read_b64_tr_b16 v[150:151], %[wb5] offset:768\n\t" \
        "ds_read_b128 v[216:219], v30 offset:64\n\t" \
        "ds_read_b128 v[220:223], v31 offset:64\n\t" \
        "s_waitcnt lgkmcnt(7)\n\t" \
        "v_mfma_f32_16x16x32_bf16 v[44:47], v[128:131], v[200:203], 0\n\t" \
        "ds_read_b64_tr_b16 v[152:153], %[wb0] offset:6144\n\t" \
        "v_mfma_f32_16x16x32_bf16 v[68:71], v[128:131], v[204:207], 0\n\t" \
        "global_load_lds_dwordx4 %[wo0], s[90:91] offset:0\n\t" \
        "ds_read_b64_tr_b16 v[154:155], %[wb0] offset:6912\n\t" \
        "v_mfma_f32_16x16x32_bf16 v[48:51], v[132:135], v[200:203], 0\n\t" \
        "ds_read_b64_tr_b16 v[156:157], %[wb1] offset:6144\n\t" \
        "v_mfma_f32_16x16x32_bf16 v[72:75], v[132:135], v[204:207], 0\n\t" \
        "global_load_lds_dwordx4 %[wo1], s[90:91] offset:1024\n\t" \
        "ds_read_b64_tr_b16 v[158:159], %[wb1] offset:6912\n\t" \
        "v_mfma_f32_16x16x32_bf16 v[52:55], v[136:139], v[200:203], 0\n\t" \
        "ds_read_b64_tr_b16 v[160:161], %[wb2] offset:6144\n\t" \
        "v_mfma_f32_16x16x32_bf16 v[76:79], v[136:139], v[204:207], 0\n\t" \
        "global_load_lds_dwordx4 %[wo2], s[90:91] offset:2048\n\t" \
        "ds_read_b64_tr_b16 v[162:163], %[wb2] offset:6912\n\t" \
        "s_waitcnt lgkmcnt(10)\n\t" \
        "v_mfma_f32_16x16x32_bf16 v[56:59], v[140:143], v[200:203], 0\n\t" \
        "ds_read_b64_tr_b16 v[164:165], %[wb3] offset:6144\n\t" \
        "v_mfma_f32_16x16x32_bf16 v[80:83], v[140:143], v[204:207], 0\n\t" \
        "global_load_lds_dwordx4 %[wo3], s[90:91] offset:3072\n\t" \
        "ds_read_b64_tr_b16 v[166:167], %[wb3] offset:6912\n\t" \
        "v_mfma_f32_16x16x32_bf16 v[60:63], v[144:147], v[200:203], 0\n\t" \
        "ds_read_b64_tr_b16 v[168:169], %[wb4] offset:6144\n\t" \
        "v_mfma_f32_16x16x32_bf16 v[84:87], v[144:147], v[204:207], 0\n\t" \
        "s_mov_b32 exec_lo, 0\n\t" \
        "global_load_lds_dwordx4 %[wo4], s[90:91] offset:3584\n\t" \
        "s_mov_b32 exec_lo, -1\n\t" \
        "ds_read_b64_tr_b16 v[170:171], %[wb4] offset:6912\n\t" \
        "s_waitcnt lgkmcnt(10)\n\t" \
        "v_mfma_f32_16x16x32_bf16 v[64:67], v[148:151], v[200:203], 0\n\t" \
        "ds_read_b64_tr_b16 v[172:173], %[wb5] offset:6144\n\t" \
        "v_mfma_f32_16x16x32_bf16 v[88:91], v[148:151], v[204:207], 0\n\t" \
        "ds_read_b64_tr_b16 v[174:175], %[wb5] offset:6912\n\t" \
        "s_waitcnt lgkmcnt(8)\n\t" \
        "v_mfma_f32_16x16x32_bf16 v[44:47], v[152:155], v[216:219], v[44:47]\n\t" \
        "ds_read_b128 v[232:235], v30 offset:128\n\t" \
        "ds_read_b128 v[236:239], v31 offset:128\n\t" \
        "v_mfma_f32_16x16x32_bf16 v[68:71], v[152:155], v[220:223], v[68:71]\n\t" \
        "ds_read_b64_tr_b16 v[176:177], %[wb0] offset:12288\n\t" \
        "v_mfma_f32_16x16x32_bf16 v[48:51], v[156:159], v[216:219], v[48:51]\n\t" \
        "ds_read_b64_tr_b16 v[178:179], %[wb0] offset:13056\n\t" \
        "s_sub_u32 %[slot], 0, %[slot]\n\t" \
        "v_mfma_f32_16x16x32_bf16 v[72:75], v[156:159], v[220:223], v[72:75]\n\t" \
        "ds_read_b64_tr_b16 v[180:181], %[wb1] offset:12288\n\t" \
        "s_sub_u32 %[cnt], %[cnt], 1\n\t" \
        "s_waitcnt lgkmcnt(9)\n\t" \
        "v_mfma_f32_16x16x32_bf16 v[52:55], v[160:163], v[216:219], v[52:55]\n\t" \
        "ds_read_b64_tr_b16 v[182:183], %[wb1] offset:13056\n\t" \
        "s_sub_u32 m0, %[wsum], m0\n\t" \
        "v_mfma_f32_16x16x32_bf16 v[76:79], v[160:163], v[220:223], v[76:79]\n\t" \
        "ds_read_b64_tr_b16 v[184:185], %[wb2] offset:12288\n\t" \
        "s_sub_u32 s90, s90, 18432\n\t" \
        "v_mfma_f32_16x16x32_bf16 v[56:59], v[164:167], v[216:219], v[56:59]\n\t" \
        "ds_read_b64_tr_b16 v[186:187], %[wb2] offset:13056\n\t" \
        "s_subb_u32 s91, s91, 0\n\t" \
        "v_mfma_f32_16x16x32_bf16 v[80:83], v[164:167], v[220:223], v[80:83]\n\t" \
        "ds_read_b128 v[36:39], %[taddr] offset:256\n\t" \
        "s_waitcnt vmcnt(11)\n\t" \
        "s_waitcnt lgkmcnt(9)\n\t" \
        "v_mfma_f32_16x16x32_bf16 v[60:63], v[168:171], v[216:219], v[60:63]\n\t" \
        "ds_read_b128 v[208:211], v32 offset:0\n\t" \
        "v_mfma_f32_16x16x32_bf16 v[84:87], v[168:171], v[220:223], v[84:87]\n\t" \
        "ds_read_b128 v[224:227], v32 offset:64\n\t" \
        "v_mfma_f32_16x16x32_bf16 v[64:67], v[172:175], v[216:219], v[64:67]\n\t" \
        "ds_read_b128 v[240:243], v32 offset:128\n\t" \
        "v_mfma_f32_16x16x32_bf16 v[88:91], v[172:175], v[220:223], v[88:91]\n\t" \
        "ds_read_b64_tr_b16 v[188:189], %[wb3] offset:12288\n\t" \
        "s_waitcnt lgkmcnt(3)\n\t" \
        "v_mfma_f32_16x16x32_bf16 v[92:95], v[128:131], v[208:211], 0\n\t" \
        "ds_read_b64_tr_b16 v[190:191], %[wb3] offset:13056\n\t" \
        "v_add_u32 %[taddr], 0x100, %[taddr]\n\t" \
        "v_xor_b32 v30, v36, %[kq]\n\t" \
        "v_mfma_f32_16x16x32_bf16 v[96:99], v[132:135], v[208:211], 0\n\t" \
        "ds_read_b64_tr_b16 v[192:193], %[wb4] offset:12288\n\t" \
        "v_xor_b32 v31, v37, %[kq]\n\t" \
        "v_mfma_f32_16x16x32_bf16 v[100:103], v[136:139], v[208:211], 0\n\t" \
        "ds_read_b64_tr_b16 v[194:195], %[wb4] offset:13056\n\t" \
        "v_xor_b32 v32, v38, %[kq]\n\t" \
        "v_mfma_f32_16x16x32_bf16 v[104:107], v[140:143], v[208:211], 0\n\t" \
        "ds_read_b64_tr_b16 v[196:197], %[wb5] offset:12288\n\t" \
        "v_add_u32 %[wb0], %[slot], %[wb0]\n\t" \
        "v_mfma_f32_16x16x32_bf16 v[108:111], v[144:147], v[208:211], 0\n\t" \
        "ds_read_b64_tr_b16 v[198:199], %[wb5] offset:13056\n\t" \
        "v_add_u32 %[wb1], %[slot], %[wb1]\n\t" \
        "v_mfma_f32_16x16x32_bf16 v[112:115], v[148:151], v[208:211], 0\n\t" \
        "s_waitcnt vmcnt(7)\n\t" \
        "v_add_u32 %[wb2], %[slot], %[wb2]\n\t" \
        "s_waitcnt lgkmcnt(7)\n\t" \
        "v_mfma_f32_16x16x32_bf16 v[92:95], v[152:155], v[224:227], v[92:95]\n\t" \
        "ds_read_b128 v[212:215], v33 offset:0\n\t" \
        "v_mfma_f32_16x16x32_bf16 v[96:99], v[156:159], v[224:227], v[96:99]\n\t" \
        "ds_read_b128 v[228:231], v33 offset:64\n\t" \
        "v_mfma_f32_16x16x32_bf16 v[100:103], v[160:163], v[224:227], v[100:103]\n\t" \
        "ds_read_b128 v[244:247], v33 offset:128\n\t" \
        "v_mfma_f32_16x16x32_bf16 v[104:107], v[164:167], v[224:227], v[104:107]\n\t" \
        "v_add_u32 %[wb3], %[slot], %[wb3]\n\t" \
        "v_add_u32 %[wb4], %[slot], %[wb4]\n\t" \
        "v_mfma_f32_16x16x32_bf16 v[108:111], v[168:171], v[224:227], v[108:111]\n\t" \
        "v_add_u32 %[wb5], %[slot], %[wb5]\n\t" \
        "v_mfma_f32_16x16x32_bf16 v[112:115], v[172:175], v[224:227], v[112:115]\n\t" \
        "v_xor_b32 v33, v39, %[kq]\n\t" \
        "s_waitcnt lgkmcnt(1)\n\t" \
        "s_branch .La_g3%=\n\t" \
        ".La_top%=:\n\t" \
        "s_waitcnt vmcnt(0) lgkmcnt(0)\n\t" \
        "s_barrier\n\t" \
        "v_mfma_f32_16x16x32_bf16 v[44:47], v[176:179], v[232:235], v[44:47]\n\t" \
        "ds_read_b128 v[200:203], v30 offset:0\n\t" \
        "v_mfma_f32_16x16x32_bf16 v[68:71], v[176:179], v[236:239], v[68:71]\n\t" \
        "global_load_lds_dwordx4 %[wo0], s[90:91] offset:0\n\t" \
        "v_mfma_f32_16x16x32_bf16 v[92:95], v[176:179], v[240:243], v[92:95]\n\t" \
        "ds_read_b64_tr_b16 v[128:129], %[wb0] offset:0\n\t" \
        "v_mfma_f32_16x16x32_bf16 v[48:51], v[180:183], v[232:235], v[48:51]\n\t" \
        "ds_read_b64_tr_b16 v[130:131], %[wb0] offset:768\n\t" \
        "v_mfma_f32_16x16x32_bf16 v[72:75], v[180:183], v[236:239], v[72:75]\n\t" \
        "global_load_lds_dwordx4 %[wo1], s[90:91] offset:1024\n\t" \
        "ds_read_b64_tr_b16 v[132:133], %[wb1] offset:0\n\t" \
        "v_mfma_f32_16x16x32_bf16 v[96:99], v[180:183], v[240:243], v[96:99]\n\t" \
        "ds_read_b128 v[204:207], v31 offset:0\n\t" \
        "v_mfma_f32_16x16x32_bf16 v[52:55], v[184:187], v[232:235], v[52:55]\n\t" \
        "s_mov_b32 exec_lo, 0\n\t" \
        "global_load_lds_dwordx4 %[wo4], s[90:91] offset:3584\n\t" \
        "s_mov_b32 exec_lo, -1\n\t" \
        "v_mfma_f32_16x16x32_bf16 v[76:79], v[184:187], v[236:239], v[76:79]\n\t" \
        "global_load_lds_dwordx4 %[wo2], s[90:91] offset:2048\n\t" \
        "ds_read_b64_tr_b16 v[134:135], %[wb1] offset:768\n\t" \
        "v_mfma_f32_16x16x32_bf16 v[100:103], v[184:187], v[240:243], v[100:103]\n\t" \
        "ds_read_b64_tr_b16 v[136:137], %[wb2] offset:0\n\t" \
        "v_mfma_f32_16x16x32_bf16 v[56:59], v[188:191], v[232:235], v[56:59]\n\t" \
        "ds_read_b64_tr_b16 v[138:139], %[wb2] offset:768\n\t" \
        "v_mfma_f32_16x16x32_bf16 v[80:83], v[188:191], v[236:239], v[80:83]\n\t" \
        "ds_read_b128 v[208:211], v32 offset:0\n\t" \
        "v_mfma_f32_16x16x32_bf16 v[104:107], v[188:191], v[240:243], v[104:107]\n\t" \
        "global_load_lds_dwordx4 %[wo3], s[90:91] offset:3072\n\t" \
        "ds_read_b64_tr_b16 v[140:141], %[wb3] offset:0\n\t" \
        "v_mfma_f32_16x16x32_bf16 v[60:63], v[192:195], v[232:235], v[60:63]\n\t" \
        "ds_read_b64_tr_b16 v[142:143], %[wb3] offset:768\n\t" \
        "v_mfma_f32_16x16x32_bf16 v[84:87], v[192:195], v[236:239], v[84:87]\n\t" \
        "ds_read_b64_tr_b16 v[144:145], %[wb4] offset:0\n\t" \
        "v_mfma_f32_16x16x32_bf16 v[108:111], v[192:195], v[240:243], v[108:111]\n\t" \
        "ds_read_b128 v[212:215], v33 offset:0\n\t" \
        "v_mfma_f32_16x16x32_bf16 v[64:67], v[196:199], v[232:235], v[64:67]\n\t" \
        "ds_read_b64_tr_b16 v[146:147], %[wb4] offset:768\n\t" \
        "v_mfma_f32_16x16x32_bf16 v[88:91], v[196:199], v[236:239], v[88:91]\n\t" \
        "ds_read_b64_tr_b16 v[148:149], %[wb5] offset:0\n\t" \
        "v_mfma_f32_16x16x32_bf16 v[112:115], v[196:199], v[240:243], v[112:115]\n\t" \
        "ds_read_b64_tr_b16 v[150:151], %[wb5] offset:768\n\t" \
        ".La_p0%=:\n\t" \
        "s_waitcnt lgkmcnt(7)\n\t" \
        "v_mfma_f32_16x16x32_bf16 v[44:47], v[128:131], v[200:203], v[44:47]\n\t" \
        "ds_read_b128 v[216:219], v30 offset:64\n\t" \
        "v_mfma_f32_16x16x32_bf16 v[68:71], v[128:131], v[204:207], v[68:71]\n\t" \
        "s_sub_u32 %[slot], 0, %[slot]\n\t" \
        "v_mfma_f32_16x16x32_bf16 v[92:95], v[128:131], v[208:211], v[92:95]\n\t" \
        "s_sub_u32 %[cnt], %[cnt], 1\n\t" \
        "ds_read_b64_tr_b16 v[152:153], %[wb0] offset:6144\n\t" \
        "v_mfma_f32_16x16x32_bf16 v[48:51], v[132:135], v[200:203], v[48:51]\n\t" \
        "s_sub_u32 m0, %[wsum], m0\n\t" \
        "ds_read_b64_tr_b16 v[154:155], %[wb0] offset:6912\n\t" \
        "v_mfma_f32_16x16x32_bf16 v[72:75], v[132:135], v[204:207], v[72:75]\n\t" \
        "s_sub_u32 s90, s90, 18432\n\t" \
        "ds_read_b64_tr_b16 v[156:157], %[wb1] offset:6144\n\t" \
        "v_mfma_f32_16x16x32_bf16 v[96:99], v[132:135], v[208:211], v[96:99]\n\t" \
        "ds_read_b128 v[220:223], v31 offset:64\n\t" \
        "s_waitcnt lgkmcnt(5)\n\t" \
        "v_mfma_f32_16x16x32_bf16 v[52:55], v[136:139], v[200:203], v[52:55]\n\t" \
        "ds_read_b128 v[36:39], %[taddr] offset:256\n\t" \
        "v_mfma_f32_16x16x32_bf16 v[76:79], v[136:139], v[204:207], v[76:79]\n\t" \
        "s_subb_u32 s91, s91, 0\n\t" \
        "ds_read_b64_tr_b16 v[158:159], %[wb1] offset:6912\n\t" \
        "v_mfma_f32_16x16x32_bf16 v[100:103], v[136:139], v[208:211], v[100:103]\n\t" \
        "ds_read_b64_tr_b16 v[160:161], %[wb2] offset:6144\n\t" \
        "v_mfma_f32_16x16x32_bf16 v[56:59], v[140:143], v[200:203], v[56:59]\n\t" \
        "ds_read_b64_tr_b16 v[162:163], %[wb2] offset:6912\n\t" \
        "v_mfma_f32_16x16x32_bf16 v[80:83], v[140:143], v[204:207], v[80:83]\n\t" \
        "ds_read_b128 v[224:227], v32 offset:64\n\t" \
        "v_mfma_f32_16x16x32_bf16 v[104:107], v[140:143], v[208:211], v[104:107]\n\t" \
        "ds_read_b64_tr_b16 v[164:165], %[wb3] offset:6144\n\t" \
        "v_mfma_f32_16x16x32_bf16 v[60:63], v[144:147], v[200:203], v[60:63]\n\t" \
        "ds_read_b64_tr_b16 v[166:167], %[wb3] offset:6912\n\t" \
        "v_mfma_f32_16x16x32_bf16 v[84:87], v[144:147], v[204:207], v[84:87]\n\t" \
        "ds_read_b64_tr_b16 v[168:169], %[wb4] offset:6144\n\t" \
        "v_mfma_f32_16x16x32_bf16 v[108:111], v[144:147], v[208:211], v[108:111]\n\t" \
        "ds_read_b128 v[228:231], v33 offset:64\n\t" \
        "v_mfma_f32_16x16x32_bf16 v[64:67], v[148:151], v[200:203], v[64:67]\n\t" \
        "ds_read_b64_tr_b16 v[170:171], %[wb4] offset:6912\n\t" \
        "v_mfma_f32_16x16x32_bf16 v[88:91], v[148:151], v[204:207], v[88:91]\n\t" \
        "ds_read_b64_tr_b16 v[172:173], %[wb5] offset:6144\n\t" \
        "v_mfma_f32_16x16x32_bf16 v[112:115], v[148:151], v[208:211], v[112:115]\n\t" \
        "ds_read_b64_tr_b16 v[174:175], %[wb5] offset:6912\n\t" \
        ".La_p1%=:\n\t" \
        "s_waitcnt lgkmcnt(7)\n\t" \
        "v_mfma_f32_16x16x32_bf16 v[44:47], v[152:155], v[216:219], v[44:47]\n\t" \
        "ds_read_b128 v[232:235], v30 offset:128\n\t" \
        "v_mfma_f32_16x16x32_bf16 v[68:71], v[152:155], v[220:223], v[68:71]\n\t" \
        "v_add_u32 %[taddr], 0x100, %[taddr]\n\t" \
        "v_mfma_f32_16x16x32_bf16 v[92:95], v[152:155], v[224:227], v[92:95]\n\t" \
        "ds_read_b64_tr_b16 v[176:177], %[wb0] offset:12288\n\t" \
        "v_mfma_f32_16x16x32_bf16 v[48:51], v[156:159], v[216:219], v[48:51]\n\t" \
        "ds_read_b64_tr_b16 v[178:179], %[wb0] offset:13056\n\t" \
        "v_mfma_f32_16x16x32_bf16 v[72:75], v[156:159], v[220:223], v[72:75]\n\t" \
        "v_add_u32 %[wb0], %[slot], %[wb0]\n\t" \
        "ds_read_b64_tr_b16 v[180:181], %[wb1] offset:12288\n\t" \
        "v_mfma_f32_16x16x32_bf16 v[96:99], v[156:159], v[224:227], v[96:99]\n\t" \
        "ds_read_b128 v[236:239], v31 offset:128\n\t" \
        "s_waitcnt lgkmcnt(5)\n\t" \
        "v_mfma_f32_16x16x32_bf16 v[52:55], v[160:163], v[216:219], v[52:55]\n\t" \
        "v_xor_b32 v30, v36, %[kq]\n\t" \
        "v_mfma_f32_16x16x32_bf16 v[76:79], v[160:163], v[220:223], v[76:79]\n\t" \
        "v_xor_b32 v31, v37, %[kq]\n\t" \
        "ds_read_b64_tr_b16 v[182:183], %[wb1] offset:13056\n\t" \
        "v_mfma_f32_16x16x32_bf16 v[100:103], v[160:163], v[224:227], v[100:103]\n\t" \
        "v_add_u32 %[wb1], %[slot], %[wb1]\n\t" \
        "ds_read_b64_tr_b16 v[184:185], %[wb2] offset:12288\n\t" \
        "v_mfma_f32_16x16x32_bf16 v[56:59], v[164:167], v[216:219], v[56:59]\n\t" \
        "ds_read_b64_tr_b16 v[186:187], %[wb2] offset:13056\n\t" \
        "v_mfma_f32_16x16x32_bf16 v[80:83], v[164:167], v[220:223], v[80:83]\n\t" \
        "ds_read_b128 v[240:243], v32 offset:128\n\t" \
        "v_mfma_f32_16x16x32_bf16 v[104:107], v[164:167], v[224:227], v[104:107]\n\t" \
        "v_xor_b32 v32, v38, %[kq]\n\t" \
        "ds_read_b64_tr_b16 v[188:189], %[wb3] offset:12288\n\t" \
        "v_mfma_f32_16x16x32_bf16 v[60:63], v[168:171], v[216:219], v[60:63]\n\t" \
        "v_add_u32 %[wb2], %[slot], %[wb2]\n\t" \
        "ds_read_b64_tr_b16 v[190:191], %[wb3] offset:13056\n\t" \
        "v_mfma_f32_16x16x32_bf16 v[84:87], v[168:171], v[220:223], v[84:87]\n\t" \
        "v_add_u32 %[wb3], %[slot], %[wb3]\n\t" \
        "ds_read_b64_tr_b16 v[192:193], %[wb4] offset:12288\n\t" \
        "v_mfma_f32_16x16x32_bf16 v[108:111], v[168:171], v[224:227], v[108:111]\n\t" \
        "ds_read_b128 v[244:247], v33 offset:128\n\t" \
        "v_mfma_f32_16x16x32_bf16 v[64:67], v[172:175], v[216:219], v[64:67]\n\t" \
        "v_xor_b32 v33, v39, %[kq]\n\t" \
        "ds_read_b64_tr_b16 v[194:195], %[wb4] offset:13056\n\t" \
        "v_mfma_f32_16x16x32_bf16 v[88:91], v[172:175], v[220:223], v[88:91]\n\t" \
        "v_add_u32 %[wb4], %[slot], %[wb4]\n\t" \
        "ds_read_b64_tr_b16 v[196:197], %[wb5] offset:12288\n\t" \
        "v_mfma_f32_16x16x32_bf16 v[112:115], v[172:175], v[224:227], v[112:115]\n\t" \
        "ds_read_b64_tr_b16 v[198:199], %[wb5] offset:13056\n\t" \
        "v_add_u32 %[wb5], %[slot], %[wb5]\n\t" \
        ".La_g3%=:\n\t" \
        "s_cmp_lg_u32 %[cnt], 1\n\t" \
        "s_cbranch_vccz .La_nh%=\n\t" \
        "v_mfma_f32_16x16x32_bf16 v[116:119], v[128:131], v[212:215], v[116:119]\n\t" \
        "v_mfma_f32_16x16x32_bf16 v[120:123], v[132:135], v[212:215], v[120:123]\n\t" \
        "v_mfma_f32_16x16x32_bf16 v[124:127], v[136:139], v[212:215], v[124:127]\n\t" \
        "v_mfma_f32_16x16x32_bf16 v[116:119], v[152:155], v[228:231], v[116:119]\n\t" \
        "v_mfma_f32_16x16x32_bf16 v[120:123], v[156:159], v[228:231], v[120:123]\n\t" \
        "v_mfma_f32_16x16x32_bf16 v[124:127], v[160:163], v[228:231], v[124:127]\n\t" \
        "s_waitcnt lgkmcnt(0)\n\t" \
        "v_mfma_f32_16x16x32_bf16 v[116:119], v[176:179], v[244:247], v[116:119]\n\t" \
        "v_mfma_f32_16x16x32_bf16 v[120:123], v[180:183], v[244:247], v[120:123]\n\t" \
        "v_mfma_f32_16x16x32_bf16 v[124:127], v[184:187], v[244:247], v[124:127]\n\t" \
        ".La_nh%=:\n\t" \
        "s_cbranch_scc1 .La_top%=\n\t" \
        "s_waitcnt lgkmcnt(0)\n\t" \
        "s_memrealtime %[st2]\n\t" \
        "s_memtime %[cy2]\n\t" \
        "s_waitcnt vmcnt(0) lgkmcnt(0)\n\t" \
        "s_barrier\n\t" \
        "v_mfma_f32_16x16x32_bf16 v[44:47], v[176:179], v[232:235], v[44:47]\n\t" \
        "ds_read_b128 v[212:215], v33 offset:0\n\t" \
        "ds_read_b64_tr_b16 v[128:129], %[wb0] offset:0\n\t" \
        "v_mfma_f32_16x16x32_bf16 v[68:71], v[176:179], v[236:239], v[68:71]\n\t" \
        "ds_read_b64_tr_b16 v[130:131], %[wb0] offset:768\n\t" \
        "ds_read_b64_tr_b16 v[152:153], %[wb0] offset:6144\n\t" \
        "v_mfma_f32_16x16x32_bf16 v[92:95], v[176:179], v[240:243], v[92:95]\n\t" \
        "ds_read_b64_tr_b16 v[154:155], %[wb0] offset:6912\n\t" \
        "ds_read_b128 v[228:231], v33 offset:64\n\t" \
        "v_mfma_f32_16x16x32_bf16 v[48:51], v[180:183], v[232:235], v[48:51]\n\t" \
        "ds_read_b64_tr_b16 v[132:133], %[wb1] offset:0\n\t" \
        "ds_read_b64_tr_b16 v[134:135], %[wb1] offset:768\n\t" \
        "v_mfma_f32_16x16x32_bf16 v[72:75], v[180:183], v[236:239], v[72:75]\n\t" \
        "ds_read_b64_tr_b16 v[156:157], %[wb1] offset:6144\n\t" \
        "ds_read_b64_tr_b16 v[158:159], %[wb1] offset:6912\n\t" \
        "v_mfma_f32_16x16x32_bf16 v[96:99], v[180:183], v[240:243], v[96:99]\n\t" \
        "ds_read_b128 v[244:247], v33 offset:128\n\t" \
        "ds_read_b64_tr_b16 v[136:137], %[wb2] offset:0\n\t" \
        "v_mfma_f32_16x16x32_bf16 v[52:55], v[184:187], v[232:235], v[52:55]\n\t" \
        "ds_read_b64_tr_b16 v[138:139], %[wb2] offset:768\n\t" \
        "ds_read_b64_tr_b16 v[160:161], %[wb2] offset:6144\n\t" \
        "v_mfma_f32_16x16x32_bf16 v[76:79], v[184:187], v[236:239], v[76:79]\n\t" \
        "ds_read_b64_tr_b16 v[162:163], %[wb2] offset:6912\n\t" \
        "ds_read_b128 v[200:203], v30 offset:0\n\t" \
        "v_mfma_f32_16x16x32_bf16 v[100:103], v[184:187], v[240:243], v[100:103]\n\t" \
        "ds_read_b64_tr_b16 v[184:185], %[wb2] offset:12288\n\t" \
        "ds_read_b64_tr_b16 v[186:187], %[wb2] offset:13056\n\t" \
        "v_mfma_f32_16x16x32_bf16 v[56:59], v[188:191], v[232:235], v[56:59]\n\t" \
        "ds_read_b64_tr_b16 v[140:141], %[wb3] offset:0\n\t" \
        "ds_read_b64_tr_b16 v[142:143], %[wb3] offset:768\n\t" \
        "v_mfma_f32_16x16x32_bf16 v[80:83], v[188:191], v[236:239], v[80:83]\n\t" \
        "ds_read_b128 v[216:219], v30 offset:64\n\t" \
        "ds_read_b64_tr_b16 v[164:165], %[wb3] offset:6144\n\t" \
        "v_mfma_f32_16x16x32_bf16 v[104:107], v[188:191], v[240:243], v[104:107]\n\t" \
        "ds_read_b64_tr_b16 v[166:167], %[wb3] offset:6912\n\t" \
        "ds_read_b64_tr_b16 v[176:177], %[wb0] offset:12288\n\t" \
        "v_mfma_f32_16x16x32_bf16 v[60:63], v[192:195], v[232:235], v[60:63]\n\t" \
        "ds_read_b64_tr_b16 v[178:179], %[wb0] offset:13056\n\t" \
        "ds_read_b64_tr_b16 v[180:181], %[wb1] offset:12288\n\t" \
        "v_mfma_f32_16x16x32_bf16 v[84:87], v[192:195], v[236:239], v[84:87]\n\t" \
        "ds_read_b64_tr_b16 v[182:183], %[wb1] offset:13056\n\t" \
        "ds_read_b64_tr_b16 v[188:189], %[wb3] offset:12288\n\t" \
        "v_mfma_f32_16x16x32_bf16 v[108:111], v[192:195], v[240:243], v[108:111]\n\t" \
        "ds_read_b64_tr_b16 v[190:191], %[wb3] offset:13056\n\t" \
        "v_mfma_f32_16x16x32_bf16 v[64:67], v[196:199], v[232:235], v[64:67]\n\t" \
        "v_mfma_f32_16x16x32_bf16 v[88:91], v[196:199], v[236:239], v[88:91]\n\t" \
        "v_mfma_f32_16x16x32_bf16 v[112:115], v[196:199], v[240:243], v[112:115]\n\t" \
        "ds_read_b128 v[232:235], v30 offset:128\n\t" \
        "s_lshl_b32 %[slot], %[par], 2\n\t" \
        "v_add_u32 v40, %[slot], %[ol]\n\t" \
        "s_sub_u32 %[slot], 0x80, %[slot]\n\t" \
        "v_xor_b32 v41, %[par], %[ol]\n\t" \
        "v_add_u32 v42, %[slot], %[ol]\n\t" \
        "v_add_u32 v41, 64, v41\n\t" \
        "s_mov_b32 %[ab], 0\n\t" \
        "s_waitcnt lgkmcnt(3)\n\t" \
        "s_cbranch_vccz .La_l1e%=\n\t" \
        "v_mfma_f32_16x16x32_bf16 v[124:127], v[136:139], v[212:215], v[124:127]\n\t" \
        "ds_read_b64_tr_b16 v[144:145], %[wb4] offset:0\n\t" \
        "ds_read_b64_tr_b16 v[146:147], %[wb4] offset:768\n\t" \
        "v_mfma_f32_16x16x32_bf16 v[124:127], v[160:163], v[228:231], v[124:127]\n\t" \
        "ds_read_b64_tr_b16 v[148:149], %[wb5] offset:0\n\t" \
        "ds_read_b64_tr_b16 v[150:151], %[wb5] offset:768\n\t" \
        "v_mfma_f32_16x16x32_bf16 v[124:127], v[184:187], v[244:247], v[124:127]\n\t" \
        "ds_read_b64_tr_b16 v[168:169], %[wb4] offset:6144\n\t" \
        "ds_read_b64_tr_b16 v[170:171], %[wb4] offset:6912\n\t" \
        "v_mfma_f32_16x16x32_bf16 v[116:119], v[128:131], v[212:215], v[116:119]\n\t" \
        "ds_read_b64_tr_b16 v[172:173], %[wb5] offset:6144\n\t" \
        "ds_read_b64_tr_b16 v[174:175], %[wb5] offset:6912\n\t" \
        "v_mfma_f32_16x16x32_bf16 v[116:119], v[152:155], v[228:231], v[116:119]\n\t" \
        "ds_read_b64_tr_b16 v[192:193], %[wb4] offset:12288\n\t" \
        "ds_read_b64_tr_b16 v[194:195], %[wb4] offset:13056\n\t" \
        "v_mfma_f32_16x16x32_bf16 v[116:119], v[176:179], v[244:247], v[116:119]\n\t" \
        "ds_read_b64_tr_b16 v[196:197], %[wb5] offset:12288\n\t" \
        "ds_read_b64_tr_b16 v[198:199], %[wb5] offset:13056\n\t" \
        "v_mfma_f32_16x16x32_bf16 v[120:123], v[132:135], v[212:215], v[120:123]\n\t" \
        "ds_read_b128 v[204:207], v31 offset:0\n\t" \
        "ds_read_b128 v[220:223], v31 offset:64\n\t" \
        "v_mfma_f32_16x16x32_bf16 v[120:123], v[156:159], v[228:231], v[120:123]\n\t" \
        "ds_read_b128 v[236:239], v31 offset:128\n\t" \
        "ds_read_b128 v[208:211], v32 offset:0\n\t" \
        "v_mfma_f32_16x16x32_bf16 v[120:123], v[180:183], v[244:247], v[120:123]\n\t" \
        "ds_read_b128 v[224:227], v32 offset:64\n\t" \
        "ds_read_b128 v[240:243], v32 offset:128\n\t" \
        "v_cvt_pk_bf16_f32 v124, v124, v125\n\t" \
        "v_cvt_pk_bf16_f32 v125, v126, v127\n\t" \
        "ds_write_b64 %[xa], v[124:125]\n\t" \
        ".La_l1%=:\n\t" \
        "v_mfma_f32_16x16x32_bf16 v[44:47], v[128:131], v[200:203], v[44:47]\n\t" \
        "v_mfma_f32_16x16x32_bf16 v[48:51], v[132:135], v[200:203], v[48:51]\n\t" \
        "v_mfma_f32_16x16x32_bf16 v[44:47], v[152:155], v[216:219], v[44:47]\n\t" \
        "v_mfma_f32_16x16x32_bf16 v[48:51], v[156:159], v[216:219], v[48:51]\n\t" \
        "s_waitcnt lgkmcnt(14)\n\t" \
        "v_mfma_f32_16x16x32_bf16 v[44:47], v[176:179], v[232:235], v[44:47]\n\t" \
        "v_mfma_f32_16x16x32_bf16 v[48:51], v[180:183], v[232:235], v[48:51]\n\t" \
        "v_mfma_f32_16x16x32_bf16 v[52:55], v[136:139], v[200:203], v[52:55]\n\t" \
        "v_mfma_f32_16x16x32_bf16 v[56:59], v[140:143], v[200:203], v[56:59]\n\t" \
        "v_mfma_f32_16x16x32_bf16 v[52:55], v[160:163], v[216:219], v[52:55]\n\t" \
        "s_nop 3\n\t" \
        "v_cvt_pk_bf16_f32 v44, v44, v45\n\t" \
        "v_cvt_pk_bf16_f32 v45, v46, v47\n\t" \
        "v_mfma_f32_16x16x32_bf16 v[56:59], v[164:167], v[216:219], v[56:59]\n\t" \
        "v_cvt_pk_bf16_f32 v46, v48, v49\n\t" \
        "v_cvt_pk_bf16_f32 v47, v50, v51\n\t" \
        "v_mfma_f32_16x16x32_bf16 v[52:55], v[184:187], v[232:235], v[52:55]\n\t" \
        "v_permlane16_swap_b32 v44, v46\n\t" \
        "v_permlane16_swap_b32 v45, v47\n\t" \
        "v_mfma_f32_16x16x32_bf16 v[56:59], v[188:191], v[232:235], v[56:59]\n\t" \
        "global_store_dwordx4 v40, v[44:47], %[ob0] sc1\n\t" \
        "s_waitcnt lgkmcnt(0)\n\t" \
        "s_barrier\n\t" \
        "s_cbranch_vccz .La_l2%=\n\t" \
        "ds_read_b64 v[126:127], %[xa] offset:2304\n\t" \
        "v_cvt_pk_bf16_f32 v116, v116, v117\n\t" \
        "v_cvt_pk_bf16_f32 v117, v118, v119\n\t" \
        "v_cvt_pk_bf16_f32 v118, v120, v121\n\t" \
        "v_cvt_pk_bf16_f32 v119, v122, v123\n\t" \
        "s_nop 0\n\t" \
        "v_permlane16_swap_b32 v116, v118\n\t" \
        "v_permlane16_swap_b32 v117, v119\n\t" \
        "global_store_dwordx4 v40, v[116:119], %[ob3] sc1\n\t" \
        "s_cmp_lg_u32 %[par], 0\n\t" \
        "s_cbranch_scc1 .La_l2%=\n\t" \
        "s_waitcnt lgkmcnt(0)\n\t" \
        "v_permlane16_swap_b32 v124, v126\n\t" \
        "v_permlane16_swap_b32 v125, v127\n\t" \
        "global_store_dwordx4 v41, v[124:127], %[ob3] sc1\n\t" \
        ".La_l2%=:\n\t" \
        "v_mfma_f32_16x16x32_bf16 v[60:63], v[144:147], v[200:203], v[60:63]\n\t" \
        "v_mfma_f32_16x16x32_bf16 v[64:67], v[148:151], v[200:203], v[64:67]\n\t" \
        "v_mfma_f32_16x16x32_bf16 v[60:63], v[168:171], v[216:219], v[60:63]\n\t" \
        "v_cvt_pk_bf16_f32 v52, v52, v53\n\t" \
        "v_cvt_pk_bf16_f32 v53, v54, v55\n\t" \
        "v_mfma_f32_16x16x32_bf16 v[64:67], v[172:175], v[216:219], v[64:67]\n\t" \
        "v_cvt_pk_bf16_f32 v54, v56, v57\n\t" \
        "v_cvt_pk_bf16_f32 v55, v58, v59\n\t" \
        "v_mfma_f32_16x16x32_bf16 v[60:63], v[192:195], v[232:235], v[60:63]\n\t" \
        "v_permlane16_swap_b32 v52, v54\n\t" \
        "v_permlane16_swap_b32 v53, v55\n\t" \
        "v_mfma_f32_16x16x32_bf16 v[64:67], v[196:199], v[232:235], v[64:67]\n\t" \
        "global_store_dwordx4 v41, v[52:55], %[ob0] sc1\n\t" \
        "v_mfma_f32_16x16x32_bf16 v[68:71], v[128:131], v[204:207], v[68:71]\n\t" \
        "v_mfma_f32_16x16x32_bf16 v[72:75], v[132:135], v[204:207], v[72:75]\n\t" \
        "v_mfma_f32_16x16x32_bf16 v[68:71], v[152:155], v[220:223], v[68:71]\n\t" \
        "s_nop 0\n\t" \
        "v_cvt_pk_bf16_f32 v60, v60, v61\n\t" \
        "v_cvt_pk_bf16_f32 v61, v62, v63\n\t" \
        "v_mfma_f32_16x16x32_bf16 v[72:75], v[156:159], v[220:223], v[72:75]\n\t" \
        "v_cvt_pk_bf16_f32 v62, v64, v65\n\t" \
        "v_cvt_pk_bf16_f32 v63, v66, v67\n\t" \
        "v_mfma_f32_16x16x32_bf16 v[68:71], v[176:179], v[236:239], v[68:71]\n\t" \
        "v_permlane16_swap_b32 v60, v62\n\t" \
        "v_permlane16_swap_b32 v61, v63\n\t" \
        "v_mfma_f32_16x16x32_bf16 v[72:75], v[180:183], v[236:239], v[72:75]\n\t" \
        "global_store_dwordx4 v42, v[60:63], %[ob0] sc1\n\t" \
        "v_mfma_f32_16x16x32_bf16 v[76:79], v[136:139], v[204:207], v[76:79]\n\t" \
        "v_mfma_f32_16x16x32_bf16 v[80:83], v[140:143], v[204:207], v[80:83]\n\t" \
        "v_mfma_f32_16x16x32_bf16 v[76:79], v[160:163], v[220:223], v[76:79]\n\t" \
        "s_nop 0\n\t" \
        "v_cvt_pk_bf16_f32 v68, v68, v69\n\t" \
        "v_cvt_pk_bf16_f32 v69, v70, v71\n\t" \
        "v_mfma_f32_16x16x32_bf16 v[80:83], v[164:167], v[220:223], v[80:83]\n\t" \
        "v_cvt_pk_bf16_f32 v70, v72, v73\n\t" \
        "v_cvt_pk_bf16_f32 v71, v74, v75\n\t" \
        "v_mfma_f32_16x16x32_bf16 v[76:79], v[184:187], v[236:239], v[76:79]\n\t" \
        "v_permlane16_swap_b32 v68, v70\n\t" \
        "v_permlane16_swap_b32 v69, v71\n\t" \
        "v_mfma_f32_16x16x32_bf16 v[80:83], v[188:191], v[236:239], v[80:83]\n\t" \
        "global_store_dwordx4 v40, v[68:71], %[ob1] sc1\n\t" \
        "v_mfma_f32_16x16x32_bf16 v[84:87], v[144:147], v[204:207], v[84:87]\n\t" \
        "v_mfma_f32_16x16x32_bf16 v[88:91], v[148:151], v[204:207], v[88:91]\n\t" \
        "v_mfma_f32_16x16x32_bf16 v[84:87], v[168:171], v[220:223], v[84:87]\n\t" \
        "s_nop 0\n\t" \
        "v_cvt_pk_bf16_f32 v76, v76, v77\n\t" \
        "v_cvt_pk_bf16_f32 v77, v78, v79\n\t" \
        "v_mfma_f32_16x16x32_bf16 v[88:91], v[172:175], v[220:223], v[88:91]\n\t" \
        "v_cvt_pk_bf16_f32 v78, v80, v81\n\t" \
        "v_cvt_pk_bf16_f32 v79, v82, v83\n\t" \
        "v_mfma_f32_16x16x32_bf16 v[84:87], v[192:195], v[236:239], v[84:87]\n\t" \
        "v_permlane16_swap_b32 v76, v78\n\t" \
        "v_permlane16_swap_b32 v77, v79\n\t" \
        "v_mfma_f32_16x16x32_bf16 v[88:91], v[196:199], v[236:239], v[88:91]\n\t" \
        "global_store_dwordx4 v41, v[76:79], %[ob1] sc1\n\t" \
        "v_mfma_f32_16x16x32_bf16 v[92:95], v[128:131], v[208:211], v[92:95]\n\t" \
        "v_mfma_f32_16x16x32_bf16 v[96:99], v[132:135], v[208:211], v[96:99]\n\t" \
        "v_mfma_f32_16x16x32_bf16 v[92:95], v[152:155], v[224:227], v[92:95]\n\t" \
        "s_nop 0\n\t" \
        "v_cvt_pk_bf16_f32 v84, v84, v85\n\t" \
        "v_cvt_pk_bf16_f32 v85, v86, v87\n\t" \
        "v_mfma_f32_16x16x32_bf16 v[96:99], v[156:159], v[224:227], v[96:99]\n\t" \
        "v_cvt_pk_bf16_f32 v86, v88, v89\n\t" \
        "v_cvt_pk_bf16_f32 v87, v90, v91\n\t" \
        "v_mfma_f32_16x16x32_bf16 v[92:95], v[176:179], v[240:243], v[92:95]\n\t" \
        "v_permlane16_swap_b32 v84, v86\n\t" \
        "v_permlane16_swap_b32 v85, v87\n\t" \
        "v_mfma_f32_16x16x32_bf16 v[96:99], v[180:183], v[240:243], v[96:99]\n\t" \
        "global_store_dwordx4 v42, v[84:87], %[ob1] sc1\n\t" \
        "v_mfma_f32_16x16x32_bf16 v[100:103], v[136:139], v[208:211], v[100:103]\n\t" \
        "v_mfma_f32_16x16x32_bf16 v[104:107], v[140:143], v[208:211], v[104:107]\n\t" \
        "v_mfma_f32_16x16x32_bf16 v[100:103], v[160:163], v[224:227], v[100:103]\n\t" \
        "s_nop 0\n\t" \
        "v_cvt_pk_bf16_f32 v92, v92, v93\n\t" \
        "v_cvt_pk_bf16_f32 v93, v94, v95\n\t" \
        "v_mfma_f32_16x16x32_bf16 v[104:107], v[164:167], v[224:227], v[104:107]\n\t" \
        "v_cvt_pk_bf16_f32 v94, v96, v97\n\t" \
        "v_cvt_pk_bf16_f32 v95, v98, v99\n\t" \
        "v_mfma_f32_16x16x32_bf16 v[100:103], v[184:187], v[240:243], v[100:103]\n\t" \
        "v_permlane16_swap_b32 v92, v94\n\t" \
        "v_permlane16_swap_b32 v93, v95\n\t" \
        "v_mfma_f32_16x16x32_bf16 v[104:107], v[188:191], v[240:243], v[104:107]\n\t" \
        "global_store_dwordx4 v40, v[92:95], %[ob2] sc1\n\t" \
        "v_mfma_f32_16x16x32_bf16 v[108:111], v[144:147], v[208:211], v[108:111]\n\t" \
        "v_mfma_f32_16x16x32_bf16 v[112:115], v[148:151], v[208:211], v[112:115]\n\t" \
        "v_mfma_f32_16x16x32_bf16 v[108:111], v[168:171], v[224:227], v[108:111]\n\t" \
        "s_nop 0\n\t" \
        "v_cvt_pk_bf16_f32 v100, v100, v101\n\t" \
        "v_cvt_pk_bf16_f32 v101, v102, v103\n\t" \
        "v_mfma_f32_16x16x32_bf16 v[112:115], v[172:175], v[224:227], v[112:115]\n\t" \
        "v_cvt_pk_bf16_f32 v102, v104, v105\n\t" \
        "v_cvt_pk_bf16_f32 v103, v106, v107\n\t" \
        "v_mfma_f32_16x16x32_bf16 v[108:111], v[192:195], v[240:243], v[108:111]\n\t" \
        "v_permlane16_swap_b32 v100, v102\n\t" \
        "v_permlane16_swap_b32 v101, v103\n\t" \
        "v_mfma_f32_16x16x32_bf16 v[112:115], v[196:199], v[240:243], v[112:115]\n\t" \
        "global_store_dwordx4 v41, v[100:103], %[ob2] sc1\n\t" \
        "s_nop 3\n\t" \
        "v_cvt_pk_bf16_f32 v108, v108, v109\n\t" \
        "v_cvt_pk_bf16_f32 v109, v110, v111\n\t" \
        "s_nop 0\n\t" \
        "v_cvt_pk_bf16_f32 v110, v112, v113\n\t" \
        "v_cvt_pk_bf16_f32 v111, v114, v115\n\t" \
        "s_nop 0\n\t" \
        "v_permlane16_swap_b32 v108, v110\n\t" \
        "v_permlane16_swap_b32 v109, v111\n\t" \
        "global_store_dwordx4 v42, v[108:111], %[ob2] sc1\n\t" \
        "s_branch .Lc_end%=\n\t" \
        ".La_l1e%=:\n\t" \
        "ds_read_b64_tr_b16 v[144:145], %[wb4] offset:0\n\t" \
        "ds_read_b64_tr_b16 v[146:147], %[wb4] offset:768\n\t" \
        "ds_read_b64_tr_b16 v[148:149], %[wb5] offset:0\n\t" \
        "ds_read_b64_tr_b16 v[150:151], %[wb5] offset:768\n\t" \
        "ds_read_b64_tr_b16 v[168:169], %[wb4] offset:6144\n\t" \
        "ds_read_b64_tr_b16 v[170:171], %[wb4] offset:6912\n\t" \
        "ds_read_b64_tr_b16 v[172:173], %[wb5] offset:6144\n\t" \
        "ds_read_b64_tr_b16 v[174:175], %[wb5] offset:6912\n\t" \
        "ds_read_b64_tr_b16 v[192:193], %[wb4] offset:12288\n\t" \
        "ds_read_b64_tr_b16 v[194:195], %[wb4] offset:13056\n\t" \
        "ds_read_b64_tr_b16 v[196:197], %[wb5] offset:12288\n\t" \
        "ds_read_b64_tr_b16 v[198:199], %[wb5] offset:13056\n\t" \
        "ds_read_b128 v[204:207], v31 offset:0\n\t" \
        "ds_read_b128 v[220:223], v31 offset:64\n\t" \
        "ds_read_b128 v[236:239], v31 offset:128\n\t" \
        "ds_read_b128 v[208:211], v32 offset:0\n\t" \
        "ds_read_b128 v[224:227], v32 offset:64\n\t" \
        "ds_read_b128 v[240:243], v32 offset:128\n\t" \
        "s_branch .La_l1%=\n\t" \
        ".Lb_start%=:\n\t" \
        "s_waitcnt lgkmcnt(0)\n\t" \
        "v_or3_b32 v40, v40, v41, v42\n\t" \
        "v_or_b32 v40, v40, v43\n\t" \
        "s_nop 0\n\t" \
        "v_readfirstlane_b32 %[ab], v40\n\t" \
        "v_mov_b32 v40, 0\n\t" \
        "v_mov_b32 v41, 0\n\t" \
        "v_mov_b32 v42, 0\n\t" \
        "v_mov_b32 v43, 0\n\t" \
        "s_cmp_eq_u32 %[ab], 0\n\t" \
        "s_cbranch_scc1 .Lb_go%=\n\t" \
        "s_and_b32 %[ab], %[ab], 2\n\t" \
        "s_cbranch_scc1 .Lc_end%=\n\t" \
        "s_barrier\n\t" \
        "s_branch .Lb_go%=\n\t" \
        ".Lb_top%=:\n\t" \
        "s_waitcnt vmcnt(0) lgkmcnt(0)\n\t" \
        "s_barrier\n\t" \
        ".Lb_go%=:\n\t" \
        "v_mfma_f32_16x16x32_bf16 v[92:95], v[232:235], v[124:127], 0\n\t" \
        "ds_read_b128 v[36:39], %[taddr]\n\t" \
        "ds_read_b64 v[34:35], %[waddr]\n\t" \
        "v_mfma_f32_16x16x32_bf16 v[96:99], v[236:239], v[124:127], 0\n\t" \
        "ds_read_b64_tr_b16 v[128:129], %[wb0] offset:0\n\t" \
        "ds_read_b64_tr_b16 v[130:131], %[wb0] offset:768\n\t" \
        "global_load_lds_dwordx4 %[wo0], s[90:91] offset:0\n\t" \
        "v_mfma_f32_16x16x32_bf16 v[100:103], v[240:243], v[40:43], 0\n\t" \
        "ds_read_b64_tr_b16 v[132:133], %[wb1] offset:0\n\t" \
        "global_load_lds_dwordx4 %[wo1], s[90:91] offset:1024\n\t" \
        "v_mfma_f32_16x16x32_bf16 v[104:107], v[244:247], v[40:43], 0\n\t" \
        "ds_read_b64_tr_b16 v[134:135], %[wb1] offset:768\n\t" \
        "global_load_lds_dwordx4 %[wo2], s[90:91] offset:2048\n\t" \
        "v_mfma_f32_16x16x32_bf16 v[44:47], v[152:155], v[116:119], v[44:47]\n\t" \
        "ds_read_b64_tr_b16 v[136:137], %[wb2] offset:0\n\t" \
        "global_load_lds_dwordx4 %[wo3], s[90:91] offset:3072\n\t" \
        "v_mfma_f32_16x16x32_bf16 v[68:71], v[152:155], v[120:123], v[68:71]\n\t" \
        "ds_read_b64_tr_b16 v[138:139], %[wb2] offset:768\n\t" \
        "v_cvt_pk_bf16_f32 v248, v92, v93\n\t" \
        "v_cvt_pk_bf16_f32 v249, v94, v95\n\t" \
        "v_mfma_f32_16x16x32_bf16 v[48:51], v[156:159], v[116:119], v[48:51]\n\t" \
        "s_waitcnt lgkmcnt(7)\n\t" \
        "v_xor_b32 v30, v36, %[kq]\n\t" \
        "v_xor_b32 v31, v37, %[kq]\n\t" \
        "v_cvt_pk_bf16_f32 v250, v96, v97\n\t" \
        "v_mfma_f32_16x16x32_bf16 v[72:75], v[156:159], v[120:123], v[72:75]\n\t" \
        "v_xor_b32 v32, v38, %[kq]\n\t" \
        "v_xor_b32 v33, v39, %[kq]\n\t" \
        "v_cvt_pk_bf16_f32 v251, v98, v99\n\t" \
        "ds_read_b64_tr_b16 v[200:201], v30 offset:0\n\t" \
        "v_mfma_f32_16x16x32_bf16 v[52:55], v[160:163], v[116:119], v[52:55]\n\t" \
        "v_cvt_pk_bf16_f32 v252, v100, v101\n\t" \
        "v_cvt_pk_bf16_f32 v253, v102, v103\n\t" \
        "ds_read_b64_tr_b16 v[202:203], v31 offset:0\n\t" \
        "v_mfma_f32_16x16x32_bf16 v[76:79], v[160:163], v[120:123], v[76:79]\n\t" \
        "v_cvt_pk_bf16_f32 v254, v104, v105\n\t" \
        "ds_read_b64_tr_b16 v[204:205], v30 offset:8\n\t" \
        "v_mfma_f32_16x16x32_bf16 v[56:59], v[164:167], v[116:119], v[56:59]\n\t" \
        "v_cvt_pk_bf16_f32 v255, v106, v107\n\t" \
        "ds_read_b64_tr_b16 v[206:207], v31 offset:8\n\t" \
        "v_mfma_f32_16x16x32_bf16 v[80:83], v[164:167], v[120:123], v[80:83]\n\t" \
        "ds_read_b64_tr_b16 v[208:209], v32 offset:0\n\t" \
        "s_waitcnt lgkmcnt(11)\n\t" \
        "v_and_b32 v124, v34, %[sm0]\n\t" \
        "v_and_b32 v125, v34, %[sm1]\n\t" \
        "v_mfma_f32_16x16x32_bf16 v[60:63], v[168:171], v[116:119], v[60:63]\n\t" \
        "ds_read_b64_tr_b16 v[210:211], v33 offset:0\n\t" \
        "v_and_b32 v126, v34, %[sm2]\n\t" \
        "v_and_b32 v127, v34, %[sm3]\n\t" \
        "v_mfma_f32_16x16x32_bf16 v[84:87], v[168:171], v[120:123], v[84:87]\n\t" \
        "ds_read_b64_tr_b16 v[212:213], v32 offset:8\n\t" \
        "v_and_b32 v40, v35, %[sm0]\n\t" \
        "v_mfma_f32_16x16x32_bf16 v[64:67], v[172:175], v[116:119], v[64:67]\n\t" \
        "ds_read_b64_tr_b16 v[214:215], v33 offset:8\n\t" \
        "v_and_b32 v41, v35, %[sm1]\n\t" \
        "v_and_b32 v42, v35, %[sm2]\n\t" \
        "v_mfma_f32_16x16x32_bf16 v[88:91], v[172:175], v[120:123], v[88:91]\n\t" \
        "v_and_b32 v43, v35, %[sm3]\n\t" \
        "s_mov_b32 exec_lo, 0\n\t" \
        "global_load_lds_dwordx4 %[wo4], s[90:91] offset:3584\n\t" \
        "s_mov_b32 exec_lo, -1\n\t" \
        "v_mfma_f32_16x16x32_bf16 v[44:47], v[176:179], v[248:251], v[44:47]\n\t" \
        "ds_read_b64_tr_b16 v[216:217], v30 offset:64\n\t" \
        "ds_read_b64_tr_b16 v[218:219], v31 offset:64\n\t" \
        "s_sub_u32 %[slot], 0, %[slot]\n\t" \
        "v_mfma_f32_16x16x32_bf16 v[68:71], v[176:179], v[252:255], v[68:71]\n\t" \
        "ds_read_b64_tr_b16 v[220:221], v30 offset:72\n\t" \
        "v_add_u32 %[taddr], 0x100, %[taddr]\n\t" \
        "v_mfma_f32_16x16x32_bf16 v[48:51], v[180:183], v[248:251], v[48:51]\n\t" \
        "ds_read_b64_tr_b16 v[222:223], v31 offset:72\n\t" \
        "ds_read_b64_tr_b16 v[224:225], v32 offset:64\n\t" \
        "s_sub_u32 %[cnt], %[cnt], 1\n\t" \
        "v_mfma_f32_16x16x32_bf16 v[72:75], v[180:183], v[252:255], v[72:75]\n\t" \
        "ds_read_b64_tr_b16 v[226:227], v33 offset:64\n\t" \
        "v_add_u32 %[waddr], 0x80, %[waddr]\n\t" \
        "s_waitcnt lgkmcnt(10)\n\t" \
        "v_mfma_f32_16x16x32_bf16 v[92:95], v[200:203], v[124:127], 0\n\t" \
        "ds_read_b64_tr_b16 v[228:229], v32 offset:72\n\t" \
        "ds_read_b64_tr_b16 v[230:231], v33 offset:72\n\t" \
        "s_sub_u32 m0, %[wsum], m0\n\t" \
        "v_mfma_f32_16x16x32_bf16 v[96:99], v[204:207], v[124:127], 0\n\t" \
        "ds_read_b64_tr_b16 v[140:141], %[wb3] offset:0\n\t" \
        "s_cmp_gt_u32 %[cnt], 1\n\t" \
        "s_waitcnt lgkmcnt(9)\n\t" \
        "v_mfma_f32_16x16x32_bf16 v[100:103], v[208:211], v[40:43], 0\n\t" \
        "ds_read_b64_tr_b16 v[142:143], %[wb3] offset:768\n\t" \
        "ds_read_b64_tr_b16 v[144:145], %[wb4] offset:0\n\t" \
        "s_cselect_b32 %[ab], 18432, 0\n\t" \
        "v_mfma_f32_16x16x32_bf16 v[104:107], v[212:215], v[40:43], 0\n\t" \
        "ds_read_b64_tr_b16 v[146:147], %[wb4] offset:768\n\t" \
        "s_sub_u32 s90, s90, %[ab]\n\t" \
        "v_mfma_f32_16x16x32_bf16 v[52:55], v[184:187], v[248:251], v[52:55]\n\t" \
        "ds_read_b64_tr_b16 v[148:149], %[wb5] offset:0\n\t" \
        "s_subb_u32 s91, s91, 0\n\t" \
        "v_mfma_f32_16x16x32_bf16 v[76:79], v[184:187], v[252:255], v[76:79]\n\t" \
        "ds_read_b64_tr_b16 v[150:151], %[wb5] offset:768\n\t" \
        "ds_read_b64_tr_b16 v[152:153], %[wb0] offset:6144\n\t" \
        "v_cvt_pk_bf16_f32 v108, v92, v93\n\t" \
        "v_cvt_pk_bf16_f32 v109, v94, v95\n\t" \
        "v_mfma_f32_16x16x32_bf16 v[56:59], v[188:191], v[248:251], v[56:59]\n\t" \
        "ds_read_b64_tr_b16 v[154:155], %[wb0] offset:6912\n\t" \
        "v_cvt_pk_bf16_f32 v110, v96, v97\n\t" \
        "v_mfma_f32_16x16x32_bf16 v[80:83], v[188:191], v[252:255], v[80:83]\n\t" \
        "ds_read_b64_tr_b16 v[156:157], %[wb1] offset:6144\n\t" \
        "ds_read_b64_tr_b16 v[158:159], %[wb1] offset:6912\n\t" \
        "v_cvt_pk_bf16_f32 v111, v98, v99\n\t" \
        "v_mfma_f32_16x16x32_bf16 v[60:63], v[192:195], v[248:251], v[60:63]\n\t" \
        "ds_read_b64_tr_b16 v[160:161], %[wb2] offset:6144\n\t" \
        "v_cvt_pk_bf16_f32 v112, v100, v101\n\t" \
        "v_mfma_f32_16x16x32_bf16 v[84:87], v[192:195], v[252:255], v[84:87]\n\t" \
        "ds_read_b64_tr_b16 v[162:163], %[wb2] offset:6912\n\t" \
        "ds_read_b64_tr_b16 v[164:165], %[wb3] offset:6144\n\t" \
        "v_cvt_pk_bf16_f32 v113, v102, v103\n\t" \
        "v_mfma_f32_16x16x32_bf16 v[64:67], v[196:199], v[248:251], v[64:67]\n\t" \
        "ds_read_b64_tr_b16 v[166:167], %[wb3] offset:6912\n\t" \
        "v_cvt_pk_bf16_f32 v114, v104, v105\n\t" \
        "v_mfma_f32_16x16x32_bf16 v[88:91], v[196:199], v[252:255], v[88:91]\n\t" \
        "ds_read_b64_tr_b16 v[168:169], %[wb4] offset:6144\n\t" \
        "v_cvt_pk_bf16_f32 v115, v106, v107\n\t" \
        "v_mfma_f32_16x16x32_bf16 v[44:47], v[128:131], v[108:111], v[44:47]\n\t" \
        "ds_read_b64_tr_b16 v[232:233], v30 offset:128\n\t" \
        "ds_read_b64_tr_b16 v[234:235], v31 offset:128\n\t" \
        "s_waitcnt lgkmcnt(14)\n\t" \
        "v_mfma_f32_16x16x32_bf16 v[92:95], v[216:219], v[124:127], 0\n\t" \
        "ds_read_b64_tr_b16 v[236:237], v30 offset:136\n\t" \
        "ds_read_b64_tr_b16 v[238:239], v31 offset:136\n\t" \
        "v_mfma_f32_16x16x32_bf16 v[96:99], v[220:223], v[124:127], 0\n\t" \
        "ds_read_b64_tr_b16 v[240:241], v32 offset:128\n\t" \
        "v_mfma_f32_16x16x32_bf16 v[100:103], v[224:227], v[40:43], 0\n\t" \
        "ds_read_b64_tr_b16 v[242:243], v33 offset:128\n\t" \
        "ds_read_b64_tr_b16 v[244:245], v32 offset:136\n\t" \
        "v_mfma_f32_16x16x32_bf16 v[104:107], v[228:231], v[40:43], 0\n\t" \
        "ds_read_b64_tr_b16 v[246:247], v33 offset:136\n\t" \
        "ds_read_b64_tr_b16 v[170:171], %[wb4] offset:6912\n\t" \
        "v_mfma_f32_16x16x32_bf16 v[68:71], v[128:131], v[112:115], v[68:71]\n\t" \
        "ds_read_b64_tr_b16 v[172:173], %[wb5] offset:6144\n\t" \
        "v_mfma_f32_16x16x32_bf16 v[48:51], v[132:135], v[108:111], v[48:51]\n\t" \
        "ds_read_b64_tr_b16 v[174:175], %[wb5] offset:6912\n\t" \
        "ds_read_b64_tr_b16 v[176:177], %[wb0] offset:12288\n\t" \
        "v_cvt_pk_bf16_f32 v116, v92, v93\n\t" \
        "v_cvt_pk_bf16_f32 v117, v94, v95\n\t" \
        "v_mfma_f32_16x16x32_bf16 v[72:75], v[132:135], v[112:115], v[72:75]\n\t" \
        "ds_read_b64_tr_b16 v[178:179], %[wb0] offset:13056\n\t" \
        "ds_read_b64_tr_b16 v[180:181], %[wb1] offset:12288\n\t" \
        "v_cvt_pk_bf16_f32 v118, v96, v97\n\t" \
        "v_mfma_f32_16x16x32_bf16 v[52:55], v[136:139], v[108:111], v[52:55]\n\t" \
        "ds_read_b64_tr_b16 v[182:183], %[wb1] offset:13056\n\t" \
        "v_cvt_pk_bf16_f32 v119, v98, v99\n\t" \
        "v_add_u32 %[wb0], %[slot], %[wb0]\n\t" \
        "v_mfma_f32_16x16x32_bf16 v[76:79], v[136:139], v[112:115], v[76:79]\n\t" \
        "ds_read_b64_tr_b16 v[184:185], %[wb2] offset:12288\n\t" \
        "ds_read_b64_tr_b16 v[186:187], %[wb2] offset:13056\n\t" \
        "v_cvt_pk_bf16_f32 v120, v100, v101\n\t" \
        "v_add_u32 %[wb1], %[slot], %[wb1]\n\t" \
        "v_mfma_f32_16x16x32_bf16 v[56:59], v[140:143], v[108:111], v[56:59]\n\t" \
        "ds_read_b64_tr_b16 v[188:189], %[wb3] offset:12288\n\t" \
        "ds_read_b64_tr_b16 v[190:191], %[wb3] offset:13056\n\t" \
        "v_cvt_pk_bf16_f32 v121, v102, v103\n\t" \
        "v_add_u32 %[wb2], %[slot], %[wb2]\n\t" \
        "v_mfma_f32_16x16x32_bf16 v[80:83], v[140:143], v[112:115], v[80:83]\n\t" \
        "ds_read_b64_tr_b16 v[192:193], %[wb4] offset:12288\n\t" \
        "v_cvt_pk_bf16_f32 v122, v104, v105\n\t" \
        "v_add_u32 %[wb3], %[slot], %[wb3]\n\t" \
        "s_waitcnt lgkmcnt(14)\n\t" \
        "v_mfma_f32_16x16x32_bf16 v[60:63], v[144:147], v[108:111], v[60:63]\n\t" \
        "ds_read_b64_tr_b16 v[194:195], %[wb4] offset:13056\n\t" \
        "ds_read_b64_tr_b16 v[196:197], %[wb5] offset:12288\n\t" \
        "v_cvt_pk_bf16_f32 v123, v106, v107\n\t" \
        "v_mfma_f32_16x16x32_bf16 v[84:87], v[144:147], v[112:115], v[84:87]\n\t" \
        "ds_read_b64_tr_b16 v[198:199], %[wb5] offset:13056\n\t" \
        "v_add_u32 %[wb4], %[slot], %[wb4]\n\t" \
        "v_mfma_f32_16x16x32_bf16 v[64:67], v[148:151], v[108:111], v[64:67]\n\t" \
        "v_add_u32 %[wb5], %[slot], %[wb5]\n\t" \
        "v_mfma_f32_16x16x32_bf16 v[88:91], v[148:151], v[112:115], v[88:91]\n\t" \
        "s_cmp_lg_u32 %[cnt], 0\n\t" \
        "s_cbranch_scc1 .Lb_top%=\n\t" \
        "s_waitcnt lgkmcnt(0)\n\t" \
        "v_mfma_f32_16x16x32_bf16 v[92:95], v[232:235], v[124:127], 0\n\t" \
        "v_mfma_f32_16x16x32_bf16 v[96:99], v[236:239], v[124:127], 0\n\t" \
        "v_mfma_f32_16x16x32_bf16 v[100:103], v[240:243], v[40:43], 0\n\t" \
        "v_mfma_f32_16x16x32_bf16 v[104:107], v[244:247], v[40:43], 0\n\t" \
        "v_mfma_f32_16x16x32_bf16 v[44:47], v[152:155], v[116:119], v[44:47]\n\t" \
        "v_mfma_f32_16x16x32_bf16 v[68:71], v[152:155], v[120:123], v[68:71]\n\t" \
        "v_mfma_f32_16x16x32_bf16 v[48:51], v[156:159], v[116:119], v[48:51]\n\t" \
        "v_mfma_f32_16x16x32_bf16 v[72:75], v[156:159], v[120:123], v[72:75]\n\t" \
        "v_mfma_f32_16x16x32_bf16 v[52:55], v[160:163], v[116:119], v[52:55]\n\t" \
        "v_mfma_f32_16x16x32_bf16 v[76:79], v[160:163], v[120:123], v[76:79]\n\t" \
        "v_cvt_pk_bf16_f32 v232, v92, v93\n\t" \
        "v_cvt_pk_bf16_f32 v233, v94, v95\n\t" \
        "v_mfma_f32_16x16x32_bf16 v[56:59], v[164:167], v[116:119], v[56:59]\n\t" \
        "v_cvt_pk_bf16_f32 v234, v96, v97\n\t" \
        "v_cvt_pk_bf16_f32 v235, v98, v99\n\t" \
        "v_mfma_f32_16x16x32_bf16 v[80:83], v[164:167], v[120:123], v[80:83]\n\t" \
        "v_cvt_pk_bf16_f32 v236, v100, v101\n\t" \
        "v_mfma_f32_16x16x32_bf16 v[60:63], v[168:171], v[116:119], v[60:63]\n\t" \
        "v_cvt_pk_bf16_f32 v237, v102, v103\n\t" \
        "v_cvt_pk_bf16_f32 v238, v104, v105\n\t" \
        "v_mfma_f32_16x16x32_bf16 v[84:87], v[168:171], v[120:123], v[84:87]\n\t" \
        "v_cvt_pk_bf16_f32 v239, v106, v107\n\t" \
        "v_mfma_f32_16x16x32_bf16 v[64:67], v[172:175], v[116:119], v[64:67]\n\t" \
        "v_mfma_f32_16x16x32_bf16 v[88:91], v[172:175], v[120:123], v[88:91]\n\t" \
        ".Lc_tail%=:\n\t" \
        "s_mov_b32 %[ab], 0\n\t" \
        "s_waitcnt vmcnt(0) lgkmcnt(0)\n\t" \
        "s_memrealtime %[st2]\n\t" \
        "s_memtime %[cy2]\n\t" \
        "s_waitcnt lgkmcnt(0)\n\t" \
        "v_mfma_f32_16x16x32_bf16 v[44:47], v[176:179], v[232:235], v[44:47]\n\t" \
        "s_lshl_b32 %[slot], %[par], 2\n\t" \
        "v_add_u32 v30, %[slot], %[ol]\n\t" \
        "v_mfma_f32_16x16x32_bf16 v[48:51], v[180:183], v[232:235], v[48:51]\n\t" \
        "s_sub_u32 %[slot], 0x80, %[slot]\n\t" \
        "v_xor_b32 v31, %[par], %[ol]\n\t" \
        "v_mfma_f32_16x16x32_bf16 v[52:55], v[184:187], v[232:235], v[52:55]\n\t" \
        "v_add_u32 v32, %[slot], %[ol]\n\t" \
        "v_add_u32 v31, 64, v31\n\t" \
        "v_mfma_f32_16x16x32_bf16 v[56:59], v[188:191], v[232:235], v[56:59]\n\t" \
        "v_mfma_f32_16x16x32_bf16 v[60:63], v[192:195], v[232:235], v[60:63]\n\t" \
        "v_mfma_f32_16x16x32_bf16 v[64:67], v[196:199], v[232:235], v[64:67]\n\t" \
        "v_mfma_f32_16x16x32_bf16 v[68:71], v[176:179], v[236:239], v[68:71]\n\t" \
        "v_mfma_f32_16x16x32_bf16 v[72:75], v[180:183], v[236:239], v[72:75]\n\t" \
        "v_mfma_f32_16x16x32_bf16 v[76:79], v[184:187], v[236:239], v[76:79]\n\t" \
        "v_mfma_f32_16x16x32_bf16 v[80:83], v[188:191], v[236:239], v[80:83]\n\t" \
        "v_mfma_f32_16x16x32_bf16 v[84:87], v[192:195], v[236:239], v[84:87]\n\t" \
        "v_mfma_f32_16x16x32_bf16 v[88:91], v[196:199], v[236:239], v[88:91]\n\t" \
        "s_bitcmp1_b32 %[vm], 6\n\t" \
        "s_cbranch_scc0 .Lc_h1%=\n\t" \
        "s_nop 2\n\t" \
        "v_add_f32 v44, v44, v68\n\t" \
        "v_add_f32 v45, v45, v69\n\t" \
        "v_add_f32 v46, v46, v70\n\t" \
        "v_add_f32 v47, v47, v71\n\t" \
        "v_add_f32 v48, v48, v72\n\t" \
        "v_add_f32 v49, v49, v73\n\t" \
        "v_add_f32 v50, v50, v74\n\t" \
        "v_add_f32 v51, v51, v75\n\t" \
        "v_add_f32 v52, v52, v76\n\t" \
        "v_add_f32 v53, v53, v77\n\t" \
        "v_add_f32 v54, v54, v78\n\t" \
        "v_add_f32 v55, v55, v79\n\t" \
        "v_add_f32 v56, v56, v80\n\t" \
        "v_add_f32 v57, v57, v81\n\t" \
        "v_add_f32 v58, v58, v82\n\t" \
        "v_add_f32 v59, v59, v83\n\t" \
        "v_add_f32 v60, v60, v84\n\t" \
        "v_add_f32 v61, v61, v85\n\t" \
        "v_add_f32 v62, v62, v86\n\t" \
        "v_add_f32 v63, v63, v87\n\t" \
        "v_add_f32 v64, v64, v88\n\t" \
        "v_add_f32 v65, v65, v89\n\t" \
        "v_add_f32 v66, v66, v90\n\t" \
        "v_add_f32 v67, v67, v91\n\t" \
        ".Lc_h1%=:\n\t" \
        "s_bitcmp1_b32 %[vm], 0\n\t" \
        "s_cbranch_scc0 .Lc_end%=\n\t" \
        "v_cvt_pk_bf16_f32 v44, v44, v45\n\t" \
        "v_cvt_pk_bf16_f32 v45, v46, v47\n\t" \
        "v_cvt_pk_bf16_f32 v46, v48, v49\n\t" \
        "v_cvt_pk_bf16_f32 v47, v50, v51\n\t" \
        "s_nop 0\n\t" \
        "v_permlane16_swap_b32 v44, v46\n\t" \
        "v_permlane16_swap_b32 v45, v47\n\t" \
        "global_store_dwordx4 v30, v[44:47], %[ob0] sc1\n\t" \
        "v_cvt_pk_bf16_f32 v52, v52, v53\n\t" \
        "v_cvt_pk_bf16_f32 v53, v54, v55\n\t" \
        "v_cvt_pk_bf16_f32 v54, v56, v57\n\t" \
        "v_cvt_pk_bf16_f32 v55, v58, v59\n\t" \
        "s_nop 0\n\t" \
        "v_permlane16_swap_b32 v52, v54\n\t" \
        "v_permlane16_swap_b32 v53, v55\n\t" \
        "global_store_dwordx4 v31, v[52:55], %[ob0] sc1\n\t" \
        "v_cvt_pk_bf16_f32 v60, v60, v61\n\t" \
        "v_cvt_pk_bf16_f32 v61, v62, v63\n\t" \
        "v_cvt_pk_bf16_f32 v62, v64, v65\n\t" \
        "v_cvt_pk_bf16_f32 v63, v66, v67\n\t" \
        "s_nop 0\n\t" \
        "v_permlane16_swap_b32 v60, v62\n\t" \
        "v_permlane16_swap_b32 v61, v63\n\t" \
        "global_store_dwordx4 v32, v[60:63], %[ob0] sc1\n\t" \
        "s_bitcmp1_b32 %[vm], 1\n\t" \
        "s_cbranch_scc0 .Lc_end%=\n\t" \
        "v_cvt_pk_bf16_f32 v68, v68, v69\n\t" \
        "v_cvt_pk_bf16_f32 v69, v70, v71\n\t" \
        "v_cvt_pk_bf16_f32 v70, v72, v73\n\t" \
        "v_cvt_pk_bf16_f32 v71, v74, v75\n\t" \
        "s_nop 0\n\t" \
        "v_permlane16_swap_b32 v68, v70\n\t" \
        "v_permlane16_swap_b32 v69, v71\n\t" \
        "global_store_dwordx4 v30, v[68:71], %[ob1] sc1\n\t" \
        "v_cvt_pk_bf16_f32 v76, v76, v77\n\t" \
        "v_cvt_pk_bf16_f32 v77, v78, v79\n\t" \
        "v_cvt_pk_bf16_f32 v78, v80, v81\n\t" \
        "v_cvt_pk_bf16_f32 v79, v82, v83\n\t" \
        "s_nop 0\n\t" \
        "v_permlane16_swap_b32 v76, v78\n\t" \
        "v_permlane16_swap_b32 v77, v79\n\t" \
        "global_store_dwordx4 v31, v[76:79], %[ob1] sc1\n\t" \
        "v_cvt_pk_bf16_f32 v84, v84, v85\n\t" \
        "v_cvt_pk_bf16_f32 v85, v86, v87\n\t" \
        "v_cvt_pk_bf16_f32 v86, v88, v89\n\t" \
        "v_cvt_pk_bf16_f32 v87, v90, v91\n\t" \
        "s_nop 0\n\t" \
        "v_permlane16_swap_b32 v84, v86\n\t" \
        "v_permlane16_swap_b32 v85, v87\n\t" \
        "global_store_dwordx4 v32, v[84:87], %[ob1] sc1\n\t" \
        ".Lc_end%=:\n\t" \
        "s_memrealtime %[st3]\n\t" \
        "s_waitcnt vmcnt(0) lgkmcnt(0)\n\t" \
        "s_memrealtime %[st4]\n\t" \
        "s_waitcnt lgkmcnt(0)\n\t" \
        "s_waitcnt lgkmcnt(0)\n\t" \
        "s_nop 1\n\t"
#define COMPUTE_ASM_CLOBBERS "s90", "s91", "v30", "v31", "v32", "v33", "v34", "v35", "v36", "v37", "v38", "v39", "v40", "v41", "v42", "v43", "v44", "v45", "v46", "v47", "v48", "v49", "v50", "v51", "v52", "v53", "v54", "v55", "v56", "v57", "v58", "v59", "v60", "v61", "v62", "v63", "v64", "v65", "v66", "v67", "v68", "v69", "v70", "v71", "v72", "v73", "v74", "v75", "v76", "v77", "v78", "v79", "v80", "v81", "v82", "v83", "v84", "v85", "v86", "v87", "v88", "v89", "v90", "v91", "v92", "v93", "v94", "v95", "v96", "v97", "v98", "v99", "v100", "v101", "v102", "v103", "v104", "v105", "v106", "v107", "v108", "v109", "v110", "v111", "v112", "v113", "v114", "v115", "v116", "v117", "v118", "v119", "v120", "v121", "v122", "v123", "v124", "v125", "v126", "v127", "v128", "v129", "v130", "v131", "v132", "v133", "v134", "v135", "v136", "v137", "v138", "v139", "v140", "v141", "v142", "v143", "v144", "v145", "v146", "v147", "v148", "v149", "v150", "v151", "v152", "v153", "v154", "v155", "v156", "v157", "v158", "v159", "v160", "v161", "v162", "v163", "v164", "v165", "v166", "v167", "v168", "v169", "v170", "v171", "v172", "v173", "v174", "v175", "v176", "v177", "v178", "v179", "v180", "v181", "v182", "v183", "v184", "v185", "v186", "v187", "v188", "v189", "v190", "v191", "v192", "v193", "v194", "v195", "v196", "v197", "v198", "v199", "v200", "v201", "v202", "v203", "v204", "v205", "v206", "v207", "v208", "v209", "v210", "v211", "v212", "v213", "v214", "v215", "v216", "v217", "v218", "v219", "v220", "v221", "v222", "v223", "v224", "v225", "v226", "v227", "v228", "v229", "v230", "v231", "v232", "v233", "v234", "v235", "v236", "v237", "v238", "v239", "v240", "v241", "v242", "v243", "v244", "v245", "v246", "v247", "v248", "v249", "v250", "v251", "v252", "v253", "v254", "v255"
#define HELPER_ASM_TEXT \
        "s_mov_b64 s[90:91], %[cnts]\n\t" \
        "ds_read_u16 v120, %[da]\n\t" \
        "ds_read_u16 v121, %[da] offset:8\n\t" \
        "ds_read_b32 v122, %[dw]\n\t" \
        "v_mov_b32 v156, %[flg]\n\t" \
        "s_mov_b32 %[e0], 0\n\t" \
        "s_mov_b32 %[dead], 0\n\t" \
        "s_mov_b32 %[dead1], 0\n\t" \
        "s_mov_b32 %[ub], 0\n\t" \
        "s_waitcnt lgkmcnt(0)\n\t" \
        "v_lshlrev_b32 v116, 5, v120\n\t" \
        "v_bitop3_b32 v116, v116, %[am], %[cx] bitop3:0x6a\n\t" \
        "v_xor_b32 v117, 8, v116\n\t" \
        "v_lshlrev_b32 v118, 5, v121\n\t" \
        "v_bitop3_b32 v118, v118, %[am], %[cx] bitop3:0x6a\n\t" \
        "v_xor_b32 v119, 8, v118\n\t" \
        "v_and_b32 v112, v122, %[sm0]\n\t" \
        "v_and_b32 v113, v122, %[sm1]\n\t" \
        "v_and_b32 v114, v122, %[sm2]\n\t" \
        "v_and_b32 v115, v122, %[sm3]\n\t" \
        "ds_read_u16 v120, %[da] offset:64\n\t" \
        "ds_read_u16 v121, %[da] offset:72\n\t" \
        "ds_read_b32 v122, %[dw] offset:128\n\t" \
        "s_waitcnt vmcnt(0) lgkmcnt(0)\n\t" \
        "s_barrier\n\t" \
        "ds_read_b128 v[152:155], v156\n\t" \
        "ds_read_b64_tr_b16 v[64:65], v116 offset:0\n\t" \
        "ds_read_b64_tr_b16 v[66:67], v118 offset:0\n\t" \
        "ds_read_b64_tr_b16 v[68:69], v117 offset:0\n\t" \
        "ds_read_b64_tr_b16 v[70:71], v119 offset:0\n\t" \
        "ds_read_b64_tr_b16 v[72:73], v116 offset:64\n\t" \
        "ds_read_b64_tr_b16 v[74:75], v118 offset:64\n\t" \
        "ds_read_b64_tr_b16 v[76:77], v117 offset:64\n\t" \
        "ds_read_b64_tr_b16 v[78:79], v119 offset:64\n\t" \
        "ds_read_b64_tr_b16 v[80:81], v116 offset:128\n\t" \
        "ds_read_b64_tr_b16 v[82:83], v118 offset:128\n\t" \
        "ds_read_b64_tr_b16 v[84:85], v117 offset:128\n\t" \
        "ds_read_b64_tr_b16 v[86:87], v119 offset:128\n\t" \
        "s_waitcnt lgkmcnt(12)\n\t" \
        "v_or3_b32 v152, v152, v153, v154\n\t" \
        "v_or_b32 v152, v152, v155\n\t" \
        "s_nop 0\n\t" \
        "v_readfirstlane_b32 %[af], v152\n\t" \
        "s_nop 0\n\t" \
        "s_bitcmp1_b32 %[af], 1\n\t" \
        "s_cbranch_scc1 .Lh_end%=\n\t" \
        "s_or_b32 s92, %[must], %[af]\n\t" \
        "s_cbranch_scc0 .Lh_bar%=\n\t" \
        ".Lh_prod%=:\n\t" \
        "s_add_u32 s92, %[e0], 16\n\t" \
        "s_cmp_ge_i32 s92, %[nent]\n\t" \
        "s_cbranch_scc1 .Lh_last%=\n\t" \
        "s_waitcnt lgkmcnt(10)\n\t" \
        "v_mfma_f32_16x16x32_bf16 v[88:91], v[64:67], v[112:115], 0\n\t" \
        "v_lshlrev_b32 v116, 5, v120\n\t" \
        "v_bitop3_b32 v116, v116, %[am], %[cx] bitop3:0x6a\n\t" \
        "v_xor_b32 v117, 8, v116\n\t" \
        "v_lshlrev_b32 v118, 5, v121\n\t" \
        "v_bitop3_b32 v118, v118, %[am], %[cx] bitop3:0x6a\n\t" \
        "v_xor_b32 v119, 8, v118\n\t" \
        "ds_read_u16 v120, %[da] offset:128\n\t" \
        "ds_read_u16 v121, %[da] offset:136\n\t" \
        "ds_read_b64_tr_b16 v[64:65], v116 offset:0\n\t" \
        "ds_read_b64_tr_b16 v[66:67], v118 offset:0\n\t" \
        "s_waitcnt lgkmcnt(12)\n\t" \
        "v_mfma_f32_16x16x32_bf16 v[92:95], v[68:71], v[112:115], 0\n\t" \
        "ds_read_b64_tr_b16 v[68:69], v117 offset:0\n\t" \
        "ds_read_b64_tr_b16 v[70:71], v119 offset:0\n\t" \
        "s_waitcnt lgkmcnt(12)\n\t" \
        "v_mfma_f32_16x16x32_bf16 v[96:99], v[72:75], v[112:115], 0\n\t" \
        "ds_read_b64_tr_b16 v[72:73], v116 offset:64\n\t" \
        "ds_read_b64_tr_b16 v[74:75], v118 offset:64\n\t" \
        "s_waitcnt lgkmcnt(12)\n\t" \
        "v_mfma_f32_16x16x32_bf16 v[100:103], v[76:79], v[112:115], 0\n\t" \
        "ds_read_b64_tr_b16 v[76:77], v117 offset:64\n\t" \
        "ds_read_b64_tr_b16 v[78:79], v119 offset:64\n\t" \
        "s_waitcnt lgkmcnt(12)\n\t" \
        "v_mfma_f32_16x16x32_bf16 v[104:107], v[80:83], v[112:115], 0\n\t" \
        "ds_read_b64_tr_b16 v[80:81], v116 offset:128\n\t" \
        "ds_read_b64_tr_b16 v[82:83], v118 offset:128\n\t" \
        "s_waitcnt lgkmcnt(12)\n\t" \
        "v_mfma_f32_16x16x32_bf16 v[108:111], v[84:87], v[112:115], 0\n\t" \
        "ds_read_b64_tr_b16 v[84:85], v117 offset:128\n\t" \
        "ds_read_b64_tr_b16 v[86:87], v119 offset:128\n\t" \
        "v_and_b32 v112, v122, %[sm0]\n\t" \
        "v_and_b32 v113, v122, %[sm1]\n\t" \
        "v_and_b32 v114, v122, %[sm2]\n\t" \
        "v_and_b32 v115, v122, %[sm3]\n\t" \
        "ds_read_b32 v122, %[dw] offset:256\n\t" \
        "s_bitcmp1_b32 %[two], 0\n\t" \
        "s_cbranch_scc1 .Lh_two%=\n\t" \
        ".Lh_back%=:\n\t" \
        "v_add_u32 v156, %[wo], %[wb]\n\t" \
        "v_xor_b32 v157, 8, v156\n\t" \
        "v_cvt_pk_bf16_f32 v88, v88, v89\n\t" \
        "v_cvt_pk_bf16_f32 v89, v90, v91\n\t" \
        "ds_write_b64 v156, v[88:89] offset:0\n\t" \
        "v_cvt_pk_bf16_f32 v92, v92, v93\n\t" \
        "v_cvt_pk_bf16_f32 v93, v94, v95\n\t" \
        "ds_write_b64 v157, v[92:93] offset:0\n\t" \
        "v_cvt_pk_bf16_f32 v96, v96, v97\n\t" \
        "v_cvt_pk_bf16_f32 v97, v98, v99\n\t" \
        "ds_write_b64 v156, v[96:97] offset:64\n\t" \
        "v_cvt_pk_bf16_f32 v100, v100, v101\n\t" \
        "v_cvt_pk_bf16_f32 v101, v102, v103\n\t" \
        "ds_write_b64 v157, v[100:101] offset:64\n\t" \
        "v_cvt_pk_bf16_f32 v104, v104, v105\n\t" \
        "v_cvt_pk_bf16_f32 v105, v106, v107\n\t" \
        "ds_write_b64 v156, v[104:105] offset:128\n\t" \
        "v_cvt_pk_bf16_f32 v108, v108, v109\n\t" \
        "v_cvt_pk_bf16_f32 v109, v110, v111\n\t" \
        "ds_write_b64 v157, v[108:109] offset:128\n\t" \
        "v_add_u32 %[wo], 0xc00, %[wo]\n\t" \
        "s_add_u32 %[e0], %[e0], 16\n\t" \
        "v_subrev_u32 v158, %[cap192], %[wo]\n\t" \
        "s_lshr_b32 %[two], %[two], 1\n\t" \
        "v_add_u32 %[da], 64, %[da]\n\t" \
        "v_add_u32 %[dw], 0x80, %[dw]\n\t" \
        "v_min_u32 %[wo], %[wo], v158\n\t" \
        "s_cmp_lt_i32 %[e0], %[must]\n\t" \
        "s_cbranch_scc1 .Lh_prod%=\n\t" \
        "s_cmp_ge_i32 %[e0], %[nent]\n\t" \
        "s_cbranch_scc1 .Lh_end%=\n\t" \
        ".Lh_bar%=:\n\t" \
        "s_waitcnt lgkmcnt(0)\n\t" \
        "s_cmp_eq_u32 %[af], 0\n\t" \
        "s_cbranch_scc1 .Lh_nobar%=\n\t" \
        "s_barrier\n\t" \
        ".Lh_nobar%=:\n\t" \
        "s_mov_b32 %[af], 1\n\t" \
        "s_add_u32 %[ub], %[ub], 1\n\t" \
        "s_add_u32 %[dead], %[dead1], %[capm]\n\t" \
        "s_mov_b32 %[dead1], %[must]\n\t" \
        "s_and_b32 s92, s90, 127\n\t" \
        "s_add_u32 %[must], %[must], s92\n\t" \
        "s_lshr_b64 s[90:91], s[90:91], 7\n\t" \
        "s_cmp_le_i32 %[e0], %[dead]\n\t" \
        "s_cbranch_scc1 .Lh_prod%=\n\t" \
        "s_branch .Lh_bar%=\n\t" \
        ".Lh_last%=:\n\t" \
        "s_waitcnt lgkmcnt(10)\n\t" \
        "v_mfma_f32_16x16x32_bf16 v[88:91], v[64:67], v[112:115], 0\n\t" \
        "s_waitcnt lgkmcnt(8)\n\t" \
        "v_mfma_f32_16x16x32_bf16 v[92:95], v[68:71], v[112:115], 0\n\t" \
        "s_waitcnt lgkmcnt(6)\n\t" \
        "v_mfma_f32_16x16x32_bf16 v[96:99], v[72:75], v[112:115], 0\n\t" \
        "s_waitcnt lgkmcnt(4)\n\t" \
        "v_mfma_f32_16x16x32_bf16 v[100:103], v[76:79], v[112:115], 0\n\t" \
        "s_waitcnt lgkmcnt(2)\n\t" \
        "v_mfma_f32_16x16x32_bf16 v[104:107], v[80:83], v[112:115], 0\n\t" \
        "s_waitcnt lgkmcnt(0)\n\t" \
        "v_mfma_f32_16x16x32_bf16 v[108:111], v[84:87], v[112:115], 0\n\t" \
        "s_bitcmp1_b32 %[two], 0\n\t" \
        "s_cbranch_scc0 .Lh_back%=\n\t" \
        ".Lh_two%=:\n\t" \
        "ds_read_u16 v156, %[da]\n\t" \
        "ds_read_u16 v158, %[da] offset:8\n\t" \
        "ds_read_b32 v127, %[dw] offset:4\n\t" \
        "s_waitcnt lgkmcnt(0)\n\t" \
        "v_lshrrev_b32 v124, 15, v156\n\t" \
        "v_lshlrev_b32 v156, 5, v156\n\t" \
        "v_bitop3_b32 v156, v156, %[am], %[cx] bitop3:0x6a\n\t" \
        "v_mul_u32_u24 v124, 3968, v124\n\t" \
        "v_add_u32 v156, v156, v124\n\t" \
        "v_xor_b32 v157, 8, v156\n\t" \
        "v_lshrrev_b32 v125, 15, v158\n\t" \
        "v_lshlrev_b32 v158, 5, v158\n\t" \
        "v_bitop3_b32 v158, v158, %[am], %[cx] bitop3:0x6a\n\t" \
        "v_mul_u32_u24 v125, 3968, v125\n\t" \
        "v_add_u32 v158, v158, v125\n\t" \
        "v_xor_b32 v159, 8, v158\n\t" \
        "ds_read_b64_tr_b16 v[128:129], v156 offset:0\n\t" \
        "ds_read_b64_tr_b16 v[130:131], v158 offset:0\n\t" \
        "ds_read_b64_tr_b16 v[132:133], v157 offset:0\n\t" \
        "ds_read_b64_tr_b16 v[134:135], v159 offset:0\n\t" \
        "ds_read_b64_tr_b16 v[136:137], v156 offset:64\n\t" \
        "ds_read_b64_tr_b16 v[138:139], v158 offset:64\n\t" \
        "ds_read_b64_tr_b16 v[140:141], v157 offset:64\n\t" \
        "ds_read_b64_tr_b16 v[142:143], v159 offset:64\n\t" \
        "ds_read_b64_tr_b16 v[144:145], v156 offset:128\n\t" \
        "ds_read_b64_tr_b16 v[146:147], v158 offset:128\n\t" \
        "ds_read_b64_tr_b16 v[148:149], v157 offset:128\n\t" \
        "ds_read_b64_tr_b16 v[150:151], v159 offset:128\n\t" \
        "v_and_b32 v124, v127, %[sm0]\n\t" \
        "v_and_b32 v125, v127, %[sm1]\n\t" \
        "v_and_b32 v126, v127, %[sm2]\n\t" \
        "v_and_b32 v127, v127, %[sm3]\n\t" \
        "s_nop 0\n\t" \
        "s_waitcnt lgkmcnt(10)\n\t" \
        "v_mfma_f32_16x16x32_bf16 v[88:91], v[128:131], v[124:127], v[88:91]\n\t" \
        "s_waitcnt lgkmcnt(8)\n\t" \
        "v_mfma_f32_16x16x32_bf16 v[92:95], v[132:135], v[124:127], v[92:95]\n\t" \
        "s_waitcnt lgkmcnt(6)\n\t" \
        "v_mfma_f32_16x16x32_bf16 v[96:99], v[136:139], v[124:127], v[96:99]\n\t" \
        "s_waitcnt lgkmcnt(4)\n\t" \
        "v_mfma_f32_16x16x32_bf16 v[100:103], v[140:143], v[124:127], v[100:103]\n\t" \
        "s_waitcnt lgkmcnt(2)\n\t" \
        "v_mfma_f32_16x16x32_bf16 v[104:107], v[144:147], v[124:127], v[104:107]\n\t" \
        "s_waitcnt lgkmcnt(0)\n\t" \
        "v_mfma_f32_16x16x32_bf16 v[108:111], v[148:151], v[124:127], v[108:111]\n\t" \
        "s_nop 7\n\t" \
        "s_branch .Lh_back%=\n\t" \
        ".Lh_end%=:\n\t" \
        "s_waitcnt lgkmcnt(0)\n\t"
#define HELPER_ASM_CLOBBERS "s90", "s91", "s92", "v64", "v65", "v66", "v67", "v68", "v69", "v70", "v71", "v72", "v73", "v74", "v75", "v76", "v77", "v78", "v79", "v80", "v81", "v82", "v83", "v84", "v85", "v86", "v87", "v88", "v89", "v90", "v91", "v92", "v93", "v94", "v95", "v96", "v97", "v98", "v99", "v100", "v101", "v102", "v103", "v104", "v105", "v106", "v107", "v108", "v109", "v110", "v111", "v112", "v113", "v114", "v115", "v116", "v117", "v118", "v119", "v120", "v121", "v122", "v123", "v124", "v125", "v126", "v127", "v128", "v129", "v130", "v131", "v132", "v133", "v134", "v135", "v136", "v137", "v138", "v139", "v140", "v141", "v142", "v143", "v144", "v145", "v146", "v147", "v148", "v149", "v150", "v151", "v152", "v153", "v154", "v155", "v156", "v157", "v158", "v159"

#define STAMP(i)
#define HT(var)
#define HACC(i, a, b)

__device__ __forceinline__ float bf_lo(unsigned u) { return __uint_as_float(u << 16); }
__device__ __forceinline__ float bf_hi(unsigned u) { return __uint_as_float(u & 0xffff0000u); }
__device__ __forceinline__ unsigned cvt_pk(float lo, float hi) {
    bf16x2 r = {(__bf16)lo, (__bf16)hi};
    unsigned u;
    __builtin_memcpy(&u, &r, 4);
    return u;
}
__device__ __forceinline__ f32x4 mfma16(u32x4 a, u32x4 b, f32x4 c) {
    bf16x8 A, B;
    __builtin_memcpy(&A, &a, 16);
    __builtin_memcpy(&B, &b, 16);
    return __builtin_amdgcn_mfma_f32_16x16x32_bf16(A, B, c, 0, 0, 0);
}
__device__ __forceinline__ u32x2 tr_read(unsigned a) {
    s16x4 v = __builtin_amdgcn_ds_read_tr16_b64_v4i16((lds_s16x4*)(uintptr_t)a);
    u32x2 u;
    __builtin_memcpy(&u, &v, 8);
    return u;
}
__device__ __forceinline__ u32x4 tr_frag(unsigned a0, unsigned a1) {
    u32x2 a = tr_read(a0), b = tr_read(a1);
    return (u32x4){a[0], a[1], b[0], b[1]};
}
__device__ __forceinline__ int smin(int a, int b) { int r; asm("s_min_i32 %0, %1, %2" : "=s"(r) : "s"(a), "s"(b) : "scc"); return r; }
__device__ __forceinline__ int smax(int a, int b) { int r; asm("s_max_i32 %0, %1, %2" : "=s"(r) : "s"(a), "s"(b) : "scc"); return r; }
__device__ __forceinline__ unsigned sneg(int v) { unsigned r; asm("s_ashr_i32 %0, %1, 31" : "=s"(r) : "s"(v) : "scc"); return r; }
#define UNI(v) __builtin_amdgcn_readfirstlane(v)
__device__ __forceinline__ unsigned cvt_pk1(float lo, float hi) {
    unsigned r;
    asm("v_cvt_pk_bf16_f32 %0, %1, %2" : "=v"(r) : "v"(lo), "v"(hi));
    return r;
}
__device__ __forceinline__ unsigned clamp_pk(unsigned v, unsigned lo, unsigned hi) {
    typedef __attribute__((ext_vector_type(2))) short i16x2;
    i16x2 r, a, b;
    __builtin_memcpy(&r, &v, 4);
    __builtin_memcpy(&a, &lo, 4);
    __builtin_memcpy(&b, &hi, 4);
    r = __builtin_elementwise_min(__builtin_elementwise_max(r, a), b);
    __builtin_memcpy(&v, &r, 4);
    return v;
}
__device__ __forceinline__ unsigned clip_pk(unsigned v) {
    typedef __attribute__((ext_vector_type(2))) short i16x2;
    i16x2 r;
    __builtin_memcpy(&r, &v, 4);
    r = __builtin_elementwise_min(__builtin_elementwise_max(r, (i16x2){0, 0}), (i16x2){0x435f, 0x435f});
    __builtin_memcpy(&v, &r, 4);
    return v;
}
__device__ __forceinline__ void win_row(const unsigned (&vc)[4], const void* base, unsigned dst, unsigned ml, unsigned mh) {
    asm volatile("s_mov_b32 m0, %5\n\ts_mov_b32 exec_hi, 0x0fffffff\n\ts_mov_b32 exec_lo, %6\n\t"
                 "global_load_lds_dwordx4 %0, %4" XPOL "\n\t"
                 "s_mov_b32 exec_lo, -1\n\t"
                 "global_load_lds_dwordx4 %1, %4 offset:960" XPOL "\n\t"
                 "global_load_lds_dwordx4 %2, %4 offset:1920" XPOL "\n\t"
                 "s_mov_b32 exec_hi, %7\n\t"
                 "global_load_lds_dwordx4 %3, %4 offset:2880" XPOL "\n\t"
                 "s_mov_b32 exec_hi, -1"
                 :: "v"(vc[0]), "v"(vc[1]), "v"(vc[2]), "v"(vc[3]), "s"(base), "s"(dst), "s"(ml), "s"(mh) : "memory");
}

#define LIST_ENTRIES(mask, pos, tab, imm, lb) do { unsigned t0_, t1_; \
    asm volatile("s_mov_b64 exec, %2\n\t" \
                 "v_mbcnt_lo_u32_b32 %0, exec_lo, 0\n\t" \
                 "v_add_u32 %1, %6, %4\n\t" \
                 "v_mbcnt_hi_u32_b32 %0, exec_hi, %0\n\t" \
                 "v_lshl_add_u32 %0, %0, 2, %5\n\t" \
                 "ds_write2_b32 %0, %3, %1 offset1:64\n\t" \
                 "s_mov_b64 exec, -1" \
                 : "=&v"(t0_), "=&v"(t1_) : "s"(mask), "v"(pos), "v"(tab), "s"(lb), "i"(imm) : "memory"); } while (0)

struct TileTab { unsigned v[256]; };

struct LaneC {
    unsigned col0, col3, vp[4], rowa, rowh, px36, pfw, wo[5];
};
__device__ __forceinline__ LaneC lane_consts(const int w4, const unsigned lane) {
    LaneC c;
    const unsigned cp = (lane * 43) >> 9, pc = lane - cp * 12, s = lane >> 4;
    c.col0 = cp;
    c.col3 = 15 + cp;
#pragma unroll
    for (int i = 0; i < 4; ++i) {
        const unsigned col = 5 * i + cp;
        c.vp[i] = ((pc ^ ((col & 4) >> 1)) << 4) + ((i == 1 || i == 2) ? cp * (CH * 2) : (i == 3 ? -2880u + 4096u : 0u));
    }
    c.vp[0] += 4096; c.vp[1] += 4096; c.vp[2] += 4096;
    c.rowa = s < 3 ? 4 * s + w4 : 12 + (w4 >> 1);
    c.rowh = 4 * (s & 1) + w4;
    c.px36 = (lane & 15) * 36;
    c.pfw = min(((blockIdx.x >> 3) * 4u + w4) * 9u + min(lane, 8u), 1151u) * 128;
#pragma unroll
    for (int k = 0; k < 5; ++k) {
        const unsigned o = k < 4 ? k * 64 : 224, d = w4 * 288 + o + lane;
        c.wo[k] = ((d ^ ((__umul24(d, 683) >> 15) & 2)) - o) * 16;
    }
    return c;
}

template <bool FIRST>
__device__ __forceinline__ int run_rows(const unsigned short* __restrict__ x, const unsigned short* __restrict__ off,
                                        const unsigned short* __restrict__ wgt, unsigned short* __restrict__ out,
                                        const int wv, const int lane, LaneC lc, const int y0, const int x0, const int nrows,
                                        const int thin
                                        , const unsigned long long stk, unsigned long long* ws, unsigned tl
                                        ) {
    {
        const int w4 = wv & 3;
        const int nf = (FIRST && nrows > 8) ? 3 : 2;
        const bool h2 = nrows <= 4;
        const int hrow = 12 + (w4 >> 1);
        const int wrows = nrows + 4;
        const int wsz = ((LDS_BYTES - WIN_OFF - wrows * PITCH) >> 2) & ~63;
        const unsigned wtab = WIN_OFF + wrows * PITCH + w4 * wsz;
        int aborted;
        const int s = lane >> 4, px = lane & 15;
        unsigned rw;
        {
            const unsigned ma = FIRST ? sneg(8 - nrows) : 0u, mh1 = sneg(4 - nrows);
            rw = (lc.rowa & ma) | (lc.rowh & ~ma & (mh1 | 3u));
        }
        const bool valid = rw < (unsigned)nrows;
        const int row = valid ? rw : 0;
        u32x4 oda, odb;
        unsigned odc;
        {
            const unsigned o = __umul24(row, IMG * 36) + lc.px36;
            asm volatile("s_cmp_lt_u32 %5, 4\n\ts_cbranch_scc1 .Ldcn_o%=\n\ts_setprio 2\n\ts_mov_b32 m0, %12\n\t"
                         "global_load_dwordx4 %0, %3, %4" OPOL "\n\tglobal_load_dwordx4 %1, %3, %4 offset:16" OPOL "\n\tglobal_load_dword %2, %3, %4 offset:32" OPOL "\n\t"
                         "global_load_lds_dwordx4 %6, %11\n\t"
                         "global_load_lds_dwordx4 %7, %11 offset:1024\n\t"
                         "global_load_lds_dwordx4 %8, %11 offset:2048\n\t"
                         "global_load_lds_dwordx4 %9, %11 offset:3072\n\t"
                         "s_mov_b32 exec_lo, 0\n\t"
                         "global_load_lds_dwordx4 %10, %11 offset:3584\n\t"
                         "s_mov_b32 exec_lo, -1\n\ts_mov_b32 m0, %15\n\ts_nop 0\n\t"
                         "global_load_lds_dword %13, %14\n.Ldcn_o%=:"
                         : "=&v"(oda), "=&v"(odb), "=&v"(odc)
                         : "v"(o), "s"((const unsigned char*)off + (y0 * IMG + x0) * 36), "s"(wv),
                           "v"(lc.wo[0]), "v"(lc.wo[1]), "v"(lc.wo[2]), "v"(lc.wo[3]), "v"(lc.wo[4]),
                           "s"((const unsigned char*)wgt + 8 * WTAP), "s"(w4 * 4608), "v"(lc.pfw), "s"(wgt), "i"(LDS_BYTES - 256) : "memory", "scc");
        }
        unsigned vc[4];
        vc[0] = __umul24(max(lc.col0, (unsigned)smax(2 - x0, 0)), CH * 2) + lc.vp[0];
        vc[1] = lc.vp[1];
        vc[2] = lc.vp[2];
        vc[3] = __umul24(min(lc.col3, (unsigned)(IMG + 1 - x0)), CH * 2) + lc.vp[3];
        const unsigned char* xb = (const unsigned char*)x + ((x0 - 2) * (CH * 2) - 4096);
        const unsigned ml = ~0u << (12 * thin), mh = 0x0fffffffu >> (12 * thin);
        int wr = wv + 3 * thin;
        {
            const int lim = smin(wrows, (wv & 4) ? 0 : 12 + 3 * thin);
#pragma unroll 1
            for (; wr < lim; wr += 4) {
                const int gy = smin(smax(y0 - 2 + wr, 0), IMG - 1);
                win_row(vc, xb + gy * ROWB, WIN_OFF + wr * PITCH, ml, mh);
            }
        }
        __builtin_amdgcn_sched_barrier(0);
        const int hpix = y0 + row, wpix = x0 + px;
        unsigned (&wo)[5] = lc.wo;
        if (wv < 4) {
            const unsigned char* wsrc = (const unsigned char*)wgt + 7 * WTAP;
            unsigned taddr = TBL_OFF + wv * 2304 + (nf == 3 ? lane & 15 : lane >> 2) * 16;
            unsigned waddr = wtab + (lane & 15) * 8;
            const unsigned kq = nf == 3 ? lane & 48 : (lane & 3) << 4;
            const int q = lane >> 4, par = wv & 1;
            const int sel = (q == ((lane >> 2) & 3)) ? (lane & 3) : 4;
            unsigned wb[6];
#pragma unroll
            for (int i = 0; i < 6; ++i) {
                const int nb = par ? (0x102354 >> (4 * i)) & 7 : i;
                wb[i] = (8 * q + ((lane >> 2) & 3)) * 192 + (lane & 3) * 8 + ((nb ^ (q & 1)) << 5);
            }
            const unsigned ol = (lane & 15) * 192 + (q & 1) * 32 + (q >> 1) * 16;
            const unsigned char* ob = (const unsigned char*)out + (y0 * IMG + x0) * (CH * 2);
            const unsigned char* ob0 = ob + wv * ROWB;
            const unsigned char* ob1 = ob0 + 4 * ROWB;
            const unsigned char* ob2 = ob0 + 8 * ROWB;
            const unsigned char* ob3 = ob + hrow * ROWB;
            const int vm = UNI((wv < nrows ? 1 : 0) | (wv + 4 < nrows ? 2 : 0) | ((nf == 3 && wv + 8 < nrows) ? 4 : 0) |
                               (hrow < nrows ? 8 + 16 : 0) | (nf == 3 ? 32 : 0) | (h2 ? 64 : 0) | thin << 7);
            unsigned sm[4], xa = TBL_OFF + wv * 2304 + lane * 8, kq2 = kq, ol2 = ol;
#pragma unroll
            for (int k = 0; k < 4; ++k) sm[k] = sel == k ? ~0u : 0u;
            asm volatile("" : "+v"(taddr), "+v"(waddr), "+v"(kq2), "+v"(ol2), "+v"(xa), "+v"(wb[0]), "+v"(wb[1]), "+v"(wb[2]), "+v"(wb[3]), "+v"(wb[4]), "+v"(wb[5]),
                              "+v"(wo[0]), "+v"(wo[1]), "+v"(wo[2]), "+v"(wo[3]), "+v"(wo[4]), "+v"(sm[0]), "+v"(sm[1]), "+v"(sm[2]), "+v"(sm[3]));
            if (thin) {
                asm volatile("s_waitcnt vmcnt(8)" ::: "memory");
                wr = 15 + (wv >> 1);
                win_row(vc, xb + smin(y0 - 2 + wr, IMG - 1) * ROWB, WIN_OFF + wr * PITCH, ml, mh);
                wr = 1 + (wv >> 1);
                asm volatile("s_mov_b32 m0, %3\n\ts_mov_b32 exec_hi, 0x0fffffff\n\ts_mov_b32 exec_lo, %4\n\t"
                             "global_load_lds_dwordx4 %0, %2" XPOL "\n\t"
                             "s_mov_b32 exec_lo, -1\n\ts_mov_b32 exec_hi, %5\n\t"
                             "global_load_lds_dwordx4 %1, %2 offset:960" XPOL "\n\t"
                             "s_mov_b32 exec_hi, -1"
                             :: "v"(par ? vc[2] : vc[0]), "v"(par ? vc[3] : vc[1]), "s"(xb + (y0 - 2 + wr) * ROWB + par * 1920),
                                "s"(WIN_OFF + wr * PITCH + par * 1920), "s"(par ? ~0u : ml), "s"(par ? mh : 0x0fffffffu) : "memory");
            } else {
#pragma unroll 1
                for (; wr < wrows; wr += 4) {
                    const int gy = smin(smax(y0 - 2 + wr, 0), IMG - 1);
                    win_row(vc, xb + gy * ROWB, WIN_OFF + wr * PITCH, ml, mh);
                }
            }
            STAMP(1);
            int slot, cnt;
            unsigned long long st0, st1, st2, st3, st4, cy1, cy2;
            asm volatile(COMPUTE_ASM_TEXT
                         : [taddr] "+v"(taddr), [waddr] "+v"(waddr),
                           [wb0] "+v"(wb[0]), [wb1] "+v"(wb[1]), [wb2] "+v"(wb[2]), [wb3] "+v"(wb[3]), [wb4] "+v"(wb[4]), [wb5] "+v"(wb[5]), [slot] "=&s"(slot), [cnt] "=&s"(cnt), [ab] "=&s"(aborted)
                           , [st0] "=&s"(st0), [st1] "=&s"(st1), [st2] "=&s"(st2), [st3] "=&s"(st3), [st4] "=&s"(st4), [cy1] "=&s"(cy1), [cy2] "=&s"(cy2)
                         : [kq] "v"(kq2), [ol] "v"(ol2), [par] "s"(par * 32), [xa] "v"(xa),
                           [ob0] "s"(ob0), [ob1] "s"(ob1), [ob2] "s"(ob2), [ob3] "s"(ob3), [vm] "s"(vm),
                           [wo0] "v"(wo[0]), [wo1] "v"(wo[1]), [wo2] "v"(wo[2]), [wo3] "v"(wo[3]), [wo4] "v"(wo[4]),
                           [sm0] "v"(sm[0]), [sm1] "v"(sm[1]), [sm2] "v"(sm[2]), [sm3] "v"(sm[3]),
                           [wsrc0] "s"(wsrc), [wdst] "s"(WTAP + w4 * 4608), [wsum] "s"(WTAP + 2 * w4 * 4608)
                         : "memory", "scc", "vcc", COMPUTE_ASM_CLOBBERS);
            if (FIRST && PFC) asm volatile(".Ldcn_pf_end:");
            {
                if (FIRST && wv == 0) {
                    const unsigned long long sv[6] = {stk, st0, st1, st2, st3, st4};
                    __hip_atomic_store(ws + blockIdx.x * 2, stk, __ATOMIC_RELAXED, __HIP_MEMORY_SCOPE_AGENT);
                    __hip_atomic_store(ws + blockIdx.x * 2 + 1, (1) ? sv[0] : ~0ull, __ATOMIC_RELAXED, __HIP_MEMORY_SCOPE_AGENT);
                    if (blockIdx.x == 100) {
                        while (__builtin_amdgcn_s_memrealtime() < stk + 2500) __builtin_amdgcn_s_sleep(2);
                        unsigned long long mn = ~0ull, mx = 0, sum = 0, n = 0, mi = ~0ull;
                        for (int i = 0; i < 255; ++i) {
                            unsigned long long v = __hip_atomic_load(ws + 2 * i, __ATOMIC_RELAXED, __HIP_MEMORY_SCOPE_AGENT);
                            mn = v < mn ? v : mn;
                        }
                        for (int i = 0; i < 255; ++i) {
                            unsigned long long e = __hip_atomic_load(ws + 2 * i + 1, __ATOMIC_RELAXED, __HIP_MEMORY_SCOPE_AGENT);
                            if (e != ~0ull) { e -= mn; mx = e > mx ? e : mx; mi = e < mi ? e : mi; sum += e; ++n; }
                        }
                        unsigned long long d = 0; if (d > 9000) d = 9000;
                        while (__builtin_amdgcn_s_memrealtime() < mn + 30000 + d) __builtin_amdgcn_s_sleep(2);
                    }
                }
            }
        } else {
            const int wto = nf == 2 ? 1152 : 0, lsz = FIRST ? 256 : 1152;
            const int cap = UNI((unsigned)(wsz - wto - 4 * lsz) / 192);
            const unsigned lst = wtab + wto;
            const unsigned scr = lst + 4 * lsz;
            const unsigned tw = TBL_OFF + w4 * 2304 + px * 16 + s * 4;
            if (nf == 3) {
#pragma unroll
                for (int kj = 0; kj < 3; ++kj) {
                    const int col = px + kj + 1;
                    const unsigned b = WIN_OFF + (row + 1) * PITCH + col * 192 + ((col & 4) << 3);
#pragma unroll
                    for (int ki = 0; ki < 3; ++ki)
                        *(lds_u32*)(uintptr_t)(tw + (8 - ki * 3 - kj) * 256) = b + ki * PITCH;
                }
            }
            float hf[3], wf[3];
            unsigned rg[9];
            {
                unsigned r3[3];
#pragma unroll
                for (int k = 0; k < 3; ++k) {
                    hf[k] = (float)(hpix + k - 1);
                    wf[k] = (float)(wpix + k - 1);
                    r3[k] = clip_pk(cvt_pk(hf[k], wf[k]));
                }
#pragma unroll
                for (int u = 0; u < 9; ++u) rg[u] = __builtin_amdgcn_perm(r3[(8 - u) % 3], r3[(8 - u) / 3], 0x07060100u);
            }
            const unsigned long long vmask = __ballot(valid);
            const bool up = lane >= 32, low = h2 && (s & 1), upp = h2 && !(s & 1);
            const unsigned long long emask = h2 ? vmask & 0x0000ffff0000ffffull : vmask;
            float hk[5], wk[5];
#pragma unroll
            for (int k = 0; k < 5; ++k) {
                const int t0 = 8 - 2 * k, t1 = k < 4 ? 7 - 2 * k : 0;
                hk[k] = up ? hf[t1 / 3] : hf[t0 / 3];
                wk[k] = up ? wf[t1 % 3] : wf[t0 % 3];
            }
            unsigned tal = TBL_OFF + w4 * 2304 + (px >> 2) * 64 + (px & 1) * 32 + (s & 1) * 8 + (px & 2) * 2, ta = tal + (up ? 256 : 0);
            unsigned tql = wtab + px * 8 + (s & 1) * 4, tq = tql + (up ? 128 : 0), wce = low ? 0u : 0x3f80u;
            const unsigned wlo = UNI(cvt_pk((float)max(y0 - 2, 0), (float)max(x0 - 2, 0)));
            const unsigned whi = UNI(cvt_pk((float)(y0 + nrows + 1), (float)(x0 + 17)));
            const unsigned wbs = WIN_OFF - (y0 - 2) * PITCH - (x0 - 2) * 192;
            const float k1 = h2 ? 0.f : 1.f, k2 = h2 ? -1.f : 0.f;
            asm volatile("" : "+v"(hk[0]), "+v"(hk[1]), "+v"(hk[2]), "+v"(hk[3]), "+v"(hk[4]), "+v"(wk[0]), "+v"(wk[1]), "+v"(wk[2]), "+v"(wk[3]), "+v"(wk[4]),
                              "+v"(tal), "+v"(ta), "+v"(tql), "+v"(tq), "+v"(wce));
            asm volatile("s_waitcnt vmcnt(6)" : "+v"(oda), "+v"(odb), "+v"(odc), "+v"(rg[0]), "+v"(rg[1]), "+v"(rg[2]), "+v"(rg[3]), "+v"(rg[4]),
                                                "+v"(rg[5]), "+v"(rg[6]), "+v"(rg[7]), "+v"(rg[8]) :: "memory");
            STAMP(1);
            int nent = 0;
            unsigned long long cnts = 0;
            unsigned flag = 0;
            if (nf == 2) {
                const unsigned od[10] = {odc, odb[3], odb[2], odb[1], odb[0], oda[3], oda[2], oda[1], oda[0], oda[0]};
                unsigned pp[5];
                unsigned long long mk[5], mall = 0;
#pragma unroll
                for (int k = 0; k < 5; ++k) {
                    const unsigned o = up ? od[2 * k + 1] : od[2 * k];
                    pp[k] = clip_pk(cvt_pk1(hk[k] + bf_lo(o), wk[k] + bf_hi(o)));
                    const unsigned pc = clamp_pk(pp[k], wlo, whi);
                    const float py = bf_lo(pc), pxf = bf_hi(pc);
                    const float fy = floorf(py), fx = floorf(pxf), cy = ceilf(py), cx = ceilf(pxf);
                    const float wy = py - fy, wx = pxf - fx;
                    const unsigned r0 = (unsigned)(low ? cy : fy), r1 = (unsigned)(upp ? fy : cy), c0 = (unsigned)fx, c1 = (unsigned)cx;
                    const unsigned a0 = (__umul24(r0, PITCH) + (__umul24(c0, 192) + wbs)) | ((c0 * 8 + 16) & 32);
                    const unsigned a1 = (__umul24(r1, PITCH) + (__umul24(c1, 192) + wbs)) | ((c1 * 8 + 16) & 32);
                    const float wy1 = wy * k1, wz = wx + wy1;
                    const unsigned ab = cvt_pk1(fmaf(wy, k2, 1.f), 1.f - wz);
                    const float f = low ? wy : bf_lo(ab);
                    const unsigned ev = __float_as_uint(fminf(wy1, wx)) | (pc ^ pp[k]);
                    const unsigned wn = cvt_pk1(f * bf_hi(ab), f * wz);
                    const unsigned wp = ev ? wce : wn;
                    mk[k] = __builtin_amdgcn_uicmp(ev, 0u, 33) & (k < 4 ? emask : emask & 0xffffffffull);
                    mall |= mk[k];
                    const unsigned ta2 = k < 4 ? ta : tal, tq2 = k < 4 ? tq : tql;
                    *(lds_u32*)(uintptr_t)(ta2 + k * 512) = a0;
                    *(lds_u32*)(uintptr_t)(ta2 + k * 512 + 16) = a1;
                    *(lds_u32*)(uintptr_t)(tq2 + k * 256) = wp;
                }
                if (FIRST && mall) {
                    int cn[10];
                    unsigned c0 = 0, c1 = 0;
#pragma unroll
                    for (int k = 0; k < 5; ++k) {
                        cn[2 * k] = __popc((unsigned)mk[k]);
                        cn[2 * k + 1] = __popc((unsigned)(mk[k] >> 32));
                        nent += cn[2 * k] + cn[2 * k + 1];
                        if (k < 2) c0 |= (cn[2 * k] | cn[2 * k + 1] << 7) << (14 * k);
                        else if (k < 4) c1 |= (cn[2 * k] | cn[2 * k + 1] << 7) << (14 * (k - 2));
                    }
                    if (nent > 64) {
                        flag = 2;
                        nent = 0;
                    } else {
                        cnts = c0 | ((unsigned long long)c1 << 28) | ((unsigned long long)cn[8] << 56);
                        unsigned lb = lst;
#pragma unroll
                        for (int k = 0; k < 5; ++k) {
                            LIST_ENTRIES(mk[k], pp[k], ta, k * 512, lb);
                            lb += (cn[2 * k] + cn[2 * k + 1]) * 4;
                        }
                        flag = cn[0] ? 1 : 0;
                    }
                }
                if (!FIRST && mall) {
                    int cn[10];
#pragma unroll
                    for (int k = 0; k < 5; ++k) {
                        cn[2 * k] = __popc((unsigned)mk[k]);
                        cn[2 * k + 1] = __popc((unsigned)(mk[k] >> 32));
                    }
                    int worst = 0, ea = 0, eb = cn[0];
#pragma unroll
                    for (int u = 0; u < 8; ++u) {
                        eb += cn[u + 1];
                        worst = max(worst, ((eb + 15) & ~15) - ea);
                        ea += cn[u];
                    }
                    if (worst > cap) {
                        flag = 2;
                    } else {
                        int pos = 0;
#pragma unroll
                        for (int k = 0; k < 5; ++k) {
                            if ((mk[k] >> lane) & 1) {
                                const unsigned idx = __builtin_amdgcn_mbcnt_hi((unsigned)(mk[k] >> 32), __builtin_amdgcn_mbcnt_lo((unsigned)mk[k], 0));
                                const unsigned e = __umul24(idx, 192) + pos;
                                const unsigned la = lst + nent * 4 + idx * 4;
                                const unsigned ad = (scr + min(e, e - cap * 192)) | ((la << 1) & 16) | (la & 32);
                                *(lds_u32*)(uintptr_t)(ta + k * 512) = ad;
                                *(lds_u32*)(uintptr_t)(ta + k * 512 + 16) = ad;
                                *(lds_u32*)(uintptr_t)la = pp[k];
                            }
                            const int c2 = cn[2 * k] + cn[2 * k + 1];
                            nent += c2;
                            pos += c2 * 192;
                            pos = min((unsigned)pos, (unsigned)(pos - cap * 192));
                            cnts |= (unsigned long long)(cn[2 * k] | (cn[2 * k + 1] << 7)) << (14 * k);
                        }
                        flag = cn[0] ? 1 : 0;
                    }
                }
            } else {
                const unsigned od[9] = {odc, odb[3], odb[2], odb[1], odb[0], oda[3], oda[2], oda[1], oda[0]};
                unsigned pp[9];
                unsigned long long mk[9], mall = 0;
#pragma unroll
                for (int u = 0; u < 9; ++u) {
                    pp[u] = clip_pk(cvt_pk1(hf[(8 - u) / 3] + bf_lo(od[u]), wf[(8 - u) % 3] + bf_hi(od[u])));
                    mk[u] = __builtin_amdgcn_uicmp(pp[u], rg[u], 33) & vmask;
                    mall |= mk[u];
                }
                if (mall) {
                    unsigned c0 = 0, c1 = 0;
                    int cn[9];
#pragma unroll
                    for (int u = 0; u < 9; ++u) {
                        cn[u] = __popcll(mk[u]);
                        nent += cn[u];
                        if (u < 4) c0 |= cn[u] << (7 * u);
                        else c1 |= cn[u] << (7 * (u - 4) > 21 ? 0 : 7 * (u - 4)) & (u == 8 ? 0 : ~0u);
                    }
                    if (nent > min(cap & ~15, 64)) {
                        flag = 2;
                        nent = 0;
                    } else {
                        cnts = c0 | ((unsigned long long)c1 << 28) | ((unsigned long long)cn[8] << 56);
                        unsigned lb = lst;
#pragma unroll
                        for (int u = 0; u < 9; ++u) {
                            LIST_ENTRIES(mk[u], pp[u], tw, u * 256, lb);
                            lb += cn[u] * 4;
                        }
                        flag = cn[0] ? 1 : 0;
                    }
                }
            }
            *(lds_u32*)(uintptr_t)(FLAG_OFF + w4 * 4) = flag;
            const int q = lane >> 4;
            const unsigned wbase = scr + ((q ^ ((px >> 1) & 1) ^ ((px >> 2) & 2)) << 4);
            int must = (int)(cnts & 127);
            cnts >>= 7;
            int af, ub = 0;
            unsigned two = 0;
            bool far = thin;
            if (nent) {
                const float oy = (float)(y0 - 2), ox = (float)(x0 - 2), lo = (float)thin, ly = (float)(wrows - 1 - thin), lx = (float)(19 - thin);
#pragma unroll 1
                for (int e = 0; e < (FIRST ? 1 : nent); e += 64) {
                    const unsigned la = lst + (e + lane) * 4;
                    const unsigned pc = *(const lds_u32*)(uintptr_t)(lst + min(e + lane, nent - 1) * 4);
                    if (FIRST && lane < nent) {
                        const unsigned te = *(const lds_u32*)(uintptr_t)(la + 256);
                        const unsigned ad = (scr + lane * 192) | ((lane << 3) & 16) | ((lane << 2) & 32);
                        *(lds_u32*)(uintptr_t)te = ad;
                        *(lds_u32*)(uintptr_t)(te + (nf == 2 ? 16 : 0)) = ad;
                    }
                    const float py = bf_lo(pc), pxf = bf_hi(pc);
                    const float wy = py - floorf(py), wx = pxf - floorf(pxf);
                    const unsigned ab = cvt_pk(1.f - wy, 1.f - wx);
                    const float a = bf_lo(ab), b = bf_hi(ab);
                    const unsigned w01 = cvt_pk(a * b, a * wx), w23 = cvt_pk(wy * b, wy * wx);
                    const float yr = py - oy, xr = pxf - ox;
                    const float yc = __builtin_amdgcn_fmed3f(yr, lo, ly), xc = __builtin_amdgcn_fmed3f(xr, lo, lx);
                    far = far || __any(yc != yr || xc != xr);
                    const unsigned iy = (unsigned)yc, ix = (unsigned)xc;
                    const unsigned cb = __umul24(iy, PITCH / 32) + __umul24(ix, 6) + WIN_OFF / 32;
                    const unsigned c00 = cb + ((ix >> 2) & 1), c01 = cb + 6 + (((ix + 1) >> 2) & 1);
                    const bool fy = wy != 0.f, fx = wx != 0.f;
                    const unsigned c10 = c00 + (fy ? PITCH / 32 : 0);
                    const unsigned long long tm = __ballot(fx && fy);
                    if ((e + lane) * 4 < lsz) {
                        *(lds_u32*)(uintptr_t)(la + lsz) = c00 | ((fx ? c01 : c10) << 16) | ((fx && fy) ? 0x80008000u : 0u);
                        *(lds_u32x2*)(uintptr_t)(lst + 2 * lsz + (e + lane) * 8) =
                            (u32x2){fx ? w01 : __builtin_amdgcn_perm(w23, w01, 0x05040100u), (fx && fy) ? w23 : 0u};
                    }
                    const unsigned tl = (unsigned)tm, thi = (unsigned)(tm >> 32);
                    two |= (((tl & 0xffffu) ? 1u : 0u) | ((tl >> 16) ? 2u : 0u) | ((thi & 0xffffu) ? 4u : 0u) | ((thi >> 16) ? 8u : 0u)) << (e >> 4);
                }
            }
            if (nent == 0) {
                STAMP(2);
                asm volatile("s_waitcnt vmcnt(0) lgkmcnt(0)\n\ts_barrier" ::: "memory");
                const u32x4 f = *(const lds_u32x4*)(uintptr_t)FLAG_OFF;
                af = UNI(f[0] | f[1] | f[2] | f[3]);
            } else if (__builtin_expect(!far, 1)) {
                two = UNI(two);
                unsigned da = lst + lsz + (4 * q + ((lane >> 3) & 1)) * 4 + ((lane >> 1) & 2);
                unsigned dw = lst + 2 * lsz + px * 8;
                unsigned wof = px * 192;
                const int sel = (q == (px >> 2)) ? (px & 3) : 4;
                int e0, dead, dead1;
                STAMP(2);
                asm volatile(HELPER_ASM_TEXT
                             : [da] "+v"(da), [dw] "+v"(dw), [wo] "+v"(wof), [must] "+s"(must), [two] "+s"(two),
                               [e0] "=&s"(e0), [dead] "=&s"(dead), [dead1] "=&s"(dead1), [ub] "=&s"(ub), [af] "=&s"(af)
                             : [cx] "v"(((lane & 3) << 4) | ((q & 1) << 3)), [wb] "v"(wbase + ((px & 4) << 1)),
                               [sm0] "v"(sel == 0 ? ~0u : 0u), [sm1] "v"(sel == 1 ? ~0u : 0u), [sm2] "v"(sel == 2 ? ~0u : 0u), [sm3] "v"(sel == 3 ? ~0u : 0u),
                               [am] "s"(0x3ffe0), [flg] "s"(FLAG_OFF), [cnts] "s"(cnts), [nent] "s"(nent), [cap192] "s"(cap * 192), [capm] "s"(cap - 16)
                             : "memory", "scc", HELPER_ASM_CLOBBERS);
            } else {
                asm volatile("s_waitcnt vmcnt(0) lgkmcnt(0)\n\ts_barrier" ::: "memory");
                const u32x4 f = *(const lds_u32x4*)(uintptr_t)FLAG_OFF;
                af = UNI(f[0] | f[1] | f[2] | f[3]);
                int e0 = 0, lim = -1, dead1 = 0;
                unsigned wof = px * 192;
                while (!(af & 2) && e0 < nent) {
                    if (e0 >= must && e0 > lim) {
                        asm volatile("s_waitcnt lgkmcnt(0)" ::: "memory");
                        if (af) asm volatile("s_barrier" ::: "memory");
                        af = 1;
                        ++ub;
                        lim = dead1 + cap - 16;
                        dead1 = must;
                        must += (int)(cnts & 127);
                        cnts >>= 7;
                        continue;
                    }
                    lim = -1;
                    const unsigned pc = *(const lds_u32*)(uintptr_t)(lst + min(e0 + px, nent - 1) * 4);
                    const float py = bf_lo(pc), pxf = bf_hi(pc);
                    const int gy = (int)py, gx = (int)pxf;
                    const float wy = py - (float)gy, wx = pxf - (float)gx;
                    const unsigned ab = cvt_pk(1.f - wy, 1.f - wx);
                    const float a = bf_lo(ab), b = bf_hi(ab);
                    const unsigned w01 = cvt_pk(a * b, a * wx), w23 = cvt_pk(wy * b, wy * wx);
                    const unsigned o00 = (gy * IMG + gx) * (CH * 2) + q * 16;
                    const unsigned dx = gx < IMG - 1 ? CH * 2 : 0, dy = gy < IMG - 1 ? ROWB : 0;
                    const float wc[4] = {bf_lo(w01), bf_hi(w01), bf_lo(w23), bf_hi(w23)};
#pragma unroll
                    for (int j = 0; j < 3; ++j) {
                        float vv[8];
#pragma unroll
                        for (int c = 0; c < 4; ++c) {
                            const u32x4 gv = *(const glb_u32x4*)((uintptr_t)x + (o00 + ((c & 1) ? dx : 0u) + ((c & 2) ? dy : 0u) + j * 64));
#pragma unroll
                            for (int k = 0; k < 4; ++k) {
                                vv[2 * k] = c ? fmaf(wc[c], bf_lo(gv[k]), vv[2 * k]) : wc[0] * bf_lo(gv[k]);
                                vv[2 * k + 1] = c ? fmaf(wc[c], bf_hi(gv[k]), vv[2 * k + 1]) : wc[0] * bf_hi(gv[k]);
                            }
                        }
                        *(lds_u32x4*)(uintptr_t)(wbase + wof + j * 64) =
                            (u32x4){cvt_pk(vv[0], vv[1]), cvt_pk(vv[2], vv[3]), cvt_pk(vv[4], vv[5]), cvt_pk(vv[6], vv[7])};
                    }
                    e0 += 16;
                    wof += 3072;
                    wof = min(wof, wof - cap * 192);
                }
                asm volatile("s_waitcnt lgkmcnt(0)" ::: "memory");
            }
            aborted = af & 2;
            if (!aborted) {
                STAMP(3);
                if (FIRST) asm volatile("s_endpgm");
                if (ub == 0) {
                    if (af) asm volatile("s_barrier" ::: "memory");
                    ub = 1;
                }
                for (; ub <= 8; ++ub) asm volatile("s_barrier" ::: "memory");
            }
        }
        return UNI(aborted);
    }
}

__global__ __launch_bounds__(NTHREADS, 2) void dcn_kernel(const unsigned short* x_, const unsigned short* off_,
                                                          const unsigned short* wgt_, unsigned short* out_, const TileTab tiles, unsigned long long* ws) {
    const unsigned long long tstart = __builtin_amdgcn_s_memrealtime();
    __shared__ __attribute__((aligned(64))) unsigned char lds[LDS_BYTES];
    asm volatile("" :: "v"(lds) : "memory");
    unsigned tile;
    u32x8 ka;
    asm volatile("s_lshl2_add_u32 %0, %3, 32\n\ts_load_dwordx8 %1, %2, 0x0\n\ts_load_dword %0, %2, %0"
                 : "=&s"(tile), "=&s"(ka) : "s"(__builtin_amdgcn_kernarg_segment_ptr()), "s"(blockIdx.x) : "scc");
    {
        unsigned long long pc;
        unsigned o0 = (threadIdx.x * PFC + ((blockIdx.x >> 3) & (PFC - 1))) * 128u;
        asm volatile("s_getpc_b64 %0\n.Ldcn_pf_start:\n\ts_mov_b32 m0, %2\n\tv_cmp_gt_u32 vcc, .Ldcn_pf_end-.Ldcn_pf_start, %1\n\t"
                     "s_cbranch_vccz .Ldcn_pf_skip\n\ts_mov_b64 exec, vcc\n\tglobal_load_lds_dword %1, %0\n\ts_mov_b64 exec, -1\n.Ldcn_pf_skip:"
                     : "=&s"(pc) : "v"(o0), "i"(LDS_BYTES - 256) : "memory", "vcc");
    }
    const int wv = UNI(threadIdx.x >> 6);
    LaneC lc = lane_consts(wv & 3, threadIdx.x & 63);
    asm volatile("" : "+v"(lc.col0), "+v"(lc.col3), "+v"(lc.vp[0]), "+v"(lc.vp[1]), "+v"(lc.vp[2]), "+v"(lc.vp[3]),
                      "+v"(lc.rowa), "+v"(lc.rowh), "+v"(lc.px36), "+v"(lc.pfw), "+v"(lc.wo[0]), "+v"(lc.wo[1]), "+v"(lc.wo[2]), "+v"(lc.wo[3]), "+v"(lc.wo[4]));
    asm volatile("s_waitcnt lgkmcnt(0)" : "+s"(tile), "+s"(ka));
    const unsigned short* x = (const unsigned short*)(((unsigned long long)ka[1] << 32) | ka[0]);
    const unsigned short* off = (const unsigned short*)(((unsigned long long)ka[3] << 32) | ka[2]);
    const unsigned short* wgt = (const unsigned short*)(((unsigned long long)ka[5] << 32) | ka[4]);
    unsigned short* out = (unsigned short*)(((unsigned long long)ka[7] << 32) | ka[6]);
    STAMP(0);
    #define STK , tstart, ws, tile
    int ty0 = tile & 255, x0 = (tile >> 8) & 255, trows = (tile >> 16) & 255;
    asm("" : "+s"(ty0), "+s"(x0), "+s"(trows));
    if (!run_rows<true>(x, off, wgt, out, wv, threadIdx.x & 63, lc, ty0, x0, trows, tile >> 24 STK)) return;
    int y0 = ty0, split = trows > 8 ? 8 : 4;
    for (;;) {
        int lane;
        asm volatile("s_waitcnt vmcnt(0) lgkmcnt(0)\n\ts_barrier\n\tv_mbcnt_lo_u32_b32 %0, -1, 0\n\tv_mbcnt_hi_u32_b32 %0, -1, %0" : "=v"(lane) :: "memory");
        y0 = UNI(y0);
        const int nrows = UNI(min(split, ty0 + trows - y0));
        int wv2 = wv, x2 = x0;
        asm volatile("" : "+s"(wv2), "+s"(x2));
        if (run_rows<false>(x, off, wgt, out, wv2, lane & 63, lane_consts(wv2 & 3, lane & 63), y0, x2, nrows, 0 STK)) split = 4;
        else if ((y0 += nrows) >= ty0 + trows) break;
    }
}

#define NWG 255
#define THIN 1
#define QEXTRA 0, 0, 0, 0, 0, 0, 0, 0
static void make_tiles(TileTab& tt) {
    unsigned tl[256];
    int n = 0;
    for (int s = 0; s < 14; ++s) {
        int y = 0;
        while (y < IMG) {
            int r;
            if (s == 0) r = y < 16 ? 4 : 8;
            else if (y < 16) r = (s == 1 && y < 8) ? 4 : 8;
            else if (s == 1 || y == 16) r = 12;
            else r = 14;
            if (r > IMG - y) r = IMG - y;
            tl[n++] = (unsigned)((y << 24) | (s << 16) | r);
            y += r;
        }
    }
    auto key = [](unsigned u) {
        const unsigned y = u >> 24, s = (u >> 16) & 255, r = u & 255;
        const bool quick = r <= 4 || (r <= 8 && (y >= 32 || s >= 2));
        const bool inner = y >= 34 && s >= 3;
        const bool full = r == 14 || r <= 8;
        return (quick ? (1u << 30) + (s == 0 ? (1u << 29) : 0u) : inner ? (1u << 29) : full ? 0u : (1u << 28)) + (u >> 8);
    };
    for (int i = 1; i < n; ++i) {
        const unsigned v = tl[i];
        int j = i;
        for (; j > 0 && key(tl[j - 1]) > key(v); --j) tl[j] = tl[j - 1];
        tl[j] = v;
    }
    int nq = 0;
    for (int i = 0; i < n; ++i) nq += key(tl[i]) >> 30;
    int big = 0, small = n - nq;
    unsigned asg[256];
    for (int k = 0; k < 8; ++k) {
        const int slots = (n - k + 7) >> 3;
        const int extra[8] = {QEXTRA};
        const int share = (nq + 7 - k) / 8 + extra[k];
        for (int i = 0; i < slots; ++i) asg[i * 8 + k] = tl[i < slots - share ? big++ : small++];
    }
    for (int b = 0; b < 256; ++b) {
        const unsigned v = asg[b < n ? b : 0];
        const unsigned y = v >> 24, s = (v >> 16) & 255, r = v & 255;
        tt.v[b] = y | (s * 16) << 8 | r << 16 | (unsigned)(THIN && r == 14 && y >= 34 && s >= 3) << 24;
    }
}

extern "C" void kernel_launch(void* const* d_in, const int* in_sizes, int n_in,
                              void* d_out, int out_size, void* d_ws, size_t ws_size,
                              hipStream_t stream) {
    TileTab tt;
    make_tiles(tt);
    dcn_kernel<<<NWG, NTHREADS, 0, stream>>>((const unsigned short*)d_in[0], (const unsigned short*)d_in[1],
                                             (const unsigned short*)d_in[2], (unsigned short*)d_out, tt, (unsigned long long*)d_ws);
}
```

```cpp
#include <hip/hip_runtime.h>
#include <stdint.h>

#define IMG 224
#define CH 96
#define ROWB (IMG * CH * 2)
#define NTHREADS 256
#define WTAP 18432
#define LDS_BYTES 163840

typedef __attribute__((ext_vector_type(2))) __bf16 bf16x2;
typedef __attribute__((ext_vector_type(8))) __bf16 bf16x8;
typedef __attribute__((ext_vector_type(4))) float f32x4;
typedef __attribute__((ext_vector_type(4))) unsigned u32x4;
typedef __attribute__((address_space(1))) u32x4 glb_u32x4;
typedef __attribute__((address_space(1))) unsigned glb_u32;
typedef __attribute__((ext_vector_type(2))) unsigned u32x2;
typedef __attribute__((address_space(1))) u32x2 glb_u32x2;
typedef __attribute__((ext_vector_type(4))) short s16x4;
typedef __attribute__((address_space(3))) s16x4 lds_s16x4;
typedef __attribute__((address_space(3))) u32x4 lds_u32x4;

#define DCN_ASM_TEXT \
        "s_load_dwordx2 s[24:25], s[0:1], 0x10\n\t" \
        "s_load_dwordx2 s[20:21], s[0:1], 0x8\n\t" \
        "s_load_dwordx2 s[16:17], s[0:1], 0x0\n\t" \
        "s_load_dwordx2 s[28:29], s[0:1], 0x18\n\t" \
        "v_lshrrev_b32 v3, 6, v0\n\t" \
        "s_getpc_b64 s[4:5]\n\t" \
        ".Lpf0%=:\n\t" \
        "s_bfe_u32 s6, s2, 0x20003\n\t" \
        "v_readfirstlane_b32 s35, v3\n\t" \
        "v_lshl_add_u32 v3, v0, 2, s6\n\t" \
        "s_mov_b32 m0, 0x27f00\n\t" \
        "v_lshlrev_b32 v3, 7, v3\n\t" \
        "v_and_b32 v2, 63, v0\n\t" \
        "v_cmp_gt_u32 vcc, .Lpf1%=-.Lpf0%=, v3\n\t" \
        "s_sub_u32 s3, s2, 25\n\t" \
        "s_cbranch_vccz .Lpf_skip%=\n\t" \
        "s_mov_b64 exec, vcc\n\t" \
        "global_load_lds_dword v3, s[4:5]\n\t" \
        "s_mov_b64 exec, -1\n\t" \
        ".Lpf_skip%=:\n\t" \
        "s_cmp_lt_u32 s3, 143\n\t" \
        "s_cbranch_scc1 .Ldec_R%=\n\t" \
        "s_cmp_lt_u32 s2, 196\n\t" \
        "s_cbranch_scc0 .Ldec_H%=\n\t" \
        "s_cmp_lt_u32 s2, 25\n\t" \
        "s_cselect_b32 s3, 0, 168\n\t" \
        "s_cselect_b32 s7, 1, 4\n\t" \
        "s_cselect_b32 s34, 14, 12\n\t" \
        "s_cselect_b32 s8, 14, 16\n\t" \
        "s_cselect_b32 s9, 28, 32\n\t" \
        "s_cselect_b32 s10, 11, 14\n\t" \
        "s_cselect_b32 s11, 28, 16\n\t" \
        "s_cselect_b32 s12, 2, 1\n\t" \
        "s_sub_u32 s3, s2, s3\n\t" \
        "s_and_b32 s4, s3, 7\n\t" \
        "s_lshr_b32 s5, s3, 3\n\t" \
        "s_min_u32 s6, s4, s7\n\t" \
        "s_mul_i32 s4, s4, 3\n\t" \
        "s_add_u32 s5, s5, s6\n\t" \
        "s_add_u32 s4, s4, s5\n\t" \
        "s_mul_i32 s5, s4, s34\n\t" \
        "s_sub_u32 s10, s4, s10\n\t" \
        "s_add_u32 s5, s5, s9\n\t" \
        "s_cmp_lt_u32 s4, s8\n\t" \
        "s_cselect_b32 s32, s5, s11\n\t" \
        "s_cselect_b32 s33, s12, s10\n\t" \
        "s_lshl_b32 s33, s33, 4\n\t" \
        ".LT_entry%=:\n\t" \
        "s_mov_b32 s4, 0x8740\n\t" \
        "s_mov_b32 s5, 0x18ec0\n\t" \
        "s_mov_b32 s6, 0x0\n\t" \
        "s_mov_b32 s7, 19\n\t" \
        "s_mov_b32 s8, 3\n\t" \
        "s_mov_b32 s9, 1\n\t" \
        "s_lshr_b32 s10, s35, 1\n\t" \
        "s_and_b32 s3, s35, 1\n\t" \
        "s_lshl3_add_u32 s10, s10, s3\n\t" \
        "s_mov_b32 s11, 1\n\t" \
        "s_branch .Lcommon%=\n\t" \
        ".Ldec_H%=:\n\t" \
        "s_sub_u32 s3, s2, 196\n\t" \
        "s_cmp_lt_u32 s3, 53\n\t" \
        "s_cbranch_scc1 .LH8dec%=\n\t" \
        "s_sub_u32 s3, s3, 53\n\t" \
        "s_lshr_b32 s33, s3, 2\n\t" \
        "s_and_b32 s3, s3, 3\n\t" \
        "s_lshl_b32 s32, s3, 2\n\t" \
        "s_mov_b32 s34, 4\n\t" \
        "s_mov_b32 s8, 0\n\t" \
        "s_mov_b32 s11, 3\n\t" \
        "s_branch .LHdec%=\n\t" \
        ".LH8dec%=:\n\t" \
        "s_and_b32 s4, s3, 7\n\t" \
        "s_lshr_b32 s5, s3, 3\n\t" \
        "s_min_u32 s6, s4, 5\n\t" \
        "s_mul_i32 s4, s4, 6\n\t" \
        "s_add_u32 s5, s5, s6\n\t" \
        "s_add_u32 s4, s4, s5\n\t" \
        "s_lshl_b32 s5, s4, 3\n\t" \
        "s_sub_u32 s6, s4, 29\n\t" \
        "s_lshr_b32 s7, s6, 1\n\t" \
        "s_and_b32 s6, s6, 1\n\t" \
        "s_add_u32 s7, s7, 2\n\t" \
        "s_lshl_b32 s6, s6, 3\n\t" \
        "s_sub_u32 s8, s5, 200\n\t" \
        "s_add_u32 s5, s5, 16\n\t" \
        "s_cmp_lt_u32 s4, 29\n\t" \
        "s_cselect_b32 s33, 1, s7\n\t" \
        "s_cselect_b32 s32, s8, s6\n\t" \
        "s_cmp_lt_u32 s4, 26\n\t" \
        "s_cselect_b32 s33, 0, s33\n\t" \
        "s_cselect_b32 s32, s5, s32\n\t" \
        "s_mov_b32 s34, 8\n\t" \
        "s_mov_b32 s8, 1\n\t" \
        "s_mov_b32 s11, 2\n\t" \
        ".LHdec%=:\n\t" \
        "s_cmp_eq_u32 s33, 0\n\t" \
        "s_cselect_b32 s6, 0x180, 0\n\t" \
        "s_lshl_b32 s33, s33, 4\n\t" \
        "s_mov_b32 s4, 0x11840\n\t" \
        "s_mov_b32 s5, 0x2840\n\t" \
        "s_mov_b32 s7, 19\n\t" \
        "s_mov_b32 s9, 2\n\t" \
        "s_mov_b32 s10, s35\n\t" \
        "s_branch .Lcommon%=\n\t" \
        ".Ldec_R%=:\n\t" \
        "s_and_b32 s4, s3, 7\n\t" \
        "s_lshr_b32 s5, s3, 3\n\t" \
        "s_mul_i32 s4, s4, 18\n\t" \
        "s_add_u32 s4, s4, s5\n\t" \
        "s_mul_i32 s5, s4, 79\n\t" \
        "s_lshr_b32 s5, s5, 10\n\t" \
        "s_mul_i32 s6, s5, 13\n\t" \
        "s_lshl_b32 s33, s5, 4\n\t" \
        "s_sub_u32 s4, s4, s6\n\t" \
        "s_add_u32 s33, s33, 48\n\t" \
        "s_mul_i32 s4, s4, 14\n\t" \
        "s_mov_b32 s34, 14\n\t" \
        "s_add_u32 s32, s4, 42\n\t" \
        "s_mov_b32 s4, 0x40\n\t" \
        "s_mov_b32 s5, 0xf840\n\t" \
        "s_mov_b32 s6, 0xc0\n\t" \
        "s_mov_b32 s7, 18\n\t" \
        "s_mov_b32 s8, 3\n\t" \
        "s_mov_b32 s9, 1\n\t" \
        "s_lshr_b32 s10, s35, 1\n\t" \
        "s_and_b32 s3, s35, 1\n\t" \
        "s_lshl3_add_u32 s10, s10, s3\n\t" \
        "s_mov_b32 s11, 0\n\t" \
        ".Lcommon%=:\n\t" \
        "s_mov_b32 s26, 0x28800\n\t" \
        "s_mov_b32 s27, 0x20000\n\t" \
        "s_mov_b32 s22, 0x1b9000\n\t" \
        "s_mov_b32 s23, 0x20000\n\t" \
        "s_mov_b32 s18, 0x930000\n\t" \
        "s_mov_b32 s19, 0x20000\n\t" \
        "s_mov_b64 s[30:31], s[18:19]\n\t" \
        "s_mul_i32 s40, s35, 0xc00\n\t" \
        "s_mul_i32 s38, s35, 0x3c0\n\t" \
        "s_mul_i32 s42, s35, 60\n\t" \
        "s_sub_u32 s43, 225, s33\n\t" \
        "v_add_u32 v32, s42, v2\n\t" \
        "s_min_u32 s43, s43, s7\n\t" \
        "v_mul_u32_u24 v33, 0x2ab, v32\n\t" \
        "s_mul_i32 s43, s43, 0xc0\n\t" \
        "v_lshrrev_b32 v33, 13, v33\n\t" \
        "s_mul_i32 s44, s33, 0xc0\n\t" \
        "v_mul_u32_u24 v34, 0xc0, v33\n\t" \
        "s_mul_i32 s36, s32, 0xa800\n\t" \
        "v_mad_i32_i24 v35, v33, -12, v32\n\t" \
        "s_add_u32 s39, s40, s5\n\t" \
        "v_and_b32 v33, 4, v33\n\t" \
        "s_add_u32 s38, s38, s4\n\t" \
        "v_min_u32 v34, s43, v34\n\t" \
        "s_sub_u32 s44, s44, 0x180\n\t" \
        "v_lshrrev_b32 v33, 1, v33\n\t" \
        "s_mul_i32 s37, s32, 224\n\t" \
        "v_max_u32 v34, s6, v34\n\t" \
        "s_add_u32 s37, s37, s33\n\t" \
        "v_xor_b32 v35, v35, v33\n\t" \
        "s_mul_i32 s37, s37, 36\n\t" \
        "v_lshl_add_u32 v35, v35, 4, v34\n\t" \
        "v_lshrrev_b32 v36, 4, v2\n\t" \
        "v_add_u32 v13, s44, v35\n\t" \
        "v_and_b32 v36, s8, v36\n\t" \
        "v_and_b32 v19, 15, v2\n\t" \
        "v_lshlrev_b32 v36, s9, v36\n\t" \
        "v_lshlrev_b32 v3, 4, v2\n\t" \
        "v_add_u32 v18, s10, v36\n\t" \
        "v_and_b32 v32, 32, v2\n\t" \
        "v_mul_u32_u24 v33, 0x1f80, v18\n\t" \
        "v_xor_b32 v5, 32, v3\n\t" \
        "v_mad_u32_u24 v17, v19, 36, v33\n\t" \
        "v_xor_b32 v4, v3, v32\n\t" \
        "s_cmp_eq_u32 s11, 0\n\t" \
        "s_mov_b32 m0, s39\n\t" \
        "s_waitcnt lgkmcnt(0)\n\t" \
        "s_cbranch_scc1 .LR_start%=\n\t" \
        "buffer_load_dwordx4 v[40:43], v17, s[20:23], s37 offen\n\t" \
        "buffer_load_dwordx4 v[44:47], v17, s[20:23], s37 offen offset:16\n\t" \
        "buffer_load_dword v48, v17, s[20:23], s37 offen offset:32\n\t" \
        "s_cmp_eq_u32 s11, 1\n\t" \
        "s_cbranch_scc1 .LT_start%=\n\t" \
        "s_branch .LH_start%=\n\t" \
        ".LR_start%=:\n\t" \
        "s_mov_b32 s57, s40\n\t" \
        "buffer_load_dwordx4 v3, s[24:27], s57 offen offset:0 lds\n\t" \
        "buffer_load_dwordx4 v4, s[24:27], s57 offen offset:1024 lds\n\t" \
        "buffer_load_dwordx4 v5, s[24:27], s57 offen offset:2048 lds\n\t" \
        "s_and_b32 s61, s35, 1\n\t" \
        "v_lshrrev_b32 v40, 4, v2\n\t" \
        "v_and_b32 v41, 15, v2\n\t" \
        "v_and_b32 v42, 16, v2\n\t" \
        "s_sub_u32 s57, s36, 0xa800\n\t" \
        "s_mov_b32 m0, s38\n\t" \
        "s_nop 0\n\t" \
        "buffer_load_dwordx4 v13, s[16:19], s57 offen sc1 lds\n\t" \
        "s_lshr_b32 s62, s35, 1\n\t" \
        "v_lshlrev_b32 v42, 1, v42\n\t" \
        "v_bfe_u32 v43, v2, 2, 2\n\t" \
        "v_mul_u32_u24 v45, 0xc0, v41\n\t" \
        "s_add_u32 s57, s36, 0x0\n\t" \
        "s_add_u32 m0, s38, 0xf80\n\t" \
        "s_nop 0\n\t" \
        "buffer_load_dwordx4 v13, s[16:19], s57 offen sc1 lds\n\t" \
        "v_lshl_or_b32 v43, v40, 3, v43\n\t" \
        "v_and_b32 v44, 3, v2\n\t" \
        "v_mul_u32_u24 v43, 0xc0, v43\n\t" \
        "s_lshl_b32 s63, s61, 7\n\t" \
        "s_add_u32 s57, s36, 0xa800\n\t" \
        "s_add_u32 m0, s38, 0x1f00\n\t" \
        "s_nop 0\n\t" \
        "buffer_load_dwordx4 v13, s[16:19], s57 offen sc1 lds\n\t" \
        "v_lshl_add_u32 v43, v44, 3, v43\n\t" \
        "s_add_u32 s63, s63, 0xf840\n\t" \
        "v_add3_u32 v6, v43, v42, s63\n\t" \
        "s_lshl_b32 s63, s61, 5\n\t" \
        "s_add_u32 s57, s36, 0x15000\n\t" \
        "s_add_u32 m0, s38, 0x2e80\n\t" \
        "s_nop 0\n\t" \
        "buffer_load_dwordx4 v13, s[16:19], s57 offen sc1 lds\n\t" \
        "v_xor_b32 v44, s63, v42\n\t" \
        "v_xor_b32 v7, 32, v6\n\t" \
        "v_add_u32 v43, 0xf880, v43\n\t" \
        "s_lshl_b32 s63, s61, 7\n\t" \
        "s_add_u32 s57, s40, 0x3000\n\t" \
        "s_add_u32 m0, s39, 0x3000\n\t" \
        "s_nop 0\n\t" \
        "buffer_load_dwordx4 v3, s[24:27], s57 offen offset:0 lds\n\t" \
        "v_add_u32 v8, v43, v44\n\t" \
        "v_lshrrev_b32 v43, 1, v2\n\t" \
        "v_and_b32 v43, 16, v43\n\t" \
        "v_add_u32 v45, v45, v43\n\t" \
        "buffer_load_dwordx4 v4, s[24:27], s57 offen offset:1024 lds\n\t" \
        "v_bfe_u32 v43, v2, 3, 1\n\t" \
        "v_add3_u32 v9, v45, v42, s63\n\t" \
        "v_cmp_eq_u32 vcc, s61, v43\n\t" \
        "v_add3_u32 v10, v45, v44, 64\n\t" \
        "buffer_load_dwordx4 v5, s[24:27], s57 offen offset:2048 lds\n\t" \
        "v_bfrev_b32 v44, 1\n\t" \
        "s_lshl_b32 s63, s35, 9\n\t" \
        "v_cndmask_b32 v10, v44, v10, vcc\n\t" \
        "s_add_u32 s63, s63, 0x21840\n\t" \
        "s_add_u32 s57, s36, 0x1f800\n\t" \
        "s_add_u32 m0, s38, 0x3e00\n\t" \
        "s_nop 0\n\t" \
        "buffer_load_dwordx4 v13, s[16:19], s57 offen sc1 lds\n\t" \
        "v_lshl_add_u32 v11, v2, 3, s63\n\t" \
        "s_lshl_b32 s63, s61, 10\n\t" \
        "s_sub_u32 s63, 0x200, s63\n\t" \
        "v_add_u32 v12, s63, v11\n\t" \
        "s_add_u32 s57, s36, 0x2a000\n\t" \
        "s_add_u32 m0, s38, 0x4d80\n\t" \
        "s_nop 0\n\t" \
        "buffer_load_dwordx4 v13, s[16:19], s57 offen sc1 lds\n\t" \
        "s_add_u32 s64, s32, s62\n\t" \
        "s_mul_i32 s63, s33, 0xc0\n\t" \
        "s_mul_i32 s64, s64, 0xa800\n\t" \
        "s_add_u32 s64, s64, s63\n\t" \
        "s_add_u32 s57, s36, 0x34800\n\t" \
        "s_add_u32 m0, s38, 0x5d00\n\t" \
        "s_nop 0\n\t" \
        "buffer_load_dwordx4 v13, s[16:19], s57 offen sc1 lds\n\t" \
        "v_and_b32 v38, 48, v2\n\t" \
        "v_mul_u32_u24 v37, 0xc0, v19\n\t" \
        "v_lshlrev_b32 v36, 3, v19\n\t" \
        "s_mul_i32 s63, s62, 0xf80\n\t" \
        "s_add_u32 s57, s36, 0x3f000\n\t" \
        "s_add_u32 m0, s38, 0x6c80\n\t" \
        "s_nop 0\n\t" \
        "buffer_load_dwordx4 v13, s[16:19], s57 offen sc1 lds\n\t" \
        "v_add_u32 v39, 8, v36\n\t" \
        "s_add_u32 s63, s63, 0x100\n\t" \
        "v_bitop3_b32 v39, v39, 32, v38 bitop3:0x6a\n\t" \
        "v_add3_u32 v14, v37, v39, s63\n\t" \
        "s_add_u32 s57, s40, 0x6000\n\t" \
        "s_add_u32 m0, s39, 0x6000\n\t" \
        "s_nop 0\n\t" \
        "buffer_load_dwordx4 v3, s[24:27], s57 offen offset:0 lds\n\t" \
        "v_add_u32 v39, 16, v36\n\t" \
        "s_add_u32 s63, s63, 0xc0\n\t" \
        "v_bitop3_b32 v39, v39, 32, v38 bitop3:0x6a\n\t" \
        "v_add3_u32 v15, v37, v39, s63\n\t" \
        "buffer_load_dwordx4 v4, s[24:27], s57 offen offset:1024 lds\n\t" \
        "v_add_u32 v39, 24, v36\n\t" \
        "s_add_u32 s63, s63, 0xc0\n\t" \
        "v_bitop3_b32 v39, v39, 32, v38 bitop3:0x6a\n\t" \
        "v_add3_u32 v16, v37, v39, s63\n\t" \
        "buffer_load_dwordx4 v5, s[24:27], s57 offen offset:2048 lds\n\t" \
        "v_add_u32 v20, 0xf000, v6\n\t" \
        "v_add_u32 v21, 0xf000, v7\n\t" \
        "v_add_u32 v22, 0xf000, v8\n\t" \
        "v_add_u32 v38, s32, v18\n\t" \
        "s_add_u32 s57, s40, 0x9000\n\t" \
        "s_add_u32 m0, s39, 0x9000\n\t" \
        "s_nop 0\n\t" \
        "buffer_load_dwordx4 v3, s[24:27], s57 offen offset:0 lds\n\t" \
        "v_add_u32 v39, s33, v19\n\t" \
        "v_lshl_add_u32 v38, v38, 1, -3\n\t" \
        "v_lshl_add_u32 v39, v39, 1, -3\n\t" \
        "v_cvt_f32_u32 v38, v38\n\t" \
        "buffer_load_dwordx4 v4, s[24:27], s57 offen offset:1024 lds\n\t" \
        "v_cvt_f32_u32 v39, v39\n\t" \
        "v_lshrrev_b32 v38, 16, v38\n\t" \
        "v_and_b32 v39, 0xffff0000, v39\n\t" \
        "v_or_b32 v38, v38, v39\n\t" \
        "buffer_load_dwordx4 v5, s[24:27], s57 offen offset:2048 lds\n\t" \
        "v_and_b32 v38, 0x7f807f80, v38\n\t" \
        "v_subrev_u32 v23, 0x04810481, v38\n\t" \
        "s_lshl_b32 s45, s35, 2\n\t" \
        "v_mov_b32 v24, s45\n\t" \
        "s_waitcnt vmcnt(15)\n\t" \
        "s_barrier\n\t" \
        "ds_read_b128 v[116:119], v14 offset:0\n\t" \
        "ds_read_b64_tr_b16 a[0:1], v6 offset:0\n\t" \
        "ds_read_b64_tr_b16 a[2:3], v6 offset:768\n\t" \
        "ds_read_b64_tr_b16 a[4:5], v7 offset:0\n\t" \
        "ds_read_b64_tr_b16 a[6:7], v7 offset:768\n\t" \
        "ds_read_b64_tr_b16 a[8:9], v8 offset:0\n\t" \
        "ds_read_b64_tr_b16 a[10:11], v8 offset:768\n\t" \
        "ds_read_b128 v[120:123], v14 offset:64\n\t" \
        "ds_read_b64_tr_b16 a[12:13], v6 offset:6144\n\t" \
        "ds_read_b64_tr_b16 a[14:15], v6 offset:6912\n\t" \
        "ds_read_b64_tr_b16 a[16:17], v7 offset:6144\n\t" \
        "ds_read_b64_tr_b16 a[18:19], v7 offset:6912\n\t" \
        "ds_read_b64_tr_b16 a[20:21], v8 offset:6144\n\t" \
        "ds_read_b64_tr_b16 a[22:23], v8 offset:6912\n\t" \
        "s_add_u32 s57, s40, 0xc000\n\t" \
        "s_add_u32 m0, s39, 0xc000\n\t" \
        "s_waitcnt lgkmcnt(11)\n\t" \
        "v_mfma_f32_16x16x32_bf16 v[32:35], a[0:3], v[116:119], 0\n\t" \
        "s_waitcnt vmcnt(13) lgkmcnt(0)\n\t" \
        "s_barrier\n\t" \
        "ds_read_b128 v[124:127], v14 offset:7936\n\t" \
        "v_mfma_f32_16x16x32_bf16 v[36:39], a[4:7], v[116:119], 0\n\t" \
        "buffer_load_dwordx4 v3, s[24:27], s57 offen offset:0 lds\n\t" \
        "v_mfma_f32_16x16x32_bf16 v[40:43], a[8:11], v[116:119], 0\n\t" \
        "ds_read_b128 v[128:131], v14 offset:8000\n\t" \
        "v_mfma_f32_16x16x32_bf16 v[32:35], a[12:15], v[120:123], v[32:35]\n\t" \
        "ds_read_b128 v[132:135], v14 offset:128\n\t" \
        "v_mfma_f32_16x16x32_bf16 v[36:39], a[16:19], v[120:123], v[36:39]\n\t" \
        "buffer_load_dwordx4 v4, s[24:27], s57 offen offset:1024 lds\n\t" \
        "v_mfma_f32_16x16x32_bf16 v[40:43], a[20:23], v[120:123], v[40:43]\n\t" \
        "s_waitcnt vmcnt(12)\n\t" \
        "s_barrier\n\t" \
        "ds_read_b64_tr_b16 a[24:25], v6 offset:12288\n\t" \
        "ds_read_b64_tr_b16 a[26:27], v6 offset:13056\n\t" \
        "ds_read_b64_tr_b16 a[28:29], v7 offset:12288\n\t" \
        "s_waitcnt lgkmcnt(5)\n\t" \
        "v_mfma_f32_16x16x32_bf16 v[44:47], a[0:3], v[124:127], 0\n\t" \
        "ds_read_b64_tr_b16 a[30:31], v7 offset:13056\n\t" \
        "ds_read_b64_tr_b16 a[32:33], v8 offset:12288\n\t" \
        "v_mfma_f32_16x16x32_bf16 v[48:51], a[4:7], v[124:127], 0\n\t" \
        "ds_read_b64_tr_b16 a[34:35], v8 offset:13056\n\t" \
        "v_mfma_f32_16x16x32_bf16 v[52:55], a[8:11], v[124:127], 0\n\t" \
        "ds_read_b128 v[136:139], v14 offset:8064\n\t" \
        "ds_read_b64_tr_b16 a[36:37], v6 offset:18432\n\t" \
        "ds_read_b64_tr_b16 a[38:39], v6 offset:19200\n\t" \
        "ds_read_b64_tr_b16 a[40:41], v7 offset:18432\n\t" \
        "ds_read_b64_tr_b16 a[42:43], v7 offset:19200\n\t" \
        "ds_read_b64_tr_b16 a[44:45], v8 offset:18432\n\t" \
        "ds_read_b64_tr_b16 a[46:47], v8 offset:19200\n\t" \
        "s_waitcnt lgkmcnt(14)\n\t" \
        "v_mfma_f32_16x16x32_bf16 v[44:47], a[12:15], v[128:131], v[44:47]\n\t" \
        "buffer_load_dwordx4 v5, s[24:27], s57 offen offset:2048 lds\n\t" \
        "v_mfma_f32_16x16x32_bf16 v[48:51], a[16:19], v[128:131], v[48:51]\n\t" \
        "ds_read_b128 v[140:143], v15 offset:0\n\t" \
        "s_add_u32 s57, s36, 0x49800\n\t" \
        "s_add_u32 m0, s38, 0x7c00\n\t" \
        "v_mfma_f32_16x16x32_bf16 v[52:55], a[20:23], v[128:131], v[52:55]\n\t" \
        "s_waitcnt vmcnt(11) lgkmcnt(1)\n\t" \
        "s_barrier\n\t" \
        "ds_read_b128 v[144:147], v15 offset:7936\n\t" \
        "v_mfma_f32_16x16x32_bf16 v[32:35], a[24:27], v[132:135], v[32:35]\n\t" \
        "buffer_load_dwordx4 v13, s[16:19], s57 offen sc1 lds\n\t" \
        "v_mfma_f32_16x16x32_bf16 v[36:39], a[28:31], v[132:135], v[36:39]\n\t" \
        "ds_read_b128 v[116:119], v14 offset:15872\n\t" \
        "v_mfma_f32_16x16x32_bf16 v[40:43], a[32:35], v[132:135], v[40:43]\n\t" \
        "ds_read_b128 v[120:123], v14 offset:15936\n\t" \
        "s_add_u32 s57, s36, 0x54000\n\t" \
        "s_add_u32 m0, s38, 0x8b80\n\t" \
        "v_mfma_f32_16x16x32_bf16 v[44:47], a[24:27], v[136:139], v[44:47]\n\t" \
        "ds_read_b128 v[124:127], v14 offset:16000\n\t" \
        "v_mfma_f32_16x16x32_bf16 v[48:51], a[28:31], v[136:139], v[48:51]\n\t" \
        "buffer_load_dwordx4 v13, s[16:19], s57 offen sc1 lds\n\t" \
        "v_mfma_f32_16x16x32_bf16 v[52:55], a[32:35], v[136:139], v[52:55]\n\t" \
        "ds_read_b128 v[128:131], v15 offset:15872\n\t" \
        "s_waitcnt lgkmcnt(5)\n\t" \
        "v_mfma_f32_16x16x32_bf16 v[32:35], a[36:39], v[140:143], v[32:35]\n\t" \
        "s_add_u32 s57, s40, 0xf000\n\t" \
        "s_add_u32 m0, s39, 0xf000\n\t" \
        "v_mfma_f32_16x16x32_bf16 v[36:39], a[40:43], v[140:143], v[36:39]\n\t" \
        "v_mfma_f32_16x16x32_bf16 v[40:43], a[44:47], v[140:143], v[40:43]\n\t" \
        "buffer_load_dwordx4 v3, s[24:27], s57 offen offset:0 lds\n\t" \
        "s_waitcnt lgkmcnt(4)\n\t" \
        "v_mfma_f32_16x16x32_bf16 v[44:47], a[36:39], v[144:147], v[44:47]\n\t" \
        "v_mfma_f32_16x16x32_bf16 v[48:51], a[40:43], v[144:147], v[48:51]\n\t" \
        "v_mfma_f32_16x16x32_bf16 v[52:55], a[44:47], v[144:147], v[52:55]\n\t" \
        "s_waitcnt vmcnt(12)\n\t" \
        "s_barrier\n\t" \
        "ds_read_b128 v[132:135], v14 offset:23808\n\t" \
        "s_waitcnt lgkmcnt(4)\n\t" \
        "v_mfma_f32_16x16x32_bf16 v[56:59], a[0:3], v[116:119], 0\n\t" \
        "buffer_load_dwordx4 v4, s[24:27], s57 offen offset:1024 lds\n\t" \
        "v_mfma_f32_16x16x32_bf16 v[60:63], a[4:7], v[116:119], 0\n\t" \
        "ds_read_b128 v[136:139], v14 offset:23872\n\t" \
        "v_mfma_f32_16x16x32_bf16 v[64:67], a[8:11], v[116:119], 0\n\t" \
        "ds_read_b128 v[140:143], v14 offset:23936\n\t" \
        "s_waitcnt lgkmcnt(4)\n\t" \
        "v_mfma_f32_16x16x32_bf16 v[56:59], a[12:15], v[120:123], v[56:59]\n\t" \
        "ds_read_b128 v[144:147], v15 offset:23808\n\t" \
        "v_mfma_f32_16x16x32_bf16 v[60:63], a[16:19], v[120:123], v[60:63]\n\t" \
        "buffer_load_dwordx4 v5, s[24:27], s57 offen offset:2048 lds\n\t" \
        "v_mfma_f32_16x16x32_bf16 v[64:67], a[20:23], v[120:123], v[64:67]\n\t" \
        "ds_read_b128 v[116:119], v15 offset:64\n\t" \
        "v_mfma_f32_16x16x32_bf16 v[56:59], a[24:27], v[124:127], v[56:59]\n\t" \
        "ds_read_b128 v[120:123], v15 offset:8000\n\t" \
        "s_add_u32 s57, s40, 0x12000\n\t" \
        "s_add_u32 m0, s39, 0x12000\n\t" \
        "v_mfma_f32_16x16x32_bf16 v[60:63], a[28:31], v[124:127], v[60:63]\n\t" \
        "v_mfma_f32_16x16x32_bf16 v[64:67], a[32:35], v[124:127], v[64:67]\n\t" \
        "buffer_load_dwordx4 v3, s[24:27], s57 offen offset:0 lds\n\t" \
        "s_waitcnt lgkmcnt(6)\n\t" \
        "v_mfma_f32_16x16x32_bf16 v[56:59], a[36:39], v[128:131], v[56:59]\n\t" \
        "ds_read_b128 v[124:127], v15 offset:15936\n\t" \
        "v_mfma_f32_16x16x32_bf16 v[60:63], a[40:43], v[128:131], v[60:63]\n\t" \
        "v_mfma_f32_16x16x32_bf16 v[64:67], a[44:47], v[128:131], v[64:67]\n\t" \
        "s_waitcnt vmcnt(12)\n\t" \
        "s_barrier\n\t" \
        "ds_read_b64_tr_b16 a[48:49], v6 offset:24576\n\t" \
        "s_waitcnt lgkmcnt(6)\n\t" \
        "v_mfma_f32_16x16x32_bf16 v[68:71], a[0:3], v[132:135], 0\n\t" \
        "buffer_load_dwordx4 v4, s[24:27], s57 offen offset:1024 lds\n\t" \
        "v_mfma_f32_16x16x32_bf16 v[72:75], a[4:7], v[132:135], 0\n\t" \
        "ds_read_b64_tr_b16 a[50:51], v6 offset:25344\n\t" \
        "v_mfma_f32_16x16x32_bf16 v[76:79], a[8:11], v[132:135], 0\n\t" \
        "ds_read_b64_tr_b16 a[52:53], v7 offset:24576\n\t" \
        "v_mfma_f32_16x16x32_bf16 v[68:71], a[12:15], v[136:139], v[68:71]\n\t" \
        "ds_read_b64_tr_b16 a[54:55], v7 offset:25344\n\t" \
        "v_mfma_f32_16x16x32_bf16 v[72:75], a[16:19], v[136:139], v[72:75]\n\t" \
        "buffer_load_dwordx4 v5, s[24:27], s57 offen offset:2048 lds\n\t" \
        "v_mfma_f32_16x16x32_bf16 v[76:79], a[20:23], v[136:139], v[76:79]\n\t" \
        "ds_read_b64_tr_b16 a[56:57], v8 offset:24576\n\t" \
        "s_waitcnt lgkmcnt(6)\n\t" \
        "v_mfma_f32_16x16x32_bf16 v[68:71], a[24:27], v[140:143], v[68:71]\n\t" \
        "ds_read_b64_tr_b16 a[58:59], v8 offset:25344\n\t" \
        "s_add_u32 s57, s40, 0x15000\n\t" \
        "s_add_u32 m0, s39, 0x15000\n\t" \
        "v_mfma_f32_16x16x32_bf16 v[72:75], a[28:31], v[140:143], v[72:75]\n\t" \
        "ds_read_b128 v[128:131], v15 offset:23872\n\t" \
        "v_mfma_f32_16x16x32_bf16 v[76:79], a[32:35], v[140:143], v[76:79]\n\t" \
        "buffer_load_dwordx4 v3, s[24:27], s57 offen offset:0 lds\n\t" \
        "v_mfma_f32_16x16x32_bf16 v[68:71], a[36:39], v[144:147], v[68:71]\n\t" \
        "ds_read_b128 v[132:135], v15 offset:128\n\t" \
        "v_mfma_f32_16x16x32_bf16 v[72:75], a[40:43], v[144:147], v[72:75]\n\t" \
        "ds_read_b64_tr_b16 a[60:61], v6 offset:30720\n\t" \
        "v_mfma_f32_16x16x32_bf16 v[76:79], a[44:47], v[144:147], v[76:79]\n\t" \
        "ds_read_b64_tr_b16 a[62:63], v6 offset:31488\n\t" \
        "s_waitcnt lgkmcnt(8)\n\t" \
        "v_mfma_f32_16x16x32_bf16 v[32:35], a[48:51], v[116:119], v[32:35]\n\t" \
        "buffer_load_dwordx4 v4, s[24:27], s57 offen offset:1024 lds\n\t" \
        "s_waitcnt lgkmcnt(6)\n\t" \
        "v_mfma_f32_16x16x32_bf16 v[36:39], a[52:55], v[116:119], v[36:39]\n\t" \
        "ds_read_b64_tr_b16 a[64:65], v7 offset:30720\n\t" \
        "s_waitcnt lgkmcnt(5)\n\t" \
        "v_mfma_f32_16x16x32_bf16 v[40:43], a[56:59], v[116:119], v[40:43]\n\t" \
        "ds_read_b64_tr_b16 a[66:67], v7 offset:31488\n\t" \
        "v_mfma_f32_16x16x32_bf16 v[44:47], a[48:51], v[120:123], v[44:47]\n\t" \
        "ds_read_b64_tr_b16 a[68:69], v8 offset:30720\n\t" \
        "v_mfma_f32_16x16x32_bf16 v[48:51], a[52:55], v[120:123], v[48:51]\n\t" \
        "buffer_load_dwordx4 v5, s[24:27], s57 offen offset:2048 lds\n\t" \
        "v_mfma_f32_16x16x32_bf16 v[52:55], a[56:59], v[120:123], v[52:55]\n\t" \
        "ds_read_b64_tr_b16 a[70:71], v8 offset:31488\n\t" \
        "v_mfma_f32_16x16x32_bf16 v[56:59], a[48:51], v[124:127], v[56:59]\n\t" \
        "ds_read_b128 v[136:139], v15 offset:8064\n\t" \
        "s_add_u32 s57, s40, 0x18000\n\t" \
        "s_mov_b32 m0, s39\n\t" \
        "v_mfma_f32_16x16x32_bf16 v[60:63], a[52:55], v[124:127], v[60:63]\n\t" \
        "ds_read_b128 v[140:143], v15 offset:16000\n\t" \
        "v_mfma_f32_16x16x32_bf16 v[64:67], a[56:59], v[124:127], v[64:67]\n\t" \
        "buffer_load_dwordx4 v3, s[24:27], s57 offen offset:0 lds\n\t" \
        "s_waitcnt lgkmcnt(7)\n\t" \
        "v_mfma_f32_16x16x32_bf16 v[68:71], a[48:51], v[128:131], v[68:71]\n\t" \
        "ds_read_b128 v[144:147], v15 offset:23936\n\t" \
        "v_mfma_f32_16x16x32_bf16 v[72:75], a[52:55], v[128:131], v[72:75]\n\t" \
        "ds_read_b128 v[116:119], v16 offset:0\n\t" \
        "v_mfma_f32_16x16x32_bf16 v[76:79], a[56:59], v[128:131], v[76:79]\n\t" \
        "s_waitcnt vmcnt(15) lgkmcnt(4)\n\t" \
        "s_barrier\n\t" \
        "ds_read_b64_tr_b16 a[72:73], v6 offset:36864\n\t" \
        "v_mfma_f32_16x16x32_bf16 v[32:35], a[60:63], v[132:135], v[32:35]\n\t" \
        "buffer_load_dwordx4 v4, s[24:27], s57 offen offset:1024 lds\n\t" \
        "v_mfma_f32_16x16x32_bf16 v[36:39], a[64:67], v[132:135], v[36:39]\n\t" \
        "ds_read_b64_tr_b16 a[74:75], v6 offset:37632\n\t" \
        "v_mfma_f32_16x16x32_bf16 v[40:43], a[68:71], v[132:135], v[40:43]\n\t" \
        "ds_read_b64_tr_b16 a[76:77], v7 offset:36864\n\t" \
        "s_waitcnt lgkmcnt(6)\n\t" \
        "v_mfma_f32_16x16x32_bf16 v[44:47], a[60:63], v[136:139], v[44:47]\n\t" \
        "ds_read_b64_tr_b16 a[78:79], v7 offset:37632\n\t" \
        "v_mfma_f32_16x16x32_bf16 v[48:51], a[64:67], v[136:139], v[48:51]\n\t" \
        "ds_read_b64_tr_b16 a[80:81], v8 offset:36864\n\t" \
        "v_mfma_f32_16x16x32_bf16 v[52:55], a[68:71], v[136:139], v[52:55]\n\t" \
        "ds_read_b64_tr_b16 a[82:83], v8 offset:37632\n\t" \
        "s_waitcnt lgkmcnt(8)\n\t" \
        "v_mfma_f32_16x16x32_bf16 v[56:59], a[60:63], v[140:143], v[56:59]\n\t" \
        "buffer_load_dwordx4 v5, s[24:27], s57 offen offset:2048 lds\n\t" \
        "v_mfma_f32_16x16x32_bf16 v[60:63], a[64:67], v[140:143], v[60:63]\n\t" \
        "ds_read_b128 v[120:123], v16 offset:7936\n\t" \
        "v_mfma_f32_16x16x32_bf16 v[64:67], a[68:71], v[140:143], v[64:67]\n\t" \
        "ds_read_b128 v[124:127], v16 offset:15872\n\t" \
        "s_waitcnt lgkmcnt(8)\n\t" \
        "v_mfma_f32_16x16x32_bf16 v[68:71], a[60:63], v[144:147], v[68:71]\n\t" \
        "ds_read_b128 v[128:131], v16 offset:23808\n\t" \
        "v_mfma_f32_16x16x32_bf16 v[72:75], a[64:67], v[144:147], v[72:75]\n\t" \
        "ds_read_b128 v[132:135], v16 offset:64\n\t" \
        "s_add_u32 s57, s40, 0x1b000\n\t" \
        "s_add_u32 m0, s39, 0x3000\n\t" \
        "v_mfma_f32_16x16x32_bf16 v[76:79], a[68:71], v[144:147], v[76:79]\n\t" \
        "ds_read_b64_tr_b16 a[84:85], v6 offset:43008\n\t" \
        "s_waitcnt lgkmcnt(9)\n\t" \
        "v_mfma_f32_16x16x32_bf16 v[32:35], a[72:75], v[116:119], v[32:35]\n\t" \
        "buffer_load_dwordx4 v3, s[24:27], s57 offen offset:0 lds\n\t" \
        "s_waitcnt lgkmcnt(7)\n\t" \
        "v_mfma_f32_16x16x32_bf16 v[36:39], a[76:79], v[116:119], v[36:39]\n\t" \
        "ds_read_b64_tr_b16 a[86:87], v6 offset:43776\n\t" \
        "s_waitcnt lgkmcnt(6)\n\t" \
        "v_mfma_f32_16x16x32_bf16 v[40:43], a[80:83], v[116:119], v[40:43]\n\t" \
        "ds_read_b64_tr_b16 a[88:89], v7 offset:43008\n\t" \
        "s_waitcnt lgkmcnt(6)\n\t" \
        "v_mfma_f32_16x16x32_bf16 v[44:47], a[72:75], v[120:123], v[44:47]\n\t" \
        "ds_read_b64_tr_b16 a[90:91], v7 offset:43776\n\t" \
        "v_mfma_f32_16x16x32_bf16 v[48:51], a[76:79], v[120:123], v[48:51]\n\t" \
        "ds_read_b64_tr_b16 a[92:93], v8 offset:43008\n\t" \
        "v_mfma_f32_16x16x32_bf16 v[52:55], a[80:83], v[120:123], v[52:55]\n\t" \
        "ds_read_b64_tr_b16 a[94:95], v8 offset:43776\n\t" \
        "s_waitcnt lgkmcnt(8)\n\t" \
        "v_mfma_f32_16x16x32_bf16 v[56:59], a[72:75], v[124:127], v[56:59]\n\t" \
        "buffer_load_dwordx4 v4, s[24:27], s57 offen offset:1024 lds\n\t" \
        "v_mfma_f32_16x16x32_bf16 v[60:63], a[76:79], v[124:127], v[60:63]\n\t" \
        "ds_read_b128 v[136:139], v16 offset:8000\n\t" \
        "v_mfma_f32_16x16x32_bf16 v[64:67], a[80:83], v[124:127], v[64:67]\n\t" \
        "ds_read_b128 v[140:143], v16 offset:15936\n\t" \
        "s_waitcnt lgkmcnt(8)\n\t" \
        "v_mfma_f32_16x16x32_bf16 v[68:71], a[72:75], v[128:131], v[68:71]\n\t" \
        "ds_read_b128 v[144:147], v16 offset:23872\n\t" \
        "v_mfma_f32_16x16x32_bf16 v[72:75], a[76:79], v[128:131], v[72:75]\n\t" \
        "ds_read_b128 v[116:119], v16 offset:128\n\t" \
        "v_mfma_f32_16x16x32_bf16 v[76:79], a[80:83], v[128:131], v[76:79]\n\t" \
        "s_waitcnt vmcnt(14) lgkmcnt(4)\n\t" \
        "s_barrier\n\t" \
        "ds_read_b64_tr_b16 a[96:97], v6 offset:49152\n\t" \
        "v_mfma_f32_16x16x32_bf16 v[32:35], a[84:87], v[132:135], v[32:35]\n\t" \
        "buffer_load_dwordx4 v5, s[24:27], s57 offen offset:2048 lds\n\t" \
        "v_mfma_f32_16x16x32_bf16 v[36:39], a[88:91], v[132:135], v[36:39]\n\t" \
        "ds_read_b64_tr_b16 a[98:99], v6 offset:49920\n\t" \
        "v_mfma_f32_16x16x32_bf16 v[40:43], a[92:95], v[132:135], v[40:43]\n\t" \
        "ds_read_b64_tr_b16 a[100:101], v7 offset:49152\n\t" \
        "s_waitcnt lgkmcnt(6)\n\t" \
        "v_mfma_f32_16x16x32_bf16 v[44:47], a[84:87], v[136:139], v[44:47]\n\t" \
        "ds_read_b64_tr_b16 a[102:103], v7 offset:49920\n\t" \
        "v_mfma_f32_16x16x32_bf16 v[48:51], a[88:91], v[136:139], v[48:51]\n\t" \
        "ds_read_b64_tr_b16 a[104:105], v8 offset:49152\n\t" \
        "v_mfma_f32_16x16x32_bf16 v[52:55], a[92:95], v[136:139], v[52:55]\n\t" \
        "ds_read_b64_tr_b16 a[106:107], v8 offset:49920\n\t" \
        "s_waitcnt lgkmcnt(8)\n\t" \
        "v_mfma_f32_16x16x32_bf16 v[56:59], a[84:87], v[140:143], v[56:59]\n\t" \
        "buffer_load_dwordx4 v[148:151], v17, s[20:23], s37 offen\n\t" \
        "v_mfma_f32_16x16x32_bf16 v[60:63], a[88:91], v[140:143], v[60:63]\n\t" \
        "ds_read_b128 v[120:123], v16 offset:8064\n\t" \
        "v_mfma_f32_16x16x32_bf16 v[64:67], a[92:95], v[140:143], v[64:67]\n\t" \
        "ds_read_b128 v[124:127], v16 offset:16000\n\t" \
        "s_waitcnt lgkmcnt(8)\n\t" \
        "v_mfma_f32_16x16x32_bf16 v[68:71], a[84:87], v[144:147], v[68:71]\n\t" \
        "ds_read_b128 v[128:131], v16 offset:23936\n\t" \
        "v_mfma_f32_16x16x32_bf16 v[72:75], a[88:91], v[144:147], v[72:75]\n\t" \
        "ds_read_b128 v[132:135], v14 offset:3968\n\t" \
        "v_mfma_f32_16x16x32_bf16 v[76:79], a[92:95], v[144:147], v[76:79]\n\t" \
        "ds_read_b64_tr_b16 a[108:109], v6 offset:55296\n\t" \
        "s_waitcnt lgkmcnt(9)\n\t" \
        "v_mfma_f32_16x16x32_bf16 v[32:35], a[96:99], v[116:119], v[32:35]\n\t" \
        "buffer_load_dwordx4 v[152:155], v17, s[20:23], s37 offen offset:16\n\t" \
        "s_waitcnt lgkmcnt(7)\n\t" \
        "v_mfma_f32_16x16x32_bf16 v[36:39], a[100:103], v[116:119], v[36:39]\n\t" \
        "ds_read_b64_tr_b16 a[110:111], v6 offset:56064\n\t" \
        "s_waitcnt lgkmcnt(6)\n\t" \
        "v_mfma_f32_16x16x32_bf16 v[40:43], a[104:107], v[116:119], v[40:43]\n\t" \
        "ds_read_b64_tr_b16 a[112:113], v7 offset:55296\n\t" \
        "s_waitcnt lgkmcnt(6)\n\t" \
        "v_mfma_f32_16x16x32_bf16 v[44:47], a[96:99], v[120:123], v[44:47]\n\t" \
        "ds_read_b64_tr_b16 a[114:115], v7 offset:56064\n\t" \
        "v_mfma_f32_16x16x32_bf16 v[48:51], a[100:103], v[120:123], v[48:51]\n\t" \
        "ds_read_b64_tr_b16 a[116:117], v8 offset:55296\n\t" \
        "v_mfma_f32_16x16x32_bf16 v[52:55], a[104:107], v[120:123], v[52:55]\n\t" \
        "ds_read_b64_tr_b16 a[118:119], v8 offset:56064\n\t" \
        "s_waitcnt lgkmcnt(8)\n\t" \
        "v_mfma_f32_16x16x32_bf16 v[56:59], a[96:99], v[124:127], v[56:59]\n\t" \
        "buffer_load_dword v156, v17, s[20:23], s37 offen offset:32\n\t" \
        "v_mfma_f32_16x16x32_bf16 v[60:63], a[100:103], v[124:127], v[60:63]\n\t" \
        "ds_read_b128 v[136:139], v14 offset:11904\n\t" \
        "v_mfma_f32_16x16x32_bf16 v[64:67], a[104:107], v[124:127], v[64:67]\n\t" \
        "ds_read_b128 v[140:143], v14 offset:19840\n\t" \
        "s_waitcnt lgkmcnt(8)\n\t" \
        "v_mfma_f32_16x16x32_bf16 v[68:71], a[96:99], v[128:131], v[68:71]\n\t" \
        "ds_read_b128 v[144:147], v14 offset:27776\n\t" \
        "v_mfma_f32_16x16x32_bf16 v[72:75], a[100:103], v[128:131], v[72:75]\n\t" \
        "ds_read_b128 v[116:119], v14 offset:4032\n\t" \
        "s_add_u32 s57, s40, 0x1e000\n\t" \
        "s_add_u32 m0, s39, 0x6000\n\t" \
        "v_mfma_f32_16x16x32_bf16 v[76:79], a[104:107], v[128:131], v[76:79]\n\t" \
        "s_waitcnt vmcnt(15) lgkmcnt(4)\n\t" \
        "s_barrier\n\t" \
        "ds_read_b64_tr_b16 a[120:121], v20 offset:0\n\t" \
        "v_mfma_f32_16x16x32_bf16 v[32:35], a[108:111], v[132:135], v[32:35]\n\t" \
        "buffer_load_dwordx4 v3, s[24:27], s57 offen offset:0 lds\n\t" \
        "v_mfma_f32_16x16x32_bf16 v[36:39], a[112:115], v[132:135], v[36:39]\n\t" \
        "ds_read_b64_tr_b16 a[122:123], v20 offset:768\n\t" \
        "v_mfma_f32_16x16x32_bf16 v[40:43], a[116:119], v[132:135], v[40:43]\n\t" \
        "ds_read_b64_tr_b16 a[124:125], v21 offset:0\n\t" \
        "s_waitcnt lgkmcnt(6)\n\t" \
        "v_mfma_f32_16x16x32_bf16 v[44:47], a[108:111], v[136:139], v[44:47]\n\t" \
        "ds_read_b64_tr_b16 a[126:127], v21 offset:768\n\t" \
        "v_mfma_f32_16x16x32_bf16 v[48:51], a[112:115], v[136:139], v[48:51]\n\t" \
        "ds_read_b64_tr_b16 a[128:129], v22 offset:0\n\t" \
        "v_mfma_f32_16x16x32_bf16 v[52:55], a[116:119], v[136:139], v[52:55]\n\t" \
        "ds_read_b64_tr_b16 a[130:131], v22 offset:768\n\t" \
        "s_waitcnt lgkmcnt(8)\n\t" \
        "v_mfma_f32_16x16x32_bf16 v[56:59], a[108:111], v[140:143], v[56:59]\n\t" \
        "buffer_load_dwordx4 v4, s[24:27], s57 offen offset:1024 lds\n\t" \
        "v_mfma_f32_16x16x32_bf16 v[60:63], a[112:115], v[140:143], v[60:63]\n\t" \
        "ds_read_b128 v[120:123], v14 offset:11968\n\t" \
        "v_mfma_f32_16x16x32_bf16 v[64:67], a[116:119], v[140:143], v[64:67]\n\t" \
        "ds_read_b128 v[124:127], v14 offset:19904\n\t" \
        "s_waitcnt lgkmcnt(8)\n\t" \
        "v_mfma_f32_16x16x32_bf16 v[68:71], a[108:111], v[144:147], v[68:71]\n\t" \
        "ds_read_b128 v[128:131], v14 offset:27840\n\t" \
        "v_mfma_f32_16x16x32_bf16 v[72:75], a[112:115], v[144:147], v[72:75]\n\t" \
        "ds_read_b128 v[132:135], v14 offset:4096\n\t" \
        "v_mfma_f32_16x16x32_bf16 v[76:79], a[116:119], v[144:147], v[76:79]\n\t" \
        "ds_read_b64_tr_b16 a[132:133], v20 offset:6144\n\t" \
        "s_waitcnt lgkmcnt(9)\n\t" \
        "v_mfma_f32_16x16x32_bf16 v[32:35], a[120:123], v[116:119], v[32:35]\n\t" \
        "buffer_load_dwordx4 v5, s[24:27], s57 offen offset:2048 lds\n\t" \
        "s_waitcnt lgkmcnt(7)\n\t" \
        "v_mfma_f32_16x16x32_bf16 v[36:39], a[124:127], v[116:119], v[36:39]\n\t" \
        "ds_read_b64_tr_b16 a[134:135], v20 offset:6912\n\t" \
        "s_waitcnt lgkmcnt(6)\n\t" \
        "v_mfma_f32_16x16x32_bf16 v[40:43], a[128:131], v[116:119], v[40:43]\n\t" \
        "ds_read_b64_tr_b16 a[136:137], v21 offset:6144\n\t" \
        "s_waitcnt lgkmcnt(6)\n\t" \
        "v_mfma_f32_16x16x32_bf16 v[44:47], a[120:123], v[120:123], v[44:47]\n\t" \
        "ds_read_b64_tr_b16 a[138:139], v21 offset:6912\n\t" \
        "v_mfma_f32_16x16x32_bf16 v[48:51], a[124:127], v[120:123], v[48:51]\n\t" \
        "ds_read_b64_tr_b16 a[140:141], v22 offset:6144\n\t" \
        "s_add_u32 s57, s40, 0x21000\n\t" \
        "s_add_u32 m0, s39, 0x9000\n\t" \
        "v_mfma_f32_16x16x32_bf16 v[52:55], a[128:131], v[120:123], v[52:55]\n\t" \
        "ds_read_b64_tr_b16 a[142:143], v22 offset:6912\n\t" \
        "s_waitcnt lgkmcnt(8)\n\t" \
        "v_mfma_f32_16x16x32_bf16 v[56:59], a[120:123], v[124:127], v[56:59]\n\t" \
        "buffer_load_dwordx4 v3, s[24:27], s57 offen offset:0 lds\n\t" \
        "v_mfma_f32_16x16x32_bf16 v[60:63], a[124:127], v[124:127], v[60:63]\n\t" \
        "ds_read_b128 v[136:139], v14 offset:12032\n\t" \
        "v_mfma_f32_16x16x32_bf16 v[64:67], a[128:131], v[124:127], v[64:67]\n\t" \
        "ds_read_b128 v[140:143], v14 offset:19968\n\t" \
        "s_waitcnt lgkmcnt(8)\n\t" \
        "v_mfma_f32_16x16x32_bf16 v[68:71], a[120:123], v[128:131], v[68:71]\n\t" \
        "ds_read_b128 v[144:147], v14 offset:27904\n\t" \
        "v_mfma_f32_16x16x32_bf16 v[72:75], a[124:127], v[128:131], v[72:75]\n\t" \
        "ds_read_b128 v[116:119], v15 offset:3968\n\t" \
        "v_mfma_f32_16x16x32_bf16 v[76:79], a[128:131], v[128:131], v[76:79]\n\t" \
        "s_waitcnt vmcnt(16) lgkmcnt(4)\n\t" \
        "s_barrier\n\t" \
        "ds_read_b64_tr_b16 a[144:145], v20 offset:12288\n\t" \
        "v_mfma_f32_16x16x32_bf16 v[32:35], a[132:135], v[132:135], v[32:35]\n\t" \
        "buffer_load_dwordx4 v4, s[24:27], s57 offen offset:1024 lds\n\t" \
        "v_mfma_f32_16x16x32_bf16 v[36:39], a[136:139], v[132:135], v[36:39]\n\t" \
        "ds_read_b64_tr_b16 a[146:147], v20 offset:13056\n\t" \
        "v_mfma_f32_16x16x32_bf16 v[40:43], a[140:143], v[132:135], v[40:43]\n\t" \
        "ds_read_b64_tr_b16 a[148:149], v21 offset:12288\n\t" \
        "s_waitcnt lgkmcnt(6)\n\t" \
        "v_mfma_f32_16x16x32_bf16 v[44:47], a[132:135], v[136:139], v[44:47]\n\t" \
        "ds_read_b64_tr_b16 a[150:151], v21 offset:13056\n\t" \
        "v_mfma_f32_16x16x32_bf16 v[48:51], a[136:139], v[136:139], v[48:51]\n\t" \
        "ds_read_b64_tr_b16 a[152:153], v22 offset:12288\n\t" \
        "v_mfma_f32_16x16x32_bf16 v[52:55], a[140:143], v[136:139], v[52:55]\n\t" \
        "ds_read_b64_tr_b16 a[154:155], v22 offset:13056\n\t" \
        "s_waitcnt lgkmcnt(8)\n\t" \
        "v_mfma_f32_16x16x32_bf16 v[56:59], a[132:135], v[140:143], v[56:59]\n\t" \
        "buffer_load_dwordx4 v5, s[24:27], s57 offen offset:2048 lds\n\t" \
        "v_mfma_f32_16x16x32_bf16 v[60:63], a[136:139], v[140:143], v[60:63]\n\t" \
        "ds_read_b128 v[120:123], v15 offset:11904\n\t" \
        "v_mfma_f32_16x16x32_bf16 v[64:67], a[140:143], v[140:143], v[64:67]\n\t" \
        "ds_read_b128 v[124:127], v15 offset:19840\n\t" \
        "s_waitcnt lgkmcnt(8)\n\t" \
        "v_mfma_f32_16x16x32_bf16 v[68:71], a[132:135], v[144:147], v[68:71]\n\t" \
        "ds_read_b128 v[128:131], v15 offset:27776\n\t" \
        "v_mfma_f32_16x16x32_bf16 v[72:75], a[136:139], v[144:147], v[72:75]\n\t" \
        "ds_read_b128 v[132:135], v15 offset:4032\n\t" \
        "s_add_u32 s57, s40, 0x24000\n\t" \
        "s_add_u32 m0, s39, 0xc000\n\t" \
        "v_mfma_f32_16x16x32_bf16 v[76:79], a[140:143], v[144:147], v[76:79]\n\t" \
        "ds_read_b64_tr_b16 a[156:157], v20 offset:18432\n\t" \
        "s_waitcnt lgkmcnt(9)\n\t" \
        "v_mfma_f32_16x16x32_bf16 v[32:35], a[144:147], v[116:119], v[32:35]\n\t" \
        "buffer_load_dwordx4 v3, s[24:27], s57 offen offset:0 lds\n\t" \
        "s_waitcnt lgkmcnt(7)\n\t" \
        "v_mfma_f32_16x16x32_bf16 v[36:39], a[148:151], v[116:119], v[36:39]\n\t" \
        "ds_read_b64_tr_b16 a[158:159], v20 offset:19200\n\t" \
        "s_waitcnt lgkmcnt(6)\n\t" \
        "v_mfma_f32_16x16x32_bf16 v[40:43], a[152:155], v[116:119], v[40:43]\n\t" \
        "ds_read_b64_tr_b16 a[160:161], v21 offset:18432\n\t" \
        "s_waitcnt lgkmcnt(6)\n\t" \
        "v_mfma_f32_16x16x32_bf16 v[44:47], a[144:147], v[120:123], v[44:47]\n\t" \
        "ds_read_b64_tr_b16 a[162:163], v21 offset:19200\n\t" \
        "v_mfma_f32_16x16x32_bf16 v[48:51], a[148:151], v[120:123], v[48:51]\n\t" \
        "ds_read_b64_tr_b16 a[164:165], v22 offset:18432\n\t" \
        "v_mfma_f32_16x16x32_bf16 v[52:55], a[152:155], v[120:123], v[52:55]\n\t" \
        "ds_read_b64_tr_b16 a[166:167], v22 offset:19200\n\t" \
        "s_waitcnt lgkmcnt(8)\n\t" \
        "v_mfma_f32_16x16x32_bf16 v[56:59], a[144:147], v[124:127], v[56:59]\n\t" \
        "buffer_load_dwordx4 v4, s[24:27], s57 offen offset:1024 lds\n\t" \
        "v_mfma_f32_16x16x32_bf16 v[60:63], a[148:151], v[124:127], v[60:63]\n\t" \
        "ds_read_b128 v[136:139], v15 offset:11968\n\t" \
        "v_mfma_f32_16x16x32_bf16 v[64:67], a[152:155], v[124:127], v[64:67]\n\t" \
        "ds_read_b128 v[140:143], v15 offset:19904\n\t" \
        "s_waitcnt lgkmcnt(8)\n\t" \
        "v_mfma_f32_16x16x32_bf16 v[68:71], a[144:147], v[128:131], v[68:71]\n\t" \
        "ds_read_b128 v[144:147], v15 offset:27840\n\t" \
        "v_mfma_f32_16x16x32_bf16 v[72:75], a[148:151], v[128:131], v[72:75]\n\t" \
        "ds_read_b128 v[116:119], v15 offset:4096\n\t" \
        "v_mfma_f32_16x16x32_bf16 v[76:79], a[152:155], v[128:131], v[76:79]\n\t" \
        "s_waitcnt vmcnt(17)\n\t" \
        "s_barrier\n\t" \
        "ds_read_b64_tr_b16 a[168:169], v20 offset:24576\n\t" \
        "s_waitcnt lgkmcnt(9)\n\t" \
        "v_mfma_f32_16x16x32_bf16 v[32:35], a[156:159], v[132:135], v[32:35]\n\t" \
        "buffer_load_dwordx4 v5, s[24:27], s57 offen offset:2048 lds\n\t" \
        "s_waitcnt lgkmcnt(7)\n\t" \
        "v_mfma_f32_16x16x32_bf16 v[36:39], a[160:163], v[132:135], v[36:39]\n\t" \
        "ds_read_b64_tr_b16 a[170:171], v20 offset:25344\n\t" \
        "s_waitcnt lgkmcnt(6)\n\t" \
        "v_mfma_f32_16x16x32_bf16 v[40:43], a[164:167], v[132:135], v[40:43]\n\t" \
        "ds_read_b64_tr_b16 a[172:173], v21 offset:24576\n\t" \
        "s_waitcnt lgkmcnt(6)\n\t" \
        "v_mfma_f32_16x16x32_bf16 v[44:47], a[156:159], v[136:139], v[44:47]\n\t" \
        "ds_read_b64_tr_b16 a[174:175], v21 offset:25344\n\t" \
        "v_mfma_f32_16x16x32_bf16 v[48:51], a[160:163], v[136:139], v[48:51]\n\t" \
        "ds_read_b64_tr_b16 a[176:177], v22 offset:24576\n\t" \
        "s_add_u32 s57, s40, 0x27000\n\t" \
        "s_add_u32 m0, s39, 0xf000\n\t" \
        "v_mfma_f32_16x16x32_bf16 v[52:55], a[164:167], v[136:139], v[52:55]\n\t" \
        "ds_read_b64_tr_b16 a[178:179], v22 offset:25344\n\t" \
        "s_waitcnt lgkmcnt(8)\n\t" \
        "v_mfma_f32_16x16x32_bf16 v[56:59], a[156:159], v[140:143], v[56:59]\n\t" \
        "buffer_load_dwordx4 v3, s[24:27], s57 offen offset:0 lds\n\t" \
        "v_mfma_f32_16x16x32_bf16 v[60:63], a[160:163], v[140:143], v[60:63]\n\t" \
        "ds_read_b128 v[120:123], v15 offset:12032\n\t" \
        "v_mfma_f32_16x16x32_bf16 v[64:67], a[164:167], v[140:143], v[64:67]\n\t" \
        "ds_read_b128 v[124:127], v15 offset:19968\n\t" \
        "s_waitcnt lgkmcnt(8)\n\t" \
        "v_mfma_f32_16x16x32_bf16 v[68:71], a[156:159], v[144:147], v[68:71]\n\t" \
        "ds_read_b128 v[128:131], v15 offset:27904\n\t" \
        "v_mfma_f32_16x16x32_bf16 v[72:75], a[160:163], v[144:147], v[72:75]\n\t" \
        "ds_read_b128 v[132:135], v16 offset:3968\n\t" \
        "v_mfma_f32_16x16x32_bf16 v[76:79], a[164:167], v[144:147], v[76:79]\n\t" \
        "ds_read_b64_tr_b16 a[180:181], v20 offset:30720\n\t" \
        "s_waitcnt lgkmcnt(9)\n\t" \
        "v_mfma_f32_16x16x32_bf16 v[32:35], a[168:171], v[116:119], v[32:35]\n\t" \
        "buffer_load_dwordx4 v4, s[24:27], s57 offen offset:1024 lds\n\t" \
        "s_waitcnt lgkmcnt(7)\n\t" \
        "v_mfma_f32_16x16x32_bf16 v[36:39], a[172:175], v[116:119], v[36:39]\n\t" \
        "ds_read_b64_tr_b16 a[182:183], v20 offset:31488\n\t" \
        "s_waitcnt lgkmcnt(6)\n\t" \
        "v_mfma_f32_16x16x32_bf16 v[40:43], a[176:179], v[116:119], v[40:43]\n\t" \
        "ds_read_b64_tr_b16 a[184:185], v21 offset:30720\n\t" \
        "s_waitcnt lgkmcnt(6)\n\t" \
        "v_mfma_f32_16x16x32_bf16 v[44:47], a[168:171], v[120:123], v[44:47]\n\t" \
        "ds_read_b64_tr_b16 a[186:187], v21 offset:31488\n\t" \
        "v_mfma_f32_16x16x32_bf16 v[48:51], a[172:175], v[120:123], v[48:51]\n\t" \
        "ds_read_b64_tr_b16 a[188:189], v22 offset:30720\n\t" \
        "v_mfma_f32_16x16x32_bf16 v[52:55], a[176:179], v[120:123], v[52:55]\n\t" \
        "ds_read_b64_tr_b16 a[190:191], v22 offset:31488\n\t" \
        "s_waitcnt lgkmcnt(8)\n\t" \
        "v_mfma_f32_16x16x32_bf16 v[56:59], a[168:171], v[124:127], v[56:59]\n\t" \
        "buffer_load_dwordx4 v5, s[24:27], s57 offen offset:2048 lds\n\t" \
        "v_mfma_f32_16x16x32_bf16 v[60:63], a[172:175], v[124:127], v[60:63]\n\t" \
        "ds_read_b128 v[136:139], v16 offset:11904\n\t" \
        "v_mfma_f32_16x16x32_bf16 v[64:67], a[176:179], v[124:127], v[64:67]\n\t" \
        "ds_read_b128 v[140:143], v16 offset:19840\n\t" \
        "s_waitcnt lgkmcnt(8)\n\t" \
        "v_mfma_f32_16x16x32_bf16 v[68:71], a[168:171], v[128:131], v[68:71]\n\t" \
        "ds_read_b128 v[144:147], v16 offset:27776\n\t" \
        "v_mfma_f32_16x16x32_bf16 v[72:75], a[172:175], v[128:131], v[72:75]\n\t" \
        "ds_read_b128 v[116:119], v16 offset:4032\n\t" \
        "s_add_u32 s57, s36, 0x5e800\n\t" \
        "s_add_u32 m0, s38, 0x9b00\n\t" \
        "v_mfma_f32_16x16x32_bf16 v[76:79], a[176:179], v[128:131], v[76:79]\n\t" \
        "s_waitcnt vmcnt(18)\n\t" \
        "s_barrier\n\t" \
        "ds_read_b64_tr_b16 a[192:193], v6 offset:0\n\t" \
        "s_waitcnt lgkmcnt(9)\n\t" \
        "v_mfma_f32_16x16x32_bf16 v[32:35], a[180:183], v[132:135], v[32:35]\n\t" \
        "buffer_load_dwordx4 v13, s[16:19], s57 offen sc1 lds\n\t" \
        "s_waitcnt lgkmcnt(7)\n\t" \
        "v_mfma_f32_16x16x32_bf16 v[36:39], a[184:187], v[132:135], v[36:39]\n\t" \
        "ds_read_b64_tr_b16 a[194:195], v6 offset:768\n\t" \
        "s_waitcnt lgkmcnt(6)\n\t" \
        "v_mfma_f32_16x16x32_bf16 v[40:43], a[188:191], v[132:135], v[40:43]\n\t" \
        "ds_read_b64_tr_b16 a[196:197], v7 offset:0\n\t" \
        "s_waitcnt lgkmcnt(6)\n\t" \
        "v_mfma_f32_16x16x32_bf16 v[44:47], a[180:183], v[136:139], v[44:47]\n\t" \
        "ds_read_b64_tr_b16 a[198:199], v7 offset:768\n\t" \
        "v_mfma_f32_16x16x32_bf16 v[48:51], a[184:187], v[136:139], v[48:51]\n\t" \
        "ds_read_b64_tr_b16 a[200:201], v8 offset:0\n\t" \
        "s_add_u32 s57, s36, 0x69000\n\t" \
        "s_add_u32 m0, s38, 0xaa80\n\t" \
        "v_mfma_f32_16x16x32_bf16 v[52:55], a[188:191], v[136:139], v[52:55]\n\t" \
        "ds_read_b64_tr_b16 a[202:203], v8 offset:768\n\t" \
        "s_waitcnt lgkmcnt(8)\n\t" \
        "v_mfma_f32_16x16x32_bf16 v[56:59], a[180:183], v[140:143], v[56:59]\n\t" \
        "buffer_load_dwordx4 v13, s[16:19], s57 offen sc1 lds\n\t" \
        "v_mfma_f32_16x16x32_bf16 v[60:63], a[184:187], v[140:143], v[60:63]\n\t" \
        "ds_read_b128 v[120:123], v16 offset:11968\n\t" \
        "v_mfma_f32_16x16x32_bf16 v[64:67], a[188:191], v[140:143], v[64:67]\n\t" \
        "ds_read_b128 v[124:127], v16 offset:19904\n\t" \
        "s_waitcnt lgkmcnt(8)\n\t" \
        "v_mfma_f32_16x16x32_bf16 v[68:71], a[180:183], v[144:147], v[68:71]\n\t" \
        "ds_read_b128 v[128:131], v16 offset:27840\n\t" \
        "v_mfma_f32_16x16x32_bf16 v[72:75], a[184:187], v[144:147], v[72:75]\n\t" \
        "ds_read_b128 v[132:135], v16 offset:4096\n\t" \
        "s_add_u32 s57, s36, 0x73800\n\t" \
        "s_add_u32 m0, s38, 0xba00\n\t" \
        "v_mfma_f32_16x16x32_bf16 v[76:79], a[188:191], v[144:147], v[76:79]\n\t" \
        "ds_read_b64_tr_b16 a[204:205], v6 offset:6144\n\t" \
        "s_waitcnt lgkmcnt(9)\n\t" \
        "v_mfma_f32_16x16x32_bf16 v[32:35], a[192:195], v[116:119], v[32:35]\n\t" \
        "buffer_load_dwordx4 v13, s[16:19], s57 offen sc1 lds\n\t" \
        "s_waitcnt lgkmcnt(7)\n\t" \
        "v_mfma_f32_16x16x32_bf16 v[36:39], a[196:199], v[116:119], v[36:39]\n\t" \
        "ds_read_b64_tr_b16 a[206:207], v6 offset:6912\n\t" \
        "s_waitcnt lgkmcnt(6)\n\t" \
        "v_mfma_f32_16x16x32_bf16 v[40:43], a[200:203], v[116:119], v[40:43]\n\t" \
        "ds_read_b64_tr_b16 a[208:209], v7 offset:6144\n\t" \
        "s_waitcnt lgkmcnt(6)\n\t" \
        "v_mfma_f32_16x16x32_bf16 v[44:47], a[192:195], v[120:123], v[44:47]\n\t" \
        "ds_read_b64_tr_b16 a[210:211], v7 offset:6912\n\t" \
        "v_mfma_f32_16x16x32_bf16 v[48:51], a[196:199], v[120:123], v[48:51]\n\t" \
        "ds_read_b64_tr_b16 a[212:213], v8 offset:6144\n\t" \
        "s_add_u32 s57, s36, 0x7e000\n\t" \
        "s_add_u32 m0, s38, 0xc980\n\t" \
        "v_mfma_f32_16x16x32_bf16 v[52:55], a[200:203], v[120:123], v[52:55]\n\t" \
        "ds_read_b64_tr_b16 a[214:215], v8 offset:6912\n\t" \
        "s_waitcnt lgkmcnt(8)\n\t" \
        "v_mfma_f32_16x16x32_bf16 v[56:59], a[192:195], v[124:127], v[56:59]\n\t" \
        "buffer_load_dwordx4 v13, s[16:19], s57 offen sc1 lds\n\t" \
        "v_mfma_f32_16x16x32_bf16 v[60:63], a[196:199], v[124:127], v[60:63]\n\t" \
        "ds_read_b128 v[136:139], v16 offset:12032\n\t" \
        "v_mfma_f32_16x16x32_bf16 v[64:67], a[200:203], v[124:127], v[64:67]\n\t" \
        "ds_read_b128 v[140:143], v16 offset:19968\n\t" \
        "s_waitcnt lgkmcnt(8)\n\t" \
        "v_mfma_f32_16x16x32_bf16 v[68:71], a[192:195], v[128:131], v[68:71]\n\t" \
        "ds_read_b128 v[144:147], v16 offset:27904\n\t" \
        "v_mfma_f32_16x16x32_bf16 v[72:75], a[196:199], v[128:131], v[72:75]\n\t" \
        "ds_read_b128 v[116:119], v14 offset:7936\n\t" \
        "s_add_u32 s57, s36, 0x88800\n\t" \
        "s_add_u32 m0, s38, 0xd900\n\t" \
        "v_mfma_f32_16x16x32_bf16 v[76:79], a[200:203], v[128:131], v[76:79]\n\t" \
        "s_waitcnt vmcnt(19)\n\t" \
        "s_barrier\n\t" \
        "ds_read_b64_tr_b16 a[216:217], v6 offset:12288\n\t" \
        "s_waitcnt lgkmcnt(9)\n\t" \
        "v_mfma_f32_16x16x32_bf16 v[32:35], a[204:207], v[132:135], v[32:35]\n\t" \
        "buffer_load_dwordx4 v13, s[16:19], s57 offen sc1 lds\n\t" \
        "s_waitcnt lgkmcnt(7)\n\t" \
        "v_mfma_f32_16x16x32_bf16 v[36:39], a[208:211], v[132:135], v[36:39]\n\t" \
        "ds_read_b64_tr_b16 a[218:219], v6 offset:13056\n\t" \
        "s_waitcnt lgkmcnt(6)\n\t" \
        "v_mfma_f32_16x16x32_bf16 v[40:43], a[212:215], v[132:135], v[40:43]\n\t" \
        "ds_read_b64_tr_b16 a[220:221], v7 offset:12288\n\t" \
        "s_waitcnt lgkmcnt(6)\n\t" \
        "v_mfma_f32_16x16x32_bf16 v[44:47], a[204:207], v[136:139], v[44:47]\n\t" \
        "ds_read_b64_tr_b16 a[222:223], v7 offset:13056\n\t" \
        "v_mfma_f32_16x16x32_bf16 v[48:51], a[208:211], v[136:139], v[48:51]\n\t" \
        "ds_read_b64_tr_b16 a[224:225], v8 offset:12288\n\t" \
        "s_add_u32 s57, s36, 0x93000\n\t" \
        "s_min_u32 s57, s57, 0x925800\n\t" \
        "s_add_u32 m0, s38, 0xe880\n\t" \
        "v_mfma_f32_16x16x32_bf16 v[52:55], a[212:215], v[136:139], v[52:55]\n\t" \
        "ds_read_b64_tr_b16 a[226:227], v8 offset:13056\n\t" \
        "s_waitcnt lgkmcnt(8)\n\t" \
        "v_mfma_f32_16x16x32_bf16 v[56:59], a[204:207], v[140:143], v[56:59]\n\t" \
        "buffer_load_dwordx4 v13, s[16:19], s57 offen sc1 lds\n\t" \
        "v_mfma_f32_16x16x32_bf16 v[60:63], a[208:211], v[140:143], v[60:63]\n\t" \
        "ds_read_b128 v[120:123], v14 offset:15872\n\t" \
        "v_mfma_f32_16x16x32_bf16 v[64:67], a[212:215], v[140:143], v[64:67]\n\t" \
        "ds_read_b128 v[124:127], v14 offset:23808\n\t" \
        "s_waitcnt lgkmcnt(8)\n\t" \
        "v_mfma_f32_16x16x32_bf16 v[68:71], a[204:207], v[144:147], v[68:71]\n\t" \
        "ds_read_b128 v[128:131], v14 offset:31744\n\t" \
        "v_mfma_f32_16x16x32_bf16 v[72:75], a[208:211], v[144:147], v[72:75]\n\t" \
        "ds_read_b128 v[132:135], v14 offset:8000\n\t" \
        "v_mfma_f32_16x16x32_bf16 v[76:79], a[212:215], v[144:147], v[76:79]\n\t" \
        "ds_read_b64_tr_b16 a[228:229], v6 offset:18432\n\t" \
        "s_waitcnt lgkmcnt(9)\n\t" \
        "v_mfma_f32_16x16x32_bf16 v[32:35], a[216:219], v[116:119], v[32:35]\n\t" \
        "ds_read_b64_tr_b16 a[230:231], v6 offset:19200\n\t" \
        "s_waitcnt lgkmcnt(8)\n\t" \
        "v_mfma_f32_16x16x32_bf16 v[36:39], a[220:223], v[116:119], v[36:39]\n\t" \
        "ds_read_b64_tr_b16 a[232:233], v7 offset:18432\n\t" \
        "s_waitcnt lgkmcnt(7)\n\t" \
        "v_mfma_f32_16x16x32_bf16 v[40:43], a[224:227], v[116:119], v[40:43]\n\t" \
        "ds_read_b64_tr_b16 a[234:235], v7 offset:19200\n\t" \
        "s_waitcnt lgkmcnt(7)\n\t" \
        "v_mfma_f32_16x16x32_bf16 v[44:47], a[216:219], v[120:123], v[44:47]\n\t" \
        "ds_read_b64_tr_b16 a[236:237], v8 offset:18432\n\t" \
        "v_mfma_f32_16x16x32_bf16 v[48:51], a[220:223], v[120:123], v[48:51]\n\t" \
        "ds_read_b64_tr_b16 a[238:239], v8 offset:19200\n\t" \
        "v_mfma_f32_16x16x32_bf16 v[52:55], a[224:227], v[120:123], v[52:55]\n\t" \
        "ds_read_b128 v[136:139], v14 offset:15936\n\t" \
        "s_waitcnt lgkmcnt(9)\n\t" \
        "v_mfma_f32_16x16x32_bf16 v[56:59], a[216:219], v[124:127], v[56:59]\n\t" \
        "ds_read_b128 v[140:143], v14 offset:23872\n\t" \
        "v_mfma_f32_16x16x32_bf16 v[60:63], a[220:223], v[124:127], v[60:63]\n\t" \
        "ds_read_b128 v[144:147], v14 offset:31808\n\t" \
        "v_mfma_f32_16x16x32_bf16 v[64:67], a[224:227], v[124:127], v[64:67]\n\t" \
        "ds_read_b128 v[116:119], v14 offset:8064\n\t" \
        "s_waitcnt lgkmcnt(10)\n\t" \
        "v_mfma_f32_16x16x32_bf16 v[68:71], a[216:219], v[128:131], v[68:71]\n\t" \
        "ds_read_b128 v[120:123], v14 offset:16000\n\t" \
        "v_mfma_f32_16x16x32_bf16 v[72:75], a[220:223], v[128:131], v[72:75]\n\t" \
        "ds_read_b128 v[124:127], v14 offset:23936\n\t" \
        "v_mfma_f32_16x16x32_bf16 v[76:79], a[224:227], v[128:131], v[76:79]\n\t" \
        "s_waitcnt vmcnt(15)\n\t" \
        "s_barrier\n\t" \
        "ds_read_b64_tr_b16 a[240:241], v6 offset:24576\n\t" \
        "v_and_b32 v148, 0x7fff7fff, v148\n\t" \
        "s_waitcnt lgkmcnt(10)\n\t" \
        "v_mfma_f32_16x16x32_bf16 v[32:35], a[228:231], v[132:135], v[32:35]\n\t" \
        "ds_read_b64_tr_b16 a[242:243], v6 offset:25344\n\t" \
        "v_and_b32 v149, 0x7fff7fff, v149\n\t" \
        "s_waitcnt lgkmcnt(10)\n\t" \
        "v_mfma_f32_16x16x32_bf16 v[36:39], a[232:235], v[132:135], v[36:39]\n\t" \
        "ds_read_b64_tr_b16 a[244:245], v7 offset:24576\n\t" \
        "v_and_b32 v150, 0x7fff7fff, v150\n\t" \
        "s_waitcnt lgkmcnt(9)\n\t" \
        "v_mfma_f32_16x16x32_bf16 v[40:43], a[236:239], v[132:135], v[40:43]\n\t" \
        "ds_read_b64_tr_b16 a[246:247], v7 offset:25344\n\t" \
        "v_and_b32 v151, 0x7fff7fff, v151\n\t" \
        "s_waitcnt lgkmcnt(9)\n\t" \
        "v_mfma_f32_16x16x32_bf16 v[44:47], a[228:231], v[136:139], v[44:47]\n\t" \
        "ds_read_b64_tr_b16 a[248:249], v8 offset:24576\n\t" \
        "v_and_b32 v152, 0x7fff7fff, v152\n\t" \
        "v_mfma_f32_16x16x32_bf16 v[48:51], a[232:235], v[136:139], v[48:51]\n\t" \
        "ds_read_b64_tr_b16 a[250:251], v8 offset:25344\n\t" \
        "v_and_b32 v153, 0x7fff7fff, v153\n\t" \
        "v_mfma_f32_16x16x32_bf16 v[52:55], a[236:239], v[136:139], v[52:55]\n\t" \
        "ds_read_b128 v[128:131], v14 offset:31872\n\t" \
        "v_and_b32 v154, 0x7fff7fff, v154\n\t" \
        "s_waitcnt lgkmcnt(10)\n\t" \
        "v_mfma_f32_16x16x32_bf16 v[56:59], a[228:231], v[140:143], v[56:59]\n\t" \
        "ds_read_b128 v[132:135], v15 offset:7936\n\t" \
        "v_and_b32 v155, 0x7fff7fff, v155\n\t" \
        "v_mfma_f32_16x16x32_bf16 v[60:63], a[232:235], v[140:143], v[60:63]\n\t" \
        "ds_read_b64_tr_b16 a[252:253], v6 offset:30720\n\t" \
        "v_and_b32 v156, 0x7fff7fff, v156\n\t" \
        "v_mfma_f32_16x16x32_bf16 v[64:67], a[236:239], v[140:143], v[64:67]\n\t" \
        "ds_read_b64_tr_b16 a[254:255], v6 offset:31488\n\t" \
        "v_pk_max_u16 v148, v148, v149\n\t" \
        "v_mfma_f32_16x16x32_bf16 v[68:71], a[228:231], v[144:147], v[68:71]\n\t" \
        "ds_read_b64_tr_b16 v[188:189], v7 offset:30720\n\t" \
        "v_pk_max_u16 v150, v150, v151\n\t" \
        "v_mfma_f32_16x16x32_bf16 v[72:75], a[232:235], v[144:147], v[72:75]\n\t" \
        "ds_read_b64_tr_b16 v[190:191], v7 offset:31488\n\t" \
        "v_pk_max_u16 v152, v152, v153\n\t" \
        "v_mfma_f32_16x16x32_bf16 v[76:79], a[236:239], v[144:147], v[76:79]\n\t" \
        "ds_read_b64_tr_b16 v[192:193], v8 offset:30720\n\t" \
        "v_pk_max_u16 v154, v154, v155\n\t" \
        "s_waitcnt lgkmcnt(10)\n\t" \
        "v_mfma_f32_16x16x32_bf16 v[32:35], a[240:243], v[116:119], v[32:35]\n\t" \
        "ds_read_b64_tr_b16 v[194:195], v8 offset:31488\n\t" \
        "v_pk_max_u16 v156, v156, v23\n\t" \
        "s_waitcnt lgkmcnt(10)\n\t" \
        "v_mfma_f32_16x16x32_bf16 v[36:39], a[244:247], v[116:119], v[36:39]\n\t" \
        "ds_read_b128 v[136:139], v15 offset:15872\n\t" \
        "v_pk_max_u16 v148, v148, v150\n\t" \
        "s_waitcnt lgkmcnt(9)\n\t" \
        "v_mfma_f32_16x16x32_bf16 v[40:43], a[248:251], v[116:119], v[40:43]\n\t" \
        "ds_read_b128 v[140:143], v15 offset:23808\n\t" \
        "v_pk_max_u16 v152, v152, v154\n\t" \
        "v_mfma_f32_16x16x32_bf16 v[44:47], a[240:243], v[120:123], v[44:47]\n\t" \
        "ds_read_b128 v[144:147], v15 offset:31744\n\t" \
        "v_pk_max_u16 v148, v148, v156\n\t" \
        "v_mfma_f32_16x16x32_bf16 v[48:51], a[244:247], v[120:123], v[48:51]\n\t" \
        "ds_read_b128 v[116:119], v15 offset:8000\n\t" \
        "v_pk_max_u16 v148, v148, v152\n\t" \
        "v_mfma_f32_16x16x32_bf16 v[52:55], a[248:251], v[120:123], v[52:55]\n\t" \
        "ds_read_b128 v[120:123], v15 offset:15936\n\t" \
        "v_cmp_ne_u32 vcc, v148, v23\n\t" \
        "s_nop 0\n\t" \
        "s_or_b32 s56, vcc_lo, vcc_hi\n\t" \
        "v_mfma_f32_16x16x32_bf16 v[56:59], a[240:243], v[124:127], v[56:59]\n\t" \
        "v_mov_b32 v149, s56\n\t" \
        "v_mfma_f32_16x16x32_bf16 v[60:63], a[244:247], v[124:127], v[60:63]\n\t" \
        "ds_write_b32 v24, v149\n\t" \
        "v_mfma_f32_16x16x32_bf16 v[64:67], a[248:251], v[124:127], v[64:67]\n\t" \
        "ds_read_b128 v[124:127], v15 offset:23872\n\t" \
        "s_waitcnt lgkmcnt(9)\n\t" \
        "v_mfma_f32_16x16x32_bf16 v[68:71], a[240:243], v[128:131], v[68:71]\n\t" \
        "v_mfma_f32_16x16x32_bf16 v[72:75], a[244:247], v[128:131], v[72:75]\n\t" \
        "v_mfma_f32_16x16x32_bf16 v[76:79], a[248:251], v[128:131], v[76:79]\n\t" \
        "s_waitcnt vmcnt(12)\n\t" \
        "s_barrier\n\t" \
        "ds_read_b64_tr_b16 v[196:197], v6 offset:36864\n\t" \
        "v_mfma_f32_16x16x32_bf16 v[32:35], a[252:255], v[132:135], v[32:35]\n\t" \
        "ds_read_b64_tr_b16 v[198:199], v6 offset:37632\n\t" \
        "v_mfma_f32_16x16x32_bf16 v[36:39], v[188:191], v[132:135], v[36:39]\n\t" \
        "ds_read_b64_tr_b16 v[200:201], v7 offset:36864\n\t" \
        "s_waitcnt lgkmcnt(7)\n\t" \
        "v_mfma_f32_16x16x32_bf16 v[40:43], v[192:195], v[132:135], v[40:43]\n\t" \
        "ds_read_b64_tr_b16 v[202:203], v7 offset:37632\n\t" \
        "v_mfma_f32_16x16x32_bf16 v[44:47], a[252:255], v[136:139], v[44:47]\n\t" \
        "ds_read_b64_tr_b16 v[204:205], v8 offset:36864\n\t" \
        "v_mov_b32 v184, 0\n\t" \
        "v_mfma_f32_16x16x32_bf16 v[48:51], v[188:191], v[136:139], v[48:51]\n\t" \
        "ds_read_b64_tr_b16 v[206:207], v8 offset:37632\n\t" \
        "v_mfma_f32_16x16x32_bf16 v[52:55], v[192:195], v[136:139], v[52:55]\n\t" \
        "ds_read_b128 v[128:131], v15 offset:31808\n\t" \
        "v_mfma_f32_16x16x32_bf16 v[56:59], a[252:255], v[140:143], v[56:59]\n\t" \
        "ds_read_b128 v[132:135], v15 offset:8064\n\t" \
        "v_mfma_f32_16x16x32_bf16 v[60:63], v[188:191], v[140:143], v[60:63]\n\t" \
        "ds_read_b64_tr_b16 v[208:209], v6 offset:43008\n\t" \
        "v_mfma_f32_16x16x32_bf16 v[64:67], v[192:195], v[140:143], v[64:67]\n\t" \
        "ds_read_b64_tr_b16 v[210:211], v6 offset:43776\n\t" \
        "v_mfma_f32_16x16x32_bf16 v[68:71], a[252:255], v[144:147], v[68:71]\n\t" \
        "ds_read_b64_tr_b16 v[212:213], v7 offset:43008\n\t" \
        "v_mfma_f32_16x16x32_bf16 v[72:75], v[188:191], v[144:147], v[72:75]\n\t" \
        "ds_read_b64_tr_b16 v[214:215], v7 offset:43776\n\t" \
        "v_mfma_f32_16x16x32_bf16 v[76:79], v[192:195], v[144:147], v[76:79]\n\t" \
        "ds_read_b64_tr_b16 v[216:217], v8 offset:43008\n\t" \
        "s_waitcnt lgkmcnt(10)\n\t" \
        "v_mfma_f32_16x16x32_bf16 v[32:35], v[196:199], v[116:119], v[32:35]\n\t" \
        "ds_read_b64_tr_b16 v[218:219], v8 offset:43776\n\t" \
        "s_waitcnt lgkmcnt(10)\n\t" \
        "v_mfma_f32_16x16x32_bf16 v[36:39], v[200:203], v[116:119], v[36:39]\n\t" \
        "ds_read_b128 v[136:139], v15 offset:16000\n\t" \
        "s_waitcnt lgkmcnt(9)\n\t" \
        "v_mfma_f32_16x16x32_bf16 v[40:43], v[204:207], v[116:119], v[40:43]\n\t" \
        "ds_read_b128 v[140:143], v15 offset:23936\n\t" \
        "v_mfma_f32_16x16x32_bf16 v[44:47], v[196:199], v[120:123], v[44:47]\n\t" \
        "ds_read_b128 v[144:147], v15 offset:31872\n\t" \
        "v_mfma_f32_16x16x32_bf16 v[48:51], v[200:203], v[120:123], v[48:51]\n\t" \
        "ds_read_b128 v[116:119], v16 offset:7936\n\t" \
        "v_mfma_f32_16x16x32_bf16 v[52:55], v[204:207], v[120:123], v[52:55]\n\t" \
        "ds_read_b128 v[120:123], v16 offset:15872\n\t" \
        "v_mfma_f32_16x16x32_bf16 v[56:59], v[196:199], v[124:127], v[56:59]\n\t" \
        "v_mfma_f32_16x16x32_bf16 v[60:63], v[200:203], v[124:127], v[60:63]\n\t" \
        "v_mfma_f32_16x16x32_bf16 v[64:67], v[204:207], v[124:127], v[64:67]\n\t" \
        "ds_read_b128 v[124:127], v16 offset:23808\n\t" \
        "s_waitcnt lgkmcnt(8)\n\t" \
        "v_mfma_f32_16x16x32_bf16 v[68:71], v[196:199], v[128:131], v[68:71]\n\t" \
        "v_mfma_f32_16x16x32_bf16 v[72:75], v[200:203], v[128:131], v[72:75]\n\t" \
        "v_mfma_f32_16x16x32_bf16 v[76:79], v[204:207], v[128:131], v[76:79]\n\t" \
        "s_waitcnt vmcnt(9)\n\t" \
        "s_barrier\n\t" \
        "ds_read_b64_tr_b16 v[220:221], v6 offset:49152\n\t" \
        "v_mfma_f32_16x16x32_bf16 v[32:35], v[208:211], v[132:135], v[32:35]\n\t" \
        "ds_read_b64_tr_b16 v[222:223], v6 offset:49920\n\t" \
        "v_mfma_f32_16x16x32_bf16 v[36:39], v[212:215], v[132:135], v[36:39]\n\t" \
        "ds_read_b64_tr_b16 v[224:225], v7 offset:49152\n\t" \
        "s_waitcnt lgkmcnt(6)\n\t" \
        "v_mfma_f32_16x16x32_bf16 v[40:43], v[216:219], v[132:135], v[40:43]\n\t" \
        "ds_read_b64_tr_b16 v[226:227], v7 offset:49920\n\t" \
        "v_mfma_f32_16x16x32_bf16 v[44:47], v[208:211], v[136:139], v[44:47]\n\t" \
        "ds_read_b64_tr_b16 v[228:229], v8 offset:49152\n\t" \
        "v_mfma_f32_16x16x32_bf16 v[48:51], v[212:215], v[136:139], v[48:51]\n\t" \
        "ds_read_b64_tr_b16 v[230:231], v8 offset:49920\n\t" \
        "v_mfma_f32_16x16x32_bf16 v[52:55], v[216:219], v[136:139], v[52:55]\n\t" \
        "ds_read_b128 v[128:131], v16 offset:31744\n\t" \
        "v_mfma_f32_16x16x32_bf16 v[56:59], v[208:211], v[140:143], v[56:59]\n\t" \
        "ds_read_b128 v[184:187], v184\n\t" \
        "v_mfma_f32_16x16x32_bf16 v[60:63], v[212:215], v[140:143], v[60:63]\n\t" \
        "ds_read_b128 v[132:135], v16 offset:8000\n\t" \
        "v_mfma_f32_16x16x32_bf16 v[64:67], v[216:219], v[140:143], v[64:67]\n\t" \
        "ds_read_b64_tr_b16 v[232:233], v6 offset:55296\n\t" \
        "v_mfma_f32_16x16x32_bf16 v[68:71], v[208:211], v[144:147], v[68:71]\n\t" \
        "ds_read_b64_tr_b16 v[234:235], v6 offset:56064\n\t" \
        "v_mfma_f32_16x16x32_bf16 v[72:75], v[212:215], v[144:147], v[72:75]\n\t" \
        "ds_read_b64_tr_b16 v[236:237], v7 offset:55296\n\t" \
        "v_mfma_f32_16x16x32_bf16 v[76:79], v[216:219], v[144:147], v[76:79]\n\t" \
        "ds_read_b64_tr_b16 v[238:239], v7 offset:56064\n\t" \
        "s_waitcnt lgkmcnt(10)\n\t" \
        "v_mfma_f32_16x16x32_bf16 v[32:35], v[220:223], v[116:119], v[32:35]\n\t" \
        "ds_read_b64_tr_b16 v[240:241], v8 offset:55296\n\t" \
        "s_waitcnt lgkmcnt(10)\n\t" \
        "v_mfma_f32_16x16x32_bf16 v[36:39], v[224:227], v[116:119], v[36:39]\n\t" \
        "ds_read_b64_tr_b16 v[242:243], v8 offset:56064\n\t" \
        "s_waitcnt lgkmcnt(9)\n\t" \
        "v_mfma_f32_16x16x32_bf16 v[40:43], v[228:231], v[116:119], v[40:43]\n\t" \
        "ds_read_b128 v[136:139], v16 offset:15936\n\t" \
        "s_waitcnt lgkmcnt(8)\n\t" \
        "v_or3_b32 v184, v184, v185, v186\n\t" \
        "v_mfma_f32_16x16x32_bf16 v[44:47], v[220:223], v[120:123], v[44:47]\n\t" \
        "ds_read_b128 v[140:143], v16 offset:23872\n\t" \
        "v_or_b32 v184, v184, v187\n\t" \
        "v_mfma_f32_16x16x32_bf16 v[48:51], v[224:227], v[120:123], v[48:51]\n\t" \
        "ds_read_b128 v[144:147], v16 offset:31808\n\t" \
        "v_readfirstlane_b32 s56, v184\n\t" \
        "v_mfma_f32_16x16x32_bf16 v[52:55], v[228:231], v[120:123], v[52:55]\n\t" \
        "ds_read_b128 v[116:119], v16 offset:8064\n\t" \
        "s_cmp_lg_u32 s56, 0\n\t" \
        "s_cbranch_scc1 .LR_abort%=\n\t" \
        "v_mfma_f32_16x16x32_bf16 v[56:59], v[220:223], v[124:127], v[56:59]\n\t" \
        "ds_read_b128 v[120:123], v16 offset:16000\n\t" \
        "v_mfma_f32_16x16x32_bf16 v[60:63], v[224:227], v[124:127], v[60:63]\n\t" \
        "v_mfma_f32_16x16x32_bf16 v[64:67], v[228:231], v[124:127], v[64:67]\n\t" \
        "ds_read_b128 v[124:127], v16 offset:23936\n\t" \
        "v_mfma_f32_16x16x32_bf16 v[68:71], v[220:223], v[128:131], v[68:71]\n\t" \
        "v_mfma_f32_16x16x32_bf16 v[72:75], v[224:227], v[128:131], v[72:75]\n\t" \
        "v_mfma_f32_16x16x32_bf16 v[76:79], v[228:231], v[128:131], v[76:79]\n\t" \
        "s_waitcnt vmcnt(6)\n\t" \
        "s_barrier\n\t" \
        "ds_read_b64_tr_b16 v[244:245], v20 offset:0\n\t" \
        "s_waitcnt lgkmcnt(7)\n\t" \
        "v_mfma_f32_16x16x32_bf16 v[32:35], v[232:235], v[132:135], v[32:35]\n\t" \
        "ds_read_b64_tr_b16 v[246:247], v20 offset:768\n\t" \
        "v_mfma_f32_16x16x32_bf16 v[36:39], v[236:239], v[132:135], v[36:39]\n\t" \
        "ds_read_b64_tr_b16 v[248:249], v21 offset:0\n\t" \
        "v_mfma_f32_16x16x32_bf16 v[40:43], v[240:243], v[132:135], v[40:43]\n\t" \
        "ds_read_b64_tr_b16 v[250:251], v21 offset:768\n\t" \
        "s_waitcnt lgkmcnt(7)\n\t" \
        "v_mfma_f32_16x16x32_bf16 v[44:47], v[232:235], v[136:139], v[44:47]\n\t" \
        "ds_read_b64_tr_b16 v[252:253], v22 offset:0\n\t" \
        "v_mfma_f32_16x16x32_bf16 v[48:51], v[236:239], v[136:139], v[48:51]\n\t" \
        "ds_read_b64_tr_b16 v[254:255], v22 offset:768\n\t" \
        "v_mfma_f32_16x16x32_bf16 v[52:55], v[240:243], v[136:139], v[52:55]\n\t" \
        "ds_read_b128 v[128:131], v16 offset:31872\n\t" \
        "v_mfma_f32_16x16x32_bf16 v[56:59], v[232:235], v[140:143], v[56:59]\n\t" \
        "ds_read_b128 v[132:135], v14 offset:31744\n\t" \
        "v_mfma_f32_16x16x32_bf16 v[60:63], v[236:239], v[140:143], v[60:63]\n\t" \
        "ds_read_b128 v[136:139], v14 offset:31808\n\t" \
        "v_mfma_f32_16x16x32_bf16 v[64:67], v[240:243], v[140:143], v[64:67]\n\t" \
        "ds_read_b128 v[140:143], v14 offset:31872\n\t" \
        "v_mfma_f32_16x16x32_bf16 v[68:71], v[232:235], v[144:147], v[68:71]\n\t" \
        "v_mfma_f32_16x16x32_bf16 v[72:75], v[236:239], v[144:147], v[72:75]\n\t" \
        "v_mfma_f32_16x16x32_bf16 v[76:79], v[240:243], v[144:147], v[76:79]\n\t" \
        "ds_read_b128 v[144:147], v15 offset:31744\n\t" \
        "s_waitcnt lgkmcnt(8)\n\t" \
        "v_mfma_f32_16x16x32_bf16 v[32:35], v[244:247], v[116:119], v[32:35]\n\t" \
        "s_waitcnt lgkmcnt(7)\n\t" \
        "v_mfma_f32_16x16x32_bf16 v[36:39], v[248:251], v[116:119], v[36:39]\n\t" \
        "s_waitcnt lgkmcnt(5)\n\t" \
        "v_mfma_f32_16x16x32_bf16 v[40:43], v[252:255], v[116:119], v[40:43]\n\t" \
        "ds_read_b128 v[116:119], v15 offset:31808\n\t" \
        "v_mfma_f32_16x16x32_bf16 v[44:47], v[244:247], v[120:123], v[44:47]\n\t" \
        "v_mfma_f32_16x16x32_bf16 v[48:51], v[248:251], v[120:123], v[48:51]\n\t" \
        "v_mfma_f32_16x16x32_bf16 v[52:55], v[252:255], v[120:123], v[52:55]\n\t" \
        "ds_read_b128 v[120:123], v15 offset:31872\n\t" \
        "v_mfma_f32_16x16x32_bf16 v[56:59], v[244:247], v[124:127], v[56:59]\n\t" \
        "v_mfma_f32_16x16x32_bf16 v[60:63], v[248:251], v[124:127], v[60:63]\n\t" \
        "v_mfma_f32_16x16x32_bf16 v[64:67], v[252:255], v[124:127], v[64:67]\n\t" \
        "ds_read_b128 v[124:127], v16 offset:31744\n\t" \
        "s_waitcnt lgkmcnt(4)\n\t" \
        "v_mfma_f32_16x16x32_bf16 v[68:71], v[244:247], v[128:131], v[68:71]\n\t" \
        "v_mfma_f32_16x16x32_bf16 v[72:75], v[248:251], v[128:131], v[72:75]\n\t" \
        "v_mfma_f32_16x16x32_bf16 v[76:79], v[252:255], v[128:131], v[76:79]\n\t" \
        "ds_read_b128 v[128:131], v16 offset:31808\n\t" \
        "v_mfma_f32_16x16x32_bf16 v[80:83], a[0:3], v[132:135], 0\n\t" \
        "v_mfma_f32_16x16x32_bf16 v[84:87], a[4:7], v[132:135], 0\n\t" \
        "v_mfma_f32_16x16x32_bf16 v[88:91], a[8:11], v[132:135], 0\n\t" \
        "v_cvt_pk_bf16_f32 v40, v40, v41\n\t" \
        "v_cvt_pk_bf16_f32 v41, v42, v43\n\t" \
        "v_mfma_f32_16x16x32_bf16 v[80:83], a[12:15], v[136:139], v[80:83]\n\t" \
        "ds_write_b64 v11, v[40:41] offset:0\n\t" \
        "v_cvt_pk_bf16_f32 v52, v52, v53\n\t" \
        "v_mfma_f32_16x16x32_bf16 v[84:87], a[16:19], v[136:139], v[84:87]\n\t" \
        "v_cvt_pk_bf16_f32 v53, v54, v55\n\t" \
        "v_cvt_pk_bf16_f32 v64, v64, v65\n\t" \
        "v_mfma_f32_16x16x32_bf16 v[88:91], a[20:23], v[136:139], v[88:91]\n\t" \
        "ds_write_b64 v11, v[52:53] offset:2048\n\t" \
        "v_cvt_pk_bf16_f32 v65, v66, v67\n\t" \
        "v_mfma_f32_16x16x32_bf16 v[80:83], a[24:27], v[140:143], v[80:83]\n\t" \
        "ds_write_b64 v11, v[64:65] offset:4096\n\t" \
        "v_cvt_pk_bf16_f32 v76, v76, v77\n\t" \
        "v_mfma_f32_16x16x32_bf16 v[84:87], a[28:31], v[140:143], v[84:87]\n\t" \
        "ds_read_b128 v[132:135], v16 offset:31872\n\t" \
        "v_cvt_pk_bf16_f32 v77, v78, v79\n\t" \
        "v_mfma_f32_16x16x32_bf16 v[88:91], a[32:35], v[140:143], v[88:91]\n\t" \
        "ds_write_b64 v11, v[76:77] offset:6144\n\t" \
        "v_cvt_pk_bf16_f32 v32, v32, v33\n\t" \
        "s_waitcnt lgkmcnt(6)\n\t" \
        "v_mfma_f32_16x16x32_bf16 v[80:83], a[36:39], v[144:147], v[80:83]\n\t" \
        "v_cvt_pk_bf16_f32 v33, v34, v35\n\t" \
        "v_cvt_pk_bf16_f32 v34, v36, v37\n\t" \
        "v_mfma_f32_16x16x32_bf16 v[84:87], a[40:43], v[144:147], v[84:87]\n\t" \
        "v_cvt_pk_bf16_f32 v35, v38, v39\n\t" \
        "v_permlane16_swap_b32 v32, v34\n\t" \
        "v_mfma_f32_16x16x32_bf16 v[88:91], a[44:47], v[144:147], v[88:91]\n\t" \
        "v_permlane16_swap_b32 v33, v35\n\t" \
        "v_mfma_f32_16x16x32_bf16 v[80:83], a[48:51], v[116:119], v[80:83]\n\t" \
        "v_mfma_f32_16x16x32_bf16 v[84:87], a[52:55], v[116:119], v[84:87]\n\t" \
        "v_mfma_f32_16x16x32_bf16 v[88:91], a[56:59], v[116:119], v[88:91]\n\t" \
        "v_mfma_f32_16x16x32_bf16 v[80:83], a[60:63], v[120:123], v[80:83]\n\t" \
        "v_mfma_f32_16x16x32_bf16 v[84:87], a[64:67], v[120:123], v[84:87]\n\t" \
        "v_mfma_f32_16x16x32_bf16 v[88:91], a[68:71], v[120:123], v[88:91]\n\t" \
        "v_mfma_f32_16x16x32_bf16 v[80:83], a[72:75], v[124:127], v[80:83]\n\t" \
        "v_mfma_f32_16x16x32_bf16 v[84:87], a[76:79], v[124:127], v[84:87]\n\t" \
        "s_waitcnt vmcnt(0) lgkmcnt(0)\n\t" \
        "s_barrier\n\t" \
        "ds_read_b128 v[136:139], v14 offset:35712\n\t" \
        "v_mfma_f32_16x16x32_bf16 v[88:91], a[80:83], v[124:127], v[88:91]\n\t" \
        "ds_read_b128 v[140:143], v14 offset:35776\n\t" \
        "v_mfma_f32_16x16x32_bf16 v[80:83], a[84:87], v[128:131], v[80:83]\n\t" \
        "ds_read_b128 v[144:147], v14 offset:35840\n\t" \
        "v_mfma_f32_16x16x32_bf16 v[84:87], a[88:91], v[128:131], v[84:87]\n\t" \
        "ds_read_b128 v[116:119], v15 offset:35712\n\t" \
        "v_mfma_f32_16x16x32_bf16 v[88:91], a[92:95], v[128:131], v[88:91]\n\t" \
        "ds_read_b128 v[120:123], v15 offset:35776\n\t" \
        "v_mfma_f32_16x16x32_bf16 v[80:83], a[96:99], v[132:135], v[80:83]\n\t" \
        "s_mov_b32 s56, s64\n\t" \
        "buffer_store_dwordx4 v[32:35], v9, s[28:31], s56 offen sc1\n\t" \
        "v_mfma_f32_16x16x32_bf16 v[84:87], a[100:103], v[132:135], v[84:87]\n\t" \
        "ds_read_b64 v[42:43], v12 offset:0\n\t" \
        "v_mfma_f32_16x16x32_bf16 v[88:91], a[104:107], v[132:135], v[88:91]\n\t" \
        "ds_read_b128 v[124:127], v15 offset:35840\n\t" \
        "s_waitcnt lgkmcnt(6)\n\t" \
        "v_mfma_f32_16x16x32_bf16 v[80:83], a[108:111], v[136:139], v[80:83]\n\t" \
        "ds_read_b128 v[128:131], v16 offset:35712\n\t" \
        "v_mfma_f32_16x16x32_bf16 v[84:87], a[112:115], v[136:139], v[84:87]\n\t" \
        "ds_read_b128 v[132:135], v16 offset:35776\n\t" \
        "v_mfma_f32_16x16x32_bf16 v[88:91], a[116:119], v[136:139], v[88:91]\n\t" \
        "ds_read_b128 v[136:139], v16 offset:35840\n\t" \
        "s_waitcnt lgkmcnt(8)\n\t" \
        "v_mfma_f32_16x16x32_bf16 v[80:83], a[120:123], v[140:143], v[80:83]\n\t" \
        "v_mfma_f32_16x16x32_bf16 v[84:87], a[124:127], v[140:143], v[84:87]\n\t" \
        "v_mfma_f32_16x16x32_bf16 v[88:91], a[128:131], v[140:143], v[88:91]\n\t" \
        "ds_read_b128 v[140:143], v14 offset:39680\n\t" \
        "v_cvt_pk_bf16_f32 v44, v44, v45\n\t" \
        "s_waitcnt lgkmcnt(7)\n\t" \
        "v_mfma_f32_16x16x32_bf16 v[80:83], a[132:135], v[144:147], v[80:83]\n\t" \
        "s_waitcnt lgkmcnt(5)\n\t" \
        "v_permlane16_swap_b32 v40, v42\n\t" \
        "v_permlane16_swap_b32 v41, v43\n\t" \
        "v_mfma_f32_16x16x32_bf16 v[84:87], a[136:139], v[144:147], v[84:87]\n\t" \
        "v_cvt_pk_bf16_f32 v45, v46, v47\n\t" \
        "v_cvt_pk_bf16_f32 v46, v48, v49\n\t" \
        "v_mfma_f32_16x16x32_bf16 v[88:91], a[140:143], v[144:147], v[88:91]\n\t" \
        "s_mov_b32 s56, s64\n\t" \
        "buffer_store_dwordx4 v[40:43], v10, s[28:31], s56 offen sc1\n\t" \
        "v_mfma_f32_16x16x32_bf16 v[80:83], a[144:147], v[116:119], v[80:83]\n\t" \
        "ds_read_b128 v[144:147], v14 offset:39744\n\t" \
        "v_cvt_pk_bf16_f32 v47, v50, v51\n\t" \
        "v_mfma_f32_16x16x32_bf16 v[84:87], a[148:151], v[116:119], v[84:87]\n\t" \
        "v_permlane16_swap_b32 v44, v46\n\t" \
        "v_permlane16_swap_b32 v45, v47\n\t" \
        "v_mfma_f32_16x16x32_bf16 v[88:91], a[152:155], v[116:119], v[88:91]\n\t" \
        "ds_read_b128 v[116:119], v14 offset:39808\n\t" \
        "v_mfma_f32_16x16x32_bf16 v[80:83], a[156:159], v[120:123], v[80:83]\n\t" \
        "ds_read_b64 v[54:55], v12 offset:2048\n\t" \
        "v_mfma_f32_16x16x32_bf16 v[84:87], a[160:163], v[120:123], v[84:87]\n\t" \
        "v_mfma_f32_16x16x32_bf16 v[88:91], a[164:167], v[120:123], v[88:91]\n\t" \
        "ds_read_b128 v[120:123], v15 offset:39680\n\t" \
        "s_waitcnt lgkmcnt(5)\n\t" \
        "v_mfma_f32_16x16x32_bf16 v[80:83], a[168:171], v[124:127], v[80:83]\n\t" \
        "v_mfma_f32_16x16x32_bf16 v[84:87], a[172:175], v[124:127], v[84:87]\n\t" \
        "v_mfma_f32_16x16x32_bf16 v[88:91], a[176:179], v[124:127], v[88:91]\n\t" \
        "ds_read_b128 v[124:127], v15 offset:39744\n\t" \
        "v_mfma_f32_16x16x32_bf16 v[80:83], a[180:183], v[128:131], v[80:83]\n\t" \
        "v_mfma_f32_16x16x32_bf16 v[84:87], a[184:187], v[128:131], v[84:87]\n\t" \
        "v_mfma_f32_16x16x32_bf16 v[88:91], a[188:191], v[128:131], v[88:91]\n\t" \
        "ds_read_b128 v[128:131], v15 offset:39808\n\t" \
        "s_waitcnt lgkmcnt(3)\n\t" \
        "v_permlane16_swap_b32 v52, v54\n\t" \
        "v_mfma_f32_16x16x32_bf16 v[80:83], a[192:195], v[132:135], v[80:83]\n\t" \
        "v_permlane16_swap_b32 v53, v55\n\t" \
        "v_mfma_f32_16x16x32_bf16 v[84:87], a[196:199], v[132:135], v[84:87]\n\t" \
        "v_mfma_f32_16x16x32_bf16 v[88:91], a[200:203], v[132:135], v[88:91]\n\t" \
        "ds_read_b128 v[132:135], v16 offset:39680\n\t" \
        "v_mfma_f32_16x16x32_bf16 v[80:83], a[204:207], v[136:139], v[80:83]\n\t" \
        "v_mfma_f32_16x16x32_bf16 v[84:87], a[208:211], v[136:139], v[84:87]\n\t" \
        "v_mfma_f32_16x16x32_bf16 v[88:91], a[212:215], v[136:139], v[88:91]\n\t" \
        "ds_read_b128 v[136:139], v16 offset:39744\n\t" \
        "v_mfma_f32_16x16x32_bf16 v[80:83], a[216:219], v[140:143], v[80:83]\n\t" \
        "s_add_u32 s56, s64, 0x15000\n\t" \
        "buffer_store_dwordx4 v[44:47], v9, s[28:31], s56 offen sc1\n\t" \
        "v_mfma_f32_16x16x32_bf16 v[84:87], a[220:223], v[140:143], v[84:87]\n\t" \
        "v_mfma_f32_16x16x32_bf16 v[88:91], a[224:227], v[140:143], v[88:91]\n\t" \
        "ds_read_b128 v[140:143], v16 offset:39808\n\t" \
        "v_mfma_f32_16x16x32_bf16 v[80:83], a[228:231], v[144:147], v[80:83]\n\t" \
        "v_mfma_f32_16x16x32_bf16 v[84:87], a[232:235], v[144:147], v[84:87]\n\t" \
        "v_mfma_f32_16x16x32_bf16 v[88:91], a[236:239], v[144:147], v[88:91]\n\t" \
        "ds_read_b128 v[144:147], v14 offset:39680\n\t" \
        "v_mfma_f32_16x16x32_bf16 v[80:83], a[240:243], v[116:119], v[80:83]\n\t" \
        "v_mfma_f32_16x16x32_bf16 v[84:87], a[244:247], v[116:119], v[84:87]\n\t" \
        "s_add_u32 s56, s64, 0x15000\n\t" \
        "buffer_store_dwordx4 v[52:55], v10, s[28:31], s56 offen sc1\n\t" \
        "v_mfma_f32_16x16x32_bf16 v[88:91], a[248:251], v[116:119], v[88:91]\n\t" \
        "ds_read_b128 v[116:119], v14 offset:39744\n\t" \
        "v_cvt_pk_bf16_f32 v56, v56, v57\n\t" \
        "s_waitcnt lgkmcnt(4)\n\t" \
        "v_mfma_f32_16x16x32_bf16 v[80:83], a[252:255], v[120:123], v[80:83]\n\t" \
        "v_cvt_pk_bf16_f32 v57, v58, v59\n\t" \
        "v_cvt_pk_bf16_f32 v58, v60, v61\n\t" \
        "v_mfma_f32_16x16x32_bf16 v[84:87], v[188:191], v[120:123], v[84:87]\n\t" \
        "v_cvt_pk_bf16_f32 v59, v62, v63\n\t" \
        "v_permlane16_swap_b32 v56, v58\n\t" \
        "v_mfma_f32_16x16x32_bf16 v[88:91], v[192:195], v[120:123], v[88:91]\n\t" \
        "ds_read_b128 v[120:123], v14 offset:39808\n\t" \
        "v_permlane16_swap_b32 v57, v59\n\t" \
        "v_mfma_f32_16x16x32_bf16 v[80:83], v[196:199], v[124:127], v[80:83]\n\t" \
        "ds_read_b64 v[66:67], v12 offset:4096\n\t" \
        "v_mfma_f32_16x16x32_bf16 v[84:87], v[200:203], v[124:127], v[84:87]\n\t" \
        "v_mfma_f32_16x16x32_bf16 v[88:91], v[204:207], v[124:127], v[88:91]\n\t" \
        "ds_read_b128 v[124:127], v15 offset:39680\n\t" \
        "v_mfma_f32_16x16x32_bf16 v[80:83], v[208:211], v[128:131], v[80:83]\n\t" \
        "v_mfma_f32_16x16x32_bf16 v[84:87], v[212:215], v[128:131], v[84:87]\n\t" \
        "v_mfma_f32_16x16x32_bf16 v[88:91], v[216:219], v[128:131], v[88:91]\n\t" \
        "ds_read_b128 v[128:131], v15 offset:39744\n\t" \
        "v_mfma_f32_16x16x32_bf16 v[80:83], v[220:223], v[132:135], v[80:83]\n\t" \
        "v_mfma_f32_16x16x32_bf16 v[84:87], v[224:227], v[132:135], v[84:87]\n\t" \
        "v_mfma_f32_16x16x32_bf16 v[88:91], v[228:231], v[132:135], v[88:91]\n\t" \
        "ds_read_b128 v[132:135], v15 offset:39808\n\t" \
        "s_waitcnt lgkmcnt(3)\n\t" \
        "v_permlane16_swap_b32 v64, v66\n\t" \
        "v_mfma_f32_16x16x32_bf16 v[80:83], v[232:235], v[136:139], v[80:83]\n\t" \
        "v_permlane16_swap_b32 v65, v67\n\t" \
        "v_mfma_f32_16x16x32_bf16 v[84:87], v[236:239], v[136:139], v[84:87]\n\t" \
        "v_mfma_f32_16x16x32_bf16 v[88:91], v[240:243], v[136:139], v[88:91]\n\t" \
        "ds_read_b128 v[136:139], v16 offset:39680\n\t" \
        "v_mfma_f32_16x16x32_bf16 v[80:83], v[244:247], v[140:143], v[80:83]\n\t" \
        "v_mfma_f32_16x16x32_bf16 v[84:87], v[248:251], v[140:143], v[84:87]\n\t" \
        "v_mfma_f32_16x16x32_bf16 v[88:91], v[252:255], v[140:143], v[88:91]\n\t" \
        "ds_read_b128 v[140:143], v16 offset:39744\n\t" \
        "v_mfma_f32_16x16x32_bf16 v[92:95], a[0:3], v[144:147], 0\n\t" \
        "v_mfma_f32_16x16x32_bf16 v[96:99], a[4:7], v[144:147], 0\n\t" \
        "v_mfma_f32_16x16x32_bf16 v[100:103], a[8:11], v[144:147], 0\n\t" \
        "s_nop 5\n\t" \
        "v_cvt_pk_bf16_f32 v88, v88, v89\n\t" \
        "v_cvt_pk_bf16_f32 v89, v90, v91\n\t" \
        "v_mfma_f32_16x16x32_bf16 v[92:95], a[12:15], v[116:119], v[92:95]\n\t" \
        "s_add_u32 s56, s64, 0x2a000\n\t" \
        "buffer_store_dwordx4 v[56:59], v9, s[28:31], s56 offen sc1\n\t" \
        "v_mfma_f32_16x16x32_bf16 v[96:99], a[16:19], v[116:119], v[96:99]\n\t" \
        "ds_write_b64 v11, v[88:89] offset:8192\n\t" \
        "v_cvt_pk_bf16_f32 v80, v80, v81\n\t" \
        "v_mfma_f32_16x16x32_bf16 v[100:103], a[20:23], v[116:119], v[100:103]\n\t" \
        "ds_read_b128 v[144:147], v16 offset:39808\n\t" \
        "v_cvt_pk_bf16_f32 v81, v82, v83\n\t" \
        "v_mfma_f32_16x16x32_bf16 v[92:95], a[24:27], v[120:123], v[92:95]\n\t" \
        "ds_read_b128 v[116:119], v14 offset:43648\n\t" \
        "v_cvt_pk_bf16_f32 v82, v84, v85\n\t" \
        "v_mfma_f32_16x16x32_bf16 v[96:99], a[28:31], v[120:123], v[96:99]\n\t" \
        "v_cvt_pk_bf16_f32 v83, v86, v87\n\t" \
        "v_permlane16_swap_b32 v80, v82\n\t" \
        "v_mfma_f32_16x16x32_bf16 v[100:103], a[32:35], v[120:123], v[100:103]\n\t" \
        "ds_read_b128 v[120:123], v14 offset:43712\n\t" \
        "v_permlane16_swap_b32 v81, v83\n\t" \
        "s_waitcnt lgkmcnt(5)\n\t" \
        "v_mfma_f32_16x16x32_bf16 v[92:95], a[36:39], v[124:127], v[92:95]\n\t" \
        "v_mfma_f32_16x16x32_bf16 v[96:99], a[40:43], v[124:127], v[96:99]\n\t" \
        "s_add_u32 s56, s64, 0x2a000\n\t" \
        "buffer_store_dwordx4 v[64:67], v10, s[28:31], s56 offen sc1\n\t" \
        "v_mfma_f32_16x16x32_bf16 v[100:103], a[44:47], v[124:127], v[100:103]\n\t" \
        "ds_read_b128 v[124:127], v14 offset:43776\n\t" \
        "v_cvt_pk_bf16_f32 v68, v68, v69\n\t" \
        "v_mfma_f32_16x16x32_bf16 v[92:95], a[48:51], v[128:131], v[92:95]\n\t" \
        "v_cvt_pk_bf16_f32 v69, v70, v71\n\t" \
        "v_cvt_pk_bf16_f32 v70, v72, v73\n\t" \
        "v_mfma_f32_16x16x32_bf16 v[96:99], a[52:55], v[128:131], v[96:99]\n\t" \
        "s_waitcnt lgkmcnt(4)\n\t" \
        "s_barrier\n\t" \
        "ds_read_b64 v[90:91], v12 offset:8192\n\t" \
        "v_cvt_pk_bf16_f32 v71, v74, v75\n\t" \
        "v_mfma_f32_16x16x32_bf16 v[100:103], a[56:59], v[128:131], v[100:103]\n\t" \
        "ds_read_b128 v[128:131], v15 offset:43648\n\t" \
        "v_permlane16_swap_b32 v68, v70\n\t" \
        "v_mfma_f32_16x16x32_bf16 v[92:95], a[60:63], v[132:135], v[92:95]\n\t" \
        "ds_read_b64 v[78:79], v12 offset:6144\n\t" \
        "v_permlane16_swap_b32 v69, v71\n\t" \
        "v_mfma_f32_16x16x32_bf16 v[96:99], a[64:67], v[132:135], v[96:99]\n\t" \
        "v_mfma_f32_16x16x32_bf16 v[100:103], a[68:71], v[132:135], v[100:103]\n\t" \
        "ds_read_b128 v[132:135], v15 offset:43712\n\t" \
        "v_mfma_f32_16x16x32_bf16 v[92:95], a[72:75], v[136:139], v[92:95]\n\t" \
        "v_mfma_f32_16x16x32_bf16 v[96:99], a[76:79], v[136:139], v[96:99]\n\t" \
        "v_mfma_f32_16x16x32_bf16 v[100:103], a[80:83], v[136:139], v[100:103]\n\t" \
        "ds_read_b128 v[136:139], v15 offset:43776\n\t" \
        "v_mfma_f32_16x16x32_bf16 v[92:95], a[84:87], v[140:143], v[92:95]\n\t" \
        "s_waitcnt lgkmcnt(4)\n\t" \
        "v_permlane16_swap_b32 v88, v90\n\t" \
        "v_permlane16_swap_b32 v89, v91\n\t" \
        "v_mfma_f32_16x16x32_bf16 v[96:99], a[88:91], v[140:143], v[96:99]\n\t" \
        "v_mfma_f32_16x16x32_bf16 v[100:103], a[92:95], v[140:143], v[100:103]\n\t" \
        "ds_read_b128 v[140:143], v16 offset:43648\n\t" \
        "s_waitcnt lgkmcnt(3)\n\t" \
        "v_permlane16_swap_b32 v76, v78\n\t" \
        "v_mfma_f32_16x16x32_bf16 v[92:95], a[96:99], v[144:147], v[92:95]\n\t" \
        "s_add_u32 s56, s64, 0x54000\n\t" \
        "buffer_store_dwordx4 v[80:83], v9, s[28:31], s56 offen sc1\n\t" \
        "v_mfma_f32_16x16x32_bf16 v[96:99], a[100:103], v[144:147], v[96:99]\n\t" \
        "v_permlane16_swap_b32 v77, v79\n\t" \
        "v_mfma_f32_16x16x32_bf16 v[100:103], a[104:107], v[144:147], v[100:103]\n\t" \
        "ds_read_b128 v[144:147], v16 offset:43712\n\t" \
        "v_mfma_f32_16x16x32_bf16 v[92:95], a[108:111], v[116:119], v[92:95]\n\t" \
        "v_mfma_f32_16x16x32_bf16 v[96:99], a[112:115], v[116:119], v[96:99]\n\t" \
        "v_mfma_f32_16x16x32_bf16 v[100:103], a[116:119], v[116:119], v[100:103]\n\t" \
        "ds_read_b128 v[116:119], v16 offset:43776\n\t" \
        "v_mfma_f32_16x16x32_bf16 v[92:95], a[120:123], v[120:123], v[92:95]\n\t" \
        "v_mfma_f32_16x16x32_bf16 v[96:99], a[124:127], v[120:123], v[96:99]\n\t" \
        "s_add_u32 s56, s64, 0x54000\n\t" \
        "buffer_store_dwordx4 v[88:91], v10, s[28:31], s56 offen sc1\n\t" \
        "v_mfma_f32_16x16x32_bf16 v[100:103], a[128:131], v[120:123], v[100:103]\n\t" \
        "ds_read_b128 v[120:123], v14 offset:47616\n\t" \
        "v_mfma_f32_16x16x32_bf16 v[92:95], a[132:135], v[124:127], v[92:95]\n\t" \
        "s_add_u32 s56, s64, 0x3f000\n\t" \
        "buffer_store_dwordx4 v[68:71], v9, s[28:31], s56 offen sc1\n\t" \
        "v_mfma_f32_16x16x32_bf16 v[96:99], a[136:139], v[124:127], v[96:99]\n\t" \
        "v_mfma_f32_16x16x32_bf16 v[100:103], a[140:143], v[124:127], v[100:103]\n\t" \
        "ds_read_b128 v[124:127], v14 offset:47680\n\t" \
        "v_mfma_f32_16x16x32_bf16 v[92:95], a[144:147], v[128:131], v[92:95]\n\t" \
        "v_mfma_f32_16x16x32_bf16 v[96:99], a[148:151], v[128:131], v[96:99]\n\t" \
        "v_mfma_f32_16x16x32_bf16 v[100:103], a[152:155], v[128:131], v[100:103]\n\t" \
        "ds_read_b128 v[128:131], v14 offset:47744\n\t" \
        "s_waitcnt lgkmcnt(4)\n\t" \
        "v_mfma_f32_16x16x32_bf16 v[92:95], a[156:159], v[132:135], v[92:95]\n\t" \
        "v_mfma_f32_16x16x32_bf16 v[96:99], a[160:163], v[132:135], v[96:99]\n\t" \
        "s_add_u32 s56, s64, 0x3f000\n\t" \
        "buffer_store_dwordx4 v[76:79], v10, s[28:31], s56 offen sc1\n\t" \
        "v_mfma_f32_16x16x32_bf16 v[100:103], a[164:167], v[132:135], v[100:103]\n\t" \
        "ds_read_b128 v[132:135], v15 offset:47616\n\t" \
        "v_mfma_f32_16x16x32_bf16 v[92:95], a[168:171], v[136:139], v[92:95]\n\t" \
        "v_mfma_f32_16x16x32_bf16 v[96:99], a[172:175], v[136:139], v[96:99]\n\t" \
        "v_mfma_f32_16x16x32_bf16 v[100:103], a[176:179], v[136:139], v[100:103]\n\t" \
        "ds_read_b128 v[136:139], v15 offset:47680\n\t" \
        "v_mfma_f32_16x16x32_bf16 v[92:95], a[180:183], v[140:143], v[92:95]\n\t" \
        "v_mfma_f32_16x16x32_bf16 v[96:99], a[184:187], v[140:143], v[96:99]\n\t" \
        "v_mfma_f32_16x16x32_bf16 v[100:103], a[188:191], v[140:143], v[100:103]\n\t" \
        "ds_read_b128 v[140:143], v15 offset:47744\n\t" \
        "v_mfma_f32_16x16x32_bf16 v[92:95], a[192:195], v[144:147], v[92:95]\n\t" \
        "v_mfma_f32_16x16x32_bf16 v[96:99], a[196:199], v[144:147], v[96:99]\n\t" \
        "v_mfma_f32_16x16x32_bf16 v[100:103], a[200:203], v[144:147], v[100:103]\n\t" \
        "ds_read_b128 v[144:147], v16 offset:47616\n\t" \
        "s_waitcnt lgkmcnt(4)\n\t" \
        "v_mfma_f32_16x16x32_bf16 v[92:95], a[204:207], v[116:119], v[92:95]\n\t" \
        "v_mfma_f32_16x16x32_bf16 v[96:99], a[208:211], v[116:119], v[96:99]\n\t" \
        "v_mfma_f32_16x16x32_bf16 v[100:103], a[212:215], v[116:119], v[100:103]\n\t" \
        "ds_read_b128 v[116:119], v16 offset:47680\n\t" \
        "v_mfma_f32_16x16x32_bf16 v[92:95], a[216:219], v[120:123], v[92:95]\n\t" \
        "v_mfma_f32_16x16x32_bf16 v[96:99], a[220:223], v[120:123], v[96:99]\n\t" \
        "v_mfma_f32_16x16x32_bf16 v[100:103], a[224:227], v[120:123], v[100:103]\n\t" \
        "ds_read_b128 v[120:123], v16 offset:47744\n\t" \
        "v_mfma_f32_16x16x32_bf16 v[92:95], a[228:231], v[124:127], v[92:95]\n\t" \
        "v_mfma_f32_16x16x32_bf16 v[96:99], a[232:235], v[124:127], v[96:99]\n\t" \
        "v_mfma_f32_16x16x32_bf16 v[100:103], a[236:239], v[124:127], v[100:103]\n\t" \
        "ds_read_b128 v[124:127], v14 offset:47616\n\t" \
        "v_mfma_f32_16x16x32_bf16 v[92:95], a[240:243], v[128:131], v[92:95]\n\t" \
        "v_mfma_f32_16x16x32_bf16 v[96:99], a[244:247], v[128:131], v[96:99]\n\t" \
        "v_mfma_f32_16x16x32_bf16 v[100:103], a[248:251], v[128:131], v[100:103]\n\t" \
        "ds_read_b128 v[128:131], v14 offset:47680\n\t" \
        "s_waitcnt lgkmcnt(4)\n\t" \
        "v_mfma_f32_16x16x32_bf16 v[92:95], a[252:255], v[132:135], v[92:95]\n\t" \
        "v_mfma_f32_16x16x32_bf16 v[96:99], v[188:191], v[132:135], v[96:99]\n\t" \
        "v_mfma_f32_16x16x32_bf16 v[100:103], v[192:195], v[132:135], v[100:103]\n\t" \
        "ds_read_b128 v[132:135], v14 offset:47744\n\t" \
        "v_mfma_f32_16x16x32_bf16 v[92:95], v[196:199], v[136:139], v[92:95]\n\t" \
        "v_mfma_f32_16x16x32_bf16 v[96:99], v[200:203], v[136:139], v[96:99]\n\t" \
        "v_mfma_f32_16x16x32_bf16 v[100:103], v[204:207], v[136:139], v[100:103]\n\t" \
        "ds_read_b128 v[136:139], v15 offset:47616\n\t" \
        "v_mfma_f32_16x16x32_bf16 v[92:95], v[208:211], v[140:143], v[92:95]\n\t" \
        "v_mfma_f32_16x16x32_bf16 v[96:99], v[212:215], v[140:143], v[96:99]\n\t" \
        "v_mfma_f32_16x16x32_bf16 v[100:103], v[216:219], v[140:143], v[100:103]\n\t" \
        "ds_read_b128 v[140:143], v15 offset:47680\n\t" \
        "v_mfma_f32_16x16x32_bf16 v[92:95], v[220:223], v[144:147], v[92:95]\n\t" \
        "v_mfma_f32_16x16x32_bf16 v[96:99], v[224:227], v[144:147], v[96:99]\n\t" \
        "v_mfma_f32_16x16x32_bf16 v[100:103], v[228:231], v[144:147], v[100:103]\n\t" \
        "ds_read_b128 v[144:147], v15 offset:47744\n\t" \
        "s_waitcnt lgkmcnt(4)\n\t" \
        "v_mfma_f32_16x16x32_bf16 v[92:95], v[232:235], v[116:119], v[92:95]\n\t" \
        "v_mfma_f32_16x16x32_bf16 v[96:99], v[236:239], v[116:119], v[96:99]\n\t" \
        "v_mfma_f32_16x16x32_bf16 v[100:103], v[240:243], v[116:119], v[100:103]\n\t" \
        "ds_read_b128 v[116:119], v16 offset:47616\n\t" \
        "v_mfma_f32_16x16x32_bf16 v[92:95], v[244:247], v[120:123], v[92:95]\n\t" \
        "v_mfma_f32_16x16x32_bf16 v[96:99], v[248:251], v[120:123], v[96:99]\n\t" \
        "v_mfma_f32_16x16x32_bf16 v[100:103], v[252:255], v[120:123], v[100:103]\n\t" \
        "ds_read_b128 v[120:123], v16 offset:47680\n\t" \
        "v_mfma_f32_16x16x32_bf16 v[104:107], a[0:3], v[124:127], 0\n\t" \
        "v_mfma_f32_16x16x32_bf16 v[108:111], a[4:7], v[124:127], 0\n\t" \
        "v_mfma_f32_16x16x32_bf16 v[112:115], a[8:11], v[124:127], 0\n\t" \
        "s_nop 5\n\t" \
        "v_cvt_pk_bf16_f32 v100, v100, v101\n\t" \
        "v_cvt_pk_bf16_f32 v101, v102, v103\n\t" \
        "v_mfma_f32_16x16x32_bf16 v[104:107], a[12:15], v[128:131], v[104:107]\n\t" \
        "ds_write_b64 v11, v[100:101] offset:10240\n\t" \
        "v_cvt_pk_bf16_f32 v92, v92, v93\n\t" \
        "v_mfma_f32_16x16x32_bf16 v[108:111], a[16:19], v[128:131], v[108:111]\n\t" \
        "ds_read_b128 v[124:127], v16 offset:47744\n\t" \
        "v_cvt_pk_bf16_f32 v93, v94, v95\n\t" \
        "v_mfma_f32_16x16x32_bf16 v[112:115], a[20:23], v[128:131], v[112:115]\n\t" \
        "ds_read_b128 v[128:131], v14 offset:51584\n\t" \
        "v_cvt_pk_bf16_f32 v94, v96, v97\n\t" \
        "s_waitcnt lgkmcnt(5)\n\t" \
        "v_mfma_f32_16x16x32_bf16 v[104:107], a[24:27], v[132:135], v[104:107]\n\t" \
        "v_cvt_pk_bf16_f32 v95, v98, v99\n\t" \
        "v_permlane16_swap_b32 v92, v94\n\t" \
        "v_mfma_f32_16x16x32_bf16 v[108:111], a[28:31], v[132:135], v[108:111]\n\t" \
        "v_permlane16_swap_b32 v93, v95\n\t" \
        "v_mfma_f32_16x16x32_bf16 v[112:115], a[32:35], v[132:135], v[112:115]\n\t" \
        "ds_read_b128 v[132:135], v14 offset:51648\n\t" \
        "v_mfma_f32_16x16x32_bf16 v[104:107], a[36:39], v[136:139], v[104:107]\n\t" \
        "v_mfma_f32_16x16x32_bf16 v[108:111], a[40:43], v[136:139], v[108:111]\n\t" \
        "v_mfma_f32_16x16x32_bf16 v[112:115], a[44:47], v[136:139], v[112:115]\n\t" \
        "ds_read_b128 v[136:139], v14 offset:51712\n\t" \
        "v_mfma_f32_16x16x32_bf16 v[104:107], a[48:51], v[140:143], v[104:107]\n\t" \
        "v_mfma_f32_16x16x32_bf16 v[108:111], a[52:55], v[140:143], v[108:111]\n\t" \
        "s_waitcnt lgkmcnt(4)\n\t" \
        "s_barrier\n\t" \
        "ds_read_b64 v[102:103], v12 offset:10240\n\t" \
        "v_mfma_f32_16x16x32_bf16 v[112:115], a[56:59], v[140:143], v[112:115]\n\t" \
        "ds_read_b128 v[140:143], v15 offset:51584\n\t" \
        "v_mfma_f32_16x16x32_bf16 v[104:107], a[60:63], v[144:147], v[104:107]\n\t" \
        "v_mfma_f32_16x16x32_bf16 v[108:111], a[64:67], v[144:147], v[108:111]\n\t" \
        "v_mfma_f32_16x16x32_bf16 v[112:115], a[68:71], v[144:147], v[112:115]\n\t" \
        "ds_read_b128 v[144:147], v15 offset:51648\n\t" \
        "v_mfma_f32_16x16x32_bf16 v[104:107], a[72:75], v[116:119], v[104:107]\n\t" \
        "v_mfma_f32_16x16x32_bf16 v[108:111], a[76:79], v[116:119], v[108:111]\n\t" \
        "v_mfma_f32_16x16x32_bf16 v[112:115], a[80:83], v[116:119], v[112:115]\n\t" \
        "ds_read_b128 v[116:119], v15 offset:51712\n\t" \
        "v_mfma_f32_16x16x32_bf16 v[104:107], a[84:87], v[120:123], v[104:107]\n\t" \
        "s_waitcnt lgkmcnt(3)\n\t" \
        "v_permlane16_swap_b32 v100, v102\n\t" \
        "v_permlane16_swap_b32 v101, v103\n\t" \
        "v_mfma_f32_16x16x32_bf16 v[108:111], a[88:91], v[120:123], v[108:111]\n\t" \
        "v_mfma_f32_16x16x32_bf16 v[112:115], a[92:95], v[120:123], v[112:115]\n\t" \
        "ds_read_b128 v[120:123], v16 offset:51584\n\t" \
        "v_mfma_f32_16x16x32_bf16 v[104:107], a[96:99], v[124:127], v[104:107]\n\t" \
        "s_add_u32 s56, s64, 0x69000\n\t" \
        "buffer_store_dwordx4 v[92:95], v9, s[28:31], s56 offen sc1\n\t" \
        "v_mfma_f32_16x16x32_bf16 v[108:111], a[100:103], v[124:127], v[108:111]\n\t" \
        "v_mfma_f32_16x16x32_bf16 v[112:115], a[104:107], v[124:127], v[112:115]\n\t" \
        "ds_read_b128 v[124:127], v16 offset:51648\n\t" \
        "v_mfma_f32_16x16x32_bf16 v[104:107], a[108:111], v[128:131], v[104:107]\n\t" \
        "v_mfma_f32_16x16x32_bf16 v[108:111], a[112:115], v[128:131], v[108:111]\n\t" \
        "v_mfma_f32_16x16x32_bf16 v[112:115], a[116:119], v[128:131], v[112:115]\n\t" \
        "ds_read_b128 v[128:131], v16 offset:51712\n\t" \
        "v_mfma_f32_16x16x32_bf16 v[104:107], a[120:123], v[132:135], v[104:107]\n\t" \
        "v_mfma_f32_16x16x32_bf16 v[108:111], a[124:127], v[132:135], v[108:111]\n\t" \
        "s_add_u32 s56, s64, 0x69000\n\t" \
        "buffer_store_dwordx4 v[100:103], v10, s[28:31], s56 offen sc1\n\t" \
        "v_mfma_f32_16x16x32_bf16 v[112:115], a[128:131], v[132:135], v[112:115]\n\t" \
        "ds_read_b128 v[132:135], v14 offset:55552\n\t" \
        "v_mfma_f32_16x16x32_bf16 v[104:107], a[132:135], v[136:139], v[104:107]\n\t" \
        "v_mfma_f32_16x16x32_bf16 v[108:111], a[136:139], v[136:139], v[108:111]\n\t" \
        "v_mfma_f32_16x16x32_bf16 v[112:115], a[140:143], v[136:139], v[112:115]\n\t" \
        "ds_read_b128 v[136:139], v14 offset:55616\n\t" \
        "s_waitcnt lgkmcnt(4)\n\t" \
        "v_mfma_f32_16x16x32_bf16 v[104:107], a[144:147], v[140:143], v[104:107]\n\t" \
        "v_mfma_f32_16x16x32_bf16 v[108:111], a[148:151], v[140:143], v[108:111]\n\t" \
        "v_mfma_f32_16x16x32_bf16 v[112:115], a[152:155], v[140:143], v[112:115]\n\t" \
        "ds_read_b128 v[140:143], v14 offset:55680\n\t" \
        "v_mfma_f32_16x16x32_bf16 v[104:107], a[156:159], v[144:147], v[104:107]\n\t" \
        "v_mfma_f32_16x16x32_bf16 v[108:111], a[160:163], v[144:147], v[108:111]\n\t" \
        "v_mfma_f32_16x16x32_bf16 v[112:115], a[164:167], v[144:147], v[112:115]\n\t" \
        "ds_read_b128 v[144:147], v15 offset:55552\n\t" \
        "v_mfma_f32_16x16x32_bf16 v[104:107], a[168:171], v[116:119], v[104:107]\n\t" \
        "v_mfma_f32_16x16x32_bf16 v[108:111], a[172:175], v[116:119], v[108:111]\n\t" \
        "v_mfma_f32_16x16x32_bf16 v[112:115], a[176:179], v[116:119], v[112:115]\n\t" \
        "ds_read_b128 v[116:119], v15 offset:55616\n\t" \
        "v_mfma_f32_16x16x32_bf16 v[104:107], a[180:183], v[120:123], v[104:107]\n\t" \
        "v_mfma_f32_16x16x32_bf16 v[108:111], a[184:187], v[120:123], v[108:111]\n\t" \
        "v_mfma_f32_16x16x32_bf16 v[112:115], a[188:191], v[120:123], v[112:115]\n\t" \
        "ds_read_b128 v[120:123], v15 offset:55680\n\t" \
        "s_waitcnt lgkmcnt(4)\n\t" \
        "v_mfma_f32_16x16x32_bf16 v[104:107], a[192:195], v[124:127], v[104:107]\n\t" \
        "v_mfma_f32_16x16x32_bf16 v[108:111], a[196:199], v[124:127], v[108:111]\n\t" \
        "v_mfma_f32_16x16x32_bf16 v[112:115], a[200:203], v[124:127], v[112:115]\n\t" \
        "ds_read_b128 v[124:127], v16 offset:55552\n\t" \
        "v_mfma_f32_16x16x32_bf16 v[104:107], a[204:207], v[128:131], v[104:107]\n\t" \
        "v_mfma_f32_16x16x32_bf16 v[108:111], a[208:211], v[128:131], v[108:111]\n\t" \
        "v_mfma_f32_16x16x32_bf16 v[112:115], a[212:215], v[128:131], v[112:115]\n\t" \
        "ds_read_b128 v[128:131], v16 offset:55616\n\t" \
        "v_mfma_f32_16x16x32_bf16 v[104:107], a[216:219], v[132:135], v[104:107]\n\t" \
        "v_mfma_f32_16x16x32_bf16 v[108:111], a[220:223], v[132:135], v[108:111]\n\t" \
        "v_mfma_f32_16x16x32_bf16 v[112:115], a[224:227], v[132:135], v[112:115]\n\t" \
        "ds_read_b128 v[132:135], v16 offset:55680\n\t" \
        "v_mfma_f32_16x16x32_bf16 v[104:107], a[228:231], v[136:139], v[104:107]\n\t" \
        "v_mfma_f32_16x16x32_bf16 v[108:111], a[232:235], v[136:139], v[108:111]\n\t" \
        "v_mfma_f32_16x16x32_bf16 v[112:115], a[236:239], v[136:139], v[112:115]\n\t" \
        "s_waitcnt lgkmcnt(3)\n\t" \
        "v_mfma_f32_16x16x32_bf16 v[104:107], a[240:243], v[140:143], v[104:107]\n\t" \
        "v_mfma_f32_16x16x32_bf16 v[108:111], a[244:247], v[140:143], v[108:111]\n\t" \
        "v_mfma_f32_16x16x32_bf16 v[112:115], a[248:251], v[140:143], v[112:115]\n\t" \
        "v_mfma_f32_16x16x32_bf16 v[104:107], a[252:255], v[144:147], v[104:107]\n\t" \
        "v_mfma_f32_16x16x32_bf16 v[108:111], v[188:191], v[144:147], v[108:111]\n\t" \
        "v_mfma_f32_16x16x32_bf16 v[112:115], v[192:195], v[144:147], v[112:115]\n\t" \
        "v_mfma_f32_16x16x32_bf16 v[104:107], v[196:199], v[116:119], v[104:107]\n\t" \
        "v_mfma_f32_16x16x32_bf16 v[108:111], v[200:203], v[116:119], v[108:111]\n\t" \
        "v_mfma_f32_16x16x32_bf16 v[112:115], v[204:207], v[116:119], v[112:115]\n\t" \
        "v_mfma_f32_16x16x32_bf16 v[104:107], v[208:211], v[120:123], v[104:107]\n\t" \
        "v_mfma_f32_16x16x32_bf16 v[108:111], v[212:215], v[120:123], v[108:111]\n\t" \
        "v_mfma_f32_16x16x32_bf16 v[112:115], v[216:219], v[120:123], v[112:115]\n\t" \
        "s_waitcnt lgkmcnt(0)\n\t" \
        "v_mfma_f32_16x16x32_bf16 v[104:107], v[220:223], v[124:127], v[104:107]\n\t" \
        "v_mfma_f32_16x16x32_bf16 v[108:111], v[224:227], v[124:127], v[108:111]\n\t" \
        "v_mfma_f32_16x16x32_bf16 v[112:115], v[228:231], v[124:127], v[112:115]\n\t" \
        "v_mfma_f32_16x16x32_bf16 v[104:107], v[232:235], v[128:131], v[104:107]\n\t" \
        "v_mfma_f32_16x16x32_bf16 v[108:111], v[236:239], v[128:131], v[108:111]\n\t" \
        "v_mfma_f32_16x16x32_bf16 v[112:115], v[240:243], v[128:131], v[112:115]\n\t" \
        "v_mfma_f32_16x16x32_bf16 v[104:107], v[244:247], v[132:135], v[104:107]\n\t" \
        "v_mfma_f32_16x16x32_bf16 v[108:111], v[248:251], v[132:135], v[108:111]\n\t" \
        "v_mfma_f32_16x16x32_bf16 v[112:115], v[252:255], v[132:135], v[112:115]\n\t" \
        "s_nop 9\n\t" \
        "v_cvt_pk_bf16_f32 v112, v112, v113\n\t" \
        "v_cvt_pk_bf16_f32 v113, v114, v115\n\t" \
        "v_cvt_pk_bf16_f32 v104, v104, v105\n\t" \
        "v_cvt_pk_bf16_f32 v105, v106, v107\n\t" \
        "v_cvt_pk_bf16_f32 v106, v108, v109\n\t" \
        "ds_write_b64 v11, v[112:113] offset:12288\n\t" \
        "v_cvt_pk_bf16_f32 v107, v110, v111\n\t" \
        "v_permlane16_swap_b32 v104, v106\n\t" \
        "s_nop 0\n\t" \
        "v_permlane16_swap_b32 v105, v107\n\t" \
        "s_add_u32 s56, s64, 0x7e000\n\t" \
        "buffer_store_dwordx4 v[104:107], v9, s[28:31], s56 offen sc1\n\t" \
        "s_waitcnt lgkmcnt(0)\n\t" \
        "s_barrier\n\t" \
        "ds_read_b64 v[114:115], v12 offset:12288\n\t" \
        "s_waitcnt lgkmcnt(0)\n\t" \
        "v_permlane16_swap_b32 v112, v114\n\t" \
        "v_permlane16_swap_b32 v113, v115\n\t" \
        "s_add_u32 s56, s64, 0x7e000\n\t" \
        "buffer_store_dwordx4 v[112:115], v10, s[28:31], s56 offen sc1\n\t" \
        "s_endpgm\n\t" \
        ".LR_abort%=:\n\t" \
        "s_waitcnt vmcnt(0) lgkmcnt(0)\n\t" \
        "s_barrier\n\t" \
        "s_nop 7\n\t" \
        "s_nop 7\n\t" \
        "s_branch .LT_entry%=\n\t" \
        ".LT_start%=:\n\t" \
        "s_sub_u32 s57, s36, 0x15000\n\t" \
        "s_mov_b32 m0, s38\n\t" \
        "s_nop 0\n\t" \
        "buffer_load_dwordx4 v13, s[16:19], s57 offen sc1 lds\n\t" \
        "v_lshrrev_b32 v164, 4, v2\n\t" \
        "v_add_u32 v168, s32, v18\n\t" \
        "v_add_u32 v169, s33, v19\n\t" \
        "s_mul_i32 s42, s35, 2304\n\t" \
        "v_lshl_add_u32 v165, v168, 1, -3\n\t" \
        "v_lshl_add_u32 v166, v169, 1, -3\n\t" \
        "s_add_u32 s42, s42, 64\n\t" \
        "v_lshl_add_u32 v16, v19, 4, s42\n\t" \
        "v_cmp_gt_u32 vcc, s34, v18\n\t" \
        "v_lshl_add_u32 v16, v164, 2, v16\n\t" \
        "s_mov_b64 s[10:11], vcc\n\t" \
        "s_movk_i32 s42, -1152\n\t" \
        "v_cvt_f32_u32 v60, v165\n\t" \
        "v_cvt_f32_u32 v63, v166\n\t" \
        "v_add_u32 v165, 2, v165\n\t" \
        "v_add_u32 v166, 2, v166\n\t" \
        "v_cvt_f32_u32 v61, v165\n\t" \
        "v_cvt_f32_u32 v64, v166\n\t" \
        "s_sub_u32 s57, s36, 0xa800\n\t" \
        "s_add_u32 m0, s38, 0xf80\n\t" \
        "s_nop 0\n\t" \
        "buffer_load_dwordx4 v13, s[16:19], s57 offen sc1 lds\n\t" \
        "v_add_u32 v165, 2, v165\n\t" \
        "v_add_u32 v166, 2, v166\n\t" \
        "v_cvt_f32_u32 v62, v165\n\t" \
        "v_cvt_f32_u32 v65, v166\n\t" \
        "v_mov_b32 v165, 0x7c00\n\t" \
        "v_mov_b32 v166, 0x7f800000\n\t" \
        "v_lshrrev_b32 v60, 23, v60\n\t" \
        "v_and_b32 v63, 0x7f800000, v63\n\t" \
        "v_lshl_add_u32 v60, v60, 7, s42\n\t" \
        "v_add_u32 v63, 0xfb800000, v63\n\t" \
        "v_cndmask_b32 v60, v165, v60, s[10:11]\n\t" \
        "v_cndmask_b32 v63, v166, v63, s[10:11]\n\t" \
        "v_lshrrev_b32 v61, 23, v61\n\t" \
        "v_and_b32 v64, 0x7f800000, v64\n\t" \
        "v_lshl_add_u32 v61, v61, 7, s42\n\t" \
        "v_add_u32 v64, 0xfb800000, v64\n\t" \
        "v_cndmask_b32 v61, v165, v61, s[10:11]\n\t" \
        "v_cndmask_b32 v64, v166, v64, s[10:11]\n\t" \
        "s_add_u32 s57, s36, 0x0\n\t" \
        "s_add_u32 m0, s38, 0x1f00\n\t" \
        "s_nop 0\n\t" \
        "buffer_load_dwordx4 v13, s[16:19], s57 offen sc1 lds\n\t" \
        "v_lshrrev_b32 v62, 23, v62\n\t" \
        "v_and_b32 v65, 0x7f800000, v65\n\t" \
        "v_lshl_add_u32 v62, v62, 7, s42\n\t" \
        "v_add_u32 v65, 0xfb800000, v65\n\t" \
        "v_cndmask_b32 v62, v165, v62, s[10:11]\n\t" \
        "v_cndmask_b32 v65, v166, v65, s[10:11]\n\t" \
        "v_add_u32 v168, -1, v168\n\t" \
        "v_add_u32 v169, -1, v169\n\t" \
        "v_lshl_or_b32 v50, v168, 16, v16\n\t" \
        "v_lshl_or_b32 v50, v169, 24, v50\n\t" \
        "v_add_u32 v51, 0x1000100, v50\n\t" \
        "v_add_u32 v52, 0x2000200, v50\n\t" \
        "v_add_u32 v53, 0x10300, v50\n\t" \
        "v_add_u32 v54, 0x1010400, v50\n\t" \
        "v_add_u32 v55, 0x2010500, v50\n\t" \
        "v_add_u32 v56, 0x20600, v50\n\t" \
        "v_add_u32 v57, 0x1020700, v50\n\t" \
        "v_add_u32 v58, 0x2020800, v50\n\t" \
        "s_add_u32 s57, s36, 0xa800\n\t" \
        "s_add_u32 m0, s38, 0x2e80\n\t" \
        "s_nop 0\n\t" \
        "buffer_load_dwordx4 v13, s[16:19], s57 offen sc1 lds\n\t" \
        "v_lshlrev_b32 v66, 3, v2\n\t" \
        "v_add_u32 v66, 0x1ae40, v66\n\t" \
        "v_mul_u32_u24 v174, 0xf80, v18\n\t" \
        "v_mul_u32_u24 v177, 0xc0, v19\n\t" \
        "v_lshlrev_b32 v178, 3, v19\n\t" \
        "v_add_u32 v170, 0x9780, v174\n\t" \
        "v_and_b32 v14, 48, v2\n\t" \
        "v_add_u32 v170, v170, v177\n\t" \
        "v_add_u32 v173, 8, v178\n\t" \
        "v_and_b32 v173, 32, v173\n\t" \
        "v_add_u32 v174, v170, v173\n\t" \
        "v_add_u32 v175, 0xf80, v174\n\t" \
        "v_add_u32 v176, 0x1f00, v174\n\t" \
        "ds_write2st64_b32 v16, v174, v175 offset0:0 offset1:3\n\t" \
        "ds_write_b32 v16, v176 offset:1536\n\t" \
        "v_add_u32 v170, 0xc0, v170\n\t" \
        "v_add_u32 v173, 16, v178\n\t" \
        "v_and_b32 v173, 32, v173\n\t" \
        "s_add_u32 s57, s36, 0x15000\n\t" \
        "s_add_u32 m0, s38, 0x3e00\n\t" \
        "s_nop 0\n\t" \
        "buffer_load_dwordx4 v13, s[16:19], s57 offen sc1 lds\n\t" \
        "v_add_u32 v174, v170, v173\n\t" \
        "v_add_u32 v175, 0xf80, v174\n\t" \
        "v_add_u32 v176, 0x1f00, v174\n\t" \
        "ds_write2st64_b32 v16, v174, v175 offset0:1 offset1:4\n\t" \
        "ds_write_b32 v16, v176 offset:1792\n\t" \
        "v_add_u32 v170, 0xc0, v170\n\t" \
        "v_add_u32 v173, 24, v178\n\t" \
        "v_and_b32 v173, 32, v173\n\t" \
        "v_add_u32 v174, v170, v173\n\t" \
        "v_add_u32 v175, 0xf80, v174\n\t" \
        "v_add_u32 v176, 0x1f00, v174\n\t" \
        "ds_write2st64_b32 v16, v174, v175 offset0:2 offset1:5\n\t" \
        "ds_write_b32 v16, v176 offset:2048\n\t" \
        "s_and_b32 s61, s35, 1\n\t" \
        "v_lshrrev_b32 v170, 4, v2\n\t" \
        "v_and_b32 v171, 15, v2\n\t" \
        "v_and_b32 v172, 16, v2\n\t" \
        "s_lshr_b32 s62, s35, 1\n\t" \
        "s_add_u32 s57, s36, 0x1f800\n\t" \
        "s_add_u32 m0, s38, 0x4d80\n\t" \
        "s_nop 0\n\t" \
        "buffer_load_dwordx4 v13, s[16:19], s57 offen sc1 lds\n\t" \
        "v_lshlrev_b32 v172, 1, v172\n\t" \
        "v_bfe_u32 v173, v2, 2, 2\n\t" \
        "v_mul_u32_u24 v175, 0xc0, v171\n\t" \
        "v_lshl_or_b32 v173, v170, 3, v173\n\t" \
        "v_and_b32 v174, 3, v2\n\t" \
        "v_mul_u32_u24 v173, 0xc0, v173\n\t" \
        "s_lshl_b32 s63, s61, 7\n\t" \
        "v_lshl_add_u32 v173, v174, 3, v173\n\t" \
        "s_add_u32 s63, s63, 0x18ec0\n\t" \
        "v_add3_u32 v6, v173, v172, s63\n\t" \
        "s_lshl_b32 s63, s61, 5\n\t" \
        "v_xor_b32 v174, s63, v172\n\t" \
        "v_xor_b32 v7, 32, v6\n\t" \
        "v_add_u32 v173, 0x18f00, v173\n\t" \
        "s_lshl_b32 s63, s61, 7\n\t" \
        "v_add_u32 v8, v173, v174\n\t" \
        "v_lshrrev_b32 v173, 1, v2\n\t" \
        "v_and_b32 v173, 16, v173\n\t" \
        "s_add_u32 s57, s36, 0x2a000\n\t" \
        "s_add_u32 m0, s38, 0x5d00\n\t" \
        "s_nop 0\n\t" \
        "buffer_load_dwordx4 v13, s[16:19], s57 offen sc1 lds\n\t" \
        "v_add_u32 v175, v175, v173\n\t" \
        "v_bfe_u32 v173, v2, 3, 1\n\t" \
        "v_add3_u32 v9, v175, v172, s63\n\t" \
        "v_cmp_eq_u32 vcc, s61, v173\n\t" \
        "v_add3_u32 v10, v175, v174, 64\n\t" \
        "v_bfrev_b32 v174, 1\n\t" \
        "s_lshl_b32 s63, s35, 9\n\t" \
        "v_cndmask_b32 v10, v174, v10, vcc\n\t" \
        "s_add_u32 s63, s63, 0x18ec0\n\t" \
        "v_lshl_add_u32 v11, v2, 3, s63\n\t" \
        "s_lshl_b32 s63, s61, 10\n\t" \
        "s_sub_u32 s63, 0x200, s63\n\t" \
        "v_add_u32 v12, s63, v11\n\t" \
        "s_add_u32 s64, s32, s62\n\t" \
        "s_mul_i32 s63, s33, 0xc0\n\t" \
        "s_mul_i32 s64, s64, 0xa800\n\t" \
        "s_add_u32 s64, s64, s63\n\t" \
        "s_mul_i32 s62, s62, 2304\n\t" \
        "s_add_u32 s57, s36, 0x34800\n\t" \
        "s_add_u32 m0, s38, 0x6c80\n\t" \
        "s_nop 0\n\t" \
        "buffer_load_dwordx4 v13, s[16:19], s57 offen sc1 lds\n\t" \
        "s_add_u32 s62, s62, 64\n\t" \
        "v_lshl_add_u32 v15, v19, 4, s62\n\t" \
        "s_lshl_b32 s48, s35, 10\n\t" \
        "s_and_b32 s55, s35, 1\n\t" \
        "s_add_u32 s48, s48, 0x19e40\n\t" \
        "s_mul_i32 s55, s55, 0x3180\n\t" \
        "s_cmp_lt_u32 s35, 2\n\t" \
        "s_cselect_b32 s49, 0, 0x30c0\n\t" \
        "s_cselect_b32 s53, 1, -1\n\t" \
        "s_add_u32 s49, s49, s55\n\t" \
        "s_mul_i32 s53, s53, 192\n\t" \
        "s_mov_b32 s52, 0x3ffe0\n\t" \
        "s_add_u32 s49, s49, 0x2440\n\t" \
        "v_lshrrev_b32 v170, 4, v2\n\t" \
        "v_and_b32 v24, 15, v2\n\t" \
        "v_bfe_u32 v171, v2, 3, 1\n\t" \
        "v_lshl_add_u32 v171, v170, 2, v171\n\t" \
        "v_lshrrev_b32 v172, 1, v2\n\t" \
        "s_add_u32 s57, s36, 0x3f000\n\t" \
        "s_add_u32 m0, s38, 0x7c00\n\t" \
        "s_nop 0\n\t" \
        "buffer_load_dwordx4 v13, s[16:19], s57 offen sc1 lds\n\t" \
        "v_and_b32 v172, 2, v172\n\t" \
        "v_lshl_add_u32 v171, v171, 2, v172\n\t" \
        "s_add_u32 s55, s48, 512\n\t" \
        "v_add_u32 v20, s55, v171\n\t" \
        "v_lshl_add_u32 v21, v24, 3, s48\n\t" \
        "v_bfe_u32 v171, v2, 1, 1\n\t" \
        "v_xor_b32 v171, v171, v170\n\t" \
        "v_lshrrev_b32 v172, 2, v24\n\t" \
        "v_and_b32 v173, 2, v172\n\t" \
        "v_xor_b32 v171, v171, v173\n\t" \
        "v_and_b32 v173, 4, v2\n\t" \
        "v_lshlrev_b32 v171, 4, v171\n\t" \
        "v_lshl_add_u32 v171, v173, 1, v171\n\t" \
        "v_mul_i32_i24 v22, s53, v24\n\t" \
        "v_add3_u32 v22, v22, v171, s49\n\t" \
        "v_and_b32 v171, 3, v2\n\t" \
        "v_and_b32 v173, 1, v170\n\t" \
        "v_lshlrev_b32 v173, 3, v173\n\t" \
        "s_add_u32 s57, s36, 0x49800\n\t" \
        "s_add_u32 m0, s38, 0x8b80\n\t" \
        "s_nop 0\n\t" \
        "buffer_load_dwordx4 v13, s[16:19], s57 offen sc1 lds\n\t" \
        "v_lshl_or_b32 v23, v171, 4, v173\n\t" \
        "v_bfe_u32 v172, v2, 2, 2\n\t" \
        "v_and_b32 v171, 3, v2\n\t" \
        "v_cmp_eq_u32 vcc, v170, v172\n\t" \
        "v_lshlrev_b32 v171, v171, 1\n\t" \
        "s_nop 0\n\t" \
        "v_cndmask_b32 v171, 0, v171, vcc\n\t" \
        "v_bfe_i32 v25, v171, 0, 1\n\t" \
        "v_bfe_i32 v26, v171, 1, 1\n\t" \
        "v_bfe_i32 v27, v171, 2, 1\n\t" \
        "v_bfe_i32 v28, v171, 3, 1\n\t" \
        "s_waitcnt vmcnt(10)\n\t" \
        "s_mov_b32 s45, s48\n\t" \
        "s_mov_b32 s54, 0\n\t" \
        "v_cmp_nlt_f32 s[4:5], |v40|, v63\n\t" \
        "v_cmp_nlt_f16 s[8:9], |v40|, v60\n\t" \
        "v_cmp_nlt_f32 s[6:7], |v41|, v64\n\t" \
        "v_cmp_nlt_f16 s[10:11], |v41|, v60\n\t" \
        "s_or_b64 s[12:13], s[4:5], s[8:9]\n\t" \
        "v_mbcnt_lo_u32_b32 v160, s12, 0\n\t" \
        "v_mbcnt_hi_u32_b32 v160, s13, v160\n\t" \
        "s_bcnt1_i32_b64 s55, s[12:13]\n\t" \
        "v_lshl_add_u32 v160, v160, 3, s45\n\t" \
        "s_lshl3_add_u32 s45, s55, s45\n\t" \
        "v_cndmask_b32 v160, v66, v160, s[12:13]\n\t" \
        "ds_write2_b32 v160, v40, v50 offset1:1\n\t" \
        "s_add_u32 s57, s36, 0x54000\n\t" \
        "s_add_u32 m0, s38, 0x9b00\n\t" \
        "s_nop 0\n\t" \
        "buffer_load_dwordx4 v13, s[16:19], s57 offen sc1 lds\n\t" \
        "s_add_u32 s57, s36, 0x5e800\n\t" \
        "s_add_u32 m0, s38, 0xaa80\n\t" \
        "s_nop 0\n\t" \
        "buffer_load_dwordx4 v13, s[16:19], s57 offen sc1 lds\n\t" \
        "v_cmp_nlt_f32 s[4:5], |v42|, v65\n\t" \
        "v_cmp_nlt_f16 s[8:9], |v42|, v60\n\t" \
        "s_or_b64 s[12:13], s[6:7], s[10:11]\n\t" \
        "v_mbcnt_lo_u32_b32 v161, s12, 0\n\t" \
        "v_mbcnt_hi_u32_b32 v161, s13, v161\n\t" \
        "s_bcnt1_i32_b64 s55, s[12:13]\n\t" \
        "v_lshl_add_u32 v161, v161, 3, s45\n\t" \
        "s_lshl3_add_u32 s45, s55, s45\n\t" \
        "v_cndmask_b32 v161, v66, v161, s[12:13]\n\t" \
        "ds_write2_b32 v161, v41, v51 offset1:1\n\t" \
        "s_add_u32 s57, s36, 0x69000\n\t" \
        "s_add_u32 m0, s38, 0xba00\n\t" \
        "s_nop 0\n\t" \
        "buffer_load_dwordx4 v13, s[16:19], s57 offen sc1 lds\n\t" \
        "s_add_u32 s57, s36, 0x73800\n\t" \
        "s_add_u32 m0, s38, 0xc980\n\t" \
        "s_nop 0\n\t" \
        "buffer_load_dwordx4 v13, s[16:19], s57 offen sc1 lds\n\t" \
        "v_cmp_nlt_f32 s[6:7], |v43|, v63\n\t" \
        "v_cmp_nlt_f16 s[10:11], |v43|, v61\n\t" \
        "s_or_b64 s[12:13], s[4:5], s[8:9]\n\t" \
        "v_mbcnt_lo_u32_b32 v160, s12, 0\n\t" \
        "v_mbcnt_hi_u32_b32 v160, s13, v160\n\t" \
        "s_bcnt1_i32_b64 s55, s[12:13]\n\t" \
        "v_lshl_add_u32 v160, v160, 3, s45\n\t" \
        "s_lshl3_add_u32 s45, s55, s45\n\t" \
        "v_cndmask_b32 v160, v66, v160, s[12:13]\n\t" \
        "ds_write2_b32 v160, v42, v52 offset1:1\n\t" \
        "s_add_u32 s57, s36, 0x7e000\n\t" \
        "s_min_u32 s57, s57, 0x925800\n\t" \
        "s_add_u32 m0, s38, 0xd900\n\t" \
        "s_nop 0\n\t" \
        "buffer_load_dwordx4 v13, s[16:19], s57 offen sc1 lds\n\t" \
        "v_cmp_nlt_f32 s[4:5], |v44|, v64\n\t" \
        "v_cmp_nlt_f16 s[8:9], |v44|, v61\n\t" \
        "s_or_b64 s[12:13], s[6:7], s[10:11]\n\t" \
        "v_mbcnt_lo_u32_b32 v161, s12, 0\n\t" \
        "v_mbcnt_hi_u32_b32 v161, s13, v161\n\t" \
        "s_bcnt1_i32_b64 s55, s[12:13]\n\t" \
        "v_lshl_add_u32 v161, v161, 3, s45\n\t" \
        "s_lshl3_add_u32 s45, s55, s45\n\t" \
        "v_cndmask_b32 v161, v66, v161, s[12:13]\n\t" \
        "ds_write2_b32 v161, v43, v53 offset1:1\n\t" \
        "s_add_u32 s57, s36, 0x88800\n\t" \
        "s_min_u32 s57, s57, 0x925800\n\t" \
        "s_add_u32 m0, s38, 0xe880\n\t" \
        "s_nop 0\n\t" \
        "buffer_load_dwordx4 v13, s[16:19], s57 offen sc1 lds\n\t" \
        "s_add_u32 s57, s36, 0x93000\n\t" \
        "s_min_u32 s57, s57, 0x925800\n\t" \
        "s_add_u32 m0, s38, 0xf800\n\t" \
        "s_nop 0\n\t" \
        "buffer_load_dwordx4 v13, s[16:19], s57 offen sc1 lds\n\t" \
        "v_cmp_nlt_f32 s[6:7], |v45|, v65\n\t" \
        "v_cmp_nlt_f16 s[10:11], |v45|, v61\n\t" \
        "s_or_b64 s[12:13], s[4:5], s[8:9]\n\t" \
        "v_mbcnt_lo_u32_b32 v160, s12, 0\n\t" \
        "v_mbcnt_hi_u32_b32 v160, s13, v160\n\t" \
        "s_bcnt1_i32_b64 s55, s[12:13]\n\t" \
        "v_lshl_add_u32 v160, v160, 3, s45\n\t" \
        "s_lshl3_add_u32 s45, s55, s45\n\t" \
        "v_cndmask_b32 v160, v66, v160, s[12:13]\n\t" \
        "ds_write2_b32 v160, v44, v54 offset1:1\n\t" \
        "s_add_u32 s57, s36, 0x9d800\n\t" \
        "s_min_u32 s57, s57, 0x925800\n\t" \
        "s_add_u32 m0, s38, 0x10780\n\t" \
        "s_nop 0\n\t" \
        "buffer_load_dwordx4 v13, s[16:19], s57 offen sc1 lds\n\t" \
        "v_cmp_nlt_f32 s[4:5], |v46|, v63\n\t" \
        "v_cmp_nlt_f16 s[8:9], |v46|, v62\n\t" \
        "s_or_b64 s[12:13], s[6:7], s[10:11]\n\t" \
        "v_mbcnt_lo_u32_b32 v161, s12, 0\n\t" \
        "v_mbcnt_hi_u32_b32 v161, s13, v161\n\t" \
        "s_bcnt1_i32_b64 s55, s[12:13]\n\t" \
        "v_lshl_add_u32 v161, v161, 3, s45\n\t" \
        "s_lshl3_add_u32 s45, s55, s45\n\t" \
        "v_cndmask_b32 v161, v66, v161, s[12:13]\n\t" \
        "ds_write2_b32 v161, v45, v55 offset1:1\n\t" \
        "s_mov_b32 s57, s40\n\t" \
        "s_add_u32 m0, s39, 0x3000\n\t" \
        "s_nop 0\n\t" \
        "buffer_load_dwordx4 v3, s[24:27], s57 offen offset:0 lds\n\t" \
        "buffer_load_dwordx4 v4, s[24:27], s57 offen offset:1024 lds\n\t" \
        "v_cmp_nlt_f32 s[6:7], |v47|, v64\n\t" \
        "v_cmp_nlt_f16 s[10:11], |v47|, v62\n\t" \
        "s_or_b64 s[12:13], s[4:5], s[8:9]\n\t" \
        "v_mbcnt_lo_u32_b32 v160, s12, 0\n\t" \
        "v_mbcnt_hi_u32_b32 v160, s13, v160\n\t" \
        "s_bcnt1_i32_b64 s55, s[12:13]\n\t" \
        "v_lshl_add_u32 v160, v160, 3, s45\n\t" \
        "s_lshl3_add_u32 s45, s55, s45\n\t" \
        "v_cndmask_b32 v160, v66, v160, s[12:13]\n\t" \
        "ds_write2_b32 v160, v46, v56 offset1:1\n\t" \
        "buffer_load_dwordx4 v5, s[24:27], s57 offen offset:2048 lds\n\t" \
        "v_cmp_nlt_f32 s[4:5], |v48|, v65\n\t" \
        "v_cmp_nlt_f16 s[8:9], |v48|, v62\n\t" \
        "s_or_b64 s[12:13], s[6:7], s[10:11]\n\t" \
        "v_mbcnt_lo_u32_b32 v161, s12, 0\n\t" \
        "v_mbcnt_hi_u32_b32 v161, s13, v161\n\t" \
        "s_bcnt1_i32_b64 s55, s[12:13]\n\t" \
        "v_lshl_add_u32 v161, v161, 3, s45\n\t" \
        "s_lshl3_add_u32 s45, s55, s45\n\t" \
        "v_cndmask_b32 v161, v66, v161, s[12:13]\n\t" \
        "ds_write2_b32 v161, v47, v57 offset1:1\n\t" \
        "s_add_u32 s57, s40, 0x3000\n\t" \
        "s_add_u32 m0, s39, 0x6000\n\t" \
        "s_nop 0\n\t" \
        "buffer_load_dwordx4 v3, s[24:27], s57 offen offset:0 lds\n\t" \
        "buffer_load_dwordx4 v4, s[24:27], s57 offen offset:1024 lds\n\t" \
        "s_or_b64 s[12:13], s[4:5], s[8:9]\n\t" \
        "v_mbcnt_lo_u32_b32 v160, s12, 0\n\t" \
        "v_mbcnt_hi_u32_b32 v160, s13, v160\n\t" \
        "s_bcnt1_i32_b64 s55, s[12:13]\n\t" \
        "v_lshl_add_u32 v160, v160, 3, s45\n\t" \
        "s_lshl3_add_u32 s45, s55, s45\n\t" \
        "v_cndmask_b32 v160, v66, v160, s[12:13]\n\t" \
        "ds_write2_b32 v160, v48, v58 offset1:1\n\t" \
        "buffer_load_dwordx4 v5, s[24:27], s57 offen offset:2048 lds\n\t" \
        "s_sub_u32 s46, s45, s48\n\t" \
        "s_lshr_b32 s46, s46, 3\n\t" \
        "s_cmp_gt_u32 s46, 64\n\t" \
        "s_cselect_b32 s54, 1, 0\n\t" \
        "s_cselect_b32 s46, 64, s46\n\t" \
        "s_lshl_b32 s55, s35, 2\n\t" \
        "v_mov_b32 v162, s55\n\t" \
        "s_cmp_eq_u32 s46, 0\n\t" \
        "s_cbranch_scc1 .LT_none%=\n\t" \
        "s_sub_u32 s55, s46, 1\n\t" \
        "v_min_u32 v68, s55, v2\n\t" \
        "v_lshl_add_u32 v85, v68, 3, s48\n\t" \
        "s_waitcnt lgkmcnt(0)\n\t" \
        "ds_read_b64 v[70:71], v85\n\t" \
        "v_mul_i32_i24 v69, s53, v68\n\t" \
        "v_lshlrev_b32 v85, 3, v68\n\t" \
        "v_lshlrev_b32 v86, 2, v68\n\t" \
        "v_add_u32 v69, s49, v69\n\t" \
        "v_and_b32 v85, 16, v85\n\t" \
        "v_and_b32 v86, 32, v86\n\t" \
        "v_mov_b32 v87, 124\n\t" \
        "v_or3_b32 v69, v69, v85, v86\n\t" \
        "v_cvt_f32_u32 v76, s32\n\t" \
        "v_cvt_f32_u32 v77, s33\n\t" \
        "v_add_f32 v80, -2.0, v76\n\t" \
        "v_add_f32 v81, -2.0, v77\n\t" \
        "v_cvt_f32_u32 v82, 17\n\t" \
        "s_waitcnt lgkmcnt(0)\n\t" \
        "v_lshlrev_b32 v72, 16, v70\n\t" \
        "v_and_b32 v73, 0xffff0000, v70\n\t" \
        "v_cvt_f32_ubyte2 v76, v71\n\t" \
        "v_cvt_f32_ubyte3 v77, v71\n\t" \
        "v_add_f32 v72, v72, v76\n\t" \
        "v_add_f32 v73, v73, v77\n\t" \
        "v_cvt_pk_bf16_f32 v70, v72, v73\n\t" \
        "v_pk_max_i16 v70, v70, 0\n\t" \
        "v_mov_b32 v76, 0x435f435f\n\t" \
        "v_pk_min_i16 v70, v70, v76\n\t" \
        "v_and_b32 v71, 0xffff, v71\n\t" \
        "v_lshlrev_b32 v72, 16, v70\n\t" \
        "v_and_b32 v73, 0xffff0000, v70\n\t" \
        "ds_write_b32 v71, v69\n\t" \
        "v_fract_f32 v74, v72\n\t" \
        "v_fract_f32 v75, v73\n\t" \
        "v_sub_f32 v72, v72, v80\n\t" \
        "v_sub_f32 v73, v73, v81\n\t" \
        "v_sub_f32 v76, 1.0, v74\n\t" \
        "v_sub_f32 v77, 1.0, v75\n\t" \
        "v_med3_f32 v80, v72, 0, v82\n\t" \
        "v_mov_b32 v85, 0x41980000\n\t" \
        "v_cvt_pk_bf16_f32 v76, v76, v77\n\t" \
        "v_med3_f32 v81, v73, 0, v85\n\t" \
        "v_cmp_neq_f32 s[4:5], v80, v72\n\t" \
        "v_and_b32 v77, 0xffff0000, v76\n\t" \
        "v_cmp_neq_f32 s[6:7], v81, v73\n\t" \
        "v_lshlrev_b32 v76, 16, v76\n\t" \
        "v_cvt_u32_f32 v80, v80\n\t" \
        "v_cvt_u32_f32 v81, v81\n\t" \
        "v_mul_f32 v78, v76, v77\n\t" \
        "v_mul_f32 v76, v76, v75\n\t" \
        "v_mul_f32 v79, v74, v77\n\t" \
        "v_mul_f32 v77, v74, v75\n\t" \
        "s_or_b64 s[4:5], s[4:5], s[6:7]\n\t" \
        "v_cvt_pk_bf16_f32 v78, v78, v76\n\t" \
        "v_cvt_pk_bf16_f32 v79, v79, v77\n\t" \
        "s_cselect_b32 s54, 1, s54\n\t" \
        "v_cmp_neq_f32 s[4:5], 0, v75\n\t" \
        "v_cmp_neq_f32 s[6:7], 0, v74\n\t" \
        "v_mul_u32_u24 v82, 124, v80\n\t" \
        "v_add_u32 v85, 1, v81\n\t" \
        "v_mad_u32_u24 v82, v81, 6, v82\n\t" \
        "v_bfe_u32 v86, v81, 2, 1\n\t" \
        "v_bfe_u32 v85, v85, 2, 1\n\t" \
        "s_mov_b32 s55, 0x43a\n\t" \
        "v_add3_u32 v83, v82, v86, s55\n\t" \
        "s_mov_b32 s55, 0x440\n\t" \
        "v_add3_u32 v84, v82, v85, s55\n\t" \
        "s_and_b64 s[50:51], s[4:5], s[6:7]\n\t" \
        "v_cndmask_b32 v85, 0, v87, s[6:7]\n\t" \
        "v_mov_b32 v76, 0x05040100\n\t" \
        "v_add_u32 v85, v83, v85\n\t" \
        "v_perm_b32 v76, v79, v78, v76\n\t" \
        "v_cndmask_b32 v85, v85, v84, s[4:5]\n\t" \
        "v_mov_b32 v77, 0x80008000\n\t" \
        "v_cndmask_b32 v70, v76, v78, s[4:5]\n\t" \
        "v_lshl_or_b32 v83, v85, 16, v83\n\t" \
        "v_cndmask_b32 v77, 0, v77, s[50:51]\n\t" \
        "v_cndmask_b32 v71, 0, v79, s[50:51]\n\t" \
        "v_or_b32 v83, v83, v77\n\t" \
        "v_lshlrev_b32 v85, 2, v2\n\t" \
        "s_add_u32 s55, s48, 512\n\t" \
        "v_lshl_add_u32 v86, v2, 3, s48\n\t" \
        "v_add_u32 v85, s55, v85\n\t" \
        "ds_write_b64 v86, v[70:71]\n\t" \
        "ds_write_b32 v85, v83\n\t" \
        "s_cmp_lg_u32 s54, 0\n\t" \
        "s_cselect_b32 s55, 0x10000, s46\n\t" \
        "v_mov_b32 v163, s55\n\t" \
        "ds_write_b32 v162, v163\n\t" \
        "s_waitcnt lgkmcnt(0)\n\t" \
        "ds_read_u16 v120, v20\n\t" \
        "ds_read_u16 v121, v20 offset:8\n\t" \
        "ds_read_b32 v122, v21\n\t" \
        "v_mov_b32 v156, 0\n\t" \
        "s_mov_b32 s47, 0\n\t" \
        "s_waitcnt lgkmcnt(0)\n\t" \
        "v_lshlrev_b32 v116, 5, v120\n\t" \
        "v_bitop3_b32 v116, v116, s52, v23 bitop3:0x6a\n\t" \
        "v_xor_b32 v117, 8, v116\n\t" \
        "v_lshlrev_b32 v118, 5, v121\n\t" \
        "v_bitop3_b32 v118, v118, s52, v23 bitop3:0x6a\n\t" \
        "v_xor_b32 v119, 8, v118\n\t" \
        "v_and_b32 v112, v122, v25\n\t" \
        "v_and_b32 v113, v122, v26\n\t" \
        "v_and_b32 v114, v122, v27\n\t" \
        "v_and_b32 v115, v122, v28\n\t" \
        "ds_read_u16 v120, v20 offset:64\n\t" \
        "ds_read_u16 v121, v20 offset:72\n\t" \
        "ds_read_b32 v122, v21 offset:128\n\t" \
        "s_waitcnt vmcnt(6) lgkmcnt(0)\n\t" \
        "s_barrier\n\t" \
        "ds_read_b128 v[152:155], v156\n\t" \
        "ds_read_b64_tr_b16 v[64:65], v116 offset:0\n\t" \
        "ds_read_b64_tr_b16 v[66:67], v118 offset:0\n\t" \
        "ds_read_b64_tr_b16 v[68:69], v117 offset:0\n\t" \
        "ds_read_b64_tr_b16 v[70:71], v119 offset:0\n\t" \
        "ds_read_b64_tr_b16 v[72:73], v116 offset:64\n\t" \
        "ds_read_b64_tr_b16 v[74:75], v118 offset:64\n\t" \
        "ds_read_b64_tr_b16 v[76:77], v117 offset:64\n\t" \
        "ds_read_b64_tr_b16 v[78:79], v119 offset:64\n\t" \
        "ds_read_b64_tr_b16 v[80:81], v116 offset:128\n\t" \
        "ds_read_b64_tr_b16 v[82:83], v118 offset:128\n\t" \
        "ds_read_b64_tr_b16 v[84:85], v117 offset:128\n\t" \
        "ds_read_b64_tr_b16 v[86:87], v119 offset:128\n\t" \
        "s_add_u32 s57, s40, 0x6000\n\t" \
        "s_add_u32 m0, s39, 0x9000\n\t" \
        "s_nop 0\n\t" \
        "buffer_load_dwordx4 v3, s[24:27], s57 offen offset:0 lds\n\t" \
        "buffer_load_dwordx4 v4, s[24:27], s57 offen offset:1024 lds\n\t" \
        "buffer_load_dwordx4 v5, s[24:27], s57 offen offset:2048 lds\n\t" \
        "s_waitcnt lgkmcnt(12)\n\t" \
        "v_add_u32 v152, v152, v154\n\t" \
        "v_add_u32 v153, v153, v155\n\t" \
        "v_max_u32 v152, v152, v153\n\t" \
        "s_nop 0\n\t" \
        "v_readfirstlane_b32 s55, v152\n\t" \
        "s_nop 0\n\t" \
        "s_cmp_gt_u32 s55, 66\n\t" \
        "s_cbranch_scc1 .Lfail%=\n\t" \
        ".LTeprod%=:\n\t" \
        "s_add_u32 s55, s47, 16\n\t" \
        "s_cmp_ge_i32 s55, s46\n\t" \
        "s_cbranch_scc1 .LTelast%=\n\t" \
        "s_waitcnt lgkmcnt(10)\n\t" \
        "v_mfma_f32_16x16x32_bf16 v[88:91], v[64:67], v[112:115], 0\n\t" \
        "v_lshlrev_b32 v116, 5, v120\n\t" \
        "v_bitop3_b32 v116, v116, s52, v23 bitop3:0x6a\n\t" \
        "v_xor_b32 v117, 8, v116\n\t" \
        "v_lshlrev_b32 v118, 5, v121\n\t" \
        "v_bitop3_b32 v118, v118, s52, v23 bitop3:0x6a\n\t" \
        "v_xor_b32 v119, 8, v118\n\t" \
        "ds_read_u16 v120, v20 offset:128\n\t" \
        "ds_read_u16 v121, v20 offset:136\n\t" \
        "ds_read_b64_tr_b16 v[64:65], v116 offset:0\n\t" \
        "ds_read_b64_tr_b16 v[66:67], v118 offset:0\n\t" \
        "s_waitcnt lgkmcnt(12)\n\t" \
        "v_mfma_f32_16x16x32_bf16 v[92:95], v[68:71], v[112:115], 0\n\t" \
        "ds_read_b64_tr_b16 v[68:69], v117 offset:0\n\t" \
        "ds_read_b64_tr_b16 v[70:71], v119 offset:0\n\t" \
        "s_waitcnt lgkmcnt(12)\n\t" \
        "v_mfma_f32_16x16x32_bf16 v[96:99], v[72:75], v[112:115], 0\n\t" \
        "ds_read_b64_tr_b16 v[72:73], v116 offset:64\n\t" \
        "ds_read_b64_tr_b16 v[74:75], v118 offset:64\n\t" \
        "s_waitcnt lgkmcnt(12)\n\t" \
        "v_mfma_f32_16x16x32_bf16 v[100:103], v[76:79], v[112:115], 0\n\t" \
        "ds_read_b64_tr_b16 v[76:77], v117 offset:64\n\t" \
        "ds_read_b64_tr_b16 v[78:79], v119 offset:64\n\t" \
        "s_waitcnt lgkmcnt(12)\n\t" \
        "v_mfma_f32_16x16x32_bf16 v[104:107], v[80:83], v[112:115], 0\n\t" \
        "ds_read_b64_tr_b16 v[80:81], v116 offset:128\n\t" \
        "ds_read_b64_tr_b16 v[82:83], v118 offset:128\n\t" \
        "s_waitcnt lgkmcnt(12)\n\t" \
        "v_mfma_f32_16x16x32_bf16 v[108:111], v[84:87], v[112:115], 0\n\t" \
        "ds_read_b64_tr_b16 v[84:85], v117 offset:128\n\t" \
        "ds_read_b64_tr_b16 v[86:87], v119 offset:128\n\t" \
        "v_and_b32 v112, v122, v25\n\t" \
        "v_and_b32 v113, v122, v26\n\t" \
        "v_and_b32 v114, v122, v27\n\t" \
        "v_and_b32 v115, v122, v28\n\t" \
        "ds_read_b32 v122, v21 offset:256\n\t" \
        "s_and_b32 s55, s50, 0xffff\n\t" \
        "s_cbranch_scc1 .LTetwo%=\n\t" \
        ".LTeback%=:\n\t" \
        "s_sub_u32 s55, s46, s47\n\t" \
        "v_xor_b32 v156, 8, v22\n\t" \
        "v_cmp_gt_u32 vcc, s55, v24\n\t" \
        "s_mov_b64 exec, vcc\n\t" \
        "v_cvt_pk_bf16_f32 v88, v88, v89\n\t" \
        "v_cvt_pk_bf16_f32 v89, v90, v91\n\t" \
        "ds_write_b64 v22, v[88:89] offset:0\n\t" \
        "v_cvt_pk_bf16_f32 v92, v92, v93\n\t" \
        "v_cvt_pk_bf16_f32 v93, v94, v95\n\t" \
        "ds_write_b64 v156, v[92:93] offset:0\n\t" \
        "v_cvt_pk_bf16_f32 v96, v96, v97\n\t" \
        "v_cvt_pk_bf16_f32 v97, v98, v99\n\t" \
        "ds_write_b64 v22, v[96:97] offset:64\n\t" \
        "v_cvt_pk_bf16_f32 v100, v100, v101\n\t" \
        "v_cvt_pk_bf16_f32 v101, v102, v103\n\t" \
        "ds_write_b64 v156, v[100:101] offset:64\n\t" \
        "v_cvt_pk_bf16_f32 v104, v104, v105\n\t" \
        "v_cvt_pk_bf16_f32 v105, v106, v107\n\t" \
        "ds_write_b64 v22, v[104:105] offset:128\n\t" \
        "v_cvt_pk_bf16_f32 v108, v108, v109\n\t" \
        "v_cvt_pk_bf16_f32 v109, v110, v111\n\t" \
        "ds_write_b64 v156, v[108:109] offset:128\n\t" \
        "s_mov_b64 exec, -1\n\t" \
        "s_lshl_b32 s55, s53, 4\n\t" \
        "s_add_u32 s47, s47, 16\n\t" \
        "v_add_u32 v22, s55, v22\n\t" \
        "s_lshr_b64 s[50:51], s[50:51], 16\n\t" \
        "v_add_u32 v20, 64, v20\n\t" \
        "v_add_u32 v21, 0x80, v21\n\t" \
        "s_cmp_lt_i32 s47, s46\n\t" \
        "s_cbranch_scc1 .LTeprod%=\n\t" \
        "s_branch .LTeend%=\n\t" \
        ".LTelast%=:\n\t" \
        "s_waitcnt lgkmcnt(10)\n\t" \
        "v_mfma_f32_16x16x32_bf16 v[88:91], v[64:67], v[112:115], 0\n\t" \
        "s_waitcnt lgkmcnt(8)\n\t" \
        "v_mfma_f32_16x16x32_bf16 v[92:95], v[68:71], v[112:115], 0\n\t" \
        "s_waitcnt lgkmcnt(6)\n\t" \
        "v_mfma_f32_16x16x32_bf16 v[96:99], v[72:75], v[112:115], 0\n\t" \
        "s_waitcnt lgkmcnt(4)\n\t" \
        "v_mfma_f32_16x16x32_bf16 v[100:103], v[76:79], v[112:115], 0\n\t" \
        "s_waitcnt lgkmcnt(2)\n\t" \
        "v_mfma_f32_16x16x32_bf16 v[104:107], v[80:83], v[112:115], 0\n\t" \
        "s_waitcnt lgkmcnt(0)\n\t" \
        "v_mfma_f32_16x16x32_bf16 v[108:111], v[84:87], v[112:115], 0\n\t" \
        "s_and_b32 s55, s50, 0xffff\n\t" \
        "s_cbranch_scc1 .LTetwo%=\n\t" \
        "s_nop 1\n\t" \
        "s_branch .LTeback%=\n\t" \
        ".LTetwo%=:\n\t" \
        "ds_read_u16 v156, v20\n\t" \
        "ds_read_u16 v158, v20 offset:8\n\t" \
        "ds_read_b32 v127, v21 offset:4\n\t" \
        "s_waitcnt lgkmcnt(0)\n\t" \
        "v_lshrrev_b32 v124, 15, v156\n\t" \
        "v_lshlrev_b32 v156, 5, v156\n\t" \
        "v_bitop3_b32 v156, v156, s52, v23 bitop3:0x6a\n\t" \
        "v_mul_u32_u24 v124, 3968, v124\n\t" \
        "v_add_u32 v156, v156, v124\n\t" \
        "v_xor_b32 v157, 8, v156\n\t" \
        "v_lshrrev_b32 v125, 15, v158\n\t" \
        "v_lshlrev_b32 v158, 5, v158\n\t" \
        "v_bitop3_b32 v158, v158, s52, v23 bitop3:0x6a\n\t" \
        "v_mul_u32_u24 v125, 3968, v125\n\t" \
        "v_add_u32 v158, v158, v125\n\t" \
        "v_xor_b32 v159, 8, v158\n\t" \
        "ds_read_b64_tr_b16 v[128:129], v156 offset:0\n\t" \
        "ds_read_b64_tr_b16 v[130:131], v158 offset:0\n\t" \
        "ds_read_b64_tr_b16 v[132:133], v157 offset:0\n\t" \
        "ds_read_b64_tr_b16 v[134:135], v159 offset:0\n\t" \
        "ds_read_b64_tr_b16 v[136:137], v156 offset:64\n\t" \
        "ds_read_b64_tr_b16 v[138:139], v158 offset:64\n\t" \
        "ds_read_b64_tr_b16 v[140:141], v157 offset:64\n\t" \
        "ds_read_b64_tr_b16 v[142:143], v159 offset:64\n\t" \
        "ds_read_b64_tr_b16 v[144:145], v156 offset:128\n\t" \
        "ds_read_b64_tr_b16 v[146:147], v158 offset:128\n\t" \
        "ds_read_b64_tr_b16 v[148:149], v157 offset:128\n\t" \
        "ds_read_b64_tr_b16 v[150:151], v159 offset:128\n\t" \
        "v_and_b32 v124, v127, v25\n\t" \
        "v_and_b32 v125, v127, v26\n\t" \
        "v_and_b32 v126, v127, v27\n\t" \
        "v_and_b32 v127, v127, v28\n\t" \
        "s_nop 0\n\t" \
        "s_waitcnt lgkmcnt(10)\n\t" \
        "v_mfma_f32_16x16x32_bf16 v[88:91], v[128:131], v[124:127], v[88:91]\n\t" \
        "s_waitcnt lgkmcnt(8)\n\t" \
        "v_mfma_f32_16x16x32_bf16 v[92:95], v[132:135], v[124:127], v[92:95]\n\t" \
        "s_waitcnt lgkmcnt(6)\n\t" \
        "v_mfma_f32_16x16x32_bf16 v[96:99], v[136:139], v[124:127], v[96:99]\n\t" \
        "s_waitcnt lgkmcnt(4)\n\t" \
        "v_mfma_f32_16x16x32_bf16 v[100:103], v[140:143], v[124:127], v[100:103]\n\t" \
        "s_waitcnt lgkmcnt(2)\n\t" \
        "v_mfma_f32_16x16x32_bf16 v[104:107], v[144:147], v[124:127], v[104:107]\n\t" \
        "s_waitcnt lgkmcnt(0)\n\t" \
        "v_mfma_f32_16x16x32_bf16 v[108:111], v[148:151], v[124:127], v[108:111]\n\t" \
        "s_nop 7\n\t" \
        "s_branch .LTeback%=\n\t" \
        ".LTeend%=:\n\t" \
        "s_branch .LT_join%=\n\t" \
        ".LT_none%=:\n\t" \
        "v_mov_b32 v163, 0\n\t" \
        "ds_write_b32 v162, v163\n\t" \
        "s_waitcnt vmcnt(6) lgkmcnt(0)\n\t" \
        "s_barrier\n\t" \
        "ds_read_b128 v[152:155], v163\n\t" \
        "s_add_u32 s57, s40, 0x6000\n\t" \
        "s_add_u32 m0, s39, 0x9000\n\t" \
        "s_nop 0\n\t" \
        "buffer_load_dwordx4 v3, s[24:27], s57 offen offset:0 lds\n\t" \
        "buffer_load_dwordx4 v4, s[24:27], s57 offen offset:1024 lds\n\t" \
        "buffer_load_dwordx4 v5, s[24:27], s57 offen offset:2048 lds\n\t" \
        "s_waitcnt lgkmcnt(0)\n\t" \
        "v_add_u32 v152, v152, v154\n\t" \
        "v_add_u32 v153, v153, v155\n\t" \
        "v_max_u32 v152, v152, v153\n\t" \
        "s_nop 0\n\t" \
        "v_readfirstlane_b32 s55, v152\n\t" \
        "s_nop 0\n\t" \
        "s_cmp_gt_u32 s55, 66\n\t" \
        "s_cbranch_scc1 .Lfail%=\n\t" \
        ".LT_join%=:\n\t" \
        "s_waitcnt vmcnt(6) lgkmcnt(0)\n\t" \
        "s_barrier\n\t" \
        "ds_read_b128 v[148:151], v15 offset:0\n\t" \
        "s_waitcnt lgkmcnt(0)\n\t" \
        "v_xor_b32 v148, v148, v14\n\t" \
        "ds_read_b128 v[116:119], v148 offset:0\n\t" \
        "ds_read_b64_tr_b16 a[0:1], v6 offset:12288\n\t" \
        "ds_read_b64_tr_b16 a[2:3], v6 offset:13056\n\t" \
        "v_xor_b32 v149, v149, v14\n\t" \
        "ds_read_b64_tr_b16 a[4:5], v7 offset:12288\n\t" \
        "ds_read_b64_tr_b16 a[6:7], v7 offset:13056\n\t" \
        "ds_read_b64_tr_b16 a[8:9], v8 offset:12288\n\t" \
        "ds_read_b64_tr_b16 a[10:11], v8 offset:13056\n\t" \
        "ds_read_b128 v[120:123], v149 offset:0\n\t" \
        "v_xor_b32 v150, v150, v14\n\t" \
        "ds_read_b128 v[124:127], v150 offset:0\n\t" \
        "v_xor_b32 v151, v151, v14\n\t" \
        "ds_read_b128 v[128:131], v151 offset:0\n\t" \
        "s_add_u32 s57, s40, 0x9000\n\t" \
        "s_add_u32 m0, s39, 0xc000\n\t" \
        "s_waitcnt lgkmcnt(7)\n\t" \
        "v_mfma_f32_16x16x32_bf16 v[32:35], a[0:3], v[116:119], 0\n\t" \
        "buffer_load_dwordx4 v3, s[24:27], s57 offen offset:0 lds\n\t" \
        "s_waitcnt lgkmcnt(5)\n\t" \
        "v_mfma_f32_16x16x32_bf16 v[36:39], a[4:7], v[116:119], 0\n\t" \
        "ds_read_b128 v[132:135], v148 offset:64\n\t" \
        "s_waitcnt lgkmcnt(4)\n\t" \
        "v_mfma_f32_16x16x32_bf16 v[40:43], a[8:11], v[116:119], 0\n\t" \
        "ds_read_b64_tr_b16 a[12:13], v6 offset:18432\n\t" \
        "s_waitcnt lgkmcnt(4)\n\t" \
        "v_mfma_f32_16x16x32_bf16 v[44:47], a[0:3], v[120:123], 0\n\t" \
        "ds_read_b64_tr_b16 a[14:15], v6 offset:19200\n\t" \
        "v_mfma_f32_16x16x32_bf16 v[48:51], a[4:7], v[120:123], 0\n\t" \
        "buffer_load_dwordx4 v4, s[24:27], s57 offen offset:1024 lds\n\t" \
        "v_mfma_f32_16x16x32_bf16 v[52:55], a[8:11], v[120:123], 0\n\t" \
        "ds_read_b64_tr_b16 a[16:17], v7 offset:18432\n\t" \
        "s_waitcnt lgkmcnt(5)\n\t" \
        "v_mfma_f32_16x16x32_bf16 v[56:59], a[0:3], v[124:127], 0\n\t" \
        "ds_read_b64_tr_b16 a[18:19], v7 offset:19200\n\t" \
        "ds_read_b64_tr_b16 a[20:21], v8 offset:18432\n\t" \
        "v_mfma_f32_16x16x32_bf16 v[60:63], a[4:7], v[124:127], 0\n\t" \
        "ds_read_b64_tr_b16 a[22:23], v8 offset:19200\n\t" \
        "v_mfma_f32_16x16x32_bf16 v[64:67], a[8:11], v[124:127], 0\n\t" \
        "ds_read_b128 v[136:139], v149 offset:64\n\t" \
        "s_waitcnt lgkmcnt(8)\n\t" \
        "v_mfma_f32_16x16x32_bf16 v[68:71], a[0:3], v[128:131], 0\n\t" \
        "buffer_load_dwordx4 v5, s[24:27], s57 offen offset:2048 lds\n\t" \
        "v_mfma_f32_16x16x32_bf16 v[72:75], a[4:7], v[128:131], 0\n\t" \
        "ds_read_b128 v[140:143], v150 offset:64\n\t" \
        "s_add_u32 s57, s40, 0xc000\n\t" \
        "s_mov_b32 m0, s39\n\t" \
        "v_mfma_f32_16x16x32_bf16 v[76:79], a[8:11], v[128:131], 0\n\t" \
        "s_waitcnt vmcnt(6) lgkmcnt(2)\n\t" \
        "s_barrier\n\t" \
        "ds_read_b128 v[144:147], v151 offset:64\n\t" \
        "v_mfma_f32_16x16x32_bf16 v[32:35], a[12:15], v[132:135], v[32:35]\n\t" \
        "buffer_load_dwordx4 v3, s[24:27], s57 offen offset:0 lds\n\t" \
        "v_mfma_f32_16x16x32_bf16 v[36:39], a[16:19], v[132:135], v[36:39]\n\t" \
        "ds_read_b128 v[116:119], v148 offset:128\n\t" \
        "v_mfma_f32_16x16x32_bf16 v[40:43], a[20:23], v[132:135], v[40:43]\n\t" \
        "ds_read_b128 v[152:155], v15 offset:256\n\t" \
        "s_waitcnt lgkmcnt(4)\n\t" \
        "v_mfma_f32_16x16x32_bf16 v[44:47], a[12:15], v[136:139], v[44:47]\n\t" \
        "ds_read_b64_tr_b16 a[24:25], v6 offset:24576\n\t" \
        "v_mfma_f32_16x16x32_bf16 v[48:51], a[16:19], v[136:139], v[48:51]\n\t" \
        "buffer_load_dwordx4 v4, s[24:27], s57 offen offset:1024 lds\n\t" \
        "v_mfma_f32_16x16x32_bf16 v[52:55], a[20:23], v[136:139], v[52:55]\n\t" \
        "ds_read_b64_tr_b16 a[26:27], v6 offset:25344\n\t" \
        "ds_read_b64_tr_b16 a[28:29], v7 offset:24576\n\t" \
        "s_waitcnt lgkmcnt(6)\n\t" \
        "v_mfma_f32_16x16x32_bf16 v[56:59], a[12:15], v[140:143], v[56:59]\n\t" \
        "ds_read_b64_tr_b16 a[30:31], v7 offset:25344\n\t" \
        "ds_read_b64_tr_b16 a[32:33], v8 offset:24576\n\t" \
        "v_mfma_f32_16x16x32_bf16 v[60:63], a[16:19], v[140:143], v[60:63]\n\t" \
        "ds_read_b64_tr_b16 a[34:35], v8 offset:25344\n\t" \
        "v_mfma_f32_16x16x32_bf16 v[64:67], a[20:23], v[140:143], v[64:67]\n\t" \
        "ds_read_b128 v[120:123], v149 offset:128\n\t" \
        "s_waitcnt lgkmcnt(7)\n\t" \
        "v_xor_b32 v152, v152, v14\n\t" \
        "v_mfma_f32_16x16x32_bf16 v[68:71], a[12:15], v[144:147], v[68:71]\n\t" \
        "buffer_load_dwordx4 v5, s[24:27], s57 offen offset:2048 lds\n\t" \
        "v_mfma_f32_16x16x32_bf16 v[72:75], a[16:19], v[144:147], v[72:75]\n\t" \
        "ds_read_b128 v[124:127], v150 offset:128\n\t" \
        "s_add_u32 s57, s40, 0xf000\n\t" \
        "s_add_u32 m0, s39, 0x3000\n\t" \
        "v_xor_b32 v153, v153, v14\n\t" \
        "v_mfma_f32_16x16x32_bf16 v[76:79], a[20:23], v[144:147], v[76:79]\n\t" \
        "ds_read_b128 v[128:131], v151 offset:128\n\t" \
        "v_xor_b32 v154, v154, v14\n\t" \
        "s_waitcnt lgkmcnt(7)\n\t" \
        "v_mfma_f32_16x16x32_bf16 v[32:35], a[24:27], v[116:119], v[32:35]\n\t" \
        "buffer_load_dwordx4 v3, s[24:27], s57 offen offset:0 lds\n\t" \
        "s_waitcnt lgkmcnt(5)\n\t" \
        "v_mfma_f32_16x16x32_bf16 v[36:39], a[28:31], v[116:119], v[36:39]\n\t" \
        "ds_read_b128 v[132:135], v152 offset:0\n\t" \
        "v_xor_b32 v155, v155, v14\n\t" \
        "s_waitcnt lgkmcnt(4)\n\t" \
        "v_mfma_f32_16x16x32_bf16 v[40:43], a[32:35], v[116:119], v[40:43]\n\t" \
        "ds_read_b64_tr_b16 a[36:37], v6 offset:30720\n\t" \
        "s_waitcnt lgkmcnt(4)\n\t" \
        "v_mfma_f32_16x16x32_bf16 v[44:47], a[24:27], v[120:123], v[44:47]\n\t" \
        "ds_read_b64_tr_b16 a[38:39], v6 offset:31488\n\t" \
        "v_mfma_f32_16x16x32_bf16 v[48:51], a[28:31], v[120:123], v[48:51]\n\t" \
        "buffer_load_dwordx4 v4, s[24:27], s57 offen offset:1024 lds\n\t" \
        "v_mfma_f32_16x16x32_bf16 v[52:55], a[32:35], v[120:123], v[52:55]\n\t" \
        "ds_read_b64_tr_b16 a[40:41], v7 offset:30720\n\t" \
        "s_waitcnt lgkmcnt(5)\n\t" \
        "v_mfma_f32_16x16x32_bf16 v[56:59], a[24:27], v[124:127], v[56:59]\n\t" \
        "ds_read_b64_tr_b16 a[42:43], v7 offset:31488\n\t" \
        "ds_read_b64_tr_b16 a[44:45], v8 offset:30720\n\t" \
        "v_mfma_f32_16x16x32_bf16 v[60:63], a[28:31], v[124:127], v[60:63]\n\t" \
        "ds_read_b64_tr_b16 a[46:47], v8 offset:31488\n\t" \
        "v_mfma_f32_16x16x32_bf16 v[64:67], a[32:35], v[124:127], v[64:67]\n\t" \
        "ds_read_b128 v[136:139], v153 offset:0\n\t" \
        "s_waitcnt lgkmcnt(8)\n\t" \
        "v_mfma_f32_16x16x32_bf16 v[68:71], a[24:27], v[128:131], v[68:71]\n\t" \
        "buffer_load_dwordx4 v5, s[24:27], s57 offen offset:2048 lds\n\t" \
        "v_mfma_f32_16x16x32_bf16 v[72:75], a[28:31], v[128:131], v[72:75]\n\t" \
        "ds_read_b128 v[140:143], v154 offset:0\n\t" \
        "v_mfma_f32_16x16x32_bf16 v[76:79], a[32:35], v[128:131], v[76:79]\n\t" \
        "s_waitcnt vmcnt(9) lgkmcnt(2)\n\t" \
        "s_barrier\n\t" \
        "ds_read_b128 v[144:147], v155 offset:0\n\t" \
        "s_add_u32 s57, s40, 0x12000\n\t" \
        "s_add_u32 m0, s39, 0x6000\n\t" \
        "v_mfma_f32_16x16x32_bf16 v[32:35], a[36:39], v[132:135], v[32:35]\n\t" \
        "ds_read_b128 v[116:119], v152 offset:64\n\t" \
        "v_mfma_f32_16x16x32_bf16 v[36:39], a[40:43], v[132:135], v[36:39]\n\t" \
        "buffer_load_dwordx4 v3, s[24:27], s57 offen offset:0 lds\n\t" \
        "v_mfma_f32_16x16x32_bf16 v[40:43], a[44:47], v[132:135], v[40:43]\n\t" \
        "ds_read_b64_tr_b16 a[48:49], v6 offset:36864\n\t" \
        "s_waitcnt lgkmcnt(4)\n\t" \
        "v_mfma_f32_16x16x32_bf16 v[44:47], a[36:39], v[136:139], v[44:47]\n\t" \
        "ds_read_b64_tr_b16 a[50:51], v6 offset:37632\n\t" \
        "v_mfma_f32_16x16x32_bf16 v[48:51], a[40:43], v[136:139], v[48:51]\n\t" \
        "ds_read_b64_tr_b16 a[52:53], v7 offset:36864\n\t" \
        "v_mfma_f32_16x16x32_bf16 v[52:55], a[44:47], v[136:139], v[52:55]\n\t" \
        "buffer_load_dwordx4 v4, s[24:27], s57 offen offset:1024 lds\n\t" \
        "s_waitcnt lgkmcnt(5)\n\t" \
        "v_mfma_f32_16x16x32_bf16 v[56:59], a[36:39], v[140:143], v[56:59]\n\t" \
        "ds_read_b64_tr_b16 a[54:55], v7 offset:37632\n\t" \
        "ds_read_b64_tr_b16 a[56:57], v8 offset:36864\n\t" \
        "v_mfma_f32_16x16x32_bf16 v[60:63], a[40:43], v[140:143], v[60:63]\n\t" \
        "ds_read_b64_tr_b16 a[58:59], v8 offset:37632\n\t" \
        "v_mfma_f32_16x16x32_bf16 v[64:67], a[44:47], v[140:143], v[64:67]\n\t" \
        "ds_read_b128 v[120:123], v153 offset:64\n\t" \
        "s_waitcnt lgkmcnt(8)\n\t" \
        "v_mfma_f32_16x16x32_bf16 v[68:71], a[36:39], v[144:147], v[68:71]\n\t" \
        "buffer_load_dwordx4 v5, s[24:27], s57 offen offset:2048 lds\n\t" \
        "v_mfma_f32_16x16x32_bf16 v[72:75], a[40:43], v[144:147], v[72:75]\n\t" \
        "ds_read_b128 v[124:127], v154 offset:64\n\t" \
        "v_mfma_f32_16x16x32_bf16 v[76:79], a[44:47], v[144:147], v[76:79]\n\t" \
        "ds_read_b128 v[128:131], v155 offset:64\n\t" \
        "s_waitcnt lgkmcnt(7)\n\t" \
        "v_mfma_f32_16x16x32_bf16 v[32:35], a[48:51], v[116:119], v[32:35]\n\t" \
        "ds_read_b128 v[132:135], v152 offset:128\n\t" \
        "s_waitcnt lgkmcnt(6)\n\t" \
        "v_mfma_f32_16x16x32_bf16 v[36:39], a[52:55], v[116:119], v[36:39]\n\t" \
        "ds_read_b128 v[156:159], v15 offset:512\n\t" \
        "s_waitcnt lgkmcnt(5)\n\t" \
        "v_mfma_f32_16x16x32_bf16 v[40:43], a[56:59], v[116:119], v[40:43]\n\t" \
        "ds_read_b64_tr_b16 a[60:61], v6 offset:43008\n\t" \
        "s_waitcnt lgkmcnt(5)\n\t" \
        "v_mfma_f32_16x16x32_bf16 v[44:47], a[48:51], v[120:123], v[44:47]\n\t" \
        "ds_read_b64_tr_b16 a[62:63], v6 offset:43776\n\t" \
        "v_mfma_f32_16x16x32_bf16 v[48:51], a[52:55], v[120:123], v[48:51]\n\t" \
        "ds_read_b64_tr_b16 a[64:65], v7 offset:43008\n\t" \
        "v_mfma_f32_16x16x32_bf16 v[52:55], a[56:59], v[120:123], v[52:55]\n\t" \
        "ds_read_b64_tr_b16 a[66:67], v7 offset:43776\n\t" \
        "s_waitcnt lgkmcnt(7)\n\t" \
        "v_mfma_f32_16x16x32_bf16 v[56:59], a[48:51], v[124:127], v[56:59]\n\t" \
        "ds_read_b64_tr_b16 a[68:69], v8 offset:43008\n\t" \
        "v_mfma_f32_16x16x32_bf16 v[60:63], a[52:55], v[124:127], v[60:63]\n\t" \
        "ds_read_b64_tr_b16 a[70:71], v8 offset:43776\n\t" \
        "s_waitcnt lgkmcnt(6)\n\t" \
        "v_xor_b32 v156, v156, v14\n\t" \
        "v_mfma_f32_16x16x32_bf16 v[64:67], a[56:59], v[124:127], v[64:67]\n\t" \
        "ds_read_b128 v[136:139], v153 offset:128\n\t" \
        "v_xor_b32 v157, v157, v14\n\t" \
        "v_mfma_f32_16x16x32_bf16 v[68:71], a[48:51], v[128:131], v[68:71]\n\t" \
        "ds_read_b128 v[140:143], v154 offset:128\n\t" \
        "v_xor_b32 v158, v158, v14\n\t" \
        "v_mfma_f32_16x16x32_bf16 v[72:75], a[52:55], v[128:131], v[72:75]\n\t" \
        "ds_read_b128 v[144:147], v155 offset:128\n\t" \
        "v_xor_b32 v159, v159, v14\n\t" \
        "v_mfma_f32_16x16x32_bf16 v[76:79], a[56:59], v[128:131], v[76:79]\n\t" \
        "s_waitcnt vmcnt(9) lgkmcnt(3)\n\t" \
        "s_barrier\n\t" \
        "ds_read_b128 v[116:119], v156 offset:0\n\t" \
        "s_add_u32 s57, s40, 0x15000\n\t" \
        "s_add_u32 m0, s39, 0x9000\n\t" \
        "v_mfma_f32_16x16x32_bf16 v[32:35], a[60:63], v[132:135], v[32:35]\n\t" \
        "ds_read_b64_tr_b16 a[72:73], v6 offset:49152\n\t" \
        "v_mfma_f32_16x16x32_bf16 v[36:39], a[64:67], v[132:135], v[36:39]\n\t" \
        "buffer_load_dwordx4 v3, s[24:27], s57 offen offset:0 lds\n\t" \
        "v_mfma_f32_16x16x32_bf16 v[40:43], a[68:71], v[132:135], v[40:43]\n\t" \
        "ds_read_b64_tr_b16 a[74:75], v6 offset:49920\n\t" \
        "s_waitcnt lgkmcnt(5)\n\t" \
        "v_mfma_f32_16x16x32_bf16 v[44:47], a[60:63], v[136:139], v[44:47]\n\t" \
        "ds_read_b64_tr_b16 a[76:77], v7 offset:49152\n\t" \
        "v_mfma_f32_16x16x32_bf16 v[48:51], a[64:67], v[136:139], v[48:51]\n\t" \
        "ds_read_b64_tr_b16 a[78:79], v7 offset:49920\n\t" \
        "v_mfma_f32_16x16x32_bf16 v[52:55], a[68:71], v[136:139], v[52:55]\n\t" \
        "ds_read_b64_tr_b16 a[80:81], v8 offset:49152\n\t" \
        "s_waitcnt lgkmcnt(7)\n\t" \
        "v_mfma_f32_16x16x32_bf16 v[56:59], a[60:63], v[140:143], v[56:59]\n\t" \
        "ds_read_b64_tr_b16 a[82:83], v8 offset:49920\n\t" \
        "v_mfma_f32_16x16x32_bf16 v[60:63], a[64:67], v[140:143], v[60:63]\n\t" \
        "buffer_load_dwordx4 v4, s[24:27], s57 offen offset:1024 lds\n\t" \
        "v_mfma_f32_16x16x32_bf16 v[64:67], a[68:71], v[140:143], v[64:67]\n\t" \
        "ds_read_b128 v[120:123], v157 offset:0\n\t" \
        "s_waitcnt lgkmcnt(8)\n\t" \
        "v_mfma_f32_16x16x32_bf16 v[68:71], a[60:63], v[144:147], v[68:71]\n\t" \
        "ds_read_b128 v[124:127], v158 offset:0\n\t" \
        "v_mfma_f32_16x16x32_bf16 v[72:75], a[64:67], v[144:147], v[72:75]\n\t" \
        "ds_read_b128 v[128:131], v159 offset:0\n\t" \
        "v_mfma_f32_16x16x32_bf16 v[76:79], a[68:71], v[144:147], v[76:79]\n\t" \
        "ds_read_b128 v[132:135], v156 offset:64\n\t" \
        "s_waitcnt lgkmcnt(8)\n\t" \
        "v_mfma_f32_16x16x32_bf16 v[32:35], a[72:75], v[116:119], v[32:35]\n\t" \
        "ds_read_b64_tr_b16 a[84:85], v6 offset:55296\n\t" \
        "s_waitcnt lgkmcnt(7)\n\t" \
        "v_mfma_f32_16x16x32_bf16 v[36:39], a[76:79], v[116:119], v[36:39]\n\t" \
        "buffer_load_dwordx4 v5, s[24:27], s57 offen offset:2048 lds\n\t" \
        "s_waitcnt lgkmcnt(5)\n\t" \
        "v_mfma_f32_16x16x32_bf16 v[40:43], a[80:83], v[116:119], v[40:43]\n\t" \
        "ds_read_b64_tr_b16 a[86:87], v6 offset:56064\n\t" \
        "s_waitcnt lgkmcnt(5)\n\t" \
        "v_mfma_f32_16x16x32_bf16 v[44:47], a[72:75], v[120:123], v[44:47]\n\t" \
        "ds_read_b64_tr_b16 a[88:89], v7 offset:55296\n\t" \
        "v_mfma_f32_16x16x32_bf16 v[48:51], a[76:79], v[120:123], v[48:51]\n\t" \
        "ds_read_b64_tr_b16 a[90:91], v7 offset:56064\n\t" \
        "v_mfma_f32_16x16x32_bf16 v[52:55], a[80:83], v[120:123], v[52:55]\n\t" \
        "ds_read_b64_tr_b16 a[92:93], v8 offset:55296\n\t" \
        "s_waitcnt lgkmcnt(7)\n\t" \
        "v_mfma_f32_16x16x32_bf16 v[56:59], a[72:75], v[124:127], v[56:59]\n\t" \
        "ds_read_b64_tr_b16 a[94:95], v8 offset:56064\n\t" \
        "v_mfma_f32_16x16x32_bf16 v[60:63], a[76:79], v[124:127], v[60:63]\n\t" \
        "ds_read_b128 v[136:139], v157 offset:64\n\t" \
        "v_mfma_f32_16x16x32_bf16 v[64:67], a[80:83], v[124:127], v[64:67]\n\t" \
        "ds_read_b128 v[140:143], v158 offset:64\n\t" \
        "s_waitcnt lgkmcnt(9)\n\t" \
        "v_mfma_f32_16x16x32_bf16 v[68:71], a[72:75], v[128:131], v[68:71]\n\t" \
        "ds_read_b128 v[144:147], v159 offset:64\n\t" \
        "v_mfma_f32_16x16x32_bf16 v[72:75], a[76:79], v[128:131], v[72:75]\n\t" \
        "ds_read_b128 v[116:119], v156 offset:128\n\t" \
        "v_mfma_f32_16x16x32_bf16 v[76:79], a[80:83], v[128:131], v[76:79]\n\t" \
        "s_waitcnt vmcnt(9) lgkmcnt(4)\n\t" \
        "s_barrier\n\t" \
        "ds_read_b128 v[160:163], v15 offset:768\n\t" \
        "s_add_u32 s57, s40, 0x18000\n\t" \
        "s_add_u32 m0, s39, 0xc000\n\t" \
        "v_mfma_f32_16x16x32_bf16 v[32:35], a[84:87], v[132:135], v[32:35]\n\t" \
        "ds_read_b64_tr_b16 a[96:97], v6 offset:0\n\t" \
        "v_mfma_f32_16x16x32_bf16 v[36:39], a[88:91], v[132:135], v[36:39]\n\t" \
        "buffer_load_dwordx4 v3, s[24:27], s57 offen offset:0 lds\n\t" \
        "v_mfma_f32_16x16x32_bf16 v[40:43], a[92:95], v[132:135], v[40:43]\n\t" \
        "ds_read_b64_tr_b16 a[98:99], v6 offset:768\n\t" \
        "s_waitcnt lgkmcnt(6)\n\t" \
        "v_mfma_f32_16x16x32_bf16 v[44:47], a[84:87], v[136:139], v[44:47]\n\t" \
        "ds_read_b64_tr_b16 a[100:101], v7 offset:0\n\t" \
        "v_mfma_f32_16x16x32_bf16 v[48:51], a[88:91], v[136:139], v[48:51]\n\t" \
        "ds_read_b64_tr_b16 a[102:103], v7 offset:768\n\t" \
        "v_mfma_f32_16x16x32_bf16 v[52:55], a[92:95], v[136:139], v[52:55]\n\t" \
        "ds_read_b64_tr_b16 a[104:105], v8 offset:0\n\t" \
        "s_waitcnt lgkmcnt(5)\n\t" \
        "v_xor_b32 v160, v160, v14\n\t" \
        "v_mfma_f32_16x16x32_bf16 v[56:59], a[84:87], v[140:143], v[56:59]\n\t" \
        "ds_read_b64_tr_b16 a[106:107], v8 offset:768\n\t" \
        "v_xor_b32 v161, v161, v14\n\t" \
        "v_mfma_f32_16x16x32_bf16 v[60:63], a[88:91], v[140:143], v[60:63]\n\t" \
        "buffer_load_dwordx4 v4, s[24:27], s57 offen offset:1024 lds\n\t" \
        "v_mfma_f32_16x16x32_bf16 v[64:67], a[92:95], v[140:143], v[64:67]\n\t" \
        "ds_read_b128 v[120:123], v157 offset:128\n\t" \
        "v_xor_b32 v162, v162, v14\n\t" \
        "v_mfma_f32_16x16x32_bf16 v[68:71], a[84:87], v[144:147], v[68:71]\n\t" \
        "ds_read_b128 v[124:127], v158 offset:128\n\t" \
        "v_xor_b32 v163, v163, v14\n\t" \
        "v_mfma_f32_16x16x32_bf16 v[72:75], a[88:91], v[144:147], v[72:75]\n\t" \
        "ds_read_b128 v[128:131], v159 offset:128\n\t" \
        "v_mfma_f32_16x16x32_bf16 v[76:79], a[92:95], v[144:147], v[76:79]\n\t" \
        "ds_read_b128 v[132:135], v160 offset:0\n\t" \
        "s_waitcnt lgkmcnt(8)\n\t" \
        "v_mfma_f32_16x16x32_bf16 v[32:35], a[96:99], v[116:119], v[32:35]\n\t" \
        "ds_read_b64_tr_b16 a[108:109], v6 offset:6144\n\t" \
        "s_waitcnt lgkmcnt(7)\n\t" \
        "v_mfma_f32_16x16x32_bf16 v[36:39], a[100:103], v[116:119], v[36:39]\n\t" \
        "buffer_load_dwordx4 v5, s[24:27], s57 offen offset:2048 lds\n\t" \
        "s_waitcnt lgkmcnt(5)\n\t" \
        "v_mfma_f32_16x16x32_bf16 v[40:43], a[104:107], v[116:119], v[40:43]\n\t" \
        "ds_read_b64_tr_b16 a[110:111], v6 offset:6912\n\t" \
        "s_waitcnt lgkmcnt(5)\n\t" \
        "v_mfma_f32_16x16x32_bf16 v[44:47], a[96:99], v[120:123], v[44:47]\n\t" \
        "ds_read_b64_tr_b16 a[112:113], v7 offset:6144\n\t" \
        "v_mfma_f32_16x16x32_bf16 v[48:51], a[100:103], v[120:123], v[48:51]\n\t" \
        "ds_read_b64_tr_b16 a[114:115], v7 offset:6912\n\t" \
        "v_mfma_f32_16x16x32_bf16 v[52:55], a[104:107], v[120:123], v[52:55]\n\t" \
        "ds_read_b64_tr_b16 a[116:117], v8 offset:6144\n\t" \
        "s_waitcnt lgkmcnt(7)\n\t" \
        "v_mfma_f32_16x16x32_bf16 v[56:59], a[96:99], v[124:127], v[56:59]\n\t" \
        "ds_read_b64_tr_b16 a[118:119], v8 offset:6912\n\t" \
        "v_mfma_f32_16x16x32_bf16 v[60:63], a[100:103], v[124:127], v[60:63]\n\t" \
        "ds_read_b128 v[136:139], v161 offset:0\n\t" \
        "v_mfma_f32_16x16x32_bf16 v[64:67], a[104:107], v[124:127], v[64:67]\n\t" \
        "ds_read_b128 v[140:143], v162 offset:0\n\t" \
        "s_waitcnt lgkmcnt(9)\n\t" \
        "v_mfma_f32_16x16x32_bf16 v[68:71], a[96:99], v[128:131], v[68:71]\n\t" \
        "ds_read_b128 v[144:147], v163 offset:0\n\t" \
        "v_mfma_f32_16x16x32_bf16 v[72:75], a[100:103], v[128:131], v[72:75]\n\t" \
        "ds_read_b128 v[116:119], v160 offset:64\n\t" \
        "v_mfma_f32_16x16x32_bf16 v[76:79], a[104:107], v[128:131], v[76:79]\n\t" \
        "s_waitcnt vmcnt(9) lgkmcnt(4)\n\t" \
        "s_barrier\n\t" \
        "ds_read_b64_tr_b16 a[120:121], v6 offset:12288\n\t" \
        "s_add_u32 s57, s40, 0x1b000\n\t" \
        "s_mov_b32 m0, s39\n\t" \
        "v_mfma_f32_16x16x32_bf16 v[32:35], a[108:111], v[132:135], v[32:35]\n\t" \
        "ds_read_b64_tr_b16 a[122:123], v6 offset:13056\n\t" \
        "v_mfma_f32_16x16x32_bf16 v[36:39], a[112:115], v[132:135], v[36:39]\n\t" \
        "buffer_load_dwordx4 v3, s[24:27], s57 offen offset:0 lds\n\t" \
        "v_mfma_f32_16x16x32_bf16 v[40:43], a[116:119], v[132:135], v[40:43]\n\t" \
        "ds_read_b64_tr_b16 a[124:125], v7 offset:12288\n\t" \
        "s_waitcnt lgkmcnt(6)\n\t" \
        "v_mfma_f32_16x16x32_bf16 v[44:47], a[108:111], v[136:139], v[44:47]\n\t" \
        "ds_read_b64_tr_b16 a[126:127], v7 offset:13056\n\t" \
        "v_mfma_f32_16x16x32_bf16 v[48:51], a[112:115], v[136:139], v[48:51]\n\t" \
        "ds_read_b64_tr_b16 a[128:129], v8 offset:12288\n\t" \
        "v_mfma_f32_16x16x32_bf16 v[52:55], a[116:119], v[136:139], v[52:55]\n\t" \
        "ds_read_b64_tr_b16 a[130:131], v8 offset:13056\n\t" \
        "s_waitcnt lgkmcnt(8)\n\t" \
        "v_mfma_f32_16x16x32_bf16 v[56:59], a[108:111], v[140:143], v[56:59]\n\t" \
        "ds_read_b128 v[120:123], v161 offset:64\n\t" \
        "v_mfma_f32_16x16x32_bf16 v[60:63], a[112:115], v[140:143], v[60:63]\n\t" \
        "buffer_load_dwordx4 v4, s[24:27], s57 offen offset:1024 lds\n\t" \
        "v_mfma_f32_16x16x32_bf16 v[64:67], a[116:119], v[140:143], v[64:67]\n\t" \
        "ds_read_b128 v[124:127], v162 offset:64\n\t" \
        "s_waitcnt lgkmcnt(8)\n\t" \
        "v_mfma_f32_16x16x32_bf16 v[68:71], a[108:111], v[144:147], v[68:71]\n\t" \
        "ds_read_b128 v[128:131], v163 offset:64\n\t" \
        "v_mfma_f32_16x16x32_bf16 v[72:75], a[112:115], v[144:147], v[72:75]\n\t" \
        "ds_read_b128 v[132:135], v160 offset:128\n\t" \
        "v_mfma_f32_16x16x32_bf16 v[76:79], a[116:119], v[144:147], v[76:79]\n\t" \
        "ds_read_b128 v[164:167], v15 offset:1024\n\t" \
        "s_waitcnt lgkmcnt(9)\n\t" \
        "v_mfma_f32_16x16x32_bf16 v[32:35], a[120:123], v[116:119], v[32:35]\n\t" \
        "ds_read_b64_tr_b16 a[132:133], v6 offset:18432\n\t" \
        "s_waitcnt lgkmcnt(8)\n\t" \
        "v_mfma_f32_16x16x32_bf16 v[36:39], a[124:127], v[116:119], v[36:39]\n\t" \
        "buffer_load_dwordx4 v5, s[24:27], s57 offen offset:2048 lds\n\t" \
        "s_waitcnt lgkmcnt(6)\n\t" \
        "v_mfma_f32_16x16x32_bf16 v[40:43], a[128:131], v[116:119], v[40:43]\n\t" \
        "ds_read_b64_tr_b16 a[134:135], v6 offset:19200\n\t" \
        "s_waitcnt lgkmcnt(6)\n\t" \
        "v_mfma_f32_16x16x32_bf16 v[44:47], a[120:123], v[120:123], v[44:47]\n\t" \
        "ds_read_b64_tr_b16 a[136:137], v7 offset:18432\n\t" \
        "v_mfma_f32_16x16x32_bf16 v[48:51], a[124:127], v[120:123], v[48:51]\n\t" \
        "ds_read_b64_tr_b16 a[138:139], v7 offset:19200\n\t" \
        "v_mfma_f32_16x16x32_bf16 v[52:55], a[128:131], v[120:123], v[52:55]\n\t" \
        "ds_read_b64_tr_b16 a[140:141], v8 offset:18432\n\t" \
        "s_waitcnt lgkmcnt(5)\n\t" \
        "v_xor_b32 v164, v164, v14\n\t" \
        "v_mfma_f32_16x16x32_bf16 v[56:59], a[120:123], v[124:127], v[56:59]\n\t" \
        "ds_read_b64_tr_b16 a[142:143], v8 offset:19200\n\t" \
        "v_xor_b32 v165, v165, v14\n\t" \
        "v_mfma_f32_16x16x32_bf16 v[60:63], a[124:127], v[124:127], v[60:63]\n\t" \
        "ds_read_b128 v[136:139], v161 offset:128\n\t" \
        "v_xor_b32 v166, v166, v14\n\t" \
        "v_mfma_f32_16x16x32_bf16 v[64:67], a[128:131], v[124:127], v[64:67]\n\t" \
        "ds_read_b128 v[140:143], v162 offset:128\n\t" \
        "v_xor_b32 v167, v167, v14\n\t" \
        "v_mfma_f32_16x16x32_bf16 v[68:71], a[120:123], v[128:131], v[68:71]\n\t" \
        "ds_read_b128 v[144:147], v163 offset:128\n\t" \
        "v_mfma_f32_16x16x32_bf16 v[72:75], a[124:127], v[128:131], v[72:75]\n\t" \
        "ds_read_b128 v[116:119], v164 offset:0\n\t" \
        "v_mfma_f32_16x16x32_bf16 v[76:79], a[128:131], v[128:131], v[76:79]\n\t" \
        "s_waitcnt vmcnt(9) lgkmcnt(4)\n\t" \
        "s_barrier\n\t" \
        "ds_read_b64_tr_b16 a[144:145], v6 offset:24576\n\t" \
        "s_add_u32 s57, s40, 0x1e000\n\t" \
        "s_add_u32 m0, s39, 0x3000\n\t" \
        "v_mfma_f32_16x16x32_bf16 v[32:35], a[132:135], v[132:135], v[32:35]\n\t" \
        "ds_read_b64_tr_b16 a[146:147], v6 offset:25344\n\t" \
        "v_mfma_f32_16x16x32_bf16 v[36:39], a[136:139], v[132:135], v[36:39]\n\t" \
        "buffer_load_dwordx4 v3, s[24:27], s57 offen offset:0 lds\n\t" \
        "v_mfma_f32_16x16x32_bf16 v[40:43], a[140:143], v[132:135], v[40:43]\n\t" \
        "ds_read_b64_tr_b16 a[148:149], v7 offset:24576\n\t" \
        "s_waitcnt lgkmcnt(6)\n\t" \
        "v_mfma_f32_16x16x32_bf16 v[44:47], a[132:135], v[136:139], v[44:47]\n\t" \
        "ds_read_b64_tr_b16 a[150:151], v7 offset:25344\n\t" \
        "v_mfma_f32_16x16x32_bf16 v[48:51], a[136:139], v[136:139], v[48:51]\n\t" \
        "ds_read_b64_tr_b16 a[152:153], v8 offset:24576\n\t" \
        "v_mfma_f32_16x16x32_bf16 v[52:55], a[140:143], v[136:139], v[52:55]\n\t" \
        "ds_read_b64_tr_b16 a[154:155], v8 offset:25344\n\t" \
        "s_waitcnt lgkmcnt(8)\n\t" \
        "v_mfma_f32_16x16x32_bf16 v[56:59], a[132:135], v[140:143], v[56:59]\n\t" \
        "ds_read_b128 v[120:123], v165 offset:0\n\t" \
        "v_mfma_f32_16x16x32_bf16 v[60:63], a[136:139], v[140:143], v[60:63]\n\t" \
        "buffer_load_dwordx4 v4, s[24:27], s57 offen offset:1024 lds\n\t" \
        "v_mfma_f32_16x16x32_bf16 v[64:67], a[140:143], v[140:143], v[64:67]\n\t" \
        "ds_read_b128 v[124:127], v166 offset:0\n\t" \
        "s_waitcnt lgkmcnt(8)\n\t" \
        "v_mfma_f32_16x16x32_bf16 v[68:71], a[132:135], v[144:147], v[68:71]\n\t" \
        "ds_read_b128 v[128:131], v167 offset:0\n\t" \
        "v_mfma_f32_16x16x32_bf16 v[72:75], a[136:139], v[144:147], v[72:75]\n\t" \
        "ds_read_b128 v[132:135], v164 offset:64\n\t" \
        "v_mfma_f32_16x16x32_bf16 v[76:79], a[140:143], v[144:147], v[76:79]\n\t" \
        "ds_read_b64_tr_b16 a[156:157], v6 offset:30720\n\t" \
        "s_waitcnt lgkmcnt(9)\n\t" \
        "v_mfma_f32_16x16x32_bf16 v[32:35], a[144:147], v[116:119], v[32:35]\n\t" \
        "ds_read_b64_tr_b16 a[158:159], v6 offset:31488\n\t" \
        "s_waitcnt lgkmcnt(8)\n\t" \
        "v_mfma_f32_16x16x32_bf16 v[36:39], a[148:151], v[116:119], v[36:39]\n\t" \
        "buffer_load_dwordx4 v5, s[24:27], s57 offen offset:2048 lds\n\t" \
        "s_waitcnt lgkmcnt(6)\n\t" \
        "v_mfma_f32_16x16x32_bf16 v[40:43], a[152:155], v[116:119], v[40:43]\n\t" \
        "ds_read_b64_tr_b16 a[160:161], v7 offset:30720\n\t" \
        "s_waitcnt lgkmcnt(6)\n\t" \
        "v_mfma_f32_16x16x32_bf16 v[44:47], a[144:147], v[120:123], v[44:47]\n\t" \
        "ds_read_b64_tr_b16 a[162:163], v7 offset:31488\n\t" \
        "v_mfma_f32_16x16x32_bf16 v[48:51], a[148:151], v[120:123], v[48:51]\n\t" \
        "ds_read_b64_tr_b16 a[164:165], v8 offset:30720\n\t" \
        "v_mfma_f32_16x16x32_bf16 v[52:55], a[152:155], v[120:123], v[52:55]\n\t" \
        "ds_read_b64_tr_b16 a[166:167], v8 offset:31488\n\t" \
        "s_waitcnt lgkmcnt(8)\n\t" \
        "v_mfma_f32_16x16x32_bf16 v[56:59], a[144:147], v[124:127], v[56:59]\n\t" \
        "ds_read_b128 v[136:139], v165 offset:64\n\t" \
        "v_mfma_f32_16x16x32_bf16 v[60:63], a[148:151], v[124:127], v[60:63]\n\t" \
        "ds_read_b128 v[140:143], v166 offset:64\n\t" \
        "v_mfma_f32_16x16x32_bf16 v[64:67], a[152:155], v[124:127], v[64:67]\n\t" \
        "ds_read_b128 v[144:147], v167 offset:64\n\t" \
        "s_waitcnt lgkmcnt(9)\n\t" \
        "v_mfma_f32_16x16x32_bf16 v[68:71], a[144:147], v[128:131], v[68:71]\n\t" \
        "ds_read_b128 v[116:119], v164 offset:128\n\t" \
        "v_mfma_f32_16x16x32_bf16 v[72:75], a[148:151], v[128:131], v[72:75]\n\t" \
        "ds_read_b128 v[168:171], v15 offset:1280\n\t" \
        "v_mfma_f32_16x16x32_bf16 v[76:79], a[152:155], v[128:131], v[76:79]\n\t" \
        "s_waitcnt vmcnt(9) lgkmcnt(5)\n\t" \
        "s_barrier\n\t" \
        "ds_read_b64_tr_b16 a[168:169], v6 offset:36864\n\t" \
        "s_add_u32 s57, s40, 0x21000\n\t" \
        "s_add_u32 m0, s39, 0x6000\n\t" \
        "v_mfma_f32_16x16x32_bf16 v[32:35], a[156:159], v[132:135], v[32:35]\n\t" \
        "ds_read_b64_tr_b16 a[170:171], v6 offset:37632\n\t" \
        "v_mfma_f32_16x16x32_bf16 v[36:39], a[160:163], v[132:135], v[36:39]\n\t" \
        "buffer_load_dwordx4 v3, s[24:27], s57 offen offset:0 lds\n\t" \
        "v_mfma_f32_16x16x32_bf16 v[40:43], a[164:167], v[132:135], v[40:43]\n\t" \
        "ds_read_b64_tr_b16 a[172:173], v7 offset:36864\n\t" \
        "s_waitcnt lgkmcnt(7)\n\t" \
        "v_mfma_f32_16x16x32_bf16 v[44:47], a[156:159], v[136:139], v[44:47]\n\t" \
        "ds_read_b64_tr_b16 a[174:175], v7 offset:37632\n\t" \
        "v_mfma_f32_16x16x32_bf16 v[48:51], a[160:163], v[136:139], v[48:51]\n\t" \
        "ds_read_b64_tr_b16 a[176:177], v8 offset:36864\n\t" \
        "s_waitcnt lgkmcnt(5)\n\t" \
        "v_xor_b32 v168, v168, v14\n\t" \
        "v_mfma_f32_16x16x32_bf16 v[52:55], a[164:167], v[136:139], v[52:55]\n\t" \
        "ds_read_b64_tr_b16 a[178:179], v8 offset:37632\n\t" \
        "v_xor_b32 v169, v169, v14\n\t" \
        "v_mfma_f32_16x16x32_bf16 v[56:59], a[156:159], v[140:143], v[56:59]\n\t" \
        "ds_read_b128 v[120:123], v165 offset:128\n\t" \
        "v_xor_b32 v170, v170, v14\n\t" \
        "v_mfma_f32_16x16x32_bf16 v[60:63], a[160:163], v[140:143], v[60:63]\n\t" \
        "buffer_load_dwordx4 v4, s[24:27], s57 offen offset:1024 lds\n\t" \
        "v_mfma_f32_16x16x32_bf16 v[64:67], a[164:167], v[140:143], v[64:67]\n\t" \
        "ds_read_b128 v[124:127], v166 offset:128\n\t" \
        "v_xor_b32 v171, v171, v14\n\t" \
        "v_mfma_f32_16x16x32_bf16 v[68:71], a[156:159], v[144:147], v[68:71]\n\t" \
        "ds_read_b128 v[128:131], v167 offset:128\n\t" \
        "v_mfma_f32_16x16x32_bf16 v[72:75], a[160:163], v[144:147], v[72:75]\n\t" \
        "ds_read_b128 v[132:135], v168 offset:0\n\t" \
        "v_mfma_f32_16x16x32_bf16 v[76:79], a[164:167], v[144:147], v[76:79]\n\t" \
        "ds_read_b64_tr_b16 a[180:181], v6 offset:43008\n\t" \
        "s_waitcnt lgkmcnt(9)\n\t" \
        "v_mfma_f32_16x16x32_bf16 v[32:35], a[168:171], v[116:119], v[32:35]\n\t" \
        "ds_read_b64_tr_b16 a[182:183], v6 offset:43776\n\t" \
        "s_waitcnt lgkmcnt(8)\n\t" \
        "v_mfma_f32_16x16x32_bf16 v[36:39], a[172:175], v[116:119], v[36:39]\n\t" \
        "buffer_load_dwordx4 v5, s[24:27], s57 offen offset:2048 lds\n\t" \
        "s_waitcnt lgkmcnt(6)\n\t" \
        "v_mfma_f32_16x16x32_bf16 v[40:43], a[176:179], v[116:119], v[40:43]\n\t" \
        "ds_read_b64_tr_b16 a[184:185], v7 offset:43008\n\t" \
        "s_waitcnt lgkmcnt(6)\n\t" \
        "v_mfma_f32_16x16x32_bf16 v[44:47], a[168:171], v[120:123], v[44:47]\n\t" \
        "ds_read_b64_tr_b16 a[186:187], v7 offset:43776\n\t" \
        "v_mfma_f32_16x16x32_bf16 v[48:51], a[172:175], v[120:123], v[48:51]\n\t" \
        "ds_read_b64_tr_b16 a[188:189], v8 offset:43008\n\t" \
        "v_mfma_f32_16x16x32_bf16 v[52:55], a[176:179], v[120:123], v[52:55]\n\t" \
        "ds_read_b64_tr_b16 a[190:191], v8 offset:43776\n\t" \
        "s_waitcnt lgkmcnt(8)\n\t" \
        "v_mfma_f32_16x16x32_bf16 v[56:59], a[168:171], v[124:127], v[56:59]\n\t" \
        "ds_read_b128 v[136:139], v169 offset:0\n\t" \
        "v_mfma_f32_16x16x32_bf16 v[60:63], a[172:175], v[124:127], v[60:63]\n\t" \
        "ds_read_b128 v[140:143], v170 offset:0\n\t" \
        "v_mfma_f32_16x16x32_bf16 v[64:67], a[176:179], v[124:127], v[64:67]\n\t" \
        "ds_read_b128 v[144:147], v171 offset:0\n\t" \
        "s_waitcnt lgkmcnt(9)\n\t" \
        "v_mfma_f32_16x16x32_bf16 v[68:71], a[168:171], v[128:131], v[68:71]\n\t" \
        "ds_read_b128 v[116:119], v168 offset:64\n\t" \
        "v_mfma_f32_16x16x32_bf16 v[72:75], a[172:175], v[128:131], v[72:75]\n\t" \
        "ds_read_b128 v[120:123], v169 offset:64\n\t" \
        "v_mfma_f32_16x16x32_bf16 v[76:79], a[176:179], v[128:131], v[76:79]\n\t" \
        "s_waitcnt vmcnt(9) lgkmcnt(5)\n\t" \
        "s_barrier\n\t" \
        "ds_read_b64_tr_b16 a[192:193], v6 offset:49152\n\t" \
        "s_add_u32 s57, s40, 0x24000\n\t" \
        "s_add_u32 m0, s39, 0x9000\n\t" \
        "v_mfma_f32_16x16x32_bf16 v[32:35], a[180:183], v[132:135], v[32:35]\n\t" \
        "ds_read_b64_tr_b16 a[194:195], v6 offset:49920\n\t" \
        "v_mfma_f32_16x16x32_bf16 v[36:39], a[184:187], v[132:135], v[36:39]\n\t" \
        "buffer_load_dwordx4 v3, s[24:27], s57 offen offset:0 lds\n\t" \
        "v_mfma_f32_16x16x32_bf16 v[40:43], a[188:191], v[132:135], v[40:43]\n\t" \
        "ds_read_b64_tr_b16 a[196:197], v7 offset:49152\n\t" \
        "s_waitcnt lgkmcnt(7)\n\t" \
        "v_mfma_f32_16x16x32_bf16 v[44:47], a[180:183], v[136:139], v[44:47]\n\t" \
        "ds_read_b64_tr_b16 a[198:199], v7 offset:49920\n\t" \
        "v_mfma_f32_16x16x32_bf16 v[48:51], a[184:187], v[136:139], v[48:51]\n\t" \
        "ds_read_b64_tr_b16 a[200:201], v8 offset:49152\n\t" \
        "v_mfma_f32_16x16x32_bf16 v[52:55], a[188:191], v[136:139], v[52:55]\n\t" \
        "ds_read_b64_tr_b16 a[202:203], v8 offset:49920\n\t" \
        "s_waitcnt lgkmcnt(9)\n\t" \
        "v_mfma_f32_16x16x32_bf16 v[56:59], a[180:183], v[140:143], v[56:59]\n\t" \
        "ds_read_b128 v[124:127], v170 offset:64\n\t" \
        "v_mfma_f32_16x16x32_bf16 v[60:63], a[184:187], v[140:143], v[60:63]\n\t" \
        "buffer_load_dwordx4 v4, s[24:27], s57 offen offset:1024 lds\n\t" \
        "v_mfma_f32_16x16x32_bf16 v[64:67], a[188:191], v[140:143], v[64:67]\n\t" \
        "ds_read_b128 v[128:131], v171 offset:64\n\t" \
        "s_waitcnt lgkmcnt(8)\n\t" \
        "v_mfma_f32_16x16x32_bf16 v[68:71], a[180:183], v[144:147], v[68:71]\n\t" \
        "ds_read_b128 v[132:135], v168 offset:128\n\t" \
        "v_mfma_f32_16x16x32_bf16 v[72:75], a[184:187], v[144:147], v[72:75]\n\t" \
        "ds_read_b128 v[172:175], v15 offset:1536\n\t" \
        "v_mfma_f32_16x16x32_bf16 v[76:79], a[188:191], v[144:147], v[76:79]\n\t" \
        "ds_read_b64_tr_b16 a[204:205], v6 offset:55296\n\t" \
        "s_waitcnt lgkmcnt(9)\n\t" \
        "v_mfma_f32_16x16x32_bf16 v[32:35], a[192:195], v[116:119], v[32:35]\n\t" \
        "ds_read_b64_tr_b16 a[206:207], v6 offset:56064\n\t" \
        "s_waitcnt lgkmcnt(8)\n\t" \
        "v_mfma_f32_16x16x32_bf16 v[36:39], a[196:199], v[116:119], v[36:39]\n\t" \
        "buffer_load_dwordx4 v5, s[24:27], s57 offen offset:2048 lds\n\t" \
        "s_waitcnt lgkmcnt(6)\n\t" \
        "v_mfma_f32_16x16x32_bf16 v[40:43], a[200:203], v[116:119], v[40:43]\n\t" \
        "ds_read_b64_tr_b16 a[208:209], v7 offset:55296\n\t" \
        "v_mfma_f32_16x16x32_bf16 v[44:47], a[192:195], v[120:123], v[44:47]\n\t" \
        "ds_read_b64_tr_b16 a[210:211], v7 offset:56064\n\t" \
        "v_mfma_f32_16x16x32_bf16 v[48:51], a[196:199], v[120:123], v[48:51]\n\t" \
        "ds_read_b64_tr_b16 a[212:213], v8 offset:55296\n\t" \
        "s_waitcnt lgkmcnt(5)\n\t" \
        "v_xor_b32 v172, v172, v14\n\t" \
        "v_mfma_f32_16x16x32_bf16 v[52:55], a[200:203], v[120:123], v[52:55]\n\t" \
        "ds_read_b64_tr_b16 a[214:215], v8 offset:56064\n\t" \
        "v_xor_b32 v173, v173, v14\n\t" \
        "v_mfma_f32_16x16x32_bf16 v[56:59], a[192:195], v[124:127], v[56:59]\n\t" \
        "ds_read_b128 v[136:139], v169 offset:128\n\t" \
        "v_xor_b32 v174, v174, v14\n\t" \
        "v_mfma_f32_16x16x32_bf16 v[60:63], a[196:199], v[124:127], v[60:63]\n\t" \
        "ds_read_b128 v[140:143], v170 offset:128\n\t" \
        "v_xor_b32 v175, v175, v14\n\t" \
        "v_mfma_f32_16x16x32_bf16 v[64:67], a[200:203], v[124:127], v[64:67]\n\t" \
        "ds_read_b128 v[144:147], v171 offset:128\n\t" \
        "v_mfma_f32_16x16x32_bf16 v[68:71], a[192:195], v[128:131], v[68:71]\n\t" \
        "ds_read_b128 v[116:119], v172 offset:0\n\t" \
        "v_mfma_f32_16x16x32_bf16 v[72:75], a[196:199], v[128:131], v[72:75]\n\t" \
        "ds_read_b128 v[120:123], v173 offset:0\n\t" \
        "v_mfma_f32_16x16x32_bf16 v[76:79], a[200:203], v[128:131], v[76:79]\n\t" \
        "s_waitcnt vmcnt(9) lgkmcnt(5)\n\t" \
        "s_barrier\n\t" \
        "ds_read_b64_tr_b16 a[216:217], v6 offset:0\n\t" \
        "s_add_u32 s57, s40, 0x27000\n\t" \
        "s_add_u32 m0, s39, 0xc000\n\t" \
        "v_mfma_f32_16x16x32_bf16 v[32:35], a[204:207], v[132:135], v[32:35]\n\t" \
        "ds_read_b64_tr_b16 a[218:219], v6 offset:768\n\t" \
        "v_mfma_f32_16x16x32_bf16 v[36:39], a[208:211], v[132:135], v[36:39]\n\t" \
        "buffer_load_dwordx4 v3, s[24:27], s57 offen offset:0 lds\n\t" \
        "v_mfma_f32_16x16x32_bf16 v[40:43], a[212:215], v[132:135], v[40:43]\n\t" \
        "ds_read_b64_tr_b16 a[220:221], v7 offset:0\n\t" \
        "s_waitcnt lgkmcnt(7)\n\t" \
        "v_mfma_f32_16x16x32_bf16 v[44:47], a[204:207], v[136:139], v[44:47]\n\t" \
        "ds_read_b64_tr_b16 a[222:223], v7 offset:768\n\t" \
        "v_mfma_f32_16x16x32_bf16 v[48:51], a[208:211], v[136:139], v[48:51]\n\t" \
        "ds_read_b64_tr_b16 a[224:225], v8 offset:0\n\t" \
        "v_mfma_f32_16x16x32_bf16 v[52:55], a[212:215], v[136:139], v[52:55]\n\t" \
        "ds_read_b64_tr_b16 a[226:227], v8 offset:768\n\t" \
        "s_waitcnt lgkmcnt(9)\n\t" \
        "v_mfma_f32_16x16x32_bf16 v[56:59], a[204:207], v[140:143], v[56:59]\n\t" \
        "ds_read_b128 v[124:127], v174 offset:0\n\t" \
        "v_mfma_f32_16x16x32_bf16 v[60:63], a[208:211], v[140:143], v[60:63]\n\t" \
        "buffer_load_dwordx4 v4, s[24:27], s57 offen offset:1024 lds\n\t" \
        "v_mfma_f32_16x16x32_bf16 v[64:67], a[212:215], v[140:143], v[64:67]\n\t" \
        "ds_read_b128 v[128:131], v175 offset:0\n\t" \
        "s_waitcnt lgkmcnt(8)\n\t" \
        "v_mfma_f32_16x16x32_bf16 v[68:71], a[204:207], v[144:147], v[68:71]\n\t" \
        "ds_read_b128 v[132:135], v172 offset:64\n\t" \
        "v_mfma_f32_16x16x32_bf16 v[72:75], a[208:211], v[144:147], v[72:75]\n\t" \
        "ds_read_b64_tr_b16 a[228:229], v6 offset:6144\n\t" \
        "v_mfma_f32_16x16x32_bf16 v[76:79], a[212:215], v[144:147], v[76:79]\n\t" \
        "ds_read_b64_tr_b16 a[230:231], v6 offset:6912\n\t" \
        "s_waitcnt lgkmcnt(9)\n\t" \
        "v_mfma_f32_16x16x32_bf16 v[32:35], a[216:219], v[116:119], v[32:35]\n\t" \
        "ds_read_b64_tr_b16 a[232:233], v7 offset:6144\n\t" \
        "s_waitcnt lgkmcnt(8)\n\t" \
        "v_mfma_f32_16x16x32_bf16 v[36:39], a[220:223], v[116:119], v[36:39]\n\t" \
        "buffer_load_dwordx4 v5, s[24:27], s57 offen offset:2048 lds\n\t" \
        "s_waitcnt lgkmcnt(6)\n\t" \
        "v_mfma_f32_16x16x32_bf16 v[40:43], a[224:227], v[116:119], v[40:43]\n\t" \
        "ds_read_b64_tr_b16 a[234:235], v7 offset:6912\n\t" \
        "v_mfma_f32_16x16x32_bf16 v[44:47], a[216:219], v[120:123], v[44:47]\n\t" \
        "ds_read_b64_tr_b16 a[236:237], v8 offset:6144\n\t" \
        "v_mfma_f32_16x16x32_bf16 v[48:51], a[220:223], v[120:123], v[48:51]\n\t" \
        "ds_read_b64_tr_b16 a[238:239], v8 offset:6912\n\t" \
        "v_mfma_f32_16x16x32_bf16 v[52:55], a[224:227], v[120:123], v[52:55]\n\t" \
        "ds_read_b128 v[136:139], v173 offset:64\n\t" \
        "s_waitcnt lgkmcnt(9)\n\t" \
        "v_mfma_f32_16x16x32_bf16 v[56:59], a[216:219], v[124:127], v[56:59]\n\t" \
        "ds_read_b128 v[140:143], v174 offset:64\n\t" \
        "v_mfma_f32_16x16x32_bf16 v[60:63], a[220:223], v[124:127], v[60:63]\n\t" \
        "ds_read_b128 v[144:147], v175 offset:64\n\t" \
        "v_mfma_f32_16x16x32_bf16 v[64:67], a[224:227], v[124:127], v[64:67]\n\t" \
        "ds_read_b128 v[116:119], v172 offset:128\n\t" \
        "s_waitcnt lgkmcnt(9)\n\t" \
        "v_mfma_f32_16x16x32_bf16 v[68:71], a[216:219], v[128:131], v[68:71]\n\t" \
        "ds_read_b128 v[176:179], v15 offset:1792\n\t" \
        "v_mfma_f32_16x16x32_bf16 v[72:75], a[220:223], v[128:131], v[72:75]\n\t" \
        "ds_read_b128 v[120:123], v173 offset:128\n\t" \
        "v_mfma_f32_16x16x32_bf16 v[76:79], a[224:227], v[128:131], v[76:79]\n\t" \
        "s_waitcnt vmcnt(9)\n\t" \
        "s_barrier\n\t" \
        "ds_read_b64_tr_b16 a[240:241], v6 offset:12288\n\t" \
        "s_waitcnt lgkmcnt(10)\n\t" \
        "v_mfma_f32_16x16x32_bf16 v[32:35], a[228:231], v[132:135], v[32:35]\n\t" \
        "ds_read_b64_tr_b16 a[242:243], v6 offset:13056\n\t" \
        "s_waitcnt lgkmcnt(10)\n\t" \
        "v_mfma_f32_16x16x32_bf16 v[36:39], a[232:235], v[132:135], v[36:39]\n\t" \
        "ds_read_b64_tr_b16 a[244:245], v7 offset:12288\n\t" \
        "s_waitcnt lgkmcnt(9)\n\t" \
        "v_mfma_f32_16x16x32_bf16 v[40:43], a[236:239], v[132:135], v[40:43]\n\t" \
        "ds_read_b64_tr_b16 a[246:247], v7 offset:13056\n\t" \
        "s_waitcnt lgkmcnt(9)\n\t" \
        "v_mfma_f32_16x16x32_bf16 v[44:47], a[228:231], v[136:139], v[44:47]\n\t" \
        "ds_read_b64_tr_b16 a[248:249], v8 offset:12288\n\t" \
        "s_waitcnt lgkmcnt(6)\n\t" \
        "v_xor_b32 v176, v176, v14\n\t" \
        "v_mfma_f32_16x16x32_bf16 v[48:51], a[232:235], v[136:139], v[48:51]\n\t" \
        "ds_read_b64_tr_b16 a[250:251], v8 offset:13056\n\t" \
        "v_xor_b32 v177, v177, v14\n\t" \
        "v_mfma_f32_16x16x32_bf16 v[52:55], a[236:239], v[136:139], v[52:55]\n\t" \
        "ds_read_b128 v[124:127], v174 offset:128\n\t" \
        "v_xor_b32 v178, v178, v14\n\t" \
        "v_mfma_f32_16x16x32_bf16 v[56:59], a[228:231], v[140:143], v[56:59]\n\t" \
        "ds_read_b128 v[128:131], v175 offset:128\n\t" \
        "v_xor_b32 v179, v179, v14\n\t" \
        "v_mfma_f32_16x16x32_bf16 v[60:63], a[232:235], v[140:143], v[60:63]\n\t" \
        "ds_read_b128 v[132:135], v176 offset:0\n\t" \
        "v_mfma_f32_16x16x32_bf16 v[64:67], a[236:239], v[140:143], v[64:67]\n\t" \
        "ds_read_b64_tr_b16 a[252:253], v6 offset:18432\n\t" \
        "v_mfma_f32_16x16x32_bf16 v[68:71], a[228:231], v[144:147], v[68:71]\n\t" \
        "ds_read_b64_tr_b16 a[254:255], v6 offset:19200\n\t" \
        "v_mfma_f32_16x16x32_bf16 v[72:75], a[232:235], v[144:147], v[72:75]\n\t" \
        "ds_read_b64_tr_b16 v[188:189], v7 offset:18432\n\t" \
        "v_mfma_f32_16x16x32_bf16 v[76:79], a[236:239], v[144:147], v[76:79]\n\t" \
        "ds_read_b64_tr_b16 v[190:191], v7 offset:19200\n\t" \
        "s_waitcnt lgkmcnt(10)\n\t" \
        "v_mfma_f32_16x16x32_bf16 v[32:35], a[240:243], v[116:119], v[32:35]\n\t" \
        "ds_read_b64_tr_b16 v[192:193], v8 offset:18432\n\t" \
        "s_waitcnt lgkmcnt(10)\n\t" \
        "v_mfma_f32_16x16x32_bf16 v[36:39], a[244:247], v[116:119], v[36:39]\n\t" \
        "ds_read_b64_tr_b16 v[194:195], v8 offset:19200\n\t" \
        "s_waitcnt lgkmcnt(9)\n\t" \
        "v_mfma_f32_16x16x32_bf16 v[40:43], a[248:251], v[116:119], v[40:43]\n\t" \
        "ds_read_b128 v[136:139], v177 offset:0\n\t" \
        "v_mfma_f32_16x16x32_bf16 v[44:47], a[240:243], v[120:123], v[44:47]\n\t" \
        "ds_read_b128 v[140:143], v178 offset:0\n\t" \
        "v_mfma_f32_16x16x32_bf16 v[48:51], a[244:247], v[120:123], v[48:51]\n\t" \
        "ds_read_b128 v[144:147], v179 offset:0\n\t" \
        "v_mfma_f32_16x16x32_bf16 v[52:55], a[248:251], v[120:123], v[52:55]\n\t" \
        "ds_read_b128 v[116:119], v176 offset:64\n\t" \
        "s_waitcnt lgkmcnt(10)\n\t" \
        "v_mfma_f32_16x16x32_bf16 v[56:59], a[240:243], v[124:127], v[56:59]\n\t" \
        "ds_read_b128 v[120:123], v177 offset:64\n\t" \
        "v_mfma_f32_16x16x32_bf16 v[60:63], a[244:247], v[124:127], v[60:63]\n\t" \
        "ds_read_b128 v[180:183], v15 offset:2048\n\t" \
        "v_mfma_f32_16x16x32_bf16 v[64:67], a[248:251], v[124:127], v[64:67]\n\t" \
        "ds_read_b128 v[124:127], v178 offset:64\n\t" \
        "v_mfma_f32_16x16x32_bf16 v[68:71], a[240:243], v[128:131], v[68:71]\n\t" \
        "ds_read_b96 v[148:150], v15 offset:4608\n\t" \
        "v_mfma_f32_16x16x32_bf16 v[72:75], a[244:247], v[128:131], v[72:75]\n\t" \
        "ds_read_b96 v[152:154], v15 offset:4864\n\t" \
        "v_mfma_f32_16x16x32_bf16 v[76:79], a[248:251], v[128:131], v[76:79]\n\t" \
        "s_waitcnt vmcnt(6)\n\t" \
        "s_barrier\n\t" \
        "ds_read_b64_tr_b16 v[196:197], v6 offset:24576\n\t" \
        "s_waitcnt lgkmcnt(10)\n\t" \
        "v_mfma_f32_16x16x32_bf16 v[32:35], a[252:255], v[132:135], v[32:35]\n\t" \
        "ds_read_b64_tr_b16 v[198:199], v6 offset:25344\n\t" \
        "v_mfma_f32_16x16x32_bf16 v[36:39], v[188:191], v[132:135], v[36:39]\n\t" \
        "ds_read_b64_tr_b16 v[200:201], v7 offset:24576\n\t" \
        "s_waitcnt lgkmcnt(6)\n\t" \
        "v_xor_b32 v180, v180, v14\n\t" \
        "v_mfma_f32_16x16x32_bf16 v[40:43], v[192:195], v[132:135], v[40:43]\n\t" \
        "ds_read_b64_tr_b16 v[202:203], v7 offset:25344\n\t" \
        "v_xor_b32 v181, v181, v14\n\t" \
        "v_mfma_f32_16x16x32_bf16 v[44:47], a[252:255], v[136:139], v[44:47]\n\t" \
        "ds_read_b64_tr_b16 v[204:205], v8 offset:24576\n\t" \
        "v_xor_b32 v182, v182, v14\n\t" \
        "v_mfma_f32_16x16x32_bf16 v[48:51], v[188:191], v[136:139], v[48:51]\n\t" \
        "ds_read_b64_tr_b16 v[206:207], v8 offset:25344\n\t" \
        "v_xor_b32 v183, v183, v14\n\t" \
        "v_mfma_f32_16x16x32_bf16 v[52:55], v[192:195], v[136:139], v[52:55]\n\t" \
        "ds_read_b128 v[128:131], v179 offset:64\n\t" \
        "s_waitcnt lgkmcnt(8)\n\t" \
        "v_xor_b32 v148, v148, v14\n\t" \
        "v_mfma_f32_16x16x32_bf16 v[56:59], a[252:255], v[140:143], v[56:59]\n\t" \
        "ds_read_b128 v[132:135], v176 offset:128\n\t" \
        "s_waitcnt lgkmcnt(8)\n\t" \
        "v_xor_b32 v152, v152, v14\n\t" \
        "v_mfma_f32_16x16x32_bf16 v[60:63], v[188:191], v[140:143], v[60:63]\n\t" \
        "ds_read_b64_tr_b16 v[208:209], v6 offset:30720\n\t" \
        "v_xor_b32 v150, v150, v14\n\t" \
        "v_mfma_f32_16x16x32_bf16 v[64:67], v[192:195], v[140:143], v[64:67]\n\t" \
        "ds_read_b64_tr_b16 v[210:211], v6 offset:31488\n\t" \
        "v_xor_b32 v154, v154, v14\n\t" \
        "v_mfma_f32_16x16x32_bf16 v[68:71], a[252:255], v[144:147], v[68:71]\n\t" \
        "ds_read_b64_tr_b16 v[212:213], v7 offset:30720\n\t" \
        "v_xor_b32 v149, v149, v14\n\t" \
        "v_mfma_f32_16x16x32_bf16 v[72:75], v[188:191], v[144:147], v[72:75]\n\t" \
        "ds_read_b64_tr_b16 v[214:215], v7 offset:31488\n\t" \
        "v_xor_b32 v153, v153, v14\n\t" \
        "v_mfma_f32_16x16x32_bf16 v[76:79], v[192:195], v[144:147], v[76:79]\n\t" \
        "ds_read_b64_tr_b16 v[216:217], v8 offset:30720\n\t" \
        "s_waitcnt lgkmcnt(10)\n\t" \
        "v_mfma_f32_16x16x32_bf16 v[32:35], v[196:199], v[116:119], v[32:35]\n\t" \
        "ds_read_b64_tr_b16 v[218:219], v8 offset:31488\n\t" \
        "s_waitcnt lgkmcnt(10)\n\t" \
        "v_mfma_f32_16x16x32_bf16 v[36:39], v[200:203], v[116:119], v[36:39]\n\t" \
        "ds_read_b128 v[136:139], v177 offset:128\n\t" \
        "s_waitcnt lgkmcnt(9)\n\t" \
        "v_mfma_f32_16x16x32_bf16 v[40:43], v[204:207], v[116:119], v[40:43]\n\t" \
        "ds_read_b128 v[140:143], v178 offset:128\n\t" \
        "v_mfma_f32_16x16x32_bf16 v[44:47], v[196:199], v[120:123], v[44:47]\n\t" \
        "ds_read_b128 v[144:147], v179 offset:128\n\t" \
        "v_mfma_f32_16x16x32_bf16 v[48:51], v[200:203], v[120:123], v[48:51]\n\t" \
        "ds_read_b128 v[116:119], v180 offset:0\n\t" \
        "v_mfma_f32_16x16x32_bf16 v[52:55], v[204:207], v[120:123], v[52:55]\n\t" \
        "ds_read_b128 v[120:123], v181 offset:0\n\t" \
        "v_mfma_f32_16x16x32_bf16 v[56:59], v[196:199], v[124:127], v[56:59]\n\t" \
        "ds_read_b96 v[156:158], v15 offset:5120\n\t" \
        "v_mfma_f32_16x16x32_bf16 v[60:63], v[200:203], v[124:127], v[60:63]\n\t" \
        "ds_read_b96 v[160:162], v15 offset:5376\n\t" \
        "v_mfma_f32_16x16x32_bf16 v[64:67], v[204:207], v[124:127], v[64:67]\n\t" \
        "ds_read_b128 v[124:127], v182 offset:0\n\t" \
        "s_waitcnt lgkmcnt(10)\n\t" \
        "v_mfma_f32_16x16x32_bf16 v[68:71], v[196:199], v[128:131], v[68:71]\n\t" \
        "ds_read_b96 v[164:166], v15 offset:5632\n\t" \
        "v_mfma_f32_16x16x32_bf16 v[72:75], v[200:203], v[128:131], v[72:75]\n\t" \
        "ds_read_b96 v[168:170], v15 offset:5888\n\t" \
        "v_mfma_f32_16x16x32_bf16 v[76:79], v[204:207], v[128:131], v[76:79]\n\t" \
        "s_waitcnt vmcnt(3)\n\t" \
        "s_barrier\n\t" \
        "ds_read_b64_tr_b16 v[220:221], v6 offset:36864\n\t" \
        "v_mfma_f32_16x16x32_bf16 v[32:35], v[208:211], v[132:135], v[32:35]\n\t" \
        "ds_read_b64_tr_b16 v[222:223], v6 offset:37632\n\t" \
        "s_waitcnt lgkmcnt(6)\n\t" \
        "v_xor_b32 v156, v156, v14\n\t" \
        "v_mfma_f32_16x16x32_bf16 v[36:39], v[212:215], v[132:135], v[36:39]\n\t" \
        "ds_read_b64_tr_b16 v[224:225], v7 offset:36864\n\t" \
        "s_waitcnt lgkmcnt(6)\n\t" \
        "v_xor_b32 v160, v160, v14\n\t" \
        "v_mfma_f32_16x16x32_bf16 v[40:43], v[216:219], v[132:135], v[40:43]\n\t" \
        "ds_read_b64_tr_b16 v[226:227], v7 offset:37632\n\t" \
        "v_xor_b32 v158, v158, v14\n\t" \
        "v_mfma_f32_16x16x32_bf16 v[44:47], v[208:211], v[136:139], v[44:47]\n\t" \
        "ds_read_b64_tr_b16 v[228:229], v8 offset:36864\n\t" \
        "s_waitcnt lgkmcnt(6)\n\t" \
        "v_xor_b32 v164, v164, v14\n\t" \
        "v_mfma_f32_16x16x32_bf16 v[48:51], v[212:215], v[136:139], v[48:51]\n\t" \
        "ds_read_b64_tr_b16 v[230:231], v8 offset:37632\n\t" \
        "s_waitcnt lgkmcnt(6)\n\t" \
        "v_xor_b32 v168, v168, v14\n\t" \
        "v_mfma_f32_16x16x32_bf16 v[52:55], v[216:219], v[136:139], v[52:55]\n\t" \
        "ds_read_b128 v[128:131], v183 offset:0\n\t" \
        "v_xor_b32 v162, v162, v14\n\t" \
        "v_mfma_f32_16x16x32_bf16 v[56:59], v[208:211], v[140:143], v[56:59]\n\t" \
        "ds_read_b128 v[132:135], v180 offset:64\n\t" \
        "v_xor_b32 v166, v166, v14\n\t" \
        "v_mfma_f32_16x16x32_bf16 v[60:63], v[212:215], v[140:143], v[60:63]\n\t" \
        "ds_read_b64_tr_b16 v[232:233], v6 offset:43008\n\t" \
        "v_xor_b32 v157, v157, v14\n\t" \
        "v_mfma_f32_16x16x32_bf16 v[64:67], v[216:219], v[140:143], v[64:67]\n\t" \
        "ds_read_b64_tr_b16 v[234:235], v6 offset:43776\n\t" \
        "v_xor_b32 v170, v170, v14\n\t" \
        "v_mfma_f32_16x16x32_bf16 v[68:71], v[208:211], v[144:147], v[68:71]\n\t" \
        "ds_read_b64_tr_b16 v[236:237], v7 offset:43008\n\t" \
        "v_xor_b32 v161, v161, v14\n\t" \
        "v_mfma_f32_16x16x32_bf16 v[72:75], v[212:215], v[144:147], v[72:75]\n\t" \
        "ds_read_b64_tr_b16 v[238:239], v7 offset:43776\n\t" \
        "v_xor_b32 v165, v165, v14\n\t" \
        "v_mfma_f32_16x16x32_bf16 v[76:79], v[216:219], v[144:147], v[76:79]\n\t" \
        "ds_read_b64_tr_b16 v[240:241], v8 offset:43008\n\t" \
        "v_xor_b32 v169, v169, v14\n\t" \
        "s_waitcnt lgkmcnt(10)\n\t" \
        "v_mfma_f32_16x16x32_bf16 v[32:35], v[220:223], v[116:119], v[32:35]\n\t" \
        "ds_read_b64_tr_b16 v[242:243], v8 offset:43776\n\t" \
        "s_waitcnt lgkmcnt(10)\n\t" \
        "v_mfma_f32_16x16x32_bf16 v[36:39], v[224:227], v[116:119], v[36:39]\n\t" \
        "ds_read_b128 v[136:139], v181 offset:64\n\t" \
        "s_waitcnt lgkmcnt(9)\n\t" \
        "v_mfma_f32_16x16x32_bf16 v[40:43], v[228:231], v[116:119], v[40:43]\n\t" \
        "ds_read_b128 v[140:143], v182 offset:64\n\t" \
        "v_mfma_f32_16x16x32_bf16 v[44:47], v[220:223], v[120:123], v[44:47]\n\t" \
        "ds_read_b128 v[144:147], v183 offset:64\n\t" \
        "v_mfma_f32_16x16x32_bf16 v[48:51], v[224:227], v[120:123], v[48:51]\n\t" \
        "ds_read_b128 v[116:119], v180 offset:128\n\t" \
        "v_mfma_f32_16x16x32_bf16 v[52:55], v[228:231], v[120:123], v[52:55]\n\t" \
        "ds_read_b128 v[120:123], v181 offset:128\n\t" \
        "v_mfma_f32_16x16x32_bf16 v[56:59], v[220:223], v[124:127], v[56:59]\n\t" \
        "ds_read_b96 v[172:174], v15 offset:6144\n\t" \
        "v_mfma_f32_16x16x32_bf16 v[60:63], v[224:227], v[124:127], v[60:63]\n\t" \
        "ds_read_b96 v[176:178], v15 offset:6400\n\t" \
        "v_mfma_f32_16x16x32_bf16 v[64:67], v[228:231], v[124:127], v[64:67]\n\t" \
        "ds_read_b128 v[124:127], v182 offset:128\n\t" \
        "s_waitcnt lgkmcnt(10)\n\t" \
        "v_mfma_f32_16x16x32_bf16 v[68:71], v[220:223], v[128:131], v[68:71]\n\t" \
        "v_mfma_f32_16x16x32_bf16 v[72:75], v[224:227], v[128:131], v[72:75]\n\t" \
        "v_mfma_f32_16x16x32_bf16 v[76:79], v[228:231], v[128:131], v[76:79]\n\t" \
        "s_waitcnt vmcnt(0)\n\t" \
        "s_barrier\n\t" \
        "ds_read_b64_tr_b16 v[244:245], v6 offset:49152\n\t" \
        "v_mfma_f32_16x16x32_bf16 v[32:35], v[232:235], v[132:135], v[32:35]\n\t" \
        "ds_read_b64_tr_b16 v[246:247], v6 offset:49920\n\t" \
        "s_waitcnt lgkmcnt(4)\n\t" \
        "v_xor_b32 v172, v172, v14\n\t" \
        "v_mfma_f32_16x16x32_bf16 v[36:39], v[236:239], v[132:135], v[36:39]\n\t" \
        "ds_read_b64_tr_b16 v[248:249], v7 offset:49152\n\t" \
        "s_waitcnt lgkmcnt(4)\n\t" \
        "v_xor_b32 v176, v176, v14\n\t" \
        "v_mfma_f32_16x16x32_bf16 v[40:43], v[240:243], v[132:135], v[40:43]\n\t" \
        "ds_read_b64_tr_b16 v[250:251], v7 offset:49920\n\t" \
        "v_xor_b32 v174, v174, v14\n\t" \
        "v_mfma_f32_16x16x32_bf16 v[44:47], v[232:235], v[136:139], v[44:47]\n\t" \
        "ds_read_b64_tr_b16 v[252:253], v8 offset:49152\n\t" \
        "v_xor_b32 v178, v178, v14\n\t" \
        "v_mfma_f32_16x16x32_bf16 v[48:51], v[236:239], v[136:139], v[48:51]\n\t" \
        "ds_read_b64_tr_b16 v[254:255], v8 offset:49920\n\t" \
        "v_xor_b32 v173, v173, v14\n\t" \
        "v_mfma_f32_16x16x32_bf16 v[52:55], v[240:243], v[136:139], v[52:55]\n\t" \
        "ds_read_b128 v[128:131], v183 offset:128\n\t" \
        "v_xor_b32 v177, v177, v14\n\t" \
        "v_mfma_f32_16x16x32_bf16 v[56:59], v[232:235], v[140:143], v[56:59]\n\t" \
        "ds_read_b128 v[132:135], v148 offset:0\n\t" \
        "v_mfma_f32_16x16x32_bf16 v[60:63], v[236:239], v[140:143], v[60:63]\n\t" \
        "ds_read_b128 v[136:139], v148 offset:64\n\t" \
        "v_mfma_f32_16x16x32_bf16 v[64:67], v[240:243], v[140:143], v[64:67]\n\t" \
        "ds_read_b128 v[140:143], v148 offset:128\n\t" \
        "v_mfma_f32_16x16x32_bf16 v[68:71], v[232:235], v[144:147], v[68:71]\n\t" \
        "ds_read_b96 v[180:182], v15 offset:6656\n\t" \
        "v_mfma_f32_16x16x32_bf16 v[72:75], v[236:239], v[144:147], v[72:75]\n\t" \
        "v_mfma_f32_16x16x32_bf16 v[76:79], v[240:243], v[144:147], v[76:79]\n\t" \
        "ds_read_b128 v[144:147], v152 offset:0\n\t" \
        "s_waitcnt lgkmcnt(9)\n\t" \
        "v_mfma_f32_16x16x32_bf16 v[32:35], v[244:247], v[116:119], v[32:35]\n\t" \
        "s_waitcnt lgkmcnt(8)\n\t" \
        "v_mfma_f32_16x16x32_bf16 v[36:39], v[248:251], v[116:119], v[36:39]\n\t" \
        "s_waitcnt lgkmcnt(6)\n\t" \
        "v_mfma_f32_16x16x32_bf16 v[40:43], v[252:255], v[116:119], v[40:43]\n\t" \
        "ds_read_b128 v[116:119], v152 offset:64\n\t" \
        "v_mfma_f32_16x16x32_bf16 v[44:47], v[244:247], v[120:123], v[44:47]\n\t" \
        "s_waitcnt lgkmcnt(2)\n\t" \
        "v_xor_b32 v180, v180, v14\n\t" \
        "v_xor_b32 v182, v182, v14\n\t" \
        "v_mfma_f32_16x16x32_bf16 v[48:51], v[248:251], v[120:123], v[48:51]\n\t" \
        "v_xor_b32 v181, v181, v14\n\t" \
        "v_mfma_f32_16x16x32_bf16 v[52:55], v[252:255], v[120:123], v[52:55]\n\t" \
        "ds_read_b128 v[120:123], v152 offset:128\n\t" \
        "v_mfma_f32_16x16x32_bf16 v[56:59], v[244:247], v[124:127], v[56:59]\n\t" \
        "v_mfma_f32_16x16x32_bf16 v[60:63], v[248:251], v[124:127], v[60:63]\n\t" \
        "v_mfma_f32_16x16x32_bf16 v[64:67], v[252:255], v[124:127], v[64:67]\n\t" \
        "ds_read_b128 v[124:127], v156 offset:0\n\t" \
        "v_mfma_f32_16x16x32_bf16 v[68:71], v[244:247], v[128:131], v[68:71]\n\t" \
        "v_mfma_f32_16x16x32_bf16 v[72:75], v[248:251], v[128:131], v[72:75]\n\t" \
        "v_mfma_f32_16x16x32_bf16 v[76:79], v[252:255], v[128:131], v[76:79]\n\t" \
        "ds_read_b128 v[128:131], v156 offset:64\n\t" \
        "v_mfma_f32_16x16x32_bf16 v[80:83], a[0:3], v[132:135], 0\n\t" \
        "v_mfma_f32_16x16x32_bf16 v[84:87], a[4:7], v[132:135], 0\n\t" \
        "v_mfma_f32_16x16x32_bf16 v[88:91], a[8:11], v[132:135], 0\n\t" \
        "v_cvt_pk_bf16_f32 v40, v40, v41\n\t" \
        "v_cvt_pk_bf16_f32 v41, v42, v43\n\t" \
        "v_mfma_f32_16x16x32_bf16 v[80:83], a[12:15], v[136:139], v[80:83]\n\t" \
        "ds_write_b64 v11, v[40:41] offset:0\n\t" \
        "v_cvt_pk_bf16_f32 v52, v52, v53\n\t" \
        "v_mfma_f32_16x16x32_bf16 v[84:87], a[16:19], v[136:139], v[84:87]\n\t" \
        "v_cvt_pk_bf16_f32 v53, v54, v55\n\t" \
        "v_cvt_pk_bf16_f32 v64, v64, v65\n\t" \
        "v_mfma_f32_16x16x32_bf16 v[88:91], a[20:23], v[136:139], v[88:91]\n\t" \
        "ds_write_b64 v11, v[52:53] offset:2048\n\t" \
        "v_cvt_pk_bf16_f32 v65, v66, v67\n\t" \
        "v_mfma_f32_16x16x32_bf16 v[80:83], a[24:27], v[140:143], v[80:83]\n\t" \
        "ds_write_b64 v11, v[64:65] offset:4096\n\t" \
        "v_cvt_pk_bf16_f32 v76, v76, v77\n\t" \
        "v_mfma_f32_16x16x32_bf16 v[84:87], a[28:31], v[140:143], v[84:87]\n\t" \
        "ds_read_b128 v[132:135], v156 offset:128\n\t" \
        "v_cvt_pk_bf16_f32 v77, v78, v79\n\t" \
        "v_mfma_f32_16x16x32_bf16 v[88:91], a[32:35], v[140:143], v[88:91]\n\t" \
        "ds_write_b64 v11, v[76:77] offset:6144\n\t" \
        "v_cvt_pk_bf16_f32 v32, v32, v33\n\t" \
        "s_waitcnt lgkmcnt(6)\n\t" \
        "v_mfma_f32_16x16x32_bf16 v[80:83], a[36:39], v[144:147], v[80:83]\n\t" \
        "ds_read_b128 v[136:139], v160 offset:0\n\t" \
        "v_cvt_pk_bf16_f32 v33, v34, v35\n\t" \
        "v_mfma_f32_16x16x32_bf16 v[84:87], a[40:43], v[144:147], v[84:87]\n\t" \
        "ds_read_b128 v[140:143], v160 offset:64\n\t" \
        "v_cvt_pk_bf16_f32 v34, v36, v37\n\t" \
        "v_mfma_f32_16x16x32_bf16 v[88:91], a[44:47], v[144:147], v[88:91]\n\t" \
        "ds_read_b128 v[144:147], v160 offset:128\n\t" \
        "v_cvt_pk_bf16_f32 v35, v38, v39\n\t" \
        "v_mfma_f32_16x16x32_bf16 v[80:83], a[48:51], v[116:119], v[80:83]\n\t" \
        "v_permlane16_swap_b32 v32, v34\n\t" \
        "v_permlane16_swap_b32 v33, v35\n\t" \
        "v_mfma_f32_16x16x32_bf16 v[84:87], a[52:55], v[116:119], v[84:87]\n\t" \
        "v_mfma_f32_16x16x32_bf16 v[88:91], a[56:59], v[116:119], v[88:91]\n\t" \
        "ds_read_b128 v[116:119], v164 offset:0\n\t" \
        "v_mfma_f32_16x16x32_bf16 v[80:83], a[60:63], v[120:123], v[80:83]\n\t" \
        "v_mfma_f32_16x16x32_bf16 v[84:87], a[64:67], v[120:123], v[84:87]\n\t" \
        "v_mfma_f32_16x16x32_bf16 v[88:91], a[68:71], v[120:123], v[88:91]\n\t" \
        "ds_read_b128 v[120:123], v164 offset:64\n\t" \
        "v_mfma_f32_16x16x32_bf16 v[80:83], a[72:75], v[124:127], v[80:83]\n\t" \
        "v_mfma_f32_16x16x32_bf16 v[84:87], a[76:79], v[124:127], v[84:87]\n\t" \
        "s_waitcnt lgkmcnt(5)\n\t" \
        "s_barrier\n\t" \
        "ds_read_b64 v[42:43], v12 offset:0\n\t" \
        "v_mfma_f32_16x16x32_bf16 v[88:91], a[80:83], v[124:127], v[88:91]\n\t" \
        "ds_read_b128 v[124:127], v164 offset:128\n\t" \
        "v_mfma_f32_16x16x32_bf16 v[80:83], a[84:87], v[128:131], v[80:83]\n\t" \
        "v_mfma_f32_16x16x32_bf16 v[84:87], a[88:91], v[128:131], v[84:87]\n\t" \
        "v_mfma_f32_16x16x32_bf16 v[88:91], a[92:95], v[128:131], v[88:91]\n\t" \
        "ds_read_b128 v[128:131], v168 offset:0\n\t" \
        "v_mfma_f32_16x16x32_bf16 v[80:83], a[96:99], v[132:135], v[80:83]\n\t" \
        "s_mov_b32 s56, s64\n\t" \
        "buffer_store_dwordx4 v[32:35], v9, s[28:31], s56 offen sc1\n\t" \
        "v_mfma_f32_16x16x32_bf16 v[84:87], a[100:103], v[132:135], v[84:87]\n\t" \
        "v_mfma_f32_16x16x32_bf16 v[88:91], a[104:107], v[132:135], v[88:91]\n\t" \
        "ds_read_b128 v[132:135], v168 offset:64\n\t" \
        "s_waitcnt lgkmcnt(5)\n\t" \
        "v_mfma_f32_16x16x32_bf16 v[80:83], a[108:111], v[136:139], v[80:83]\n\t" \
        "s_waitcnt lgkmcnt(3)\n\t" \
        "v_permlane16_swap_b32 v40, v42\n\t" \
        "v_permlane16_swap_b32 v41, v43\n\t" \
        "v_mfma_f32_16x16x32_bf16 v[84:87], a[112:115], v[136:139], v[84:87]\n\t" \
        "v_mfma_f32_16x16x32_bf16 v[88:91], a[116:119], v[136:139], v[88:91]\n\t" \
        "ds_read_b128 v[136:139], v168 offset:128\n\t" \
        "v_mfma_f32_16x16x32_bf16 v[80:83], a[120:123], v[140:143], v[80:83]\n\t" \
        "v_mfma_f32_16x16x32_bf16 v[84:87], a[124:127], v[140:143], v[84:87]\n\t" \
        "s_mov_b32 s56, s64\n\t" \
        "buffer_store_dwordx4 v[40:43], v10, s[28:31], s56 offen sc1\n\t" \
        "v_mfma_f32_16x16x32_bf16 v[88:91], a[128:131], v[140:143], v[88:91]\n\t" \
        "ds_read_b128 v[140:143], v172 offset:0\n\t" \
        "v_cvt_pk_bf16_f32 v44, v44, v45\n\t" \
        "v_mfma_f32_16x16x32_bf16 v[80:83], a[132:135], v[144:147], v[80:83]\n\t" \
        "v_cvt_pk_bf16_f32 v45, v46, v47\n\t" \
        "v_cvt_pk_bf16_f32 v46, v48, v49\n\t" \
        "v_mfma_f32_16x16x32_bf16 v[84:87], a[136:139], v[144:147], v[84:87]\n\t" \
        "v_cvt_pk_bf16_f32 v47, v50, v51\n\t" \
        "v_permlane16_swap_b32 v44, v46\n\t" \
        "v_mfma_f32_16x16x32_bf16 v[88:91], a[140:143], v[144:147], v[88:91]\n\t" \
        "ds_read_b128 v[144:147], v172 offset:64\n\t" \
        "v_permlane16_swap_b32 v45, v47\n\t" \
        "v_mfma_f32_16x16x32_bf16 v[80:83], a[144:147], v[116:119], v[80:83]\n\t" \
        "ds_read_b64 v[54:55], v12 offset:2048\n\t" \
        "v_mfma_f32_16x16x32_bf16 v[84:87], a[148:151], v[116:119], v[84:87]\n\t" \
        "v_mfma_f32_16x16x32_bf16 v[88:91], a[152:155], v[116:119], v[88:91]\n\t" \
        "ds_read_b128 v[116:119], v172 offset:128\n\t" \
        "v_mfma_f32_16x16x32_bf16 v[80:83], a[156:159], v[120:123], v[80:83]\n\t" \
        "v_mfma_f32_16x16x32_bf16 v[84:87], a[160:163], v[120:123], v[84:87]\n\t" \
        "v_mfma_f32_16x16x32_bf16 v[88:91], a[164:167], v[120:123], v[88:91]\n\t" \
        "ds_read_b128 v[120:123], v176 offset:0\n\t" \
        "s_waitcnt lgkmcnt(5)\n\t" \
        "v_mfma_f32_16x16x32_bf16 v[80:83], a[168:171], v[124:127], v[80:83]\n\t" \
        "v_mfma_f32_16x16x32_bf16 v[84:87], a[172:175], v[124:127], v[84:87]\n\t" \
        "v_mfma_f32_16x16x32_bf16 v[88:91], a[176:179], v[124:127], v[88:91]\n\t" \
        "ds_read_b128 v[124:127], v176 offset:64\n\t" \
        "s_waitcnt lgkmcnt(3)\n\t" \
        "v_permlane16_swap_b32 v52, v54\n\t" \
        "v_mfma_f32_16x16x32_bf16 v[80:83], a[180:183], v[128:131], v[80:83]\n\t" \
        "v_permlane16_swap_b32 v53, v55\n\t" \
        "v_mfma_f32_16x16x32_bf16 v[84:87], a[184:187], v[128:131], v[84:87]\n\t" \
        "v_mfma_f32_16x16x32_bf16 v[88:91], a[188:191], v[128:131], v[88:91]\n\t" \
        "ds_read_b128 v[128:131], v176 offset:128\n\t" \
        "v_mfma_f32_16x16x32_bf16 v[80:83], a[192:195], v[132:135], v[80:83]\n\t" \
        "v_mfma_f32_16x16x32_bf16 v[84:87], a[196:199], v[132:135], v[84:87]\n\t" \
        "v_mfma_f32_16x16x32_bf16 v[88:91], a[200:203], v[132:135], v[88:91]\n\t" \
        "ds_read_b128 v[132:135], v180 offset:0\n\t" \
        "v_mfma_f32_16x16x32_bf16 v[80:83], a[204:207], v[136:139], v[80:83]\n\t" \
        "v_mfma_f32_16x16x32_bf16 v[84:87], a[208:211], v[136:139], v[84:87]\n\t" \
        "v_mfma_f32_16x16x32_bf16 v[88:91], a[212:215], v[136:139], v[88:91]\n\t" \
        "ds_read_b128 v[136:139], v180 offset:64\n\t" \
        "v_mfma_f32_16x16x32_bf16 v[80:83], a[216:219], v[140:143], v[80:83]\n\t" \
        "s_add_u32 s56, s64, 0x15000\n\t" \
        "buffer_store_dwordx4 v[44:47], v9, s[28:31], s56 offen sc1\n\t" \
        "v_mfma_f32_16x16x32_bf16 v[84:87], a[220:223], v[140:143], v[84:87]\n\t" \
        "v_mfma_f32_16x16x32_bf16 v[88:91], a[224:227], v[140:143], v[88:91]\n\t" \
        "ds_read_b128 v[140:143], v180 offset:128\n\t" \
        "v_mfma_f32_16x16x32_bf16 v[80:83], a[228:231], v[144:147], v[80:83]\n\t" \
        "v_mfma_f32_16x16x32_bf16 v[84:87], a[232:235], v[144:147], v[84:87]\n\t" \
        "v_mfma_f32_16x16x32_bf16 v[88:91], a[236:239], v[144:147], v[88:91]\n\t" \
        "ds_read_b128 v[144:147], v149 offset:0\n\t" \
        "s_waitcnt lgkmcnt(4)\n\t" \
        "v_mfma_f32_16x16x32_bf16 v[80:83], a[240:243], v[116:119], v[80:83]\n\t" \
        "v_mfma_f32_16x16x32_bf16 v[84:87], a[244:247], v[116:119], v[84:87]\n\t" \
        "s_add_u32 s56, s64, 0x15000\n\t" \
        "buffer_store_dwordx4 v[52:55], v10, s[28:31], s56 offen sc1\n\t" \
        "v_mfma_f32_16x16x32_bf16 v[88:91], a[248:251], v[116:119], v[88:91]\n\t" \
        "ds_read_b128 v[116:119], v149 offset:64\n\t" \
        "v_cvt_pk_bf16_f32 v56, v56, v57\n\t" \
        "v_mfma_f32_16x16x32_bf16 v[80:83], a[252:255], v[120:123], v[80:83]\n\t" \
        "v_cvt_pk_bf16_f32 v57, v58, v59\n\t" \
        "v_cvt_pk_bf16_f32 v58, v60, v61\n\t" \
        "v_mfma_f32_16x16x32_bf16 v[84:87], v[188:191], v[120:123], v[84:87]\n\t" \
        "v_cvt_pk_bf16_f32 v59, v62, v63\n\t" \
        "v_permlane16_swap_b32 v56, v58\n\t" \
        "v_mfma_f32_16x16x32_bf16 v[88:91], v[192:195], v[120:123], v[88:91]\n\t" \
        "ds_read_b128 v[120:123], v149 offset:128\n\t" \
        "v_permlane16_swap_b32 v57, v59\n\t" \
        "v_mfma_f32_16x16x32_bf16 v[80:83], v[196:199], v[124:127], v[80:83]\n\t" \
        "ds_read_b64 v[66:67], v12 offset:4096\n\t" \
        "v_mfma_f32_16x16x32_bf16 v[84:87], v[200:203], v[124:127], v[84:87]\n\t" \
        "v_mfma_f32_16x16x32_bf16 v[88:91], v[204:207], v[124:127], v[88:91]\n\t" \
        "ds_read_b128 v[124:127], v153 offset:0\n\t" \
        "v_mfma_f32_16x16x32_bf16 v[80:83], v[208:211], v[128:131], v[80:83]\n\t" \
        "v_mfma_f32_16x16x32_bf16 v[84:87], v[212:215], v[128:131], v[84:87]\n\t" \
        "v_mfma_f32_16x16x32_bf16 v[88:91], v[216:219], v[128:131], v[88:91]\n\t" \
        "ds_read_b128 v[128:131], v153 offset:64\n\t" \
        "s_waitcnt lgkmcnt(5)\n\t" \
        "v_mfma_f32_16x16x32_bf16 v[80:83], v[220:223], v[132:135], v[80:83]\n\t" \
        "v_mfma_f32_16x16x32_bf16 v[84:87], v[224:227], v[132:135], v[84:87]\n\t" \
        "v_mfma_f32_16x16x32_bf16 v[88:91], v[228:231], v[132:135], v[88:91]\n\t" \
        "ds_read_b128 v[132:135], v153 offset:128\n\t" \
        "s_waitcnt lgkmcnt(3)\n\t" \
        "v_permlane16_swap_b32 v64, v66\n\t" \
        "v_mfma_f32_16x16x32_bf16 v[80:83], v[232:235], v[136:139], v[80:83]\n\t" \
        "v_permlane16_swap_b32 v65, v67\n\t" \
        "v_mfma_f32_16x16x32_bf16 v[84:87], v[236:239], v[136:139], v[84:87]\n\t" \
        "v_mfma_f32_16x16x32_bf16 v[88:91], v[240:243], v[136:139], v[88:91]\n\t" \
        "ds_read_b128 v[136:139], v157 offset:0\n\t" \
        "v_mfma_f32_16x16x32_bf16 v[80:83], v[244:247], v[140:143], v[80:83]\n\t" \
        "v_mfma_f32_16x16x32_bf16 v[84:87], v[248:251], v[140:143], v[84:87]\n\t" \
        "v_mfma_f32_16x16x32_bf16 v[88:91], v[252:255], v[140:143], v[88:91]\n\t" \
        "s_bitcmp0_b32 s34, 1\n\t" \
        "s_cbranch_scc1 .LT12%=\n\t" \
        "ds_read_b128 v[140:143], v157 offset:64\n\t" \
        "v_mfma_f32_16x16x32_bf16 v[92:95], a[0:3], v[144:147], 0\n\t" \
        "v_mfma_f32_16x16x32_bf16 v[96:99], a[4:7], v[144:147], 0\n\t" \
        "v_mfma_f32_16x16x32_bf16 v[100:103], a[8:11], v[144:147], 0\n\t" \
        "s_nop 3\n\t" \
        "v_cvt_pk_bf16_f32 v88, v88, v89\n\t" \
        "v_cvt_pk_bf16_f32 v89, v90, v91\n\t" \
        "v_mfma_f32_16x16x32_bf16 v[92:95], a[12:15], v[116:119], v[92:95]\n\t" \
        "s_add_u32 s56, s64, 0x2a000\n\t" \
        "buffer_store_dwordx4 v[56:59], v9, s[28:31], s56 offen sc1\n\t" \
        "v_mfma_f32_16x16x32_bf16 v[96:99], a[16:19], v[116:119], v[96:99]\n\t" \
        "ds_write_b64 v11, v[88:89] offset:8192\n\t" \
        "v_cvt_pk_bf16_f32 v80, v80, v81\n\t" \
        "v_mfma_f32_16x16x32_bf16 v[100:103], a[20:23], v[116:119], v[100:103]\n\t" \
        "ds_read_b128 v[144:147], v157 offset:128\n\t" \
        "v_cvt_pk_bf16_f32 v81, v82, v83\n\t" \
        "v_mfma_f32_16x16x32_bf16 v[92:95], a[24:27], v[120:123], v[92:95]\n\t" \
        "ds_read_b128 v[116:119], v161 offset:0\n\t" \
        "v_cvt_pk_bf16_f32 v82, v84, v85\n\t" \
        "v_mfma_f32_16x16x32_bf16 v[96:99], a[28:31], v[120:123], v[96:99]\n\t" \
        "v_cvt_pk_bf16_f32 v83, v86, v87\n\t" \
        "v_permlane16_swap_b32 v80, v82\n\t" \
        "v_mfma_f32_16x16x32_bf16 v[100:103], a[32:35], v[120:123], v[100:103]\n\t" \
        "ds_read_b128 v[120:123], v161 offset:64\n\t" \
        "v_permlane16_swap_b32 v81, v83\n\t" \
        "s_waitcnt lgkmcnt(5)\n\t" \
        "v_mfma_f32_16x16x32_bf16 v[92:95], a[36:39], v[124:127], v[92:95]\n\t" \
        "v_mfma_f32_16x16x32_bf16 v[96:99], a[40:43], v[124:127], v[96:99]\n\t" \
        "s_add_u32 s56, s64, 0x2a000\n\t" \
        "buffer_store_dwordx4 v[64:67], v10, s[28:31], s56 offen sc1\n\t" \
        "v_mfma_f32_16x16x32_bf16 v[100:103], a[44:47], v[124:127], v[100:103]\n\t" \
        "ds_read_b128 v[124:127], v161 offset:128\n\t" \
        "v_cvt_pk_bf16_f32 v68, v68, v69\n\t" \
        "v_mfma_f32_16x16x32_bf16 v[92:95], a[48:51], v[128:131], v[92:95]\n\t" \
        "v_cvt_pk_bf16_f32 v69, v70, v71\n\t" \
        "v_cvt_pk_bf16_f32 v70, v72, v73\n\t" \
        "v_mfma_f32_16x16x32_bf16 v[96:99], a[52:55], v[128:131], v[96:99]\n\t" \
        "s_waitcnt lgkmcnt(4)\n\t" \
        "s_barrier\n\t" \
        "ds_read_b64 v[90:91], v12 offset:8192\n\t" \
        "v_cvt_pk_bf16_f32 v71, v74, v75\n\t" \
        "v_mfma_f32_16x16x32_bf16 v[100:103], a[56:59], v[128:131], v[100:103]\n\t" \
        "ds_read_b128 v[128:131], v165 offset:0\n\t" \
        "v_permlane16_swap_b32 v68, v70\n\t" \
        "v_mfma_f32_16x16x32_bf16 v[92:95], a[60:63], v[132:135], v[92:95]\n\t" \
        "ds_read_b64 v[78:79], v12 offset:6144\n\t" \
        "v_permlane16_swap_b32 v69, v71\n\t" \
        "v_mfma_f32_16x16x32_bf16 v[96:99], a[64:67], v[132:135], v[96:99]\n\t" \
        "v_mfma_f32_16x16x32_bf16 v[100:103], a[68:71], v[132:135], v[100:103]\n\t" \
        "ds_read_b128 v[132:135], v165 offset:64\n\t" \
        "v_mfma_f32_16x16x32_bf16 v[92:95], a[72:75], v[136:139], v[92:95]\n\t" \
        "v_mfma_f32_16x16x32_bf16 v[96:99], a[76:79], v[136:139], v[96:99]\n\t" \
        "v_mfma_f32_16x16x32_bf16 v[100:103], a[80:83], v[136:139], v[100:103]\n\t" \
        "ds_read_b128 v[136:139], v165 offset:128\n\t" \
        "v_mfma_f32_16x16x32_bf16 v[92:95], a[84:87], v[140:143], v[92:95]\n\t" \
        "s_waitcnt lgkmcnt(4)\n\t" \
        "v_permlane16_swap_b32 v88, v90\n\t" \
        "v_permlane16_swap_b32 v89, v91\n\t" \
        "v_mfma_f32_16x16x32_bf16 v[96:99], a[88:91], v[140:143], v[96:99]\n\t" \
        "v_mfma_f32_16x16x32_bf16 v[100:103], a[92:95], v[140:143], v[100:103]\n\t" \
        "ds_read_b128 v[140:143], v169 offset:0\n\t" \
        "s_waitcnt lgkmcnt(3)\n\t" \
        "v_permlane16_swap_b32 v76, v78\n\t" \
        "v_mfma_f32_16x16x32_bf16 v[92:95], a[96:99], v[144:147], v[92:95]\n\t" \
        "s_add_u32 s56, s64, 0x54000\n\t" \
        "buffer_store_dwordx4 v[80:83], v9, s[28:31], s56 offen sc1\n\t" \
        "v_mfma_f32_16x16x32_bf16 v[96:99], a[100:103], v[144:147], v[96:99]\n\t" \
        "v_permlane16_swap_b32 v77, v79\n\t" \
        "v_mfma_f32_16x16x32_bf16 v[100:103], a[104:107], v[144:147], v[100:103]\n\t" \
        "ds_read_b128 v[144:147], v169 offset:64\n\t" \
        "v_mfma_f32_16x16x32_bf16 v[92:95], a[108:111], v[116:119], v[92:95]\n\t" \
        "v_mfma_f32_16x16x32_bf16 v[96:99], a[112:115], v[116:119], v[96:99]\n\t" \
        "v_mfma_f32_16x16x32_bf16 v[100:103], a[116:119], v[116:119], v[100:103]\n\t" \
        "ds_read_b128 v[116:119], v169 offset:128\n\t" \
        "v_mfma_f32_16x16x32_bf16 v[92:95], a[120:123], v[120:123], v[92:95]\n\t" \
        "v_mfma_f32_16x16x32_bf16 v[96:99], a[124:127], v[120:123], v[96:99]\n\t" \
        "s_add_u32 s56, s64, 0x54000\n\t" \
        "buffer_store_dwordx4 v[88:91], v10, s[28:31], s56 offen sc1\n\t" \
        "v_mfma_f32_16x16x32_bf16 v[100:103], a[128:131], v[120:123], v[100:103]\n\t" \
        "ds_read_b128 v[120:123], v173 offset:0\n\t" \
        "v_mfma_f32_16x16x32_bf16 v[92:95], a[132:135], v[124:127], v[92:95]\n\t" \
        "s_add_u32 s56, s64, 0x3f000\n\t" \
        "buffer_store_dwordx4 v[68:71], v9, s[28:31], s56 offen sc1\n\t" \
        "v_mfma_f32_16x16x32_bf16 v[96:99], a[136:139], v[124:127], v[96:99]\n\t" \
        "v_mfma_f32_16x16x32_bf16 v[100:103], a[140:143], v[124:127], v[100:103]\n\t" \
        "ds_read_b128 v[124:127], v173 offset:64\n\t" \
        "v_mfma_f32_16x16x32_bf16 v[92:95], a[144:147], v[128:131], v[92:95]\n\t" \
        "v_mfma_f32_16x16x32_bf16 v[96:99], a[148:151], v[128:131], v[96:99]\n\t" \
        "v_mfma_f32_16x16x32_bf16 v[100:103], a[152:155], v[128:131], v[100:103]\n\t" \
        "ds_read_b128 v[128:131], v173 offset:128\n\t" \
        "s_waitcnt lgkmcnt(4)\n\t" \
        "v_mfma_f32_16x16x32_bf16 v[92:95], a[156:159], v[132:135], v[92:95]\n\t" \
        "v_mfma_f32_16x16x32_bf16 v[96:99], a[160:163], v[132:135], v[96:99]\n\t" \
        "s_add_u32 s56, s64, 0x3f000\n\t" \
        "buffer_store_dwordx4 v[76:79], v10, s[28:31], s56 offen sc1\n\t" \
        "v_mfma_f32_16x16x32_bf16 v[100:103], a[164:167], v[132:135], v[100:103]\n\t" \
        "ds_read_b128 v[132:135], v177 offset:0\n\t" \
        "v_mfma_f32_16x16x32_bf16 v[92:95], a[168:171], v[136:139], v[92:95]\n\t" \
        "v_mfma_f32_16x16x32_bf16 v[96:99], a[172:175], v[136:139], v[96:99]\n\t" \
        "v_mfma_f32_16x16x32_bf16 v[100:103], a[176:179], v[136:139], v[100:103]\n\t" \
        "ds_read_b128 v[136:139], v177 offset:64\n\t" \
        "v_mfma_f32_16x16x32_bf16 v[92:95], a[180:183], v[140:143], v[92:95]\n\t" \
        "v_mfma_f32_16x16x32_bf16 v[96:99], a[184:187], v[140:143], v[96:99]\n\t" \
        "v_mfma_f32_16x16x32_bf16 v[100:103], a[188:191], v[140:143], v[100:103]\n\t" \
        "ds_read_b128 v[140:143], v177 offset:128\n\t" \
        "v_mfma_f32_16x16x32_bf16 v[92:95], a[192:195], v[144:147], v[92:95]\n\t" \
        "v_mfma_f32_16x16x32_bf16 v[96:99], a[196:199], v[144:147], v[96:99]\n\t" \
        "v_mfma_f32_16x16x32_bf16 v[100:103], a[200:203], v[144:147], v[100:103]\n\t" \
        "ds_read_b128 v[144:147], v181 offset:0\n\t" \
        "s_waitcnt lgkmcnt(4)\n\t" \
        "v_mfma_f32_16x16x32_bf16 v[92:95], a[204:207], v[116:119], v[92:95]\n\t" \
        "v_mfma_f32_16x16x32_bf16 v[96:99], a[208:211], v[116:119], v[96:99]\n\t" \
        "v_mfma_f32_16x16x32_bf16 v[100:103], a[212:215], v[116:119], v[100:103]\n\t" \
        "ds_read_b128 v[116:119], v181 offset:64\n\t" \
        "v_mfma_f32_16x16x32_bf16 v[92:95], a[216:219], v[120:123], v[92:95]\n\t" \
        "v_mfma_f32_16x16x32_bf16 v[96:99], a[220:223], v[120:123], v[96:99]\n\t" \
        "v_mfma_f32_16x16x32_bf16 v[100:103], a[224:227], v[120:123], v[100:103]\n\t" \
        "ds_read_b128 v[120:123], v181 offset:128\n\t" \
        "v_mfma_f32_16x16x32_bf16 v[92:95], a[228:231], v[124:127], v[92:95]\n\t" \
        "v_mfma_f32_16x16x32_bf16 v[96:99], a[232:235], v[124:127], v[96:99]\n\t" \
        "v_mfma_f32_16x16x32_bf16 v[100:103], a[236:239], v[124:127], v[100:103]\n\t" \
        "ds_read_b128 v[124:127], v150 offset:0\n\t" \
        "v_mfma_f32_16x16x32_bf16 v[92:95], a[240:243], v[128:131], v[92:95]\n\t" \
        "v_mfma_f32_16x16x32_bf16 v[96:99], a[244:247], v[128:131], v[96:99]\n\t" \
        "v_mfma_f32_16x16x32_bf16 v[100:103], a[248:251], v[128:131], v[100:103]\n\t" \
        "ds_read_b128 v[128:131], v150 offset:64\n\t" \
        "s_waitcnt lgkmcnt(4)\n\t" \
        "v_mfma_f32_16x16x32_bf16 v[92:95], a[252:255], v[132:135], v[92:95]\n\t" \
        "v_mfma_f32_16x16x32_bf16 v[96:99], v[188:191], v[132:135], v[96:99]\n\t" \
        "v_mfma_f32_16x16x32_bf16 v[100:103], v[192:195], v[132:135], v[100:103]\n\t" \
        "ds_read_b128 v[132:135], v150 offset:128\n\t" \
        "v_mfma_f32_16x16x32_bf16 v[92:95], v[196:199], v[136:139], v[92:95]\n\t" \
        "v_mfma_f32_16x16x32_bf16 v[96:99], v[200:203], v[136:139], v[96:99]\n\t" \
        "v_mfma_f32_16x16x32_bf16 v[100:103], v[204:207], v[136:139], v[100:103]\n\t" \
        "ds_read_b128 v[136:139], v154 offset:0\n\t" \
        "v_mfma_f32_16x16x32_bf16 v[92:95], v[208:211], v[140:143], v[92:95]\n\t" \
        "v_mfma_f32_16x16x32_bf16 v[96:99], v[212:215], v[140:143], v[96:99]\n\t" \
        "v_mfma_f32_16x16x32_bf16 v[100:103], v[216:219], v[140:143], v[100:103]\n\t" \
        "ds_read_b128 v[140:143], v154 offset:64\n\t" \
        "v_mfma_f32_16x16x32_bf16 v[92:95], v[220:223], v[144:147], v[92:95]\n\t" \
        "v_mfma_f32_16x16x32_bf16 v[96:99], v[224:227], v[144:147], v[96:99]\n\t" \
        "v_mfma_f32_16x16x32_bf16 v[100:103], v[228:231], v[144:147], v[100:103]\n\t" \
        "ds_read_b128 v[144:147], v154 offset:128\n\t" \
        "s_waitcnt lgkmcnt(4)\n\t" \
        "v_mfma_f32_16x16x32_bf16 v[92:95], v[232:235], v[116:119], v[92:95]\n\t" \
        "v_mfma_f32_16x16x32_bf16 v[96:99], v[236:239], v[116:119], v[96:99]\n\t" \
        "v_mfma_f32_16x16x32_bf16 v[100:103], v[240:243], v[116:119], v[100:103]\n\t" \
        "ds_read_b128 v[116:119], v158 offset:0\n\t" \
        "v_mfma_f32_16x16x32_bf16 v[92:95], v[244:247], v[120:123], v[92:95]\n\t" \
        "v_mfma_f32_16x16x32_bf16 v[96:99], v[248:251], v[120:123], v[96:99]\n\t" \
        "v_mfma_f32_16x16x32_bf16 v[100:103], v[252:255], v[120:123], v[100:103]\n\t" \
        "ds_read_b128 v[120:123], v158 offset:64\n\t" \
        "v_mfma_f32_16x16x32_bf16 v[104:107], a[0:3], v[124:127], 0\n\t" \
        "v_mfma_f32_16x16x32_bf16 v[108:111], a[4:7], v[124:127], 0\n\t" \
        "v_mfma_f32_16x16x32_bf16 v[112:115], a[8:11], v[124:127], 0\n\t" \
        "s_nop 5\n\t" \
        "v_cvt_pk_bf16_f32 v100, v100, v101\n\t" \
        "v_cvt_pk_bf16_f32 v101, v102, v103\n\t" \
        "v_mfma_f32_16x16x32_bf16 v[104:107], a[12:15], v[128:131], v[104:107]\n\t" \
        "ds_write_b64 v11, v[100:101] offset:10240\n\t" \
        "v_cvt_pk_bf16_f32 v92, v92, v93\n\t" \
        "v_mfma_f32_16x16x32_bf16 v[108:111], a[16:19], v[128:131], v[108:111]\n\t" \
        "ds_read_b128 v[124:127], v158 offset:128\n\t" \
        "v_cvt_pk_bf16_f32 v93, v94, v95\n\t" \
        "v_mfma_f32_16x16x32_bf16 v[112:115], a[20:23], v[128:131], v[112:115]\n\t" \
        "ds_read_b128 v[128:131], v162 offset:0\n\t" \
        "v_cvt_pk_bf16_f32 v94, v96, v97\n\t" \
        "s_waitcnt lgkmcnt(5)\n\t" \
        "v_mfma_f32_16x16x32_bf16 v[104:107], a[24:27], v[132:135], v[104:107]\n\t" \
        "v_cvt_pk_bf16_f32 v95, v98, v99\n\t" \
        "v_permlane16_swap_b32 v92, v94\n\t" \
        "v_mfma_f32_16x16x32_bf16 v[108:111], a[28:31], v[132:135], v[108:111]\n\t" \
        "v_permlane16_swap_b32 v93, v95\n\t" \
        "v_mfma_f32_16x16x32_bf16 v[112:115], a[32:35], v[132:135], v[112:115]\n\t" \
        "ds_read_b128 v[132:135], v162 offset:64\n\t" \
        "v_mfma_f32_16x16x32_bf16 v[104:107], a[36:39], v[136:139], v[104:107]\n\t" \
        "v_mfma_f32_16x16x32_bf16 v[108:111], a[40:43], v[136:139], v[108:111]\n\t" \
        "v_mfma_f32_16x16x32_bf16 v[112:115], a[44:47], v[136:139], v[112:115]\n\t" \
        "ds_read_b128 v[136:139], v162 offset:128\n\t" \
        "v_mfma_f32_16x16x32_bf16 v[104:107], a[48:51], v[140:143], v[104:107]\n\t" \
        "v_mfma_f32_16x16x32_bf16 v[108:111], a[52:55], v[140:143], v[108:111]\n\t" \
        "s_waitcnt lgkmcnt(4)\n\t" \
        "s_barrier\n\t" \
        "ds_read_b64 v[102:103], v12 offset:10240\n\t" \
        "v_mfma_f32_16x16x32_bf16 v[112:115], a[56:59], v[140:143], v[112:115]\n\t" \
        "ds_read_b128 v[140:143], v166 offset:0\n\t" \
        "v_mfma_f32_16x16x32_bf16 v[104:107], a[60:63], v[144:147], v[104:107]\n\t" \
        "v_mfma_f32_16x16x32_bf16 v[108:111], a[64:67], v[144:147], v[108:111]\n\t" \
        "v_mfma_f32_16x16x32_bf16 v[112:115], a[68:71], v[144:147], v[112:115]\n\t" \
        "ds_read_b128 v[144:147], v166 offset:64\n\t" \
        "v_mfma_f32_16x16x32_bf16 v[104:107], a[72:75], v[116:119], v[104:107]\n\t" \
        "v_mfma_f32_16x16x32_bf16 v[108:111], a[76:79], v[116:119], v[108:111]\n\t" \
        "v_mfma_f32_16x16x32_bf16 v[112:115], a[80:83], v[116:119], v[112:115]\n\t" \
        "ds_read_b128 v[116:119], v166 offset:128\n\t" \
        "v_mfma_f32_16x16x32_bf16 v[104:107], a[84:87], v[120:123], v[104:107]\n\t" \
        "s_waitcnt lgkmcnt(3)\n\t" \
        "v_permlane16_swap_b32 v100, v102\n\t" \
        "v_permlane16_swap_b32 v101, v103\n\t" \
        "v_mfma_f32_16x16x32_bf16 v[108:111], a[88:91], v[120:123], v[108:111]\n\t" \
        "v_mfma_f32_16x16x32_bf16 v[112:115], a[92:95], v[120:123], v[112:115]\n\t" \
        "ds_read_b128 v[120:123], v170 offset:0\n\t" \
        "v_mfma_f32_16x16x32_bf16 v[104:107], a[96:99], v[124:127], v[104:107]\n\t" \
        "s_add_u32 s56, s64, 0x69000\n\t" \
        "buffer_store_dwordx4 v[92:95], v9, s[28:31], s56 offen sc1\n\t" \
        "v_mfma_f32_16x16x32_bf16 v[108:111], a[100:103], v[124:127], v[108:111]\n\t" \
        "v_mfma_f32_16x16x32_bf16 v[112:115], a[104:107], v[124:127], v[112:115]\n\t" \
        "ds_read_b128 v[124:127], v170 offset:64\n\t" \
        "v_mfma_f32_16x16x32_bf16 v[104:107], a[108:111], v[128:131], v[104:107]\n\t" \
        "v_mfma_f32_16x16x32_bf16 v[108:111], a[112:115], v[128:131], v[108:111]\n\t" \
        "v_mfma_f32_16x16x32_bf16 v[112:115], a[116:119], v[128:131], v[112:115]\n\t" \
        "ds_read_b128 v[128:131], v170 offset:128\n\t" \
        "v_mfma_f32_16x16x32_bf16 v[104:107], a[120:123], v[132:135], v[104:107]\n\t" \
        "v_mfma_f32_16x16x32_bf16 v[108:111], a[124:127], v[132:135], v[108:111]\n\t" \
        "s_add_u32 s56, s64, 0x69000\n\t" \
        "buffer_store_dwordx4 v[100:103], v10, s[28:31], s56 offen sc1\n\t" \
        "v_mfma_f32_16x16x32_bf16 v[112:115], a[128:131], v[132:135], v[112:115]\n\t" \
        "ds_read_b128 v[132:135], v174 offset:0\n\t" \
        "v_mfma_f32_16x16x32_bf16 v[104:107], a[132:135], v[136:139], v[104:107]\n\t" \
        "v_mfma_f32_16x16x32_bf16 v[108:111], a[136:139], v[136:139], v[108:111]\n\t" \
        "v_mfma_f32_16x16x32_bf16 v[112:115], a[140:143], v[136:139], v[112:115]\n\t" \
        "ds_read_b128 v[136:139], v174 offset:64\n\t" \
        "s_waitcnt lgkmcnt(4)\n\t" \
        "v_mfma_f32_16x16x32_bf16 v[104:107], a[144:147], v[140:143], v[104:107]\n\t" \
        "v_mfma_f32_16x16x32_bf16 v[108:111], a[148:151], v[140:143], v[108:111]\n\t" \
        "v_mfma_f32_16x16x32_bf16 v[112:115], a[152:155], v[140:143], v[112:115]\n\t" \
        "ds_read_b128 v[140:143], v174 offset:128\n\t" \
        "v_mfma_f32_16x16x32_bf16 v[104:107], a[156:159], v[144:147], v[104:107]\n\t" \
        "v_mfma_f32_16x16x32_bf16 v[108:111], a[160:163], v[144:147], v[108:111]\n\t" \
        "v_mfma_f32_16x16x32_bf16 v[112:115], a[164:167], v[144:147], v[112:115]\n\t" \
        "ds_read_b128 v[144:147], v178 offset:0\n\t" \
        "v_mfma_f32_16x16x32_bf16 v[104:107], a[168:171], v[116:119], v[104:107]\n\t" \
        "v_mfma_f32_16x16x32_bf16 v[108:111], a[172:175], v[116:119], v[108:111]\n\t" \
        "v_mfma_f32_16x16x32_bf16 v[112:115], a[176:179], v[116:119], v[112:115]\n\t" \
        "ds_read_b128 v[116:119], v178 offset:64\n\t" \
        "v_mfma_f32_16x16x32_bf16 v[104:107], a[180:183], v[120:123], v[104:107]\n\t" \
        "v_mfma_f32_16x16x32_bf16 v[108:111], a[184:187], v[120:123], v[108:111]\n\t" \
        "v_mfma_f32_16x16x32_bf16 v[112:115], a[188:191], v[120:123], v[112:115]\n\t" \
        "ds_read_b128 v[120:123], v178 offset:128\n\t" \
        "s_waitcnt lgkmcnt(4)\n\t" \
        "v_mfma_f32_16x16x32_bf16 v[104:107], a[192:195], v[124:127], v[104:107]\n\t" \
        "v_mfma_f32_16x16x32_bf16 v[108:111], a[196:199], v[124:127], v[108:111]\n\t" \
        "v_mfma_f32_16x16x32_bf16 v[112:115], a[200:203], v[124:127], v[112:115]\n\t" \
        "ds_read_b128 v[124:127], v182 offset:0\n\t" \
        "v_mfma_f32_16x16x32_bf16 v[104:107], a[204:207], v[128:131], v[104:107]\n\t" \
        "v_mfma_f32_16x16x32_bf16 v[108:111], a[208:211], v[128:131], v[108:111]\n\t" \
        "v_mfma_f32_16x16x32_bf16 v[112:115], a[212:215], v[128:131], v[112:115]\n\t" \
        "ds_read_b128 v[128:131], v182 offset:64\n\t" \
        "v_mfma_f32_16x16x32_bf16 v[104:107], a[216:219], v[132:135], v[104:107]\n\t" \
        "v_mfma_f32_16x16x32_bf16 v[108:111], a[220:223], v[132:135], v[108:111]\n\t" \
        "v_mfma_f32_16x16x32_bf16 v[112:115], a[224:227], v[132:135], v[112:115]\n\t" \
        "ds_read_b128 v[132:135], v182 offset:128\n\t" \
        "v_mfma_f32_16x16x32_bf16 v[104:107], a[228:231], v[136:139], v[104:107]\n\t" \
        "v_mfma_f32_16x16x32_bf16 v[108:111], a[232:235], v[136:139], v[108:111]\n\t" \
        "v_mfma_f32_16x16x32_bf16 v[112:115], a[236:239], v[136:139], v[112:115]\n\t" \
        "s_waitcnt lgkmcnt(3)\n\t" \
        "v_mfma_f32_16x16x32_bf16 v[104:107], a[240:243], v[140:143], v[104:107]\n\t" \
        "v_mfma_f32_16x16x32_bf16 v[108:111], a[244:247], v[140:143], v[108:111]\n\t" \
        "v_mfma_f32_16x16x32_bf16 v[112:115], a[248:251], v[140:143], v[112:115]\n\t" \
        "v_mfma_f32_16x16x32_bf16 v[104:107], a[252:255], v[144:147], v[104:107]\n\t" \
        "v_mfma_f32_16x16x32_bf16 v[108:111], v[188:191], v[144:147], v[108:111]\n\t" \
        "v_mfma_f32_16x16x32_bf16 v[112:115], v[192:195], v[144:147], v[112:115]\n\t" \
        "v_mfma_f32_16x16x32_bf16 v[104:107], v[196:199], v[116:119], v[104:107]\n\t" \
        "v_mfma_f32_16x16x32_bf16 v[108:111], v[200:203], v[116:119], v[108:111]\n\t" \
        "v_mfma_f32_16x16x32_bf16 v[112:115], v[204:207], v[116:119], v[112:115]\n\t" \
        "v_mfma_f32_16x16x32_bf16 v[104:107], v[208:211], v[120:123], v[104:107]\n\t" \
        "v_mfma_f32_16x16x32_bf16 v[108:111], v[212:215], v[120:123], v[108:111]\n\t" \
        "v_mfma_f32_16x16x32_bf16 v[112:115], v[216:219], v[120:123], v[112:115]\n\t" \
        "s_waitcnt lgkmcnt(0)\n\t" \
        "v_mfma_f32_16x16x32_bf16 v[104:107], v[220:223], v[124:127], v[104:107]\n\t" \
        "v_mfma_f32_16x16x32_bf16 v[108:111], v[224:227], v[124:127], v[108:111]\n\t" \
        "v_mfma_f32_16x16x32_bf16 v[112:115], v[228:231], v[124:127], v[112:115]\n\t" \
        "v_mfma_f32_16x16x32_bf16 v[104:107], v[232:235], v[128:131], v[104:107]\n\t" \
        "v_mfma_f32_16x16x32_bf16 v[108:111], v[236:239], v[128:131], v[108:111]\n\t" \
        "v_mfma_f32_16x16x32_bf16 v[112:115], v[240:243], v[128:131], v[112:115]\n\t" \
        "v_mfma_f32_16x16x32_bf16 v[104:107], v[244:247], v[132:135], v[104:107]\n\t" \
        "v_mfma_f32_16x16x32_bf16 v[108:111], v[248:251], v[132:135], v[108:111]\n\t" \
        "v_mfma_f32_16x16x32_bf16 v[112:115], v[252:255], v[132:135], v[112:115]\n\t" \
        "s_nop 9\n\t" \
        "v_cvt_pk_bf16_f32 v112, v112, v113\n\t" \
        "v_cvt_pk_bf16_f32 v113, v114, v115\n\t" \
        "v_cvt_pk_bf16_f32 v104, v104, v105\n\t" \
        "v_cvt_pk_bf16_f32 v105, v106, v107\n\t" \
        "v_cvt_pk_bf16_f32 v106, v108, v109\n\t" \
        "ds_write_b64 v11, v[112:113] offset:12288\n\t" \
        "v_cvt_pk_bf16_f32 v107, v110, v111\n\t" \
        "v_permlane16_swap_b32 v104, v106\n\t" \
        "s_nop 0\n\t" \
        "v_permlane16_swap_b32 v105, v107\n\t" \
        "s_add_u32 s56, s64, 0x7e000\n\t" \
        "buffer_store_dwordx4 v[104:107], v9, s[28:31], s56 offen sc1\n\t" \
        "s_waitcnt lgkmcnt(0)\n\t" \
        "s_barrier\n\t" \
        "ds_read_b64 v[114:115], v12 offset:12288\n\t" \
        "s_waitcnt lgkmcnt(0)\n\t" \
        "v_permlane16_swap_b32 v112, v114\n\t" \
        "v_permlane16_swap_b32 v113, v115\n\t" \
        "s_add_u32 s56, s64, 0x7e000\n\t" \
        "buffer_store_dwordx4 v[112:115], v10, s[28:31], s56 offen sc1\n\t" \
        "s_endpgm\n\t" \
        ".LT12%=:\n\t" \
        "ds_read_b128 v[140:143], v157 offset:64\n\t" \
        "v_mfma_f32_16x16x32_bf16 v[92:95], a[0:3], v[144:147], 0\n\t" \
        "v_mfma_f32_16x16x32_bf16 v[96:99], a[4:7], v[144:147], 0\n\t" \
        "v_mfma_f32_16x16x32_bf16 v[100:103], a[8:11], v[144:147], 0\n\t" \
        "s_nop 3\n\t" \
        "v_cvt_pk_bf16_f32 v88, v88, v89\n\t" \
        "v_cvt_pk_bf16_f32 v89, v90, v91\n\t" \
        "v_mfma_f32_16x16x32_bf16 v[92:95], a[12:15], v[116:119], v[92:95]\n\t" \
        "s_add_u32 s56, s64, 0x2a000\n\t" \
        "buffer_store_dwordx4 v[56:59], v9, s[28:31], s56 offen sc1\n\t" \
        "v_mfma_f32_16x16x32_bf16 v[96:99], a[16:19], v[116:119], v[96:99]\n\t" \
        "ds_write_b64 v11, v[88:89] offset:8192\n\t" \
        "v_cvt_pk_bf16_f32 v80, v80, v81\n\t" \
        "v_mfma_f32_16x16x32_bf16 v[100:103], a[20:23], v[116:119], v[100:103]\n\t" \
        "ds_read_b128 v[144:147], v157 offset:128\n\t" \
        "v_cvt_pk_bf16_f32 v81, v82, v83\n\t" \
        "v_mfma_f32_16x16x32_bf16 v[92:95], a[24:27], v[120:123], v[92:95]\n\t" \
        "ds_read_b128 v[116:119], v161 offset:0\n\t" \
        "v_cvt_pk_bf16_f32 v82, v84, v85\n\t" \
        "v_mfma_f32_16x16x32_bf16 v[96:99], a[28:31], v[120:123], v[96:99]\n\t" \
        "v_cvt_pk_bf16_f32 v83, v86, v87\n\t" \
        "v_permlane16_swap_b32 v80, v82\n\t" \
        "v_mfma_f32_16x16x32_bf16 v[100:103], a[32:35], v[120:123], v[100:103]\n\t" \
        "ds_read_b128 v[120:123], v161 offset:64\n\t" \
        "v_permlane16_swap_b32 v81, v83\n\t" \
        "s_waitcnt lgkmcnt(5)\n\t" \
        "v_mfma_f32_16x16x32_bf16 v[92:95], a[36:39], v[124:127], v[92:95]\n\t" \
        "v_mfma_f32_16x16x32_bf16 v[96:99], a[40:43], v[124:127], v[96:99]\n\t" \
        "s_add_u32 s56, s64, 0x2a000\n\t" \
        "buffer_store_dwordx4 v[64:67], v10, s[28:31], s56 offen sc1\n\t" \
        "v_mfma_f32_16x16x32_bf16 v[100:103], a[44:47], v[124:127], v[100:103]\n\t" \
        "ds_read_b128 v[124:127], v161 offset:128\n\t" \
        "v_cvt_pk_bf16_f32 v68, v68, v69\n\t" \
        "v_mfma_f32_16x16x32_bf16 v[92:95], a[48:51], v[128:131], v[92:95]\n\t" \
        "v_cvt_pk_bf16_f32 v69, v70, v71\n\t" \
        "v_cvt_pk_bf16_f32 v70, v72, v73\n\t" \
        "v_mfma_f32_16x16x32_bf16 v[96:99], a[52:55], v[128:131], v[96:99]\n\t" \
        "s_waitcnt lgkmcnt(4)\n\t" \
        "s_barrier\n\t" \
        "ds_read_b64 v[90:91], v12 offset:8192\n\t" \
        "v_cvt_pk_bf16_f32 v71, v74, v75\n\t" \
        "v_mfma_f32_16x16x32_bf16 v[100:103], a[56:59], v[128:131], v[100:103]\n\t" \
        "ds_read_b128 v[128:131], v165 offset:0\n\t" \
        "v_permlane16_swap_b32 v68, v70\n\t" \
        "v_mfma_f32_16x16x32_bf16 v[92:95], a[60:63], v[132:135], v[92:95]\n\t" \
        "ds_read_b64 v[78:79], v12 offset:6144\n\t" \
        "v_permlane16_swap_b32 v69, v71\n\t" \
        "v_mfma_f32_16x16x32_bf16 v[96:99], a[64:67], v[132:135], v[96:99]\n\t" \
        "v_mfma_f32_16x16x32_bf16 v[100:103], a[68:71], v[132:135], v[100:103]\n\t" \
        "ds_read_b128 v[132:135], v165 offset:64\n\t" \
        "v_mfma_f32_16x16x32_bf16 v[92:95], a[72:75], v[136:139], v[92:95]\n\t" \
        "v_mfma_f32_16x16x32_bf16 v[96:99], a[76:79], v[136:139], v[96:99]\n\t" \
        "v_mfma_f32_16x16x32_bf16 v[100:103], a[80:83], v[136:139], v[100:103]\n\t" \
        "ds_read_b128 v[136:139], v165 offset:128\n\t" \
        "v_mfma_f32_16x16x32_bf16 v[92:95], a[84:87], v[140:143], v[92:95]\n\t" \
        "s_waitcnt lgkmcnt(4)\n\t" \
        "v_permlane16_swap_b32 v88, v90\n\t" \
        "v_permlane16_swap_b32 v89, v91\n\t" \
        "v_mfma_f32_16x16x32_bf16 v[96:99], a[88:91], v[140:143], v[96:99]\n\t" \
        "v_mfma_f32_16x16x32_bf16 v[100:103], a[92:95], v[140:143], v[100:103]\n\t" \
        "ds_read_b128 v[140:143], v169 offset:0\n\t" \
        "s_waitcnt lgkmcnt(3)\n\t" \
        "v_permlane16_swap_b32 v76, v78\n\t" \
        "v_mfma_f32_16x16x32_bf16 v[92:95], a[96:99], v[144:147], v[92:95]\n\t" \
        "s_add_u32 s56, s64, 0x54000\n\t" \
        "buffer_store_dwordx4 v[80:83], v9, s[28:31], s56 offen sc1\n\t" \
        "v_mfma_f32_16x16x32_bf16 v[96:99], a[100:103], v[144:147], v[96:99]\n\t" \
        "v_permlane16_swap_b32 v77, v79\n\t" \
        "v_mfma_f32_16x16x32_bf16 v[100:103], a[104:107], v[144:147], v[100:103]\n\t" \
        "ds_read_b128 v[144:147], v169 offset:64\n\t" \
        "v_mfma_f32_16x16x32_bf16 v[92:95], a[108:111], v[116:119], v[92:95]\n\t" \
        "v_mfma_f32_16x16x32_bf16 v[96:99], a[112:115], v[116:119], v[96:99]\n\t" \
        "v_mfma_f32_16x16x32_bf16 v[100:103], a[116:119], v[116:119], v[100:103]\n\t" \
        "ds_read_b128 v[116:119], v169 offset:128\n\t" \
        "v_mfma_f32_16x16x32_bf16 v[92:95], a[120:123], v[120:123], v[92:95]\n\t" \
        "v_mfma_f32_16x16x32_bf16 v[96:99], a[124:127], v[120:123], v[96:99]\n\t" \
        "s_add_u32 s56, s64, 0x54000\n\t" \
        "buffer_store_dwordx4 v[88:91], v10, s[28:31], s56 offen sc1\n\t" \
        "v_mfma_f32_16x16x32_bf16 v[100:103], a[128:131], v[120:123], v[100:103]\n\t" \
        "ds_read_b128 v[120:123], v173 offset:0\n\t" \
        "v_mfma_f32_16x16x32_bf16 v[92:95], a[132:135], v[124:127], v[92:95]\n\t" \
        "s_add_u32 s56, s64, 0x3f000\n\t" \
        "buffer_store_dwordx4 v[68:71], v9, s[28:31], s56 offen sc1\n\t" \
        "v_mfma_f32_16x16x32_bf16 v[96:99], a[136:139], v[124:127], v[96:99]\n\t" \
        "v_mfma_f32_16x16x32_bf16 v[100:103], a[140:143], v[124:127], v[100:103]\n\t" \
        "ds_read_b128 v[124:127], v173 offset:64\n\t" \
        "v_mfma_f32_16x16x32_bf16 v[92:95], a[144:147], v[128:131], v[92:95]\n\t" \
        "v_mfma_f32_16x16x32_bf16 v[96:99], a[148:151], v[128:131], v[96:99]\n\t" \
        "v_mfma_f32_16x16x32_bf16 v[100:103], a[152:155], v[128:131], v[100:103]\n\t" \
        "ds_read_b128 v[128:131], v173 offset:128\n\t" \
        "s_waitcnt lgkmcnt(4)\n\t" \
        "v_mfma_f32_16x16x32_bf16 v[92:95], a[156:159], v[132:135], v[92:95]\n\t" \
        "v_mfma_f32_16x16x32_bf16 v[96:99], a[160:163], v[132:135], v[96:99]\n\t" \
        "s_add_u32 s56, s64, 0x3f000\n\t" \
        "buffer_store_dwordx4 v[76:79], v10, s[28:31], s56 offen sc1\n\t" \
        "v_mfma_f32_16x16x32_bf16 v[100:103], a[164:167], v[132:135], v[100:103]\n\t" \
        "ds_read_b128 v[132:135], v177 offset:0\n\t" \
        "v_mfma_f32_16x16x32_bf16 v[92:95], a[168:171], v[136:139], v[92:95]\n\t" \
        "v_mfma_f32_16x16x32_bf16 v[96:99], a[172:175], v[136:139], v[96:99]\n\t" \
        "v_mfma_f32_16x16x32_bf16 v[100:103], a[176:179], v[136:139], v[100:103]\n\t" \
        "ds_read_b128 v[136:139], v177 offset:64\n\t" \
        "v_mfma_f32_16x16x32_bf16 v[92:95], a[180:183], v[140:143], v[92:95]\n\t" \
        "v_mfma_f32_16x16x32_bf16 v[96:99], a[184:187], v[140:143], v[96:99]\n\t" \
        "v_mfma_f32_16x16x32_bf16 v[100:103], a[188:191], v[140:143], v[100:103]\n\t" \
        "ds_read_b128 v[140:143], v177 offset:128\n\t" \
        "v_mfma_f32_16x16x32_bf16 v[92:95], a[192:195], v[144:147], v[92:95]\n\t" \
        "v_mfma_f32_16x16x32_bf16 v[96:99], a[196:199], v[144:147], v[96:99]\n\t" \
        "v_mfma_f32_16x16x32_bf16 v[100:103], a[200:203], v[144:147], v[100:103]\n\t" \
        "ds_read_b128 v[144:147], v181 offset:0\n\t" \
        "s_waitcnt lgkmcnt(4)\n\t" \
        "v_mfma_f32_16x16x32_bf16 v[92:95], a[204:207], v[116:119], v[92:95]\n\t" \
        "v_mfma_f32_16x16x32_bf16 v[96:99], a[208:211], v[116:119], v[96:99]\n\t" \
        "v_mfma_f32_16x16x32_bf16 v[100:103], a[212:215], v[116:119], v[100:103]\n\t" \
        "ds_read_b128 v[116:119], v181 offset:64\n\t" \
        "v_mfma_f32_16x16x32_bf16 v[92:95], a[216:219], v[120:123], v[92:95]\n\t" \
        "v_mfma_f32_16x16x32_bf16 v[96:99], a[220:223], v[120:123], v[96:99]\n\t" \
        "v_mfma_f32_16x16x32_bf16 v[100:103], a[224:227], v[120:123], v[100:103]\n\t" \
        "ds_read_b128 v[120:123], v181 offset:128\n\t" \
        "v_mfma_f32_16x16x32_bf16 v[92:95], a[228:231], v[124:127], v[92:95]\n\t" \
        "v_mfma_f32_16x16x32_bf16 v[96:99], a[232:235], v[124:127], v[96:99]\n\t" \
        "v_mfma_f32_16x16x32_bf16 v[100:103], a[236:239], v[124:127], v[100:103]\n\t" \
        "v_mfma_f32_16x16x32_bf16 v[92:95], a[240:243], v[128:131], v[92:95]\n\t" \
        "v_mfma_f32_16x16x32_bf16 v[96:99], a[244:247], v[128:131], v[96:99]\n\t" \
        "v_mfma_f32_16x16x32_bf16 v[100:103], a[248:251], v[128:131], v[100:103]\n\t" \
        "s_waitcnt lgkmcnt(2)\n\t" \
        "v_mfma_f32_16x16x32_bf16 v[92:95], a[252:255], v[132:135], v[92:95]\n\t" \
        "v_mfma_f32_16x16x32_bf16 v[96:99], v[188:191], v[132:135], v[96:99]\n\t" \
        "v_mfma_f32_16x16x32_bf16 v[100:103], v[192:195], v[132:135], v[100:103]\n\t" \
        "v_mfma_f32_16x16x32_bf16 v[92:95], v[196:199], v[136:139], v[92:95]\n\t" \
        "v_mfma_f32_16x16x32_bf16 v[96:99], v[200:203], v[136:139], v[96:99]\n\t" \
        "v_mfma_f32_16x16x32_bf16 v[100:103], v[204:207], v[136:139], v[100:103]\n\t" \
        "v_mfma_f32_16x16x32_bf16 v[92:95], v[208:211], v[140:143], v[92:95]\n\t" \
        "v_mfma_f32_16x16x32_bf16 v[96:99], v[212:215], v[140:143], v[96:99]\n\t" \
        "v_mfma_f32_16x16x32_bf16 v[100:103], v[216:219], v[140:143], v[100:103]\n\t" \
        "v_mfma_f32_16x16x32_bf16 v[92:95], v[220:223], v[144:147], v[92:95]\n\t" \
        "v_mfma_f32_16x16x32_bf16 v[96:99], v[224:227], v[144:147], v[96:99]\n\t" \
        "v_mfma_f32_16x16x32_bf16 v[100:103], v[228:231], v[144:147], v[100:103]\n\t" \
        "s_waitcnt lgkmcnt(0)\n\t" \
        "v_mfma_f32_16x16x32_bf16 v[92:95], v[232:235], v[116:119], v[92:95]\n\t" \
        "v_mfma_f32_16x16x32_bf16 v[96:99], v[236:239], v[116:119], v[96:99]\n\t" \
        "v_mfma_f32_16x16x32_bf16 v[100:103], v[240:243], v[116:119], v[100:103]\n\t" \
        "v_mfma_f32_16x16x32_bf16 v[92:95], v[244:247], v[120:123], v[92:95]\n\t" \
        "v_mfma_f32_16x16x32_bf16 v[96:99], v[248:251], v[120:123], v[96:99]\n\t" \
        "v_mfma_f32_16x16x32_bf16 v[100:103], v[252:255], v[120:123], v[100:103]\n\t" \
        "s_nop 9\n\t" \
        "v_cvt_pk_bf16_f32 v100, v100, v101\n\t" \
        "v_cvt_pk_bf16_f32 v101, v102, v103\n\t" \
        "v_cvt_pk_bf16_f32 v92, v92, v93\n\t" \
        "v_cvt_pk_bf16_f32 v93, v94, v95\n\t" \
        "v_cvt_pk_bf16_f32 v94, v96, v97\n\t" \
        "ds_write_b64 v11, v[100:101] offset:10240\n\t" \
        "v_cvt_pk_bf16_f32 v95, v98, v99\n\t" \
        "v_permlane16_swap_b32 v92, v94\n\t" \
        "s_nop 0\n\t" \
        "v_permlane16_swap_b32 v93, v95\n\t" \
        "s_add_u32 s56, s64, 0x69000\n\t" \
        "buffer_store_dwordx4 v[92:95], v9, s[28:31], s56 offen sc1\n\t" \
        "s_waitcnt lgkmcnt(0)\n\t" \
        "s_barrier\n\t" \
        "ds_read_b64 v[102:103], v12 offset:10240\n\t" \
        "s_waitcnt lgkmcnt(0)\n\t" \
        "v_permlane16_swap_b32 v100, v102\n\t" \
        "v_permlane16_swap_b32 v101, v103\n\t" \
        "s_add_u32 s56, s64, 0x69000\n\t" \
        "buffer_store_dwordx4 v[100:103], v10, s[28:31], s56 offen sc1\n\t" \
        "s_endpgm\n\t" \
        ".LH_start%=:\n\t" \
        "s_sub_i32 s57, s36, 0x15000\n\t" \
        "s_max_i32 s57, s57, 0\n\t" \
        "s_mov_b32 m0, s38\n\t" \
        "s_nop 0\n\t" \
        "buffer_load_dwordx4 v13, s[16:19], s57 offen sc1 lds\n\t" \
        "s_mov_b32 s58, s11\n\t" \
        "s_mov_b32 s62, 0\n\t" \
        "s_mov_b32 s63, -1\n\t" \
        "s_mov_b32 s42, 0xffff0000\n\t" \
        "s_mov_b32 s43, 0xc0\n\t" \
        "s_mov_b32 s44, 0xf80\n\t" \
        "s_mov_b32 s45, 0x435f435f\n\t" \
        "v_add_u32 v162, s32, v18\n\t" \
        "s_sub_i32 s57, s36, 0xa800\n\t" \
        "s_max_i32 s57, s57, 0\n\t" \
        "s_add_u32 m0, s38, 0xf80\n\t" \
        "s_nop 0\n\t" \
        "buffer_load_dwordx4 v13, s[16:19], s57 offen sc1 lds\n\t" \
        "v_add_u32 v163, s33, v19\n\t" \
        "v_add_u32 v162, -1, v162\n\t" \
        "v_add_u32 v163, -1, v163\n\t" \
        "v_cndmask_b32 v161, 0, 1.0, s[62:63]\n\t" \
        "v_cvt_f32_i32 v55, v162\n\t" \
        "v_cvt_f32_i32 v62, v163\n\t" \
        "v_add_f32 v56, v55, v161\n\t" \
        "v_add_f32 v59, v62, v161\n\t" \
        "s_mov_b32 s57, s36\n\t" \
        "s_add_u32 m0, s38, 0x1f00\n\t" \
        "s_nop 0\n\t" \
        "buffer_load_dwordx4 v13, s[16:19], s57 offen sc1 lds\n\t" \
        "v_add_f32 v57, 1.0, v55\n\t" \
        "v_add_f32 v62, 2.0, v62\n\t" \
        "v_add_f32 v58, 2.0, v55\n\t" \
        "v_add_f32 v61, 1.0, v59\n\t" \
        "v_fma_f32 v60, v161, -2.0, v62\n\t" \
        "v_bfe_u32 v160, v2, 4, 1\n\t" \
        "v_lshrrev_b32 v162, 2, v19\n\t" \
        "v_and_b32 v163, 1, v19\n\t" \
        "s_add_u32 s57, s36, 0xa800\n\t" \
        "s_add_u32 m0, s38, 0x2e80\n\t" \
        "s_nop 0\n\t" \
        "buffer_load_dwordx4 v13, s[16:19], s57 offen sc1 lds\n\t" \
        "v_lshlrev_b32 v162, 5, v162\n\t" \
        "v_lshl_add_u32 v162, v163, 4, v162\n\t" \
        "v_and_b32 v163, 2, v19\n\t" \
        "v_lshl_add_u32 v162, v160, 3, v162\n\t" \
        "v_lshlrev_b32 v161, 2, v2\n\t" \
        "v_lshl_add_u32 v162, v163, 1, v162\n\t" \
        "v_and_b32 v161, 0x80, v161\n\t" \
        "s_mul_i32 s55, s35, 2560\n\t" \
        "s_add_u32 s57, s36, 0x15000\n\t" \
        "s_add_u32 m0, s38, 0x3e00\n\t" \
        "s_nop 0\n\t" \
        "buffer_load_dwordx4 v13, s[16:19], s57 offen sc1 lds\n\t" \
        "s_add_u32 s55, s55, 64\n\t" \
        "v_add3_u32 v63, v162, v161, s55\n\t" \
        "v_bfe_u32 v162, v2, 2, 1\n\t" \
        "v_lshrrev_b32 v163, 3, v2\n\t" \
        "v_mul_u32_u24 v162, 0x500, v162\n\t" \
        "v_lshlrev_b32 v163, 4, v163\n\t" \
        "v_add3_u32 v9, v162, v163, s55\n\t" \
        "s_mul_i32 s55, s35, 1280\n\t" \
        "s_add_u32 s57, s36, 0x1f800\n\t" \
        "s_add_u32 m0, s38, 0x4d80\n\t" \
        "s_nop 0\n\t" \
        "buffer_load_dwordx4 v13, s[16:19], s57 offen sc1 lds\n\t" \
        "s_add_u32 s55, s55, 119360\n\t" \
        "v_lshl_add_u32 v10, v19, 3, s55\n\t" \
        "v_lshl_add_u32 v162, v160, 2, v161\n\t" \
        "v_add_u32 v64, v10, v162\n\t" \
        "v_mov_b32 v29, 0x3f80\n\t" \
        "s_mul_i32 s55, s32, 0xf80\n\t" \
        "s_mul_i32 s56, s33, 0xc0\n\t" \
        "s_add_u32 s55, s55, s56\n\t" \
        "s_add_u32 s57, s36, 0x2a000\n\t" \
        "s_min_u32 s57, s57, 0x925800\n\t" \
        "s_add_u32 m0, s38, 0x5d00\n\t" \
        "s_nop 0\n\t" \
        "buffer_load_dwordx4 v13, s[16:19], s57 offen sc1 lds\n\t" \
        "s_sub_u32 s55, 0x13740, s55\n\t" \
        "v_mov_b32 v65, s55\n\t" \
        "s_sub_i32 s55, s32, 2\n\t" \
        "s_sub_i32 s56, s33, 2\n\t" \
        "s_max_i32 s55, s55, 0\n\t" \
        "s_max_i32 s56, s56, 0\n\t" \
        "v_cvt_f32_u32 v162, s55\n\t" \
        "v_cvt_f32_u32 v163, s56\n\t" \
        "s_add_u32 s57, s36, 0x34800\n\t" \
        "s_min_u32 s57, s57, 0x925800\n\t" \
        "s_add_u32 m0, s38, 0x6c80\n\t" \
        "s_nop 0\n\t" \
        "buffer_load_dwordx4 v13, s[16:19], s57 offen sc1 lds\n\t" \
        "s_add_u32 s55, s32, 9\n\t" \
        "s_add_u32 s56, s33, 17\n\t" \
        "v_cvt_pk_bf16_f32 v14, v162, v163\n\t" \
        "v_cvt_f32_u32 v162, s55\n\t" \
        "v_cvt_f32_u32 v163, s56\n\t" \
        "s_cmp_eq_u32 s58, 3\n\t" \
        "s_cselect_b32 s64, 0xffff0000, 0\n\t" \
        "s_mov_b32 s65, s64\n\t" \
        "s_add_u32 s57, s36, 0x3f000\n\t" \
        "s_min_u32 s57, s57, 0x925800\n\t" \
        "s_add_u32 m0, s38, 0x7c00\n\t" \
        "s_nop 0\n\t" \
        "buffer_load_dwordx4 v13, s[16:19], s57 offen sc1 lds\n\t" \
        "v_cvt_pk_bf16_f32 v15, v162, v163\n\t" \
        "v_cmp_lt_u32 vcc, 47, v2\n\t" \
        "v_lshlrev_b32 v162, 4, v2\n\t" \
        "v_xor_b32 v163, 32, v162\n\t" \
        "v_cndmask_b32 v3, v162, v163, vcc\n\t" \
        "v_cndmask_b32 v4, v163, v162, s[62:63]\n\t" \
        "v_cmp_lt_u32 vcc, 15, v2\n\t" \
        "s_nop 1\n\t" \
        "s_add_u32 s57, s36, 0x49800\n\t" \
        "s_min_u32 s57, s57, 0x925800\n\t" \
        "s_add_u32 m0, s38, 0x8b80\n\t" \
        "s_nop 0\n\t" \
        "buffer_load_dwordx4 v13, s[16:19], s57 offen sc1 lds\n\t" \
        "v_cndmask_b32 v5, v162, v163, vcc\n\t" \
        "v_lshrrev_b32 v164, 4, v2\n\t" \
        "v_bfe_u32 v162, v2, 2, 2\n\t" \
        "v_and_b32 v163, 3, v2\n\t" \
        "v_lshl_add_u32 v162, v164, 2, v162\n\t" \
        "v_lshlrev_b32 v11, 3, v163\n\t" \
        "v_and_b32 v165, 1, v164\n\t" \
        "v_mul_u32_u24 v162, 0xc0, v162\n\t" \
        "s_add_u32 s57, s36, 0x54000\n\t" \
        "s_min_u32 s57, s57, 0x925800\n\t" \
        "s_add_u32 m0, s38, 0x9b00\n\t" \
        "s_nop 0\n\t" \
        "buffer_load_dwordx4 v13, s[16:19], s57 offen sc1 lds\n\t" \
        "v_xor_b32 v12, 32, v11\n\t" \
        "v_lshl_add_u32 v162, v165, 5, v162\n\t" \
        "v_lshlrev_b32 v163, 6, v165\n\t" \
        "s_mov_b32 s55, 0x2840\n\t" \
        "v_add3_u32 v6, v162, v11, s55\n\t" \
        "v_sub_u32 v7, v6, v163\n\t" \
        "v_mul_u32_u24 v162, 0xc0, v19\n\t" \
        "v_lshrrev_b32 v164, 1, v164\n\t" \
        "s_add_u32 s57, s36, 0x5e800\n\t" \
        "s_min_u32 s57, s57, 0x925800\n\t" \
        "s_add_u32 m0, s38, 0xaa80\n\t" \
        "s_nop 0\n\t" \
        "buffer_load_dwordx4 v13, s[16:19], s57 offen sc1 lds\n\t" \
        "v_lshl_add_u32 v162, v165, 5, v162\n\t" \
        "v_lshl_add_u32 v8, v164, 4, v162\n\t" \
        "s_add_u32 s55, s32, s35\n\t" \
        "s_mul_i32 s56, s33, 0xc0\n\t" \
        "s_mul_i32 s55, s55, 0xa800\n\t" \
        "s_add_u32 s59, s55, s56\n\t" \
        "s_add_u32 s60, s59, 0x2a000\n\t" \
        "v_lshrrev_b32 v166, 4, v2\n\t" \
        "s_mov_b32 s57, s40\n\t" \
        "s_mov_b32 m0, s39\n\t" \
        "s_nop 0\n\t" \
        "buffer_load_dwordx4 v3, s[24:27], s57 offen offset:0 lds\n\t" \
        "v_bfe_u32 v168, v2, 2, 2\n\t" \
        "v_and_b32 v167, 3, v2\n\t" \
        "v_cmp_eq_u32 vcc, v166, v168\n\t" \
        "v_lshlrev_b32 v167, v167, 1\n\t" \
        "s_nop 0\n\t" \
        "v_cndmask_b32 v167, 0, v167, vcc\n\t" \
        "v_bfe_i32 v25, v167, 0, 1\n\t" \
        "v_bfe_i32 v26, v167, 1, 1\n\t" \
        "buffer_load_dwordx4 v4, s[24:27], s57 offen offset:1024 lds\n\t" \
        "v_bfe_i32 v27, v167, 2, 1\n\t" \
        "v_bfe_i32 v28, v167, 3, 1\n\t" \
        "buffer_load_dwordx4 v5, s[24:27], s57 offen offset:2048 lds\n\t" \
        "s_waitcnt vmcnt(15)\n\t" \
        "s_cmp_eq_u32 s58, 3\n\t" \
        "s_cbranch_scc1 .LH4p%=\n\t" \
        "v_cndmask_b32 v140, v40, v41, s[62:63]\n\t" \
        "v_lshlrev_b32 v141, 16, v140\n\t" \
        "v_and_b32 v142, s42, v140\n\t" \
        "v_add_f32 v141, v55, v141\n\t" \
        "v_add_f32 v142, v59, v142\n\t" \
        "v_cvt_pk_bf16_f32 v50, v141, v142\n\t" \
        "v_pk_max_i16 v50, v50, 0\n\t" \
        "v_pk_min_i16 v50, v50, s45\n\t" \
        "v_lshlrev_b32 v141, 16, v50\n\t" \
        "v_and_b32 v142, s42, v50\n\t" \
        "v_pk_min_i16 v66, v50, v50\n\t" \
        "v_fract_f32 v143, v141\n\t" \
        "v_fract_f32 v144, v142\n\t" \
        "s_add_u32 s57, s40, 0x3000\n\t" \
        "s_add_u32 m0, s39, 0x3000\n\t" \
        "s_nop 0\n\t" \
        "buffer_load_dwordx4 v3, s[24:27], s57 offen offset:0 lds\n\t" \
        "v_cvt_u32_f32 v145, v141\n\t" \
        "v_cvt_u32_f32 v146, v142\n\t" \
        "v_cmp_neq_f32 s[10:11], 0, v143\n\t" \
        "v_cmp_neq_f32 vcc, 0, v144\n\t" \
        "v_add_u32 v146, 2, v146\n\t" \
        "v_pk_max_i16 v67, v50, v50\n\t" \
        "v_mad_u32_u24 v148, v146, s43, v65\n\t" \
        "v_bfe_u32 v147, v146, 2, 1\n\t" \
        "v_mad_u32_u24 v148, v145, s44, v148\n\t" \
        "v_add_u32 v146, 1, v146\n\t" \
        "v_lshl_add_u32 v149, v147, 5, v148\n\t" \
        "v_bfe_u32 v147, v146, 2, 1\n\t" \
        "buffer_load_dwordx4 v4, s[24:27], s57 offen offset:1024 lds\n\t" \
        "v_add_u32 v148, s43, v148\n\t" \
        "v_add_u32 v150, s44, v149\n\t" \
        "v_lshl_add_u32 v148, v147, 5, v148\n\t" \
        "v_cndmask_b32 v150, v149, v150, s[10:11]\n\t" \
        "v_add_f32 v144, v144, v143\n\t" \
        "v_cndmask_b32 v150, v150, v148, vcc\n\t" \
        "v_sub_f32 v152, 1.0, v144\n\t" \
        "s_and_b64 s[4:5], s[10:11], vcc\n\t" \
        "ds_write2st64_b32 v63, v149, v150 offset0:0 offset1:5\n\t" \
        "v_cvt_pk_bf16_f32 v151, v152, v144\n\t" \
        "v_cndmask_b32 v151, v151, v29, s[4:5]\n\t" \
        "ds_write_b32 v64, v151 offset:0\n\t" \
        "v_cndmask_b32 v140, v42, v43, s[62:63]\n\t" \
        "v_lshlrev_b32 v141, 16, v140\n\t" \
        "v_and_b32 v142, s42, v140\n\t" \
        "v_add_f32 v141, v56, v141\n\t" \
        "v_add_f32 v142, v60, v142\n\t" \
        "v_cvt_pk_bf16_f32 v51, v141, v142\n\t" \
        "v_pk_max_i16 v51, v51, 0\n\t" \
        "v_pk_min_i16 v51, v51, s45\n\t" \
        "v_lshlrev_b32 v141, 16, v51\n\t" \
        "v_and_b32 v142, s42, v51\n\t" \
        "v_pk_min_i16 v66, v66, v51\n\t" \
        "v_fract_f32 v143, v141\n\t" \
        "v_fract_f32 v144, v142\n\t" \
        "v_cvt_u32_f32 v145, v141\n\t" \
        "v_cvt_u32_f32 v146, v142\n\t" \
        "v_cmp_neq_f32 s[10:11], 0, v143\n\t" \
        "v_cmp_neq_f32 vcc, 0, v144\n\t" \
        "v_add_u32 v146, 2, v146\n\t" \
        "v_pk_max_i16 v67, v67, v51\n\t" \
        "buffer_load_dwordx4 v5, s[24:27], s57 offen offset:2048 lds\n\t" \
        "v_mad_u32_u24 v148, v146, s43, v65\n\t" \
        "v_bfe_u32 v147, v146, 2, 1\n\t" \
        "v_mad_u32_u24 v148, v145, s44, v148\n\t" \
        "v_add_u32 v146, 1, v146\n\t" \
        "v_lshl_add_u32 v149, v147, 5, v148\n\t" \
        "v_bfe_u32 v147, v146, 2, 1\n\t" \
        "v_add_u32 v148, s43, v148\n\t" \
        "v_add_u32 v150, s44, v149\n\t" \
        "v_lshl_add_u32 v148, v147, 5, v148\n\t" \
        "v_cndmask_b32 v150, v149, v150, s[10:11]\n\t" \
        "v_add_f32 v144, v144, v143\n\t" \
        "v_cndmask_b32 v150, v150, v148, vcc\n\t" \
        "v_sub_f32 v152, 1.0, v144\n\t" \
        "s_and_b64 s[6:7], s[10:11], vcc\n\t" \
        "ds_write2st64_b32 v63, v149, v150 offset0:1 offset1:6\n\t" \
        "v_cvt_pk_bf16_f32 v151, v152, v144\n\t" \
        "v_cndmask_b32 v151, v151, v29, s[6:7]\n\t" \
        "ds_write_b32 v64, v151 offset:256\n\t" \
        "v_cndmask_b32 v140, v44, v45, s[62:63]\n\t" \
        "v_lshlrev_b32 v141, 16, v140\n\t" \
        "v_and_b32 v142, s42, v140\n\t" \
        "v_add_f32 v141, v57, v141\n\t" \
        "v_add_f32 v142, v61, v142\n\t" \
        "v_cvt_pk_bf16_f32 v52, v141, v142\n\t" \
        "v_pk_max_i16 v52, v52, 0\n\t" \
        "v_pk_min_i16 v52, v52, s45\n\t" \
        "v_lshlrev_b32 v141, 16, v52\n\t" \
        "v_and_b32 v142, s42, v52\n\t" \
        "v_pk_min_i16 v66, v66, v52\n\t" \
        "v_fract_f32 v143, v141\n\t" \
        "v_fract_f32 v144, v142\n\t" \
        "v_cvt_u32_f32 v145, v141\n\t" \
        "v_cvt_u32_f32 v146, v142\n\t" \
        "v_cmp_neq_f32 s[10:11], 0, v143\n\t" \
        "v_cmp_neq_f32 vcc, 0, v144\n\t" \
        "v_add_u32 v146, 2, v146\n\t" \
        "v_pk_max_i16 v67, v67, v52\n\t" \
        "s_add_u32 s57, s40, 0x6000\n\t" \
        "s_add_u32 m0, s39, 0x6000\n\t" \
        "s_nop 0\n\t" \
        "buffer_load_dwordx4 v3, s[24:27], s57 offen offset:0 lds\n\t" \
        "v_mad_u32_u24 v148, v146, s43, v65\n\t" \
        "v_bfe_u32 v147, v146, 2, 1\n\t" \
        "v_mad_u32_u24 v148, v145, s44, v148\n\t" \
        "v_add_u32 v146, 1, v146\n\t" \
        "v_lshl_add_u32 v149, v147, 5, v148\n\t" \
        "v_bfe_u32 v147, v146, 2, 1\n\t" \
        "v_add_u32 v148, s43, v148\n\t" \
        "v_add_u32 v150, s44, v149\n\t" \
        "v_lshl_add_u32 v148, v147, 5, v148\n\t" \
        "v_cndmask_b32 v150, v149, v150, s[10:11]\n\t" \
        "v_add_f32 v144, v144, v143\n\t" \
        "v_cndmask_b32 v150, v150, v148, vcc\n\t" \
        "v_sub_f32 v152, 1.0, v144\n\t" \
        "s_and_b64 s[8:9], s[10:11], vcc\n\t" \
        "ds_write2st64_b32 v63, v149, v150 offset0:2 offset1:7\n\t" \
        "v_cvt_pk_bf16_f32 v151, v152, v144\n\t" \
        "v_cndmask_b32 v151, v151, v29, s[8:9]\n\t" \
        "ds_write_b32 v64, v151 offset:512\n\t" \
        "v_cndmask_b32 v140, v46, v47, s[62:63]\n\t" \
        "v_lshlrev_b32 v141, 16, v140\n\t" \
        "v_and_b32 v142, s42, v140\n\t" \
        "v_add_f32 v141, v58, v141\n\t" \
        "v_add_f32 v142, v59, v142\n\t" \
        "v_cvt_pk_bf16_f32 v53, v141, v142\n\t" \
        "v_pk_max_i16 v53, v53, 0\n\t" \
        "v_pk_min_i16 v53, v53, s45\n\t" \
        "v_lshlrev_b32 v141, 16, v53\n\t" \
        "v_and_b32 v142, s42, v53\n\t" \
        "v_pk_min_i16 v66, v66, v53\n\t" \
        "v_fract_f32 v143, v141\n\t" \
        "v_fract_f32 v144, v142\n\t" \
        "v_cvt_u32_f32 v145, v141\n\t" \
        "v_cvt_u32_f32 v146, v142\n\t" \
        "v_cmp_neq_f32 s[10:11], 0, v143\n\t" \
        "v_cmp_neq_f32 vcc, 0, v144\n\t" \
        "v_add_u32 v146, 2, v146\n\t" \
        "v_pk_max_i16 v67, v67, v53\n\t" \
        "buffer_load_dwordx4 v4, s[24:27], s57 offen offset:1024 lds\n\t" \
        "v_mad_u32_u24 v148, v146, s43, v65\n\t" \
        "v_bfe_u32 v147, v146, 2, 1\n\t" \
        "v_mad_u32_u24 v148, v145, s44, v148\n\t" \
        "v_add_u32 v146, 1, v146\n\t" \
        "v_lshl_add_u32 v149, v147, 5, v148\n\t" \
        "v_bfe_u32 v147, v146, 2, 1\n\t" \
        "v_add_u32 v148, s43, v148\n\t" \
        "v_add_u32 v150, s44, v149\n\t" \
        "v_lshl_add_u32 v148, v147, 5, v148\n\t" \
        "v_cndmask_b32 v150, v149, v150, s[10:11]\n\t" \
        "v_add_f32 v144, v144, v143\n\t" \
        "v_cndmask_b32 v150, v150, v148, vcc\n\t" \
        "v_sub_f32 v152, 1.0, v144\n\t" \
        "s_and_b64 s[12:13], s[10:11], vcc\n\t" \
        "ds_write2st64_b32 v63, v149, v150 offset0:3 offset1:8\n\t" \
        "v_cvt_pk_bf16_f32 v151, v152, v144\n\t" \
        "v_cndmask_b32 v151, v151, v29, s[12:13]\n\t" \
        "ds_write_b32 v64, v151 offset:768\n\t" \
        "v_lshlrev_b32 v141, 16, v48\n\t" \
        "v_and_b32 v142, s42, v48\n\t" \
        "v_add_f32 v141, v58, v141\n\t" \
        "v_add_f32 v142, v62, v142\n\t" \
        "v_cvt_pk_bf16_f32 v54, v141, v142\n\t" \
        "v_pk_max_i16 v54, v54, 0\n\t" \
        "v_pk_min_i16 v54, v54, s45\n\t" \
        "v_lshlrev_b32 v141, 16, v54\n\t" \
        "v_and_b32 v142, s42, v54\n\t" \
        "v_pk_min_i16 v66, v66, v54\n\t" \
        "v_fract_f32 v143, v141\n\t" \
        "v_fract_f32 v144, v142\n\t" \
        "v_cvt_u32_f32 v145, v141\n\t" \
        "v_cvt_u32_f32 v146, v142\n\t" \
        "v_cmp_neq_f32 s[10:11], 0, v143\n\t" \
        "v_cmp_neq_f32 vcc, 0, v144\n\t" \
        "v_add_u32 v146, 2, v146\n\t" \
        "v_pk_max_i16 v67, v67, v54\n\t" \
        "v_mad_u32_u24 v148, v146, s43, v65\n\t" \
        "buffer_load_dwordx4 v5, s[24:27], s57 offen offset:2048 lds\n\t" \
        "v_bfe_u32 v147, v146, 2, 1\n\t" \
        "v_mad_u32_u24 v148, v145, s44, v148\n\t" \
        "v_add_u32 v146, 1, v146\n\t" \
        "v_lshl_add_u32 v149, v147, 5, v148\n\t" \
        "v_bfe_u32 v147, v146, 2, 1\n\t" \
        "v_add_u32 v148, s43, v148\n\t" \
        "v_add_u32 v150, s44, v149\n\t" \
        "v_lshl_add_u32 v148, v147, 5, v148\n\t" \
        "v_cndmask_b32 v150, v149, v150, s[10:11]\n\t" \
        "v_add_f32 v144, v144, v143\n\t" \
        "v_cndmask_b32 v150, v150, v148, vcc\n\t" \
        "v_sub_f32 v152, 1.0, v144\n\t" \
        "s_and_b64 s[14:15], s[10:11], vcc\n\t" \
        "ds_write2st64_b32 v63, v149, v150 offset0:4 offset1:9\n\t" \
        "v_cvt_pk_bf16_f32 v151, v152, v144\n\t" \
        "v_cndmask_b32 v151, v151, v29, s[14:15]\n\t" \
        "ds_write_b32 v64, v151 offset:1024\n\t" \
        "v_pk_max_i16 v140, v66, v14\n\t" \
        "v_pk_min_i16 v141, v67, v15\n\t" \
        "v_cmp_ne_u32 s[10:11], v140, v66\n\t" \
        "v_cmp_ne_u32 vcc, v141, v67\n\t" \
        "s_nop 0\n\t" \
        "s_or_b64 s[10:11], s[10:11], vcc\n\t" \
        "s_cselect_b32 s54, 1, 0\n\t" \
        "s_or_b64 s[10:11], s[4:5], s[6:7]\n\t" \
        "s_or_b64 s[56:57], s[8:9], s[12:13]\n\t" \
        "s_mov_b32 s15, 0\n\t" \
        "s_or_b64 s[10:11], s[10:11], s[56:57]\n\t" \
        "s_or_b64 s[10:11], s[10:11], s[14:15]\n\t" \
        "s_cmp_eq_u64 s[10:11], 0\n\t" \
        "s_cbranch_scc1 .LH_none%=\n\t" \
        "s_lshl_b32 s48, s35, 10\n\t" \
        "s_and_b32 s55, s35, 1\n\t" \
        "s_add_u32 s48, s48, 0x1e640\n\t" \
        "s_mul_i32 s55, s55, 0x2a00\n\t" \
        "s_cmp_lt_u32 s35, 2\n\t" \
        "s_cselect_b32 s49, 0, 0x2940\n\t" \
        "s_cselect_b32 s53, 1, -1\n\t" \
        "s_add_u32 s49, s49, s55\n\t" \
        "s_mul_i32 s53, s53, 192\n\t" \
        "s_mov_b32 s52, 0x3ffe0\n\t" \
        "s_add_u32 s49, s49, 0x1f640\n\t" \
        "v_lshrrev_b32 v166, 4, v2\n\t" \
        "v_and_b32 v24, 15, v2\n\t" \
        "v_bfe_u32 v167, v2, 3, 1\n\t" \
        "v_lshl_add_u32 v167, v166, 2, v167\n\t" \
        "v_lshrrev_b32 v168, 1, v2\n\t" \
        "v_and_b32 v168, 2, v168\n\t" \
        "v_lshl_add_u32 v167, v167, 2, v168\n\t" \
        "s_add_u32 s55, s48, 512\n\t" \
        "v_add_u32 v20, s55, v167\n\t" \
        "v_lshl_add_u32 v21, v24, 3, s48\n\t" \
        "v_bfe_u32 v167, v2, 1, 1\n\t" \
        "v_xor_b32 v167, v167, v166\n\t" \
        "v_lshrrev_b32 v168, 2, v24\n\t" \
        "v_and_b32 v169, 2, v168\n\t" \
        "v_xor_b32 v167, v167, v169\n\t" \
        "v_and_b32 v169, 4, v2\n\t" \
        "v_lshlrev_b32 v167, 4, v167\n\t" \
        "v_lshl_add_u32 v167, v169, 1, v167\n\t" \
        "v_mul_i32_i24 v22, s53, v24\n\t" \
        "v_add3_u32 v22, v22, v167, s49\n\t" \
        "v_and_b32 v167, 3, v2\n\t" \
        "v_and_b32 v169, 1, v166\n\t" \
        "v_lshlrev_b32 v169, 3, v169\n\t" \
        "v_lshl_or_b32 v23, v167, 4, v169\n\t" \
        "s_mov_b32 s45, s48\n\t" \
        "v_mbcnt_lo_u32_b32 v140, s4, 0\n\t" \
        "v_mbcnt_hi_u32_b32 v140, s5, v140\n\t" \
        "s_bcnt1_i32_b64 s55, s[4:5]\n\t" \
        "v_add_u32 v141, 0, v63\n\t" \
        "v_lshl_add_u32 v140, v140, 3, s45\n\t" \
        "s_lshl3_add_u32 s45, s55, s45\n\t" \
        "s_mov_b64 exec, s[4:5]\n\t" \
        "ds_write2_b32 v140, v50, v141 offset1:1\n\t" \
        "s_mov_b64 exec, -1\n\t" \
        "v_mbcnt_lo_u32_b32 v140, s6, 0\n\t" \
        "v_mbcnt_hi_u32_b32 v140, s7, v140\n\t" \
        "s_bcnt1_i32_b64 s55, s[6:7]\n\t" \
        "v_add_u32 v141, 256, v63\n\t" \
        "v_lshl_add_u32 v140, v140, 3, s45\n\t" \
        "s_lshl3_add_u32 s45, s55, s45\n\t" \
        "s_mov_b64 exec, s[6:7]\n\t" \
        "ds_write2_b32 v140, v51, v141 offset1:1\n\t" \
        "s_mov_b64 exec, -1\n\t" \
        "v_mbcnt_lo_u32_b32 v140, s8, 0\n\t" \
        "v_mbcnt_hi_u32_b32 v140, s9, v140\n\t" \
        "s_bcnt1_i32_b64 s55, s[8:9]\n\t" \
        "v_add_u32 v141, 512, v63\n\t" \
        "v_lshl_add_u32 v140, v140, 3, s45\n\t" \
        "s_lshl3_add_u32 s45, s55, s45\n\t" \
        "s_mov_b64 exec, s[8:9]\n\t" \
        "ds_write2_b32 v140, v52, v141 offset1:1\n\t" \
        "s_mov_b64 exec, -1\n\t" \
        "v_mbcnt_lo_u32_b32 v140, s12, 0\n\t" \
        "v_mbcnt_hi_u32_b32 v140, s13, v140\n\t" \
        "s_bcnt1_i32_b64 s55, s[12:13]\n\t" \
        "v_add_u32 v141, 768, v63\n\t" \
        "v_lshl_add_u32 v140, v140, 3, s45\n\t" \
        "s_lshl3_add_u32 s45, s55, s45\n\t" \
        "s_mov_b64 exec, s[12:13]\n\t" \
        "ds_write2_b32 v140, v53, v141 offset1:1\n\t" \
        "s_mov_b64 exec, -1\n\t" \
        "v_mbcnt_lo_u32_b32 v140, s14, 0\n\t" \
        "v_mbcnt_hi_u32_b32 v140, s15, v140\n\t" \
        "s_bcnt1_i32_b64 s55, s[14:15]\n\t" \
        "v_add_u32 v141, 1024, v63\n\t" \
        "v_lshl_add_u32 v140, v140, 3, s45\n\t" \
        "s_lshl3_add_u32 s45, s55, s45\n\t" \
        "s_mov_b64 exec, s[14:15]\n\t" \
        "ds_write2_b32 v140, v54, v141 offset1:1\n\t" \
        "s_mov_b64 exec, -1\n\t" \
        "s_sub_u32 s46, s45, s48\n\t" \
        "s_lshr_b32 s46, s46, 3\n\t" \
        "s_cmp_gt_u32 s46, 64\n\t" \
        "s_cselect_b32 s54, 1, s54\n\t" \
        "s_cselect_b32 s46, 64, s46\n\t" \
        "s_lshl_b32 s55, s35, 2\n\t" \
        "v_mov_b32 v162, s55\n\t" \
        "s_sub_u32 s55, s46, 1\n\t" \
        "v_min_u32 v68, s55, v2\n\t" \
        "v_lshl_add_u32 v85, v68, 3, s48\n\t" \
        "s_waitcnt lgkmcnt(0)\n\t" \
        "ds_read_b64 v[70:71], v85\n\t" \
        "v_mul_i32_i24 v69, s53, v68\n\t" \
        "v_lshlrev_b32 v85, 3, v68\n\t" \
        "v_lshlrev_b32 v86, 2, v68\n\t" \
        "v_add_u32 v69, s49, v69\n\t" \
        "v_and_b32 v85, 16, v85\n\t" \
        "v_and_b32 v86, 32, v86\n\t" \
        "v_mov_b32 v87, 124\n\t" \
        "v_or3_b32 v69, v69, v85, v86\n\t" \
        "v_cvt_f32_u32 v76, s32\n\t" \
        "v_cvt_f32_u32 v77, s33\n\t" \
        "v_add_f32 v80, -2.0, v76\n\t" \
        "v_add_f32 v81, -2.0, v77\n\t" \
        "v_cvt_f32_u32 v82, 11\n\t" \
        "s_waitcnt lgkmcnt(0)\n\t" \
        "v_and_b32 v71, 0xffff, v71\n\t" \
        "v_lshlrev_b32 v72, 16, v70\n\t" \
        "v_and_b32 v73, 0xffff0000, v70\n\t" \
        "ds_write_b32 v71, v69\n\t" \
        "ds_write_b32 v71, v69 offset:1280\n\t" \
        "v_fract_f32 v74, v72\n\t" \
        "v_fract_f32 v75, v73\n\t" \
        "v_sub_f32 v72, v72, v80\n\t" \
        "v_sub_f32 v73, v73, v81\n\t" \
        "v_sub_f32 v76, 1.0, v74\n\t" \
        "v_sub_f32 v77, 1.0, v75\n\t" \
        "v_med3_f32 v80, v72, 0, v82\n\t" \
        "v_mov_b32 v85, 0x41980000\n\t" \
        "v_cvt_pk_bf16_f32 v76, v76, v77\n\t" \
        "v_med3_f32 v81, v73, 0, v85\n\t" \
        "v_cmp_neq_f32 s[4:5], v80, v72\n\t" \
        "v_and_b32 v77, 0xffff0000, v76\n\t" \
        "v_cmp_neq_f32 s[6:7], v81, v73\n\t" \
        "v_lshlrev_b32 v76, 16, v76\n\t" \
        "v_cvt_u32_f32 v80, v80\n\t" \
        "v_cvt_u32_f32 v81, v81\n\t" \
        "v_mul_f32 v78, v76, v77\n\t" \
        "v_mul_f32 v76, v76, v75\n\t" \
        "v_mul_f32 v79, v74, v77\n\t" \
        "v_mul_f32 v77, v74, v75\n\t" \
        "s_or_b64 s[4:5], s[4:5], s[6:7]\n\t" \
        "v_cvt_pk_bf16_f32 v78, v78, v76\n\t" \
        "v_cvt_pk_bf16_f32 v79, v79, v77\n\t" \
        "s_cselect_b32 s54, 1, s54\n\t" \
        "v_cmp_neq_f32 s[4:5], 0, v75\n\t" \
        "v_cmp_neq_f32 s[6:7], 0, v74\n\t" \
        "v_mul_u32_u24 v82, 124, v80\n\t" \
        "v_add_u32 v85, 1, v81\n\t" \
        "v_mad_u32_u24 v82, v81, 6, v82\n\t" \
        "v_bfe_u32 v86, v81, 2, 1\n\t" \
        "v_bfe_u32 v85, v85, 2, 1\n\t" \
        "s_mov_b32 s55, 0x8c2\n\t" \
        "v_add3_u32 v83, v82, v86, s55\n\t" \
        "s_mov_b32 s55, 0x8c8\n\t" \
        "v_add3_u32 v84, v82, v85, s55\n\t" \
        "s_and_b64 s[50:51], s[4:5], s[6:7]\n\t" \
        "v_cndmask_b32 v85, 0, v87, s[6:7]\n\t" \
        "v_mov_b32 v76, 0x05040100\n\t" \
        "v_add_u32 v85, v83, v85\n\t" \
        "v_perm_b32 v76, v79, v78, v76\n\t" \
        "v_cndmask_b32 v85, v85, v84, s[4:5]\n\t" \
        "v_mov_b32 v77, 0x80008000\n\t" \
        "v_cndmask_b32 v70, v76, v78, s[4:5]\n\t" \
        "v_lshl_or_b32 v83, v85, 16, v83\n\t" \
        "v_cndmask_b32 v77, 0, v77, s[50:51]\n\t" \
        "v_cndmask_b32 v71, 0, v79, s[50:51]\n\t" \
        "v_or_b32 v83, v83, v77\n\t" \
        "v_lshlrev_b32 v85, 2, v2\n\t" \
        "s_add_u32 s55, s48, 512\n\t" \
        "v_lshl_add_u32 v86, v2, 3, s48\n\t" \
        "v_add_u32 v85, s55, v85\n\t" \
        "ds_write_b64 v86, v[70:71]\n\t" \
        "ds_write_b32 v85, v83\n\t" \
        "s_cmp_lg_u32 s54, 0\n\t" \
        "s_cselect_b32 s55, 0x10000, s46\n\t" \
        "v_mov_b32 v163, s55\n\t" \
        "ds_write_b32 v162, v163\n\t" \
        "s_waitcnt lgkmcnt(0)\n\t" \
        "ds_read_u16 v120, v20\n\t" \
        "ds_read_u16 v121, v20 offset:8\n\t" \
        "ds_read_b32 v122, v21\n\t" \
        "v_mov_b32 v156, 0\n\t" \
        "s_mov_b32 s47, 0\n\t" \
        "s_waitcnt lgkmcnt(0)\n\t" \
        "v_lshlrev_b32 v116, 5, v120\n\t" \
        "v_bitop3_b32 v116, v116, s52, v23 bitop3:0x6a\n\t" \
        "v_xor_b32 v117, 8, v116\n\t" \
        "v_lshlrev_b32 v118, 5, v121\n\t" \
        "v_bitop3_b32 v118, v118, s52, v23 bitop3:0x6a\n\t" \
        "v_xor_b32 v119, 8, v118\n\t" \
        "v_and_b32 v112, v122, v25\n\t" \
        "v_and_b32 v113, v122, v26\n\t" \
        "v_and_b32 v114, v122, v27\n\t" \
        "v_and_b32 v115, v122, v28\n\t" \
        "ds_read_u16 v120, v20 offset:64\n\t" \
        "ds_read_u16 v121, v20 offset:72\n\t" \
        "ds_read_b32 v122, v21 offset:128\n\t" \
        "s_waitcnt vmcnt(6) lgkmcnt(0)\n\t" \
        "s_barrier\n\t" \
        "ds_read_b128 v[152:155], v156\n\t" \
        "ds_read_b64_tr_b16 v[64:65], v116 offset:0\n\t" \
        "ds_read_b64_tr_b16 v[66:67], v118 offset:0\n\t" \
        "ds_read_b64_tr_b16 v[68:69], v117 offset:0\n\t" \
        "ds_read_b64_tr_b16 v[70:71], v119 offset:0\n\t" \
        "ds_read_b64_tr_b16 v[72:73], v116 offset:64\n\t" \
        "ds_read_b64_tr_b16 v[74:75], v118 offset:64\n\t" \
        "ds_read_b64_tr_b16 v[76:77], v117 offset:64\n\t" \
        "ds_read_b64_tr_b16 v[78:79], v119 offset:64\n\t" \
        "ds_read_b64_tr_b16 v[80:81], v116 offset:128\n\t" \
        "ds_read_b64_tr_b16 v[82:83], v118 offset:128\n\t" \
        "ds_read_b64_tr_b16 v[84:85], v117 offset:128\n\t" \
        "ds_read_b64_tr_b16 v[86:87], v119 offset:128\n\t" \
        "s_waitcnt lgkmcnt(12)\n\t" \
        "v_add_u32 v152, v152, v154\n\t" \
        "v_add_u32 v153, v153, v155\n\t" \
        "v_max_u32 v152, v152, v153\n\t" \
        "s_nop 0\n\t" \
        "v_readfirstlane_b32 s55, v152\n\t" \
        "s_nop 0\n\t" \
        "s_cmp_gt_u32 s55, 56\n\t" \
        "s_cbranch_scc1 .Lfail%=\n\t" \
        ".LHeprod%=:\n\t" \
        "s_add_u32 s55, s47, 16\n\t" \
        "s_cmp_ge_i32 s55, s46\n\t" \
        "s_cbranch_scc1 .LHelast%=\n\t" \
        "s_waitcnt lgkmcnt(10)\n\t" \
        "v_mfma_f32_16x16x32_bf16 v[88:91], v[64:67], v[112:115], 0\n\t" \
        "v_lshlrev_b32 v116, 5, v120\n\t" \
        "v_bitop3_b32 v116, v116, s52, v23 bitop3:0x6a\n\t" \
        "v_xor_b32 v117, 8, v116\n\t" \
        "v_lshlrev_b32 v118, 5, v121\n\t" \
        "v_bitop3_b32 v118, v118, s52, v23 bitop3:0x6a\n\t" \
        "v_xor_b32 v119, 8, v118\n\t" \
        "ds_read_u16 v120, v20 offset:128\n\t" \
        "ds_read_u16 v121, v20 offset:136\n\t" \
        "ds_read_b64_tr_b16 v[64:65], v116 offset:0\n\t" \
        "ds_read_b64_tr_b16 v[66:67], v118 offset:0\n\t" \
        "s_waitcnt lgkmcnt(12)\n\t" \
        "v_mfma_f32_16x16x32_bf16 v[92:95], v[68:71], v[112:115], 0\n\t" \
        "ds_read_b64_tr_b16 v[68:69], v117 offset:0\n\t" \
        "ds_read_b64_tr_b16 v[70:71], v119 offset:0\n\t" \
        "s_waitcnt lgkmcnt(12)\n\t" \
        "v_mfma_f32_16x16x32_bf16 v[96:99], v[72:75], v[112:115], 0\n\t" \
        "ds_read_b64_tr_b16 v[72:73], v116 offset:64\n\t" \
        "ds_read_b64_tr_b16 v[74:75], v118 offset:64\n\t" \
        "s_waitcnt lgkmcnt(12)\n\t" \
        "v_mfma_f32_16x16x32_bf16 v[100:103], v[76:79], v[112:115], 0\n\t" \
        "ds_read_b64_tr_b16 v[76:77], v117 offset:64\n\t" \
        "ds_read_b64_tr_b16 v[78:79], v119 offset:64\n\t" \
        "s_waitcnt lgkmcnt(12)\n\t" \
        "v_mfma_f32_16x16x32_bf16 v[104:107], v[80:83], v[112:115], 0\n\t" \
        "ds_read_b64_tr_b16 v[80:81], v116 offset:128\n\t" \
        "ds_read_b64_tr_b16 v[82:83], v118 offset:128\n\t" \
        "s_waitcnt lgkmcnt(12)\n\t" \
        "v_mfma_f32_16x16x32_bf16 v[108:111], v[84:87], v[112:115], 0\n\t" \
        "ds_read_b64_tr_b16 v[84:85], v117 offset:128\n\t" \
        "ds_read_b64_tr_b16 v[86:87], v119 offset:128\n\t" \
        "v_and_b32 v112, v122, v25\n\t" \
        "v_and_b32 v113, v122, v26\n\t" \
        "v_and_b32 v114, v122, v27\n\t" \
        "v_and_b32 v115, v122, v28\n\t" \
        "ds_read_b32 v122, v21 offset:256\n\t" \
        "s_and_b32 s55, s50, 0xffff\n\t" \
        "s_cbranch_scc1 .LHetwo%=\n\t" \
        ".LHeback%=:\n\t" \
        "s_sub_u32 s55, s46, s47\n\t" \
        "v_xor_b32 v156, 8, v22\n\t" \
        "v_cmp_gt_u32 vcc, s55, v24\n\t" \
        "s_mov_b64 exec, vcc\n\t" \
        "v_cvt_pk_bf16_f32 v88, v88, v89\n\t" \
        "v_cvt_pk_bf16_f32 v89, v90, v91\n\t" \
        "ds_write_b64 v22, v[88:89] offset:0\n\t" \
        "v_cvt_pk_bf16_f32 v92, v92, v93\n\t" \
        "v_cvt_pk_bf16_f32 v93, v94, v95\n\t" \
        "ds_write_b64 v156, v[92:93] offset:0\n\t" \
        "v_cvt_pk_bf16_f32 v96, v96, v97\n\t" \
        "v_cvt_pk_bf16_f32 v97, v98, v99\n\t" \
        "ds_write_b64 v22, v[96:97] offset:64\n\t" \
        "v_cvt_pk_bf16_f32 v100, v100, v101\n\t" \
        "v_cvt_pk_bf16_f32 v101, v102, v103\n\t" \
        "ds_write_b64 v156, v[100:101] offset:64\n\t" \
        "v_cvt_pk_bf16_f32 v104, v104, v105\n\t" \
        "v_cvt_pk_bf16_f32 v105, v106, v107\n\t" \
        "ds_write_b64 v22, v[104:105] offset:128\n\t" \
        "v_cvt_pk_bf16_f32 v108, v108, v109\n\t" \
        "v_cvt_pk_bf16_f32 v109, v110, v111\n\t" \
        "ds_write_b64 v156, v[108:109] offset:128\n\t" \
        "s_mov_b64 exec, -1\n\t" \
        "s_lshl_b32 s55, s53, 4\n\t" \
        "s_add_u32 s47, s47, 16\n\t" \
        "v_add_u32 v22, s55, v22\n\t" \
        "s_lshr_b64 s[50:51], s[50:51], 16\n\t" \
        "v_add_u32 v20, 64, v20\n\t" \
        "v_add_u32 v21, 0x80, v21\n\t" \
        "s_cmp_lt_i32 s47, s46\n\t" \
        "s_cbranch_scc1 .LHeprod%=\n\t" \
        "s_branch .LHeend%=\n\t" \
        ".LHelast%=:\n\t" \
        "s_waitcnt lgkmcnt(10)\n\t" \
        "v_mfma_f32_16x16x32_bf16 v[88:91], v[64:67], v[112:115], 0\n\t" \
        "s_waitcnt lgkmcnt(8)\n\t" \
        "v_mfma_f32_16x16x32_bf16 v[92:95], v[68:71], v[112:115], 0\n\t" \
        "s_waitcnt lgkmcnt(6)\n\t" \
        "v_mfma_f32_16x16x32_bf16 v[96:99], v[72:75], v[112:115], 0\n\t" \
        "s_waitcnt lgkmcnt(4)\n\t" \
        "v_mfma_f32_16x16x32_bf16 v[100:103], v[76:79], v[112:115], 0\n\t" \
        "s_waitcnt lgkmcnt(2)\n\t" \
        "v_mfma_f32_16x16x32_bf16 v[104:107], v[80:83], v[112:115], 0\n\t" \
        "s_waitcnt lgkmcnt(0)\n\t" \
        "v_mfma_f32_16x16x32_bf16 v[108:111], v[84:87], v[112:115], 0\n\t" \
        "s_and_b32 s55, s50, 0xffff\n\t" \
        "s_cbranch_scc1 .LHetwo%=\n\t" \
        "s_nop 1\n\t" \
        "s_branch .LHeback%=\n\t" \
        ".LHetwo%=:\n\t" \
        "ds_read_u16 v156, v20\n\t" \
        "ds_read_u16 v158, v20 offset:8\n\t" \
        "ds_read_b32 v127, v21 offset:4\n\t" \
        "s_waitcnt lgkmcnt(0)\n\t" \
        "v_lshrrev_b32 v124, 15, v156\n\t" \
        "v_lshlrev_b32 v156, 5, v156\n\t" \
        "v_bitop3_b32 v156, v156, s52, v23 bitop3:0x6a\n\t" \
        "v_mul_u32_u24 v124, 3968, v124\n\t" \
        "v_add_u32 v156, v156, v124\n\t" \
        "v_xor_b32 v157, 8, v156\n\t" \
        "v_lshrrev_b32 v125, 15, v158\n\t" \
        "v_lshlrev_b32 v158, 5, v158\n\t" \
        "v_bitop3_b32 v158, v158, s52, v23 bitop3:0x6a\n\t" \
        "v_mul_u32_u24 v125, 3968, v125\n\t" \
        "v_add_u32 v158, v158, v125\n\t" \
        "v_xor_b32 v159, 8, v158\n\t" \
        "ds_read_b64_tr_b16 v[128:129], v156 offset:0\n\t" \
        "ds_read_b64_tr_b16 v[130:131], v158 offset:0\n\t" \
        "ds_read_b64_tr_b16 v[132:133], v157 offset:0\n\t" \
        "ds_read_b64_tr_b16 v[134:135], v159 offset:0\n\t" \
        "ds_read_b64_tr_b16 v[136:137], v156 offset:64\n\t" \
        "ds_read_b64_tr_b16 v[138:139], v158 offset:64\n\t" \
        "ds_read_b64_tr_b16 v[140:141], v157 offset:64\n\t" \
        "ds_read_b64_tr_b16 v[142:143], v159 offset:64\n\t" \
        "ds_read_b64_tr_b16 v[144:145], v156 offset:128\n\t" \
        "ds_read_b64_tr_b16 v[146:147], v158 offset:128\n\t" \
        "ds_read_b64_tr_b16 v[148:149], v157 offset:128\n\t" \
        "ds_read_b64_tr_b16 v[150:151], v159 offset:128\n\t" \
        "v_and_b32 v124, v127, v25\n\t" \
        "v_and_b32 v125, v127, v26\n\t" \
        "v_and_b32 v126, v127, v27\n\t" \
        "v_and_b32 v127, v127, v28\n\t" \
        "s_nop 0\n\t" \
        "s_waitcnt lgkmcnt(10)\n\t" \
        "v_mfma_f32_16x16x32_bf16 v[88:91], v[128:131], v[124:127], v[88:91]\n\t" \
        "s_waitcnt lgkmcnt(8)\n\t" \
        "v_mfma_f32_16x16x32_bf16 v[92:95], v[132:135], v[124:127], v[92:95]\n\t" \
        "s_waitcnt lgkmcnt(6)\n\t" \
        "v_mfma_f32_16x16x32_bf16 v[96:99], v[136:139], v[124:127], v[96:99]\n\t" \
        "s_waitcnt lgkmcnt(4)\n\t" \
        "v_mfma_f32_16x16x32_bf16 v[100:103], v[140:143], v[124:127], v[100:103]\n\t" \
        "s_waitcnt lgkmcnt(2)\n\t" \
        "v_mfma_f32_16x16x32_bf16 v[104:107], v[144:147], v[124:127], v[104:107]\n\t" \
        "s_waitcnt lgkmcnt(0)\n\t" \
        "v_mfma_f32_16x16x32_bf16 v[108:111], v[148:151], v[124:127], v[108:111]\n\t" \
        "s_nop 7\n\t" \
        "s_branch .LHeback%=\n\t" \
        ".LHeend%=:\n\t" \
        "s_branch .LH_join%=\n\t" \
        ".LH4p%=:\n\t" \
        "s_nop 4\n\t" \
        "v_cndmask_b32 v140, v40, v41, s[62:63]\n\t" \
        "v_lshlrev_b32 v141, 16, v140\n\t" \
        "v_and_b32 v142, s42, v140\n\t" \
        "v_add_f32 v141, v55, v141\n\t" \
        "v_add_f32 v142, v59, v142\n\t" \
        "v_cvt_pk_bf16_f32 v50, v141, v142\n\t" \
        "v_pk_max_i16 v50, v50, 0\n\t" \
        "v_pk_min_i16 v50, v50, s45\n\t" \
        "v_lshlrev_b32 v141, 16, v50\n\t" \
        "v_and_b32 v142, s42, v50\n\t" \
        "v_pk_min_i16 v66, v50, v50\n\t" \
        "v_fract_f32 v143, v141\n\t" \
        "v_fract_f32 v144, v142\n\t" \
        "v_cvt_u32_f32 v145, v141\n\t" \
        "s_add_u32 s57, s40, 0x3000\n\t" \
        "s_add_u32 m0, s39, 0x3000\n\t" \
        "s_nop 0\n\t" \
        "buffer_load_dwordx4 v3, s[24:27], s57 offen offset:0 lds\n\t" \
        "v_cvt_u32_f32 v146, v142\n\t" \
        "v_cmp_neq_f32 s[10:11], 0, v143\n\t" \
        "v_cmp_neq_f32 vcc, 0, v144\n\t" \
        "v_add_u32 v146, 2, v146\n\t" \
        "v_pk_max_i16 v67, v50, v50\n\t" \
        "s_and_b64 s[10:11], s[10:11], s[64:65]\n\t" \
        "v_mad_u32_u24 v148, v146, s43, v65\n\t" \
        "v_bfe_u32 v147, v146, 2, 1\n\t" \
        "v_sub_f32 v141, 1.0, v143\n\t" \
        "v_addc_co_u32 v145, s[4:5], 0, v145, s[10:11]\n\t" \
        "v_sub_f32 v142, 1.0, v144\n\t" \
        "v_add_u32 v146, 1, v146\n\t" \
        "v_mad_u32_u24 v148, v145, s44, v148\n\t" \
        "v_cvt_pk_bf16_f32 v152, v141, v142\n\t" \
        "buffer_load_dwordx4 v4, s[24:27], s57 offen offset:1024 lds\n\t" \
        "v_lshl_add_u32 v149, v147, 5, v148\n\t" \
        "v_bfe_u32 v147, v146, 2, 1\n\t" \
        "v_add_u32 v148, s43, v148\n\t" \
        "v_lshlrev_b32 v141, 16, v152\n\t" \
        "v_lshl_add_u32 v148, v147, 5, v148\n\t" \
        "v_and_b32 v152, s42, v152\n\t" \
        "v_cndmask_b32 v141, v141, v143, s[64:65]\n\t" \
        "v_cndmask_b32 v150, v149, v148, vcc\n\t" \
        "v_mul_f32 v152, v141, v152\n\t" \
        "v_mul_f32 v144, v141, v144\n\t" \
        "ds_write2st64_b32 v63, v149, v150 offset0:0 offset1:5\n\t" \
        "v_cvt_pk_bf16_f32 v151, v152, v144\n\t" \
        "ds_write_b32 v64, v151 offset:0\n\t" \
        "v_cndmask_b32 v140, v42, v43, s[62:63]\n\t" \
        "v_lshlrev_b32 v141, 16, v140\n\t" \
        "v_and_b32 v142, s42, v140\n\t" \
        "v_add_f32 v141, v56, v141\n\t" \
        "v_add_f32 v142, v60, v142\n\t" \
        "v_cvt_pk_bf16_f32 v51, v141, v142\n\t" \
        "v_pk_max_i16 v51, v51, 0\n\t" \
        "v_pk_min_i16 v51, v51, s45\n\t" \
        "v_lshlrev_b32 v141, 16, v51\n\t" \
        "v_and_b32 v142, s42, v51\n\t" \
        "v_pk_min_i16 v66, v66, v51\n\t" \
        "v_fract_f32 v143, v141\n\t" \
        "v_fract_f32 v144, v142\n\t" \
        "v_cvt_u32_f32 v145, v141\n\t" \
        "v_cvt_u32_f32 v146, v142\n\t" \
        "v_cmp_neq_f32 s[10:11], 0, v143\n\t" \
        "v_cmp_neq_f32 vcc, 0, v144\n\t" \
        "v_add_u32 v146, 2, v146\n\t" \
        "v_pk_max_i16 v67, v67, v51\n\t" \
        "s_and_b64 s[10:11], s[10:11], s[64:65]\n\t" \
        "v_mad_u32_u24 v148, v146, s43, v65\n\t" \
        "buffer_load_dwordx4 v5, s[24:27], s57 offen offset:2048 lds\n\t" \
        "v_bfe_u32 v147, v146, 2, 1\n\t" \
        "v_sub_f32 v141, 1.0, v143\n\t" \
        "v_addc_co_u32 v145, s[4:5], 0, v145, s[10:11]\n\t" \
        "v_sub_f32 v142, 1.0, v144\n\t" \
        "v_add_u32 v146, 1, v146\n\t" \
        "v_mad_u32_u24 v148, v145, s44, v148\n\t" \
        "v_cvt_pk_bf16_f32 v152, v141, v142\n\t" \
        "v_lshl_add_u32 v149, v147, 5, v148\n\t" \
        "v_bfe_u32 v147, v146, 2, 1\n\t" \
        "v_add_u32 v148, s43, v148\n\t" \
        "v_lshlrev_b32 v141, 16, v152\n\t" \
        "v_lshl_add_u32 v148, v147, 5, v148\n\t" \
        "v_and_b32 v152, s42, v152\n\t" \
        "v_cndmask_b32 v141, v141, v143, s[64:65]\n\t" \
        "v_cndmask_b32 v150, v149, v148, vcc\n\t" \
        "v_mul_f32 v152, v141, v152\n\t" \
        "v_mul_f32 v144, v141, v144\n\t" \
        "ds_write2st64_b32 v63, v149, v150 offset0:1 offset1:6\n\t" \
        "v_cvt_pk_bf16_f32 v151, v152, v144\n\t" \
        "ds_write_b32 v64, v151 offset:256\n\t" \
        "v_cndmask_b32 v140, v44, v45, s[62:63]\n\t" \
        "v_lshlrev_b32 v141, 16, v140\n\t" \
        "v_and_b32 v142, s42, v140\n\t" \
        "v_add_f32 v141, v57, v141\n\t" \
        "v_add_f32 v142, v61, v142\n\t" \
        "v_cvt_pk_bf16_f32 v52, v141, v142\n\t" \
        "v_pk_max_i16 v52, v52, 0\n\t" \
        "v_pk_min_i16 v52, v52, s45\n\t" \
        "v_lshlrev_b32 v141, 16, v52\n\t" \
        "v_and_b32 v142, s42, v52\n\t" \
        "v_pk_min_i16 v66, v66, v52\n\t" \
        "v_fract_f32 v143, v141\n\t" \
        "v_fract_f32 v144, v142\n\t" \
        "v_cvt_u32_f32 v145, v141\n\t" \
        "v_cvt_u32_f32 v146, v142\n\t" \
        "v_cmp_neq_f32 s[10:11], 0, v143\n\t" \
        "v_cmp_neq_f32 vcc, 0, v144\n\t" \
        "v_add_u32 v146, 2, v146\n\t" \
        "v_pk_max_i16 v67, v67, v52\n\t" \
        "s_and_b64 s[10:11], s[10:11], s[64:65]\n\t" \
        "v_mad_u32_u24 v148, v146, s43, v65\n\t" \
        "s_add_u32 s57, s40, 0x6000\n\t" \
        "s_add_u32 m0, s39, 0x6000\n\t" \
        "s_nop 0\n\t" \
        "buffer_load_dwordx4 v3, s[24:27], s57 offen offset:0 lds\n\t" \
        "v_bfe_u32 v147, v146, 2, 1\n\t" \
        "v_sub_f32 v141, 1.0, v143\n\t" \
        "v_addc_co_u32 v145, s[4:5], 0, v145, s[10:11]\n\t" \
        "v_sub_f32 v142, 1.0, v144\n\t" \
        "v_add_u32 v146, 1, v146\n\t" \
        "v_mad_u32_u24 v148, v145, s44, v148\n\t" \
        "v_cvt_pk_bf16_f32 v152, v141, v142\n\t" \
        "v_lshl_add_u32 v149, v147, 5, v148\n\t" \
        "v_bfe_u32 v147, v146, 2, 1\n\t" \
        "v_add_u32 v148, s43, v148\n\t" \
        "v_lshlrev_b32 v141, 16, v152\n\t" \
        "v_lshl_add_u32 v148, v147, 5, v148\n\t" \
        "v_and_b32 v152, s42, v152\n\t" \
        "v_cndmask_b32 v141, v141, v143, s[64:65]\n\t" \
        "v_cndmask_b32 v150, v149, v148, vcc\n\t" \
        "v_mul_f32 v152, v141, v152\n\t" \
        "v_mul_f32 v144, v141, v144\n\t" \
        "ds_write2st64_b32 v63, v149, v150 offset0:2 offset1:7\n\t" \
        "v_cvt_pk_bf16_f32 v151, v152, v144\n\t" \
        "ds_write_b32 v64, v151 offset:512\n\t" \
        "v_cndmask_b32 v140, v46, v47, s[62:63]\n\t" \
        "v_lshlrev_b32 v141, 16, v140\n\t" \
        "v_and_b32 v142, s42, v140\n\t" \
        "v_add_f32 v141, v58, v141\n\t" \
        "v_add_f32 v142, v59, v142\n\t" \
        "v_cvt_pk_bf16_f32 v53, v141, v142\n\t" \
        "v_pk_max_i16 v53, v53, 0\n\t" \
        "v_pk_min_i16 v53, v53, s45\n\t" \
        "v_lshlrev_b32 v141, 16, v53\n\t" \
        "v_and_b32 v142, s42, v53\n\t" \
        "v_pk_min_i16 v66, v66, v53\n\t" \
        "v_fract_f32 v143, v141\n\t" \
        "v_fract_f32 v144, v142\n\t" \
        "v_cvt_u32_f32 v145, v141\n\t" \
        "v_cvt_u32_f32 v146, v142\n\t" \
        "v_cmp_neq_f32 s[10:11], 0, v143\n\t" \
        "v_cmp_neq_f32 vcc, 0, v144\n\t" \
        "v_add_u32 v146, 2, v146\n\t" \
        "v_pk_max_i16 v67, v67, v53\n\t" \
        "s_and_b64 s[10:11], s[10:11], s[64:65]\n\t" \
        "v_mad_u32_u24 v148, v146, s43, v65\n\t" \
        "buffer_load_dwordx4 v4, s[24:27], s57 offen offset:1024 lds\n\t" \
        "v_bfe_u32 v147, v146, 2, 1\n\t" \
        "v_sub_f32 v141, 1.0, v143\n\t" \
        "v_addc_co_u32 v145, s[4:5], 0, v145, s[10:11]\n\t" \
        "v_sub_f32 v142, 1.0, v144\n\t" \
        "v_add_u32 v146, 1, v146\n\t" \
        "v_mad_u32_u24 v148, v145, s44, v148\n\t" \
        "v_cvt_pk_bf16_f32 v152, v141, v142\n\t" \
        "v_lshl_add_u32 v149, v147, 5, v148\n\t" \
        "v_bfe_u32 v147, v146, 2, 1\n\t" \
        "v_add_u32 v148, s43, v148\n\t" \
        "v_lshlrev_b32 v141, 16, v152\n\t" \
        "v_lshl_add_u32 v148, v147, 5, v148\n\t" \
        "v_and_b32 v152, s42, v152\n\t" \
        "v_cndmask_b32 v141, v141, v143, s[64:65]\n\t" \
        "v_cndmask_b32 v150, v149, v148, vcc\n\t" \
        "v_mul_f32 v152, v141, v152\n\t" \
        "v_mul_f32 v144, v141, v144\n\t" \
        "ds_write2st64_b32 v63, v149, v150 offset0:3 offset1:8\n\t" \
        "v_cvt_pk_bf16_f32 v151, v152, v144\n\t" \
        "ds_write_b32 v64, v151 offset:768\n\t" \
        "v_lshlrev_b32 v141, 16, v48\n\t" \
        "v_and_b32 v142, s42, v48\n\t" \
        "v_add_f32 v141, v58, v141\n\t" \
        "v_add_f32 v142, v62, v142\n\t" \
        "v_cvt_pk_bf16_f32 v54, v141, v142\n\t" \
        "v_pk_max_i16 v54, v54, 0\n\t" \
        "v_pk_min_i16 v54, v54, s45\n\t" \
        "v_lshlrev_b32 v141, 16, v54\n\t" \
        "v_and_b32 v142, s42, v54\n\t" \
        "v_pk_min_i16 v66, v66, v54\n\t" \
        "v_fract_f32 v143, v141\n\t" \
        "v_fract_f32 v144, v142\n\t" \
        "v_cvt_u32_f32 v145, v141\n\t" \
        "v_cvt_u32_f32 v146, v142\n\t" \
        "v_cmp_neq_f32 s[10:11], 0, v143\n\t" \
        "v_cmp_neq_f32 vcc, 0, v144\n\t" \
        "v_add_u32 v146, 2, v146\n\t" \
        "v_pk_max_i16 v67, v67, v54\n\t" \
        "s_and_b64 s[10:11], s[10:11], s[64:65]\n\t" \
        "v_mad_u32_u24 v148, v146, s43, v65\n\t" \
        "v_bfe_u32 v147, v146, 2, 1\n\t" \
        "buffer_load_dwordx4 v5, s[24:27], s57 offen offset:2048 lds\n\t" \
        "v_sub_f32 v141, 1.0, v143\n\t" \
        "v_addc_co_u32 v145, s[4:5], 0, v145, s[10:11]\n\t" \
        "v_sub_f32 v142, 1.0, v144\n\t" \
        "v_add_u32 v146, 1, v146\n\t" \
        "v_mad_u32_u24 v148, v145, s44, v148\n\t" \
        "v_cvt_pk_bf16_f32 v152, v141, v142\n\t" \
        "v_lshl_add_u32 v149, v147, 5, v148\n\t" \
        "v_bfe_u32 v147, v146, 2, 1\n\t" \
        "v_add_u32 v148, s43, v148\n\t" \
        "v_lshlrev_b32 v141, 16, v152\n\t" \
        "v_lshl_add_u32 v148, v147, 5, v148\n\t" \
        "v_and_b32 v152, s42, v152\n\t" \
        "v_cndmask_b32 v141, v141, v143, s[64:65]\n\t" \
        "v_cndmask_b32 v150, v149, v148, vcc\n\t" \
        "v_mul_f32 v152, v141, v152\n\t" \
        "v_mul_f32 v144, v141, v144\n\t" \
        "ds_write2st64_b32 v63, v149, v150 offset0:4 offset1:9\n\t" \
        "v_cvt_pk_bf16_f32 v151, v152, v144\n\t" \
        "ds_write_b32 v64, v151 offset:1024\n\t" \
        "v_pk_max_i16 v140, v66, v14\n\t" \
        "v_pk_min_i16 v141, v67, v15\n\t" \
        "v_cmp_ne_u32 s[10:11], v140, v66\n\t" \
        "v_cmp_ne_u32 vcc, v141, v67\n\t" \
        "s_nop 0\n\t" \
        "s_or_b64 s[10:11], s[10:11], vcc\n\t" \
        "s_cselect_b32 s54, 1, 0\n\t" \
        ".LH_none%=:\n\t" \
        "s_lshl_b32 s55, s35, 2\n\t" \
        "s_cmp_lg_u32 s54, 0\n\t" \
        "v_mov_b32 v162, s55\n\t" \
        "s_cselect_b32 s55, 0x10000, 0\n\t" \
        "v_mov_b32 v163, s55\n\t" \
        "ds_write_b32 v162, v163\n\t" \
        "v_mov_b32 v163, 0\n\t" \
        "s_waitcnt vmcnt(6) lgkmcnt(0)\n\t" \
        "s_barrier\n\t" \
        "ds_read_b128 v[152:155], v163\n\t" \
        "s_waitcnt lgkmcnt(0)\n\t" \
        "v_add_u32 v152, v152, v154\n\t" \
        "v_add_u32 v153, v153, v155\n\t" \
        "v_max_u32 v152, v152, v153\n\t" \
        "s_nop 0\n\t" \
        "v_readfirstlane_b32 s55, v152\n\t" \
        "s_nop 0\n\t" \
        "s_cmp_gt_u32 s55, 56\n\t" \
        "s_cbranch_scc1 .Lfail%=\n\t" \
        ".LH_join%=:\n\t" \
        "s_waitcnt lgkmcnt(0)\n\t" \
        "ds_read_b128 v[170:173], v9 offset:0\n\t" \
        "ds_read_b64 v[150:151], v10 offset:0\n\t" \
        "ds_read_b64_tr_b16 a[0:1], v6 offset:0\n\t" \
        "ds_read_b64_tr_b16 a[2:3], v6 offset:3072\n\t" \
        "ds_read_b64_tr_b16 a[4:5], v7 offset:32\n\t" \
        "ds_read_b64_tr_b16 a[6:7], v7 offset:3104\n\t" \
        "ds_read_b64_tr_b16 a[8:9], v6 offset:64\n\t" \
        "ds_read_b64_tr_b16 a[10:11], v6 offset:3136\n\t" \
        "s_waitcnt lgkmcnt(7)\n\t" \
        "v_xor_b32 v134, v170, v11\n\t" \
        "v_xor_b32 v135, v171, v11\n\t" \
        "v_xor_b32 v136, v172, v11\n\t" \
        "v_xor_b32 v138, v170, v12\n\t" \
        "v_xor_b32 v137, v173, v11\n\t" \
        "v_xor_b32 v139, v171, v12\n\t" \
        "v_xor_b32 v140, v172, v12\n\t" \
        "v_xor_b32 v141, v173, v12\n\t" \
        "ds_read_b128 v[174:177], v9 offset:128\n\t" \
        "ds_read_b64_tr_b16 a[96:97], v134 offset:0\n\t" \
        "ds_read_b64_tr_b16 a[98:99], v135 offset:0\n\t" \
        "ds_read_b64_tr_b16 a[100:101], v138 offset:0\n\t" \
        "s_waitcnt lgkmcnt(10)\n\t" \
        "v_and_b32 v154, v150, v25\n\t" \
        "ds_read_b64_tr_b16 a[102:103], v139 offset:0\n\t" \
        "ds_read_b64_tr_b16 a[104:105], v136 offset:0\n\t" \
        "v_and_b32 v155, v150, v26\n\t" \
        "ds_read_b64_tr_b16 a[106:107], v137 offset:0\n\t" \
        "ds_read_b64_tr_b16 a[108:109], v140 offset:0\n\t" \
        "v_and_b32 v156, v150, v27\n\t" \
        "ds_read_b64_tr_b16 a[110:111], v141 offset:0\n\t" \
        "ds_read_b64_tr_b16 a[112:113], v134 offset:64\n\t" \
        "v_and_b32 v157, v150, v28\n\t" \
        "ds_read_b64_tr_b16 a[114:115], v135 offset:64\n\t" \
        "ds_read_b64_tr_b16 a[116:117], v138 offset:64\n\t" \
        "ds_read_b64_tr_b16 a[118:119], v139 offset:64\n\t" \
        "ds_read_b64_tr_b16 a[120:121], v136 offset:64\n\t" \
        "ds_read_b64_tr_b16 a[122:123], v137 offset:64\n\t" \
        "ds_read_b64_tr_b16 a[124:125], v140 offset:64\n\t" \
        "v_and_b32 v158, v151, v25\n\t" \
        "ds_read_b64_tr_b16 a[126:127], v141 offset:64\n\t" \
        "ds_read_b64_tr_b16 a[128:129], v134 offset:128\n\t" \
        "v_and_b32 v159, v151, v26\n\t" \
        "ds_read_b64_tr_b16 a[130:131], v135 offset:128\n\t" \
        "ds_read_b64_tr_b16 a[132:133], v138 offset:128\n\t" \
        "v_and_b32 v160, v151, v27\n\t" \
        "ds_read_b64_tr_b16 a[134:135], v139 offset:128\n\t" \
        "ds_read_b64_tr_b16 a[136:137], v136 offset:128\n\t" \
        "s_waitcnt lgkmcnt(15)\n\t" \
        "v_mfma_f32_16x16x32_bf16 v[102:105], a[96:99], v[154:157], 0\n\t" \
        "ds_read_b64_tr_b16 a[138:139], v137 offset:128\n\t" \
        "ds_read_b64_tr_b16 a[140:141], v140 offset:128\n\t" \
        "v_and_b32 v161, v151, v28\n\t" \
        "v_xor_b32 v142, v174, v11\n\t" \
        "v_xor_b32 v143, v175, v11\n\t" \
        "v_xor_b32 v144, v176, v11\n\t" \
        "v_xor_b32 v146, v174, v12\n\t" \
        "v_xor_b32 v145, v177, v11\n\t" \
        "v_xor_b32 v147, v175, v12\n\t" \
        "v_mfma_f32_16x16x32_bf16 v[106:109], a[100:103], v[154:157], 0\n\t" \
        "s_add_u32 m0, s39, 36864\n\t" \
        "s_add_u32 s57, s40, 36864\n\t" \
        "buffer_load_dwordx4 v3, s[24:27], s57 offen offset:0 lds\n\t" \
        "ds_read_b64_tr_b16 a[142:143], v141 offset:128\n\t" \
        "v_mfma_f32_16x16x32_bf16 v[110:113], a[104:107], v[158:161], 0\n\t" \
        "ds_read_b64 v[152:153], v10 offset:128\n\t" \
        "ds_read_b64_tr_b16 a[12:13], v7 offset:96\n\t" \
        "v_xor_b32 v148, v176, v12\n\t" \
        "v_xor_b32 v149, v177, v12\n\t" \
        "s_waitcnt lgkmcnt(15)\n\t" \
        "v_mfma_f32_16x16x32_bf16 v[114:117], a[108:111], v[158:161], 0\n\t" \
        "ds_read_b64_tr_b16 a[14:15], v7 offset:3168\n\t" \
        "v_mfma_f32_16x16x32_bf16 v[118:121], a[112:115], v[154:157], 0\n\t" \
        "ds_read_b64_tr_b16 a[16:17], v6 offset:128\n\t" \
        "v_cvt_pk_bf16_f32 v78, v102, v103\n\t" \
        "v_cvt_pk_bf16_f32 v79, v104, v105\n\t" \
        "s_waitcnt lgkmcnt(15)\n\t" \
        "v_mfma_f32_16x16x32_bf16 v[122:125], a[116:119], v[154:157], 0\n\t" \
        "ds_read_b64_tr_b16 a[18:19], v6 offset:3200\n\t" \
        "ds_read_b64_tr_b16 a[20:21], v7 offset:160\n\t" \
        "v_cvt_pk_bf16_f32 v80, v106, v107\n\t" \
        "v_cvt_pk_bf16_f32 v81, v108, v109\n\t" \
        "s_waitcnt lgkmcnt(15)\n\t" \
        "v_mfma_f32_16x16x32_bf16 v[126:129], a[120:123], v[158:161], 0\n\t" \
        "buffer_load_dwordx4 v4, s[24:27], s57 offen offset:1024 lds\n\t" \
        "v_cvt_pk_bf16_f32 v82, v110, v111\n\t" \
        "s_waitcnt lgkmcnt(14)\n\t" \
        "v_mfma_f32_16x16x32_bf16 v[130:133], a[124:127], v[158:161], 0\n\t" \
        "ds_read_b64_tr_b16 a[22:23], v7 offset:3232\n\t" \
        "v_cvt_pk_bf16_f32 v83, v112, v113\n\t" \
        "v_cvt_pk_bf16_f32 v84, v114, v115\n\t" \
        "s_waitcnt lgkmcnt(13)\n\t" \
        "v_mfma_f32_16x16x32_bf16 v[102:105], a[128:131], v[154:157], 0\n\t" \
        "ds_read_b64_tr_b16 a[96:97], v142 offset:0\n\t" \
        "v_cvt_pk_bf16_f32 v85, v116, v117\n\t" \
        "s_waitcnt lgkmcnt(12)\n\t" \
        "v_mfma_f32_16x16x32_bf16 v[106:109], a[132:135], v[154:157], 0\n\t" \
        "ds_read_b64_tr_b16 a[98:99], v143 offset:0\n\t" \
        "ds_read_b64_tr_b16 a[100:101], v146 offset:0\n\t" \
        "s_waitcnt lgkmcnt(12)\n\t" \
        "v_mfma_f32_16x16x32_bf16 v[110:113], a[136:139], v[158:161], 0\n\t" \
        "ds_read_b64_tr_b16 a[102:103], v147 offset:0\n\t" \
        "v_cvt_pk_bf16_f32 v86, v118, v119\n\t" \
        "s_waitcnt lgkmcnt(11)\n\t" \
        "v_mfma_f32_16x16x32_bf16 v[114:117], a[140:143], v[158:161], 0\n\t" \
        "buffer_load_dwordx4 v5, s[24:27], s57 offen offset:2048 lds\n\t" \
        "v_mfma_f32_16x16x32_bf16 v[30:33], a[0:3], v[78:81], 0\n\t" \
        "ds_read_b64_tr_b16 a[104:105], v144 offset:0\n\t" \
        "s_waitcnt lgkmcnt(10)\n\t" \
        "v_and_b32 v162, v152, v25\n\t" \
        "v_mfma_f32_16x16x32_bf16 v[54:57], a[0:3], v[82:85], 0\n\t" \
        "ds_read_b64_tr_b16 a[106:107], v145 offset:0\n\t" \
        "ds_read_b64_tr_b16 a[108:109], v148 offset:0\n\t" \
        "v_mfma_f32_16x16x32_bf16 v[34:37], a[4:7], v[78:81], 0\n\t" \
        "ds_read_b64_tr_b16 a[110:111], v149 offset:0\n\t" \
        "v_and_b32 v163, v152, v26\n\t" \
        "v_mfma_f32_16x16x32_bf16 v[58:61], a[4:7], v[82:85], 0\n\t" \
        "ds_read_b64_tr_b16 a[24:25], v6 offset:6144\n\t" \
        "v_and_b32 v164, v152, v27\n\t" \
        "v_mfma_f32_16x16x32_bf16 v[38:41], a[8:11], v[78:81], 0\n\t" \
        "s_add_u32 m0, s39, 49152\n\t" \
        "s_add_u32 s57, s40, 49152\n\t" \
        "buffer_load_dwordx4 v3, s[24:27], s57 offen offset:0 lds\n\t" \
        "v_mfma_f32_16x16x32_bf16 v[62:65], a[8:11], v[82:85], 0\n\t" \
        "ds_read_b64_tr_b16 a[26:27], v6 offset:9216\n\t" \
        "ds_read_b64_tr_b16 a[28:29], v7 offset:6176\n\t" \
        "v_and_b32 v165, v152, v28\n\t" \
        "v_and_b32 v166, v153, v25\n\t" \
        "s_waitcnt lgkmcnt(11)\n\t" \
        "v_mfma_f32_16x16x32_bf16 v[42:45], a[12:15], v[78:81], 0\n\t" \
        "ds_read_b64_tr_b16 a[30:31], v7 offset:9248\n\t" \
        "v_cvt_pk_bf16_f32 v87, v120, v121\n\t" \
        "v_mfma_f32_16x16x32_bf16 v[66:69], a[12:15], v[82:85], 0\n\t" \
        "ds_read_b64_tr_b16 a[32:33], v6 offset:6208\n\t" \
        "ds_read_b64_tr_b16 a[34:35], v6 offset:9280\n\t" \
        "v_cvt_pk_bf16_f32 v88, v122, v123\n\t" \
        "v_and_b32 v167, v153, v26\n\t" \
        "v_mfma_f32_16x16x32_bf16 v[46:49], a[16:19], v[78:81], 0\n\t" \
        "ds_read_b64_tr_b16 a[36:37], v7 offset:6240\n\t" \
        "v_cvt_pk_bf16_f32 v89, v124, v125\n\t" \
        "v_cvt_pk_bf16_f32 v90, v126, v127\n\t" \
        "v_mfma_f32_16x16x32_bf16 v[70:73], a[16:19], v[82:85], 0\n\t" \
        "buffer_load_dwordx4 v4, s[24:27], s57 offen offset:1024 lds\n\t" \
        "v_mfma_f32_16x16x32_bf16 v[50:53], a[20:23], v[78:81], 0\n\t" \
        "ds_read_b64_tr_b16 a[38:39], v7 offset:9312\n\t" \
        "v_and_b32 v168, v153, v27\n\t" \
        "v_mfma_f32_16x16x32_bf16 v[74:77], a[20:23], v[82:85], 0\n\t" \
        "ds_read_b64_tr_b16 a[40:41], v6 offset:6272\n\t" \
        "ds_read_b64_tr_b16 a[42:43], v6 offset:9344\n\t" \
        "v_cvt_pk_bf16_f32 v91, v128, v129\n\t" \
        "v_cvt_pk_bf16_f32 v92, v130, v131\n\t" \
        "s_waitcnt lgkmcnt(11)\n\t" \
        "v_mfma_f32_16x16x32_bf16 v[118:121], a[96:99], v[162:165], 0\n\t" \
        "s_waitcnt vmcnt(8)\n\t" \
        "s_barrier\n\t" \
        "ds_read_b64_tr_b16 a[44:45], v7 offset:6304\n\t" \
        "v_and_b32 v169, v153, v28\n\t" \
        "v_cvt_pk_bf16_f32 v93, v132, v133\n\t" \
        "v_mfma_f32_16x16x32_bf16 v[122:125], a[100:103], v[162:165], 0\n\t" \
        "ds_read_b64_tr_b16 a[46:47], v7 offset:9376\n\t" \
        "v_cvt_pk_bf16_f32 v94, v102, v103\n\t" \
        "v_mfma_f32_16x16x32_bf16 v[126:129], a[104:107], v[166:169], 0\n\t" \
        "buffer_load_dwordx4 v5, s[24:27], s57 offen offset:2048 lds\n\t" \
        "s_waitcnt lgkmcnt(11)\n\t" \
        "v_mfma_f32_16x16x32_bf16 v[130:133], a[108:111], v[166:169], 0\n\t" \
        "ds_read_b64_tr_b16 a[112:113], v142 offset:64\n\t" \
        "ds_read_b64_tr_b16 a[114:115], v143 offset:64\n\t" \
        "s_waitcnt lgkmcnt(11)\n\t" \
        "v_mfma_f32_16x16x32_bf16 v[30:33], a[24:27], v[86:89], v[30:33]\n\t" \
        "ds_read_b64_tr_b16 a[116:117], v146 offset:64\n\t" \
        "v_cvt_pk_bf16_f32 v95, v104, v105\n\t" \
        "v_mfma_f32_16x16x32_bf16 v[54:57], a[24:27], v[90:93], v[54:57]\n\t" \
        "ds_read_b64_tr_b16 a[118:119], v147 offset:64\n\t" \
        "ds_read_b64_tr_b16 a[120:121], v144 offset:64\n\t" \
        "s_waitcnt lgkmcnt(11)\n\t" \
        "v_mfma_f32_16x16x32_bf16 v[34:37], a[28:31], v[86:89], v[34:37]\n\t" \
        "ds_read_b64_tr_b16 a[122:123], v145 offset:64\n\t" \
        "v_cvt_pk_bf16_f32 v96, v106, v107\n\t" \
        "v_mfma_f32_16x16x32_bf16 v[58:61], a[28:31], v[90:93], v[58:61]\n\t" \
        "ds_read_b64_tr_b16 a[124:125], v148 offset:64\n\t" \
        "v_cvt_pk_bf16_f32 v97, v108, v109\n\t" \
        "v_mfma_f32_16x16x32_bf16 v[38:41], a[32:35], v[86:89], v[38:41]\n\t" \
        "ds_read_b64_tr_b16 a[126:127], v149 offset:64\n\t" \
        "ds_read_b64_tr_b16 a[48:49], v6 offset:12288\n\t" \
        "v_mfma_f32_16x16x32_bf16 v[62:65], a[32:35], v[90:93], v[62:65]\n\t" \
        "ds_read_b64_tr_b16 a[50:51], v6 offset:15360\n\t" \
        "v_cvt_pk_bf16_f32 v98, v110, v111\n\t" \
        "s_waitcnt lgkmcnt(12)\n\t" \
        "v_mfma_f32_16x16x32_bf16 v[42:45], a[36:39], v[86:89], v[42:45]\n\t" \
        "ds_read_b128 v[170:173], v9 offset:256\n\t" \
        "v_cvt_pk_bf16_f32 v99, v112, v113\n\t" \
        "v_mfma_f32_16x16x32_bf16 v[66:69], a[36:39], v[90:93], v[66:69]\n\t" \
        "ds_read_b64_tr_b16 a[52:53], v7 offset:12320\n\t" \
        "ds_read_b64_tr_b16 a[54:55], v7 offset:15392\n\t" \
        "v_mfma_f32_16x16x32_bf16 v[46:49], a[40:43], v[86:89], v[46:49]\n\t" \
        "ds_read_b64_tr_b16 a[56:57], v6 offset:12352\n\t" \
        "v_cvt_pk_bf16_f32 v100, v114, v115\n\t" \
        "v_mfma_f32_16x16x32_bf16 v[70:73], a[40:43], v[90:93], v[70:73]\n\t" \
        "ds_read_b64_tr_b16 a[58:59], v6 offset:15424\n\t" \
        "v_cvt_pk_bf16_f32 v101, v116, v117\n\t" \
        "s_waitcnt lgkmcnt(13)\n\t" \
        "v_mfma_f32_16x16x32_bf16 v[50:53], a[44:47], v[86:89], v[50:53]\n\t" \
        "ds_read_b64_tr_b16 a[60:61], v7 offset:12384\n\t" \
        "ds_read_b64_tr_b16 a[62:63], v7 offset:15456\n\t" \
        "v_mfma_f32_16x16x32_bf16 v[74:77], a[44:47], v[90:93], v[74:77]\n\t" \
        "ds_read_b64_tr_b16 a[64:65], v6 offset:12416\n\t" \
        "v_cvt_pk_bf16_f32 v78, v118, v119\n\t" \
        "v_mfma_f32_16x16x32_bf16 v[102:105], a[112:115], v[162:165], 0\n\t" \
        "ds_read_b64_tr_b16 a[66:67], v6 offset:15488\n\t" \
        "ds_read_b64_tr_b16 a[68:69], v7 offset:12448\n\t" \
        "s_waitcnt lgkmcnt(14)\n\t" \
        "v_mfma_f32_16x16x32_bf16 v[106:109], a[116:119], v[162:165], 0\n\t" \
        "ds_read_b64_tr_b16 a[70:71], v7 offset:15520\n\t" \
        "v_cvt_pk_bf16_f32 v79, v120, v121\n\t" \
        "v_mfma_f32_16x16x32_bf16 v[110:113], a[120:123], v[166:169], 0\n\t" \
        "ds_read_b64_tr_b16 a[128:129], v142 offset:128\n\t" \
        "v_cvt_pk_bf16_f32 v80, v122, v123\n\t" \
        "s_waitcnt lgkmcnt(13)\n\t" \
        "v_mfma_f32_16x16x32_bf16 v[114:117], a[124:127], v[166:169], 0\n\t" \
        "ds_read_b64_tr_b16 a[130:131], v143 offset:128\n\t" \
        "ds_read_b64_tr_b16 a[132:133], v146 offset:128\n\t" \
        "s_waitcnt lgkmcnt(14)\n\t" \
        "v_mfma_f32_16x16x32_bf16 v[30:33], a[48:51], v[94:97], v[30:33]\n\t" \
        "ds_read_b64_tr_b16 a[134:135], v147 offset:128\n\t" \
        "s_waitcnt lgkmcnt(14)\n\t" \
        "v_xor_b32 v134, v170, v11\n\t" \
        "v_mfma_f32_16x16x32_bf16 v[54:57], a[48:51], v[98:101], v[54:57]\n\t" \
        "ds_read_b64_tr_b16 a[136:137], v144 offset:128\n\t" \
        "v_xor_b32 v135, v171, v11\n\t" \
        "s_waitcnt lgkmcnt(13)\n\t" \
        "v_mfma_f32_16x16x32_bf16 v[34:37], a[52:55], v[94:97], v[34:37]\n\t" \
        "ds_read_b64_tr_b16 a[138:139], v145 offset:128\n\t" \
        "ds_read_b64_tr_b16 a[140:141], v148 offset:128\n\t" \
        "v_mfma_f32_16x16x32_bf16 v[58:61], a[52:55], v[98:101], v[58:61]\n\t" \
        "ds_read_b64_tr_b16 a[142:143], v149 offset:128\n\t" \
        "v_xor_b32 v136, v172, v11\n\t" \
        "s_waitcnt lgkmcnt(14)\n\t" \
        "v_mfma_f32_16x16x32_bf16 v[38:41], a[56:59], v[94:97], v[38:41]\n\t" \
        "ds_read_b64_tr_b16 a[72:73], v6 offset:18432\n\t" \
        "ds_read_b64_tr_b16 a[74:75], v6 offset:21504\n\t" \
        "v_mfma_f32_16x16x32_bf16 v[62:65], a[56:59], v[98:101], v[62:65]\n\t" \
        "ds_read_b64_tr_b16 a[76:77], v7 offset:18464\n\t" \
        "v_xor_b32 v138, v170, v12\n\t" \
        "s_waitcnt lgkmcnt(14)\n\t" \
        "v_mfma_f32_16x16x32_bf16 v[42:45], a[60:63], v[94:97], v[42:45]\n\t" \
        "ds_read_b64 v[150:151], v10 offset:256\n\t" \
        "v_xor_b32 v137, v173, v11\n\t" \
        "v_mfma_f32_16x16x32_bf16 v[66:69], a[60:63], v[98:101], v[66:69]\n\t" \
        "ds_read_b64_tr_b16 a[78:79], v7 offset:21536\n\t" \
        "ds_read_b64_tr_b16 a[80:81], v6 offset:18496\n\t" \
        "v_xor_b32 v139, v171, v12\n\t" \
        "s_waitcnt lgkmcnt(14)\n\t" \
        "v_mfma_f32_16x16x32_bf16 v[46:49], a[64:67], v[94:97], v[46:49]\n\t" \
        "ds_read_b64_tr_b16 a[82:83], v6 offset:21568\n\t" \
        "v_xor_b32 v140, v172, v12\n\t" \
        "v_mfma_f32_16x16x32_bf16 v[70:73], a[64:67], v[98:101], v[70:73]\n\t" \
        "ds_read_b64_tr_b16 a[84:85], v7 offset:18528\n\t" \
        "v_xor_b32 v141, v173, v12\n\t" \
        "v_mfma_f32_16x16x32_bf16 v[50:53], a[68:71], v[94:97], v[50:53]\n\t" \
        "ds_read_b64_tr_b16 a[86:87], v7 offset:21600\n\t" \
        "ds_read_b64_tr_b16 a[88:89], v6 offset:18560\n\t" \
        "v_cvt_pk_bf16_f32 v81, v124, v125\n\t" \
        "v_cvt_pk_bf16_f32 v82, v126, v127\n\t" \
        "v_mfma_f32_16x16x32_bf16 v[74:77], a[68:71], v[98:101], v[74:77]\n\t" \
        "ds_read_b64_tr_b16 a[90:91], v6 offset:21632\n\t" \
        "v_cvt_pk_bf16_f32 v83, v128, v129\n\t" \
        "v_cvt_pk_bf16_f32 v84, v130, v131\n\t" \
        "s_waitcnt lgkmcnt(14)\n\t" \
        "v_mfma_f32_16x16x32_bf16 v[118:121], a[128:131], v[162:165], 0\n\t" \
        "s_waitcnt vmcnt(6)\n\t" \
        "s_barrier\n\t" \
        "ds_read_b64_tr_b16 a[92:93], v7 offset:18592\n\t" \
        "ds_read_b64_tr_b16 a[94:95], v7 offset:21664\n\t" \
        "v_cvt_pk_bf16_f32 v85, v132, v133\n\t" \
        "v_mfma_f32_16x16x32_bf16 v[122:125], a[132:135], v[162:165], 0\n\t" \
        "ds_read_b64_tr_b16 a[96:97], v134 offset:0\n\t" \
        "v_cvt_pk_bf16_f32 v86, v102, v103\n\t" \
        "s_waitcnt lgkmcnt(14)\n\t" \
        "v_mfma_f32_16x16x32_bf16 v[126:129], a[136:139], v[166:169], 0\n\t" \
        "ds_read_b64_tr_b16 a[98:99], v135 offset:0\n\t" \
        "v_cvt_pk_bf16_f32 v87, v104, v105\n\t" \
        "v_mfma_f32_16x16x32_bf16 v[130:133], a[140:143], v[166:169], 0\n\t" \
        "s_add_u32 m0, s39, 0\n\t" \
        "s_add_u32 s57, s40, 61440\n\t" \
        "buffer_load_dwordx4 v3, s[24:27], s57 offen offset:0 lds\n\t" \
        "s_waitcnt lgkmcnt(12)\n\t" \
        "v_mfma_f32_16x16x32_bf16 v[30:33], a[72:75], v[78:81], v[30:33]\n\t" \
        "ds_read_b64_tr_b16 a[100:101], v138 offset:0\n\t" \
        "ds_read_b64_tr_b16 a[102:103], v139 offset:0\n\t" \
        "v_mfma_f32_16x16x32_bf16 v[54:57], a[72:75], v[82:85], v[54:57]\n\t" \
        "ds_read_b64_tr_b16 a[104:105], v136 offset:0\n\t" \
        "s_waitcnt lgkmcnt(12)\n\t" \
        "v_and_b32 v154, v150, v25\n\t" \
        "v_mfma_f32_16x16x32_bf16 v[34:37], a[76:79], v[78:81], v[34:37]\n\t" \
        "ds_read_b64_tr_b16 a[106:107], v137 offset:0\n\t" \
        "ds_read_b64_tr_b16 a[108:109], v140 offset:0\n\t" \
        "v_mfma_f32_16x16x32_bf16 v[58:61], a[76:79], v[82:85], v[58:61]\n\t" \
        "ds_read_b64_tr_b16 a[110:111], v141 offset:0\n\t" \
        "v_and_b32 v155, v150, v26\n\t" \
        "s_waitcnt lgkmcnt(13)\n\t" \
        "v_mfma_f32_16x16x32_bf16 v[38:41], a[80:83], v[78:81], v[38:41]\n\t" \
        "ds_read_b64_tr_b16 a[0:1], v6 offset:24576\n\t" \
        "v_and_b32 v156, v150, v27\n\t" \
        "v_and_b32 v157, v150, v28\n\t" \
        "v_mfma_f32_16x16x32_bf16 v[62:65], a[80:83], v[82:85], v[62:65]\n\t" \
        "buffer_load_dwordx4 v4, s[24:27], s57 offen offset:1024 lds\n\t" \
        "s_waitcnt lgkmcnt(11)\n\t" \
        "v_mfma_f32_16x16x32_bf16 v[42:45], a[84:87], v[78:81], v[42:45]\n\t" \
        "ds_read_b64_tr_b16 a[2:3], v6 offset:27648\n\t" \
        "ds_read_b64_tr_b16 a[4:5], v7 offset:24608\n\t" \
        "v_and_b32 v158, v151, v25\n\t" \
        "v_mfma_f32_16x16x32_bf16 v[66:69], a[84:87], v[82:85], v[66:69]\n\t" \
        "ds_read_b64_tr_b16 a[6:7], v7 offset:27680\n\t" \
        "v_cvt_pk_bf16_f32 v88, v106, v107\n\t" \
        "v_and_b32 v159, v151, v26\n\t" \
        "v_mfma_f32_16x16x32_bf16 v[46:49], a[88:91], v[78:81], v[46:49]\n\t" \
        "ds_read_b64_tr_b16 a[8:9], v6 offset:24640\n\t" \
        "ds_read_b64_tr_b16 a[10:11], v6 offset:27712\n\t" \
        "v_cvt_pk_bf16_f32 v89, v108, v109\n\t" \
        "v_cvt_pk_bf16_f32 v90, v110, v111\n\t" \
        "v_mfma_f32_16x16x32_bf16 v[70:73], a[88:91], v[82:85], v[70:73]\n\t" \
        "ds_read_b64_tr_b16 a[12:13], v7 offset:24672\n\t" \
        "v_and_b32 v160, v151, v27\n\t" \
        "s_waitcnt lgkmcnt(13)\n\t" \
        "v_mfma_f32_16x16x32_bf16 v[50:53], a[92:95], v[78:81], v[50:53]\n\t" \
        "ds_read_b64_tr_b16 a[14:15], v7 offset:27744\n\t" \
        "ds_read_b64_tr_b16 a[16:17], v6 offset:24704\n\t" \
        "v_cvt_pk_bf16_f32 v91, v112, v113\n\t" \
        "v_cvt_pk_bf16_f32 v92, v114, v115\n\t" \
        "v_mfma_f32_16x16x32_bf16 v[74:77], a[92:95], v[82:85], v[74:77]\n\t" \
        "buffer_load_dwordx4 v5, s[24:27], s57 offen offset:2048 lds\n\t" \
        "v_mfma_f32_16x16x32_bf16 v[102:105], a[96:99], v[154:157], 0\n\t" \
        "ds_read_b64_tr_b16 a[18:19], v6 offset:27776\n\t" \
        "v_and_b32 v161, v151, v28\n\t" \
        "v_cvt_pk_bf16_f32 v93, v116, v117\n\t" \
        "s_waitcnt lgkmcnt(11)\n\t" \
        "v_mfma_f32_16x16x32_bf16 v[106:109], a[100:103], v[154:157], 0\n\t" \
        "ds_read_b64_tr_b16 a[20:21], v7 offset:24736\n\t" \
        "v_cvt_pk_bf16_f32 v94, v118, v119\n\t" \
        "v_mfma_f32_16x16x32_bf16 v[110:113], a[104:107], v[158:161], 0\n\t" \
        "ds_read_b64_tr_b16 a[22:23], v7 offset:27808\n\t" \
        "ds_read_b64_tr_b16 a[112:113], v134 offset:64\n\t" \
        "s_waitcnt lgkmcnt(12)\n\t" \
        "v_mfma_f32_16x16x32_bf16 v[114:117], a[108:111], v[158:161], 0\n\t" \
        "ds_read_b64_tr_b16 a[114:115], v135 offset:64\n\t" \
        "v_cvt_pk_bf16_f32 v95, v120, v121\n\t" \
        "s_waitcnt lgkmcnt(12)\n\t" \
        "v_mfma_f32_16x16x32_bf16 v[30:33], a[0:3], v[86:89], v[30:33]\n\t" \
        "ds_read_b64_tr_b16 a[116:117], v138 offset:64\n\t" \
        "v_cvt_pk_bf16_f32 v96, v122, v123\n\t" \
        "v_mfma_f32_16x16x32_bf16 v[54:57], a[0:3], v[90:93], v[54:57]\n\t" \
        "ds_read_b64_tr_b16 a[118:119], v139 offset:64\n\t" \
        "ds_read_b64_tr_b16 a[120:121], v136 offset:64\n\t" \
        "s_waitcnt lgkmcnt(13)\n\t" \
        "v_mfma_f32_16x16x32_bf16 v[34:37], a[4:7], v[86:89], v[34:37]\n\t" \
        "ds_read_b64_tr_b16 a[122:123], v137 offset:64\n\t" \
        "v_cvt_pk_bf16_f32 v97, v124, v125\n\t" \
        "v_mfma_f32_16x16x32_bf16 v[58:61], a[4:7], v[90:93], v[58:61]\n\t" \
        "ds_read_b64_tr_b16 a[124:125], v140 offset:64\n\t" \
        "ds_read_b64_tr_b16 a[126:127], v141 offset:64\n\t" \
        "s_waitcnt lgkmcnt(13)\n\t" \
        "v_mfma_f32_16x16x32_bf16 v[38:41], a[8:11], v[86:89], v[38:41]\n\t" \
        "ds_read_b64_tr_b16 a[24:25], v6 offset:30720\n\t" \
        "v_cvt_pk_bf16_f32 v98, v126, v127\n\t" \
        "v_mfma_f32_16x16x32_bf16 v[62:65], a[8:11], v[90:93], v[62:65]\n\t" \
        "ds_read_b64_tr_b16 a[26:27], v6 offset:33792\n\t" \
        "ds_read_b128 v[174:177], v9 offset:384\n\t" \
        "s_waitcnt lgkmcnt(14)\n\t" \
        "v_mfma_f32_16x16x32_bf16 v[42:45], a[12:15], v[86:89], v[42:45]\n\t" \
        "ds_read_b64_tr_b16 a[28:29], v7 offset:30752\n\t" \
        "v_cvt_pk_bf16_f32 v99, v128, v129\n\t" \
        "v_mfma_f32_16x16x32_bf16 v[66:69], a[12:15], v[90:93], v[66:69]\n\t" \
        "ds_read_b64_tr_b16 a[30:31], v7 offset:33824\n\t" \
        "v_cvt_pk_bf16_f32 v100, v130, v131\n\t" \
        "s_waitcnt lgkmcnt(14)\n\t" \
        "v_mfma_f32_16x16x32_bf16 v[46:49], a[16:19], v[86:89], v[46:49]\n\t" \
        "ds_read_b64_tr_b16 a[32:33], v6 offset:30784\n\t" \
        "ds_read_b64_tr_b16 a[34:35], v6 offset:33856\n\t" \
        "v_mfma_f32_16x16x32_bf16 v[70:73], a[16:19], v[90:93], v[70:73]\n\t" \
        "ds_read_b64_tr_b16 a[36:37], v7 offset:30816\n\t" \
        "v_cvt_pk_bf16_f32 v101, v132, v133\n\t" \
        "s_waitcnt lgkmcnt(14)\n\t" \
        "v_mfma_f32_16x16x32_bf16 v[50:53], a[20:23], v[86:89], v[50:53]\n\t" \
        "ds_read_b64_tr_b16 a[38:39], v7 offset:33888\n\t" \
        "ds_read_b64_tr_b16 a[40:41], v6 offset:30848\n\t" \
        "v_mfma_f32_16x16x32_bf16 v[74:77], a[20:23], v[90:93], v[74:77]\n\t" \
        "ds_read_b64_tr_b16 a[42:43], v6 offset:33920\n\t" \
        "v_cvt_pk_bf16_f32 v78, v102, v103\n\t" \
        "v_mfma_f32_16x16x32_bf16 v[118:121], a[112:115], v[154:157], 0\n\t" \
        "s_waitcnt vmcnt(6)\n\t" \
        "s_barrier\n\t" \
        "ds_read_b64_tr_b16 a[44:45], v7 offset:30880\n\t" \
        "v_cvt_pk_bf16_f32 v79, v104, v105\n\t" \
        "s_waitcnt lgkmcnt(14)\n\t" \
        "v_mfma_f32_16x16x32_bf16 v[122:125], a[116:119], v[154:157], 0\n\t" \
        "ds_read_b64_tr_b16 a[46:47], v7 offset:33952\n\t" \
        "ds_read_b64_tr_b16 a[128:129], v134 offset:128\n\t" \
        "v_mfma_f32_16x16x32_bf16 v[126:129], a[120:123], v[158:161], 0\n\t" \
        "ds_read_b64_tr_b16 a[130:131], v135 offset:128\n\t" \
        "s_waitcnt lgkmcnt(12)\n\t" \
        "v_xor_b32 v142, v174, v11\n\t" \
        "v_mfma_f32_16x16x32_bf16 v[130:133], a[124:127], v[158:161], 0\n\t" \
        "s_add_u32 m0, s39, 12288\n\t" \
        "s_add_u32 s57, s40, 73728\n\t" \
        "buffer_load_dwordx4 v3, s[24:27], s57 offen offset:0 lds\n\t" \
        "v_mfma_f32_16x16x32_bf16 v[30:33], a[24:27], v[94:97], v[30:33]\n\t" \
        "ds_read_b64_tr_b16 a[132:133], v138 offset:128\n\t" \
        "ds_read_b64_tr_b16 a[134:135], v139 offset:128\n\t" \
        "v_mfma_f32_16x16x32_bf16 v[54:57], a[24:27], v[98:101], v[54:57]\n\t" \
        "ds_read_b64_tr_b16 a[136:137], v136 offset:128\n\t" \
        "v_xor_b32 v143, v175, v11\n\t" \
        "s_waitcnt lgkmcnt(13)\n\t" \
        "v_mfma_f32_16x16x32_bf16 v[34:37], a[28:31], v[94:97], v[34:37]\n\t" \
        "ds_read_b64_tr_b16 a[138:139], v137 offset:128\n\t" \
        "v_xor_b32 v144, v176, v11\n\t" \
        "v_mfma_f32_16x16x32_bf16 v[58:61], a[28:31], v[98:101], v[58:61]\n\t" \
        "ds_read_b64_tr_b16 a[140:141], v140 offset:128\n\t" \
        "ds_read_b64_tr_b16 a[142:143], v141 offset:128\n\t" \
        "s_waitcnt lgkmcnt(13)\n\t" \
        "v_mfma_f32_16x16x32_bf16 v[38:41], a[32:35], v[94:97], v[38:41]\n\t" \
        "ds_read_b64_tr_b16 a[48:49], v6 offset:36864\n\t" \
        "v_xor_b32 v146, v174, v12\n\t" \
        "v_mfma_f32_16x16x32_bf16 v[62:65], a[32:35], v[98:101], v[62:65]\n\t" \
        "buffer_load_dwordx4 v4, s[24:27], s57 offen offset:1024 lds\n\t" \
        "s_waitcnt lgkmcnt(11)\n\t" \
        "v_mfma_f32_16x16x32_bf16 v[42:45], a[36:39], v[94:97], v[42:45]\n\t" \
        "ds_read_b64_tr_b16 a[50:51], v6 offset:39936\n\t" \
        "ds_read_b64_tr_b16 a[52:53], v7 offset:36896\n\t" \
        "v_xor_b32 v145, v177, v11\n\t" \
        "v_xor_b32 v147, v175, v12\n\t" \
        "v_mfma_f32_16x16x32_bf16 v[66:69], a[36:39], v[98:101], v[66:69]\n\t" \
        "ds_read_b64 v[152:153], v10 offset:384\n\t" \
        "v_xor_b32 v148, v176, v12\n\t" \
        "v_xor_b32 v149, v177, v12\n\t" \
        "v_mfma_f32_16x16x32_bf16 v[46:49], a[40:43], v[94:97], v[46:49]\n\t" \
        "ds_read_b64_tr_b16 a[54:55], v7 offset:39968\n\t" \
        "ds_read_b64_tr_b16 a[56:57], v6 offset:36928\n\t" \
        "v_cvt_pk_bf16_f32 v80, v106, v107\n\t" \
        "v_mfma_f32_16x16x32_bf16 v[70:73], a[40:43], v[98:101], v[70:73]\n\t" \
        "ds_read_b64_tr_b16 a[58:59], v6 offset:40000\n\t" \
        "v_cvt_pk_bf16_f32 v81, v108, v109\n\t" \
        "v_cvt_pk_bf16_f32 v82, v110, v111\n\t" \
        "s_waitcnt lgkmcnt(13)\n\t" \
        "v_mfma_f32_16x16x32_bf16 v[50:53], a[44:47], v[94:97], v[50:53]\n\t" \
        "ds_read_b64_tr_b16 a[60:61], v7 offset:36960\n\t" \
        "v_cvt_pk_bf16_f32 v83, v112, v113\n\t" \
        "v_cvt_pk_bf16_f32 v84, v114, v115\n\t" \
        "v_mfma_f32_16x16x32_bf16 v[74:77], a[44:47], v[98:101], v[74:77]\n\t" \
        "buffer_load_dwordx4 v5, s[24:27], s57 offen offset:2048 lds\n\t" \
        "v_mfma_f32_16x16x32_bf16 v[102:105], a[128:131], v[154:157], 0\n\t" \
        "ds_read_b64_tr_b16 a[62:63], v7 offset:40032\n\t" \
        "ds_read_b64_tr_b16 a[64:65], v6 offset:36992\n\t" \
        "v_cvt_pk_bf16_f32 v85, v116, v117\n\t" \
        "s_waitcnt lgkmcnt(12)\n\t" \
        "v_mfma_f32_16x16x32_bf16 v[106:109], a[132:135], v[154:157], 0\n\t" \
        "ds_read_b64_tr_b16 a[66:67], v6 offset:40064\n\t" \
        "v_cvt_pk_bf16_f32 v86, v118, v119\n\t" \
        "v_mfma_f32_16x16x32_bf16 v[110:113], a[136:139], v[158:161], 0\n\t" \
        "ds_read_b64_tr_b16 a[68:69], v7 offset:37024\n\t" \
        "ds_read_b64_tr_b16 a[70:71], v7 offset:40096\n\t" \
        "s_waitcnt lgkmcnt(12)\n\t" \
        "v_mfma_f32_16x16x32_bf16 v[114:117], a[140:143], v[158:161], 0\n\t" \
        "ds_read_b64_tr_b16 a[96:97], v142 offset:0\n\t" \
        "v_cvt_pk_bf16_f32 v87, v120, v121\n\t" \
        "s_waitcnt lgkmcnt(12)\n\t" \
        "v_mfma_f32_16x16x32_bf16 v[30:33], a[48:51], v[78:81], v[30:33]\n\t" \
        "ds_read_b64_tr_b16 a[98:99], v143 offset:0\n\t" \
        "s_waitcnt lgkmcnt(11)\n\t" \
        "v_and_b32 v162, v152, v25\n\t" \
        "v_mfma_f32_16x16x32_bf16 v[54:57], a[48:51], v[82:85], v[54:57]\n\t" \
        "ds_read_b64_tr_b16 a[100:101], v146 offset:0\n\t" \
        "ds_read_b64_tr_b16 a[102:103], v147 offset:0\n\t" \
        "s_waitcnt lgkmcnt(12)\n\t" \
        "v_mfma_f32_16x16x32_bf16 v[34:37], a[52:55], v[78:81], v[34:37]\n\t" \
        "ds_read_b64_tr_b16 a[104:105], v144 offset:0\n\t" \
        "v_and_b32 v163, v152, v26\n\t" \
        "v_mfma_f32_16x16x32_bf16 v[58:61], a[52:55], v[82:85], v[58:61]\n\t" \
        "ds_read_b64_tr_b16 a[106:107], v145 offset:0\n\t" \
        "ds_read_b64_tr_b16 a[108:109], v148 offset:0\n\t" \
        "s_waitcnt lgkmcnt(13)\n\t" \
        "v_mfma_f32_16x16x32_bf16 v[38:41], a[56:59], v[78:81], v[38:41]\n\t" \
        "ds_read_b64_tr_b16 a[110:111], v149 offset:0\n\t" \
        "v_and_b32 v164, v152, v27\n\t" \
        "v_mfma_f32_16x16x32_bf16 v[62:65], a[56:59], v[82:85], v[62:65]\n\t" \
        "ds_read_b64_tr_b16 a[72:73], v6 offset:43008\n\t" \
        "v_and_b32 v165, v152, v28\n\t" \
        "s_waitcnt lgkmcnt(13)\n\t" \
        "v_mfma_f32_16x16x32_bf16 v[42:45], a[60:63], v[78:81], v[42:45]\n\t" \
        "ds_read_b64_tr_b16 a[74:75], v6 offset:46080\n\t" \
        "ds_read_b64_tr_b16 a[76:77], v7 offset:43040\n\t" \
        "v_mfma_f32_16x16x32_bf16 v[66:69], a[60:63], v[82:85], v[66:69]\n\t" \
        "ds_read_b64_tr_b16 a[78:79], v7 offset:46112\n\t" \
        "v_and_b32 v166, v153, v25\n\t" \
        "s_waitcnt lgkmcnt(14)\n\t" \
        "v_mfma_f32_16x16x32_bf16 v[46:49], a[64:67], v[78:81], v[46:49]\n\t" \
        "ds_read_b64_tr_b16 a[80:81], v6 offset:43072\n\t" \
        "ds_read_b64_tr_b16 a[82:83], v6 offset:46144\n\t" \
        "v_cvt_pk_bf16_f32 v88, v122, v123\n\t" \
        "v_and_b32 v167, v153, v26\n\t" \
        "v_mfma_f32_16x16x32_bf16 v[70:73], a[64:67], v[82:85], v[70:73]\n\t" \
        "ds_read_b64_tr_b16 a[84:85], v7 offset:43104\n\t" \
        "v_cvt_pk_bf16_f32 v89, v124, v125\n\t" \
        "v_cvt_pk_bf16_f32 v90, v126, v127\n\t" \
        "s_waitcnt lgkmcnt(14)\n\t" \
        "v_mfma_f32_16x16x32_bf16 v[50:53], a[68:71], v[78:81], v[50:53]\n\t" \
        "ds_read_b64_tr_b16 a[86:87], v7 offset:46176\n\t" \
        "ds_read_b64_tr_b16 a[88:89], v6 offset:43136\n\t" \
        "v_and_b32 v168, v153, v27\n\t" \
        "v_mfma_f32_16x16x32_bf16 v[74:77], a[68:71], v[82:85], v[74:77]\n\t" \
        "ds_read_b64_tr_b16 a[90:91], v6 offset:46208\n\t" \
        "v_cvt_pk_bf16_f32 v91, v128, v129\n\t" \
        "v_cvt_pk_bf16_f32 v92, v130, v131\n\t" \
        "s_waitcnt lgkmcnt(14)\n\t" \
        "v_mfma_f32_16x16x32_bf16 v[118:121], a[96:99], v[162:165], 0\n\t" \
        "s_waitcnt vmcnt(6)\n\t" \
        "s_barrier\n\t" \
        "ds_read_b64_tr_b16 a[92:93], v7 offset:43168\n\t" \
        "v_and_b32 v169, v153, v28\n\t" \
        "v_cvt_pk_bf16_f32 v93, v132, v133\n\t" \
        "v_mfma_f32_16x16x32_bf16 v[122:125], a[100:103], v[162:165], 0\n\t" \
        "ds_read_b64_tr_b16 a[94:95], v7 offset:46240\n\t" \
        "ds_read_b64_tr_b16 a[112:113], v142 offset:64\n\t" \
        "s_waitcnt lgkmcnt(14)\n\t" \
        "v_mfma_f32_16x16x32_bf16 v[126:129], a[104:107], v[166:169], 0\n\t" \
        "ds_read_b64_tr_b16 a[114:115], v143 offset:64\n\t" \
        "v_cvt_pk_bf16_f32 v94, v102, v103\n\t" \
        "s_waitcnt lgkmcnt(14)\n\t" \
        "v_mfma_f32_16x16x32_bf16 v[130:133], a[108:111], v[166:169], 0\n\t" \
        "s_add_u32 m0, s39, 24576\n\t" \
        "s_add_u32 s57, s40, 86016\n\t" \
        "buffer_load_dwordx4 v3, s[24:27], s57 offen offset:0 lds\n\t" \
        "s_waitcnt lgkmcnt(12)\n\t" \
        "v_mfma_f32_16x16x32_bf16 v[30:33], a[72:75], v[86:89], v[30:33]\n\t" \
        "ds_read_b64_tr_b16 a[116:117], v146 offset:64\n\t" \
        "ds_read_b64_tr_b16 a[118:119], v147 offset:64\n\t" \
        "v_mfma_f32_16x16x32_bf16 v[54:57], a[72:75], v[90:93], v[54:57]\n\t" \
        "ds_read_b64_tr_b16 a[120:121], v144 offset:64\n\t" \
        "v_cvt_pk_bf16_f32 v95, v104, v105\n\t" \
        "s_waitcnt lgkmcnt(13)\n\t" \
        "v_mfma_f32_16x16x32_bf16 v[34:37], a[76:79], v[86:89], v[34:37]\n\t" \
        "ds_read_b64_tr_b16 a[122:123], v145 offset:64\n\t" \
        "ds_read_b64_tr_b16 a[124:125], v148 offset:64\n\t" \
        "v_mfma_f32_16x16x32_bf16 v[58:61], a[76:79], v[90:93], v[58:61]\n\t" \
        "ds_read_b64_tr_b16 a[126:127], v149 offset:64\n\t" \
        "v_cvt_pk_bf16_f32 v96, v106, v107\n\t" \
        "s_waitcnt lgkmcnt(13)\n\t" \
        "v_mfma_f32_16x16x32_bf16 v[38:41], a[80:83], v[86:89], v[38:41]\n\t" \
        "ds_read_b64_tr_b16 a[0:1], v6 offset:49152\n\t" \
        "ds_read_b64_tr_b16 a[2:3], v6 offset:52224\n\t" \
        "v_mfma_f32_16x16x32_bf16 v[62:65], a[80:83], v[90:93], v[62:65]\n\t" \
        "buffer_load_dwordx4 v4, s[24:27], s57 offen offset:1024 lds\n\t" \
        "s_waitcnt lgkmcnt(12)\n\t" \
        "v_mfma_f32_16x16x32_bf16 v[42:45], a[84:87], v[86:89], v[42:45]\n\t" \
        "ds_read_b128 v[170:173], v9 offset:512\n\t" \
        "v_cvt_pk_bf16_f32 v97, v108, v109\n\t" \
        "v_mfma_f32_16x16x32_bf16 v[66:69], a[84:87], v[90:93], v[66:69]\n\t" \
        "ds_read_b64_tr_b16 a[4:5], v7 offset:49184\n\t" \
        "v_cvt_pk_bf16_f32 v98, v110, v111\n\t" \
        "v_mfma_f32_16x16x32_bf16 v[46:49], a[88:91], v[86:89], v[46:49]\n\t" \
        "ds_read_b64_tr_b16 a[6:7], v7 offset:52256\n\t" \
        "ds_read_b64_tr_b16 a[8:9], v6 offset:49216\n\t" \
        "v_mfma_f32_16x16x32_bf16 v[70:73], a[88:91], v[90:93], v[70:73]\n\t" \
        "ds_read_b64_tr_b16 a[10:11], v6 offset:52288\n\t" \
        "v_cvt_pk_bf16_f32 v99, v112, v113\n\t" \
        "s_waitcnt lgkmcnt(13)\n\t" \
        "v_mfma_f32_16x16x32_bf16 v[50:53], a[92:95], v[86:89], v[50:53]\n\t" \
        "ds_read_b64_tr_b16 a[12:13], v7 offset:49248\n\t" \
        "ds_read_b64_tr_b16 a[14:15], v7 offset:52320\n\t" \
        "v_cvt_pk_bf16_f32 v100, v114, v115\n\t" \
        "v_mfma_f32_16x16x32_bf16 v[74:77], a[92:95], v[90:93], v[74:77]\n\t" \
        "buffer_load_dwordx4 v5, s[24:27], s57 offen offset:2048 lds\n\t" \
        "v_mfma_f32_16x16x32_bf16 v[102:105], a[112:115], v[162:165], 0\n\t" \
        "ds_read_b64_tr_b16 a[16:17], v6 offset:49280\n\t" \
        "v_cvt_pk_bf16_f32 v101, v116, v117\n\t" \
        "s_waitcnt lgkmcnt(11)\n\t" \
        "v_mfma_f32_16x16x32_bf16 v[106:109], a[116:119], v[162:165], 0\n\t" \
        "ds_read_b64_tr_b16 a[18:19], v6 offset:52352\n\t" \
        "ds_read_b64_tr_b16 a[20:21], v7 offset:49312\n\t" \
        "v_mfma_f32_16x16x32_bf16 v[110:113], a[120:123], v[166:169], 0\n\t" \
        "ds_read_b64_tr_b16 a[22:23], v7 offset:52384\n\t" \
        "v_cvt_pk_bf16_f32 v78, v118, v119\n\t" \
        "s_waitcnt lgkmcnt(11)\n\t" \
        "v_mfma_f32_16x16x32_bf16 v[114:117], a[124:127], v[166:169], 0\n\t" \
        "ds_read_b64_tr_b16 a[128:129], v142 offset:128\n\t" \
        "s_waitcnt lgkmcnt(11)\n\t" \
        "v_xor_b32 v134, v170, v11\n\t" \
        "v_mfma_f32_16x16x32_bf16 v[30:33], a[0:3], v[94:97], v[30:33]\n\t" \
        "ds_read_b64_tr_b16 a[130:131], v143 offset:128\n\t" \
        "ds_read_b64_tr_b16 a[132:133], v146 offset:128\n\t" \
        "v_mfma_f32_16x16x32_bf16 v[54:57], a[0:3], v[98:101], v[54:57]\n\t" \
        "ds_read_b64_tr_b16 a[134:135], v147 offset:128\n\t" \
        "v_xor_b32 v135, v171, v11\n\t" \
        "s_waitcnt lgkmcnt(12)\n\t" \
        "v_mfma_f32_16x16x32_bf16 v[34:37], a[4:7], v[94:97], v[34:37]\n\t" \
        "ds_read_b64_tr_b16 a[136:137], v144 offset:128\n\t" \
        "ds_read_b64_tr_b16 a[138:139], v145 offset:128\n\t" \
        "v_mfma_f32_16x16x32_bf16 v[58:61], a[4:7], v[98:101], v[58:61]\n\t" \
        "ds_read_b64_tr_b16 a[140:141], v148 offset:128\n\t" \
        "v_xor_b32 v136, v172, v11\n\t" \
        "s_waitcnt lgkmcnt(13)\n\t" \
        "v_mfma_f32_16x16x32_bf16 v[38:41], a[8:11], v[94:97], v[38:41]\n\t" \
        "ds_read_b64_tr_b16 a[142:143], v149 offset:128\n\t" \
        "ds_read_b64_tr_b16 a[24:25], v6 offset:55296\n\t" \
        "v_mfma_f32_16x16x32_bf16 v[62:65], a[8:11], v[98:101], v[62:65]\n\t" \
        "ds_read_b64_tr_b16 a[26:27], v6 offset:58368\n\t" \
        "v_xor_b32 v138, v170, v12\n\t" \
        "s_waitcnt lgkmcnt(14)\n\t" \
        "v_mfma_f32_16x16x32_bf16 v[42:45], a[12:15], v[94:97], v[42:45]\n\t" \
        "ds_read_b64_tr_b16 a[28:29], v7 offset:55328\n\t" \
        "v_xor_b32 v137, v173, v11\n\t" \
        "v_mfma_f32_16x16x32_bf16 v[66:69], a[12:15], v[98:101], v[66:69]\n\t" \
        "ds_read_b64 v[150:151], v10 offset:512\n\t" \
        "ds_read_b64_tr_b16 a[30:31], v7 offset:58400\n\t" \
        "v_xor_b32 v139, v171, v12\n\t" \
        "v_xor_b32 v140, v172, v12\n\t" \
        "s_waitcnt lgkmcnt(14)\n\t" \
        "v_mfma_f32_16x16x32_bf16 v[46:49], a[16:19], v[94:97], v[46:49]\n\t" \
        "ds_read_b64_tr_b16 a[32:33], v6 offset:55360\n\t" \
        "v_xor_b32 v141, v173, v12\n\t" \
        "v_cvt_pk_bf16_f32 v79, v120, v121\n\t" \
        "v_mfma_f32_16x16x32_bf16 v[70:73], a[16:19], v[98:101], v[70:73]\n\t" \
        "ds_read_b64_tr_b16 a[34:35], v6 offset:58432\n\t" \
        "ds_read_b64_tr_b16 a[36:37], v7 offset:55392\n\t" \
        "v_cvt_pk_bf16_f32 v80, v122, v123\n\t" \
        "s_waitcnt lgkmcnt(15)\n\t" \
        "v_mfma_f32_16x16x32_bf16 v[50:53], a[20:23], v[94:97], v[50:53]\n\t" \
        "ds_read_b64_tr_b16 a[38:39], v7 offset:58464\n\t" \
        "v_cvt_pk_bf16_f32 v81, v124, v125\n\t" \
        "v_cvt_pk_bf16_f32 v82, v126, v127\n\t" \
        "v_mfma_f32_16x16x32_bf16 v[74:77], a[20:23], v[98:101], v[74:77]\n\t" \
        "ds_read_b64_tr_b16 a[40:41], v6 offset:55424\n\t" \
        "ds_read_b64_tr_b16 a[42:43], v6 offset:58496\n\t" \
        "v_cvt_pk_bf16_f32 v83, v128, v129\n\t" \
        "v_cvt_pk_bf16_f32 v84, v130, v131\n\t" \
        "s_waitcnt lgkmcnt(15)\n\t" \
        "v_mfma_f32_16x16x32_bf16 v[118:121], a[128:131], v[162:165], 0\n\t" \
        "s_waitcnt vmcnt(6)\n\t" \
        "s_barrier\n\t" \
        "ds_read_b64_tr_b16 a[44:45], v7 offset:55456\n\t" \
        "v_cvt_pk_bf16_f32 v85, v132, v133\n\t" \
        "v_mfma_f32_16x16x32_bf16 v[122:125], a[132:135], v[162:165], 0\n\t" \
        "ds_read_b64_tr_b16 a[46:47], v7 offset:58528\n\t" \
        "v_cvt_pk_bf16_f32 v86, v102, v103\n\t" \
        "s_waitcnt lgkmcnt(14)\n\t" \
        "v_mfma_f32_16x16x32_bf16 v[126:129], a[136:139], v[166:169], 0\n\t" \
        "ds_read_b64_tr_b16 a[96:97], v134 offset:0\n\t" \
        "ds_read_b64_tr_b16 a[98:99], v135 offset:0\n\t" \
        "s_waitcnt lgkmcnt(14)\n\t" \
        "v_mfma_f32_16x16x32_bf16 v[130:133], a[140:143], v[166:169], 0\n\t" \
        "s_add_u32 m0, s39, 36864\n\t" \
        "s_add_u32 s57, s40, 98304\n\t" \
        "buffer_load_dwordx4 v3, s[24:27], s57 offen offset:0 lds\n\t" \
        "s_waitcnt lgkmcnt(13)\n\t" \
        "v_mfma_f32_16x16x32_bf16 v[30:33], a[24:27], v[78:81], v[30:33]\n\t" \
        "ds_read_b64_tr_b16 a[100:101], v138 offset:0\n\t" \
        "s_waitcnt lgkmcnt(12)\n\t" \
        "v_and_b32 v154, v150, v25\n\t" \
        "v_mfma_f32_16x16x32_bf16 v[54:57], a[24:27], v[82:85], v[54:57]\n\t" \
        "ds_read_b64_tr_b16 a[102:103], v139 offset:0\n\t" \
        "ds_read_b64_tr_b16 a[104:105], v136 offset:0\n\t" \
        "s_waitcnt lgkmcnt(13)\n\t" \
        "v_mfma_f32_16x16x32_bf16 v[34:37], a[28:31], v[78:81], v[34:37]\n\t" \
        "ds_read_b64_tr_b16 a[106:107], v137 offset:0\n\t" \
        "v_and_b32 v155, v150, v26\n\t" \
        "v_mfma_f32_16x16x32_bf16 v[58:61], a[28:31], v[82:85], v[58:61]\n\t" \
        "ds_read_b64_tr_b16 a[108:109], v140 offset:0\n\t" \
        "ds_read_b64_tr_b16 a[110:111], v141 offset:0\n\t" \
        "v_and_b32 v156, v150, v27\n\t" \
        "s_waitcnt lgkmcnt(13)\n\t" \
        "v_mfma_f32_16x16x32_bf16 v[38:41], a[32:35], v[78:81], v[38:41]\n\t" \
        "ds_read_b64_tr_b16 a[48:49], v6 offset:0\n\t" \
        "v_and_b32 v157, v150, v28\n\t" \
        "v_and_b32 v158, v151, v25\n\t" \
        "v_mfma_f32_16x16x32_bf16 v[62:65], a[32:35], v[82:85], v[62:65]\n\t" \
        "buffer_load_dwordx4 v4, s[24:27], s57 offen offset:1024 lds\n\t" \
        "s_waitcnt lgkmcnt(11)\n\t" \
        "v_mfma_f32_16x16x32_bf16 v[42:45], a[36:39], v[78:81], v[42:45]\n\t" \
        "ds_read_b64_tr_b16 a[50:51], v6 offset:3072\n\t" \
        "ds_read_b64_tr_b16 a[52:53], v7 offset:32\n\t" \
        "v_cvt_pk_bf16_f32 v87, v104, v105\n\t" \
        "v_mfma_f32_16x16x32_bf16 v[66:69], a[36:39], v[82:85], v[66:69]\n\t" \
        "ds_read_b64_tr_b16 a[54:55], v7 offset:3104\n\t" \
        "v_cvt_pk_bf16_f32 v88, v106, v107\n\t" \
        "v_and_b32 v159, v151, v26\n\t" \
        "v_mfma_f32_16x16x32_bf16 v[46:49], a[40:43], v[78:81], v[46:49]\n\t" \
        "ds_read_b64_tr_b16 a[56:57], v6 offset:64\n\t" \
        "v_cvt_pk_bf16_f32 v89, v108, v109\n\t" \
        "v_cvt_pk_bf16_f32 v90, v110, v111\n\t" \
        "v_mfma_f32_16x16x32_bf16 v[70:73], a[40:43], v[82:85], v[70:73]\n\t" \
        "ds_read_b64_tr_b16 a[58:59], v6 offset:3136\n\t" \
        "ds_read_b64_tr_b16 a[60:61], v7 offset:96\n\t" \
        "v_and_b32 v160, v151, v27\n\t" \
        "s_waitcnt lgkmcnt(13)\n\t" \
        "v_mfma_f32_16x16x32_bf16 v[50:53], a[44:47], v[78:81], v[50:53]\n\t" \
        "ds_read_b64_tr_b16 a[62:63], v7 offset:3168\n\t" \
        "v_cvt_pk_bf16_f32 v91, v112, v113\n\t" \
        "v_cvt_pk_bf16_f32 v92, v114, v115\n\t" \
        "v_mfma_f32_16x16x32_bf16 v[74:77], a[44:47], v[82:85], v[74:77]\n\t" \
        "buffer_load_dwordx4 v5, s[24:27], s57 offen offset:2048 lds\n\t" \
        "v_mfma_f32_16x16x32_bf16 v[102:105], a[96:99], v[154:157], 0\n\t" \
        "ds_read_b64_tr_b16 a[64:65], v6 offset:128\n\t" \
        "ds_read_b64_tr_b16 a[66:67], v6 offset:3200\n\t" \
        "v_and_b32 v161, v151, v28\n\t" \
        "v_cvt_pk_bf16_f32 v93, v116, v117\n\t" \
        "s_waitcnt lgkmcnt(12)\n\t" \
        "v_mfma_f32_16x16x32_bf16 v[106:109], a[100:103], v[154:157], 0\n\t" \
        "ds_read_b64_tr_b16 a[68:69], v7 offset:160\n\t" \
        "v_cvt_pk_bf16_f32 v94, v118, v119\n\t" \
        "v_mfma_f32_16x16x32_bf16 v[110:113], a[104:107], v[158:161], 0\n\t" \
        "ds_read_b64_tr_b16 a[70:71], v7 offset:3232\n\t" \
        "v_cvt_pk_bf16_f32 v95, v120, v121\n\t" \
        "s_waitcnt lgkmcnt(11)\n\t" \
        "v_mfma_f32_16x16x32_bf16 v[114:117], a[108:111], v[158:161], 0\n\t" \
        "ds_read_b64_tr_b16 a[112:113], v134 offset:64\n\t" \
        "ds_read_b64_tr_b16 a[114:115], v135 offset:64\n\t" \
        "s_waitcnt lgkmcnt(12)\n\t" \
        "v_mfma_f32_16x16x32_bf16 v[30:33], a[48:51], v[86:89], v[30:33]\n\t" \
        "ds_read_b64_tr_b16 a[116:117], v138 offset:64\n\t" \
        "v_cvt_pk_bf16_f32 v96, v122, v123\n\t" \
        "v_mfma_f32_16x16x32_bf16 v[54:57], a[48:51], v[90:93], v[54:57]\n\t" \
        "ds_read_b64_tr_b16 a[118:119], v139 offset:64\n\t" \
        "ds_read_b64_tr_b16 a[120:121], v136 offset:64\n\t" \
        "s_waitcnt lgkmcnt(13)\n\t" \
        "v_mfma_f32_16x16x32_bf16 v[34:37], a[52:55], v[86:89], v[34:37]\n\t" \
        "ds_read_b64_tr_b16 a[122:123], v137 offset:64\n\t" \
        "v_cvt_pk_bf16_f32 v97, v124, v125\n\t" \
        "v_mfma_f32_16x16x32_bf16 v[58:61], a[52:55], v[90:93], v[58:61]\n\t" \
        "ds_read_b64_tr_b16 a[124:125], v140 offset:64\n\t" \
        "v_cvt_pk_bf16_f32 v98, v126, v127\n\t" \
        "s_waitcnt lgkmcnt(12)\n\t" \
        "v_mfma_f32_16x16x32_bf16 v[38:41], a[56:59], v[86:89], v[38:41]\n\t" \
        "ds_read_b64_tr_b16 a[126:127], v141 offset:64\n\t" \
        "ds_read_b64_tr_b16 a[72:73], v6 offset:6144\n\t" \
        "v_mfma_f32_16x16x32_bf16 v[62:65], a[56:59], v[90:93], v[62:65]\n\t" \
        "ds_read_b64_tr_b16 a[74:75], v6 offset:9216\n\t" \
        "v_cvt_pk_bf16_f32 v99, v128, v129\n\t" \
        "s_waitcnt lgkmcnt(14)\n\t" \
        "v_mfma_f32_16x16x32_bf16 v[42:45], a[60:63], v[86:89], v[42:45]\n\t" \
        "ds_read_b128 v[174:177], v9 offset:640\n\t" \
        "ds_read_b64_tr_b16 a[76:77], v7 offset:6176\n\t" \
        "v_mfma_f32_16x16x32_bf16 v[66:69], a[60:63], v[90:93], v[66:69]\n\t" \
        "ds_read_b64_tr_b16 a[78:79], v7 offset:9248\n\t" \
        "v_cvt_pk_bf16_f32 v100, v130, v131\n\t" \
        "s_waitcnt lgkmcnt(14)\n\t" \
        "v_mfma_f32_16x16x32_bf16 v[46:49], a[64:67], v[86:89], v[46:49]\n\t" \
        "ds_read_b64_tr_b16 a[80:81], v6 offset:6208\n\t" \
        "ds_read_b64_tr_b16 a[82:83], v6 offset:9280\n\t" \
        "v_mfma_f32_16x16x32_bf16 v[70:73], a[64:67], v[90:93], v[70:73]\n\t" \
        "ds_read_b64_tr_b16 a[84:85], v7 offset:6240\n\t" \
        "v_cvt_pk_bf16_f32 v101, v132, v133\n\t" \
        "s_waitcnt lgkmcnt(14)\n\t" \
        "v_mfma_f32_16x16x32_bf16 v[50:53], a[68:71], v[86:89], v[50:53]\n\t" \
        "ds_read_b64_tr_b16 a[86:87], v7 offset:9312\n\t" \
        "v_cvt_pk_bf16_f32 v78, v102, v103\n\t" \
        "v_mfma_f32_16x16x32_bf16 v[74:77], a[68:71], v[90:93], v[74:77]\n\t" \
        "ds_read_b64_tr_b16 a[88:89], v6 offset:6272\n\t" \
        "ds_read_b64_tr_b16 a[90:91], v6 offset:9344\n\t" \
        "v_mfma_f32_16x16x32_bf16 v[118:121], a[112:115], v[154:157], 0\n\t" \
        "s_waitcnt vmcnt(6)\n\t" \
        "s_barrier\n\t" \
        "ds_read_b64_tr_b16 a[92:93], v7 offset:6304\n\t" \
        "v_cvt_pk_bf16_f32 v79, v104, v105\n\t" \
        "s_waitcnt lgkmcnt(14)\n\t" \
        "v_mfma_f32_16x16x32_bf16 v[122:125], a[116:119], v[154:157], 0\n\t" \
        "ds_read_b64_tr_b16 a[94:95], v7 offset:9376\n\t" \
        "ds_read_b64_tr_b16 a[128:129], v134 offset:128\n\t" \
        "v_mfma_f32_16x16x32_bf16 v[126:129], a[120:123], v[158:161], 0\n\t" \
        "ds_read_b64_tr_b16 a[130:131], v135 offset:128\n\t" \
        "v_cvt_pk_bf16_f32 v80, v106, v107\n\t" \
        "s_waitcnt lgkmcnt(14)\n\t" \
        "v_mfma_f32_16x16x32_bf16 v[130:133], a[124:127], v[158:161], 0\n\t" \
        "s_add_u32 m0, s39, 49152\n\t" \
        "s_add_u32 s57, s40, 110592\n\t" \
        "buffer_load_dwordx4 v3, s[24:27], s57 offen offset:0 lds\n\t" \
        "s_waitcnt lgkmcnt(13)\n\t" \
        "v_mfma_f32_16x16x32_bf16 v[30:33], a[72:75], v[94:97], v[30:33]\n\t" \
        "ds_read_b64_tr_b16 a[132:133], v138 offset:128\n\t" \
        "s_waitcnt lgkmcnt(12)\n\t" \
        "v_xor_b32 v142, v174, v11\n\t" \
        "v_mfma_f32_16x16x32_bf16 v[54:57], a[72:75], v[98:101], v[54:57]\n\t" \
        "ds_read_b64_tr_b16 a[134:135], v139 offset:128\n\t" \
        "ds_read_b64_tr_b16 a[136:137], v136 offset:128\n\t" \
        "s_waitcnt lgkmcnt(13)\n\t" \
        "v_mfma_f32_16x16x32_bf16 v[34:37], a[76:79], v[94:97], v[34:37]\n\t" \
        "ds_read_b64_tr_b16 a[138:139], v137 offset:128\n\t" \
        "v_xor_b32 v143, v175, v11\n\t" \
        "v_mfma_f32_16x16x32_bf16 v[58:61], a[76:79], v[98:101], v[58:61]\n\t" \
        "ds_read_b64_tr_b16 a[140:141], v140 offset:128\n\t" \
        "ds_read_b64_tr_b16 a[142:143], v141 offset:128\n\t" \
        "s_waitcnt lgkmcnt(13)\n\t" \
        "v_mfma_f32_16x16x32_bf16 v[38:41], a[80:83], v[94:97], v[38:41]\n\t" \
        "ds_read_b64_tr_b16 a[0:1], v6 offset:12288\n\t" \
        "v_xor_b32 v144, v176, v11\n\t" \
        "v_mfma_f32_16x16x32_bf16 v[62:65], a[80:83], v[98:101], v[62:65]\n\t" \
        "buffer_load_dwordx4 v4, s[24:27], s57 offen offset:1024 lds\n\t" \
        "s_waitcnt lgkmcnt(11)\n\t" \
        "v_mfma_f32_16x16x32_bf16 v[42:45], a[84:87], v[94:97], v[42:45]\n\t" \
        "ds_read_b64_tr_b16 a[2:3], v6 offset:15360\n\t" \
        "ds_read_b64_tr_b16 a[4:5], v7 offset:12320\n\t" \
        "v_xor_b32 v146, v174, v12\n\t" \
        "v_xor_b32 v145, v177, v11\n\t" \
        "v_mfma_f32_16x16x32_bf16 v[66:69], a[84:87], v[98:101], v[66:69]\n\t" \
        "ds_read_b64 v[152:153], v10 offset:640\n\t" \
        "v_xor_b32 v147, v175, v12\n\t" \
        "v_xor_b32 v148, v176, v12\n\t" \
        "v_mfma_f32_16x16x32_bf16 v[46:49], a[88:91], v[94:97], v[46:49]\n\t" \
        "ds_read_b64_tr_b16 a[6:7], v7 offset:15392\n\t" \
        "v_xor_b32 v149, v177, v12\n\t" \
        "v_mfma_f32_16x16x32_bf16 v[70:73], a[88:91], v[98:101], v[70:73]\n\t" \
        "ds_read_b64_tr_b16 a[8:9], v6 offset:12352\n\t" \
        "ds_read_b64_tr_b16 a[10:11], v6 offset:15424\n\t" \
        "v_cvt_pk_bf16_f32 v81, v108, v109\n\t" \
        "v_cvt_pk_bf16_f32 v82, v110, v111\n\t" \
        "s_waitcnt lgkmcnt(13)\n\t" \
        "v_mfma_f32_16x16x32_bf16 v[50:53], a[92:95], v[94:97], v[50:53]\n\t" \
        "ds_read_b64_tr_b16 a[12:13], v7 offset:12384\n\t" \
        "v_cvt_pk_bf16_f32 v83, v112, v113\n\t" \
        "v_cvt_pk_bf16_f32 v84, v114, v115\n\t" \
        "v_mfma_f32_16x16x32_bf16 v[74:77], a[92:95], v[98:101], v[74:77]\n\t" \
        "buffer_load_dwordx4 v5, s[24:27], s57 offen offset:2048 lds\n\t" \
        "v_mfma_f32_16x16x32_bf16 v[102:105], a[128:131], v[154:157], 0\n\t" \
        "ds_read_b64_tr_b16 a[14:15], v7 offset:15456\n\t" \
        "ds_read_b64_tr_b16 a[16:17], v6 offset:12416\n\t" \
        "v_cvt_pk_bf16_f32 v85, v116, v117\n\t" \
        "s_waitcnt lgkmcnt(12)\n\t" \
        "v_mfma_f32_16x16x32_bf16 v[106:109], a[132:135], v[154:157], 0\n\t" \
        "ds_read_b64_tr_b16 a[18:19], v6 offset:15488\n\t" \
        "v_cvt_pk_bf16_f32 v86, v118, v119\n\t" \
        "v_mfma_f32_16x16x32_bf16 v[110:113], a[136:139], v[158:161], 0\n\t" \
        "ds_read_b64_tr_b16 a[20:21], v7 offset:12448\n\t" \
        "ds_read_b64_tr_b16 a[22:23], v7 offset:15520\n\t" \
        "s_waitcnt lgkmcnt(12)\n\t" \
        "v_mfma_f32_16x16x32_bf16 v[114:117], a[140:143], v[158:161], 0\n\t" \
        "ds_read_b64_tr_b16 a[96:97], v142 offset:0\n\t" \
        "v_cvt_pk_bf16_f32 v87, v120, v121\n\t" \
        "s_waitcnt lgkmcnt(12)\n\t" \
        "v_mfma_f32_16x16x32_bf16 v[30:33], a[0:3], v[78:81], v[30:33]\n\t" \
        "ds_read_b64_tr_b16 a[98:99], v143 offset:0\n\t" \
        "s_waitcnt lgkmcnt(11)\n\t" \
        "v_and_b32 v162, v152, v25\n\t" \
        "v_mfma_f32_16x16x32_bf16 v[54:57], a[0:3], v[82:85], v[54:57]\n\t" \
        "ds_read_b64_tr_b16 a[100:101], v146 offset:0\n\t" \
        "ds_read_b64_tr_b16 a[102:103], v147 offset:0\n\t" \
        "s_waitcnt lgkmcnt(12)\n\t" \
        "v_mfma_f32_16x16x32_bf16 v[34:37], a[4:7], v[78:81], v[34:37]\n\t" \
        "ds_read_b64_tr_b16 a[104:105], v144 offset:0\n\t" \
        "v_and_b32 v163, v152, v26\n\t" \
        "v_mfma_f32_16x16x32_bf16 v[58:61], a[4:7], v[82:85], v[58:61]\n\t" \
        "ds_read_b64_tr_b16 a[106:107], v145 offset:0\n\t" \
        "ds_read_b64_tr_b16 a[108:109], v148 offset:0\n\t" \
        "s_waitcnt lgkmcnt(13)\n\t" \
        "v_mfma_f32_16x16x32_bf16 v[38:41], a[8:11], v[78:81], v[38:41]\n\t" \
        "ds_read_b64_tr_b16 a[110:111], v149 offset:0\n\t" \
        "v_and_b32 v164, v152, v27\n\t" \
        "v_mfma_f32_16x16x32_bf16 v[62:65], a[8:11], v[82:85], v[62:65]\n\t" \
        "ds_read_b64_tr_b16 a[24:25], v6 offset:18432\n\t" \
        "ds_read_b64_tr_b16 a[26:27], v6 offset:21504\n\t" \
        "s_waitcnt lgkmcnt(14)\n\t" \
        "v_mfma_f32_16x16x32_bf16 v[42:45], a[12:15], v[78:81], v[42:45]\n\t" \
        "ds_read_b64_tr_b16 a[28:29], v7 offset:18464\n\t" \
        "v_and_b32 v165, v152, v28\n\t" \
        "v_mfma_f32_16x16x32_bf16 v[66:69], a[12:15], v[82:85], v[66:69]\n\t" \
        "ds_read_b64_tr_b16 a[30:31], v7 offset:21536\n\t" \
        "v_and_b32 v166, v153, v25\n\t" \
        "s_waitcnt lgkmcnt(14)\n\t" \
        "v_mfma_f32_16x16x32_bf16 v[46:49], a[16:19], v[78:81], v[46:49]\n\t" \
        "ds_read_b64_tr_b16 a[32:33], v6 offset:18496\n\t" \
        "ds_read_b64_tr_b16 a[34:35], v6 offset:21568\n\t" \
        "v_cvt_pk_bf16_f32 v88, v122, v123\n\t" \
        "v_and_b32 v167, v153, v26\n\t" \
        "v_mfma_f32_16x16x32_bf16 v[70:73], a[16:19], v[82:85], v[70:73]\n\t" \
        "ds_read_b64_tr_b16 a[36:37], v7 offset:18528\n\t" \
        "v_cvt_pk_bf16_f32 v89, v124, v125\n\t" \
        "v_cvt_pk_bf16_f32 v90, v126, v127\n\t" \
        "s_waitcnt lgkmcnt(14)\n\t" \
        "v_mfma_f32_16x16x32_bf16 v[50:53], a[20:23], v[78:81], v[50:53]\n\t" \
        "ds_read_b64_tr_b16 a[38:39], v7 offset:21600\n\t" \
        "ds_read_b64_tr_b16 a[40:41], v6 offset:18560\n\t" \
        "v_and_b32 v168, v153, v27\n\t" \
        "v_mfma_f32_16x16x32_bf16 v[74:77], a[20:23], v[82:85], v[74:77]\n\t" \
        "ds_read_b64_tr_b16 a[42:43], v6 offset:21632\n\t" \
        "v_cvt_pk_bf16_f32 v91, v128, v129\n\t" \
        "v_cvt_pk_bf16_f32 v92, v130, v131\n\t" \
        "s_waitcnt lgkmcnt(14)\n\t" \
        "v_mfma_f32_16x16x32_bf16 v[118:121], a[96:99], v[162:165], 0\n\t" \
        "s_waitcnt vmcnt(6)\n\t" \
        "s_barrier\n\t" \
        "ds_read_b64_tr_b16 a[44:45], v7 offset:18592\n\t" \
        "ds_read_b64_tr_b16 a[46:47], v7 offset:21664\n\t" \
        "v_and_b32 v169, v153, v28\n\t" \
        "v_cvt_pk_bf16_f32 v93, v132, v133\n\t" \
        "v_mfma_f32_16x16x32_bf16 v[122:125], a[100:103], v[162:165], 0\n\t" \
        "ds_read_b64_tr_b16 a[112:113], v142 offset:64\n\t" \
        "v_cvt_pk_bf16_f32 v94, v102, v103\n\t" \
        "s_waitcnt lgkmcnt(14)\n\t" \
        "v_mfma_f32_16x16x32_bf16 v[126:129], a[104:107], v[166:169], 0\n\t" \
        "ds_read_b64_tr_b16 a[114:115], v143 offset:64\n\t" \
        "v_cvt_pk_bf16_f32 v95, v104, v105\n\t" \
        "s_waitcnt lgkmcnt(14)\n\t" \
        "v_mfma_f32_16x16x32_bf16 v[130:133], a[108:111], v[166:169], 0\n\t" \
        "s_add_u32 m0, s39, 0\n\t" \
        "s_add_u32 s57, s40, 122880\n\t" \
        "buffer_load_dwordx4 v3, s[24:27], s57 offen offset:0 lds\n\t" \
        "s_waitcnt lgkmcnt(12)\n\t" \
        "v_mfma_f32_16x16x32_bf16 v[30:33], a[24:27], v[86:89], v[30:33]\n\t" \
        "ds_read_b64_tr_b16 a[116:117], v146 offset:64\n\t" \
        "ds_read_b64_tr_b16 a[118:119], v147 offset:64\n\t" \
        "v_mfma_f32_16x16x32_bf16 v[54:57], a[24:27], v[90:93], v[54:57]\n\t" \
        "ds_read_b64_tr_b16 a[120:121], v144 offset:64\n\t" \
        "v_cvt_pk_bf16_f32 v96, v106, v107\n\t" \
        "s_waitcnt lgkmcnt(13)\n\t" \
        "v_mfma_f32_16x16x32_bf16 v[34:37], a[28:31], v[86:89], v[34:37]\n\t" \
        "ds_read_b64_tr_b16 a[122:123], v145 offset:64\n\t" \
        "ds_read_b64_tr_b16 a[124:125], v148 offset:64\n\t" \
        "v_mfma_f32_16x16x32_bf16 v[58:61], a[28:31], v[90:93], v[58:61]\n\t" \
        "ds_read_b64_tr_b16 a[126:127], v149 offset:64\n\t" \
        "v_cvt_pk_bf16_f32 v97, v108, v109\n\t" \
        "s_waitcnt lgkmcnt(13)\n\t" \
        "v_mfma_f32_16x16x32_bf16 v[38:41], a[32:35], v[86:89], v[38:41]\n\t" \
        "ds_read_b64_tr_b16 a[48:49], v6 offset:24576\n\t" \
        "ds_read_b64_tr_b16 a[50:51], v6 offset:27648\n\t" \
        "v_mfma_f32_16x16x32_bf16 v[62:65], a[32:35], v[90:93], v[62:65]\n\t" \
        "buffer_load_dwordx4 v4, s[24:27], s57 offen offset:1024 lds\n\t" \
        "s_waitcnt lgkmcnt(12)\n\t" \
        "v_mfma_f32_16x16x32_bf16 v[42:45], a[36:39], v[86:89], v[42:45]\n\t" \
        "ds_read_b128 v[170:173], v9 offset:768\n\t" \
        "v_cvt_pk_bf16_f32 v98, v110, v111\n\t" \
        "v_mfma_f32_16x16x32_bf16 v[66:69], a[36:39], v[90:93], v[66:69]\n\t" \
        "ds_read_b64_tr_b16 a[52:53], v7 offset:24608\n\t" \
        "v_cvt_pk_bf16_f32 v99, v112, v113\n\t" \
        "v_mfma_f32_16x16x32_bf16 v[46:49], a[40:43], v[86:89], v[46:49]\n\t" \
        "ds_read_b64_tr_b16 a[54:55], v7 offset:27680\n\t" \
        "ds_read_b64_tr_b16 a[56:57], v6 offset:24640\n\t" \
        "v_mfma_f32_16x16x32_bf16 v[70:73], a[40:43], v[90:93], v[70:73]\n\t" \
        "ds_read_b64_tr_b16 a[58:59], v6 offset:27712\n\t" \
        "v_cvt_pk_bf16_f32 v100, v114, v115\n\t" \
        "s_waitcnt lgkmcnt(13)\n\t" \
        "v_mfma_f32_16x16x32_bf16 v[50:53], a[44:47], v[86:89], v[50:53]\n\t" \
        "ds_read_b64_tr_b16 a[60:61], v7 offset:24672\n\t" \
        "ds_read_b64_tr_b16 a[62:63], v7 offset:27744\n\t" \
        "v_mfma_f32_16x16x32_bf16 v[74:77], a[44:47], v[90:93], v[74:77]\n\t" \
        "buffer_load_dwordx4 v5, s[24:27], s57 offen offset:2048 lds\n\t" \
        "v_mfma_f32_16x16x32_bf16 v[102:105], a[112:115], v[162:165], 0\n\t" \
        "ds_read_b64_tr_b16 a[64:65], v6 offset:24704\n\t" \
        "v_cvt_pk_bf16_f32 v101, v116, v117\n\t" \
        "s_waitcnt lgkmcnt(11)\n\t" \
        "v_mfma_f32_16x16x32_bf16 v[106:109], a[116:119], v[162:165], 0\n\t" \
        "ds_read_b64_tr_b16 a[66:67], v6 offset:27776\n\t" \
        "ds_read_b64_tr_b16 a[68:69], v7 offset:24736\n\t" \
        "v_mfma_f32_16x16x32_bf16 v[110:113], a[120:123], v[166:169], 0\n\t" \
        "ds_read_b64_tr_b16 a[70:71], v7 offset:27808\n\t" \
        "v_cvt_pk_bf16_f32 v78, v118, v119\n\t" \
        "s_waitcnt lgkmcnt(11)\n\t" \
        "v_mfma_f32_16x16x32_bf16 v[114:117], a[124:127], v[166:169], 0\n\t" \
        "ds_read_b64_tr_b16 a[128:129], v142 offset:128\n\t" \
        "s_waitcnt lgkmcnt(11)\n\t" \
        "v_xor_b32 v134, v170, v11\n\t" \
        "v_mfma_f32_16x16x32_bf16 v[30:33], a[48:51], v[94:97], v[30:33]\n\t" \
        "ds_read_b64_tr_b16 a[130:131], v143 offset:128\n\t" \
        "ds_read_b64_tr_b16 a[132:133], v146 offset:128\n\t" \
        "v_mfma_f32_16x16x32_bf16 v[54:57], a[48:51], v[98:101], v[54:57]\n\t" \
        "ds_read_b64_tr_b16 a[134:135], v147 offset:128\n\t" \
        "v_xor_b32 v135, v171, v11\n\t" \
        "s_waitcnt lgkmcnt(12)\n\t" \
        "v_mfma_f32_16x16x32_bf16 v[34:37], a[52:55], v[94:97], v[34:37]\n\t" \
        "ds_read_b64_tr_b16 a[136:137], v144 offset:128\n\t" \
        "ds_read_b64_tr_b16 a[138:139], v145 offset:128\n\t" \
        "v_mfma_f32_16x16x32_bf16 v[58:61], a[52:55], v[98:101], v[58:61]\n\t" \
        "ds_read_b64_tr_b16 a[140:141], v148 offset:128\n\t" \
        "v_xor_b32 v136, v172, v11\n\t" \
        "s_waitcnt lgkmcnt(13)\n\t" \
        "v_mfma_f32_16x16x32_bf16 v[38:41], a[56:59], v[94:97], v[38:41]\n\t" \
        "ds_read_b64_tr_b16 a[142:143], v149 offset:128\n\t" \
        "ds_read_b64_tr_b16 a[72:73], v6 offset:30720\n\t" \
        "v_mfma_f32_16x16x32_bf16 v[62:65], a[56:59], v[98:101], v[62:65]\n\t" \
        "ds_read_b64_tr_b16 a[74:75], v6 offset:33792\n\t" \
        "v_xor_b32 v138, v170, v12\n\t" \
        "s_waitcnt lgkmcnt(14)\n\t" \
        "v_mfma_f32_16x16x32_bf16 v[42:45], a[60:63], v[94:97], v[42:45]\n\t" \
        "ds_read_b64_tr_b16 a[76:77], v7 offset:30752\n\t" \
        "v_xor_b32 v137, v173, v11\n\t" \
        "v_mfma_f32_16x16x32_bf16 v[66:69], a[60:63], v[98:101], v[66:69]\n\t" \
        "ds_read_b64 v[150:151], v10 offset:768\n\t" \
        "ds_read_b64_tr_b16 a[78:79], v7 offset:33824\n\t" \
        "v_xor_b32 v139, v171, v12\n\t" \
        "v_xor_b32 v140, v172, v12\n\t" \
        "s_waitcnt lgkmcnt(14)\n\t" \
        "v_mfma_f32_16x16x32_bf16 v[46:49], a[64:67], v[94:97], v[46:49]\n\t" \
        "ds_read_b64_tr_b16 a[80:81], v6 offset:30784\n\t" \
        "v_xor_b32 v141, v173, v12\n\t" \
        "v_cvt_pk_bf16_f32 v79, v120, v121\n\t" \
        "v_mfma_f32_16x16x32_bf16 v[70:73], a[64:67], v[98:101], v[70:73]\n\t" \
        "ds_read_b64_tr_b16 a[82:83], v6 offset:33856\n\t" \
        "ds_read_b64_tr_b16 a[84:85], v7 offset:30816\n\t" \
        "v_cvt_pk_bf16_f32 v80, v122, v123\n\t" \
        "s_waitcnt lgkmcnt(15)\n\t" \
        "v_mfma_f32_16x16x32_bf16 v[50:53], a[68:71], v[94:97], v[50:53]\n\t" \
        "ds_read_b64_tr_b16 a[86:87], v7 offset:33888\n\t" \
        "v_cvt_pk_bf16_f32 v81, v124, v125\n\t" \
        "v_cvt_pk_bf16_f32 v82, v126, v127\n\t" \
        "v_mfma_f32_16x16x32_bf16 v[74:77], a[68:71], v[98:101], v[74:77]\n\t" \
        "ds_read_b64_tr_b16 a[88:89], v6 offset:30848\n\t" \
        "ds_read_b64_tr_b16 a[90:91], v6 offset:33920\n\t" \
        "v_cvt_pk_bf16_f32 v83, v128, v129\n\t" \
        "v_cvt_pk_bf16_f32 v84, v130, v131\n\t" \
        "s_waitcnt lgkmcnt(15)\n\t" \
        "v_mfma_f32_16x16x32_bf16 v[118:121], a[128:131], v[162:165], 0\n\t" \
        "s_waitcnt vmcnt(6)\n\t" \
        "s_barrier\n\t" \
        "ds_read_b64_tr_b16 a[92:93], v7 offset:30880\n\t" \
        "v_cvt_pk_bf16_f32 v85, v132, v133\n\t" \
        "v_mfma_f32_16x16x32_bf16 v[122:125], a[132:135], v[162:165], 0\n\t" \
        "ds_read_b64_tr_b16 a[94:95], v7 offset:33952\n\t" \
        "v_cvt_pk_bf16_f32 v86, v102, v103\n\t" \
        "s_waitcnt lgkmcnt(14)\n\t" \
        "v_mfma_f32_16x16x32_bf16 v[126:129], a[136:139], v[166:169], 0\n\t" \
        "ds_read_b64_tr_b16 a[96:97], v134 offset:0\n\t" \
        "ds_read_b64_tr_b16 a[98:99], v135 offset:0\n\t" \
        "s_waitcnt lgkmcnt(14)\n\t" \
        "v_mfma_f32_16x16x32_bf16 v[130:133], a[140:143], v[166:169], 0\n\t" \
        "s_add_u32 m0, s39, 12288\n\t" \
        "s_add_u32 s57, s40, 135168\n\t" \
        "buffer_load_dwordx4 v3, s[24:27], s57 offen offset:0 lds\n\t" \
        "s_waitcnt lgkmcnt(13)\n\t" \
        "v_mfma_f32_16x16x32_bf16 v[30:33], a[72:75], v[78:81], v[30:33]\n\t" \
        "ds_read_b64_tr_b16 a[100:101], v138 offset:0\n\t" \
        "s_waitcnt lgkmcnt(12)\n\t" \
        "v_and_b32 v154, v150, v25\n\t" \
        "v_mfma_f32_16x16x32_bf16 v[54:57], a[72:75], v[82:85], v[54:57]\n\t" \
        "ds_read_b64_tr_b16 a[102:103], v139 offset:0\n\t" \
        "ds_read_b64_tr_b16 a[104:105], v136 offset:0\n\t" \
        "s_waitcnt lgkmcnt(13)\n\t" \
        "v_mfma_f32_16x16x32_bf16 v[34:37], a[76:79], v[78:81], v[34:37]\n\t" \
        "ds_read_b64_tr_b16 a[106:107], v137 offset:0\n\t" \
        "v_and_b32 v155, v150, v26\n\t" \
        "v_mfma_f32_16x16x32_bf16 v[58:61], a[76:79], v[82:85], v[58:61]\n\t" \
        "ds_read_b64_tr_b16 a[108:109], v140 offset:0\n\t" \
        "ds_read_b64_tr_b16 a[110:111], v141 offset:0\n\t" \
        "v_and_b32 v156, v150, v27\n\t" \
        "s_waitcnt lgkmcnt(13)\n\t" \
        "v_mfma_f32_16x16x32_bf16 v[38:41], a[80:83], v[78:81], v[38:41]\n\t" \
        "ds_read_b64_tr_b16 a[0:1], v6 offset:36864\n\t" \
        "v_and_b32 v157, v150, v28\n\t" \
        "v_and_b32 v158, v151, v25\n\t" \
        "v_mfma_f32_16x16x32_bf16 v[62:65], a[80:83], v[82:85], v[62:65]\n\t" \
        "buffer_load_dwordx4 v4, s[24:27], s57 offen offset:1024 lds\n\t" \
        "s_waitcnt lgkmcnt(11)\n\t" \
        "v_mfma_f32_16x16x32_bf16 v[42:45], a[84:87], v[78:81], v[42:45]\n\t" \
        "ds_read_b64_tr_b16 a[2:3], v6 offset:39936\n\t" \
        "ds_read_b64_tr_b16 a[4:5], v7 offset:36896\n\t" \
        "v_cvt_pk_bf16_f32 v87, v104, v105\n\t" \
        "v_mfma_f32_16x16x32_bf16 v[66:69], a[84:87], v[82:85], v[66:69]\n\t" \
        "ds_read_b64_tr_b16 a[6:7], v7 offset:39968\n\t" \
        "v_cvt_pk_bf16_f32 v88, v106, v107\n\t" \
        "v_and_b32 v159, v151, v26\n\t" \
        "v_mfma_f32_16x16x32_bf16 v[46:49], a[88:91], v[78:81], v[46:49]\n\t" \
        "ds_read_b64_tr_b16 a[8:9], v6 offset:36928\n\t" \
        "v_cvt_pk_bf16_f32 v89, v108, v109\n\t" \
        "v_cvt_pk_bf16_f32 v90, v110, v111\n\t" \
        "v_mfma_f32_16x16x32_bf16 v[70:73], a[88:91], v[82:85], v[70:73]\n\t" \
        "ds_read_b64_tr_b16 a[10:11], v6 offset:40000\n\t" \
        "ds_read_b64_tr_b16 a[12:13], v7 offset:36960\n\t" \
        "v_and_b32 v160, v151, v27\n\t" \
        "s_waitcnt lgkmcnt(13)\n\t" \
        "v_mfma_f32_16x16x32_bf16 v[50:53], a[92:95], v[78:81], v[50:53]\n\t" \
        "ds_read_b64_tr_b16 a[14:15], v7 offset:40032\n\t" \
        "v_cvt_pk_bf16_f32 v91, v112, v113\n\t" \
        "v_cvt_pk_bf16_f32 v92, v114, v115\n\t" \
        "v_mfma_f32_16x16x32_bf16 v[74:77], a[92:95], v[82:85], v[74:77]\n\t" \
        "buffer_load_dwordx4 v5, s[24:27], s57 offen offset:2048 lds\n\t" \
        "v_mfma_f32_16x16x32_bf16 v[102:105], a[96:99], v[154:157], 0\n\t" \
        "ds_read_b64_tr_b16 a[16:17], v6 offset:36992\n\t" \
        "ds_read_b64_tr_b16 a[18:19], v6 offset:40064\n\t" \
        "v_and_b32 v161, v151, v28\n\t" \
        "v_cvt_pk_bf16_f32 v93, v116, v117\n\t" \
        "s_waitcnt lgkmcnt(12)\n\t" \
        "v_mfma_f32_16x16x32_bf16 v[106:109], a[100:103], v[154:157], 0\n\t" \
        "ds_read_b64_tr_b16 a[20:21], v7 offset:37024\n\t" \
        "v_cvt_pk_bf16_f32 v94, v118, v119\n\t" \
        "v_mfma_f32_16x16x32_bf16 v[110:113], a[104:107], v[158:161], 0\n\t" \
        "ds_read_b64_tr_b16 a[22:23], v7 offset:40096\n\t" \
        "v_cvt_pk_bf16_f32 v95, v120, v121\n\t" \
        "s_waitcnt lgkmcnt(11)\n\t" \
        "v_mfma_f32_16x16x32_bf16 v[114:117], a[108:111], v[158:161], 0\n\t" \
        "ds_read_b64_tr_b16 a[112:113], v134 offset:64\n\t" \
        "ds_read_b64_tr_b16 a[114:115], v135 offset:64\n\t" \
        "s_waitcnt lgkmcnt(12)\n\t" \
        "v_mfma_f32_16x16x32_bf16 v[30:33], a[0:3], v[86:89], v[30:33]\n\t" \
        "ds_read_b64_tr_b16 a[116:117], v138 offset:64\n\t" \
        "v_cvt_pk_bf16_f32 v96, v122, v123\n\t" \
        "v_mfma_f32_16x16x32_bf16 v[54:57], a[0:3], v[90:93], v[54:57]\n\t" \
        "ds_read_b64_tr_b16 a[118:119], v139 offset:64\n\t" \
        "ds_read_b64_tr_b16 a[120:121], v136 offset:64\n\t" \
        "s_waitcnt lgkmcnt(13)\n\t" \
        "v_mfma_f32_16x16x32_bf16 v[34:37], a[4:7], v[86:89], v[34:37]\n\t" \
        "ds_read_b64_tr_b16 a[122:123], v137 offset:64\n\t" \
        "v_cvt_pk_bf16_f32 v97, v124, v125\n\t" \
        "v_mfma_f32_16x16x32_bf16 v[58:61], a[4:7], v[90:93], v[58:61]\n\t" \
        "ds_read_b64_tr_b16 a[124:125], v140 offset:64\n\t" \
        "v_cvt_pk_bf16_f32 v98, v126, v127\n\t" \
        "s_waitcnt lgkmcnt(12)\n\t" \
        "v_mfma_f32_16x16x32_bf16 v[38:41], a[8:11], v[86:89], v[38:41]\n\t" \
        "ds_read_b64_tr_b16 a[126:127], v141 offset:64\n\t" \
        "ds_read_b64_tr_b16 a[24:25], v6 offset:43008\n\t" \
        "v_mfma_f32_16x16x32_bf16 v[62:65], a[8:11], v[90:93], v[62:65]\n\t" \
        "ds_read_b64_tr_b16 a[26:27], v6 offset:46080\n\t" \
        "v_cvt_pk_bf16_f32 v99, v128, v129\n\t" \
        "s_waitcnt lgkmcnt(14)\n\t" \
        "v_mfma_f32_16x16x32_bf16 v[42:45], a[12:15], v[86:89], v[42:45]\n\t" \
        "ds_read_b128 v[174:177], v9 offset:896\n\t" \
        "ds_read_b64_tr_b16 a[28:29], v7 offset:43040\n\t" \
        "v_mfma_f32_16x16x32_bf16 v[66:69], a[12:15], v[90:93], v[66:69]\n\t" \
        "ds_read_b64_tr_b16 a[30:31], v7 offset:46112\n\t" \
        "v_cvt_pk_bf16_f32 v100, v130, v131\n\t" \
        "s_waitcnt lgkmcnt(14)\n\t" \
        "v_mfma_f32_16x16x32_bf16 v[46:49], a[16:19], v[86:89], v[46:49]\n\t" \
        "ds_read_b64_tr_b16 a[32:33], v6 offset:43072\n\t" \
        "ds_read_b64_tr_b16 a[34:35], v6 offset:46144\n\t" \
        "v_mfma_f32_16x16x32_bf16 v[70:73], a[16:19], v[90:93], v[70:73]\n\t" \
        "ds_read_b64_tr_b16 a[36:37], v7 offset:43104\n\t" \
        "v_cvt_pk_bf16_f32 v101, v132, v133\n\t" \
        "s_waitcnt lgkmcnt(14)\n\t" \
        "v_mfma_f32_16x16x32_bf16 v[50:53], a[20:23], v[86:89], v[50:53]\n\t" \
        "ds_read_b64_tr_b16 a[38:39], v7 offset:46176\n\t" \
        "v_cvt_pk_bf16_f32 v78, v102, v103\n\t" \
        "v_mfma_f32_16x16x32_bf16 v[74:77], a[20:23], v[90:93], v[74:77]\n\t" \
        "ds_read_b64_tr_b16 a[40:41], v6 offset:43136\n\t" \
        "ds_read_b64_tr_b16 a[42:43], v6 offset:46208\n\t" \
        "v_mfma_f32_16x16x32_bf16 v[118:121], a[112:115], v[154:157], 0\n\t" \
        "s_waitcnt vmcnt(6)\n\t" \
        "s_barrier\n\t" \
        "ds_read_b64_tr_b16 a[44:45], v7 offset:43168\n\t" \
        "v_cvt_pk_bf16_f32 v79, v104, v105\n\t" \
        "s_waitcnt lgkmcnt(14)\n\t" \
        "v_mfma_f32_16x16x32_bf16 v[122:125], a[116:119], v[154:157], 0\n\t" \
        "ds_read_b64_tr_b16 a[46:47], v7 offset:46240\n\t" \
        "ds_read_b64_tr_b16 a[128:129], v134 offset:128\n\t" \
        "v_mfma_f32_16x16x32_bf16 v[126:129], a[120:123], v[158:161], 0\n\t" \
        "ds_read_b64_tr_b16 a[130:131], v135 offset:128\n\t" \
        "v_cvt_pk_bf16_f32 v80, v106, v107\n\t" \
        "s_waitcnt lgkmcnt(14)\n\t" \
        "v_mfma_f32_16x16x32_bf16 v[130:133], a[124:127], v[158:161], 0\n\t" \
        "s_add_u32 m0, s39, 24576\n\t" \
        "s_add_u32 s57, s40, 147456\n\t" \
        "buffer_load_dwordx4 v3, s[24:27], s57 offen offset:0 lds\n\t" \
        "s_waitcnt lgkmcnt(13)\n\t" \
        "v_mfma_f32_16x16x32_bf16 v[30:33], a[24:27], v[94:97], v[30:33]\n\t" \
        "ds_read_b64_tr_b16 a[132:133], v138 offset:128\n\t" \
        "s_waitcnt lgkmcnt(12)\n\t" \
        "v_xor_b32 v142, v174, v11\n\t" \
        "v_mfma_f32_16x16x32_bf16 v[54:57], a[24:27], v[98:101], v[54:57]\n\t" \
        "ds_read_b64_tr_b16 a[134:135], v139 offset:128\n\t" \
        "ds_read_b64_tr_b16 a[136:137], v136 offset:128\n\t" \
        "s_waitcnt lgkmcnt(13)\n\t" \
        "v_mfma_f32_16x16x32_bf16 v[34:37], a[28:31], v[94:97], v[34:37]\n\t" \
        "ds_read_b64_tr_b16 a[138:139], v137 offset:128\n\t" \
        "v_xor_b32 v143, v175, v11\n\t" \
        "v_mfma_f32_16x16x32_bf16 v[58:61], a[28:31], v[98:101], v[58:61]\n\t" \
        "ds_read_b64_tr_b16 a[140:141], v140 offset:128\n\t" \
        "ds_read_b64_tr_b16 a[142:143], v141 offset:128\n\t" \
        "s_waitcnt lgkmcnt(13)\n\t" \
        "v_mfma_f32_16x16x32_bf16 v[38:41], a[32:35], v[94:97], v[38:41]\n\t" \
        "ds_read_b64_tr_b16 a[48:49], v6 offset:49152\n\t" \
        "v_xor_b32 v144, v176, v11\n\t" \
        "v_mfma_f32_16x16x32_bf16 v[62:65], a[32:35], v[98:101], v[62:65]\n\t" \
        "buffer_load_dwordx4 v4, s[24:27], s57 offen offset:1024 lds\n\t" \
        "s_waitcnt lgkmcnt(11)\n\t" \
        "v_mfma_f32_16x16x32_bf16 v[42:45], a[36:39], v[94:97], v[42:45]\n\t" \
        "ds_read_b64_tr_b16 a[50:51], v6 offset:52224\n\t" \
        "ds_read_b64_tr_b16 a[52:53], v7 offset:49184\n\t" \
        "v_xor_b32 v146, v174, v12\n\t" \
        "v_xor_b32 v145, v177, v11\n\t" \
        "v_mfma_f32_16x16x32_bf16 v[66:69], a[36:39], v[98:101], v[66:69]\n\t" \
        "ds_read_b64 v[152:153], v10 offset:896\n\t" \
        "v_xor_b32 v147, v175, v12\n\t" \
        "v_xor_b32 v148, v176, v12\n\t" \
        "v_mfma_f32_16x16x32_bf16 v[46:49], a[40:43], v[94:97], v[46:49]\n\t" \
        "ds_read_b64_tr_b16 a[54:55], v7 offset:52256\n\t" \
        "v_xor_b32 v149, v177, v12\n\t" \
        "v_mfma_f32_16x16x32_bf16 v[70:73], a[40:43], v[98:101], v[70:73]\n\t" \
        "ds_read_b64_tr_b16 a[56:57], v6 offset:49216\n\t" \
        "ds_read_b64_tr_b16 a[58:59], v6 offset:52288\n\t" \
        "v_cvt_pk_bf16_f32 v81, v108, v109\n\t" \
        "v_cvt_pk_bf16_f32 v82, v110, v111\n\t" \
        "s_waitcnt lgkmcnt(13)\n\t" \
        "v_mfma_f32_16x16x32_bf16 v[50:53], a[44:47], v[94:97], v[50:53]\n\t" \
        "ds_read_b64_tr_b16 a[60:61], v7 offset:49248\n\t" \
        "v_cvt_pk_bf16_f32 v83, v112, v113\n\t" \
        "v_cvt_pk_bf16_f32 v84, v114, v115\n\t" \
        "v_mfma_f32_16x16x32_bf16 v[74:77], a[44:47], v[98:101], v[74:77]\n\t" \
        "buffer_load_dwordx4 v5, s[24:27], s57 offen offset:2048 lds\n\t" \
        "v_mfma_f32_16x16x32_bf16 v[102:105], a[128:131], v[154:157], 0\n\t" \
        "ds_read_b64_tr_b16 a[62:63], v7 offset:52320\n\t" \
        "ds_read_b64_tr_b16 a[64:65], v6 offset:49280\n\t" \
        "v_cvt_pk_bf16_f32 v85, v116, v117\n\t" \
        "s_waitcnt lgkmcnt(12)\n\t" \
        "v_mfma_f32_16x16x32_bf16 v[106:109], a[132:135], v[154:157], 0\n\t" \
        "ds_read_b64_tr_b16 a[66:67], v6 offset:52352\n\t" \
        "v_cvt_pk_bf16_f32 v86, v118, v119\n\t" \
        "v_mfma_f32_16x16x32_bf16 v[110:113], a[136:139], v[158:161], 0\n\t" \
        "ds_read_b64_tr_b16 a[68:69], v7 offset:49312\n\t" \
        "ds_read_b64_tr_b16 a[70:71], v7 offset:52384\n\t" \
        "s_waitcnt lgkmcnt(12)\n\t" \
        "v_mfma_f32_16x16x32_bf16 v[114:117], a[140:143], v[158:161], 0\n\t" \
        "ds_read_b64_tr_b16 a[96:97], v142 offset:0\n\t" \
        "v_cvt_pk_bf16_f32 v87, v120, v121\n\t" \
        "s_waitcnt lgkmcnt(12)\n\t" \
        "v_mfma_f32_16x16x32_bf16 v[30:33], a[48:51], v[78:81], v[30:33]\n\t" \
        "ds_read_b64_tr_b16 a[98:99], v143 offset:0\n\t" \
        "s_waitcnt lgkmcnt(11)\n\t" \
        "v_and_b32 v162, v152, v25\n\t" \
        "v_mfma_f32_16x16x32_bf16 v[54:57], a[48:51], v[82:85], v[54:57]\n\t" \
        "ds_read_b64_tr_b16 a[100:101], v146 offset:0\n\t" \
        "ds_read_b64_tr_b16 a[102:103], v147 offset:0\n\t" \
        "s_waitcnt lgkmcnt(12)\n\t" \
        "v_mfma_f32_16x16x32_bf16 v[34:37], a[52:55], v[78:81], v[34:37]\n\t" \
        "ds_read_b64_tr_b16 a[104:105], v144 offset:0\n\t" \
        "v_and_b32 v163, v152, v26\n\t" \
        "v_mfma_f32_16x16x32_bf16 v[58:61], a[52:55], v[82:85], v[58:61]\n\t" \
        "ds_read_b64_tr_b16 a[106:107], v145 offset:0\n\t" \
        "ds_read_b64_tr_b16 a[108:109], v148 offset:0\n\t" \
        "s_waitcnt lgkmcnt(13)\n\t" \
        "v_mfma_f32_16x16x32_bf16 v[38:41], a[56:59], v[78:81], v[38:41]\n\t" \
        "ds_read_b64_tr_b16 a[110:111], v149 offset:0\n\t" \
        "v_and_b32 v164, v152, v27\n\t" \
        "v_mfma_f32_16x16x32_bf16 v[62:65], a[56:59], v[82:85], v[62:65]\n\t" \
        "ds_read_b64_tr_b16 a[72:73], v6 offset:55296\n\t" \
        "v_and_b32 v165, v152, v28\n\t" \
        "s_waitcnt lgkmcnt(13)\n\t" \
        "v_mfma_f32_16x16x32_bf16 v[42:45], a[60:63], v[78:81], v[42:45]\n\t" \
        "ds_read_b64_tr_b16 a[74:75], v6 offset:58368\n\t" \
        "ds_read_b64_tr_b16 a[76:77], v7 offset:55328\n\t" \
        "v_mfma_f32_16x16x32_bf16 v[66:69], a[60:63], v[82:85], v[66:69]\n\t" \
        "ds_read_b64_tr_b16 a[78:79], v7 offset:58400\n\t" \
        "v_and_b32 v166, v153, v25\n\t" \
        "s_waitcnt lgkmcnt(14)\n\t" \
        "v_mfma_f32_16x16x32_bf16 v[46:49], a[64:67], v[78:81], v[46:49]\n\t" \
        "ds_read_b64_tr_b16 a[80:81], v6 offset:55360\n\t" \
        "ds_read_b64_tr_b16 a[82:83], v6 offset:58432\n\t" \
        "v_cvt_pk_bf16_f32 v88, v122, v123\n\t" \
        "v_and_b32 v167, v153, v26\n\t" \
        "v_mfma_f32_16x16x32_bf16 v[70:73], a[64:67], v[82:85], v[70:73]\n\t" \
        "ds_read_b64_tr_b16 a[84:85], v7 offset:55392\n\t" \
        "v_cvt_pk_bf16_f32 v89, v124, v125\n\t" \
        "v_cvt_pk_bf16_f32 v90, v126, v127\n\t" \
        "s_waitcnt lgkmcnt(14)\n\t" \
        "v_mfma_f32_16x16x32_bf16 v[50:53], a[68:71], v[78:81], v[50:53]\n\t" \
        "ds_read_b64_tr_b16 a[86:87], v7 offset:58464\n\t" \
        "ds_read_b64_tr_b16 a[88:89], v6 offset:55424\n\t" \
        "v_and_b32 v168, v153, v27\n\t" \
        "v_mfma_f32_16x16x32_bf16 v[74:77], a[68:71], v[82:85], v[74:77]\n\t" \
        "ds_read_b64_tr_b16 a[90:91], v6 offset:58496\n\t" \
        "v_cvt_pk_bf16_f32 v91, v128, v129\n\t" \
        "v_cvt_pk_bf16_f32 v92, v130, v131\n\t" \
        "s_waitcnt lgkmcnt(14)\n\t" \
        "v_mfma_f32_16x16x32_bf16 v[118:121], a[96:99], v[162:165], 0\n\t" \
        "s_waitcnt vmcnt(6)\n\t" \
        "s_barrier\n\t" \
        "ds_read_b64_tr_b16 a[92:93], v7 offset:55456\n\t" \
        "v_and_b32 v169, v153, v28\n\t" \
        "v_cvt_pk_bf16_f32 v93, v132, v133\n\t" \
        "v_mfma_f32_16x16x32_bf16 v[122:125], a[100:103], v[162:165], 0\n\t" \
        "ds_read_b64_tr_b16 a[94:95], v7 offset:58528\n\t" \
        "ds_read_b64_tr_b16 a[112:113], v142 offset:64\n\t" \
        "s_waitcnt lgkmcnt(14)\n\t" \
        "v_mfma_f32_16x16x32_bf16 v[126:129], a[104:107], v[166:169], 0\n\t" \
        "ds_read_b64_tr_b16 a[114:115], v143 offset:64\n\t" \
        "v_cvt_pk_bf16_f32 v94, v102, v103\n\t" \
        "s_waitcnt lgkmcnt(14)\n\t" \
        "v_mfma_f32_16x16x32_bf16 v[130:133], a[108:111], v[166:169], 0\n\t" \
        "s_add_u32 m0, s39, 36864\n\t" \
        "s_add_u32 s57, s40, 159744\n\t" \
        "buffer_load_dwordx4 v3, s[24:27], s57 offen offset:0 lds\n\t" \
        "s_waitcnt lgkmcnt(12)\n\t" \
        "v_mfma_f32_16x16x32_bf16 v[30:33], a[72:75], v[86:89], v[30:33]\n\t" \
        "ds_read_b64_tr_b16 a[116:117], v146 offset:64\n\t" \
        "ds_read_b64_tr_b16 a[118:119], v147 offset:64\n\t" \
        "v_mfma_f32_16x16x32_bf16 v[54:57], a[72:75], v[90:93], v[54:57]\n\t" \
        "ds_read_b64_tr_b16 a[120:121], v144 offset:64\n\t" \
        "v_cvt_pk_bf16_f32 v95, v104, v105\n\t" \
        "s_waitcnt lgkmcnt(13)\n\t" \
        "v_mfma_f32_16x16x32_bf16 v[34:37], a[76:79], v[86:89], v[34:37]\n\t" \
        "ds_read_b64_tr_b16 a[122:123], v145 offset:64\n\t" \
        "v_cvt_pk_bf16_f32 v96, v106, v107\n\t" \
        "v_mfma_f32_16x16x32_bf16 v[58:61], a[76:79], v[90:93], v[58:61]\n\t" \
        "ds_read_b64_tr_b16 a[124:125], v148 offset:64\n\t" \
        "ds_read_b64_tr_b16 a[126:127], v149 offset:64\n\t" \
        "s_waitcnt lgkmcnt(13)\n\t" \
        "v_mfma_f32_16x16x32_bf16 v[38:41], a[80:83], v[86:89], v[38:41]\n\t" \
        "ds_read_b64_tr_b16 a[0:1], v6 offset:0\n\t" \
        "v_cvt_pk_bf16_f32 v97, v108, v109\n\t" \
        "v_mfma_f32_16x16x32_bf16 v[62:65], a[80:83], v[90:93], v[62:65]\n\t" \
        "buffer_load_dwordx4 v4, s[24:27], s57 offen offset:1024 lds\n\t" \
        "s_waitcnt lgkmcnt(11)\n\t" \
        "v_mfma_f32_16x16x32_bf16 v[42:45], a[84:87], v[86:89], v[42:45]\n\t" \
        "ds_read_b64_tr_b16 a[2:3], v6 offset:3072\n\t" \
        "ds_read_b128 v[170:173], v9 offset:1024\n\t" \
        "v_mfma_f32_16x16x32_bf16 v[66:69], a[84:87], v[90:93], v[66:69]\n\t" \
        "ds_read_b64_tr_b16 a[4:5], v7 offset:32\n\t" \
        "v_cvt_pk_bf16_f32 v98, v110, v111\n\t" \
        "v_mfma_f32_16x16x32_bf16 v[46:49], a[88:91], v[86:89], v[46:49]\n\t" \
        "ds_read_b64_tr_b16 a[6:7], v7 offset:3104\n\t" \
        "v_cvt_pk_bf16_f32 v99, v112, v113\n\t" \
        "v_mfma_f32_16x16x32_bf16 v[70:73], a[88:91], v[90:93], v[70:73]\n\t" \
        "ds_read_b64_tr_b16 a[8:9], v6 offset:64\n\t" \
        "ds_read_b64_tr_b16 a[10:11], v6 offset:3136\n\t" \
        "s_waitcnt lgkmcnt(13)\n\t" \
        "v_mfma_f32_16x16x32_bf16 v[50:53], a[92:95], v[86:89], v[50:53]\n\t" \
        "ds_read_b64_tr_b16 a[12:13], v7 offset:96\n\t" \
        "v_cvt_pk_bf16_f32 v100, v114, v115\n\t" \
        "v_mfma_f32_16x16x32_bf16 v[74:77], a[92:95], v[90:93], v[74:77]\n\t" \
        "buffer_load_dwordx4 v5, s[24:27], s57 offen offset:2048 lds\n\t" \
        "v_mfma_f32_16x16x32_bf16 v[102:105], a[112:115], v[162:165], 0\n\t" \
        "ds_read_b64_tr_b16 a[14:15], v7 offset:3168\n\t" \
        "ds_read_b64_tr_b16 a[16:17], v6 offset:128\n\t" \
        "v_cvt_pk_bf16_f32 v101, v116, v117\n\t" \
        "s_waitcnt lgkmcnt(12)\n\t" \
        "v_mfma_f32_16x16x32_bf16 v[106:109], a[116:119], v[162:165], 0\n\t" \
        "ds_read_b64_tr_b16 a[18:19], v6 offset:3200\n\t" \
        "v_cvt_pk_bf16_f32 v78, v118, v119\n\t" \
        "v_mfma_f32_16x16x32_bf16 v[110:113], a[120:123], v[166:169], 0\n\t" \
        "ds_read_b64_tr_b16 a[20:21], v7 offset:160\n\t" \
        "v_cvt_pk_bf16_f32 v79, v120, v121\n\t" \
        "s_waitcnt lgkmcnt(11)\n\t" \
        "v_mfma_f32_16x16x32_bf16 v[114:117], a[124:127], v[166:169], 0\n\t" \
        "ds_read_b64_tr_b16 a[22:23], v7 offset:3232\n\t" \
        "ds_read_b64_tr_b16 a[128:129], v142 offset:128\n\t" \
        "s_waitcnt lgkmcnt(12)\n\t" \
        "v_mfma_f32_16x16x32_bf16 v[30:33], a[0:3], v[94:97], v[30:33]\n\t" \
        "ds_read_b64_tr_b16 a[130:131], v143 offset:128\n\t" \
        "s_waitcnt lgkmcnt(12)\n\t" \
        "v_xor_b32 v134, v170, v11\n\t" \
        "v_mfma_f32_16x16x32_bf16 v[54:57], a[0:3], v[98:101], v[54:57]\n\t" \
        "ds_read_b64_tr_b16 a[132:133], v146 offset:128\n\t" \
        "ds_read_b64_tr_b16 a[134:135], v147 offset:128\n\t" \
        "s_waitcnt lgkmcnt(12)\n\t" \
        "v_mfma_f32_16x16x32_bf16 v[34:37], a[4:7], v[94:97], v[34:37]\n\t" \
        "ds_read_b64_tr_b16 a[136:137], v144 offset:128\n\t" \
        "v_xor_b32 v135, v171, v11\n\t" \
        "v_mfma_f32_16x16x32_bf16 v[58:61], a[4:7], v[98:101], v[58:61]\n\t" \
        "ds_read_b64_tr_b16 a[138:139], v145 offset:128\n\t" \
        "v_xor_b32 v136, v172, v11\n\t" \
        "s_waitcnt lgkmcnt(12)\n\t" \
        "v_mfma_f32_16x16x32_bf16 v[38:41], a[8:11], v[94:97], v[38:41]\n\t" \
        "ds_read_b64_tr_b16 a[140:141], v148 offset:128\n\t" \
        "ds_read_b64_tr_b16 a[142:143], v149 offset:128\n\t" \
        "v_mfma_f32_16x16x32_bf16 v[62:65], a[8:11], v[98:101], v[62:65]\n\t" \
        "ds_read_b64_tr_b16 a[24:25], v6 offset:6144\n\t" \
        "v_xor_b32 v138, v170, v12\n\t" \
        "s_waitcnt lgkmcnt(13)\n\t" \
        "v_mfma_f32_16x16x32_bf16 v[42:45], a[12:15], v[94:97], v[42:45]\n\t" \
        "ds_read_b64_tr_b16 a[26:27], v6 offset:9216\n\t" \
        "v_xor_b32 v137, v173, v11\n\t" \
        "v_mfma_f32_16x16x32_bf16 v[66:69], a[12:15], v[98:101], v[66:69]\n\t" \
        "ds_read_b64_tr_b16 a[28:29], v7 offset:6176\n\t" \
        "ds_read_b64 v[150:151], v10 offset:1024\n\t" \
        "v_xor_b32 v139, v171, v12\n\t" \
        "s_waitcnt lgkmcnt(14)\n\t" \
        "v_mfma_f32_16x16x32_bf16 v[46:49], a[16:19], v[94:97], v[46:49]\n\t" \
        "ds_read_b64_tr_b16 a[30:31], v7 offset:9248\n\t" \
        "v_xor_b32 v140, v172, v12\n\t" \
        "v_xor_b32 v141, v173, v12\n\t" \
        "v_mfma_f32_16x16x32_bf16 v[70:73], a[16:19], v[98:101], v[70:73]\n\t" \
        "ds_read_b64_tr_b16 a[32:33], v6 offset:6208\n\t" \
        "v_cvt_pk_bf16_f32 v80, v122, v123\n\t" \
        "s_waitcnt lgkmcnt(13)\n\t" \
        "v_mfma_f32_16x16x32_bf16 v[50:53], a[20:23], v[94:97], v[50:53]\n\t" \
        "ds_read_b64_tr_b16 a[34:35], v6 offset:9280\n\t" \
        "ds_read_b64_tr_b16 a[36:37], v7 offset:6240\n\t" \
        "v_cvt_pk_bf16_f32 v81, v124, v125\n\t" \
        "v_cvt_pk_bf16_f32 v82, v126, v127\n\t" \
        "v_mfma_f32_16x16x32_bf16 v[74:77], a[20:23], v[98:101], v[74:77]\n\t" \
        "ds_read_b64_tr_b16 a[38:39], v7 offset:9312\n\t" \
        "v_cvt_pk_bf16_f32 v83, v128, v129\n\t" \
        "v_cvt_pk_bf16_f32 v84, v130, v131\n\t" \
        "s_waitcnt lgkmcnt(13)\n\t" \
        "v_mfma_f32_16x16x32_bf16 v[118:121], a[128:131], v[162:165], 0\n\t" \
        "s_waitcnt vmcnt(6)\n\t" \
        "s_barrier\n\t" \
        "ds_read_b64_tr_b16 a[40:41], v6 offset:6272\n\t" \
        "ds_read_b64_tr_b16 a[42:43], v6 offset:9344\n\t" \
        "v_cvt_pk_bf16_f32 v85, v132, v133\n\t" \
        "v_mfma_f32_16x16x32_bf16 v[122:125], a[132:135], v[162:165], 0\n\t" \
        "ds_read_b64_tr_b16 a[44:45], v7 offset:6304\n\t" \
        "v_cvt_pk_bf16_f32 v86, v102, v103\n\t" \
        "s_waitcnt lgkmcnt(14)\n\t" \
        "v_mfma_f32_16x16x32_bf16 v[126:129], a[136:139], v[166:169], 0\n\t" \
        "ds_read_b64_tr_b16 a[46:47], v7 offset:9376\n\t" \
        "v_cvt_pk_bf16_f32 v87, v104, v105\n\t" \
        "s_waitcnt lgkmcnt(13)\n\t" \
        "v_mfma_f32_16x16x32_bf16 v[130:133], a[140:143], v[166:169], 0\n\t" \
        "ds_read_b64_tr_b16 a[96:97], v134 offset:0\n\t" \
        "ds_read_b64_tr_b16 a[98:99], v135 offset:0\n\t" \
        "s_waitcnt lgkmcnt(13)\n\t" \
        "v_mfma_f32_16x16x32_bf16 v[30:33], a[24:27], v[78:81], v[30:33]\n\t" \
        "ds_read_b64_tr_b16 a[100:101], v138 offset:0\n\t" \
        "s_waitcnt lgkmcnt(12)\n\t" \
        "v_and_b32 v154, v150, v25\n\t" \
        "v_mfma_f32_16x16x32_bf16 v[54:57], a[24:27], v[82:85], v[54:57]\n\t" \
        "ds_read_b64_tr_b16 a[102:103], v139 offset:0\n\t" \
        "ds_read_b64_tr_b16 a[104:105], v136 offset:0\n\t" \
        "s_waitcnt lgkmcnt(13)\n\t" \
        "v_mfma_f32_16x16x32_bf16 v[34:37], a[28:31], v[78:81], v[34:37]\n\t" \
        "ds_read_b64_tr_b16 a[106:107], v137 offset:0\n\t" \
        "v_and_b32 v155, v150, v26\n\t" \
        "v_mfma_f32_16x16x32_bf16 v[58:61], a[28:31], v[82:85], v[58:61]\n\t" \
        "ds_read_b64_tr_b16 a[108:109], v140 offset:0\n\t" \
        "v_and_b32 v156, v150, v27\n\t" \
        "s_waitcnt lgkmcnt(13)\n\t" \
        "v_mfma_f32_16x16x32_bf16 v[38:41], a[32:35], v[78:81], v[38:41]\n\t" \
        "ds_read_b64_tr_b16 a[110:111], v141 offset:0\n\t" \
        "ds_read_b64_tr_b16 a[48:49], v6 offset:12288\n\t" \
        "v_mfma_f32_16x16x32_bf16 v[62:65], a[32:35], v[82:85], v[62:65]\n\t" \
        "ds_read_b64_tr_b16 a[50:51], v6 offset:15360\n\t" \
        "v_and_b32 v157, v150, v28\n\t" \
        "s_waitcnt lgkmcnt(14)\n\t" \
        "v_mfma_f32_16x16x32_bf16 v[42:45], a[36:39], v[78:81], v[42:45]\n\t" \
        "ds_read_b64_tr_b16 a[52:53], v7 offset:12320\n\t" \
        "ds_read_b64_tr_b16 a[54:55], v7 offset:15392\n\t" \
        "v_mfma_f32_16x16x32_bf16 v[66:69], a[36:39], v[82:85], v[66:69]\n\t" \
        "ds_read_b64_tr_b16 a[56:57], v6 offset:12352\n\t" \
        "v_and_b32 v158, v151, v25\n\t" \
        "s_waitcnt lgkmcnt(14)\n\t" \
        "v_mfma_f32_16x16x32_bf16 v[46:49], a[40:43], v[78:81], v[46:49]\n\t" \
        "ds_read_b64_tr_b16 a[58:59], v6 offset:15424\n\t" \
        "ds_read_b64_tr_b16 a[60:61], v7 offset:12384\n\t" \
        "v_cvt_pk_bf16_f32 v88, v106, v107\n\t" \
        "v_and_b32 v159, v151, v26\n\t" \
        "v_mfma_f32_16x16x32_bf16 v[70:73], a[40:43], v[82:85], v[70:73]\n\t" \
        "ds_read_b64_tr_b16 a[62:63], v7 offset:15456\n\t" \
        "v_cvt_pk_bf16_f32 v89, v108, v109\n\t" \
        "v_cvt_pk_bf16_f32 v90, v110, v111\n\t" \
        "s_waitcnt lgkmcnt(14)\n\t" \
        "v_mfma_f32_16x16x32_bf16 v[50:53], a[44:47], v[78:81], v[50:53]\n\t" \
        "ds_read_b64_tr_b16 a[64:65], v6 offset:12416\n\t" \
        "ds_read_b64_tr_b16 a[66:67], v6 offset:15488\n\t" \
        "v_and_b32 v160, v151, v27\n\t" \
        "v_mfma_f32_16x16x32_bf16 v[74:77], a[44:47], v[82:85], v[74:77]\n\t" \
        "ds_read_b64_tr_b16 a[68:69], v7 offset:12448\n\t" \
        "v_cvt_pk_bf16_f32 v91, v112, v113\n\t" \
        "v_cvt_pk_bf16_f32 v92, v114, v115\n\t" \
        "v_mfma_f32_16x16x32_bf16 v[102:105], a[96:99], v[154:157], 0\n\t" \
        "ds_read_b64_tr_b16 a[70:71], v7 offset:15520\n\t" \
        "ds_read_b64_tr_b16 a[112:113], v134 offset:64\n\t" \
        "v_and_b32 v161, v151, v28\n\t" \
        "v_cvt_pk_bf16_f32 v93, v116, v117\n\t" \
        "s_waitcnt lgkmcnt(15)\n\t" \
        "v_mfma_f32_16x16x32_bf16 v[106:109], a[100:103], v[154:157], 0\n\t" \
        "ds_read_b64_tr_b16 a[114:115], v135 offset:64\n\t" \
        "v_cvt_pk_bf16_f32 v94, v118, v119\n\t" \
        "v_mfma_f32_16x16x32_bf16 v[110:113], a[104:107], v[158:161], 0\n\t" \
        "ds_read_b64_tr_b16 a[116:117], v138 offset:64\n\t" \
        "ds_read_b64_tr_b16 a[118:119], v139 offset:64\n\t" \
        "s_waitcnt lgkmcnt(15)\n\t" \
        "v_mfma_f32_16x16x32_bf16 v[114:117], a[108:111], v[158:161], 0\n\t" \
        "ds_read_b64_tr_b16 a[120:121], v136 offset:64\n\t" \
        "v_cvt_pk_bf16_f32 v95, v120, v121\n\t" \
        "s_waitcnt lgkmcnt(15)\n\t" \
        "v_mfma_f32_16x16x32_bf16 v[30:33], a[48:51], v[86:89], v[30:33]\n\t" \
        "ds_read_b64_tr_b16 a[122:123], v137 offset:64\n\t" \
        "ds_read_b64_tr_b16 a[124:125], v140 offset:64\n\t" \
        "v_mfma_f32_16x16x32_bf16 v[54:57], a[48:51], v[90:93], v[54:57]\n\t" \
        "ds_read_b64_tr_b16 a[126:127], v141 offset:64\n\t" \
        "v_cvt_pk_bf16_f32 v96, v122, v123\n\t" \
        "s_waitcnt lgkmcnt(15)\n\t" \
        "v_mfma_f32_16x16x32_bf16 v[34:37], a[52:55], v[86:89], v[34:37]\n\t" \
        "ds_read_b64_tr_b16 a[72:73], v6 offset:18432\n\t" \
        "ds_read_b64_tr_b16 a[74:75], v6 offset:21504\n\t" \
        "v_mfma_f32_16x16x32_bf16 v[58:61], a[52:55], v[90:93], v[58:61]\n\t" \
        "ds_read_b64_tr_b16 a[76:77], v7 offset:18464\n\t" \
        "v_cvt_pk_bf16_f32 v97, v124, v125\n\t" \
        "s_waitcnt lgkmcnt(15)\n\t" \
        "v_mfma_f32_16x16x32_bf16 v[38:41], a[56:59], v[86:89], v[38:41]\n\t" \
        "ds_read_b64_tr_b16 a[78:79], v7 offset:21536\n\t" \
        "ds_read_b64_tr_b16 a[80:81], v6 offset:18496\n\t" \
        "v_mfma_f32_16x16x32_bf16 v[62:65], a[56:59], v[90:93], v[62:65]\n\t" \
        "ds_read_b64_tr_b16 a[82:83], v6 offset:21568\n\t" \
        "v_cvt_pk_bf16_f32 v98, v126, v127\n\t" \
        "v_mfma_f32_16x16x32_bf16 v[42:45], a[60:63], v[86:89], v[42:45]\n\t" \
        "ds_read_b64_tr_b16 a[84:85], v7 offset:18528\n\t" \
        "ds_read_b64_tr_b16 a[86:87], v7 offset:21600\n\t" \
        "v_mfma_f32_16x16x32_bf16 v[66:69], a[60:63], v[90:93], v[66:69]\n\t" \
        "ds_read_b64_tr_b16 a[88:89], v6 offset:18560\n\t" \
        "v_cvt_pk_bf16_f32 v99, v128, v129\n\t" \
        "s_waitcnt lgkmcnt(15)\n\t" \
        "v_mfma_f32_16x16x32_bf16 v[46:49], a[64:67], v[86:89], v[46:49]\n\t" \
        "ds_read_b64_tr_b16 a[90:91], v6 offset:21632\n\t" \
        "ds_read_b64_tr_b16 a[92:93], v7 offset:18592\n\t" \
        "v_mfma_f32_16x16x32_bf16 v[70:73], a[64:67], v[90:93], v[70:73]\n\t" \
        "ds_read_b64_tr_b16 a[94:95], v7 offset:21664\n\t" \
        "v_cvt_pk_bf16_f32 v100, v130, v131\n\t" \
        "v_mfma_f32_16x16x32_bf16 v[50:53], a[68:71], v[86:89], v[50:53]\n\t" \
        "ds_read_b64_tr_b16 a[128:129], v134 offset:128\n\t" \
        "v_cvt_pk_bf16_f32 v101, v132, v133\n\t" \
        "v_mfma_f32_16x16x32_bf16 v[74:77], a[68:71], v[90:93], v[74:77]\n\t" \
        "ds_read_b64_tr_b16 a[130:131], v135 offset:128\n\t" \
        "ds_read_b64_tr_b16 a[132:133], v138 offset:128\n\t" \
        "v_mfma_f32_16x16x32_bf16 v[118:121], a[112:115], v[154:157], 0\n\t" \
        "s_waitcnt vmcnt(3)\n\t" \
        "s_barrier\n\t" \
        "ds_read_b64_tr_b16 a[134:135], v139 offset:128\n\t" \
        "v_cvt_pk_bf16_f32 v78, v102, v103\n\t" \
        "s_waitcnt lgkmcnt(14)\n\t" \
        "v_mfma_f32_16x16x32_bf16 v[122:125], a[116:119], v[154:157], 0\n\t" \
        "ds_read_b64_tr_b16 a[136:137], v136 offset:128\n\t" \
        "ds_read_b64_tr_b16 a[138:139], v137 offset:128\n\t" \
        "v_mfma_f32_16x16x32_bf16 v[126:129], a[120:123], v[158:161], 0\n\t" \
        "ds_read_b64_tr_b16 a[140:141], v140 offset:128\n\t" \
        "v_cvt_pk_bf16_f32 v79, v104, v105\n\t" \
        "v_mfma_f32_16x16x32_bf16 v[130:133], a[124:127], v[158:161], 0\n\t" \
        "ds_read_b64_tr_b16 a[142:143], v141 offset:128\n\t" \
        "ds_read_b64_tr_b16 a[0:1], v6 offset:24576\n\t" \
        "v_mfma_f32_16x16x32_bf16 v[30:33], a[72:75], v[94:97], v[30:33]\n\t" \
        "ds_read_b64_tr_b16 a[2:3], v6 offset:27648\n\t" \
        "v_cvt_pk_bf16_f32 v80, v106, v107\n\t" \
        "v_mfma_f32_16x16x32_bf16 v[54:57], a[72:75], v[98:101], v[54:57]\n\t" \
        "ds_read_b64_tr_b16 a[4:5], v7 offset:24608\n\t" \
        "ds_read_b64_tr_b16 a[6:7], v7 offset:27680\n\t" \
        "s_waitcnt lgkmcnt(15)\n\t" \
        "v_mfma_f32_16x16x32_bf16 v[34:37], a[76:79], v[94:97], v[34:37]\n\t" \
        "ds_read_b64_tr_b16 a[8:9], v6 offset:24640\n\t" \
        "v_cvt_pk_bf16_f32 v81, v108, v109\n\t" \
        "v_mfma_f32_16x16x32_bf16 v[58:61], a[76:79], v[98:101], v[58:61]\n\t" \
        "ds_read_b64_tr_b16 a[10:11], v6 offset:27712\n\t" \
        "ds_read_b64_tr_b16 a[12:13], v7 offset:24672\n\t" \
        "v_mfma_f32_16x16x32_bf16 v[38:41], a[80:83], v[94:97], v[38:41]\n\t" \
        "ds_read_b64_tr_b16 a[14:15], v7 offset:27744\n\t" \
        "v_cvt_pk_bf16_f32 v82, v110, v111\n\t" \
        "v_mfma_f32_16x16x32_bf16 v[62:65], a[80:83], v[98:101], v[62:65]\n\t" \
        "ds_read_b64_tr_b16 a[16:17], v6 offset:24704\n\t" \
        "ds_read_b64_tr_b16 a[18:19], v6 offset:27776\n\t" \
        "v_mfma_f32_16x16x32_bf16 v[42:45], a[84:87], v[94:97], v[42:45]\n\t" \
        "ds_read_b64_tr_b16 a[20:21], v7 offset:24736\n\t" \
        "v_cvt_pk_bf16_f32 v83, v112, v113\n\t" \
        "v_mfma_f32_16x16x32_bf16 v[66:69], a[84:87], v[98:101], v[66:69]\n\t" \
        "ds_read_b64_tr_b16 a[22:23], v7 offset:27808\n\t" \
        "ds_read_b64_tr_b16 a[24:25], v6 offset:30720\n\t" \
        "s_waitcnt lgkmcnt(15)\n\t" \
        "v_mfma_f32_16x16x32_bf16 v[46:49], a[88:91], v[94:97], v[46:49]\n\t" \
        "s_waitcnt vmcnt(0)\n\t" \
        "s_barrier\n\t" \
        "ds_read_b64_tr_b16 a[26:27], v6 offset:33792\n\t" \
        "v_cvt_pk_bf16_f32 v84, v114, v115\n\t" \
        "v_mfma_f32_16x16x32_bf16 v[70:73], a[88:91], v[98:101], v[70:73]\n\t" \
        "ds_read_b64_tr_b16 a[28:29], v7 offset:30752\n\t" \
        "ds_read_b64_tr_b16 a[30:31], v7 offset:33824\n\t" \
        "v_mfma_f32_16x16x32_bf16 v[50:53], a[92:95], v[94:97], v[50:53]\n\t" \
        "ds_read_b64_tr_b16 a[32:33], v6 offset:30784\n\t" \
        "v_cvt_pk_bf16_f32 v85, v116, v117\n\t" \
        "v_mfma_f32_16x16x32_bf16 v[74:77], a[92:95], v[98:101], v[74:77]\n\t" \
        "ds_read_b64_tr_b16 a[34:35], v6 offset:33856\n\t" \
        "ds_read_b64_tr_b16 a[36:37], v7 offset:30816\n\t" \
        "v_mfma_f32_16x16x32_bf16 v[102:105], a[128:131], v[154:157], 0\n\t" \
        "ds_read_b64_tr_b16 a[38:39], v7 offset:33888\n\t" \
        "v_cvt_pk_bf16_f32 v86, v118, v119\n\t" \
        "v_mfma_f32_16x16x32_bf16 v[106:109], a[132:135], v[154:157], 0\n\t" \
        "ds_read_b64_tr_b16 a[40:41], v6 offset:30848\n\t" \
        "ds_read_b64_tr_b16 a[42:43], v6 offset:33920\n\t" \
        "v_mfma_f32_16x16x32_bf16 v[110:113], a[136:139], v[158:161], 0\n\t" \
        "ds_read_b64_tr_b16 a[44:45], v7 offset:30880\n\t" \
        "v_cvt_pk_bf16_f32 v88, v122, v123\n\t" \
        "s_waitcnt lgkmcnt(15)\n\t" \
        "v_mfma_f32_16x16x32_bf16 v[114:117], a[140:143], v[158:161], 0\n\t" \
        "ds_read_b64_tr_b16 a[46:47], v7 offset:33952\n\t" \
        "ds_read_b64_tr_b16 a[48:49], v6 offset:36864\n\t" \
        "v_mfma_f32_16x16x32_bf16 v[30:33], a[0:3], v[78:81], v[30:33]\n\t" \
        "ds_read_b64_tr_b16 a[50:51], v6 offset:39936\n\t" \
        "v_cvt_pk_bf16_f32 v87, v120, v121\n\t" \
        "v_mfma_f32_16x16x32_bf16 v[34:37], a[4:7], v[78:81], v[34:37]\n\t" \
        "ds_read_b64_tr_b16 a[52:53], v7 offset:36896\n\t" \
        "ds_read_b64_tr_b16 a[54:55], v7 offset:39968\n\t" \
        "v_mfma_f32_16x16x32_bf16 v[38:41], a[8:11], v[78:81], v[38:41]\n\t" \
        "ds_read_b64_tr_b16 a[56:57], v6 offset:36928\n\t" \
        "v_cvt_pk_bf16_f32 v89, v124, v125\n\t" \
        "v_mfma_f32_16x16x32_bf16 v[42:45], a[12:15], v[78:81], v[42:45]\n\t" \
        "ds_read_b64_tr_b16 a[58:59], v6 offset:40000\n\t" \
        "v_cvt_pk_bf16_f32 v94, v102, v103\n\t" \
        "s_waitcnt lgkmcnt(14)\n\t" \
        "v_mfma_f32_16x16x32_bf16 v[46:49], a[16:19], v[78:81], v[46:49]\n\t" \
        "ds_read_b64_tr_b16 a[60:61], v7 offset:36960\n\t" \
        "ds_read_b64_tr_b16 a[62:63], v7 offset:40032\n\t" \
        "v_mfma_f32_16x16x32_bf16 v[50:53], a[20:23], v[78:81], v[50:53]\n\t" \
        "ds_read_b64_tr_b16 a[64:65], v6 offset:36992\n\t" \
        "ds_read_b64_tr_b16 a[66:67], v6 offset:40064\n\t" \
        "v_mfma_f32_16x16x32_bf16 v[30:33], a[24:27], v[86:89], v[30:33]\n\t" \
        "ds_read_b64_tr_b16 a[68:69], v7 offset:37024\n\t" \
        "v_cvt_pk_bf16_f32 v96, v106, v107\n\t" \
        "v_mfma_f32_16x16x32_bf16 v[34:37], a[28:31], v[86:89], v[34:37]\n\t" \
        "ds_read_b64_tr_b16 a[70:71], v7 offset:40096\n\t" \
        "v_cvt_pk_bf16_f32 v95, v104, v105\n\t" \
        "s_waitcnt lgkmcnt(14)\n\t" \
        "v_mfma_f32_16x16x32_bf16 v[38:41], a[32:35], v[86:89], v[38:41]\n\t" \
        "v_cvt_pk_bf16_f32 v97, v108, v109\n\t" \
        "v_cvt_pk_bf16_f32 v90, v126, v127\n\t" \
        "v_mfma_f32_16x16x32_bf16 v[42:45], a[36:39], v[86:89], v[42:45]\n\t" \
        "v_cvt_pk_bf16_f32 v92, v130, v131\n\t" \
        "v_cvt_pk_bf16_f32 v91, v128, v129\n\t" \
        "v_mfma_f32_16x16x32_bf16 v[46:49], a[40:43], v[86:89], v[46:49]\n\t" \
        "v_cvt_pk_bf16_f32 v93, v132, v133\n\t" \
        "v_cvt_pk_bf16_f32 v98, v110, v111\n\t" \
        "s_waitcnt lgkmcnt(10)\n\t" \
        "v_mfma_f32_16x16x32_bf16 v[50:53], a[44:47], v[86:89], v[50:53]\n\t" \
        "v_cvt_pk_bf16_f32 v100, v114, v115\n\t" \
        "v_cvt_pk_bf16_f32 v99, v112, v113\n\t" \
        "v_mfma_f32_16x16x32_bf16 v[30:33], a[48:51], v[94:97], v[30:33]\n\t" \
        "v_cvt_pk_bf16_f32 v101, v116, v117\n\t" \
        "s_waitcnt lgkmcnt(7)\n\t" \
        "v_mfma_f32_16x16x32_bf16 v[34:37], a[52:55], v[94:97], v[34:37]\n\t" \
        "s_waitcnt lgkmcnt(6)\n\t" \
        "v_mfma_f32_16x16x32_bf16 v[38:41], a[56:59], v[94:97], v[38:41]\n\t" \
        "s_waitcnt lgkmcnt(4)\n\t" \
        "v_mfma_f32_16x16x32_bf16 v[42:45], a[60:63], v[94:97], v[42:45]\n\t" \
        "s_waitcnt lgkmcnt(2)\n\t" \
        "v_mfma_f32_16x16x32_bf16 v[46:49], a[64:67], v[94:97], v[46:49]\n\t" \
        "s_waitcnt lgkmcnt(0)\n\t" \
        "v_mfma_f32_16x16x32_bf16 v[50:53], a[68:71], v[94:97], v[50:53]\n\t" \
        "s_cmp_eq_u32 s58, 3\n\t" \
        "s_cbranch_scc1 .LH4t%=\n\t" \
        "v_cvt_pk_bf16_f32 v30, v30, v31\n\t" \
        "v_mfma_f32_16x16x32_bf16 v[54:57], a[0:3], v[82:85], v[54:57]\n\t" \
        "v_cvt_pk_bf16_f32 v31, v32, v33\n\t" \
        "v_cvt_pk_bf16_f32 v32, v34, v35\n\t" \
        "v_mfma_f32_16x16x32_bf16 v[58:61], a[4:7], v[82:85], v[58:61]\n\t" \
        "v_cvt_pk_bf16_f32 v33, v36, v37\n\t" \
        "v_mfma_f32_16x16x32_bf16 v[62:65], a[8:11], v[82:85], v[62:65]\n\t" \
        "v_permlane16_swap_b32 v30, v32\n\t" \
        "v_mfma_f32_16x16x32_bf16 v[66:69], a[12:15], v[82:85], v[66:69]\n\t" \
        "v_permlane16_swap_b32 v31, v33\n\t" \
        "v_mfma_f32_16x16x32_bf16 v[70:73], a[16:19], v[82:85], v[70:73]\n\t" \
        "v_mfma_f32_16x16x32_bf16 v[74:77], a[20:23], v[82:85], v[74:77]\n\t" \
        "buffer_store_dwordx4 v[30:33], v8, s[28:31], s59 offen offset:0 sc1\n\t" \
        "v_mfma_f32_16x16x32_bf16 v[54:57], a[24:27], v[90:93], v[54:57]\n\t" \
        "v_cvt_pk_bf16_f32 v38, v38, v39\n\t" \
        "v_mfma_f32_16x16x32_bf16 v[58:61], a[28:31], v[90:93], v[58:61]\n\t" \
        "v_cvt_pk_bf16_f32 v39, v40, v41\n\t" \
        "v_cvt_pk_bf16_f32 v40, v42, v43\n\t" \
        "v_mfma_f32_16x16x32_bf16 v[62:65], a[32:35], v[90:93], v[62:65]\n\t" \
        "v_cvt_pk_bf16_f32 v41, v44, v45\n\t" \
        "v_mfma_f32_16x16x32_bf16 v[66:69], a[36:39], v[90:93], v[66:69]\n\t" \
        "v_permlane16_swap_b32 v38, v40\n\t" \
        "v_mfma_f32_16x16x32_bf16 v[70:73], a[40:43], v[90:93], v[70:73]\n\t" \
        "v_permlane16_swap_b32 v39, v41\n\t" \
        "v_mfma_f32_16x16x32_bf16 v[74:77], a[44:47], v[90:93], v[74:77]\n\t" \
        "v_mfma_f32_16x16x32_bf16 v[54:57], a[48:51], v[98:101], v[54:57]\n\t" \
        "buffer_store_dwordx4 v[38:41], v8, s[28:31], s59 offen offset:64 sc1\n\t" \
        "v_mfma_f32_16x16x32_bf16 v[58:61], a[52:55], v[98:101], v[58:61]\n\t" \
        "v_cvt_pk_bf16_f32 v46, v46, v47\n\t" \
        "v_mfma_f32_16x16x32_bf16 v[62:65], a[56:59], v[98:101], v[62:65]\n\t" \
        "v_cvt_pk_bf16_f32 v47, v48, v49\n\t" \
        "v_cvt_pk_bf16_f32 v48, v50, v51\n\t" \
        "v_mfma_f32_16x16x32_bf16 v[66:69], a[60:63], v[98:101], v[66:69]\n\t" \
        "v_cvt_pk_bf16_f32 v49, v52, v53\n\t" \
        "v_mfma_f32_16x16x32_bf16 v[70:73], a[64:67], v[98:101], v[70:73]\n\t" \
        "v_permlane16_swap_b32 v46, v48\n\t" \
        "v_mfma_f32_16x16x32_bf16 v[74:77], a[68:71], v[98:101], v[74:77]\n\t" \
        "v_permlane16_swap_b32 v47, v49\n\t" \
        "buffer_store_dwordx4 v[46:49], v8, s[28:31], s59 offen offset:128 sc1\n\t" \
        "v_cvt_pk_bf16_f32 v54, v54, v55\n\t" \
        "v_cvt_pk_bf16_f32 v55, v56, v57\n\t" \
        "v_cvt_pk_bf16_f32 v56, v58, v59\n\t" \
        "v_cvt_pk_bf16_f32 v57, v60, v61\n\t" \
        "s_nop 0\n\t" \
        "v_permlane16_swap_b32 v54, v56\n\t" \
        "v_permlane16_swap_b32 v55, v57\n\t" \
        "buffer_store_dwordx4 v[54:57], v8, s[28:31], s60 offen offset:0 sc1\n\t" \
        "v_cvt_pk_bf16_f32 v62, v62, v63\n\t" \
        "v_cvt_pk_bf16_f32 v63, v64, v65\n\t" \
        "v_cvt_pk_bf16_f32 v64, v66, v67\n\t" \
        "v_cvt_pk_bf16_f32 v65, v68, v69\n\t" \
        "s_nop 0\n\t" \
        "v_permlane16_swap_b32 v62, v64\n\t" \
        "v_permlane16_swap_b32 v63, v65\n\t" \
        "buffer_store_dwordx4 v[62:65], v8, s[28:31], s60 offen offset:64 sc1\n\t" \
        "v_cvt_pk_bf16_f32 v70, v70, v71\n\t" \
        "v_cvt_pk_bf16_f32 v71, v72, v73\n\t" \
        "v_cvt_pk_bf16_f32 v72, v74, v75\n\t" \
        "v_cvt_pk_bf16_f32 v73, v76, v77\n\t" \
        "s_nop 0\n\t" \
        "v_permlane16_swap_b32 v70, v72\n\t" \
        "v_permlane16_swap_b32 v71, v73\n\t" \
        "buffer_store_dwordx4 v[70:73], v8, s[28:31], s60 offen offset:128 sc1\n\t" \
        "s_endpgm\n\t" \
        ".LH4t%=:\n\t" \
        "v_mfma_f32_16x16x32_bf16 v[54:57], a[0:3], v[82:85], v[54:57]\n\t" \
        "v_mfma_f32_16x16x32_bf16 v[58:61], a[4:7], v[82:85], v[58:61]\n\t" \
        "v_mfma_f32_16x16x32_bf16 v[62:65], a[8:11], v[82:85], v[62:65]\n\t" \
        "v_mfma_f32_16x16x32_bf16 v[66:69], a[12:15], v[82:85], v[66:69]\n\t" \
        "v_mfma_f32_16x16x32_bf16 v[70:73], a[16:19], v[82:85], v[70:73]\n\t" \
        "v_mfma_f32_16x16x32_bf16 v[74:77], a[20:23], v[82:85], v[74:77]\n\t" \
        "v_mfma_f32_16x16x32_bf16 v[54:57], a[24:27], v[90:93], v[54:57]\n\t" \
        "v_mfma_f32_16x16x32_bf16 v[58:61], a[28:31], v[90:93], v[58:61]\n\t" \
        "v_mfma_f32_16x16x32_bf16 v[62:65], a[32:35], v[90:93], v[62:65]\n\t" \
        "v_mfma_f32_16x16x32_bf16 v[66:69], a[36:39], v[90:93], v[66:69]\n\t" \
        "v_mfma_f32_16x16x32_bf16 v[70:73], a[40:43], v[90:93], v[70:73]\n\t" \
        "v_mfma_f32_16x16x32_bf16 v[74:77], a[44:47], v[90:93], v[74:77]\n\t" \
        "v_mfma_f32_16x16x32_bf16 v[54:57], a[48:51], v[98:101], v[54:57]\n\t" \
        "v_mfma_f32_16x16x32_bf16 v[58:61], a[52:55], v[98:101], v[58:61]\n\t" \
        "v_mfma_f32_16x16x32_bf16 v[62:65], a[56:59], v[98:101], v[62:65]\n\t" \
        "v_mfma_f32_16x16x32_bf16 v[66:69], a[60:63], v[98:101], v[66:69]\n\t" \
        "v_mfma_f32_16x16x32_bf16 v[70:73], a[64:67], v[98:101], v[70:73]\n\t" \
        "v_mfma_f32_16x16x32_bf16 v[74:77], a[68:71], v[98:101], v[74:77]\n\t" \
        "s_nop 4\n\t" \
        "v_add_f32 v30, v30, v54\n\t" \
        "v_add_f32 v31, v31, v55\n\t" \
        "v_add_f32 v32, v32, v56\n\t" \
        "v_add_f32 v33, v33, v57\n\t" \
        "v_add_f32 v34, v34, v58\n\t" \
        "v_add_f32 v35, v35, v59\n\t" \
        "v_add_f32 v36, v36, v60\n\t" \
        "v_add_f32 v37, v37, v61\n\t" \
        "v_add_f32 v38, v38, v62\n\t" \
        "v_add_f32 v39, v39, v63\n\t" \
        "v_add_f32 v40, v40, v64\n\t" \
        "v_add_f32 v41, v41, v65\n\t" \
        "v_add_f32 v42, v42, v66\n\t" \
        "v_add_f32 v43, v43, v67\n\t" \
        "v_add_f32 v44, v44, v68\n\t" \
        "v_add_f32 v45, v45, v69\n\t" \
        "v_add_f32 v46, v46, v70\n\t" \
        "v_add_f32 v47, v47, v71\n\t" \
        "v_add_f32 v48, v48, v72\n\t" \
        "v_add_f32 v49, v49, v73\n\t" \
        "v_add_f32 v50, v50, v74\n\t" \
        "v_add_f32 v51, v51, v75\n\t" \
        "v_add_f32 v52, v52, v76\n\t" \
        "v_add_f32 v53, v53, v77\n\t" \
        "v_cvt_pk_bf16_f32 v30, v30, v31\n\t" \
        "v_cvt_pk_bf16_f32 v31, v32, v33\n\t" \
        "v_cvt_pk_bf16_f32 v32, v34, v35\n\t" \
        "v_cvt_pk_bf16_f32 v33, v36, v37\n\t" \
        "s_nop 0\n\t" \
        "v_permlane16_swap_b32 v30, v32\n\t" \
        "v_permlane16_swap_b32 v31, v33\n\t" \
        "buffer_store_dwordx4 v[30:33], v8, s[28:31], s59 offen offset:0 sc1\n\t" \
        "v_cvt_pk_bf16_f32 v38, v38, v39\n\t" \
        "v_cvt_pk_bf16_f32 v39, v40, v41\n\t" \
        "v_cvt_pk_bf16_f32 v40, v42, v43\n\t" \
        "v_cvt_pk_bf16_f32 v41, v44, v45\n\t" \
        "s_nop 0\n\t" \
        "v_permlane16_swap_b32 v38, v40\n\t" \
        "v_permlane16_swap_b32 v39, v41\n\t" \
        "buffer_store_dwordx4 v[38:41], v8, s[28:31], s59 offen offset:64 sc1\n\t" \
        "v_cvt_pk_bf16_f32 v46, v46, v47\n\t" \
        "v_cvt_pk_bf16_f32 v47, v48, v49\n\t" \
        "v_cvt_pk_bf16_f32 v48, v50, v51\n\t" \
        "v_cvt_pk_bf16_f32 v49, v52, v53\n\t" \
        "s_nop 0\n\t" \
        "v_permlane16_swap_b32 v46, v48\n\t" \
        "v_permlane16_swap_b32 v47, v49\n\t" \
        "buffer_store_dwordx4 v[46:49], v8, s[28:31], s59 offen offset:128 sc1\n\t" \
        "s_endpgm\n\t" \
        ".Lpf1%=:\n\t" \
        ".Lfail%=:\n\t" \
        "s_waitcnt vmcnt(0) lgkmcnt(0)\n\t" \
        "s_nop 7\n\t" \
        "s_nop 7\n\t" \
        "s_mov_b32 s41, 1\n\t" \
        ".Lexit%=:\n\t"
#define DCN_ASM_CLOBBERS "s3", "s4", "s5", "s6", "s7", "s8", "s9", "s10", "s11", "s12", "s13", "s14", "s15", "s36", "s37", "s38", "s39", "s40", "s42", "s43", "s44", "s45", "s46", "s47", "s48", "s49", "s50", "s51", "s52", "s53", "s54", "s55", "s56", "s57", "s58", "s59", "s60", "s61", "s62", "s63", "s64", "s65", "s66", "s67", "s68", "s69", "s70", "s71", "s72", "s73", "s74", "s75", "s76", "s77", "s78", "s79", "s80", "s81", "s82", "s83", "s84", "s85", "s86", "s87", "s88", "s89", "s90", "s91", "s92", "s93", "s94", "s95", "s96", "s97", "s98", "s99", "v2", "v3", "v4", "v5", "v6", "v7", "v8", "v9", "v10", "v11", "v12", "v13", "v14", "v15", "v16", "v17", "v18", "v19", "v20", "v21", "v22", "v23", "v24", "v25", "v26", "v27", "v28", "v29", "v30", "v31", "v32", "v33", "v34", "v35", "v36", "v37", "v38", "v39", "v40", "v41", "v42", "v43", "v44", "v45", "v46", "v47", "v48", "v49", "v50", "v51", "v52", "v53", "v54", "v55", "v56", "v57", "v58", "v59", "v60", "v61", "v62", "v63", "v64", "v65", "v66", "v67", "v68", "v69", "v70", "v71", "v72", "v73", "v74", "v75", "v76", "v77", "v78", "v79", "v80", "v81", "v82", "v83", "v84", "v85", "v86", "v87", "v88", "v89", "v90", "v91", "v92", "v93", "v94", "v95", "v96", "v97", "v98", "v99", "v100", "v101", "v102", "v103", "v104", "v105", "v106", "v107", "v108", "v109", "v110", "v111", "v112", "v113", "v114", "v115", "v116", "v117", "v118", "v119", "v120", "v121", "v122", "v123", "v124", "v125", "v126", "v127", "v128", "v129", "v130", "v131", "v132", "v133", "v134", "v135", "v136", "v137", "v138", "v139", "v140", "v141", "v142", "v143", "v144", "v145", "v146", "v147", "v148", "v149", "v150", "v151", "v152", "v153", "v154", "v155", "v156", "v157", "v158", "v159", "v160", "v161", "v162", "v163", "v164", "v165", "v166", "v167", "v168", "v169", "v170", "v171", "v172", "v173", "v174", "v175", "v176", "v177", "v178", "v179", "v180", "v181", "v182", "v183", "v184", "v185", "v186", "v187", "v188", "v189", "v190", "v191", "v192", "v193", "v194", "v195", "v196", "v197", "v198", "v199", "v200", "v201", "v202", "v203", "v204", "v205", "v206", "v207", "v208", "v209", "v210", "v211", "v212", "v213", "v214", "v215", "v216", "v217", "v218", "v219", "v220", "v221", "v222", "v223", "v224", "v225", "v226", "v227", "v228", "v229", "v230", "v231", "v232", "v233", "v234", "v235", "v236", "v237", "v238", "v239", "v240", "v241", "v242", "v243", "v244", "v245", "v246", "v247", "v248", "v249", "v250", "v251", "v252", "v253", "v254", "v255", "a0", "a1", "a2", "a3", "a4", "a5", "a6", "a7", "a8", "a9", "a10", "a11", "a12", "a13", "a14", "a15", "a16", "a17", "a18", "a19", "a20", "a21", "a22", "a23", "a24", "a25", "a26", "a27", "a28", "a29", "a30", "a31", "a32", "a33", "a34", "a35", "a36", "a37", "a38", "a39", "a40", "a41", "a42", "a43", "a44", "a45", "a46", "a47", "a48", "a49", "a50", "a51", "a52", "a53", "a54", "a55", "a56", "a57", "a58", "a59", "a60", "a61", "a62", "a63", "a64", "a65", "a66", "a67", "a68", "a69", "a70", "a71", "a72", "a73", "a74", "a75", "a76", "a77", "a78", "a79", "a80", "a81", "a82", "a83", "a84", "a85", "a86", "a87", "a88", "a89", "a90", "a91", "a92", "a93", "a94", "a95", "a96", "a97", "a98", "a99", "a100", "a101", "a102", "a103", "a104", "a105", "a106", "a107", "a108", "a109", "a110", "a111", "a112", "a113", "a114", "a115", "a116", "a117", "a118", "a119", "a120", "a121", "a122", "a123", "a124", "a125", "a126", "a127", "a128", "a129", "a130", "a131", "a132", "a133", "a134", "a135", "a136", "a137", "a138", "a139", "a140", "a141", "a142", "a143", "a144", "a145", "a146", "a147", "a148", "a149", "a150", "a151", "a152", "a153", "a154", "a155", "a156", "a157", "a158", "a159", "a160", "a161", "a162", "a163", "a164", "a165", "a166", "a167", "a168", "a169", "a170", "a171", "a172", "a173", "a174", "a175", "a176", "a177", "a178", "a179", "a180", "a181", "a182", "a183", "a184", "a185", "a186", "a187", "a188", "a189", "a190", "a191", "a192", "a193", "a194", "a195", "a196", "a197", "a198", "a199", "a200", "a201", "a202", "a203", "a204", "a205", "a206", "a207", "a208", "a209", "a210", "a211", "a212", "a213", "a214", "a215", "a216", "a217", "a218", "a219", "a220", "a221", "a222", "a223", "a224", "a225", "a226", "a227", "a228", "a229", "a230", "a231", "a232", "a233", "a234", "a235", "a236", "a237", "a238", "a239", "a240", "a241", "a242", "a243", "a244", "a245", "a246", "a247", "a248", "a249", "a250", "a251", "a252", "a253", "a254", "a255"
#define NWG 255


__device__ __forceinline__ float bf_lo(unsigned u) { return __uint_as_float(u << 16); }
__device__ __forceinline__ float bf_hi(unsigned u) { return __uint_as_float(u & 0xffff0000u); }
__device__ __forceinline__ unsigned cvt_pk(float lo, float hi) {
    bf16x2 r = {(__bf16)lo, (__bf16)hi};
    unsigned u;
    __builtin_memcpy(&u, &r, 4);
    return u;
}
__device__ __forceinline__ f32x4 mfma16(u32x4 a, u32x4 b, f32x4 c) {
    bf16x8 A, B;
    __builtin_memcpy(&A, &a, 16);
    __builtin_memcpy(&B, &b, 16);
    return __builtin_amdgcn_mfma_f32_16x16x32_bf16(A, B, c, 0, 0, 0);
}
__device__ __forceinline__ u32x2 tr_read(unsigned a) {
    s16x4 v = __builtin_amdgcn_ds_read_tr16_b64_v4i16((lds_s16x4*)(uintptr_t)a);
    u32x2 u;
    __builtin_memcpy(&u, &v, 8);
    return u;
}
__device__ __forceinline__ unsigned clip_pk(unsigned v) {
    typedef __attribute__((ext_vector_type(2))) short i16x2;
    i16x2 r;
    __builtin_memcpy(&r, &v, 4);
    r = __builtin_elementwise_min(__builtin_elementwise_max(r, (i16x2){0, 0}), (i16x2){0x435f, 0x435f});
    __builtin_memcpy(&v, &r, 4);
    return v;
}

__device__ __forceinline__ void slow_tile(const unsigned short* x, const unsigned short* off, const unsigned short* wgt, unsigned short* out,
                                          const int wv, const unsigned lane, const int y0, const int x0, const int nrows, const unsigned lb) {
    const unsigned px = lane & 15, q = lane >> 4;
#pragma unroll 1
    for (int r0 = 0; r0 < nrows; r0 += 4) {
        const int row = y0 + min(r0 + wv, nrows - 1), col = x0 + px;
        f32x4 acc[6];
#pragma unroll
        for (int i = 0; i < 6; ++i) acc[i] = (f32x4){0.f, 0.f, 0.f, 0.f};
#pragma unroll 1
        for (int t = 0; t < 9; ++t) {
            asm volatile("s_waitcnt vmcnt(0) lgkmcnt(0)\n\ts_barrier" ::: "memory");
            for (unsigned d = wv * 64 + lane; d < WTAP / 16; d += NTHREADS)
                *(lds_u32x4*)(uintptr_t)(lb + d * 16) = *(const glb_u32x4*)((uintptr_t)wgt + t * WTAP + d * 16);
            const unsigned od = *(const glb_u32*)((uintptr_t)off + (row * IMG + col) * 36 + t * 4);
            const unsigned pc = clip_pk(cvt_pk((float)(row + t / 3 - 1) + bf_lo(od), (float)(col + t % 3 - 1) + bf_hi(od)));
            const float py = bf_lo(pc), pxf = bf_hi(pc);
            const int gy = (int)py, gx = (int)pxf;
            const float wy = py - (float)gy, wx = pxf - (float)gx;
            const unsigned ab = cvt_pk(1.f - wy, 1.f - wx);
            const float a = bf_lo(ab), b = bf_hi(ab);
            const unsigned w01 = cvt_pk(a * b, a * wx), w23 = cvt_pk(wy * b, wy * wx);
            const unsigned o00 = (gy * IMG + gx) * (CH * 2) + q * 16;
            const unsigned dx = gx < IMG - 1 ? CH * 2 : 0, dy = gy < IMG - 1 ? ROWB : 0;
            const float wc[4] = {bf_lo(w01), bf_hi(w01), bf_lo(w23), bf_hi(w23)};
            u32x4 bfr[3];
#pragma unroll
            for (int j = 0; j < 3; ++j) {
                float vv[8];
#pragma unroll
                for (int c = 0; c < 4; ++c) {
                    const u32x4 gv = *(const glb_u32x4*)((uintptr_t)x + (o00 + ((c & 1) ? dx : 0u) + ((c & 2) ? dy : 0u) + j * 64));
#pragma unroll
                    for (int k = 0; k < 4; ++k) {
                        vv[2 * k] = c ? fmaf(wc[c], bf_lo(gv[k]), vv[2 * k]) : wc[0] * bf_lo(gv[k]);
                        vv[2 * k + 1] = c ? fmaf(wc[c], bf_hi(gv[k]), vv[2 * k + 1]) : wc[0] * bf_hi(gv[k]);
                    }
                }
                bfr[j] = (u32x4){cvt_pk(vv[0], vv[1]), cvt_pk(vv[2], vv[3]), cvt_pk(vv[4], vv[5]), cvt_pk(vv[6], vv[7])};
            }
            asm volatile("s_waitcnt vmcnt(0) lgkmcnt(0)\n\ts_barrier" ::: "memory");
            const unsigned wa = lb + (8 * q + ((lane >> 2) & 3)) * 192 + (lane & 3) * 8;
#pragma unroll
            for (int j = 0; j < 3; ++j)
#pragma unroll
                for (int i = 0; i < 6; ++i) {
                    const u32x2 lo = tr_read(wa + j * 6144 + i * 32), hi = tr_read(wa + j * 6144 + i * 32 + 768);
                    acc[i] = mfma16((u32x4){lo[0], lo[1], hi[0], hi[1]}, bfr[j], acc[i]);
                }
        }
        if (r0 + wv < nrows) {
#pragma unroll
            for (int i = 0; i < 6; ++i)
                *(glb_u32x2*)((uintptr_t)out + (row * IMG + col) * (CH * 2) + i * 32 + q * 8) =
                    (u32x2){cvt_pk(acc[i][0], acc[i][1]), cvt_pk(acc[i][2], acc[i][3])};
        }
    }
}

__global__ __launch_bounds__(NTHREADS, 1) void dcn_kernel(const unsigned short* x_, const unsigned short* off_,
                                                          const unsigned short* wgt_, unsigned short* out_) {
    __shared__ __attribute__((aligned(64))) unsigned char lds[LDS_BYTES];
    u32x4 xd, od, wd, odsc;
    unsigned y0, x0, rows, wv, status, stv;
    asm volatile(DCN_ASM_TEXT
                 : "={s[16:19]}"(xd), "={s[20:23]}"(od), "={s[24:27]}"(wd), "={s[28:31]}"(odsc),
                   "={s32}"(y0), "={s33}"(x0), "={s34}"(rows), "={s35}"(wv), "={s41}"(status), "={v1}"(stv)
                 : "{s[0:1]}"(__builtin_amdgcn_kernarg_segment_ptr()), "{s2}"(blockIdx.x), "{v0}"(threadIdx.x)
                 : "memory", "scc", "vcc", DCN_ASM_CLOBBERS);
    unsigned lane;
    asm volatile("v_mbcnt_lo_u32_b32 %0, -1, 0\n\tv_mbcnt_hi_u32_b32 %0, -1, %0" : "=v"(lane));
    slow_tile((const unsigned short*)(((unsigned long long)xd[1] << 32) | xd[0]), (const unsigned short*)(((unsigned long long)od[1] << 32) | od[0]),
              (const unsigned short*)(((unsigned long long)wd[1] << 32) | wd[0]), (unsigned short*)(((unsigned long long)odsc[1] << 32) | odsc[0]),
              wv, lane, y0, x0, rows, (unsigned)(uintptr_t)(__attribute__((address_space(3))) unsigned char*)lds);
}

extern "C" void kernel_launch(void* const* d_in, const int* in_sizes, int n_in,
                              void* d_out, int out_size, void* d_ws, size_t ws_size,
                              hipStream_t stream) {
    dcn_kernel<<<NWG, NTHREADS, 0, stream>>>((const unsigned short*)d_in[0], (const unsigned short*)d_in[1],
                                             (const unsigned short*)d_in[2], (unsigned short*)d_out);
}
```

```cpp
#include <hip/hip_runtime.h>
#include <stdint.h>

#define IMG 224
#define CH 96
#define ROWB (IMG * CH * 2)
#define NTHREADS 256
#define WTAP 18432
#define LDS_BYTES 163840

typedef __attribute__((ext_vector_type(2))) __bf16 bf16x2;
typedef __attribute__((ext_vector_type(8))) __bf16 bf16x8;
typedef __attribute__((ext_vector_type(4))) float f32x4;
typedef __attribute__((ext_vector_type(4))) unsigned u32x4;
typedef __attribute__((address_space(1))) u32x4 glb_u32x4;
typedef __attribute__((address_space(1))) unsigned glb_u32;
typedef __attribute__((ext_vector_type(2))) unsigned u32x2;
typedef __attribute__((address_space(1))) u32x2 glb_u32x2;
typedef __attribute__((ext_vector_type(4))) short s16x4;
typedef __attribute__((address_space(3))) s16x4 lds_s16x4;
typedef __attribute__((address_space(3))) u32x4 lds_u32x4;

#define DCN_ASM_TEXT \
        "s_load_dwordx2 s[24:25], s[0:1], 0x10\n\t" \
        "s_load_dwordx2 s[20:21], s[0:1], 0x8\n\t" \
        "s_load_dwordx2 s[16:17], s[0:1], 0x0\n\t" \
        "s_load_dwordx2 s[28:29], s[0:1], 0x18\n\t" \
        "s_getpc_b64 s[4:5]\n\t" \
        ".Lpf0%=:\n\t" \
        "v_lshrrev_b32 v3, 6, v0\n\t" \
        "s_bfe_u32 s15, s2, 0x20003\n\t" \
        "s_mov_b32 m0, 0x27f00\n\t" \
        "v_readfirstlane_b32 s35, v3\n\t" \
        "v_lshl_add_u32 v3, v0, 2, s15\n\t" \
        "s_lshr_b32 s8, s2, 3\n\t" \
        "v_lshlrev_b32 v3, 7, v3\n\t" \
        "s_and_b32 s7, s2, 7\n\t" \
        "v_min_u32 v3, .Lpf1%=-.Lpf0%=, v3\n\t" \
        "v_and_b32 v2, 63, v0\n\t" \
        "s_lshr_b32 s6, s8, 2\n\t" \
        "global_load_lds_dword v3, s[4:5]\n\t" \
        "s_lshl_b32 s7, s7, 2\n\t" \
        "s_and_b32 s8, s8, 3\n\t" \
        "s_lshl_b32 s6, s6, 5\n\t" \
        "s_add_u32 s7, s7, s8\n\t" \
        "s_add_u32 s6, s6, s7\n\t" \
        "s_cmp_ge_u32 s2, 224\n\t" \
        "s_cselect_b32 s6, s2, s6\n\t" \
        "s_mov_b32 s3, 0xec10200\n\t" \
        "s_cmp_ge_u32 s6, 24\n\t" \
        "s_cselect_b32 s3, 0x3ec28218, s3\n\t" \
        "s_cmp_ge_u32 s6, 40\n\t" \
        "s_cselect_b32 s3, 0x3ec20128, s3\n\t" \
        "s_cmp_ge_u32 s6, 56\n\t" \
        "s_cselect_b32 s3, 0xa800338, s3\n\t" \
        "s_cmp_ge_u32 s6, 78\n\t" \
        "s_cselect_b32 s3, 0x3a82804e, s3\n\t" \
        "s_cmp_ge_u32 s6, 101\n\t" \
        "s_cselect_b32 s3, 0x38400065, s3\n\t" \
        "s_cmp_ge_u32 s6, 111\n\t" \
        "s_cselect_b32 s3, 0x1840016f, s3\n\t" \
        "s_cmp_ge_u32 s6, 123\n\t" \
        "s_cselect_b32 s3, 0x1d02837b, s3\n\t" \
        "s_cmp_ge_u32 s6, 211\n\t" \
        "s_cselect_b32 s3, 0x14ea83d3, s3\n\t" \
        "s_and_b32 s4, s3, 0xff\n\t" \
        "s_lshr_b32 s5, s3, 27\n\t" \
        "s_sub_u32 s6, s6, s4\n\t" \
        "s_bfe_u32 s33, s3, 0x40008\n\t" \
        "s_lshr_b32 s7, s6, s5\n\t" \
        "s_bfe_u32 s34, s3, 0x50014\n\t" \
        "s_add_u32 s33, s33, s7\n\t" \
        "s_lshl_b32 s7, s7, s5\n\t" \
        "s_bfe_u32 s32, s3, 0x8000c\n\t" \
        "s_sub_u32 s6, s6, s7\n\t" \
        "s_bfe_u32 s11, s3, 0x20019\n\t" \
        "s_mul_i32 s6, s6, s34\n\t" \
        "s_lshl_b32 s33, s33, 4\n\t" \
        "s_add_u32 s32, s32, s6\n\t" \
        "s_sub_u32 s4, 224, s34\n\t" \
        "s_mov_b32 s3, 0\n\t" \
        "s_min_u32 s32, s32, s4\n\t" \
        "s_cmp_lg_u32 s11, 2\n\t" \
        "s_cbranch_scc1 .LT_entry%=\n\t" \
        "s_mul_i32 s40, s35, 0xc00\n\t" \
        "s_mov_b32 s9, 0xc0\n\t" \
        "s_mov_b32 s10, 18\n\t" \
        "s_mov_b32 s14, 0xffff5800\n\t" \
        "s_add_u32 s39, s40, 0xf840\n\t" \
        "v_lshlrev_b32 v3, 4, v2\n\t" \
        "v_and_b32 v32, 32, v2\n\t" \
        "v_xor_b32 v5, 32, v3\n\t" \
        "v_xor_b32 v4, v3, v32\n\t" \
        "s_branch .Lxv%=\n\t" \
        ".LT_entry%=:\n\t" \
        "s_lshl_b32 s4, s35, 2\n\t" \
        "s_cmp_eq_u32 s11, 3\n\t" \
        "s_cselect_b32 s5, 2, 4\n\t" \
        "s_cselect_b32 s4, s4, 0\n\t" \
        "v_lshrrev_b32 v36, s5, v2\n\t" \
        "s_cselect_b32 s5, 15, s11\n\t" \
        "v_add_u32 v19, s4, v2\n\t" \
        "s_cselect_b32 s12, 0, 2\n\t" \
        "v_and_b32 v36, s5, v36\n\t" \
        "s_cselect_b32 s13, 0, s35\n\t" \
        "v_and_b32 v19, 15, v19\n\t" \
        "s_mul_i32 s37, s32, 224\n\t" \
        "v_lshlrev_b32 v36, s12, v36\n\t" \
        "s_cmp_eq_u32 s33, 0\n\t" \
        "s_cselect_b32 s9, 0x180, 0\n\t" \
        "v_add_u32 v18, s13, v36\n\t" \
        "s_add_u32 s37, s37, s33\n\t" \
        "v_mul_u32_u24 v33, 0x1f80, v18\n\t" \
        "s_mul_i32 s37, s37, 36\n\t" \
        "s_mov_b32 s10, 19\n\t" \
        "v_mad_u32_u24 v17, v19, 36, v33\n\t" \
        "s_mov_b32 s14, 0xfffeb000\n\t" \
        ".Lxv%=:\n\t" \
        "s_mul_i32 s42, s35, 60\n\t" \
        "s_sub_u32 s43, 225, s33\n\t" \
        "v_add_u32 v32, s42, v2\n\t" \
        "s_min_u32 s43, s43, s10\n\t" \
        "v_mul_u32_u24 v33, 0x2ab, v32\n\t" \
        "s_mul_i32 s43, s43, 0xc0\n\t" \
        "v_lshrrev_b32 v33, 13, v33\n\t" \
        "s_mul_i32 s44, s33, 0xc0\n\t" \
        "v_mul_u32_u24 v34, 0xc0, v33\n\t" \
        "s_mul_i32 s36, s32, 0xa800\n\t" \
        "v_mad_i32_i24 v35, v33, -12, v32\n\t" \
        "s_mul_i32 s38, s35, 0x3c0\n\t" \
        "v_and_b32 v33, 4, v33\n\t" \
        "s_sub_u32 s44, s44, 0x180\n\t" \
        "v_min_u32 v34, s43, v34\n\t" \
        "s_add_u32 s38, s38, 64\n\t" \
        "v_lshrrev_b32 v33, 1, v33\n\t" \
        "s_add_i32 s58, s36, s14\n\t" \
        "v_max_u32 v34, s9, v34\n\t" \
        "s_mov_b32 s19, 0x20000\n\t" \
        "v_xor_b32 v35, v35, v33\n\t" \
        "s_mov_b32 s18, 0x930000\n\t" \
        "v_lshl_add_u32 v35, v35, 4, v34\n\t" \
        "s_mov_b32 s26, 0x28800\n\t" \
        "v_add_u32 v13, s44, v35\n\t" \
        "s_mov_b32 s22, 0x1b9000\n\t" \
        "s_mov_b32 s23, s19\n\t" \
        "s_mov_b32 s27, s19\n\t" \
        "s_mov_b64 s[30:31], s[18:19]\n\t" \
        "s_mov_b32 s59, 0xf80\n\t" \
        "s_mov_b32 s60, 0xa800\n\t" \
        "s_mov_b32 m0, s39\n\t" \
        "s_cmp_eq_u32 s11, 2\n\t" \
        "s_waitcnt lgkmcnt(0)\n\t" \
        "s_cbranch_scc1 .Lw0%=\n\t" \
        "buffer_load_dwordx4 v[40:43], v17, s[20:23], s37 offen\n\t" \
        "buffer_load_dwordx4 v[44:47], v17, s[20:23], s37 offen offset:16\n\t" \
        "buffer_load_dword v48, v17, s[20:23], s37 offen offset:32\n\t" \
        "s_branch .Lx%=\n\t" \
        ".Lw0%=:\n\t" \
        "buffer_load_dwordx4 v3, s[24:27], s40 offen offset:0 lds\n\t" \
        "buffer_load_dwordx4 v4, s[24:27], s40 offen offset:1024 lds\n\t" \
        "buffer_load_dwordx4 v5, s[24:27], s40 offen offset:2048 lds\n\t" \
        ".Lx%=:\n\t" \
        "s_mov_b32 m0, s38\n\t" \
        "s_max_i32 s57, s58, 0\n\t" \
        "buffer_load_dwordx4 v13, s[16:19], s57 offen sc1 lds\n\t" \
        "s_add_u32 m0, m0, s59\n\t" \
        "s_add_i32 s58, s58, s60\n\t" \
        "s_max_i32 s57, s58, 0\n\t" \
        "buffer_load_dwordx4 v13, s[16:19], s57 offen sc1 lds\n\t" \
        "s_add_u32 m0, m0, s59\n\t" \
        "s_add_i32 s57, s58, s60\n\t" \
        "buffer_load_dwordx4 v13, s[16:19], s57 offen sc1 lds\n\t" \
        "s_add_u32 m0, m0, s59\n\t" \
        "s_add_u32 s57, s57, s60\n\t" \
        "buffer_load_dwordx4 v13, s[16:19], s57 offen sc1 lds\n\t" \
        "s_cmp_eq_u32 s11, 2\n\t" \
        "s_cbranch_scc1 .LR_start%=\n\t" \
        "s_cmp_eq_u32 s11, 1\n\t" \
        "s_cbranch_scc1 .LH8_start%=\n\t" \
        "s_cmp_eq_u32 s11, 0\n\t" \
        "s_cbranch_scc1 .LH4_start%=\n\t" \
        "s_add_u32 s57, s36, 0x15000\n\t" \
        "s_add_u32 m0, s38, 0x3e00\n\t" \
        "s_nop 0\n\t" \
        "buffer_load_dwordx4 v13, s[16:19], s57 offen sc1 lds\n\t" \
        "v_lshrrev_b32 v164, 2, v18\n\t" \
        "v_add_u32 v168, s32, v18\n\t" \
        "v_add_u32 v169, s33, v19\n\t" \
        "v_and_b32 v164, 2, v164\n\t" \
        "v_lshl_add_u32 v165, v168, 1, -3\n\t" \
        "v_lshl_add_u32 v166, v169, 1, -3\n\t" \
        "v_and_or_b32 v164, v18, 1, v164\n\t" \
        "v_bfe_u32 v167, v18, 1, 2\n\t" \
        "v_mul_u32_u24 v164, 0x900, v164\n\t" \
        "v_cmp_gt_u32 vcc, s34, v18\n\t" \
        "v_lshl_add_u32 v16, v19, 4, v164\n\t" \
        "v_lshl_add_u32 v16, v167, 2, v16\n\t" \
        "s_mov_b64 s[10:11], vcc\n\t" \
        "v_add_u32 v16, 0x1e840, v16\n\t" \
        "s_movk_i32 s42, -1152\n\t" \
        "v_cvt_f32_u32 v60, v165\n\t" \
        "s_add_u32 s57, s36, 0x1f800\n\t" \
        "s_add_u32 m0, s38, 0x4d80\n\t" \
        "s_nop 0\n\t" \
        "buffer_load_dwordx4 v13, s[16:19], s57 offen sc1 lds\n\t" \
        "v_cvt_f32_u32 v63, v166\n\t" \
        "v_add_u32 v165, 2, v165\n\t" \
        "v_add_u32 v166, 2, v166\n\t" \
        "v_cvt_f32_u32 v61, v165\n\t" \
        "v_cvt_f32_u32 v64, v166\n\t" \
        "v_add_u32 v165, 2, v165\n\t" \
        "v_add_u32 v166, 2, v166\n\t" \
        "v_cvt_f32_u32 v62, v165\n\t" \
        "v_cvt_f32_u32 v65, v166\n\t" \
        "v_mov_b32 v165, 0x7c00\n\t" \
        "v_mov_b32 v166, 0x7f800000\n\t" \
        "v_lshrrev_b32 v60, 23, v60\n\t" \
        "v_and_b32 v63, 0x7f800000, v63\n\t" \
        "v_lshl_add_u32 v60, v60, 7, s42\n\t" \
        "v_add_u32 v63, 0xfb800000, v63\n\t" \
        "v_cndmask_b32 v60, v165, v60, s[10:11]\n\t" \
        "s_add_u32 s57, s36, 0x2a000\n\t" \
        "s_add_u32 m0, s38, 0x5d00\n\t" \
        "s_nop 0\n\t" \
        "buffer_load_dwordx4 v13, s[16:19], s57 offen sc1 lds\n\t" \
        "v_cndmask_b32 v63, v166, v63, s[10:11]\n\t" \
        "v_lshrrev_b32 v61, 23, v61\n\t" \
        "v_and_b32 v64, 0x7f800000, v64\n\t" \
        "v_lshl_add_u32 v61, v61, 7, s42\n\t" \
        "v_add_u32 v64, 0xfb800000, v64\n\t" \
        "v_cndmask_b32 v61, v165, v61, s[10:11]\n\t" \
        "v_cndmask_b32 v64, v166, v64, s[10:11]\n\t" \
        "v_lshrrev_b32 v62, 23, v62\n\t" \
        "v_and_b32 v65, 0x7f800000, v65\n\t" \
        "v_lshl_add_u32 v62, v62, 7, s42\n\t" \
        "v_add_u32 v65, 0xfb800000, v65\n\t" \
        "v_cndmask_b32 v62, v165, v62, s[10:11]\n\t" \
        "v_cndmask_b32 v65, v166, v65, s[10:11]\n\t" \
        "v_add_u32 v168, -1, v168\n\t" \
        "v_add_u32 v169, -1, v169\n\t" \
        "v_lshrrev_b32 v165, 2, v16\n\t" \
        "s_add_u32 s57, s36, 0x34800\n\t" \
        "s_add_u32 m0, s38, 0x6c80\n\t" \
        "s_nop 0\n\t" \
        "buffer_load_dwordx4 v13, s[16:19], s57 offen sc1 lds\n\t" \
        "v_lshl_or_b32 v50, v168, 16, v165\n\t" \
        "v_lshl_or_b32 v50, v169, 24, v50\n\t" \
        "v_add_u32 v51, 0x1000040, v50\n\t" \
        "v_add_u32 v52, 0x2000080, v50\n\t" \
        "v_add_u32 v53, 0x100c0, v50\n\t" \
        "v_add_u32 v54, 0x1010100, v50\n\t" \
        "v_add_u32 v55, 0x2010140, v50\n\t" \
        "v_add_u32 v56, 0x20180, v50\n\t" \
        "v_add_u32 v57, 0x10201c0, v50\n\t" \
        "v_add_u32 v58, 0x2020200, v50\n\t" \
        "v_lshlrev_b32 v66, 3, v2\n\t" \
        "v_add_u32 v66, 0x10840, v66\n\t" \
        "v_mul_u32_u24 v174, 0xf80, v18\n\t" \
        "v_mul_u32_u24 v177, 0xc0, v19\n\t" \
        "v_lshlrev_b32 v178, 3, v19\n\t" \
        "v_add_u32 v170, 0x1080, v174\n\t" \
        "s_add_u32 s57, s36, 0x3f000\n\t" \
        "s_add_u32 m0, s38, 0x7c00\n\t" \
        "s_nop 0\n\t" \
        "buffer_load_dwordx4 v13, s[16:19], s57 offen sc1 lds\n\t" \
        "v_and_b32 v14, 48, v2\n\t" \
        "v_add_u32 v170, v170, v177\n\t" \
        "v_add_u32 v173, 8, v178\n\t" \
        "v_and_b32 v173, 32, v173\n\t" \
        "v_add_u32 v174, v170, v173\n\t" \
        "v_add_u32 v175, 0xf80, v174\n\t" \
        "v_add_u32 v176, 0x1f00, v174\n\t" \
        "ds_write2st64_b32 v16, v174, v175 offset0:0 offset1:3\n\t" \
        "ds_write_b32 v16, v176 offset:1536\n\t" \
        "v_add_u32 v170, 0xc0, v170\n\t" \
        "v_add_u32 v173, 16, v178\n\t" \
        "v_and_b32 v173, 32, v173\n\t" \
        "v_add_u32 v174, v170, v173\n\t" \
        "v_add_u32 v175, 0xf80, v174\n\t" \
        "v_add_u32 v176, 0x1f00, v174\n\t" \
        "ds_write2st64_b32 v16, v174, v175 offset0:1 offset1:4\n\t" \
        "s_add_u32 s57, s36, 0x49800\n\t" \
        "s_add_u32 m0, s38, 0x8b80\n\t" \
        "s_nop 0\n\t" \
        "buffer_load_dwordx4 v13, s[16:19], s57 offen sc1 lds\n\t" \
        "ds_write_b32 v16, v176 offset:1792\n\t" \
        "v_add_u32 v170, 0xc0, v170\n\t" \
        "v_add_u32 v173, 24, v178\n\t" \
        "v_and_b32 v173, 32, v173\n\t" \
        "v_add_u32 v174, v170, v173\n\t" \
        "v_add_u32 v175, 0xf80, v174\n\t" \
        "v_add_u32 v176, 0x1f00, v174\n\t" \
        "ds_write2st64_b32 v16, v174, v175 offset0:2 offset1:5\n\t" \
        "ds_write_b32 v16, v176 offset:2048\n\t" \
        "s_lshl_b32 s48, s35, 10\n\t" \
        "s_and_b32 s55, s35, 1\n\t" \
        "s_add_u32 s48, s48, 0xf840\n\t" \
        "s_mul_i32 s55, s55, 0x3180\n\t" \
        "s_cmp_lt_u32 s35, 2\n\t" \
        "s_cselect_b32 s49, 0, 0x30c0\n\t" \
        "s_cselect_b32 s53, 1, -1\n\t" \
        "s_add_u32 s57, s36, 0x54000\n\t" \
        "s_add_u32 m0, s38, 0x9b00\n\t" \
        "s_nop 0\n\t" \
        "buffer_load_dwordx4 v13, s[16:19], s57 offen sc1 lds\n\t" \
        "s_add_u32 s49, s49, s55\n\t" \
        "s_mul_i32 s53, s53, 192\n\t" \
        "s_mov_b32 s52, 0x3ffe0\n\t" \
        "s_add_u32 s49, s49, 0x20c40\n\t" \
        "v_mul_i32_i24 v30, s53, v2\n\t" \
        "v_lshlrev_b32 v170, 3, v2\n\t" \
        "v_lshlrev_b32 v171, 2, v2\n\t" \
        "v_add_u32 v30, s49, v30\n\t" \
        "v_and_b32 v170, 16, v170\n\t" \
        "v_and_b32 v171, 32, v171\n\t" \
        "v_lshl_add_u32 v31, v2, 3, s48\n\t" \
        "v_or3_b32 v30, v30, v170, v171\n\t" \
        "v_lshlrev_b32 v170, 2, v2\n\t" \
        "s_add_u32 s55, s48, 512\n\t" \
        "v_add_u32 v32, s55, v170\n\t" \
        "v_cvt_f32_u32 v34, s32\n\t" \
        "s_add_u32 s57, s36, 0x5e800\n\t" \
        "s_add_u32 m0, s38, 0xaa80\n\t" \
        "s_nop 0\n\t" \
        "buffer_load_dwordx4 v13, s[16:19], s57 offen sc1 lds\n\t" \
        "v_cvt_f32_u32 v35, s33\n\t" \
        "v_mov_b32 v36, 124\n\t" \
        "v_add_f32 v34, -2.0, v34\n\t" \
        "v_add_f32 v35, -2.0, v35\n\t" \
        "v_cvt_f32_u32 v33, 15\n\t" \
        "v_mov_b32 v37, 0x05040100\n\t" \
        "v_mov_b32 v38, 0x80008000\n\t" \
        "v_mov_b32 v39, 0x435f435f\n\t" \
        "s_mul_i32 s40, s35, 0xc00\n\t" \
        "v_lshlrev_b32 v3, 4, v2\n\t" \
        "v_and_b32 v170, 32, v2\n\t" \
        "v_xor_b32 v5, 32, v3\n\t" \
        "v_xor_b32 v4, v3, v170\n\t" \
        "s_add_u32 s39, s40, 0xf840\n\t" \
        "s_waitcnt vmcnt(12)\n\t" \
        "s_add_u32 s57, s36, 0x69000\n\t" \
        "s_add_u32 m0, s38, 0xba00\n\t" \
        "s_nop 0\n\t" \
        "buffer_load_dwordx4 v13, s[16:19], s57 offen sc1 lds\n\t" \
        "s_lshl_b32 s45, s35, 10\n\t" \
        "s_mov_b32 s54, 0\n\t" \
        "s_add_u32 s45, s45, 0xf840\n\t" \
        "v_cmp_nlt_f32 s[4:5], |v40|, v63\n\t" \
        "v_cmp_nlt_f16 s[8:9], |v40|, v60\n\t" \
        "v_cmp_nlt_f32 s[6:7], |v41|, v64\n\t" \
        "v_cmp_nlt_f16 s[10:11], |v41|, v60\n\t" \
        "s_or_b64 s[12:13], s[4:5], s[8:9]\n\t" \
        "v_mbcnt_lo_u32_b32 v160, s12, 0\n\t" \
        "v_mbcnt_hi_u32_b32 v160, s13, v160\n\t" \
        "s_bcnt1_i32_b64 s55, s[12:13]\n\t" \
        "v_lshl_add_u32 v160, v160, 3, s45\n\t" \
        "s_lshl3_add_u32 s45, s55, s45\n\t" \
        "v_cndmask_b32 v160, v66, v160, s[12:13]\n\t" \
        "ds_write2_b32 v160, v40, v50 offset1:1\n\t" \
        "v_cmp_nlt_f32 s[4:5], |v42|, v65\n\t" \
        "v_cmp_nlt_f16 s[8:9], |v42|, v60\n\t" \
        "s_or_b64 s[12:13], s[6:7], s[10:11]\n\t" \
        "v_mbcnt_lo_u32_b32 v161, s12, 0\n\t" \
        "v_mbcnt_hi_u32_b32 v161, s13, v161\n\t" \
        "s_add_u32 s57, s36, 0x73800\n\t" \
        "s_add_u32 m0, s38, 0xc980\n\t" \
        "s_nop 0\n\t" \
        "buffer_load_dwordx4 v13, s[16:19], s57 offen sc1 lds\n\t" \
        "s_bcnt1_i32_b64 s55, s[12:13]\n\t" \
        "v_lshl_add_u32 v161, v161, 3, s45\n\t" \
        "s_lshl3_add_u32 s45, s55, s45\n\t" \
        "v_cndmask_b32 v161, v66, v161, s[12:13]\n\t" \
        "ds_write2_b32 v161, v41, v51 offset1:1\n\t" \
        "v_cmp_nlt_f32 s[6:7], |v43|, v63\n\t" \
        "v_cmp_nlt_f16 s[10:11], |v43|, v61\n\t" \
        "s_or_b64 s[12:13], s[4:5], s[8:9]\n\t" \
        "v_mbcnt_lo_u32_b32 v160, s12, 0\n\t" \
        "v_mbcnt_hi_u32_b32 v160, s13, v160\n\t" \
        "s_bcnt1_i32_b64 s55, s[12:13]\n\t" \
        "v_lshl_add_u32 v160, v160, 3, s45\n\t" \
        "s_lshl3_add_u32 s45, s55, s45\n\t" \
        "v_cndmask_b32 v160, v66, v160, s[12:13]\n\t" \
        "ds_write2_b32 v160, v42, v52 offset1:1\n\t" \
        "v_cmp_nlt_f32 s[4:5], |v44|, v64\n\t" \
        "v_cmp_nlt_f16 s[8:9], |v44|, v61\n\t" \
        "s_or_b64 s[12:13], s[6:7], s[10:11]\n\t" \
        "v_mbcnt_lo_u32_b32 v161, s12, 0\n\t" \
        "v_mbcnt_hi_u32_b32 v161, s13, v161\n\t" \
        "s_add_u32 s57, s36, 0x7e000\n\t" \
        "s_min_u32 s57, s57, 0x925800\n\t" \
        "s_add_u32 m0, s38, 0xd900\n\t" \
        "s_nop 0\n\t" \
        "buffer_load_dwordx4 v13, s[16:19], s57 offen sc1 lds\n\t" \
        "s_bcnt1_i32_b64 s55, s[12:13]\n\t" \
        "v_lshl_add_u32 v161, v161, 3, s45\n\t" \
        "s_lshl3_add_u32 s45, s55, s45\n\t" \
        "v_cndmask_b32 v161, v66, v161, s[12:13]\n\t" \
        "ds_write2_b32 v161, v43, v53 offset1:1\n\t" \
        "v_cmp_nlt_f32 s[6:7], |v45|, v65\n\t" \
        "v_cmp_nlt_f16 s[10:11], |v45|, v61\n\t" \
        "s_or_b64 s[12:13], s[4:5], s[8:9]\n\t" \
        "v_mbcnt_lo_u32_b32 v160, s12, 0\n\t" \
        "v_mbcnt_hi_u32_b32 v160, s13, v160\n\t" \
        "s_bcnt1_i32_b64 s55, s[12:13]\n\t" \
        "v_lshl_add_u32 v160, v160, 3, s45\n\t" \
        "s_lshl3_add_u32 s45, s55, s45\n\t" \
        "v_cndmask_b32 v160, v66, v160, s[12:13]\n\t" \
        "ds_write2_b32 v160, v44, v54 offset1:1\n\t" \
        "v_cmp_nlt_f32 s[4:5], |v46|, v63\n\t" \
        "v_cmp_nlt_f16 s[8:9], |v46|, v62\n\t" \
        "s_or_b64 s[12:13], s[6:7], s[10:11]\n\t" \
        "v_mbcnt_lo_u32_b32 v161, s12, 0\n\t" \
        "v_mbcnt_hi_u32_b32 v161, s13, v161\n\t" \
        "s_add_u32 s57, s36, 0x88800\n\t" \
        "s_min_u32 s57, s57, 0x925800\n\t" \
        "s_add_u32 m0, s38, 0xe880\n\t" \
        "s_nop 0\n\t" \
        "buffer_load_dwordx4 v13, s[16:19], s57 offen sc1 lds\n\t" \
        "s_bcnt1_i32_b64 s55, s[12:13]\n\t" \
        "v_lshl_add_u32 v161, v161, 3, s45\n\t" \
        "s_lshl3_add_u32 s45, s55, s45\n\t" \
        "v_cndmask_b32 v161, v66, v161, s[12:13]\n\t" \
        "ds_write2_b32 v161, v45, v55 offset1:1\n\t" \
        "v_cmp_nlt_f32 s[6:7], |v47|, v64\n\t" \
        "v_cmp_nlt_f16 s[10:11], |v47|, v62\n\t" \
        "s_or_b64 s[12:13], s[4:5], s[8:9]\n\t" \
        "v_mbcnt_lo_u32_b32 v160, s12, 0\n\t" \
        "v_mbcnt_hi_u32_b32 v160, s13, v160\n\t" \
        "s_bcnt1_i32_b64 s55, s[12:13]\n\t" \
        "v_lshl_add_u32 v160, v160, 3, s45\n\t" \
        "s_lshl3_add_u32 s45, s55, s45\n\t" \
        "v_cndmask_b32 v160, v66, v160, s[12:13]\n\t" \
        "ds_write2_b32 v160, v46, v56 offset1:1\n\t" \
        "v_cmp_nlt_f32 s[4:5], |v48|, v65\n\t" \
        "v_cmp_nlt_f16 s[8:9], |v48|, v62\n\t" \
        "s_or_b64 s[12:13], s[6:7], s[10:11]\n\t" \
        "v_mbcnt_lo_u32_b32 v161, s12, 0\n\t" \
        "v_mbcnt_hi_u32_b32 v161, s13, v161\n\t" \
        "s_mov_b32 s57, s40\n\t" \
        "s_add_u32 m0, s39, 0x3000\n\t" \
        "s_nop 0\n\t" \
        "buffer_load_dwordx4 v3, s[24:27], s57 offen offset:0 lds\n\t" \
        "s_bcnt1_i32_b64 s55, s[12:13]\n\t" \
        "v_lshl_add_u32 v161, v161, 3, s45\n\t" \
        "s_lshl3_add_u32 s45, s55, s45\n\t" \
        "v_cndmask_b32 v161, v66, v161, s[12:13]\n\t" \
        "ds_write2_b32 v161, v47, v57 offset1:1\n\t" \
        "s_or_b64 s[12:13], s[4:5], s[8:9]\n\t" \
        "v_mbcnt_lo_u32_b32 v160, s12, 0\n\t" \
        "v_mbcnt_hi_u32_b32 v160, s13, v160\n\t" \
        "s_bcnt1_i32_b64 s55, s[12:13]\n\t" \
        "v_lshl_add_u32 v160, v160, 3, s45\n\t" \
        "s_lshl3_add_u32 s45, s55, s45\n\t" \
        "v_cndmask_b32 v160, v66, v160, s[12:13]\n\t" \
        "ds_write2_b32 v160, v48, v58 offset1:1\n\t" \
        "s_sub_u32 s46, s45, s48\n\t" \
        "s_lshr_b32 s46, s46, 3\n\t" \
        "s_cmp_gt_u32 s46, 64\n\t" \
        "s_cselect_b32 s54, 1, 0\n\t" \
        "s_cselect_b32 s46, 64, s46\n\t" \
        "buffer_load_dwordx4 v4, s[24:27], s57 offen offset:1024 lds\n\t" \
        "v_lshrrev_b32 v170, 4, v2\n\t" \
        "v_and_b32 v24, 15, v2\n\t" \
        "v_bfe_u32 v171, v2, 3, 1\n\t" \
        "v_lshl_add_u32 v171, v170, 2, v171\n\t" \
        "v_lshrrev_b32 v172, 1, v2\n\t" \
        "v_and_b32 v172, 2, v172\n\t" \
        "v_lshl_add_u32 v171, v171, 2, v172\n\t" \
        "s_add_u32 s55, s48, 512\n\t" \
        "v_add_u32 v20, s55, v171\n\t" \
        "v_lshl_add_u32 v21, v24, 3, s48\n\t" \
        "v_bfe_u32 v171, v2, 1, 1\n\t" \
        "v_xor_b32 v171, v171, v170\n\t" \
        "v_lshrrev_b32 v172, 2, v24\n\t" \
        "v_and_b32 v173, 2, v172\n\t" \
        "v_xor_b32 v171, v171, v173\n\t" \
        "v_and_b32 v173, 4, v2\n\t" \
        "v_lshlrev_b32 v171, 4, v171\n\t" \
        "v_lshl_add_u32 v171, v173, 1, v171\n\t" \
        "v_mul_i32_i24 v22, s53, v24\n\t" \
        "v_add3_u32 v22, v22, v171, s49\n\t" \
        "v_and_b32 v171, 3, v2\n\t" \
        "v_and_b32 v173, 1, v170\n\t" \
        "v_lshlrev_b32 v173, 3, v173\n\t" \
        "v_lshl_or_b32 v23, v171, 4, v173\n\t" \
        "v_bfe_u32 v172, v2, 2, 2\n\t" \
        "v_and_b32 v171, 3, v2\n\t" \
        "v_cmp_eq_u32 vcc, v170, v172\n\t" \
        "v_lshlrev_b32 v171, v171, 1\n\t" \
        "s_nop 0\n\t" \
        "v_cndmask_b32 v171, 0, v171, vcc\n\t" \
        "v_bfe_i32 v25, v171, 0, 1\n\t" \
        "v_bfe_i32 v26, v171, 1, 1\n\t" \
        "v_bfe_i32 v27, v171, 2, 1\n\t" \
        "v_bfe_i32 v28, v171, 3, 1\n\t" \
        "ds_read_b64 v[68:69], v31\n\t" \
        "v_cmp_gt_u32 s[12:13], s46, v2\n\t" \
        "buffer_load_dwordx4 v5, s[24:27], s57 offen offset:2048 lds\n\t" \
        "s_mov_b32 s14, 0x2\n\t" \
        "s_mov_b32 s15, 0x8\n\t" \
        "v_mov_b32 v83, 0x41980000\n\t" \
        "v_mov_b32 v29, 0x20002\n\t" \
        "s_waitcnt lgkmcnt(0)\n\t" \
        "v_lshlrev_b32 v70, 16, v68\n\t" \
        "v_and_b32 v71, 0xffff0000, v68\n\t" \
        "v_cvt_f32_ubyte2 v74, v69\n\t" \
        "v_cvt_f32_ubyte3 v75, v69\n\t" \
        "v_add_f32 v70, v70, v74\n\t" \
        "v_add_f32 v71, v71, v75\n\t" \
        "v_cvt_pk_bf16_f32 v68, v70, v71\n\t" \
        "v_pk_max_i16 v68, v68, 0\n\t" \
        "v_pk_min_i16 v68, v68, v39\n\t" \
        "v_and_b32 v69, 0xffff, v69\n\t" \
        "v_lshlrev_b32 v69, 2, v69\n\t" \
        "v_lshlrev_b32 v70, 16, v68\n\t" \
        "v_and_b32 v71, 0xffff0000, v68\n\t" \
        "v_cndmask_b32 v69, v66, v69, s[12:13]\n\t" \
        "ds_write_b32 v69, v30\n\t" \
        "v_fract_f32 v72, v70\n\t" \
        "v_fract_f32 v73, v71\n\t" \
        "v_sub_f32 v70, v70, v34\n\t" \
        "v_sub_f32 v71, v71, v35\n\t" \
        "v_sub_f32 v74, 1.0, v72\n\t" \
        "v_sub_f32 v75, 1.0, v73\n\t" \
        "v_med3_f32 v78, v70, 0, v33\n\t" \
        "v_cvt_pk_bf16_f32 v74, v74, v75\n\t" \
        "v_med3_f32 v79, v71, 0, v83\n\t" \
        "v_cmp_neq_f32 s[4:5], v78, v70\n\t" \
        "v_and_b32 v75, 0xffff0000, v74\n\t" \
        "v_cmp_neq_f32 s[6:7], v79, v71\n\t" \
        "v_lshlrev_b32 v74, 16, v74\n\t" \
        "v_cvt_u32_f32 v78, v78\n\t" \
        "v_cvt_u32_f32 v79, v79\n\t" \
        "s_or_b64 s[4:5], s[4:5], s[6:7]\n\t" \
        "v_mul_f32 v76, v74, v75\n\t" \
        "s_add_u32 s57, s40, 0x3000\n\t" \
        "s_add_u32 m0, s39, 0x6000\n\t" \
        "s_nop 0\n\t" \
        "buffer_load_dwordx4 v3, s[24:27], s57 offen offset:0 lds\n\t" \
        "v_mul_f32 v74, v74, v73\n\t" \
        "s_and_b64 s[4:5], s[4:5], s[12:13]\n\t" \
        "s_cselect_b32 s54, 1, s54\n\t" \
        "v_mul_f32 v77, v72, v75\n\t" \
        "v_mul_f32 v75, v72, v73\n\t" \
        "v_cmp_neq_f32 s[4:5], 0, v73\n\t" \
        "v_cmp_neq_f32 s[6:7], 0, v72\n\t" \
        "v_cvt_pk_bf16_f32 v76, v76, v74\n\t" \
        "v_cvt_pk_bf16_f32 v77, v77, v75\n\t" \
        "v_mul_u32_u24 v80, 124, v78\n\t" \
        "v_add_u32 v83, 1, v79\n\t" \
        "v_mad_u32_u24 v80, v79, 6, v80\n\t" \
        "v_bfe_u32 v84, v79, 2, 1\n\t" \
        "v_bfe_u32 v83, v83, 2, 1\n\t" \
        "s_and_b64 s[50:51], s[4:5], s[6:7]\n\t" \
        "v_add3_u32 v81, v80, v84, s14\n\t" \
        "v_add3_u32 v82, v80, v83, s15\n\t" \
        "s_and_b64 s[50:51], s[50:51], s[12:13]\n\t" \
        "v_cndmask_b32 v83, 0, v36, s[6:7]\n\t" \
        "v_perm_b32 v74, v77, v76, v37\n\t" \
        "v_add_u32 v83, v81, v83\n\t" \
        "v_cndmask_b32 v68, v74, v76, s[4:5]\n\t" \
        "v_cndmask_b32 v83, v83, v82, s[4:5]\n\t" \
        "v_cndmask_b32 v75, 0, v38, s[50:51]\n\t" \
        "v_lshl_or_b32 v81, v83, 16, v81\n\t" \
        "v_cndmask_b32 v69, 0, v77, s[50:51]\n\t" \
        "v_or_b32 v81, v81, v75\n\t" \
        "ds_write_b64 v31, v[68:69]\n\t" \
        "v_cndmask_b32 v81, v29, v81, s[12:13]\n\t" \
        "ds_write_b32 v32, v81\n\t" \
        "s_lshl_b32 s55, s35, 2\n\t" \
        "s_cmp_lg_u32 s54, 0\n\t" \
        "v_mov_b32 v162, s55\n\t" \
        "s_cselect_b32 s55, 0x10000, s46\n\t" \
        "v_mov_b32 v163, s55\n\t" \
        "ds_write_b32 v162, v163\n\t" \
        "buffer_load_dwordx4 v4, s[24:27], s57 offen offset:1024 lds\n\t" \
        "s_and_b32 s61, s35, 1\n\t" \
        "v_lshrrev_b32 v170, 4, v2\n\t" \
        "v_and_b32 v171, 15, v2\n\t" \
        "v_and_b32 v172, 16, v2\n\t" \
        "s_lshr_b32 s62, s35, 1\n\t" \
        "v_lshlrev_b32 v172, 1, v172\n\t" \
        "v_bfe_u32 v173, v2, 2, 2\n\t" \
        "v_mul_u32_u24 v175, 0xc0, v171\n\t" \
        "v_lshl_or_b32 v173, v170, 3, v173\n\t" \
        "v_and_b32 v174, 3, v2\n\t" \
        "v_mul_u32_u24 v173, 0xc0, v173\n\t" \
        "s_lshl_b32 s63, s61, 7\n\t" \
        "v_lshl_add_u32 v173, v174, 3, v173\n\t" \
        "s_add_u32 s63, s63, 0xf840\n\t" \
        "v_add3_u32 v6, v173, v172, s63\n\t" \
        "s_lshl_b32 s63, s61, 5\n\t" \
        "v_xor_b32 v174, s63, v172\n\t" \
        "v_xor_b32 v7, 32, v6\n\t" \
        "v_add_u32 v173, 0xf880, v173\n\t" \
        "s_lshl_b32 s63, s61, 7\n\t" \
        "v_add_u32 v8, v173, v174\n\t" \
        "v_lshrrev_b32 v173, 1, v2\n\t" \
        "buffer_load_dwordx4 v5, s[24:27], s57 offen offset:2048 lds\n\t" \
        "v_and_b32 v173, 16, v173\n\t" \
        "v_add_u32 v175, v175, v173\n\t" \
        "v_bfe_u32 v173, v2, 3, 1\n\t" \
        "v_add3_u32 v9, v175, v172, s63\n\t" \
        "v_cmp_eq_u32 vcc, s61, v173\n\t" \
        "v_add3_u32 v10, v175, v174, 64\n\t" \
        "v_bfrev_b32 v174, 1\n\t" \
        "s_lshl_b32 s63, s35, 9\n\t" \
        "v_cndmask_b32 v10, v174, v10, vcc\n\t" \
        "s_add_u32 s63, s63, 0xf840\n\t" \
        "v_lshl_add_u32 v11, v2, 3, s63\n\t" \
        "s_lshl_b32 s63, s61, 10\n\t" \
        "s_sub_u32 s63, 0x200, s63\n\t" \
        "v_add_u32 v12, s63, v11\n\t" \
        "s_add_u32 s64, s32, s62\n\t" \
        "s_mul_i32 s63, s33, 0xc0\n\t" \
        "s_mul_i32 s64, s64, 0xa800\n\t" \
        "s_add_u32 s64, s64, s63\n\t" \
        "v_and_b32 v170, 15, v2\n\t" \
        "s_mul_i32 s62, s62, 2304\n\t" \
        "s_add_u32 s62, s62, 124992\n\t" \
        "v_lshl_add_u32 v15, v170, 4, s62\n\t" \
        "ds_read_u16 v120, v20\n\t" \
        "ds_read_u16 v121, v20 offset:8\n\t" \
        "ds_read_b32 v122, v21\n\t" \
        "v_mov_b32 v156, 0\n\t" \
        "s_mov_b32 s47, 0\n\t" \
        "s_waitcnt lgkmcnt(0)\n\t" \
        "v_lshlrev_b32 v116, 5, v120\n\t" \
        "v_bitop3_b32 v116, v116, s52, v23 bitop3:0x6a\n\t" \
        "v_xor_b32 v117, 8, v116\n\t" \
        "v_lshlrev_b32 v118, 5, v121\n\t" \
        "v_bitop3_b32 v118, v118, s52, v23 bitop3:0x6a\n\t" \
        "v_xor_b32 v119, 8, v118\n\t" \
        "v_and_b32 v112, v122, v25\n\t" \
        "v_and_b32 v113, v122, v26\n\t" \
        "v_and_b32 v114, v122, v27\n\t" \
        "v_and_b32 v115, v122, v28\n\t" \
        "ds_read_u16 v120, v20 offset:64\n\t" \
        "ds_read_u16 v121, v20 offset:72\n\t" \
        "ds_read_b32 v122, v21 offset:128\n\t" \
        "s_waitcnt vmcnt(6) lgkmcnt(0)\n\t" \
        "s_barrier\n\t" \
        "ds_read_b128 v[152:155], v156\n\t" \
        "ds_read_b64_tr_b16 v[64:65], v116 offset:0\n\t" \
        "ds_read_b64_tr_b16 v[66:67], v118 offset:0\n\t" \
        "ds_read_b64_tr_b16 v[68:69], v117 offset:0\n\t" \
        "ds_read_b64_tr_b16 v[70:71], v119 offset:0\n\t" \
        "ds_read_b64_tr_b16 v[72:73], v116 offset:64\n\t" \
        "ds_read_b64_tr_b16 v[74:75], v118 offset:64\n\t" \
        "ds_read_b64_tr_b16 v[76:77], v117 offset:64\n\t" \
        "ds_read_b64_tr_b16 v[78:79], v119 offset:64\n\t" \
        "ds_read_b64_tr_b16 v[80:81], v116 offset:128\n\t" \
        "ds_read_b64_tr_b16 v[82:83], v118 offset:128\n\t" \
        "ds_read_b64_tr_b16 v[84:85], v117 offset:128\n\t" \
        "ds_read_b64_tr_b16 v[86:87], v119 offset:128\n\t" \
        "s_add_u32 s57, s40, 0x6000\n\t" \
        "s_add_u32 m0, s39, 0x9000\n\t" \
        "s_nop 0\n\t" \
        "buffer_load_dwordx4 v3, s[24:27], s57 offen offset:0 lds\n\t" \
        "buffer_load_dwordx4 v4, s[24:27], s57 offen offset:1024 lds\n\t" \
        "buffer_load_dwordx4 v5, s[24:27], s57 offen offset:2048 lds\n\t" \
        "s_waitcnt lgkmcnt(12)\n\t" \
        "v_add_u32 v152, v152, v154\n\t" \
        "v_add_u32 v153, v153, v155\n\t" \
        "v_max_u32 v152, v152, v153\n\t" \
        "s_nop 0\n\t" \
        "v_readfirstlane_b32 s55, v152\n\t" \
        "s_nop 0\n\t" \
        "s_cmp_gt_u32 s55, 66\n\t" \
        "s_cbranch_scc1 .Lfail%=\n\t" \
        "s_cmp_gt_u32 s46, 16\n\t" \
        "s_cbranch_scc1 .LTeprod%=\n\t" \
        ".LTelast%=:\n\t" \
        "s_waitcnt lgkmcnt(10)\n\t" \
        "v_mfma_f32_16x16x32_bf16 v[88:91], v[64:67], v[112:115], 0\n\t" \
        "s_waitcnt lgkmcnt(8)\n\t" \
        "v_mfma_f32_16x16x32_bf16 v[92:95], v[68:71], v[112:115], 0\n\t" \
        "s_waitcnt lgkmcnt(6)\n\t" \
        "v_mfma_f32_16x16x32_bf16 v[96:99], v[72:75], v[112:115], 0\n\t" \
        "s_waitcnt lgkmcnt(4)\n\t" \
        "v_mfma_f32_16x16x32_bf16 v[100:103], v[76:79], v[112:115], 0\n\t" \
        "s_waitcnt lgkmcnt(2)\n\t" \
        "v_mfma_f32_16x16x32_bf16 v[104:107], v[80:83], v[112:115], 0\n\t" \
        "s_waitcnt lgkmcnt(0)\n\t" \
        "v_mfma_f32_16x16x32_bf16 v[108:111], v[84:87], v[112:115], 0\n\t" \
        "s_and_b32 s55, s50, 0xffff\n\t" \
        "s_cbranch_scc1 .LTetwo2%=\n\t" \
        ".LTeback2%=:\n\t" \
        "s_sub_u32 s55, s46, s47\n\t" \
        "v_xor_b32 v156, 8, v22\n\t" \
        "v_cmp_gt_i32 vcc, s55, v24\n\t" \
        "s_mov_b64 exec, vcc\n\t" \
        "v_cvt_pk_bf16_f32 v88, v88, v89\n\t" \
        "v_cvt_pk_bf16_f32 v89, v90, v91\n\t" \
        "ds_write_b64 v22, v[88:89] offset:0\n\t" \
        "v_cvt_pk_bf16_f32 v92, v92, v93\n\t" \
        "v_cvt_pk_bf16_f32 v93, v94, v95\n\t" \
        "ds_write_b64 v156, v[92:93] offset:0\n\t" \
        "v_cvt_pk_bf16_f32 v96, v96, v97\n\t" \
        "v_cvt_pk_bf16_f32 v97, v98, v99\n\t" \
        "ds_write_b64 v22, v[96:97] offset:64\n\t" \
        "v_cvt_pk_bf16_f32 v100, v100, v101\n\t" \
        "v_cvt_pk_bf16_f32 v101, v102, v103\n\t" \
        "ds_write_b64 v156, v[100:101] offset:64\n\t" \
        "v_cvt_pk_bf16_f32 v104, v104, v105\n\t" \
        "v_cvt_pk_bf16_f32 v105, v106, v107\n\t" \
        "ds_write_b64 v22, v[104:105] offset:128\n\t" \
        "v_cvt_pk_bf16_f32 v108, v108, v109\n\t" \
        "v_cvt_pk_bf16_f32 v109, v110, v111\n\t" \
        "ds_write_b64 v156, v[108:109] offset:128\n\t" \
        "s_mov_b64 exec, -1\n\t" \
        "s_waitcnt vmcnt(6) lgkmcnt(0)\n\t" \
        "s_barrier\n\t" \
        "ds_read_b128 v[148:151], v15 offset:0\n\t" \
        "s_waitcnt lgkmcnt(0)\n\t" \
        "v_xor_b32 v148, v148, v14\n\t" \
        "ds_read_b128 v[116:119], v148 offset:0\n\t" \
        "ds_read_b64_tr_b16 a[0:1], v6 offset:12288\n\t" \
        "ds_read_b64_tr_b16 a[2:3], v6 offset:13056\n\t" \
        "v_xor_b32 v149, v149, v14\n\t" \
        "ds_read_b64_tr_b16 a[4:5], v7 offset:12288\n\t" \
        "ds_read_b64_tr_b16 a[6:7], v7 offset:13056\n\t" \
        "ds_read_b64_tr_b16 a[8:9], v8 offset:12288\n\t" \
        "ds_read_b64_tr_b16 a[10:11], v8 offset:13056\n\t" \
        "ds_read_b128 v[120:123], v149 offset:0\n\t" \
        "v_xor_b32 v150, v150, v14\n\t" \
        "ds_read_b128 v[124:127], v150 offset:0\n\t" \
        "v_xor_b32 v151, v151, v14\n\t" \
        "ds_read_b128 v[128:131], v151 offset:0\n\t" \
        "s_add_u32 s57, s40, 0x9000\n\t" \
        "s_add_u32 m0, s39, 0xc000\n\t" \
        "s_waitcnt lgkmcnt(7)\n\t" \
        "v_mfma_f32_16x16x32_bf16 v[32:35], a[0:3], v[116:119], 0\n\t" \
        "buffer_load_dwordx4 v3, s[24:27], s57 offen offset:0 lds\n\t" \
        "s_waitcnt lgkmcnt(5)\n\t" \
        "v_mfma_f32_16x16x32_bf16 v[36:39], a[4:7], v[116:119], 0\n\t" \
        "ds_read_b128 v[132:135], v148 offset:64\n\t" \
        "s_waitcnt lgkmcnt(4)\n\t" \
        "v_mfma_f32_16x16x32_bf16 v[40:43], a[8:11], v[116:119], 0\n\t" \
        "ds_read_b64_tr_b16 a[12:13], v6 offset:18432\n\t" \
        "s_waitcnt lgkmcnt(4)\n\t" \
        "v_mfma_f32_16x16x32_bf16 v[44:47], a[0:3], v[120:123], 0\n\t" \
        "ds_read_b64_tr_b16 a[14:15], v6 offset:19200\n\t" \
        "v_mfma_f32_16x16x32_bf16 v[48:51], a[4:7], v[120:123], 0\n\t" \
        "buffer_load_dwordx4 v4, s[24:27], s57 offen offset:1024 lds\n\t" \
        "v_mfma_f32_16x16x32_bf16 v[52:55], a[8:11], v[120:123], 0\n\t" \
        "ds_read_b64_tr_b16 a[16:17], v7 offset:18432\n\t" \
        "s_waitcnt lgkmcnt(5)\n\t" \
        "v_mfma_f32_16x16x32_bf16 v[56:59], a[0:3], v[124:127], 0\n\t" \
        "ds_read_b64_tr_b16 a[18:19], v7 offset:19200\n\t" \
        "ds_read_b64_tr_b16 a[20:21], v8 offset:18432\n\t" \
        "v_mfma_f32_16x16x32_bf16 v[60:63], a[4:7], v[124:127], 0\n\t" \
        "ds_read_b64_tr_b16 a[22:23], v8 offset:19200\n\t" \
        "v_mfma_f32_16x16x32_bf16 v[64:67], a[8:11], v[124:127], 0\n\t" \
        "ds_read_b128 v[136:139], v149 offset:64\n\t" \
        "s_waitcnt lgkmcnt(8)\n\t" \
        "v_mfma_f32_16x16x32_bf16 v[68:71], a[0:3], v[128:131], 0\n\t" \
        "buffer_load_dwordx4 v5, s[24:27], s57 offen offset:2048 lds\n\t" \
        "v_mfma_f32_16x16x32_bf16 v[72:75], a[4:7], v[128:131], 0\n\t" \
        "ds_read_b128 v[140:143], v150 offset:64\n\t" \
        "s_add_u32 s57, s40, 0xc000\n\t" \
        "s_mov_b32 m0, s39\n\t" \
        "v_mfma_f32_16x16x32_bf16 v[76:79], a[8:11], v[128:131], 0\n\t" \
        "s_waitcnt vmcnt(6) lgkmcnt(2)\n\t" \
        "s_barrier\n\t" \
        "ds_read_b128 v[144:147], v151 offset:64\n\t" \
        "v_mfma_f32_16x16x32_bf16 v[32:35], a[12:15], v[132:135], v[32:35]\n\t" \
        "buffer_load_dwordx4 v3, s[24:27], s57 offen offset:0 lds\n\t" \
        "v_mfma_f32_16x16x32_bf16 v[36:39], a[16:19], v[132:135], v[36:39]\n\t" \
        "ds_read_b128 v[116:119], v148 offset:128\n\t" \
        "v_mfma_f32_16x16x32_bf16 v[40:43], a[20:23], v[132:135], v[40:43]\n\t" \
        "ds_read_b128 v[152:155], v15 offset:256\n\t" \
        "s_waitcnt lgkmcnt(4)\n\t" \
        "v_mfma_f32_16x16x32_bf16 v[44:47], a[12:15], v[136:139], v[44:47]\n\t" \
        "ds_read_b64_tr_b16 a[24:25], v6 offset:24576\n\t" \
        "v_mfma_f32_16x16x32_bf16 v[48:51], a[16:19], v[136:139], v[48:51]\n\t" \
        "buffer_load_dwordx4 v4, s[24:27], s57 offen offset:1024 lds\n\t" \
        "v_mfma_f32_16x16x32_bf16 v[52:55], a[20:23], v[136:139], v[52:55]\n\t" \
        "ds_read_b64_tr_b16 a[26:27], v6 offset:25344\n\t" \
        "ds_read_b64_tr_b16 a[28:29], v7 offset:24576\n\t" \
        "s_waitcnt lgkmcnt(6)\n\t" \
        "v_mfma_f32_16x16x32_bf16 v[56:59], a[12:15], v[140:143], v[56:59]\n\t" \
        "ds_read_b64_tr_b16 a[30:31], v7 offset:25344\n\t" \
        "ds_read_b64_tr_b16 a[32:33], v8 offset:24576\n\t" \
        "v_mfma_f32_16x16x32_bf16 v[60:63], a[16:19], v[140:143], v[60:63]\n\t" \
        "ds_read_b64_tr_b16 a[34:35], v8 offset:25344\n\t" \
        "v_mfma_f32_16x16x32_bf16 v[64:67], a[20:23], v[140:143], v[64:67]\n\t" \
        "ds_read_b128 v[120:123], v149 offset:128\n\t" \
        "s_waitcnt lgkmcnt(7)\n\t" \
        "v_xor_b32 v152, v152, v14\n\t" \
        "v_mfma_f32_16x16x32_bf16 v[68:71], a[12:15], v[144:147], v[68:71]\n\t" \
        "buffer_load_dwordx4 v5, s[24:27], s57 offen offset:2048 lds\n\t" \
        "v_mfma_f32_16x16x32_bf16 v[72:75], a[16:19], v[144:147], v[72:75]\n\t" \
        "ds_read_b128 v[124:127], v150 offset:128\n\t" \
        "s_add_u32 s57, s40, 0xf000\n\t" \
        "s_add_u32 m0, s39, 0x3000\n\t" \
        "v_xor_b32 v153, v153, v14\n\t" \
        "v_mfma_f32_16x16x32_bf16 v[76:79], a[20:23], v[144:147], v[76:79]\n\t" \
        "ds_read_b128 v[128:131], v151 offset:128\n\t" \
        "v_xor_b32 v154, v154, v14\n\t" \
        "s_waitcnt lgkmcnt(7)\n\t" \
        "v_mfma_f32_16x16x32_bf16 v[32:35], a[24:27], v[116:119], v[32:35]\n\t" \
        "buffer_load_dwordx4 v3, s[24:27], s57 offen offset:0 lds\n\t" \
        "s_waitcnt lgkmcnt(5)\n\t" \
        "v_mfma_f32_16x16x32_bf16 v[36:39], a[28:31], v[116:119], v[36:39]\n\t" \
        "ds_read_b128 v[132:135], v152 offset:0\n\t" \
        "v_xor_b32 v155, v155, v14\n\t" \
        "s_waitcnt lgkmcnt(4)\n\t" \
        "v_mfma_f32_16x16x32_bf16 v[40:43], a[32:35], v[116:119], v[40:43]\n\t" \
        "ds_read_b64_tr_b16 a[36:37], v6 offset:30720\n\t" \
        "s_waitcnt lgkmcnt(4)\n\t" \
        "v_mfma_f32_16x16x32_bf16 v[44:47], a[24:27], v[120:123], v[44:47]\n\t" \
        "ds_read_b64_tr_b16 a[38:39], v6 offset:31488\n\t" \
        "v_mfma_f32_16x16x32_bf16 v[48:51], a[28:31], v[120:123], v[48:51]\n\t" \
        "buffer_load_dwordx4 v4, s[24:27], s57 offen offset:1024 lds\n\t" \
        "v_mfma_f32_16x16x32_bf16 v[52:55], a[32:35], v[120:123], v[52:55]\n\t" \
        "ds_read_b64_tr_b16 a[40:41], v7 offset:30720\n\t" \
        "s_waitcnt lgkmcnt(5)\n\t" \
        "v_mfma_f32_16x16x32_bf16 v[56:59], a[24:27], v[124:127], v[56:59]\n\t" \
        "ds_read_b64_tr_b16 a[42:43], v7 offset:31488\n\t" \
        "ds_read_b64_tr_b16 a[44:45], v8 offset:30720\n\t" \
        "v_mfma_f32_16x16x32_bf16 v[60:63], a[28:31], v[124:127], v[60:63]\n\t" \
        "ds_read_b64_tr_b16 a[46:47], v8 offset:31488\n\t" \
        "v_mfma_f32_16x16x32_bf16 v[64:67], a[32:35], v[124:127], v[64:67]\n\t" \
        "ds_read_b128 v[136:139], v153 offset:0\n\t" \
        "s_waitcnt lgkmcnt(8)\n\t" \
        "v_mfma_f32_16x16x32_bf16 v[68:71], a[24:27], v[128:131], v[68:71]\n\t" \
        "buffer_load_dwordx4 v5, s[24:27], s57 offen offset:2048 lds\n\t" \
        "v_mfma_f32_16x16x32_bf16 v[72:75], a[28:31], v[128:131], v[72:75]\n\t" \
        "ds_read_b128 v[140:143], v154 offset:0\n\t" \
        "v_mfma_f32_16x16x32_bf16 v[76:79], a[32:35], v[128:131], v[76:79]\n\t" \
        "s_waitcnt vmcnt(9) lgkmcnt(2)\n\t" \
        "s_barrier\n\t" \
        "ds_read_b128 v[144:147], v155 offset:0\n\t" \
        "s_add_u32 s57, s40, 0x12000\n\t" \
        "s_add_u32 m0, s39, 0x6000\n\t" \
        "v_mfma_f32_16x16x32_bf16 v[32:35], a[36:39], v[132:135], v[32:35]\n\t" \
        "ds_read_b128 v[116:119], v152 offset:64\n\t" \
        "v_mfma_f32_16x16x32_bf16 v[36:39], a[40:43], v[132:135], v[36:39]\n\t" \
        "buffer_load_dwordx4 v3, s[24:27], s57 offen offset:0 lds\n\t" \
        "v_mfma_f32_16x16x32_bf16 v[40:43], a[44:47], v[132:135], v[40:43]\n\t" \
        "ds_read_b64_tr_b16 a[48:49], v6 offset:36864\n\t" \
        "s_waitcnt lgkmcnt(4)\n\t" \
        "v_mfma_f32_16x16x32_bf16 v[44:47], a[36:39], v[136:139], v[44:47]\n\t" \
        "ds_read_b64_tr_b16 a[50:51], v6 offset:37632\n\t" \
        "v_mfma_f32_16x16x32_bf16 v[48:51], a[40:43], v[136:139], v[48:51]\n\t" \
        "ds_read_b64_tr_b16 a[52:53], v7 offset:36864\n\t" \
        "v_mfma_f32_16x16x32_bf16 v[52:55], a[44:47], v[136:139], v[52:55]\n\t" \
        "buffer_load_dwordx4 v4, s[24:27], s57 offen offset:1024 lds\n\t" \
        "s_waitcnt lgkmcnt(5)\n\t" \
        "v_mfma_f32_16x16x32_bf16 v[56:59], a[36:39], v[140:143], v[56:59]\n\t" \
        "ds_read_b64_tr_b16 a[54:55], v7 offset:37632\n\t" \
        "ds_read_b64_tr_b16 a[56:57], v8 offset:36864\n\t" \
        "v_mfma_f32_16x16x32_bf16 v[60:63], a[40:43], v[140:143], v[60:63]\n\t" \
        "ds_read_b64_tr_b16 a[58:59], v8 offset:37632\n\t" \
        "v_mfma_f32_16x16x32_bf16 v[64:67], a[44:47], v[140:143], v[64:67]\n\t" \
        "ds_read_b128 v[120:123], v153 offset:64\n\t" \
        "s_waitcnt lgkmcnt(8)\n\t" \
        "v_mfma_f32_16x16x32_bf16 v[68:71], a[36:39], v[144:147], v[68:71]\n\t" \
        "buffer_load_dwordx4 v5, s[24:27], s57 offen offset:2048 lds\n\t" \
        "v_mfma_f32_16x16x32_bf16 v[72:75], a[40:43], v[144:147], v[72:75]\n\t" \
        "ds_read_b128 v[124:127], v154 offset:64\n\t" \
        "v_mfma_f32_16x16x32_bf16 v[76:79], a[44:47], v[144:147], v[76:79]\n\t" \
        "ds_read_b128 v[128:131], v155 offset:64\n\t" \
        "s_waitcnt lgkmcnt(7)\n\t" \
        "v_mfma_f32_16x16x32_bf16 v[32:35], a[48:51], v[116:119], v[32:35]\n\t" \
        "ds_read_b128 v[132:135], v152 offset:128\n\t" \
        "s_waitcnt lgkmcnt(6)\n\t" \
        "v_mfma_f32_16x16x32_bf16 v[36:39], a[52:55], v[116:119], v[36:39]\n\t" \
        "ds_read_b128 v[156:159], v15 offset:512\n\t" \
        "s_waitcnt lgkmcnt(5)\n\t" \
        "v_mfma_f32_16x16x32_bf16 v[40:43], a[56:59], v[116:119], v[40:43]\n\t" \
        "ds_read_b64_tr_b16 a[60:61], v6 offset:43008\n\t" \
        "s_waitcnt lgkmcnt(5)\n\t" \
        "v_mfma_f32_16x16x32_bf16 v[44:47], a[48:51], v[120:123], v[44:47]\n\t" \
        "ds_read_b64_tr_b16 a[62:63], v6 offset:43776\n\t" \
        "v_mfma_f32_16x16x32_bf16 v[48:51], a[52:55], v[120:123], v[48:51]\n\t" \
        "ds_read_b64_tr_b16 a[64:65], v7 offset:43008\n\t" \
        "v_mfma_f32_16x16x32_bf16 v[52:55], a[56:59], v[120:123], v[52:55]\n\t" \
        "ds_read_b64_tr_b16 a[66:67], v7 offset:43776\n\t" \
        "s_waitcnt lgkmcnt(7)\n\t" \
        "v_mfma_f32_16x16x32_bf16 v[56:59], a[48:51], v[124:127], v[56:59]\n\t" \
        "ds_read_b64_tr_b16 a[68:69], v8 offset:43008\n\t" \
        "v_mfma_f32_16x16x32_bf16 v[60:63], a[52:55], v[124:127], v[60:63]\n\t" \
        "ds_read_b64_tr_b16 a[70:71], v8 offset:43776\n\t" \
        "s_waitcnt lgkmcnt(6)\n\t" \
        "v_xor_b32 v156, v156, v14\n\t" \
        "v_mfma_f32_16x16x32_bf16 v[64:67], a[56:59], v[124:127], v[64:67]\n\t" \
        "ds_read_b128 v[136:139], v153 offset:128\n\t" \
        "v_xor_b32 v157, v157, v14\n\t" \
        "v_mfma_f32_16x16x32_bf16 v[68:71], a[48:51], v[128:131], v[68:71]\n\t" \
        "ds_read_b128 v[140:143], v154 offset:128\n\t" \
        "v_xor_b32 v158, v158, v14\n\t" \
        "v_mfma_f32_16x16x32_bf16 v[72:75], a[52:55], v[128:131], v[72:75]\n\t" \
        "ds_read_b128 v[144:147], v155 offset:128\n\t" \
        "v_xor_b32 v159, v159, v14\n\t" \
        "v_mfma_f32_16x16x32_bf16 v[76:79], a[56:59], v[128:131], v[76:79]\n\t" \
        "s_waitcnt vmcnt(9) lgkmcnt(3)\n\t" \
        "s_barrier\n\t" \
        "ds_read_b128 v[116:119], v156 offset:0\n\t" \
        "s_add_u32 s57, s40, 0x15000\n\t" \
        "s_add_u32 m0, s39, 0x9000\n\t" \
        "v_mfma_f32_16x16x32_bf16 v[32:35], a[60:63], v[132:135], v[32:35]\n\t" \
        "ds_read_b64_tr_b16 a[72:73], v6 offset:49152\n\t" \
        "v_mfma_f32_16x16x32_bf16 v[36:39], a[64:67], v[132:135], v[36:39]\n\t" \
        "buffer_load_dwordx4 v3, s[24:27], s57 offen offset:0 lds\n\t" \
        "v_mfma_f32_16x16x32_bf16 v[40:43], a[68:71], v[132:135], v[40:43]\n\t" \
        "ds_read_b64_tr_b16 a[74:75], v6 offset:49920\n\t" \
        "s_waitcnt lgkmcnt(5)\n\t" \
        "v_mfma_f32_16x16x32_bf16 v[44:47], a[60:63], v[136:139], v[44:47]\n\t" \
        "ds_read_b64_tr_b16 a[76:77], v7 offset:49152\n\t" \
        "v_mfma_f32_16x16x32_bf16 v[48:51], a[64:67], v[136:139], v[48:51]\n\t" \
        "ds_read_b64_tr_b16 a[78:79], v7 offset:49920\n\t" \
        "v_mfma_f32_16x16x32_bf16 v[52:55], a[68:71], v[136:139], v[52:55]\n\t" \
        "ds_read_b64_tr_b16 a[80:81], v8 offset:49152\n\t" \
        "s_waitcnt lgkmcnt(7)\n\t" \
        "v_mfma_f32_16x16x32_bf16 v[56:59], a[60:63], v[140:143], v[56:59]\n\t" \
        "ds_read_b64_tr_b16 a[82:83], v8 offset:49920\n\t" \
        "v_mfma_f32_16x16x32_bf16 v[60:63], a[64:67], v[140:143], v[60:63]\n\t" \
        "buffer_load_dwordx4 v4, s[24:27], s57 offen offset:1024 lds\n\t" \
        "v_mfma_f32_16x16x32_bf16 v[64:67], a[68:71], v[140:143], v[64:67]\n\t" \
        "ds_read_b128 v[120:123], v157 offset:0\n\t" \
        "s_waitcnt lgkmcnt(8)\n\t" \
        "v_mfma_f32_16x16x32_bf16 v[68:71], a[60:63], v[144:147], v[68:71]\n\t" \
        "ds_read_b128 v[124:127], v158 offset:0\n\t" \
        "v_mfma_f32_16x16x32_bf16 v[72:75], a[64:67], v[144:147], v[72:75]\n\t" \
        "ds_read_b128 v[128:131], v159 offset:0\n\t" \
        "v_mfma_f32_16x16x32_bf16 v[76:79], a[68:71], v[144:147], v[76:79]\n\t" \
        "ds_read_b128 v[132:135], v156 offset:64\n\t" \
        "s_waitcnt lgkmcnt(8)\n\t" \
        "v_mfma_f32_16x16x32_bf16 v[32:35], a[72:75], v[116:119], v[32:35]\n\t" \
        "ds_read_b64_tr_b16 a[84:85], v6 offset:55296\n\t" \
        "s_waitcnt lgkmcnt(7)\n\t" \
        "v_mfma_f32_16x16x32_bf16 v[36:39], a[76:79], v[116:119], v[36:39]\n\t" \
        "buffer_load_dwordx4 v5, s[24:27], s57 offen offset:2048 lds\n\t" \
        "s_waitcnt lgkmcnt(5)\n\t" \
        "v_mfma_f32_16x16x32_bf16 v[40:43], a[80:83], v[116:119], v[40:43]\n\t" \
        "ds_read_b64_tr_b16 a[86:87], v6 offset:56064\n\t" \
        "s_waitcnt lgkmcnt(5)\n\t" \
        "v_mfma_f32_16x16x32_bf16 v[44:47], a[72:75], v[120:123], v[44:47]\n\t" \
        "ds_read_b64_tr_b16 a[88:89], v7 offset:55296\n\t" \
        "v_mfma_f32_16x16x32_bf16 v[48:51], a[76:79], v[120:123], v[48:51]\n\t" \
        "ds_read_b64_tr_b16 a[90:91], v7 offset:56064\n\t" \
        "v_mfma_f32_16x16x32_bf16 v[52:55], a[80:83], v[120:123], v[52:55]\n\t" \
        "ds_read_b64_tr_b16 a[92:93], v8 offset:55296\n\t" \
        "s_waitcnt lgkmcnt(7)\n\t" \
        "v_mfma_f32_16x16x32_bf16 v[56:59], a[72:75], v[124:127], v[56:59]\n\t" \
        "ds_read_b64_tr_b16 a[94:95], v8 offset:56064\n\t" \
        "v_mfma_f32_16x16x32_bf16 v[60:63], a[76:79], v[124:127], v[60:63]\n\t" \
        "ds_read_b128 v[136:139], v157 offset:64\n\t" \
        "v_mfma_f32_16x16x32_bf16 v[64:67], a[80:83], v[124:127], v[64:67]\n\t" \
        "ds_read_b128 v[140:143], v158 offset:64\n\t" \
        "s_waitcnt lgkmcnt(9)\n\t" \
        "v_mfma_f32_16x16x32_bf16 v[68:71], a[72:75], v[128:131], v[68:71]\n\t" \
        "ds_read_b128 v[144:147], v159 offset:64\n\t" \
        "v_mfma_f32_16x16x32_bf16 v[72:75], a[76:79], v[128:131], v[72:75]\n\t" \
        "ds_read_b128 v[116:119], v156 offset:128\n\t" \
        "v_mfma_f32_16x16x32_bf16 v[76:79], a[80:83], v[128:131], v[76:79]\n\t" \
        "s_waitcnt vmcnt(9) lgkmcnt(4)\n\t" \
        "s_barrier\n\t" \
        "ds_read_b128 v[160:163], v15 offset:768\n\t" \
        "s_add_u32 s57, s40, 0x18000\n\t" \
        "s_add_u32 m0, s39, 0xc000\n\t" \
        "v_mfma_f32_16x16x32_bf16 v[32:35], a[84:87], v[132:135], v[32:35]\n\t" \
        "ds_read_b64_tr_b16 a[96:97], v6 offset:0\n\t" \
        "v_mfma_f32_16x16x32_bf16 v[36:39], a[88:91], v[132:135], v[36:39]\n\t" \
        "buffer_load_dwordx4 v3, s[24:27], s57 offen offset:0 lds\n\t" \
        "v_mfma_f32_16x16x32_bf16 v[40:43], a[92:95], v[132:135], v[40:43]\n\t" \
        "ds_read_b64_tr_b16 a[98:99], v6 offset:768\n\t" \
        "s_waitcnt lgkmcnt(6)\n\t" \
        "v_mfma_f32_16x16x32_bf16 v[44:47], a[84:87], v[136:139], v[44:47]\n\t" \
        "ds_read_b64_tr_b16 a[100:101], v7 offset:0\n\t" \
        "v_mfma_f32_16x16x32_bf16 v[48:51], a[88:91], v[136:139], v[48:51]\n\t" \
        "ds_read_b64_tr_b16 a[102:103], v7 offset:768\n\t" \
        "v_mfma_f32_16x16x32_bf16 v[52:55], a[92:95], v[136:139], v[52:55]\n\t" \
        "ds_read_b64_tr_b16 a[104:105], v8 offset:0\n\t" \
        "s_waitcnt lgkmcnt(5)\n\t" \
        "v_xor_b32 v160, v160, v14\n\t" \
        "v_mfma_f32_16x16x32_bf16 v[56:59], a[84:87], v[140:143], v[56:59]\n\t" \
        "ds_read_b64_tr_b16 a[106:107], v8 offset:768\n\t" \
        "v_xor_b32 v161, v161, v14\n\t" \
        "v_mfma_f32_16x16x32_bf16 v[60:63], a[88:91], v[140:143], v[60:63]\n\t" \
        "buffer_load_dwordx4 v4, s[24:27], s57 offen offset:1024 lds\n\t" \
        "v_mfma_f32_16x16x32_bf16 v[64:67], a[92:95], v[140:143], v[64:67]\n\t" \
        "ds_read_b128 v[120:123], v157 offset:128\n\t" \
        "v_xor_b32 v162, v162, v14\n\t" \
        "v_mfma_f32_16x16x32_bf16 v[68:71], a[84:87], v[144:147], v[68:71]\n\t" \
        "ds_read_b128 v[124:127], v158 offset:128\n\t" \
        "v_xor_b32 v163, v163, v14\n\t" \
        "v_mfma_f32_16x16x32_bf16 v[72:75], a[88:91], v[144:147], v[72:75]\n\t" \
        "ds_read_b128 v[128:131], v159 offset:128\n\t" \
        "v_mfma_f32_16x16x32_bf16 v[76:79], a[92:95], v[144:147], v[76:79]\n\t" \
        "ds_read_b128 v[132:135], v160 offset:0\n\t" \
        "s_waitcnt lgkmcnt(8)\n\t" \
        "v_mfma_f32_16x16x32_bf16 v[32:35], a[96:99], v[116:119], v[32:35]\n\t" \
        "ds_read_b64_tr_b16 a[108:109], v6 offset:6144\n\t" \
        "s_waitcnt lgkmcnt(7)\n\t" \
        "v_mfma_f32_16x16x32_bf16 v[36:39], a[100:103], v[116:119], v[36:39]\n\t" \
        "buffer_load_dwordx4 v5, s[24:27], s57 offen offset:2048 lds\n\t" \
        "s_waitcnt lgkmcnt(5)\n\t" \
        "v_mfma_f32_16x16x32_bf16 v[40:43], a[104:107], v[116:119], v[40:43]\n\t" \
        "ds_read_b64_tr_b16 a[110:111], v6 offset:6912\n\t" \
        "s_waitcnt lgkmcnt(5)\n\t" \
        "v_mfma_f32_16x16x32_bf16 v[44:47], a[96:99], v[120:123], v[44:47]\n\t" \
        "ds_read_b64_tr_b16 a[112:113], v7 offset:6144\n\t" \
        "v_mfma_f32_16x16x32_bf16 v[48:51], a[100:103], v[120:123], v[48:51]\n\t" \
        "ds_read_b64_tr_b16 a[114:115], v7 offset:6912\n\t" \
        "v_mfma_f32_16x16x32_bf16 v[52:55], a[104:107], v[120:123], v[52:55]\n\t" \
        "ds_read_b64_tr_b16 a[116:117], v8 offset:6144\n\t" \
        "s_waitcnt lgkmcnt(7)\n\t" \
        "v_mfma_f32_16x16x32_bf16 v[56:59], a[96:99], v[124:127], v[56:59]\n\t" \
        "ds_read_b64_tr_b16 a[118:119], v8 offset:6912\n\t" \
        "v_mfma_f32_16x16x32_bf16 v[60:63], a[100:103], v[124:127], v[60:63]\n\t" \
        "ds_read_b128 v[136:139], v161 offset:0\n\t" \
        "v_mfma_f32_16x16x32_bf16 v[64:67], a[104:107], v[124:127], v[64:67]\n\t" \
        "ds_read_b128 v[140:143], v162 offset:0\n\t" \
        "s_waitcnt lgkmcnt(9)\n\t" \
        "v_mfma_f32_16x16x32_bf16 v[68:71], a[96:99], v[128:131], v[68:71]\n\t" \
        "ds_read_b128 v[144:147], v163 offset:0\n\t" \
        "v_mfma_f32_16x16x32_bf16 v[72:75], a[100:103], v[128:131], v[72:75]\n\t" \
        "ds_read_b128 v[116:119], v160 offset:64\n\t" \
        "v_mfma_f32_16x16x32_bf16 v[76:79], a[104:107], v[128:131], v[76:79]\n\t" \
        "s_waitcnt vmcnt(9) lgkmcnt(4)\n\t" \
        "s_barrier\n\t" \
        "ds_read_b64_tr_b16 a[120:121], v6 offset:12288\n\t" \
        "s_add_u32 s57, s40, 0x1b000\n\t" \
        "s_mov_b32 m0, s39\n\t" \
        "v_mfma_f32_16x16x32_bf16 v[32:35], a[108:111], v[132:135], v[32:35]\n\t" \
        "ds_read_b64_tr_b16 a[122:123], v6 offset:13056\n\t" \
        "v_mfma_f32_16x16x32_bf16 v[36:39], a[112:115], v[132:135], v[36:39]\n\t" \
        "buffer_load_dwordx4 v3, s[24:27], s57 offen offset:0 lds\n\t" \
        "v_mfma_f32_16x16x32_bf16 v[40:43], a[116:119], v[132:135], v[40:43]\n\t" \
        "ds_read_b64_tr_b16 a[124:125], v7 offset:12288\n\t" \
        "s_waitcnt lgkmcnt(6)\n\t" \
        "v_mfma_f32_16x16x32_bf16 v[44:47], a[108:111], v[136:139], v[44:47]\n\t" \
        "ds_read_b64_tr_b16 a[126:127], v7 offset:13056\n\t" \
        "v_mfma_f32_16x16x32_bf16 v[48:51], a[112:115], v[136:139], v[48:51]\n\t" \
        "ds_read_b64_tr_b16 a[128:129], v8 offset:12288\n\t" \
        "v_mfma_f32_16x16x32_bf16 v[52:55], a[116:119], v[136:139], v[52:55]\n\t" \
        "ds_read_b64_tr_b16 a[130:131], v8 offset:13056\n\t" \
        "s_waitcnt lgkmcnt(8)\n\t" \
        "v_mfma_f32_16x16x32_bf16 v[56:59], a[108:111], v[140:143], v[56:59]\n\t" \
        "ds_read_b128 v[120:123], v161 offset:64\n\t" \
        "v_mfma_f32_16x16x32_bf16 v[60:63], a[112:115], v[140:143], v[60:63]\n\t" \
        "buffer_load_dwordx4 v4, s[24:27], s57 offen offset:1024 lds\n\t" \
        "v_mfma_f32_16x16x32_bf16 v[64:67], a[116:119], v[140:143], v[64:67]\n\t" \
        "ds_read_b128 v[124:127], v162 offset:64\n\t" \
        "s_waitcnt lgkmcnt(8)\n\t" \
        "v_mfma_f32_16x16x32_bf16 v[68:71], a[108:111], v[144:147], v[68:71]\n\t" \
        "ds_read_b128 v[128:131], v163 offset:64\n\t" \
        "v_mfma_f32_16x16x32_bf16 v[72:75], a[112:115], v[144:147], v[72:75]\n\t" \
        "ds_read_b128 v[132:135], v160 offset:128\n\t" \
        "v_mfma_f32_16x16x32_bf16 v[76:79], a[116:119], v[144:147], v[76:79]\n\t" \
        "ds_read_b128 v[164:167], v15 offset:1024\n\t" \
        "s_waitcnt lgkmcnt(9)\n\t" \
        "v_mfma_f32_16x16x32_bf16 v[32:35], a[120:123], v[116:119], v[32:35]\n\t" \
        "ds_read_b64_tr_b16 a[132:133], v6 offset:18432\n\t" \
        "s_waitcnt lgkmcnt(8)\n\t" \
        "v_mfma_f32_16x16x32_bf16 v[36:39], a[124:127], v[116:119], v[36:39]\n\t" \
        "buffer_load_dwordx4 v5, s[24:27], s57 offen offset:2048 lds\n\t" \
        "s_waitcnt lgkmcnt(6)\n\t" \
        "v_mfma_f32_16x16x32_bf16 v[40:43], a[128:131], v[116:119], v[40:43]\n\t" \
        "ds_read_b64_tr_b16 a[134:135], v6 offset:19200\n\t" \
        "s_waitcnt lgkmcnt(6)\n\t" \
        "v_mfma_f32_16x16x32_bf16 v[44:47], a[120:123], v[120:123], v[44:47]\n\t" \
        "ds_read_b64_tr_b16 a[136:137], v7 offset:18432\n\t" \
        "v_mfma_f32_16x16x32_bf16 v[48:51], a[124:127], v[120:123], v[48:51]\n\t" \
        "ds_read_b64_tr_b16 a[138:139], v7 offset:19200\n\t" \
        "v_mfma_f32_16x16x32_bf16 v[52:55], a[128:131], v[120:123], v[52:55]\n\t" \
        "ds_read_b64_tr_b16 a[140:141], v8 offset:18432\n\t" \
        "s_waitcnt lgkmcnt(5)\n\t" \
        "v_xor_b32 v164, v164, v14\n\t" \
        "v_mfma_f32_16x16x32_bf16 v[56:59], a[120:123], v[124:127], v[56:59]\n\t" \
        "ds_read_b64_tr_b16 a[142:143], v8 offset:19200\n\t" \
        "v_xor_b32 v165, v165, v14\n\t" \
        "v_mfma_f32_16x16x32_bf16 v[60:63], a[124:127], v[124:127], v[60:63]\n\t" \
        "ds_read_b128 v[136:139], v161 offset:128\n\t" \
        "v_xor_b32 v166, v166, v14\n\t" \
        "v_mfma_f32_16x16x32_bf16 v[64:67], a[128:131], v[124:127], v[64:67]\n\t" \
        "ds_read_b128 v[140:143], v162 offset:128\n\t" \
        "v_xor_b32 v167, v167, v14\n\t" \
        "v_mfma_f32_16x16x32_bf16 v[68:71], a[120:123], v[128:131], v[68:71]\n\t" \
        "ds_read_b128 v[144:147], v163 offset:128\n\t" \
        "v_mfma_f32_16x16x32_bf16 v[72:75], a[124:127], v[128:131], v[72:75]\n\t" \
        "ds_read_b128 v[116:119], v164 offset:0\n\t" \
        "v_mfma_f32_16x16x32_bf16 v[76:79], a[128:131], v[128:131], v[76:79]\n\t" \
        "s_waitcnt vmcnt(9) lgkmcnt(4)\n\t" \
        "s_barrier\n\t" \
        "ds_read_b64_tr_b16 a[144:145], v6 offset:24576\n\t" \
        "s_add_u32 s57, s40, 0x1e000\n\t" \
        "s_add_u32 m0, s39, 0x3000\n\t" \
        "v_mfma_f32_16x16x32_bf16 v[32:35], a[132:135], v[132:135], v[32:35]\n\t" \
        "ds_read_b64_tr_b16 a[146:147], v6 offset:25344\n\t" \
        "v_mfma_f32_16x16x32_bf16 v[36:39], a[136:139], v[132:135], v[36:39]\n\t" \
        "buffer_load_dwordx4 v3, s[24:27], s57 offen offset:0 lds\n\t" \
        "v_mfma_f32_16x16x32_bf16 v[40:43], a[140:143], v[132:135], v[40:43]\n\t" \
        "ds_read_b64_tr_b16 a[148:149], v7 offset:24576\n\t" \
        "s_waitcnt lgkmcnt(6)\n\t" \
        "v_mfma_f32_16x16x32_bf16 v[44:47], a[132:135], v[136:139], v[44:47]\n\t" \
        "ds_read_b64_tr_b16 a[150:151], v7 offset:25344\n\t" \
        "v_mfma_f32_16x16x32_bf16 v[48:51], a[136:139], v[136:139], v[48:51]\n\t" \
        "ds_read_b64_tr_b16 a[152:153], v8 offset:24576\n\t" \
        "v_mfma_f32_16x16x32_bf16 v[52:55], a[140:143], v[136:139], v[52:55]\n\t" \
        "ds_read_b64_tr_b16 a[154:155], v8 offset:25344\n\t" \
        "s_waitcnt lgkmcnt(8)\n\t" \
        "v_mfma_f32_16x16x32_bf16 v[56:59], a[132:135], v[140:143], v[56:59]\n\t" \
        "ds_read_b128 v[120:123], v165 offset:0\n\t" \
        "v_mfma_f32_16x16x32_bf16 v[60:63], a[136:139], v[140:143], v[60:63]\n\t" \
        "buffer_load_dwordx4 v4, s[24:27], s57 offen offset:1024 lds\n\t" \
        "v_mfma_f32_16x16x32_bf16 v[64:67], a[140:143], v[140:143], v[64:67]\n\t" \
        "ds_read_b128 v[124:127], v166 offset:0\n\t" \
        "s_waitcnt lgkmcnt(8)\n\t" \
        "v_mfma_f32_16x16x32_bf16 v[68:71], a[132:135], v[144:147], v[68:71]\n\t" \
        "ds_read_b128 v[128:131], v167 offset:0\n\t" \
        "v_mfma_f32_16x16x32_bf16 v[72:75], a[136:139], v[144:147], v[72:75]\n\t" \
        "ds_read_b128 v[132:135], v164 offset:64\n\t" \
        "v_mfma_f32_16x16x32_bf16 v[76:79], a[140:143], v[144:147], v[76:79]\n\t" \
        "ds_read_b64_tr_b16 a[156:157], v6 offset:30720\n\t" \
        "s_waitcnt lgkmcnt(9)\n\t" \
        "v_mfma_f32_16x16x32_bf16 v[32:35], a[144:147], v[116:119], v[32:35]\n\t" \
        "ds_read_b64_tr_b16 a[158:159], v6 offset:31488\n\t" \
        "s_waitcnt lgkmcnt(8)\n\t" \
        "v_mfma_f32_16x16x32_bf16 v[36:39], a[148:151], v[116:119], v[36:39]\n\t" \
        "buffer_load_dwordx4 v5, s[24:27], s57 offen offset:2048 lds\n\t" \
        "s_waitcnt lgkmcnt(6)\n\t" \
        "v_mfma_f32_16x16x32_bf16 v[40:43], a[152:155], v[116:119], v[40:43]\n\t" \
        "ds_read_b64_tr_b16 a[160:161], v7 offset:30720\n\t" \
        "s_waitcnt lgkmcnt(6)\n\t" \
        "v_mfma_f32_16x16x32_bf16 v[44:47], a[144:147], v[120:123], v[44:47]\n\t" \
        "ds_read_b64_tr_b16 a[162:163], v7 offset:31488\n\t" \
        "v_mfma_f32_16x16x32_bf16 v[48:51], a[148:151], v[120:123], v[48:51]\n\t" \
        "ds_read_b64_tr_b16 a[164:165], v8 offset:30720\n\t" \
        "v_mfma_f32_16x16x32_bf16 v[52:55], a[152:155], v[120:123], v[52:55]\n\t" \
        "ds_read_b64_tr_b16 a[166:167], v8 offset:31488\n\t" \
        "s_waitcnt lgkmcnt(8)\n\t" \
        "v_mfma_f32_16x16x32_bf16 v[56:59], a[144:147], v[124:127], v[56:59]\n\t" \
        "ds_read_b128 v[136:139], v165 offset:64\n\t" \
        "v_mfma_f32_16x16x32_bf16 v[60:63], a[148:151], v[124:127], v[60:63]\n\t" \
        "ds_read_b128 v[140:143], v166 offset:64\n\t" \
        "v_mfma_f32_16x16x32_bf16 v[64:67], a[152:155], v[124:127], v[64:67]\n\t" \
        "ds_read_b128 v[144:147], v167 offset:64\n\t" \
        "s_waitcnt lgkmcnt(9)\n\t" \
        "v_mfma_f32_16x16x32_bf16 v[68:71], a[144:147], v[128:131], v[68:71]\n\t" \
        "ds_read_b128 v[116:119], v164 offset:128\n\t" \
        "v_mfma_f32_16x16x32_bf16 v[72:75], a[148:151], v[128:131], v[72:75]\n\t" \
        "ds_read_b128 v[168:171], v15 offset:1280\n\t" \
        "v_mfma_f32_16x16x32_bf16 v[76:79], a[152:155], v[128:131], v[76:79]\n\t" \
        "s_waitcnt vmcnt(9) lgkmcnt(5)\n\t" \
        "s_barrier\n\t" \
        "ds_read_b64_tr_b16 a[168:169], v6 offset:36864\n\t" \
        "s_add_u32 s57, s40, 0x21000\n\t" \
        "s_add_u32 m0, s39, 0x6000\n\t" \
        "v_mfma_f32_16x16x32_bf16 v[32:35], a[156:159], v[132:135], v[32:35]\n\t" \
        "ds_read_b64_tr_b16 a[170:171], v6 offset:37632\n\t" \
        "v_mfma_f32_16x16x32_bf16 v[36:39], a[160:163], v[132:135], v[36:39]\n\t" \
        "buffer_load_dwordx4 v3, s[24:27], s57 offen offset:0 lds\n\t" \
        "v_mfma_f32_16x16x32_bf16 v[40:43], a[164:167], v[132:135], v[40:43]\n\t" \
        "ds_read_b64_tr_b16 a[172:173], v7 offset:36864\n\t" \
        "s_waitcnt lgkmcnt(7)\n\t" \
        "v_mfma_f32_16x16x32_bf16 v[44:47], a[156:159], v[136:139], v[44:47]\n\t" \
        "ds_read_b64_tr_b16 a[174:175], v7 offset:37632\n\t" \
        "v_mfma_f32_16x16x32_bf16 v[48:51], a[160:163], v[136:139], v[48:51]\n\t" \
        "ds_read_b64_tr_b16 a[176:177], v8 offset:36864\n\t" \
        "s_waitcnt lgkmcnt(5)\n\t" \
        "v_xor_b32 v168, v168, v14\n\t" \
        "v_mfma_f32_16x16x32_bf16 v[52:55], a[164:167], v[136:139], v[52:55]\n\t" \
        "ds_read_b64_tr_b16 a[178:179], v8 offset:37632\n\t" \
        "v_xor_b32 v169, v169, v14\n\t" \
        "v_mfma_f32_16x16x32_bf16 v[56:59], a[156:159], v[140:143], v[56:59]\n\t" \
        "ds_read_b128 v[120:123], v165 offset:128\n\t" \
        "v_xor_b32 v170, v170, v14\n\t" \
        "v_mfma_f32_16x16x32_bf16 v[60:63], a[160:163], v[140:143], v[60:63]\n\t" \
        "buffer_load_dwordx4 v4, s[24:27], s57 offen offset:1024 lds\n\t" \
        "v_mfma_f32_16x16x32_bf16 v[64:67], a[164:167], v[140:143], v[64:67]\n\t" \
        "ds_read_b128 v[124:127], v166 offset:128\n\t" \
        "v_xor_b32 v171, v171, v14\n\t" \
        "v_mfma_f32_16x16x32_bf16 v[68:71], a[156:159], v[144:147], v[68:71]\n\t" \
        "ds_read_b128 v[128:131], v167 offset:128\n\t" \
        "v_mfma_f32_16x16x32_bf16 v[72:75], a[160:163], v[144:147], v[72:75]\n\t" \
        "ds_read_b128 v[132:135], v168 offset:0\n\t" \
        "v_mfma_f32_16x16x32_bf16 v[76:79], a[164:167], v[144:147], v[76:79]\n\t" \
        "ds_read_b64_tr_b16 a[180:181], v6 offset:43008\n\t" \
        "s_waitcnt lgkmcnt(9)\n\t" \
        "v_mfma_f32_16x16x32_bf16 v[32:35], a[168:171], v[116:119], v[32:35]\n\t" \
        "ds_read_b64_tr_b16 a[182:183], v6 offset:43776\n\t" \
        "s_waitcnt lgkmcnt(8)\n\t" \
        "v_mfma_f32_16x16x32_bf16 v[36:39], a[172:175], v[116:119], v[36:39]\n\t" \
        "buffer_load_dwordx4 v5, s[24:27], s57 offen offset:2048 lds\n\t" \
        "s_waitcnt lgkmcnt(6)\n\t" \
        "v_mfma_f32_16x16x32_bf16 v[40:43], a[176:179], v[116:119], v[40:43]\n\t" \
        "ds_read_b64_tr_b16 a[184:185], v7 offset:43008\n\t" \
        "s_waitcnt lgkmcnt(6)\n\t" \
        "v_mfma_f32_16x16x32_bf16 v[44:47], a[168:171], v[120:123], v[44:47]\n\t" \
        "ds_read_b64_tr_b16 a[186:187], v7 offset:43776\n\t" \
        "v_mfma_f32_16x16x32_bf16 v[48:51], a[172:175], v[120:123], v[48:51]\n\t" \
        "ds_read_b64_tr_b16 a[188:189], v8 offset:43008\n\t" \
        "v_mfma_f32_16x16x32_bf16 v[52:55], a[176:179], v[120:123], v[52:55]\n\t" \
        "ds_read_b64_tr_b16 a[190:191], v8 offset:43776\n\t" \
        "s_waitcnt lgkmcnt(8)\n\t" \
        "v_mfma_f32_16x16x32_bf16 v[56:59], a[168:171], v[124:127], v[56:59]\n\t" \
        "ds_read_b128 v[136:139], v169 offset:0\n\t" \
        "v_mfma_f32_16x16x32_bf16 v[60:63], a[172:175], v[124:127], v[60:63]\n\t" \
        "ds_read_b128 v[140:143], v170 offset:0\n\t" \
        "v_mfma_f32_16x16x32_bf16 v[64:67], a[176:179], v[124:127], v[64:67]\n\t" \
        "ds_read_b128 v[144:147], v171 offset:0\n\t" \
        "s_waitcnt lgkmcnt(9)\n\t" \
        "v_mfma_f32_16x16x32_bf16 v[68:71], a[168:171], v[128:131], v[68:71]\n\t" \
        "ds_read_b128 v[116:119], v168 offset:64\n\t" \
        "v_mfma_f32_16x16x32_bf16 v[72:75], a[172:175], v[128:131], v[72:75]\n\t" \
        "ds_read_b128 v[120:123], v169 offset:64\n\t" \
        "v_mfma_f32_16x16x32_bf16 v[76:79], a[176:179], v[128:131], v[76:79]\n\t" \
        "s_waitcnt vmcnt(9) lgkmcnt(5)\n\t" \
        "s_barrier\n\t" \
        "ds_read_b64_tr_b16 a[192:193], v6 offset:49152\n\t" \
        "s_add_u32 s57, s40, 0x24000\n\t" \
        "s_add_u32 m0, s39, 0x9000\n\t" \
        "v_mfma_f32_16x16x32_bf16 v[32:35], a[180:183], v[132:135], v[32:35]\n\t" \
        "ds_read_b64_tr_b16 a[194:195], v6 offset:49920\n\t" \
        "v_mfma_f32_16x16x32_bf16 v[36:39], a[184:187], v[132:135], v[36:39]\n\t" \
        "buffer_load_dwordx4 v3, s[24:27], s57 offen offset:0 lds\n\t" \
        "v_mfma_f32_16x16x32_bf16 v[40:43], a[188:191], v[132:135], v[40:43]\n\t" \
        "ds_read_b64_tr_b16 a[196:197], v7 offset:49152\n\t" \
        "s_waitcnt lgkmcnt(7)\n\t" \
        "v_mfma_f32_16x16x32_bf16 v[44:47], a[180:183], v[136:139], v[44:47]\n\t" \
        "ds_read_b64_tr_b16 a[198:199], v7 offset:49920\n\t" \
        "v_mfma_f32_16x16x32_bf16 v[48:51], a[184:187], v[136:139], v[48:51]\n\t" \
        "ds_read_b64_tr_b16 a[200:201], v8 offset:49152\n\t" \
        "v_mfma_f32_16x16x32_bf16 v[52:55], a[188:191], v[136:139], v[52:55]\n\t" \
        "ds_read_b64_tr_b16 a[202:203], v8 offset:49920\n\t" \
        "s_waitcnt lgkmcnt(9)\n\t" \
        "v_mfma_f32_16x16x32_bf16 v[56:59], a[180:183], v[140:143], v[56:59]\n\t" \
        "ds_read_b128 v[124:127], v170 offset:64\n\t" \
        "v_mfma_f32_16x16x32_bf16 v[60:63], a[184:187], v[140:143], v[60:63]\n\t" \
        "buffer_load_dwordx4 v4, s[24:27], s57 offen offset:1024 lds\n\t" \
        "v_mfma_f32_16x16x32_bf16 v[64:67], a[188:191], v[140:143], v[64:67]\n\t" \
        "ds_read_b128 v[128:131], v171 offset:64\n\t" \
        "s_waitcnt lgkmcnt(8)\n\t" \
        "v_mfma_f32_16x16x32_bf16 v[68:71], a[180:183], v[144:147], v[68:71]\n\t" \
        "ds_read_b128 v[132:135], v168 offset:128\n\t" \
        "v_mfma_f32_16x16x32_bf16 v[72:75], a[184:187], v[144:147], v[72:75]\n\t" \
        "ds_read_b128 v[172:175], v15 offset:1536\n\t" \
        "v_mfma_f32_16x16x32_bf16 v[76:79], a[188:191], v[144:147], v[76:79]\n\t" \
        "ds_read_b64_tr_b16 a[204:205], v6 offset:55296\n\t" \
        "s_waitcnt lgkmcnt(9)\n\t" \
        "v_mfma_f32_16x16x32_bf16 v[32:35], a[192:195], v[116:119], v[32:35]\n\t" \
        "ds_read_b64_tr_b16 a[206:207], v6 offset:56064\n\t" \
        "s_waitcnt lgkmcnt(8)\n\t" \
        "v_mfma_f32_16x16x32_bf16 v[36:39], a[196:199], v[116:119], v[36:39]\n\t" \
        "buffer_load_dwordx4 v5, s[24:27], s57 offen offset:2048 lds\n\t" \
        "s_waitcnt lgkmcnt(6)\n\t" \
        "v_mfma_f32_16x16x32_bf16 v[40:43], a[200:203], v[116:119], v[40:43]\n\t" \
        "ds_read_b64_tr_b16 a[208:209], v7 offset:55296\n\t" \
        "v_mfma_f32_16x16x32_bf16 v[44:47], a[192:195], v[120:123], v[44:47]\n\t" \
        "ds_read_b64_tr_b16 a[210:211], v7 offset:56064\n\t" \
        "v_mfma_f32_16x16x32_bf16 v[48:51], a[196:199], v[120:123], v[48:51]\n\t" \
        "ds_read_b64_tr_b16 a[212:213], v8 offset:55296\n\t" \
        "s_waitcnt lgkmcnt(5)\n\t" \
        "v_xor_b32 v172, v172, v14\n\t" \
        "v_mfma_f32_16x16x32_bf16 v[52:55], a[200:203], v[120:123], v[52:55]\n\t" \
        "ds_read_b64_tr_b16 a[214:215], v8 offset:56064\n\t" \
        "v_xor_b32 v173, v173, v14\n\t" \
        "v_mfma_f32_16x16x32_bf16 v[56:59], a[192:195], v[124:127], v[56:59]\n\t" \
        "ds_read_b128 v[136:139], v169 offset:128\n\t" \
        "v_xor_b32 v174, v174, v14\n\t" \
        "v_mfma_f32_16x16x32_bf16 v[60:63], a[196:199], v[124:127], v[60:63]\n\t" \
        "ds_read_b128 v[140:143], v170 offset:128\n\t" \
        "v_xor_b32 v175, v175, v14\n\t" \
        "v_mfma_f32_16x16x32_bf16 v[64:67], a[200:203], v[124:127], v[64:67]\n\t" \
        "ds_read_b128 v[144:147], v171 offset:128\n\t" \
        "v_mfma_f32_16x16x32_bf16 v[68:71], a[192:195], v[128:131], v[68:71]\n\t" \
        "ds_read_b128 v[116:119], v172 offset:0\n\t" \
        "v_mfma_f32_16x16x32_bf16 v[72:75], a[196:199], v[128:131], v[72:75]\n\t" \
        "ds_read_b128 v[120:123], v173 offset:0\n\t" \
        "v_mfma_f32_16x16x32_bf16 v[76:79], a[200:203], v[128:131], v[76:79]\n\t" \
        "s_waitcnt vmcnt(9) lgkmcnt(5)\n\t" \
        "s_barrier\n\t" \
        "ds_read_b64_tr_b16 a[216:217], v6 offset:0\n\t" \
        "s_add_u32 s57, s40, 0x27000\n\t" \
        "s_add_u32 m0, s39, 0xc000\n\t" \
        "v_mfma_f32_16x16x32_bf16 v[32:35], a[204:207], v[132:135], v[32:35]\n\t" \
        "ds_read_b64_tr_b16 a[218:219], v6 offset:768\n\t" \
        "v_mfma_f32_16x16x32_bf16 v[36:39], a[208:211], v[132:135], v[36:39]\n\t" \
        "buffer_load_dwordx4 v3, s[24:27], s57 offen offset:0 lds\n\t" \
        "v_mfma_f32_16x16x32_bf16 v[40:43], a[212:215], v[132:135], v[40:43]\n\t" \
        "ds_read_b64_tr_b16 a[220:221], v7 offset:0\n\t" \
        "s_waitcnt lgkmcnt(7)\n\t" \
        "v_mfma_f32_16x16x32_bf16 v[44:47], a[204:207], v[136:139], v[44:47]\n\t" \
        "ds_read_b64_tr_b16 a[222:223], v7 offset:768\n\t" \
        "v_mfma_f32_16x16x32_bf16 v[48:51], a[208:211], v[136:139], v[48:51]\n\t" \
        "ds_read_b64_tr_b16 a[224:225], v8 offset:0\n\t" \
        "v_mfma_f32_16x16x32_bf16 v[52:55], a[212:215], v[136:139], v[52:55]\n\t" \
        "ds_read_b64_tr_b16 a[226:227], v8 offset:768\n\t" \
        "s_waitcnt lgkmcnt(9)\n\t" \
        "v_mfma_f32_16x16x32_bf16 v[56:59], a[204:207], v[140:143], v[56:59]\n\t" \
        "ds_read_b128 v[124:127], v174 offset:0\n\t" \
        "v_mfma_f32_16x16x32_bf16 v[60:63], a[208:211], v[140:143], v[60:63]\n\t" \
        "buffer_load_dwordx4 v4, s[24:27], s57 offen offset:1024 lds\n\t" \
        "v_mfma_f32_16x16x32_bf16 v[64:67], a[212:215], v[140:143], v[64:67]\n\t" \
        "ds_read_b128 v[128:131], v175 offset:0\n\t" \
        "s_waitcnt lgkmcnt(8)\n\t" \
        "v_mfma_f32_16x16x32_bf16 v[68:71], a[204:207], v[144:147], v[68:71]\n\t" \
        "ds_read_b128 v[132:135], v172 offset:64\n\t" \
        "v_mfma_f32_16x16x32_bf16 v[72:75], a[208:211], v[144:147], v[72:75]\n\t" \
        "ds_read_b64_tr_b16 a[228:229], v6 offset:6144\n\t" \
        "v_mfma_f32_16x16x32_bf16 v[76:79], a[212:215], v[144:147], v[76:79]\n\t" \
        "ds_read_b64_tr_b16 a[230:231], v6 offset:6912\n\t" \
        "s_waitcnt lgkmcnt(9)\n\t" \
        "v_mfma_f32_16x16x32_bf16 v[32:35], a[216:219], v[116:119], v[32:35]\n\t" \
        "ds_read_b64_tr_b16 a[232:233], v7 offset:6144\n\t" \
        "s_waitcnt lgkmcnt(8)\n\t" \
        "v_mfma_f32_16x16x32_bf16 v[36:39], a[220:223], v[116:119], v[36:39]\n\t" \
        "buffer_load_dwordx4 v5, s[24:27], s57 offen offset:2048 lds\n\t" \
        "s_waitcnt lgkmcnt(6)\n\t" \
        "v_mfma_f32_16x16x32_bf16 v[40:43], a[224:227], v[116:119], v[40:43]\n\t" \
        "ds_read_b64_tr_b16 a[234:235], v7 offset:6912\n\t" \
        "v_mfma_f32_16x16x32_bf16 v[44:47], a[216:219], v[120:123], v[44:47]\n\t" \
        "ds_read_b64_tr_b16 a[236:237], v8 offset:6144\n\t" \
        "v_mfma_f32_16x16x32_bf16 v[48:51], a[220:223], v[120:123], v[48:51]\n\t" \
        "ds_read_b64_tr_b16 a[238:239], v8 offset:6912\n\t" \
        "v_mfma_f32_16x16x32_bf16 v[52:55], a[224:227], v[120:123], v[52:55]\n\t" \
        "ds_read_b128 v[136:139], v173 offset:64\n\t" \
        "s_waitcnt lgkmcnt(9)\n\t" \
        "v_mfma_f32_16x16x32_bf16 v[56:59], a[216:219], v[124:127], v[56:59]\n\t" \
        "ds_read_b128 v[140:143], v174 offset:64\n\t" \
        "v_mfma_f32_16x16x32_bf16 v[60:63], a[220:223], v[124:127], v[60:63]\n\t" \
        "ds_read_b128 v[144:147], v175 offset:64\n\t" \
        "v_mfma_f32_16x16x32_bf16 v[64:67], a[224:227], v[124:127], v[64:67]\n\t" \
        "ds_read_b128 v[116:119], v172 offset:128\n\t" \
        "s_waitcnt lgkmcnt(9)\n\t" \
        "v_mfma_f32_16x16x32_bf16 v[68:71], a[216:219], v[128:131], v[68:71]\n\t" \
        "ds_read_b128 v[176:179], v15 offset:1792\n\t" \
        "v_mfma_f32_16x16x32_bf16 v[72:75], a[220:223], v[128:131], v[72:75]\n\t" \
        "ds_read_b128 v[120:123], v173 offset:128\n\t" \
        "v_mfma_f32_16x16x32_bf16 v[76:79], a[224:227], v[128:131], v[76:79]\n\t" \
        "s_waitcnt vmcnt(9)\n\t" \
        "s_barrier\n\t" \
        "ds_read_b64_tr_b16 a[240:241], v6 offset:12288\n\t" \
        "s_waitcnt lgkmcnt(10)\n\t" \
        "v_mfma_f32_16x16x32_bf16 v[32:35], a[228:231], v[132:135], v[32:35]\n\t" \
        "ds_read_b64_tr_b16 a[242:243], v6 offset:13056\n\t" \
        "s_waitcnt lgkmcnt(10)\n\t" \
        "v_mfma_f32_16x16x32_bf16 v[36:39], a[232:235], v[132:135], v[36:39]\n\t" \
        "ds_read_b64_tr_b16 a[244:245], v7 offset:12288\n\t" \
        "s_waitcnt lgkmcnt(9)\n\t" \
        "v_mfma_f32_16x16x32_bf16 v[40:43], a[236:239], v[132:135], v[40:43]\n\t" \
        "ds_read_b64_tr_b16 a[246:247], v7 offset:13056\n\t" \
        "s_waitcnt lgkmcnt(9)\n\t" \
        "v_mfma_f32_16x16x32_bf16 v[44:47], a[228:231], v[136:139], v[44:47]\n\t" \
        "ds_read_b64_tr_b16 a[248:249], v8 offset:12288\n\t" \
        "s_waitcnt lgkmcnt(6)\n\t" \
        "v_xor_b32 v176, v176, v14\n\t" \
        "v_mfma_f32_16x16x32_bf16 v[48:51], a[232:235], v[136:139], v[48:51]\n\t" \
        "ds_read_b64_tr_b16 a[250:251], v8 offset:13056\n\t" \
        "v_xor_b32 v177, v177, v14\n\t" \
        "v_mfma_f32_16x16x32_bf16 v[52:55], a[236:239], v[136:139], v[52:55]\n\t" \
        "ds_read_b128 v[124:127], v174 offset:128\n\t" \
        "v_xor_b32 v178, v178, v14\n\t" \
        "v_mfma_f32_16x16x32_bf16 v[56:59], a[228:231], v[140:143], v[56:59]\n\t" \
        "ds_read_b128 v[128:131], v175 offset:128\n\t" \
        "v_xor_b32 v179, v179, v14\n\t" \
        "v_mfma_f32_16x16x32_bf16 v[60:63], a[232:235], v[140:143], v[60:63]\n\t" \
        "ds_read_b128 v[132:135], v176 offset:0\n\t" \
        "v_mfma_f32_16x16x32_bf16 v[64:67], a[236:239], v[140:143], v[64:67]\n\t" \
        "ds_read_b64_tr_b16 a[252:253], v6 offset:18432\n\t" \
        "v_mfma_f32_16x16x32_bf16 v[68:71], a[228:231], v[144:147], v[68:71]\n\t" \
        "ds_read_b64_tr_b16 a[254:255], v6 offset:19200\n\t" \
        "v_mfma_f32_16x16x32_bf16 v[72:75], a[232:235], v[144:147], v[72:75]\n\t" \
        "ds_read_b64_tr_b16 v[188:189], v7 offset:18432\n\t" \
        "v_mfma_f32_16x16x32_bf16 v[76:79], a[236:239], v[144:147], v[76:79]\n\t" \
        "ds_read_b64_tr_b16 v[190:191], v7 offset:19200\n\t" \
        "s_waitcnt lgkmcnt(10)\n\t" \
        "v_mfma_f32_16x16x32_bf16 v[32:35], a[240:243], v[116:119], v[32:35]\n\t" \
        "ds_read_b64_tr_b16 v[192:193], v8 offset:18432\n\t" \
        "s_waitcnt lgkmcnt(10)\n\t" \
        "v_mfma_f32_16x16x32_bf16 v[36:39], a[244:247], v[116:119], v[36:39]\n\t" \
        "ds_read_b64_tr_b16 v[194:195], v8 offset:19200\n\t" \
        "s_waitcnt lgkmcnt(9)\n\t" \
        "v_mfma_f32_16x16x32_bf16 v[40:43], a[248:251], v[116:119], v[40:43]\n\t" \
        "ds_read_b128 v[136:139], v177 offset:0\n\t" \
        "v_mfma_f32_16x16x32_bf16 v[44:47], a[240:243], v[120:123], v[44:47]\n\t" \
        "ds_read_b128 v[140:143], v178 offset:0\n\t" \
        "v_mfma_f32_16x16x32_bf16 v[48:51], a[244:247], v[120:123], v[48:51]\n\t" \
        "ds_read_b128 v[144:147], v179 offset:0\n\t" \
        "v_mfma_f32_16x16x32_bf16 v[52:55], a[248:251], v[120:123], v[52:55]\n\t" \
        "ds_read_b128 v[116:119], v176 offset:64\n\t" \
        "s_waitcnt lgkmcnt(10)\n\t" \
        "v_mfma_f32_16x16x32_bf16 v[56:59], a[240:243], v[124:127], v[56:59]\n\t" \
        "ds_read_b128 v[120:123], v177 offset:64\n\t" \
        "v_mfma_f32_16x16x32_bf16 v[60:63], a[244:247], v[124:127], v[60:63]\n\t" \
        "ds_read_b128 v[180:183], v15 offset:2048\n\t" \
        "v_mfma_f32_16x16x32_bf16 v[64:67], a[248:251], v[124:127], v[64:67]\n\t" \
        "ds_read_b128 v[124:127], v178 offset:64\n\t" \
        "v_mfma_f32_16x16x32_bf16 v[68:71], a[240:243], v[128:131], v[68:71]\n\t" \
        "ds_read_b64 v[148:149], v15 offset:4608\n\t" \
        "v_mfma_f32_16x16x32_bf16 v[72:75], a[244:247], v[128:131], v[72:75]\n\t" \
        "ds_read_b64 v[152:153], v15 offset:4864\n\t" \
        "v_mfma_f32_16x16x32_bf16 v[76:79], a[248:251], v[128:131], v[76:79]\n\t" \
        "s_waitcnt vmcnt(6)\n\t" \
        "s_barrier\n\t" \
        "ds_read_b64_tr_b16 v[196:197], v6 offset:24576\n\t" \
        "s_waitcnt lgkmcnt(10)\n\t" \
        "v_mfma_f32_16x16x32_bf16 v[32:35], a[252:255], v[132:135], v[32:35]\n\t" \
        "ds_read_b64_tr_b16 v[198:199], v6 offset:25344\n\t" \
        "v_mfma_f32_16x16x32_bf16 v[36:39], v[188:191], v[132:135], v[36:39]\n\t" \
        "ds_read_b64_tr_b16 v[200:201], v7 offset:24576\n\t" \
        "s_waitcnt lgkmcnt(6)\n\t" \
        "v_xor_b32 v180, v180, v14\n\t" \
        "v_mfma_f32_16x16x32_bf16 v[40:43], v[192:195], v[132:135], v[40:43]\n\t" \
        "ds_read_b64_tr_b16 v[202:203], v7 offset:25344\n\t" \
        "v_xor_b32 v181, v181, v14\n\t" \
        "v_mfma_f32_16x16x32_bf16 v[44:47], a[252:255], v[136:139], v[44:47]\n\t" \
        "ds_read_b64_tr_b16 v[204:205], v8 offset:24576\n\t" \
        "v_xor_b32 v182, v182, v14\n\t" \
        "v_mfma_f32_16x16x32_bf16 v[48:51], v[188:191], v[136:139], v[48:51]\n\t" \
        "ds_read_b64_tr_b16 v[206:207], v8 offset:25344\n\t" \
        "v_xor_b32 v183, v183, v14\n\t" \
        "v_mfma_f32_16x16x32_bf16 v[52:55], v[192:195], v[136:139], v[52:55]\n\t" \
        "ds_read_b128 v[128:131], v179 offset:64\n\t" \
        "s_waitcnt lgkmcnt(8)\n\t" \
        "v_xor_b32 v148, v148, v14\n\t" \
        "v_mfma_f32_16x16x32_bf16 v[56:59], a[252:255], v[140:143], v[56:59]\n\t" \
        "ds_read_b128 v[132:135], v176 offset:128\n\t" \
        "s_waitcnt lgkmcnt(8)\n\t" \
        "v_xor_b32 v152, v152, v14\n\t" \
        "v_mfma_f32_16x16x32_bf16 v[60:63], v[188:191], v[140:143], v[60:63]\n\t" \
        "ds_read_b64_tr_b16 v[208:209], v6 offset:30720\n\t" \
        "v_xor_b32 v149, v149, v14\n\t" \
        "v_mfma_f32_16x16x32_bf16 v[64:67], v[192:195], v[140:143], v[64:67]\n\t" \
        "ds_read_b64_tr_b16 v[210:211], v6 offset:31488\n\t" \
        "v_xor_b32 v153, v153, v14\n\t" \
        "v_mfma_f32_16x16x32_bf16 v[68:71], a[252:255], v[144:147], v[68:71]\n\t" \
        "ds_read_b64_tr_b16 v[212:213], v7 offset:30720\n\t" \
        "v_mfma_f32_16x16x32_bf16 v[72:75], v[188:191], v[144:147], v[72:75]\n\t" \
        "ds_read_b64_tr_b16 v[214:215], v7 offset:31488\n\t" \
        "v_mfma_f32_16x16x32_bf16 v[76:79], v[192:195], v[144:147], v[76:79]\n\t" \
        "ds_read_b64_tr_b16 v[216:217], v8 offset:30720\n\t" \
        "s_waitcnt lgkmcnt(10)\n\t" \
        "v_mfma_f32_16x16x32_bf16 v[32:35], v[196:199], v[116:119], v[32:35]\n\t" \
        "ds_read_b64_tr_b16 v[218:219], v8 offset:31488\n\t" \
        "s_waitcnt lgkmcnt(10)\n\t" \
        "v_mfma_f32_16x16x32_bf16 v[36:39], v[200:203], v[116:119], v[36:39]\n\t" \
        "ds_read_b128 v[136:139], v177 offset:128\n\t" \
        "s_waitcnt lgkmcnt(9)\n\t" \
        "v_mfma_f32_16x16x32_bf16 v[40:43], v[204:207], v[116:119], v[40:43]\n\t" \
        "ds_read_b128 v[140:143], v178 offset:128\n\t" \
        "v_mfma_f32_16x16x32_bf16 v[44:47], v[196:199], v[120:123], v[44:47]\n\t" \
        "ds_read_b128 v[144:147], v179 offset:128\n\t" \
        "v_mfma_f32_16x16x32_bf16 v[48:51], v[200:203], v[120:123], v[48:51]\n\t" \
        "ds_read_b128 v[116:119], v180 offset:0\n\t" \
        "v_mfma_f32_16x16x32_bf16 v[52:55], v[204:207], v[120:123], v[52:55]\n\t" \
        "ds_read_b128 v[120:123], v181 offset:0\n\t" \
        "v_mfma_f32_16x16x32_bf16 v[56:59], v[196:199], v[124:127], v[56:59]\n\t" \
        "ds_read_b64 v[156:157], v15 offset:5120\n\t" \
        "v_mfma_f32_16x16x32_bf16 v[60:63], v[200:203], v[124:127], v[60:63]\n\t" \
        "ds_read_b64 v[160:161], v15 offset:5376\n\t" \
        "v_mfma_f32_16x16x32_bf16 v[64:67], v[204:207], v[124:127], v[64:67]\n\t" \
        "ds_read_b128 v[124:127], v182 offset:0\n\t" \
        "s_waitcnt lgkmcnt(10)\n\t" \
        "v_mfma_f32_16x16x32_bf16 v[68:71], v[196:199], v[128:131], v[68:71]\n\t" \
        "ds_read_b64 v[164:165], v15 offset:5632\n\t" \
        "v_mfma_f32_16x16x32_bf16 v[72:75], v[200:203], v[128:131], v[72:75]\n\t" \
        "ds_read_b64 v[168:169], v15 offset:5888\n\t" \
        "v_mfma_f32_16x16x32_bf16 v[76:79], v[204:207], v[128:131], v[76:79]\n\t" \
        "s_waitcnt vmcnt(3)\n\t" \
        "s_barrier\n\t" \
        "ds_read_b64_tr_b16 v[220:221], v6 offset:36864\n\t" \
        "v_mfma_f32_16x16x32_bf16 v[32:35], v[208:211], v[132:135], v[32:35]\n\t" \
        "ds_read_b64_tr_b16 v[222:223], v6 offset:37632\n\t" \
        "s_waitcnt lgkmcnt(6)\n\t" \
        "v_xor_b32 v156, v156, v14\n\t" \
        "v_mfma_f32_16x16x32_bf16 v[36:39], v[212:215], v[132:135], v[36:39]\n\t" \
        "ds_read_b64_tr_b16 v[224:225], v7 offset:36864\n\t" \
        "s_waitcnt lgkmcnt(6)\n\t" \
        "v_xor_b32 v160, v160, v14\n\t" \
        "v_mfma_f32_16x16x32_bf16 v[40:43], v[216:219], v[132:135], v[40:43]\n\t" \
        "ds_read_b64_tr_b16 v[226:227], v7 offset:37632\n\t" \
        "v_xor_b32 v157, v157, v14\n\t" \
        "v_mfma_f32_16x16x32_bf16 v[44:47], v[208:211], v[136:139], v[44:47]\n\t" \
        "ds_read_b64_tr_b16 v[228:229], v8 offset:36864\n\t" \
        "s_waitcnt lgkmcnt(6)\n\t" \
        "v_xor_b32 v164, v164, v14\n\t" \
        "v_mfma_f32_16x16x32_bf16 v[48:51], v[212:215], v[136:139], v[48:51]\n\t" \
        "ds_read_b64_tr_b16 v[230:231], v8 offset:37632\n\t" \
        "s_waitcnt lgkmcnt(6)\n\t" \
        "v_xor_b32 v168, v168, v14\n\t" \
        "v_mfma_f32_16x16x32_bf16 v[52:55], v[216:219], v[136:139], v[52:55]\n\t" \
        "ds_read_b128 v[128:131], v183 offset:0\n\t" \
        "v_xor_b32 v161, v161, v14\n\t" \
        "v_mfma_f32_16x16x32_bf16 v[56:59], v[208:211], v[140:143], v[56:59]\n\t" \
        "ds_read_b128 v[132:135], v180 offset:64\n\t" \
        "v_xor_b32 v165, v165, v14\n\t" \
        "v_mfma_f32_16x16x32_bf16 v[60:63], v[212:215], v[140:143], v[60:63]\n\t" \
        "ds_read_b64_tr_b16 v[232:233], v6 offset:43008\n\t" \
        "v_xor_b32 v169, v169, v14\n\t" \
        "v_mfma_f32_16x16x32_bf16 v[64:67], v[216:219], v[140:143], v[64:67]\n\t" \
        "ds_read_b64_tr_b16 v[234:235], v6 offset:43776\n\t" \
        "v_mfma_f32_16x16x32_bf16 v[68:71], v[208:211], v[144:147], v[68:71]\n\t" \
        "ds_read_b64_tr_b16 v[236:237], v7 offset:43008\n\t" \
        "v_mfma_f32_16x16x32_bf16 v[72:75], v[212:215], v[144:147], v[72:75]\n\t" \
        "ds_read_b64_tr_b16 v[238:239], v7 offset:43776\n\t" \
        "v_mfma_f32_16x16x32_bf16 v[76:79], v[216:219], v[144:147], v[76:79]\n\t" \
        "ds_read_b64_tr_b16 v[240:241], v8 offset:43008\n\t" \
        "s_waitcnt lgkmcnt(10)\n\t" \
        "v_mfma_f32_16x16x32_bf16 v[32:35], v[220:223], v[116:119], v[32:35]\n\t" \
        "ds_read_b64_tr_b16 v[242:243], v8 offset:43776\n\t" \
        "s_waitcnt lgkmcnt(10)\n\t" \
        "v_mfma_f32_16x16x32_bf16 v[36:39], v[224:227], v[116:119], v[36:39]\n\t" \
        "ds_read_b128 v[136:139], v181 offset:64\n\t" \
        "s_waitcnt lgkmcnt(9)\n\t" \
        "v_mfma_f32_16x16x32_bf16 v[40:43], v[228:231], v[116:119], v[40:43]\n\t" \
        "ds_read_b128 v[140:143], v182 offset:64\n\t" \
        "v_mfma_f32_16x16x32_bf16 v[44:47], v[220:223], v[120:123], v[44:47]\n\t" \
        "ds_read_b128 v[144:147], v183 offset:64\n\t" \
        "v_mfma_f32_16x16x32_bf16 v[48:51], v[224:227], v[120:123], v[48:51]\n\t" \
        "ds_read_b128 v[116:119], v180 offset:128\n\t" \
        "v_mfma_f32_16x16x32_bf16 v[52:55], v[228:231], v[120:123], v[52:55]\n\t" \
        "ds_read_b128 v[120:123], v181 offset:128\n\t" \
        "v_mfma_f32_16x16x32_bf16 v[56:59], v[220:223], v[124:127], v[56:59]\n\t" \
        "ds_read_b64 v[172:173], v15 offset:6144\n\t" \
        "v_mfma_f32_16x16x32_bf16 v[60:63], v[224:227], v[124:127], v[60:63]\n\t" \
        "ds_read_b64 v[176:177], v15 offset:6400\n\t" \
        "v_mfma_f32_16x16x32_bf16 v[64:67], v[228:231], v[124:127], v[64:67]\n\t" \
        "ds_read_b128 v[124:127], v182 offset:128\n\t" \
        "s_waitcnt lgkmcnt(10)\n\t" \
        "v_mfma_f32_16x16x32_bf16 v[68:71], v[220:223], v[128:131], v[68:71]\n\t" \
        "v_mfma_f32_16x16x32_bf16 v[72:75], v[224:227], v[128:131], v[72:75]\n\t" \
        "v_mfma_f32_16x16x32_bf16 v[76:79], v[228:231], v[128:131], v[76:79]\n\t" \
        "s_waitcnt vmcnt(0)\n\t" \
        "s_barrier\n\t" \
        "ds_read_b64_tr_b16 v[244:245], v6 offset:49152\n\t" \
        "v_mfma_f32_16x16x32_bf16 v[32:35], v[232:235], v[132:135], v[32:35]\n\t" \
        "ds_read_b64_tr_b16 v[246:247], v6 offset:49920\n\t" \
        "s_waitcnt lgkmcnt(4)\n\t" \
        "v_xor_b32 v172, v172, v14\n\t" \
        "v_mfma_f32_16x16x32_bf16 v[36:39], v[236:239], v[132:135], v[36:39]\n\t" \
        "ds_read_b64_tr_b16 v[248:249], v7 offset:49152\n\t" \
        "s_waitcnt lgkmcnt(4)\n\t" \
        "v_xor_b32 v176, v176, v14\n\t" \
        "v_mfma_f32_16x16x32_bf16 v[40:43], v[240:243], v[132:135], v[40:43]\n\t" \
        "ds_read_b64_tr_b16 v[250:251], v7 offset:49920\n\t" \
        "v_xor_b32 v173, v173, v14\n\t" \
        "v_mfma_f32_16x16x32_bf16 v[44:47], v[232:235], v[136:139], v[44:47]\n\t" \
        "ds_read_b64_tr_b16 v[252:253], v8 offset:49152\n\t" \
        "v_xor_b32 v177, v177, v14\n\t" \
        "v_mfma_f32_16x16x32_bf16 v[48:51], v[236:239], v[136:139], v[48:51]\n\t" \
        "ds_read_b64_tr_b16 v[254:255], v8 offset:49920\n\t" \
        "v_mfma_f32_16x16x32_bf16 v[52:55], v[240:243], v[136:139], v[52:55]\n\t" \
        "ds_read_b128 v[128:131], v183 offset:128\n\t" \
        "v_mfma_f32_16x16x32_bf16 v[56:59], v[232:235], v[140:143], v[56:59]\n\t" \
        "ds_read_b128 v[132:135], v148 offset:0\n\t" \
        "v_mfma_f32_16x16x32_bf16 v[60:63], v[236:239], v[140:143], v[60:63]\n\t" \
        "ds_read_b128 v[136:139], v148 offset:64\n\t" \
        "v_mfma_f32_16x16x32_bf16 v[64:67], v[240:243], v[140:143], v[64:67]\n\t" \
        "ds_read_b128 v[140:143], v148 offset:128\n\t" \
        "v_mfma_f32_16x16x32_bf16 v[68:71], v[232:235], v[144:147], v[68:71]\n\t" \
        "ds_read_b64 v[180:181], v15 offset:6656\n\t" \
        "v_mfma_f32_16x16x32_bf16 v[72:75], v[236:239], v[144:147], v[72:75]\n\t" \
        "v_mfma_f32_16x16x32_bf16 v[76:79], v[240:243], v[144:147], v[76:79]\n\t" \
        "ds_read_b128 v[144:147], v152 offset:0\n\t" \
        "s_waitcnt lgkmcnt(9)\n\t" \
        "v_mfma_f32_16x16x32_bf16 v[32:35], v[244:247], v[116:119], v[32:35]\n\t" \
        "s_waitcnt lgkmcnt(8)\n\t" \
        "v_mfma_f32_16x16x32_bf16 v[36:39], v[248:251], v[116:119], v[36:39]\n\t" \
        "s_waitcnt lgkmcnt(6)\n\t" \
        "v_mfma_f32_16x16x32_bf16 v[40:43], v[252:255], v[116:119], v[40:43]\n\t" \
        "ds_read_b128 v[116:119], v152 offset:64\n\t" \
        "v_mfma_f32_16x16x32_bf16 v[44:47], v[244:247], v[120:123], v[44:47]\n\t" \
        "s_waitcnt lgkmcnt(2)\n\t" \
        "v_xor_b32 v180, v180, v14\n\t" \
        "v_xor_b32 v181, v181, v14\n\t" \
        "v_mfma_f32_16x16x32_bf16 v[48:51], v[248:251], v[120:123], v[48:51]\n\t" \
        "v_mfma_f32_16x16x32_bf16 v[52:55], v[252:255], v[120:123], v[52:55]\n\t" \
        "ds_read_b128 v[120:123], v152 offset:128\n\t" \
        "v_mfma_f32_16x16x32_bf16 v[56:59], v[244:247], v[124:127], v[56:59]\n\t" \
        "v_mfma_f32_16x16x32_bf16 v[60:63], v[248:251], v[124:127], v[60:63]\n\t" \
        "v_mfma_f32_16x16x32_bf16 v[64:67], v[252:255], v[124:127], v[64:67]\n\t" \
        "ds_read_b128 v[124:127], v156 offset:0\n\t" \
        "v_mfma_f32_16x16x32_bf16 v[68:71], v[244:247], v[128:131], v[68:71]\n\t" \
        "v_mfma_f32_16x16x32_bf16 v[72:75], v[248:251], v[128:131], v[72:75]\n\t" \
        "v_mfma_f32_16x16x32_bf16 v[76:79], v[252:255], v[128:131], v[76:79]\n\t" \
        "ds_read_b128 v[128:131], v156 offset:64\n\t" \
        "v_mfma_f32_16x16x32_bf16 v[80:83], a[0:3], v[132:135], 0\n\t" \
        "v_mfma_f32_16x16x32_bf16 v[84:87], a[4:7], v[132:135], 0\n\t" \
        "v_mfma_f32_16x16x32_bf16 v[88:91], a[8:11], v[132:135], 0\n\t" \
        "v_cvt_pk_bf16_f32 v40, v40, v41\n\t" \
        "v_cvt_pk_bf16_f32 v41, v42, v43\n\t" \
        "v_mfma_f32_16x16x32_bf16 v[80:83], a[12:15], v[136:139], v[80:83]\n\t" \
        "ds_write_b64 v11, v[40:41] offset:0\n\t" \
        "v_cvt_pk_bf16_f32 v52, v52, v53\n\t" \
        "v_mfma_f32_16x16x32_bf16 v[84:87], a[16:19], v[136:139], v[84:87]\n\t" \
        "v_cvt_pk_bf16_f32 v53, v54, v55\n\t" \
        "v_cvt_pk_bf16_f32 v64, v64, v65\n\t" \
        "v_mfma_f32_16x16x32_bf16 v[88:91], a[20:23], v[136:139], v[88:91]\n\t" \
        "ds_write_b64 v11, v[52:53] offset:2048\n\t" \
        "v_cvt_pk_bf16_f32 v65, v66, v67\n\t" \
        "v_mfma_f32_16x16x32_bf16 v[80:83], a[24:27], v[140:143], v[80:83]\n\t" \
        "ds_write_b64 v11, v[64:65] offset:4096\n\t" \
        "v_cvt_pk_bf16_f32 v76, v76, v77\n\t" \
        "v_mfma_f32_16x16x32_bf16 v[84:87], a[28:31], v[140:143], v[84:87]\n\t" \
        "ds_read_b128 v[132:135], v156 offset:128\n\t" \
        "v_cvt_pk_bf16_f32 v77, v78, v79\n\t" \
        "v_mfma_f32_16x16x32_bf16 v[88:91], a[32:35], v[140:143], v[88:91]\n\t" \
        "ds_write_b64 v11, v[76:77] offset:6144\n\t" \
        "v_cvt_pk_bf16_f32 v32, v32, v33\n\t" \
        "s_waitcnt lgkmcnt(6)\n\t" \
        "v_mfma_f32_16x16x32_bf16 v[80:83], a[36:39], v[144:147], v[80:83]\n\t" \
        "ds_read_b128 v[136:139], v160 offset:0\n\t" \
        "v_cvt_pk_bf16_f32 v33, v34, v35\n\t" \
        "v_mfma_f32_16x16x32_bf16 v[84:87], a[40:43], v[144:147], v[84:87]\n\t" \
        "ds_read_b128 v[140:143], v160 offset:64\n\t" \
        "v_cvt_pk_bf16_f32 v34, v36, v37\n\t" \
        "v_mfma_f32_16x16x32_bf16 v[88:91], a[44:47], v[144:147], v[88:91]\n\t" \
        "ds_read_b128 v[144:147], v160 offset:128\n\t" \
        "v_cvt_pk_bf16_f32 v35, v38, v39\n\t" \
        "v_mfma_f32_16x16x32_bf16 v[80:83], a[48:51], v[116:119], v[80:83]\n\t" \
        "v_permlane16_swap_b32 v32, v34\n\t" \
        "v_permlane16_swap_b32 v33, v35\n\t" \
        "v_mfma_f32_16x16x32_bf16 v[84:87], a[52:55], v[116:119], v[84:87]\n\t" \
        "v_mfma_f32_16x16x32_bf16 v[88:91], a[56:59], v[116:119], v[88:91]\n\t" \
        "ds_read_b128 v[116:119], v164 offset:0\n\t" \
        "v_mfma_f32_16x16x32_bf16 v[80:83], a[60:63], v[120:123], v[80:83]\n\t" \
        "v_mfma_f32_16x16x32_bf16 v[84:87], a[64:67], v[120:123], v[84:87]\n\t" \
        "v_mfma_f32_16x16x32_bf16 v[88:91], a[68:71], v[120:123], v[88:91]\n\t" \
        "ds_read_b128 v[120:123], v164 offset:64\n\t" \
        "v_mfma_f32_16x16x32_bf16 v[80:83], a[72:75], v[124:127], v[80:83]\n\t" \
        "v_mfma_f32_16x16x32_bf16 v[84:87], a[76:79], v[124:127], v[84:87]\n\t" \
        "s_waitcnt lgkmcnt(5)\n\t" \
        "s_barrier\n\t" \
        "ds_read_b64 v[42:43], v12 offset:0\n\t" \
        "v_mfma_f32_16x16x32_bf16 v[88:91], a[80:83], v[124:127], v[88:91]\n\t" \
        "ds_read_b128 v[124:127], v164 offset:128\n\t" \
        "v_mfma_f32_16x16x32_bf16 v[80:83], a[84:87], v[128:131], v[80:83]\n\t" \
        "v_mfma_f32_16x16x32_bf16 v[84:87], a[88:91], v[128:131], v[84:87]\n\t" \
        "v_mfma_f32_16x16x32_bf16 v[88:91], a[92:95], v[128:131], v[88:91]\n\t" \
        "ds_read_b128 v[128:131], v168 offset:0\n\t" \
        "v_mfma_f32_16x16x32_bf16 v[80:83], a[96:99], v[132:135], v[80:83]\n\t" \
        "s_mov_b32 s56, s64\n\t" \
        "buffer_store_dwordx4 v[32:35], v9, s[28:31], s56 offen sc1\n\t" \
        "v_mfma_f32_16x16x32_bf16 v[84:87], a[100:103], v[132:135], v[84:87]\n\t" \
        "v_mfma_f32_16x16x32_bf16 v[88:91], a[104:107], v[132:135], v[88:91]\n\t" \
        "ds_read_b128 v[132:135], v168 offset:64\n\t" \
        "s_waitcnt lgkmcnt(5)\n\t" \
        "v_mfma_f32_16x16x32_bf16 v[80:83], a[108:111], v[136:139], v[80:83]\n\t" \
        "s_waitcnt lgkmcnt(3)\n\t" \
        "v_permlane16_swap_b32 v40, v42\n\t" \
        "v_permlane16_swap_b32 v41, v43\n\t" \
        "v_mfma_f32_16x16x32_bf16 v[84:87], a[112:115], v[136:139], v[84:87]\n\t" \
        "v_mfma_f32_16x16x32_bf16 v[88:91], a[116:119], v[136:139], v[88:91]\n\t" \
        "ds_read_b128 v[136:139], v168 offset:128\n\t" \
        "v_mfma_f32_16x16x32_bf16 v[80:83], a[120:123], v[140:143], v[80:83]\n\t" \
        "v_mfma_f32_16x16x32_bf16 v[84:87], a[124:127], v[140:143], v[84:87]\n\t" \
        "s_mov_b32 s56, s64\n\t" \
        "buffer_store_dwordx4 v[40:43], v10, s[28:31], s56 offen sc1\n\t" \
        "v_mfma_f32_16x16x32_bf16 v[88:91], a[128:131], v[140:143], v[88:91]\n\t" \
        "ds_read_b128 v[140:143], v172 offset:0\n\t" \
        "v_cvt_pk_bf16_f32 v44, v44, v45\n\t" \
        "v_mfma_f32_16x16x32_bf16 v[80:83], a[132:135], v[144:147], v[80:83]\n\t" \
        "v_cvt_pk_bf16_f32 v45, v46, v47\n\t" \
        "v_cvt_pk_bf16_f32 v46, v48, v49\n\t" \
        "v_mfma_f32_16x16x32_bf16 v[84:87], a[136:139], v[144:147], v[84:87]\n\t" \
        "v_cvt_pk_bf16_f32 v47, v50, v51\n\t" \
        "v_permlane16_swap_b32 v44, v46\n\t" \
        "v_mfma_f32_16x16x32_bf16 v[88:91], a[140:143], v[144:147], v[88:91]\n\t" \
        "ds_read_b128 v[144:147], v172 offset:64\n\t" \
        "v_permlane16_swap_b32 v45, v47\n\t" \
        "v_mfma_f32_16x16x32_bf16 v[80:83], a[144:147], v[116:119], v[80:83]\n\t" \
        "ds_read_b64 v[54:55], v12 offset:2048\n\t" \
        "v_mfma_f32_16x16x32_bf16 v[84:87], a[148:151], v[116:119], v[84:87]\n\t" \
        "v_mfma_f32_16x16x32_bf16 v[88:91], a[152:155], v[116:119], v[88:91]\n\t" \
        "ds_read_b128 v[116:119], v172 offset:128\n\t" \
        "v_mfma_f32_16x16x32_bf16 v[80:83], a[156:159], v[120:123], v[80:83]\n\t" \
        "v_mfma_f32_16x16x32_bf16 v[84:87], a[160:163], v[120:123], v[84:87]\n\t" \
        "v_mfma_f32_16x16x32_bf16 v[88:91], a[164:167], v[120:123], v[88:91]\n\t" \
        "ds_read_b128 v[120:123], v176 offset:0\n\t" \
        "s_waitcnt lgkmcnt(5)\n\t" \
        "v_mfma_f32_16x16x32_bf16 v[80:83], a[168:171], v[124:127], v[80:83]\n\t" \
        "v_mfma_f32_16x16x32_bf16 v[84:87], a[172:175], v[124:127], v[84:87]\n\t" \
        "v_mfma_f32_16x16x32_bf16 v[88:91], a[176:179], v[124:127], v[88:91]\n\t" \
        "ds_read_b128 v[124:127], v176 offset:64\n\t" \
        "s_waitcnt lgkmcnt(3)\n\t" \
        "v_permlane16_swap_b32 v52, v54\n\t" \
        "v_mfma_f32_16x16x32_bf16 v[80:83], a[180:183], v[128:131], v[80:83]\n\t" \
        "v_permlane16_swap_b32 v53, v55\n\t" \
        "v_mfma_f32_16x16x32_bf16 v[84:87], a[184:187], v[128:131], v[84:87]\n\t" \
        "v_mfma_f32_16x16x32_bf16 v[88:91], a[188:191], v[128:131], v[88:91]\n\t" \
        "ds_read_b128 v[128:131], v176 offset:128\n\t" \
        "v_mfma_f32_16x16x32_bf16 v[80:83], a[192:195], v[132:135], v[80:83]\n\t" \
        "v_mfma_f32_16x16x32_bf16 v[84:87], a[196:199], v[132:135], v[84:87]\n\t" \
        "v_mfma_f32_16x16x32_bf16 v[88:91], a[200:203], v[132:135], v[88:91]\n\t" \
        "ds_read_b128 v[132:135], v180 offset:0\n\t" \
        "v_mfma_f32_16x16x32_bf16 v[80:83], a[204:207], v[136:139], v[80:83]\n\t" \
        "v_mfma_f32_16x16x32_bf16 v[84:87], a[208:211], v[136:139], v[84:87]\n\t" \
        "v_mfma_f32_16x16x32_bf16 v[88:91], a[212:215], v[136:139], v[88:91]\n\t" \
        "ds_read_b128 v[136:139], v180 offset:64\n\t" \
        "v_mfma_f32_16x16x32_bf16 v[80:83], a[216:219], v[140:143], v[80:83]\n\t" \
        "s_add_u32 s56, s64, 0x15000\n\t" \
        "buffer_store_dwordx4 v[44:47], v9, s[28:31], s56 offen sc1\n\t" \
        "v_mfma_f32_16x16x32_bf16 v[84:87], a[220:223], v[140:143], v[84:87]\n\t" \
        "v_mfma_f32_16x16x32_bf16 v[88:91], a[224:227], v[140:143], v[88:91]\n\t" \
        "ds_read_b128 v[140:143], v180 offset:128\n\t" \
        "v_mfma_f32_16x16x32_bf16 v[80:83], a[228:231], v[144:147], v[80:83]\n\t" \
        "v_mfma_f32_16x16x32_bf16 v[84:87], a[232:235], v[144:147], v[84:87]\n\t" \
        "v_mfma_f32_16x16x32_bf16 v[88:91], a[236:239], v[144:147], v[88:91]\n\t" \
        "ds_read_b128 v[144:147], v149 offset:0\n\t" \
        "s_waitcnt lgkmcnt(4)\n\t" \
        "v_mfma_f32_16x16x32_bf16 v[80:83], a[240:243], v[116:119], v[80:83]\n\t" \
        "v_mfma_f32_16x16x32_bf16 v[84:87], a[244:247], v[116:119], v[84:87]\n\t" \
        "s_add_u32 s56, s64, 0x15000\n\t" \
        "buffer_store_dwordx4 v[52:55], v10, s[28:31], s56 offen sc1\n\t" \
        "v_mfma_f32_16x16x32_bf16 v[88:91], a[248:251], v[116:119], v[88:91]\n\t" \
        "ds_read_b128 v[116:119], v149 offset:64\n\t" \
        "v_cvt_pk_bf16_f32 v56, v56, v57\n\t" \
        "v_mfma_f32_16x16x32_bf16 v[80:83], a[252:255], v[120:123], v[80:83]\n\t" \
        "v_cvt_pk_bf16_f32 v57, v58, v59\n\t" \
        "v_cvt_pk_bf16_f32 v58, v60, v61\n\t" \
        "v_mfma_f32_16x16x32_bf16 v[84:87], v[188:191], v[120:123], v[84:87]\n\t" \
        "v_cvt_pk_bf16_f32 v59, v62, v63\n\t" \
        "v_permlane16_swap_b32 v56, v58\n\t" \
        "v_mfma_f32_16x16x32_bf16 v[88:91], v[192:195], v[120:123], v[88:91]\n\t" \
        "ds_read_b128 v[120:123], v149 offset:128\n\t" \
        "v_permlane16_swap_b32 v57, v59\n\t" \
        "v_mfma_f32_16x16x32_bf16 v[80:83], v[196:199], v[124:127], v[80:83]\n\t" \
        "ds_read_b64 v[66:67], v12 offset:4096\n\t" \
        "v_mfma_f32_16x16x32_bf16 v[84:87], v[200:203], v[124:127], v[84:87]\n\t" \
        "v_mfma_f32_16x16x32_bf16 v[88:91], v[204:207], v[124:127], v[88:91]\n\t" \
        "ds_read_b128 v[124:127], v153 offset:0\n\t" \
        "v_mfma_f32_16x16x32_bf16 v[80:83], v[208:211], v[128:131], v[80:83]\n\t" \
        "v_mfma_f32_16x16x32_bf16 v[84:87], v[212:215], v[128:131], v[84:87]\n\t" \
        "v_mfma_f32_16x16x32_bf16 v[88:91], v[216:219], v[128:131], v[88:91]\n\t" \
        "ds_read_b128 v[128:131], v153 offset:64\n\t" \
        "s_waitcnt lgkmcnt(5)\n\t" \
        "v_mfma_f32_16x16x32_bf16 v[80:83], v[220:223], v[132:135], v[80:83]\n\t" \
        "v_mfma_f32_16x16x32_bf16 v[84:87], v[224:227], v[132:135], v[84:87]\n\t" \
        "v_mfma_f32_16x16x32_bf16 v[88:91], v[228:231], v[132:135], v[88:91]\n\t" \
        "ds_read_b128 v[132:135], v153 offset:128\n\t" \
        "s_waitcnt lgkmcnt(3)\n\t" \
        "v_permlane16_swap_b32 v64, v66\n\t" \
        "v_mfma_f32_16x16x32_bf16 v[80:83], v[232:235], v[136:139], v[80:83]\n\t" \
        "v_permlane16_swap_b32 v65, v67\n\t" \
        "v_mfma_f32_16x16x32_bf16 v[84:87], v[236:239], v[136:139], v[84:87]\n\t" \
        "v_mfma_f32_16x16x32_bf16 v[88:91], v[240:243], v[136:139], v[88:91]\n\t" \
        "ds_read_b128 v[136:139], v157 offset:0\n\t" \
        "v_mfma_f32_16x16x32_bf16 v[80:83], v[244:247], v[140:143], v[80:83]\n\t" \
        "v_mfma_f32_16x16x32_bf16 v[84:87], v[248:251], v[140:143], v[84:87]\n\t" \
        "v_mfma_f32_16x16x32_bf16 v[88:91], v[252:255], v[140:143], v[88:91]\n\t" \
        "ds_read_b128 v[140:143], v157 offset:64\n\t" \
        "v_mfma_f32_16x16x32_bf16 v[92:95], a[0:3], v[144:147], 0\n\t" \
        "v_mfma_f32_16x16x32_bf16 v[96:99], a[4:7], v[144:147], 0\n\t" \
        "v_mfma_f32_16x16x32_bf16 v[100:103], a[8:11], v[144:147], 0\n\t" \
        "s_nop 5\n\t" \
        "v_cvt_pk_bf16_f32 v88, v88, v89\n\t" \
        "v_cvt_pk_bf16_f32 v89, v90, v91\n\t" \
        "v_mfma_f32_16x16x32_bf16 v[92:95], a[12:15], v[116:119], v[92:95]\n\t" \
        "s_add_u32 s56, s64, 0x2a000\n\t" \
        "buffer_store_dwordx4 v[56:59], v9, s[28:31], s56 offen sc1\n\t" \
        "v_mfma_f32_16x16x32_bf16 v[96:99], a[16:19], v[116:119], v[96:99]\n\t" \
        "ds_write_b64 v11, v[88:89] offset:8192\n\t" \
        "v_cvt_pk_bf16_f32 v80, v80, v81\n\t" \
        "v_mfma_f32_16x16x32_bf16 v[100:103], a[20:23], v[116:119], v[100:103]\n\t" \
        "ds_read_b128 v[144:147], v157 offset:128\n\t" \
        "v_cvt_pk_bf16_f32 v81, v82, v83\n\t" \
        "v_mfma_f32_16x16x32_bf16 v[92:95], a[24:27], v[120:123], v[92:95]\n\t" \
        "ds_read_b128 v[116:119], v161 offset:0\n\t" \
        "v_cvt_pk_bf16_f32 v82, v84, v85\n\t" \
        "v_mfma_f32_16x16x32_bf16 v[96:99], a[28:31], v[120:123], v[96:99]\n\t" \
        "v_cvt_pk_bf16_f32 v83, v86, v87\n\t" \
        "v_permlane16_swap_b32 v80, v82\n\t" \
        "v_mfma_f32_16x16x32_bf16 v[100:103], a[32:35], v[120:123], v[100:103]\n\t" \
        "ds_read_b128 v[120:123], v161 offset:64\n\t" \
        "v_permlane16_swap_b32 v81, v83\n\t" \
        "s_waitcnt lgkmcnt(5)\n\t" \
        "v_mfma_f32_16x16x32_bf16 v[92:95], a[36:39], v[124:127], v[92:95]\n\t" \
        "v_mfma_f32_16x16x32_bf16 v[96:99], a[40:43], v[124:127], v[96:99]\n\t" \
        "s_add_u32 s56, s64, 0x2a000\n\t" \
        "buffer_store_dwordx4 v[64:67], v10, s[28:31], s56 offen sc1\n\t" \
        "v_mfma_f32_16x16x32_bf16 v[100:103], a[44:47], v[124:127], v[100:103]\n\t" \
        "ds_read_b128 v[124:127], v161 offset:128\n\t" \
        "v_cvt_pk_bf16_f32 v68, v68, v69\n\t" \
        "v_mfma_f32_16x16x32_bf16 v[92:95], a[48:51], v[128:131], v[92:95]\n\t" \
        "v_cvt_pk_bf16_f32 v69, v70, v71\n\t" \
        "v_cvt_pk_bf16_f32 v70, v72, v73\n\t" \
        "v_mfma_f32_16x16x32_bf16 v[96:99], a[52:55], v[128:131], v[96:99]\n\t" \
        "s_waitcnt lgkmcnt(4)\n\t" \
        "s_barrier\n\t" \
        "ds_read_b64 v[90:91], v12 offset:8192\n\t" \
        "v_cvt_pk_bf16_f32 v71, v74, v75\n\t" \
        "v_mfma_f32_16x16x32_bf16 v[100:103], a[56:59], v[128:131], v[100:103]\n\t" \
        "ds_read_b128 v[128:131], v165 offset:0\n\t" \
        "v_permlane16_swap_b32 v68, v70\n\t" \
        "v_mfma_f32_16x16x32_bf16 v[92:95], a[60:63], v[132:135], v[92:95]\n\t" \
        "ds_read_b64 v[78:79], v12 offset:6144\n\t" \
        "v_permlane16_swap_b32 v69, v71\n\t" \
        "v_mfma_f32_16x16x32_bf16 v[96:99], a[64:67], v[132:135], v[96:99]\n\t" \
        "v_mfma_f32_16x16x32_bf16 v[100:103], a[68:71], v[132:135], v[100:103]\n\t" \
        "ds_read_b128 v[132:135], v165 offset:64\n\t" \
        "v_mfma_f32_16x16x32_bf16 v[92:95], a[72:75], v[136:139], v[92:95]\n\t" \
        "v_mfma_f32_16x16x32_bf16 v[96:99], a[76:79], v[136:139], v[96:99]\n\t" \
        "v_mfma_f32_16x16x32_bf16 v[100:103], a[80:83], v[136:139], v[100:103]\n\t" \
        "ds_read_b128 v[136:139], v165 offset:128\n\t" \
        "v_mfma_f32_16x16x32_bf16 v[92:95], a[84:87], v[140:143], v[92:95]\n\t" \
        "s_waitcnt lgkmcnt(4)\n\t" \
        "v_permlane16_swap_b32 v88, v90\n\t" \
        "v_permlane16_swap_b32 v89, v91\n\t" \
        "v_mfma_f32_16x16x32_bf16 v[96:99], a[88:91], v[140:143], v[96:99]\n\t" \
        "v_mfma_f32_16x16x32_bf16 v[100:103], a[92:95], v[140:143], v[100:103]\n\t" \
        "ds_read_b128 v[140:143], v169 offset:0\n\t" \
        "s_waitcnt lgkmcnt(3)\n\t" \
        "v_permlane16_swap_b32 v76, v78\n\t" \
        "v_mfma_f32_16x16x32_bf16 v[92:95], a[96:99], v[144:147], v[92:95]\n\t" \
        "s_add_u32 s56, s64, 0x54000\n\t" \
        "buffer_store_dwordx4 v[80:83], v9, s[28:31], s56 offen sc1\n\t" \
        "v_mfma_f32_16x16x32_bf16 v[96:99], a[100:103], v[144:147], v[96:99]\n\t" \
        "v_permlane16_swap_b32 v77, v79\n\t" \
        "v_mfma_f32_16x16x32_bf16 v[100:103], a[104:107], v[144:147], v[100:103]\n\t" \
        "ds_read_b128 v[144:147], v169 offset:64\n\t" \
        "v_mfma_f32_16x16x32_bf16 v[92:95], a[108:111], v[116:119], v[92:95]\n\t" \
        "v_mfma_f32_16x16x32_bf16 v[96:99], a[112:115], v[116:119], v[96:99]\n\t" \
        "v_mfma_f32_16x16x32_bf16 v[100:103], a[116:119], v[116:119], v[100:103]\n\t" \
        "ds_read_b128 v[116:119], v169 offset:128\n\t" \
        "v_mfma_f32_16x16x32_bf16 v[92:95], a[120:123], v[120:123], v[92:95]\n\t" \
        "v_mfma_f32_16x16x32_bf16 v[96:99], a[124:127], v[120:123], v[96:99]\n\t" \
        "s_add_u32 s56, s64, 0x54000\n\t" \
        "buffer_store_dwordx4 v[88:91], v10, s[28:31], s56 offen sc1\n\t" \
        "v_mfma_f32_16x16x32_bf16 v[100:103], a[128:131], v[120:123], v[100:103]\n\t" \
        "ds_read_b128 v[120:123], v173 offset:0\n\t" \
        "v_mfma_f32_16x16x32_bf16 v[92:95], a[132:135], v[124:127], v[92:95]\n\t" \
        "s_add_u32 s56, s64, 0x3f000\n\t" \
        "buffer_store_dwordx4 v[68:71], v9, s[28:31], s56 offen sc1\n\t" \
        "v_mfma_f32_16x16x32_bf16 v[96:99], a[136:139], v[124:127], v[96:99]\n\t" \
        "v_mfma_f32_16x16x32_bf16 v[100:103], a[140:143], v[124:127], v[100:103]\n\t" \
        "ds_read_b128 v[124:127], v173 offset:64\n\t" \
        "v_mfma_f32_16x16x32_bf16 v[92:95], a[144:147], v[128:131], v[92:95]\n\t" \
        "v_mfma_f32_16x16x32_bf16 v[96:99], a[148:151], v[128:131], v[96:99]\n\t" \
        "v_mfma_f32_16x16x32_bf16 v[100:103], a[152:155], v[128:131], v[100:103]\n\t" \
        "ds_read_b128 v[128:131], v173 offset:128\n\t" \
        "s_waitcnt lgkmcnt(4)\n\t" \
        "v_mfma_f32_16x16x32_bf16 v[92:95], a[156:159], v[132:135], v[92:95]\n\t" \
        "v_mfma_f32_16x16x32_bf16 v[96:99], a[160:163], v[132:135], v[96:99]\n\t" \
        "s_add_u32 s56, s64, 0x3f000\n\t" \
        "buffer_store_dwordx4 v[76:79], v10, s[28:31], s56 offen sc1\n\t" \
        "v_mfma_f32_16x16x32_bf16 v[100:103], a[164:167], v[132:135], v[100:103]\n\t" \
        "ds_read_b128 v[132:135], v177 offset:0\n\t" \
        "v_mfma_f32_16x16x32_bf16 v[92:95], a[168:171], v[136:139], v[92:95]\n\t" \
        "v_mfma_f32_16x16x32_bf16 v[96:99], a[172:175], v[136:139], v[96:99]\n\t" \
        "v_mfma_f32_16x16x32_bf16 v[100:103], a[176:179], v[136:139], v[100:103]\n\t" \
        "ds_read_b128 v[136:139], v177 offset:64\n\t" \
        "v_mfma_f32_16x16x32_bf16 v[92:95], a[180:183], v[140:143], v[92:95]\n\t" \
        "v_mfma_f32_16x16x32_bf16 v[96:99], a[184:187], v[140:143], v[96:99]\n\t" \
        "v_mfma_f32_16x16x32_bf16 v[100:103], a[188:191], v[140:143], v[100:103]\n\t" \
        "ds_read_b128 v[140:143], v177 offset:128\n\t" \
        "v_mfma_f32_16x16x32_bf16 v[92:95], a[192:195], v[144:147], v[92:95]\n\t" \
        "v_mfma_f32_16x16x32_bf16 v[96:99], a[196:199], v[144:147], v[96:99]\n\t" \
        "v_mfma_f32_16x16x32_bf16 v[100:103], a[200:203], v[144:147], v[100:103]\n\t" \
        "ds_read_b128 v[144:147], v181 offset:0\n\t" \
        "s_waitcnt lgkmcnt(4)\n\t" \
        "v_mfma_f32_16x16x32_bf16 v[92:95], a[204:207], v[116:119], v[92:95]\n\t" \
        "v_mfma_f32_16x16x32_bf16 v[96:99], a[208:211], v[116:119], v[96:99]\n\t" \
        "v_mfma_f32_16x16x32_bf16 v[100:103], a[212:215], v[116:119], v[100:103]\n\t" \
        "ds_read_b128 v[116:119], v181 offset:64\n\t" \
        "v_mfma_f32_16x16x32_bf16 v[92:95], a[216:219], v[120:123], v[92:95]\n\t" \
        "v_mfma_f32_16x16x32_bf16 v[96:99], a[220:223], v[120:123], v[96:99]\n\t" \
        "v_mfma_f32_16x16x32_bf16 v[100:103], a[224:227], v[120:123], v[100:103]\n\t" \
        "ds_read_b128 v[120:123], v181 offset:128\n\t" \
        "v_mfma_f32_16x16x32_bf16 v[92:95], a[228:231], v[124:127], v[92:95]\n\t" \
        "v_mfma_f32_16x16x32_bf16 v[96:99], a[232:235], v[124:127], v[96:99]\n\t" \
        "v_mfma_f32_16x16x32_bf16 v[100:103], a[236:239], v[124:127], v[100:103]\n\t" \
        "v_mfma_f32_16x16x32_bf16 v[92:95], a[240:243], v[128:131], v[92:95]\n\t" \
        "v_mfma_f32_16x16x32_bf16 v[96:99], a[244:247], v[128:131], v[96:99]\n\t" \
        "v_mfma_f32_16x16x32_bf16 v[100:103], a[248:251], v[128:131], v[100:103]\n\t" \
        "s_waitcnt lgkmcnt(2)\n\t" \
        "v_mfma_f32_16x16x32_bf16 v[92:95], a[252:255], v[132:135], v[92:95]\n\t" \
        "v_mfma_f32_16x16x32_bf16 v[96:99], v[188:191], v[132:135], v[96:99]\n\t" \
        "v_mfma_f32_16x16x32_bf16 v[100:103], v[192:195], v[132:135], v[100:103]\n\t" \
        "v_mfma_f32_16x16x32_bf16 v[92:95], v[196:199], v[136:139], v[92:95]\n\t" \
        "v_mfma_f32_16x16x32_bf16 v[96:99], v[200:203], v[136:139], v[96:99]\n\t" \
        "v_mfma_f32_16x16x32_bf16 v[100:103], v[204:207], v[136:139], v[100:103]\n\t" \
        "v_mfma_f32_16x16x32_bf16 v[92:95], v[208:211], v[140:143], v[92:95]\n\t" \
        "v_mfma_f32_16x16x32_bf16 v[96:99], v[212:215], v[140:143], v[96:99]\n\t" \
        "v_mfma_f32_16x16x32_bf16 v[100:103], v[216:219], v[140:143], v[100:103]\n\t" \
        "v_mfma_f32_16x16x32_bf16 v[92:95], v[220:223], v[144:147], v[92:95]\n\t" \
        "v_mfma_f32_16x16x32_bf16 v[96:99], v[224:227], v[144:147], v[96:99]\n\t" \
        "v_mfma_f32_16x16x32_bf16 v[100:103], v[228:231], v[144:147], v[100:103]\n\t" \
        "s_waitcnt lgkmcnt(0)\n\t" \
        "v_mfma_f32_16x16x32_bf16 v[92:95], v[232:235], v[116:119], v[92:95]\n\t" \
        "v_mfma_f32_16x16x32_bf16 v[96:99], v[236:239], v[116:119], v[96:99]\n\t" \
        "v_mfma_f32_16x16x32_bf16 v[100:103], v[240:243], v[116:119], v[100:103]\n\t" \
        "v_mfma_f32_16x16x32_bf16 v[92:95], v[244:247], v[120:123], v[92:95]\n\t" \
        "v_mfma_f32_16x16x32_bf16 v[96:99], v[248:251], v[120:123], v[96:99]\n\t" \
        "v_mfma_f32_16x16x32_bf16 v[100:103], v[252:255], v[120:123], v[100:103]\n\t" \
        "s_nop 9\n\t" \
        "v_cvt_pk_bf16_f32 v100, v100, v101\n\t" \
        "v_cvt_pk_bf16_f32 v101, v102, v103\n\t" \
        "v_cvt_pk_bf16_f32 v92, v92, v93\n\t" \
        "v_cvt_pk_bf16_f32 v93, v94, v95\n\t" \
        "v_cvt_pk_bf16_f32 v94, v96, v97\n\t" \
        "ds_write_b64 v11, v[100:101] offset:10240\n\t" \
        "v_cvt_pk_bf16_f32 v95, v98, v99\n\t" \
        "v_permlane16_swap_b32 v92, v94\n\t" \
        "s_nop 0\n\t" \
        "v_permlane16_swap_b32 v93, v95\n\t" \
        "s_add_u32 s56, s64, 0x69000\n\t" \
        "buffer_store_dwordx4 v[92:95], v9, s[28:31], s56 offen sc1\n\t" \
        "s_waitcnt lgkmcnt(0)\n\t" \
        "s_barrier\n\t" \
        "ds_read_b64 v[102:103], v12 offset:10240\n\t" \
        "s_waitcnt lgkmcnt(0)\n\t" \
        "v_permlane16_swap_b32 v100, v102\n\t" \
        "v_permlane16_swap_b32 v101, v103\n\t" \
        "s_add_u32 s56, s64, 0x69000\n\t" \
        "buffer_store_dwordx4 v[100:103], v10, s[28:31], s56 offen sc1\n\t" \
        "s_cmp_lg_u32 s3, 0\n\t" \
        "s_cbranch_scc1 .LT_again%=\n\t" \
        "s_endpgm\n\t" \
        ".LR_start%=:\n\t" \
        "s_add_u32 s57, s40, 0x3000\n\t" \
        "s_add_u32 m0, s39, 0x3000\n\t" \
        "s_nop 0\n\t" \
        "buffer_load_dwordx4 v3, s[24:27], s57 offen offset:0 lds\n\t" \
        "v_lshrrev_b32 v38, 4, v2\n\t" \
        "s_lshr_b32 s42, s35, 1\n\t" \
        "s_and_b32 s43, s35, 1\n\t" \
        "v_and_b32 v19, 15, v2\n\t" \
        "s_lshl3_add_u32 s42, s42, s43\n\t" \
        "s_mul_i32 s37, s32, 224\n\t" \
        "v_lshl_add_u32 v18, v38, 1, s42\n\t" \
        "buffer_load_dwordx4 v4, s[24:27], s57 offen offset:1024 lds\n\t" \
        "s_add_u32 s37, s37, s33\n\t" \
        "v_mul_u32_u24 v38, 0x1f80, v18\n\t" \
        "s_mul_i32 s37, s37, 36\n\t" \
        "v_mad_u32_u24 v17, v19, 36, v38\n\t" \
        "s_and_b32 s61, s35, 1\n\t" \
        "v_lshrrev_b32 v40, 4, v2\n\t" \
        "v_and_b32 v41, 15, v2\n\t" \
        "buffer_load_dwordx4 v5, s[24:27], s57 offen offset:2048 lds\n\t" \
        "v_and_b32 v42, 16, v2\n\t" \
        "s_lshr_b32 s62, s35, 1\n\t" \
        "v_lshlrev_b32 v42, 1, v42\n\t" \
        "v_bfe_u32 v43, v2, 2, 2\n\t" \
        "v_mul_u32_u24 v45, 0xc0, v41\n\t" \
        "v_lshl_or_b32 v43, v40, 3, v43\n\t" \
        "v_and_b32 v44, 3, v2\n\t" \
        "s_add_u32 s57, s36, 0x1f800\n\t" \
        "s_add_u32 m0, s38, 0x3e00\n\t" \
        "s_nop 0\n\t" \
        "buffer_load_dwordx4 v13, s[16:19], s57 offen sc1 lds\n\t" \
        "v_mul_u32_u24 v43, 0xc0, v43\n\t" \
        "s_lshl_b32 s63, s61, 7\n\t" \
        "v_lshl_add_u32 v43, v44, 3, v43\n\t" \
        "s_add_u32 s63, s63, 0xf840\n\t" \
        "v_add3_u32 v6, v43, v42, s63\n\t" \
        "s_lshl_b32 s63, s61, 5\n\t" \
        "v_xor_b32 v44, s63, v42\n\t" \
        "s_add_u32 s57, s36, 0x2a000\n\t" \
        "s_add_u32 m0, s38, 0x4d80\n\t" \
        "s_nop 0\n\t" \
        "buffer_load_dwordx4 v13, s[16:19], s57 offen sc1 lds\n\t" \
        "v_xor_b32 v7, 32, v6\n\t" \
        "v_add_u32 v43, 0xf880, v43\n\t" \
        "s_lshl_b32 s63, s61, 7\n\t" \
        "v_add_u32 v8, v43, v44\n\t" \
        "v_lshrrev_b32 v43, 1, v2\n\t" \
        "v_and_b32 v43, 16, v43\n\t" \
        "v_add_u32 v45, v45, v43\n\t" \
        "s_add_u32 s57, s36, 0x34800\n\t" \
        "s_add_u32 m0, s38, 0x5d00\n\t" \
        "s_nop 0\n\t" \
        "buffer_load_dwordx4 v13, s[16:19], s57 offen sc1 lds\n\t" \
        "v_bfe_u32 v43, v2, 3, 1\n\t" \
        "v_add3_u32 v9, v45, v42, s63\n\t" \
        "v_cmp_eq_u32 vcc, s61, v43\n\t" \
        "v_add3_u32 v10, v45, v44, 64\n\t" \
        "v_bfrev_b32 v44, 1\n\t" \
        "s_lshl_b32 s63, s35, 9\n\t" \
        "v_cndmask_b32 v10, v44, v10, vcc\n\t" \
        "s_add_u32 s57, s36, 0x3f000\n\t" \
        "s_add_u32 m0, s38, 0x6c80\n\t" \
        "s_nop 0\n\t" \
        "buffer_load_dwordx4 v13, s[16:19], s57 offen sc1 lds\n\t" \
        "s_add_u32 s63, s63, 0x21840\n\t" \
        "v_lshl_add_u32 v11, v2, 3, s63\n\t" \
        "s_lshl_b32 s63, s61, 10\n\t" \
        "s_sub_u32 s63, 0x200, s63\n\t" \
        "v_add_u32 v12, s63, v11\n\t" \
        "s_add_u32 s64, s32, s62\n\t" \
        "s_mul_i32 s63, s33, 0xc0\n\t" \
        "s_add_u32 s57, s40, 0x6000\n\t" \
        "s_add_u32 m0, s39, 0x6000\n\t" \
        "s_nop 0\n\t" \
        "buffer_load_dwordx4 v3, s[24:27], s57 offen offset:0 lds\n\t" \
        "s_mul_i32 s64, s64, 0xa800\n\t" \
        "s_add_u32 s64, s64, s63\n\t" \
        "v_and_b32 v38, 48, v2\n\t" \
        "v_mul_u32_u24 v37, 0xc0, v19\n\t" \
        "v_lshlrev_b32 v36, 3, v19\n\t" \
        "s_mul_i32 s63, s62, 0xf80\n\t" \
        "v_add_u32 v39, 8, v36\n\t" \
        "buffer_load_dwordx4 v4, s[24:27], s57 offen offset:1024 lds\n\t" \
        "s_add_u32 s63, s63, 0x100\n\t" \
        "v_bitop3_b32 v39, v39, 32, v38 bitop3:0x6a\n\t" \
        "v_add3_u32 v14, v37, v39, s63\n\t" \
        "v_add_u32 v39, 16, v36\n\t" \
        "s_add_u32 s63, s63, 0xc0\n\t" \
        "v_bitop3_b32 v39, v39, 32, v38 bitop3:0x6a\n\t" \
        "v_add3_u32 v15, v37, v39, s63\n\t" \
        "buffer_load_dwordx4 v5, s[24:27], s57 offen offset:2048 lds\n\t" \
        "v_add_u32 v39, 24, v36\n\t" \
        "s_add_u32 s63, s63, 0xc0\n\t" \
        "v_bitop3_b32 v39, v39, 32, v38 bitop3:0x6a\n\t" \
        "v_add3_u32 v16, v37, v39, s63\n\t" \
        "v_add_u32 v20, 0xf000, v6\n\t" \
        "v_add_u32 v21, 0xf000, v7\n\t" \
        "v_add_u32 v22, 0xf000, v8\n\t" \
        "s_add_u32 s57, s40, 0x9000\n\t" \
        "s_add_u32 m0, s39, 0x9000\n\t" \
        "s_nop 0\n\t" \
        "buffer_load_dwordx4 v3, s[24:27], s57 offen offset:0 lds\n\t" \
        "v_add_u32 v38, s32, v18\n\t" \
        "v_add_u32 v39, s33, v19\n\t" \
        "v_lshl_add_u32 v38, v38, 1, -3\n\t" \
        "v_lshl_add_u32 v39, v39, 1, -3\n\t" \
        "v_cvt_f32_u32 v38, v38\n\t" \
        "v_cvt_f32_u32 v39, v39\n\t" \
        "v_lshrrev_b32 v38, 16, v38\n\t" \
        "buffer_load_dwordx4 v4, s[24:27], s57 offen offset:1024 lds\n\t" \
        "v_and_b32 v39, 0xffff0000, v39\n\t" \
        "v_or_b32 v38, v38, v39\n\t" \
        "v_and_b32 v38, 0x7f807f80, v38\n\t" \
        "v_subrev_u32 v23, 0x04810481, v38\n\t" \
        "s_lshl_b32 s45, s35, 2\n\t" \
        "v_mov_b32 v24, s45\n\t" \
        "buffer_load_dwordx4 v5, s[24:27], s57 offen offset:2048 lds\n\t" \
        "s_waitcnt vmcnt(15)\n\t" \
        "s_barrier\n\t" \
        "ds_read_b128 v[116:119], v14 offset:0\n\t" \
        "ds_read_b64_tr_b16 a[0:1], v6 offset:0\n\t" \
        "ds_read_b64_tr_b16 a[2:3], v6 offset:768\n\t" \
        "ds_read_b64_tr_b16 a[4:5], v7 offset:0\n\t" \
        "ds_read_b64_tr_b16 a[6:7], v7 offset:768\n\t" \
        "ds_read_b64_tr_b16 a[8:9], v8 offset:0\n\t" \
        "ds_read_b64_tr_b16 a[10:11], v8 offset:768\n\t" \
        "ds_read_b128 v[120:123], v14 offset:64\n\t" \
        "ds_read_b64_tr_b16 a[12:13], v6 offset:6144\n\t" \
        "ds_read_b64_tr_b16 a[14:15], v6 offset:6912\n\t" \
        "ds_read_b64_tr_b16 a[16:17], v7 offset:6144\n\t" \
        "ds_read_b64_tr_b16 a[18:19], v7 offset:6912\n\t" \
        "ds_read_b64_tr_b16 a[20:21], v8 offset:6144\n\t" \
        "ds_read_b64_tr_b16 a[22:23], v8 offset:6912\n\t" \
        "s_add_u32 s57, s40, 0xc000\n\t" \
        "s_add_u32 m0, s39, 0xc000\n\t" \
        "s_waitcnt lgkmcnt(11)\n\t" \
        "v_mfma_f32_16x16x32_bf16 v[32:35], a[0:3], v[116:119], 0\n\t" \
        "s_waitcnt vmcnt(13) lgkmcnt(0)\n\t" \
        "s_barrier\n\t" \
        "ds_read_b128 v[124:127], v14 offset:7936\n\t" \
        "v_mfma_f32_16x16x32_bf16 v[36:39], a[4:7], v[116:119], 0\n\t" \
        "buffer_load_dwordx4 v3, s[24:27], s57 offen offset:0 lds\n\t" \
        "v_mfma_f32_16x16x32_bf16 v[40:43], a[8:11], v[116:119], 0\n\t" \
        "ds_read_b128 v[128:131], v14 offset:8000\n\t" \
        "v_mfma_f32_16x16x32_bf16 v[32:35], a[12:15], v[120:123], v[32:35]\n\t" \
        "ds_read_b128 v[132:135], v14 offset:128\n\t" \
        "v_mfma_f32_16x16x32_bf16 v[36:39], a[16:19], v[120:123], v[36:39]\n\t" \
        "buffer_load_dwordx4 v4, s[24:27], s57 offen offset:1024 lds\n\t" \
        "v_mfma_f32_16x16x32_bf16 v[40:43], a[20:23], v[120:123], v[40:43]\n\t" \
        "s_waitcnt vmcnt(12)\n\t" \
        "s_barrier\n\t" \
        "ds_read_b64_tr_b16 a[24:25], v6 offset:12288\n\t" \
        "ds_read_b64_tr_b16 a[26:27], v6 offset:13056\n\t" \
        "ds_read_b64_tr_b16 a[28:29], v7 offset:12288\n\t" \
        "s_waitcnt lgkmcnt(5)\n\t" \
        "v_mfma_f32_16x16x32_bf16 v[44:47], a[0:3], v[124:127], 0\n\t" \
        "ds_read_b64_tr_b16 a[30:31], v7 offset:13056\n\t" \
        "ds_read_b64_tr_b16 a[32:33], v8 offset:12288\n\t" \
        "v_mfma_f32_16x16x32_bf16 v[48:51], a[4:7], v[124:127], 0\n\t" \
        "ds_read_b64_tr_b16 a[34:35], v8 offset:13056\n\t" \
        "v_mfma_f32_16x16x32_bf16 v[52:55], a[8:11], v[124:127], 0\n\t" \
        "ds_read_b128 v[136:139], v14 offset:8064\n\t" \
        "ds_read_b64_tr_b16 a[36:37], v6 offset:18432\n\t" \
        "ds_read_b64_tr_b16 a[38:39], v6 offset:19200\n\t" \
        "ds_read_b64_tr_b16 a[40:41], v7 offset:18432\n\t" \
        "ds_read_b64_tr_b16 a[42:43], v7 offset:19200\n\t" \
        "ds_read_b64_tr_b16 a[44:45], v8 offset:18432\n\t" \
        "ds_read_b64_tr_b16 a[46:47], v8 offset:19200\n\t" \
        "s_waitcnt lgkmcnt(14)\n\t" \
        "v_mfma_f32_16x16x32_bf16 v[44:47], a[12:15], v[128:131], v[44:47]\n\t" \
        "buffer_load_dwordx4 v5, s[24:27], s57 offen offset:2048 lds\n\t" \
        "v_mfma_f32_16x16x32_bf16 v[48:51], a[16:19], v[128:131], v[48:51]\n\t" \
        "ds_read_b128 v[140:143], v15 offset:0\n\t" \
        "s_add_u32 s57, s36, 0x49800\n\t" \
        "s_add_u32 m0, s38, 0x7c00\n\t" \
        "v_mfma_f32_16x16x32_bf16 v[52:55], a[20:23], v[128:131], v[52:55]\n\t" \
        "s_waitcnt vmcnt(11) lgkmcnt(1)\n\t" \
        "s_barrier\n\t" \
        "ds_read_b128 v[144:147], v15 offset:7936\n\t" \
        "v_mfma_f32_16x16x32_bf16 v[32:35], a[24:27], v[132:135], v[32:35]\n\t" \
        "buffer_load_dwordx4 v13, s[16:19], s57 offen sc1 lds\n\t" \
        "v_mfma_f32_16x16x32_bf16 v[36:39], a[28:31], v[132:135], v[36:39]\n\t" \
        "ds_read_b128 v[116:119], v14 offset:15872\n\t" \
        "v_mfma_f32_16x16x32_bf16 v[40:43], a[32:35], v[132:135], v[40:43]\n\t" \
        "ds_read_b128 v[120:123], v14 offset:15936\n\t" \
        "s_add_u32 s57, s36, 0x54000\n\t" \
        "s_add_u32 m0, s38, 0x8b80\n\t" \
        "v_mfma_f32_16x16x32_bf16 v[44:47], a[24:27], v[136:139], v[44:47]\n\t" \
        "ds_read_b128 v[124:127], v14 offset:16000\n\t" \
        "v_mfma_f32_16x16x32_bf16 v[48:51], a[28:31], v[136:139], v[48:51]\n\t" \
        "buffer_load_dwordx4 v13, s[16:19], s57 offen sc1 lds\n\t" \
        "v_mfma_f32_16x16x32_bf16 v[52:55], a[32:35], v[136:139], v[52:55]\n\t" \
        "ds_read_b128 v[128:131], v15 offset:15872\n\t" \
        "s_waitcnt lgkmcnt(5)\n\t" \
        "v_mfma_f32_16x16x32_bf16 v[32:35], a[36:39], v[140:143], v[32:35]\n\t" \
        "s_add_u32 s57, s40, 0xf000\n\t" \
        "s_add_u32 m0, s39, 0xf000\n\t" \
        "v_mfma_f32_16x16x32_bf16 v[36:39], a[40:43], v[140:143], v[36:39]\n\t" \
        "v_mfma_f32_16x16x32_bf16 v[40:43], a[44:47], v[140:143], v[40:43]\n\t" \
        "buffer_load_dwordx4 v3, s[24:27], s57 offen offset:0 lds\n\t" \
        "s_waitcnt lgkmcnt(4)\n\t" \
        "v_mfma_f32_16x16x32_bf16 v[44:47], a[36:39], v[144:147], v[44:47]\n\t" \
        "v_mfma_f32_16x16x32_bf16 v[48:51], a[40:43], v[144:147], v[48:51]\n\t" \
        "v_mfma_f32_16x16x32_bf16 v[52:55], a[44:47], v[144:147], v[52:55]\n\t" \
        "s_waitcnt vmcnt(12)\n\t" \
        "s_barrier\n\t" \
        "ds_read_b128 v[132:135], v14 offset:23808\n\t" \
        "s_waitcnt lgkmcnt(4)\n\t" \
        "v_mfma_f32_16x16x32_bf16 v[56:59], a[0:3], v[116:119], 0\n\t" \
        "buffer_load_dwordx4 v4, s[24:27], s57 offen offset:1024 lds\n\t" \
        "v_mfma_f32_16x16x32_bf16 v[60:63], a[4:7], v[116:119], 0\n\t" \
        "ds_read_b128 v[136:139], v14 offset:23872\n\t" \
        "v_mfma_f32_16x16x32_bf16 v[64:67], a[8:11], v[116:119], 0\n\t" \
        "ds_read_b128 v[140:143], v14 offset:23936\n\t" \
        "s_waitcnt lgkmcnt(4)\n\t" \
        "v_mfma_f32_16x16x32_bf16 v[56:59], a[12:15], v[120:123], v[56:59]\n\t" \
        "ds_read_b128 v[144:147], v15 offset:23808\n\t" \
        "v_mfma_f32_16x16x32_bf16 v[60:63], a[16:19], v[120:123], v[60:63]\n\t" \
        "buffer_load_dwordx4 v5, s[24:27], s57 offen offset:2048 lds\n\t" \
        "v_mfma_f32_16x16x32_bf16 v[64:67], a[20:23], v[120:123], v[64:67]\n\t" \
        "ds_read_b128 v[116:119], v15 offset:64\n\t" \
        "v_mfma_f32_16x16x32_bf16 v[56:59], a[24:27], v[124:127], v[56:59]\n\t" \
        "ds_read_b128 v[120:123], v15 offset:8000\n\t" \
        "s_add_u32 s57, s40, 0x12000\n\t" \
        "s_add_u32 m0, s39, 0x12000\n\t" \
        "v_mfma_f32_16x16x32_bf16 v[60:63], a[28:31], v[124:127], v[60:63]\n\t" \
        "v_mfma_f32_16x16x32_bf16 v[64:67], a[32:35], v[124:127], v[64:67]\n\t" \
        "buffer_load_dwordx4 v3, s[24:27], s57 offen offset:0 lds\n\t" \
        "s_waitcnt lgkmcnt(6)\n\t" \
        "v_mfma_f32_16x16x32_bf16 v[56:59], a[36:39], v[128:131], v[56:59]\n\t" \
        "ds_read_b128 v[124:127], v15 offset:15936\n\t" \
        "v_mfma_f32_16x16x32_bf16 v[60:63], a[40:43], v[128:131], v[60:63]\n\t" \
        "v_mfma_f32_16x16x32_bf16 v[64:67], a[44:47], v[128:131], v[64:67]\n\t" \
        "s_waitcnt vmcnt(12)\n\t" \
        "s_barrier\n\t" \
        "ds_read_b64_tr_b16 a[48:49], v6 offset:24576\n\t" \
        "s_waitcnt lgkmcnt(6)\n\t" \
        "v_mfma_f32_16x16x32_bf16 v[68:71], a[0:3], v[132:135], 0\n\t" \
        "buffer_load_dwordx4 v4, s[24:27], s57 offen offset:1024 lds\n\t" \
        "v_mfma_f32_16x16x32_bf16 v[72:75], a[4:7], v[132:135], 0\n\t" \
        "ds_read_b64_tr_b16 a[50:51], v6 offset:25344\n\t" \
        "v_mfma_f32_16x16x32_bf16 v[76:79], a[8:11], v[132:135], 0\n\t" \
        "ds_read_b64_tr_b16 a[52:53], v7 offset:24576\n\t" \
        "v_mfma_f32_16x16x32_bf16 v[68:71], a[12:15], v[136:139], v[68:71]\n\t" \
        "ds_read_b64_tr_b16 a[54:55], v7 offset:25344\n\t" \
        "v_mfma_f32_16x16x32_bf16 v[72:75], a[16:19], v[136:139], v[72:75]\n\t" \
        "buffer_load_dwordx4 v5, s[24:27], s57 offen offset:2048 lds\n\t" \
        "v_mfma_f32_16x16x32_bf16 v[76:79], a[20:23], v[136:139], v[76:79]\n\t" \
        "ds_read_b64_tr_b16 a[56:57], v8 offset:24576\n\t" \
        "s_waitcnt lgkmcnt(6)\n\t" \
        "v_mfma_f32_16x16x32_bf16 v[68:71], a[24:27], v[140:143], v[68:71]\n\t" \
        "ds_read_b64_tr_b16 a[58:59], v8 offset:25344\n\t" \
        "s_add_u32 s57, s40, 0x15000\n\t" \
        "s_add_u32 m0, s39, 0x15000\n\t" \
        "v_mfma_f32_16x16x32_bf16 v[72:75], a[28:31], v[140:143], v[72:75]\n\t" \
        "ds_read_b128 v[128:131], v15 offset:23872\n\t" \
        "v_mfma_f32_16x16x32_bf16 v[76:79], a[32:35], v[140:143], v[76:79]\n\t" \
        "buffer_load_dwordx4 v3, s[24:27], s57 offen offset:0 lds\n\t" \
        "v_mfma_f32_16x16x32_bf16 v[68:71], a[36:39], v[144:147], v[68:71]\n\t" \
        "ds_read_b128 v[132:135], v15 offset:128\n\t" \
        "v_mfma_f32_16x16x32_bf16 v[72:75], a[40:43], v[144:147], v[72:75]\n\t" \
        "ds_read_b64_tr_b16 a[60:61], v6 offset:30720\n\t" \
        "v_mfma_f32_16x16x32_bf16 v[76:79], a[44:47], v[144:147], v[76:79]\n\t" \
        "ds_read_b64_tr_b16 a[62:63], v6 offset:31488\n\t" \
        "s_waitcnt lgkmcnt(8)\n\t" \
        "v_mfma_f32_16x16x32_bf16 v[32:35], a[48:51], v[116:119], v[32:35]\n\t" \
        "buffer_load_dwordx4 v4, s[24:27], s57 offen offset:1024 lds\n\t" \
        "s_waitcnt lgkmcnt(6)\n\t" \
        "v_mfma_f32_16x16x32_bf16 v[36:39], a[52:55], v[116:119], v[36:39]\n\t" \
        "ds_read_b64_tr_b16 a[64:65], v7 offset:30720\n\t" \
        "s_waitcnt lgkmcnt(5)\n\t" \
        "v_mfma_f32_16x16x32_bf16 v[40:43], a[56:59], v[116:119], v[40:43]\n\t" \
        "ds_read_b64_tr_b16 a[66:67], v7 offset:31488\n\t" \
        "v_mfma_f32_16x16x32_bf16 v[44:47], a[48:51], v[120:123], v[44:47]\n\t" \
        "ds_read_b64_tr_b16 a[68:69], v8 offset:30720\n\t" \
        "v_mfma_f32_16x16x32_bf16 v[48:51], a[52:55], v[120:123], v[48:51]\n\t" \
        "buffer_load_dwordx4 v5, s[24:27], s57 offen offset:2048 lds\n\t" \
        "v_mfma_f32_16x16x32_bf16 v[52:55], a[56:59], v[120:123], v[52:55]\n\t" \
        "ds_read_b64_tr_b16 a[70:71], v8 offset:31488\n\t" \
        "v_mfma_f32_16x16x32_bf16 v[56:59], a[48:51], v[124:127], v[56:59]\n\t" \
        "ds_read_b128 v[136:139], v15 offset:8064\n\t" \
        "s_add_u32 s57, s40, 0x18000\n\t" \
        "s_mov_b32 m0, s39\n\t" \
        "v_mfma_f32_16x16x32_bf16 v[60:63], a[52:55], v[124:127], v[60:63]\n\t" \
        "ds_read_b128 v[140:143], v15 offset:16000\n\t" \
        "v_mfma_f32_16x16x32_bf16 v[64:67], a[56:59], v[124:127], v[64:67]\n\t" \
        "buffer_load_dwordx4 v3, s[24:27], s57 offen offset:0 lds\n\t" \
        "s_waitcnt lgkmcnt(7)\n\t" \
        "v_mfma_f32_16x16x32_bf16 v[68:71], a[48:51], v[128:131], v[68:71]\n\t" \
        "ds_read_b128 v[144:147], v15 offset:23936\n\t" \
        "v_mfma_f32_16x16x32_bf16 v[72:75], a[52:55], v[128:131], v[72:75]\n\t" \
        "ds_read_b128 v[116:119], v16 offset:0\n\t" \
        "v_mfma_f32_16x16x32_bf16 v[76:79], a[56:59], v[128:131], v[76:79]\n\t" \
        "s_waitcnt vmcnt(15) lgkmcnt(4)\n\t" \
        "s_barrier\n\t" \
        "ds_read_b64_tr_b16 a[72:73], v6 offset:36864\n\t" \
        "v_mfma_f32_16x16x32_bf16 v[32:35], a[60:63], v[132:135], v[32:35]\n\t" \
        "buffer_load_dwordx4 v4, s[24:27], s57 offen offset:1024 lds\n\t" \
        "v_mfma_f32_16x16x32_bf16 v[36:39], a[64:67], v[132:135], v[36:39]\n\t" \
        "ds_read_b64_tr_b16 a[74:75], v6 offset:37632\n\t" \
        "v_mfma_f32_16x16x32_bf16 v[40:43], a[68:71], v[132:135], v[40:43]\n\t" \
        "ds_read_b64_tr_b16 a[76:77], v7 offset:36864\n\t" \
        "s_waitcnt lgkmcnt(6)\n\t" \
        "v_mfma_f32_16x16x32_bf16 v[44:47], a[60:63], v[136:139], v[44:47]\n\t" \
        "ds_read_b64_tr_b16 a[78:79], v7 offset:37632\n\t" \
        "v_mfma_f32_16x16x32_bf16 v[48:51], a[64:67], v[136:139], v[48:51]\n\t" \
        "ds_read_b64_tr_b16 a[80:81], v8 offset:36864\n\t" \
        "v_mfma_f32_16x16x32_bf16 v[52:55], a[68:71], v[136:139], v[52:55]\n\t" \
        "ds_read_b64_tr_b16 a[82:83], v8 offset:37632\n\t" \
        "s_waitcnt lgkmcnt(8)\n\t" \
        "v_mfma_f32_16x16x32_bf16 v[56:59], a[60:63], v[140:143], v[56:59]\n\t" \
        "buffer_load_dwordx4 v5, s[24:27], s57 offen offset:2048 lds\n\t" \
        "v_mfma_f32_16x16x32_bf16 v[60:63], a[64:67], v[140:143], v[60:63]\n\t" \
        "ds_read_b128 v[120:123], v16 offset:7936\n\t" \
        "v_mfma_f32_16x16x32_bf16 v[64:67], a[68:71], v[140:143], v[64:67]\n\t" \
        "ds_read_b128 v[124:127], v16 offset:15872\n\t" \
        "s_waitcnt lgkmcnt(8)\n\t" \
        "v_mfma_f32_16x16x32_bf16 v[68:71], a[60:63], v[144:147], v[68:71]\n\t" \
        "ds_read_b128 v[128:131], v16 offset:23808\n\t" \
        "v_mfma_f32_16x16x32_bf16 v[72:75], a[64:67], v[144:147], v[72:75]\n\t" \
        "ds_read_b128 v[132:135], v16 offset:64\n\t" \
        "s_add_u32 s57, s40, 0x1b000\n\t" \
        "s_add_u32 m0, s39, 0x3000\n\t" \
        "v_mfma_f32_16x16x32_bf16 v[76:79], a[68:71], v[144:147], v[76:79]\n\t" \
        "ds_read_b64_tr_b16 a[84:85], v6 offset:43008\n\t" \
        "s_waitcnt lgkmcnt(9)\n\t" \
        "v_mfma_f32_16x16x32_bf16 v[32:35], a[72:75], v[116:119], v[32:35]\n\t" \
        "buffer_load_dwordx4 v3, s[24:27], s57 offen offset:0 lds\n\t" \
        "s_waitcnt lgkmcnt(7)\n\t" \
        "v_mfma_f32_16x16x32_bf16 v[36:39], a[76:79], v[116:119], v[36:39]\n\t" \
        "ds_read_b64_tr_b16 a[86:87], v6 offset:43776\n\t" \
        "s_waitcnt lgkmcnt(6)\n\t" \
        "v_mfma_f32_16x16x32_bf16 v[40:43], a[80:83], v[116:119], v[40:43]\n\t" \
        "ds_read_b64_tr_b16 a[88:89], v7 offset:43008\n\t" \
        "s_waitcnt lgkmcnt(6)\n\t" \
        "v_mfma_f32_16x16x32_bf16 v[44:47], a[72:75], v[120:123], v[44:47]\n\t" \
        "ds_read_b64_tr_b16 a[90:91], v7 offset:43776\n\t" \
        "v_mfma_f32_16x16x32_bf16 v[48:51], a[76:79], v[120:123], v[48:51]\n\t" \
        "ds_read_b64_tr_b16 a[92:93], v8 offset:43008\n\t" \
        "v_mfma_f32_16x16x32_bf16 v[52:55], a[80:83], v[120:123], v[52:55]\n\t" \
        "ds_read_b64_tr_b16 a[94:95], v8 offset:43776\n\t" \
        "s_waitcnt lgkmcnt(8)\n\t" \
        "v_mfma_f32_16x16x32_bf16 v[56:59], a[72:75], v[124:127], v[56:59]\n\t" \
        "buffer_load_dwordx4 v4, s[24:27], s57 offen offset:1024 lds\n\t" \
        "v_mfma_f32_16x16x32_bf16 v[60:63], a[76:79], v[124:127], v[60:63]\n\t" \
        "ds_read_b128 v[136:139], v16 offset:8000\n\t" \
        "v_mfma_f32_16x16x32_bf16 v[64:67], a[80:83], v[124:127], v[64:67]\n\t" \
        "ds_read_b128 v[140:143], v16 offset:15936\n\t" \
        "s_waitcnt lgkmcnt(8)\n\t" \
        "v_mfma_f32_16x16x32_bf16 v[68:71], a[72:75], v[128:131], v[68:71]\n\t" \
        "ds_read_b128 v[144:147], v16 offset:23872\n\t" \
        "v_mfma_f32_16x16x32_bf16 v[72:75], a[76:79], v[128:131], v[72:75]\n\t" \
        "ds_read_b128 v[116:119], v16 offset:128\n\t" \
        "v_mfma_f32_16x16x32_bf16 v[76:79], a[80:83], v[128:131], v[76:79]\n\t" \
        "s_waitcnt vmcnt(14) lgkmcnt(4)\n\t" \
        "s_barrier\n\t" \
        "ds_read_b64_tr_b16 a[96:97], v6 offset:49152\n\t" \
        "v_mfma_f32_16x16x32_bf16 v[32:35], a[84:87], v[132:135], v[32:35]\n\t" \
        "buffer_load_dwordx4 v5, s[24:27], s57 offen offset:2048 lds\n\t" \
        "v_mfma_f32_16x16x32_bf16 v[36:39], a[88:91], v[132:135], v[36:39]\n\t" \
        "ds_read_b64_tr_b16 a[98:99], v6 offset:49920\n\t" \
        "v_mfma_f32_16x16x32_bf16 v[40:43], a[92:95], v[132:135], v[40:43]\n\t" \
        "ds_read_b64_tr_b16 a[100:101], v7 offset:49152\n\t" \
        "s_waitcnt lgkmcnt(6)\n\t" \
        "v_mfma_f32_16x16x32_bf16 v[44:47], a[84:87], v[136:139], v[44:47]\n\t" \
        "ds_read_b64_tr_b16 a[102:103], v7 offset:49920\n\t" \
        "v_mfma_f32_16x16x32_bf16 v[48:51], a[88:91], v[136:139], v[48:51]\n\t" \
        "ds_read_b64_tr_b16 a[104:105], v8 offset:49152\n\t" \
        "v_mfma_f32_16x16x32_bf16 v[52:55], a[92:95], v[136:139], v[52:55]\n\t" \
        "ds_read_b64_tr_b16 a[106:107], v8 offset:49920\n\t" \
        "s_waitcnt lgkmcnt(8)\n\t" \
        "v_mfma_f32_16x16x32_bf16 v[56:59], a[84:87], v[140:143], v[56:59]\n\t" \
        "buffer_load_dwordx4 v[148:151], v17, s[20:23], s37 offen\n\t" \
        "v_mfma_f32_16x16x32_bf16 v[60:63], a[88:91], v[140:143], v[60:63]\n\t" \
        "ds_read_b128 v[120:123], v16 offset:8064\n\t" \
        "v_mfma_f32_16x16x32_bf16 v[64:67], a[92:95], v[140:143], v[64:67]\n\t" \
        "ds_read_b128 v[124:127], v16 offset:16000\n\t" \
        "s_waitcnt lgkmcnt(8)\n\t" \
        "v_mfma_f32_16x16x32_bf16 v[68:71], a[84:87], v[144:147], v[68:71]\n\t" \
        "ds_read_b128 v[128:131], v16 offset:23936\n\t" \
        "v_mfma_f32_16x16x32_bf16 v[72:75], a[88:91], v[144:147], v[72:75]\n\t" \
        "ds_read_b128 v[132:135], v14 offset:3968\n\t" \
        "v_mfma_f32_16x16x32_bf16 v[76:79], a[92:95], v[144:147], v[76:79]\n\t" \
        "ds_read_b64_tr_b16 a[108:109], v6 offset:55296\n\t" \
        "s_waitcnt lgkmcnt(9)\n\t" \
        "v_mfma_f32_16x16x32_bf16 v[32:35], a[96:99], v[116:119], v[32:35]\n\t" \
        "buffer_load_dwordx4 v[152:155], v17, s[20:23], s37 offen offset:16\n\t" \
        "s_waitcnt lgkmcnt(7)\n\t" \
        "v_mfma_f32_16x16x32_bf16 v[36:39], a[100:103], v[116:119], v[36:39]\n\t" \
        "ds_read_b64_tr_b16 a[110:111], v6 offset:56064\n\t" \
        "s_waitcnt lgkmcnt(6)\n\t" \
        "v_mfma_f32_16x16x32_bf16 v[40:43], a[104:107], v[116:119], v[40:43]\n\t" \
        "ds_read_b64_tr_b16 a[112:113], v7 offset:55296\n\t" \
        "s_waitcnt lgkmcnt(6)\n\t" \
        "v_mfma_f32_16x16x32_bf16 v[44:47], a[96:99], v[120:123], v[44:47]\n\t" \
        "ds_read_b64_tr_b16 a[114:115], v7 offset:56064\n\t" \
        "v_mfma_f32_16x16x32_bf16 v[48:51], a[100:103], v[120:123], v[48:51]\n\t" \
        "ds_read_b64_tr_b16 a[116:117], v8 offset:55296\n\t" \
        "v_mfma_f32_16x16x32_bf16 v[52:55], a[104:107], v[120:123], v[52:55]\n\t" \
        "ds_read_b64_tr_b16 a[118:119], v8 offset:56064\n\t" \
        "s_waitcnt lgkmcnt(8)\n\t" \
        "v_mfma_f32_16x16x32_bf16 v[56:59], a[96:99], v[124:127], v[56:59]\n\t" \
        "buffer_load_dword v156, v17, s[20:23], s37 offen offset:32\n\t" \
        "v_mfma_f32_16x16x32_bf16 v[60:63], a[100:103], v[124:127], v[60:63]\n\t" \
        "ds_read_b128 v[136:139], v14 offset:11904\n\t" \
        "v_mfma_f32_16x16x32_bf16 v[64:67], a[104:107], v[124:127], v[64:67]\n\t" \
        "ds_read_b128 v[140:143], v14 offset:19840\n\t" \
        "s_waitcnt lgkmcnt(8)\n\t" \
        "v_mfma_f32_16x16x32_bf16 v[68:71], a[96:99], v[128:131], v[68:71]\n\t" \
        "ds_read_b128 v[144:147], v14 offset:27776\n\t" \
        "v_mfma_f32_16x16x32_bf16 v[72:75], a[100:103], v[128:131], v[72:75]\n\t" \
        "ds_read_b128 v[116:119], v14 offset:4032\n\t" \
        "s_add_u32 s57, s40, 0x1e000\n\t" \
        "s_add_u32 m0, s39, 0x6000\n\t" \
        "v_mfma_f32_16x16x32_bf16 v[76:79], a[104:107], v[128:131], v[76:79]\n\t" \
        "s_waitcnt vmcnt(15) lgkmcnt(4)\n\t" \
        "s_barrier\n\t" \
        "ds_read_b64_tr_b16 a[120:121], v20 offset:0\n\t" \
        "v_mfma_f32_16x16x32_bf16 v[32:35], a[108:111], v[132:135], v[32:35]\n\t" \
        "buffer_load_dwordx4 v3, s[24:27], s57 offen offset:0 lds\n\t" \
        "v_mfma_f32_16x16x32_bf16 v[36:39], a[112:115], v[132:135], v[36:39]\n\t" \
        "ds_read_b64_tr_b16 a[122:123], v20 offset:768\n\t" \
        "v_mfma_f32_16x16x32_bf16 v[40:43], a[116:119], v[132:135], v[40:43]\n\t" \
        "ds_read_b64_tr_b16 a[124:125], v21 offset:0\n\t" \
        "s_waitcnt lgkmcnt(6)\n\t" \
        "v_mfma_f32_16x16x32_bf16 v[44:47], a[108:111], v[136:139], v[44:47]\n\t" \
        "ds_read_b64_tr_b16 a[126:127], v21 offset:768\n\t" \
        "v_mfma_f32_16x16x32_bf16 v[48:51], a[112:115], v[136:139], v[48:51]\n\t" \
        "ds_read_b64_tr_b16 a[128:129], v22 offset:0\n\t" \
        "v_mfma_f32_16x16x32_bf16 v[52:55], a[116:119], v[136:139], v[52:55]\n\t" \
        "ds_read_b64_tr_b16 a[130:131], v22 offset:768\n\t" \
        "s_waitcnt lgkmcnt(8)\n\t" \
        "v_mfma_f32_16x16x32_bf16 v[56:59], a[108:111], v[140:143], v[56:59]\n\t" \
        "buffer_load_dwordx4 v4, s[24:27], s57 offen offset:1024 lds\n\t" \
        "v_mfma_f32_16x16x32_bf16 v[60:63], a[112:115], v[140:143], v[60:63]\n\t" \
        "ds_read_b128 v[120:123], v14 offset:11968\n\t" \
        "v_mfma_f32_16x16x32_bf16 v[64:67], a[116:119], v[140:143], v[64:67]\n\t" \
        "ds_read_b128 v[124:127], v14 offset:19904\n\t" \
        "s_waitcnt lgkmcnt(8)\n\t" \
        "v_mfma_f32_16x16x32_bf16 v[68:71], a[108:111], v[144:147], v[68:71]\n\t" \
        "ds_read_b128 v[128:131], v14 offset:27840\n\t" \
        "v_mfma_f32_16x16x32_bf16 v[72:75], a[112:115], v[144:147], v[72:75]\n\t" \
        "ds_read_b128 v[132:135], v14 offset:4096\n\t" \
        "v_mfma_f32_16x16x32_bf16 v[76:79], a[116:119], v[144:147], v[76:79]\n\t" \
        "ds_read_b64_tr_b16 a[132:133], v20 offset:6144\n\t" \
        "s_waitcnt lgkmcnt(9)\n\t" \
        "v_mfma_f32_16x16x32_bf16 v[32:35], a[120:123], v[116:119], v[32:35]\n\t" \
        "buffer_load_dwordx4 v5, s[24:27], s57 offen offset:2048 lds\n\t" \
        "s_waitcnt lgkmcnt(7)\n\t" \
        "v_mfma_f32_16x16x32_bf16 v[36:39], a[124:127], v[116:119], v[36:39]\n\t" \
        "ds_read_b64_tr_b16 a[134:135], v20 offset:6912\n\t" \
        "s_waitcnt lgkmcnt(6)\n\t" \
        "v_mfma_f32_16x16x32_bf16 v[40:43], a[128:131], v[116:119], v[40:43]\n\t" \
        "ds_read_b64_tr_b16 a[136:137], v21 offset:6144\n\t" \
        "s_waitcnt lgkmcnt(6)\n\t" \
        "v_mfma_f32_16x16x32_bf16 v[44:47], a[120:123], v[120:123], v[44:47]\n\t" \
        "ds_read_b64_tr_b16 a[138:139], v21 offset:6912\n\t" \
        "v_mfma_f32_16x16x32_bf16 v[48:51], a[124:127], v[120:123], v[48:51]\n\t" \
        "ds_read_b64_tr_b16 a[140:141], v22 offset:6144\n\t" \
        "s_add_u32 s57, s40, 0x21000\n\t" \
        "s_add_u32 m0, s39, 0x9000\n\t" \
        "v_mfma_f32_16x16x32_bf16 v[52:55], a[128:131], v[120:123], v[52:55]\n\t" \
        "ds_read_b64_tr_b16 a[142:143], v22 offset:6912\n\t" \
        "s_waitcnt lgkmcnt(8)\n\t" \
        "v_mfma_f32_16x16x32_bf16 v[56:59], a[120:123], v[124:127], v[56:59]\n\t" \
        "buffer_load_dwordx4 v3, s[24:27], s57 offen offset:0 lds\n\t" \
        "v_mfma_f32_16x16x32_bf16 v[60:63], a[124:127], v[124:127], v[60:63]\n\t" \
        "ds_read_b128 v[136:139], v14 offset:12032\n\t" \
        "v_mfma_f32_16x16x32_bf16 v[64:67], a[128:131], v[124:127], v[64:67]\n\t" \
        "ds_read_b128 v[140:143], v14 offset:19968\n\t" \
        "s_waitcnt lgkmcnt(8)\n\t" \
        "v_mfma_f32_16x16x32_bf16 v[68:71], a[120:123], v[128:131], v[68:71]\n\t" \
        "ds_read_b128 v[144:147], v14 offset:27904\n\t" \
        "v_mfma_f32_16x16x32_bf16 v[72:75], a[124:127], v[128:131], v[72:75]\n\t" \
        "ds_read_b128 v[116:119], v15 offset:3968\n\t" \
        "v_mfma_f32_16x16x32_bf16 v[76:79], a[128:131], v[128:131], v[76:79]\n\t" \
        "s_waitcnt vmcnt(16) lgkmcnt(4)\n\t" \
        "s_barrier\n\t" \
        "ds_read_b64_tr_b16 a[144:145], v20 offset:12288\n\t" \
        "v_mfma_f32_16x16x32_bf16 v[32:35], a[132:135], v[132:135], v[32:35]\n\t" \
        "buffer_load_dwordx4 v4, s[24:27], s57 offen offset:1024 lds\n\t" \
        "v_mfma_f32_16x16x32_bf16 v[36:39], a[136:139], v[132:135], v[36:39]\n\t" \
        "ds_read_b64_tr_b16 a[146:147], v20 offset:13056\n\t" \
        "v_mfma_f32_16x16x32_bf16 v[40:43], a[140:143], v[132:135], v[40:43]\n\t" \
        "ds_read_b64_tr_b16 a[148:149], v21 offset:12288\n\t" \
        "s_waitcnt lgkmcnt(6)\n\t" \
        "v_mfma_f32_16x16x32_bf16 v[44:47], a[132:135], v[136:139], v[44:47]\n\t" \
        "ds_read_b64_tr_b16 a[150:151], v21 offset:13056\n\t" \
        "v_mfma_f32_16x16x32_bf16 v[48:51], a[136:139], v[136:139], v[48:51]\n\t" \
        "ds_read_b64_tr_b16 a[152:153], v22 offset:12288\n\t" \
        "v_mfma_f32_16x16x32_bf16 v[52:55], a[140:143], v[136:139], v[52:55]\n\t" \
        "ds_read_b64_tr_b16 a[154:155], v22 offset:13056\n\t" \
        "s_waitcnt lgkmcnt(8)\n\t" \
        "v_mfma_f32_16x16x32_bf16 v[56:59], a[132:135], v[140:143], v[56:59]\n\t" \
        "buffer_load_dwordx4 v5, s[24:27], s57 offen offset:2048 lds\n\t" \
        "v_mfma_f32_16x16x32_bf16 v[60:63], a[136:139], v[140:143], v[60:63]\n\t" \
        "ds_read_b128 v[120:123], v15 offset:11904\n\t" \
        "v_mfma_f32_16x16x32_bf16 v[64:67], a[140:143], v[140:143], v[64:67]\n\t" \
        "ds_read_b128 v[124:127], v15 offset:19840\n\t" \
        "s_waitcnt lgkmcnt(8)\n\t" \
        "v_mfma_f32_16x16x32_bf16 v[68:71], a[132:135], v[144:147], v[68:71]\n\t" \
        "ds_read_b128 v[128:131], v15 offset:27776\n\t" \
        "v_mfma_f32_16x16x32_bf16 v[72:75], a[136:139], v[144:147], v[72:75]\n\t" \
        "ds_read_b128 v[132:135], v15 offset:4032\n\t" \
        "s_add_u32 s57, s40, 0x24000\n\t" \
        "s_add_u32 m0, s39, 0xc000\n\t" \
        "v_mfma_f32_16x16x32_bf16 v[76:79], a[140:143], v[144:147], v[76:79]\n\t" \
        "ds_read_b64_tr_b16 a[156:157], v20 offset:18432\n\t" \
        "s_waitcnt lgkmcnt(9)\n\t" \
        "v_mfma_f32_16x16x32_bf16 v[32:35], a[144:147], v[116:119], v[32:35]\n\t" \
        "buffer_load_dwordx4 v3, s[24:27], s57 offen offset:0 lds\n\t" \
        "s_waitcnt lgkmcnt(7)\n\t" \
        "v_mfma_f32_16x16x32_bf16 v[36:39], a[148:151], v[116:119], v[36:39]\n\t" \
        "ds_read_b64_tr_b16 a[158:159], v20 offset:19200\n\t" \
        "s_waitcnt lgkmcnt(6)\n\t" \
        "v_mfma_f32_16x16x32_bf16 v[40:43], a[152:155], v[116:119], v[40:43]\n\t" \
        "ds_read_b64_tr_b16 a[160:161], v21 offset:18432\n\t" \
        "s_waitcnt lgkmcnt(6)\n\t" \
        "v_mfma_f32_16x16x32_bf16 v[44:47], a[144:147], v[120:123], v[44:47]\n\t" \
        "ds_read_b64_tr_b16 a[162:163], v21 offset:19200\n\t" \
        "v_mfma_f32_16x16x32_bf16 v[48:51], a[148:151], v[120:123], v[48:51]\n\t" \
        "ds_read_b64_tr_b16 a[164:165], v22 offset:18432\n\t" \
        "v_mfma_f32_16x16x32_bf16 v[52:55], a[152:155], v[120:123], v[52:55]\n\t" \
        "ds_read_b64_tr_b16 a[166:167], v22 offset:19200\n\t" \
        "s_waitcnt lgkmcnt(8)\n\t" \
        "v_mfma_f32_16x16x32_bf16 v[56:59], a[144:147], v[124:127], v[56:59]\n\t" \
        "buffer_load_dwordx4 v4, s[24:27], s57 offen offset:1024 lds\n\t" \
        "v_mfma_f32_16x16x32_bf16 v[60:63], a[148:151], v[124:127], v[60:63]\n\t" \
        "ds_read_b128 v[136:139], v15 offset:11968\n\t" \
        "v_mfma_f32_16x16x32_bf16 v[64:67], a[152:155], v[124:127], v[64:67]\n\t" \
        "ds_read_b128 v[140:143], v15 offset:19904\n\t" \
        "s_waitcnt lgkmcnt(8)\n\t" \
        "v_mfma_f32_16x16x32_bf16 v[68:71], a[144:147], v[128:131], v[68:71]\n\t" \
        "ds_read_b128 v[144:147], v15 offset:27840\n\t" \
        "v_mfma_f32_16x16x32_bf16 v[72:75], a[148:151], v[128:131], v[72:75]\n\t" \
        "ds_read_b128 v[116:119], v15 offset:4096\n\t" \
        "v_mfma_f32_16x16x32_bf16 v[76:79], a[152:155], v[128:131], v[76:79]\n\t" \
        "s_waitcnt vmcnt(17)\n\t" \
        "s_barrier\n\t" \
        "ds_read_b64_tr_b16 a[168:169], v20 offset:24576\n\t" \
        "s_waitcnt lgkmcnt(9)\n\t" \
        "v_mfma_f32_16x16x32_bf16 v[32:35], a[156:159], v[132:135], v[32:35]\n\t" \
        "buffer_load_dwordx4 v5, s[24:27], s57 offen offset:2048 lds\n\t" \
        "s_waitcnt lgkmcnt(7)\n\t" \
        "v_mfma_f32_16x16x32_bf16 v[36:39], a[160:163], v[132:135], v[36:39]\n\t" \
        "ds_read_b64_tr_b16 a[170:171], v20 offset:25344\n\t" \
        "s_waitcnt lgkmcnt(6)\n\t" \
        "v_mfma_f32_16x16x32_bf16 v[40:43], a[164:167], v[132:135], v[40:43]\n\t" \
        "ds_read_b64_tr_b16 a[172:173], v21 offset:24576\n\t" \
        "s_waitcnt lgkmcnt(6)\n\t" \
        "v_mfma_f32_16x16x32_bf16 v[44:47], a[156:159], v[136:139], v[44:47]\n\t" \
        "ds_read_b64_tr_b16 a[174:175], v21 offset:25344\n\t" \
        "v_mfma_f32_16x16x32_bf16 v[48:51], a[160:163], v[136:139], v[48:51]\n\t" \
        "ds_read_b64_tr_b16 a[176:177], v22 offset:24576\n\t" \
        "s_add_u32 s57, s40, 0x27000\n\t" \
        "s_add_u32 m0, s39, 0xf000\n\t" \
        "v_mfma_f32_16x16x32_bf16 v[52:55], a[164:167], v[136:139], v[52:55]\n\t" \
        "ds_read_b64_tr_b16 a[178:179], v22 offset:25344\n\t" \
        "s_waitcnt lgkmcnt(8)\n\t" \
        "v_mfma_f32_16x16x32_bf16 v[56:59], a[156:159], v[140:143], v[56:59]\n\t" \
        "buffer_load_dwordx4 v3, s[24:27], s57 offen offset:0 lds\n\t" \
        "v_mfma_f32_16x16x32_bf16 v[60:63], a[160:163], v[140:143], v[60:63]\n\t" \
        "ds_read_b128 v[120:123], v15 offset:12032\n\t" \
        "v_mfma_f32_16x16x32_bf16 v[64:67], a[164:167], v[140:143], v[64:67]\n\t" \
        "ds_read_b128 v[124:127], v15 offset:19968\n\t" \
        "s_waitcnt lgkmcnt(8)\n\t" \
        "v_mfma_f32_16x16x32_bf16 v[68:71], a[156:159], v[144:147], v[68:71]\n\t" \
        "ds_read_b128 v[128:131], v15 offset:27904\n\t" \
        "v_mfma_f32_16x16x32_bf16 v[72:75], a[160:163], v[144:147], v[72:75]\n\t" \
        "ds_read_b128 v[132:135], v16 offset:3968\n\t" \
        "v_mfma_f32_16x16x32_bf16 v[76:79], a[164:167], v[144:147], v[76:79]\n\t" \
        "ds_read_b64_tr_b16 a[180:181], v20 offset:30720\n\t" \
        "s_waitcnt lgkmcnt(9)\n\t" \
        "v_mfma_f32_16x16x32_bf16 v[32:35], a[168:171], v[116:119], v[32:35]\n\t" \
        "buffer_load_dwordx4 v4, s[24:27], s57 offen offset:1024 lds\n\t" \
        "s_waitcnt lgkmcnt(7)\n\t" \
        "v_mfma_f32_16x16x32_bf16 v[36:39], a[172:175], v[116:119], v[36:39]\n\t" \
        "ds_read_b64_tr_b16 a[182:183], v20 offset:31488\n\t" \
        "s_waitcnt lgkmcnt(6)\n\t" \
        "v_mfma_f32_16x16x32_bf16 v[40:43], a[176:179], v[116:119], v[40:43]\n\t" \
        "ds_read_b64_tr_b16 a[184:185], v21 offset:30720\n\t" \
        "s_waitcnt lgkmcnt(6)\n\t" \
        "v_mfma_f32_16x16x32_bf16 v[44:47], a[168:171], v[120:123], v[44:47]\n\t" \
        "ds_read_b64_tr_b16 a[186:187], v21 offset:31488\n\t" \
        "v_mfma_f32_16x16x32_bf16 v[48:51], a[172:175], v[120:123], v[48:51]\n\t" \
        "ds_read_b64_tr_b16 a[188:189], v22 offset:30720\n\t" \
        "v_mfma_f32_16x16x32_bf16 v[52:55], a[176:179], v[120:123], v[52:55]\n\t" \
        "ds_read_b64_tr_b16 a[190:191], v22 offset:31488\n\t" \
        "s_waitcnt lgkmcnt(8)\n\t" \
        "v_mfma_f32_16x16x32_bf16 v[56:59], a[168:171], v[124:127], v[56:59]\n\t" \
        "buffer_load_dwordx4 v5, s[24:27], s57 offen offset:2048 lds\n\t" \
        "v_mfma_f32_16x16x32_bf16 v[60:63], a[172:175], v[124:127], v[60:63]\n\t" \
        "ds_read_b128 v[136:139], v16 offset:11904\n\t" \
        "v_mfma_f32_16x16x32_bf16 v[64:67], a[176:179], v[124:127], v[64:67]\n\t" \
        "ds_read_b128 v[140:143], v16 offset:19840\n\t" \
        "s_waitcnt lgkmcnt(8)\n\t" \
        "v_mfma_f32_16x16x32_bf16 v[68:71], a[168:171], v[128:131], v[68:71]\n\t" \
        "ds_read_b128 v[144:147], v16 offset:27776\n\t" \
        "v_mfma_f32_16x16x32_bf16 v[72:75], a[172:175], v[128:131], v[72:75]\n\t" \
        "ds_read_b128 v[116:119], v16 offset:4032\n\t" \
        "s_add_u32 s57, s36, 0x5e800\n\t" \
        "s_add_u32 m0, s38, 0x9b00\n\t" \
        "v_mfma_f32_16x16x32_bf16 v[76:79], a[176:179], v[128:131], v[76:79]\n\t" \
        "s_waitcnt vmcnt(18)\n\t" \
        "s_barrier\n\t" \
        "ds_read_b64_tr_b16 a[192:193], v6 offset:0\n\t" \
        "s_waitcnt lgkmcnt(9)\n\t" \
        "v_mfma_f32_16x16x32_bf16 v[32:35], a[180:183], v[132:135], v[32:35]\n\t" \
        "buffer_load_dwordx4 v13, s[16:19], s57 offen sc1 lds\n\t" \
        "s_waitcnt lgkmcnt(7)\n\t" \
        "v_mfma_f32_16x16x32_bf16 v[36:39], a[184:187], v[132:135], v[36:39]\n\t" \
        "ds_read_b64_tr_b16 a[194:195], v6 offset:768\n\t" \
        "s_waitcnt lgkmcnt(6)\n\t" \
        "v_mfma_f32_16x16x32_bf16 v[40:43], a[188:191], v[132:135], v[40:43]\n\t" \
        "ds_read_b64_tr_b16 a[196:197], v7 offset:0\n\t" \
        "s_waitcnt lgkmcnt(6)\n\t" \
        "v_mfma_f32_16x16x32_bf16 v[44:47], a[180:183], v[136:139], v[44:47]\n\t" \
        "ds_read_b64_tr_b16 a[198:199], v7 offset:768\n\t" \
        "v_mfma_f32_16x16x32_bf16 v[48:51], a[184:187], v[136:139], v[48:51]\n\t" \
        "ds_read_b64_tr_b16 a[200:201], v8 offset:0\n\t" \
        "s_add_u32 s57, s36, 0x69000\n\t" \
        "s_add_u32 m0, s38, 0xaa80\n\t" \
        "v_mfma_f32_16x16x32_bf16 v[52:55], a[188:191], v[136:139], v[52:55]\n\t" \
        "ds_read_b64_tr_b16 a[202:203], v8 offset:768\n\t" \
        "s_waitcnt lgkmcnt(8)\n\t" \
        "v_mfma_f32_16x16x32_bf16 v[56:59], a[180:183], v[140:143], v[56:59]\n\t" \
        "buffer_load_dwordx4 v13, s[16:19], s57 offen sc1 lds\n\t" \
        "v_mfma_f32_16x16x32_bf16 v[60:63], a[184:187], v[140:143], v[60:63]\n\t" \
        "ds_read_b128 v[120:123], v16 offset:11968\n\t" \
        "v_mfma_f32_16x16x32_bf16 v[64:67], a[188:191], v[140:143], v[64:67]\n\t" \
        "ds_read_b128 v[124:127], v16 offset:19904\n\t" \
        "s_waitcnt lgkmcnt(8)\n\t" \
        "v_mfma_f32_16x16x32_bf16 v[68:71], a[180:183], v[144:147], v[68:71]\n\t" \
        "ds_read_b128 v[128:131], v16 offset:27840\n\t" \
        "v_mfma_f32_16x16x32_bf16 v[72:75], a[184:187], v[144:147], v[72:75]\n\t" \
        "ds_read_b128 v[132:135], v16 offset:4096\n\t" \
        "s_add_u32 s57, s36, 0x73800\n\t" \
        "s_add_u32 m0, s38, 0xba00\n\t" \
        "v_mfma_f32_16x16x32_bf16 v[76:79], a[188:191], v[144:147], v[76:79]\n\t" \
        "ds_read_b64_tr_b16 a[204:205], v6 offset:6144\n\t" \
        "s_waitcnt lgkmcnt(9)\n\t" \
        "v_mfma_f32_16x16x32_bf16 v[32:35], a[192:195], v[116:119], v[32:35]\n\t" \
        "buffer_load_dwordx4 v13, s[16:19], s57 offen sc1 lds\n\t" \
        "s_waitcnt lgkmcnt(7)\n\t" \
        "v_mfma_f32_16x16x32_bf16 v[36:39], a[196:199], v[116:119], v[36:39]\n\t" \
        "ds_read_b64_tr_b16 a[206:207], v6 offset:6912\n\t" \
        "s_waitcnt lgkmcnt(6)\n\t" \
        "v_mfma_f32_16x16x32_bf16 v[40:43], a[200:203], v[116:119], v[40:43]\n\t" \
        "ds_read_b64_tr_b16 a[208:209], v7 offset:6144\n\t" \
        "s_waitcnt lgkmcnt(6)\n\t" \
        "v_mfma_f32_16x16x32_bf16 v[44:47], a[192:195], v[120:123], v[44:47]\n\t" \
        "ds_read_b64_tr_b16 a[210:211], v7 offset:6912\n\t" \
        "v_mfma_f32_16x16x32_bf16 v[48:51], a[196:199], v[120:123], v[48:51]\n\t" \
        "ds_read_b64_tr_b16 a[212:213], v8 offset:6144\n\t" \
        "s_add_u32 s57, s36, 0x7e000\n\t" \
        "s_add_u32 m0, s38, 0xc980\n\t" \
        "v_mfma_f32_16x16x32_bf16 v[52:55], a[200:203], v[120:123], v[52:55]\n\t" \
        "ds_read_b64_tr_b16 a[214:215], v8 offset:6912\n\t" \
        "s_waitcnt lgkmcnt(8)\n\t" \
        "v_mfma_f32_16x16x32_bf16 v[56:59], a[192:195], v[124:127], v[56:59]\n\t" \
        "buffer_load_dwordx4 v13, s[16:19], s57 offen sc1 lds\n\t" \
        "v_mfma_f32_16x16x32_bf16 v[60:63], a[196:199], v[124:127], v[60:63]\n\t" \
        "ds_read_b128 v[136:139], v16 offset:12032\n\t" \
        "v_mfma_f32_16x16x32_bf16 v[64:67], a[200:203], v[124:127], v[64:67]\n\t" \
        "ds_read_b128 v[140:143], v16 offset:19968\n\t" \
        "s_waitcnt lgkmcnt(8)\n\t" \
        "v_mfma_f32_16x16x32_bf16 v[68:71], a[192:195], v[128:131], v[68:71]\n\t" \
        "ds_read_b128 v[144:147], v16 offset:27904\n\t" \
        "v_mfma_f32_16x16x32_bf16 v[72:75], a[196:199], v[128:131], v[72:75]\n\t" \
        "ds_read_b128 v[116:119], v14 offset:7936\n\t" \
        "s_add_u32 s57, s36, 0x88800\n\t" \
        "s_add_u32 m0, s38, 0xd900\n\t" \
        "v_mfma_f32_16x16x32_bf16 v[76:79], a[200:203], v[128:131], v[76:79]\n\t" \
        "s_waitcnt vmcnt(19) lgkmcnt(4)\n\t" \
        "s_barrier\n\t" \
        "ds_read_b64_tr_b16 a[216:217], v6 offset:12288\n\t" \
        "v_mfma_f32_16x16x32_bf16 v[32:35], a[204:207], v[132:135], v[32:35]\n\t" \
        "buffer_load_dwordx4 v13, s[16:19], s57 offen sc1 lds\n\t" \
        "v_mfma_f32_16x16x32_bf16 v[36:39], a[208:211], v[132:135], v[36:39]\n\t" \
        "ds_read_b64_tr_b16 a[218:219], v6 offset:13056\n\t" \
        "v_mfma_f32_16x16x32_bf16 v[40:43], a[212:215], v[132:135], v[40:43]\n\t" \
        "ds_read_b64_tr_b16 a[220:221], v7 offset:12288\n\t" \
        "s_waitcnt lgkmcnt(6)\n\t" \
        "v_mfma_f32_16x16x32_bf16 v[44:47], a[204:207], v[136:139], v[44:47]\n\t" \
        "ds_read_b64_tr_b16 a[222:223], v7 offset:13056\n\t" \
        "v_mfma_f32_16x16x32_bf16 v[48:51], a[208:211], v[136:139], v[48:51]\n\t" \
        "ds_read_b64_tr_b16 a[224:225], v8 offset:12288\n\t" \
        "s_add_u32 s57, s36, 0x93000\n\t" \
        "s_min_u32 s57, s57, 0x925800\n\t" \
        "s_add_u32 m0, s38, 0xe880\n\t" \
        "v_mfma_f32_16x16x32_bf16 v[52:55], a[212:215], v[136:139], v[52:55]\n\t" \
        "ds_read_b64_tr_b16 a[226:227], v8 offset:13056\n\t" \
        "s_waitcnt lgkmcnt(8)\n\t" \
        "v_mfma_f32_16x16x32_bf16 v[56:59], a[204:207], v[140:143], v[56:59]\n\t" \
        "buffer_load_dwordx4 v13, s[16:19], s57 offen sc1 lds\n\t" \
        "v_mfma_f32_16x16x32_bf16 v[60:63], a[208:211], v[140:143], v[60:63]\n\t" \
        "ds_read_b128 v[120:123], v14 offset:15872\n\t" \
        "v_mfma_f32_16x16x32_bf16 v[64:67], a[212:215], v[140:143], v[64:67]\n\t" \
        "ds_read_b128 v[124:127], v14 offset:23808\n\t" \
        "s_waitcnt lgkmcnt(8)\n\t" \
        "v_mfma_f32_16x16x32_bf16 v[68:71], a[204:207], v[144:147], v[68:71]\n\t" \
        "ds_read_b128 v[128:131], v14 offset:31744\n\t" \
        "v_mfma_f32_16x16x32_bf16 v[72:75], a[208:211], v[144:147], v[72:75]\n\t" \
        "ds_read_b128 v[132:135], v14 offset:8000\n\t" \
        "s_add_u32 s57, s36, 0x9d800\n\t" \
        "s_min_u32 s57, s57, 0x925800\n\t" \
        "s_add_u32 m0, s38, 0xf800\n\t" \
        "v_mfma_f32_16x16x32_bf16 v[76:79], a[212:215], v[144:147], v[76:79]\n\t" \
        "ds_read_b64_tr_b16 a[228:229], v6 offset:18432\n\t" \
        "s_waitcnt lgkmcnt(9)\n\t" \
        "v_mfma_f32_16x16x32_bf16 v[32:35], a[216:219], v[116:119], v[32:35]\n\t" \
        "buffer_load_dwordx4 v13, s[16:19], s57 offen sc1 lds\n\t" \
        "s_waitcnt lgkmcnt(7)\n\t" \
        "v_mfma_f32_16x16x32_bf16 v[36:39], a[220:223], v[116:119], v[36:39]\n\t" \
        "ds_read_b64_tr_b16 a[230:231], v6 offset:19200\n\t" \
        "s_waitcnt lgkmcnt(6)\n\t" \
        "v_mfma_f32_16x16x32_bf16 v[40:43], a[224:227], v[116:119], v[40:43]\n\t" \
        "ds_read_b64_tr_b16 a[232:233], v7 offset:18432\n\t" \
        "s_waitcnt lgkmcnt(6)\n\t" \
        "v_mfma_f32_16x16x32_bf16 v[44:47], a[216:219], v[120:123], v[44:47]\n\t" \
        "ds_read_b64_tr_b16 a[234:235], v7 offset:19200\n\t" \
        "v_mfma_f32_16x16x32_bf16 v[48:51], a[220:223], v[120:123], v[48:51]\n\t" \
        "ds_read_b64_tr_b16 a[236:237], v8 offset:18432\n\t" \
        "s_add_u32 s57, s36, 0xa8000\n\t" \
        "s_min_u32 s57, s57, 0x925800\n\t" \
        "s_add_u32 m0, s38, 0x10780\n\t" \
        "v_mfma_f32_16x16x32_bf16 v[52:55], a[224:227], v[120:123], v[52:55]\n\t" \
        "ds_read_b64_tr_b16 a[238:239], v8 offset:19200\n\t" \
        "s_waitcnt lgkmcnt(8)\n\t" \
        "v_mfma_f32_16x16x32_bf16 v[56:59], a[216:219], v[124:127], v[56:59]\n\t" \
        "buffer_load_dwordx4 v13, s[16:19], s57 offen sc1 lds\n\t" \
        "v_mfma_f32_16x16x32_bf16 v[60:63], a[220:223], v[124:127], v[60:63]\n\t" \
        "ds_read_b128 v[136:139], v14 offset:15936\n\t" \
        "v_mfma_f32_16x16x32_bf16 v[64:67], a[224:227], v[124:127], v[64:67]\n\t" \
        "ds_read_b128 v[140:143], v14 offset:23872\n\t" \
        "s_waitcnt lgkmcnt(8)\n\t" \
        "v_mfma_f32_16x16x32_bf16 v[68:71], a[216:219], v[128:131], v[68:71]\n\t" \
        "ds_read_b128 v[144:147], v14 offset:31808\n\t" \
        "v_mfma_f32_16x16x32_bf16 v[72:75], a[220:223], v[128:131], v[72:75]\n\t" \
        "ds_read_b128 v[116:119], v14 offset:8064\n\t" \
        "v_mfma_f32_16x16x32_bf16 v[76:79], a[224:227], v[128:131], v[76:79]\n\t" \
        "s_waitcnt vmcnt(17)\n\t" \
        "s_barrier\n\t" \
        "ds_read_b64_tr_b16 a[240:241], v6 offset:24576\n\t" \
        "v_and_b32 v148, 0x7fff7fff, v148\n\t" \
        "s_waitcnt lgkmcnt(9)\n\t" \
        "v_mfma_f32_16x16x32_bf16 v[32:35], a[228:231], v[132:135], v[32:35]\n\t" \
        "ds_read_b64_tr_b16 a[242:243], v6 offset:25344\n\t" \
        "v_and_b32 v149, 0x7fff7fff, v149\n\t" \
        "s_waitcnt lgkmcnt(8)\n\t" \
        "v_mfma_f32_16x16x32_bf16 v[36:39], a[232:235], v[132:135], v[36:39]\n\t" \
        "ds_read_b64_tr_b16 a[244:245], v7 offset:24576\n\t" \
        "v_and_b32 v150, 0x7fff7fff, v150\n\t" \
        "s_waitcnt lgkmcnt(7)\n\t" \
        "v_mfma_f32_16x16x32_bf16 v[40:43], a[236:239], v[132:135], v[40:43]\n\t" \
        "ds_read_b64_tr_b16 a[246:247], v7 offset:25344\n\t" \
        "v_and_b32 v151, 0x7fff7fff, v151\n\t" \
        "s_waitcnt lgkmcnt(7)\n\t" \
        "v_mfma_f32_16x16x32_bf16 v[44:47], a[228:231], v[136:139], v[44:47]\n\t" \
        "ds_read_b64_tr_b16 a[248:249], v8 offset:24576\n\t" \
        "v_and_b32 v152, 0x7fff7fff, v152\n\t" \
        "v_mfma_f32_16x16x32_bf16 v[48:51], a[232:235], v[136:139], v[48:51]\n\t" \
        "ds_read_b64_tr_b16 a[250:251], v8 offset:25344\n\t" \
        "v_and_b32 v153, 0x7fff7fff, v153\n\t" \
        "v_mfma_f32_16x16x32_bf16 v[52:55], a[236:239], v[136:139], v[52:55]\n\t" \
        "ds_read_b128 v[120:123], v14 offset:16000\n\t" \
        "v_and_b32 v154, 0x7fff7fff, v154\n\t" \
        "s_waitcnt lgkmcnt(9)\n\t" \
        "v_mfma_f32_16x16x32_bf16 v[56:59], a[228:231], v[140:143], v[56:59]\n\t" \
        "ds_read_b128 v[124:127], v14 offset:23936\n\t" \
        "v_and_b32 v155, 0x7fff7fff, v155\n\t" \
        "v_mfma_f32_16x16x32_bf16 v[60:63], a[232:235], v[140:143], v[60:63]\n\t" \
        "ds_read_b128 v[128:131], v14 offset:31872\n\t" \
        "v_and_b32 v156, 0x7fff7fff, v156\n\t" \
        "v_mfma_f32_16x16x32_bf16 v[64:67], a[236:239], v[140:143], v[64:67]\n\t" \
        "ds_read_b128 v[132:135], v15 offset:7936\n\t" \
        "v_pk_max_u16 v148, v148, v149\n\t" \
        "s_waitcnt lgkmcnt(10)\n\t" \
        "v_mfma_f32_16x16x32_bf16 v[68:71], a[228:231], v[144:147], v[68:71]\n\t" \
        "ds_read_b64_tr_b16 a[252:253], v6 offset:30720\n\t" \
        "v_pk_max_u16 v150, v150, v151\n\t" \
        "v_mfma_f32_16x16x32_bf16 v[72:75], a[232:235], v[144:147], v[72:75]\n\t" \
        "ds_read_b64_tr_b16 a[254:255], v6 offset:31488\n\t" \
        "v_pk_max_u16 v152, v152, v153\n\t" \
        "v_mfma_f32_16x16x32_bf16 v[76:79], a[236:239], v[144:147], v[76:79]\n\t" \
        "ds_read_b64_tr_b16 v[188:189], v7 offset:30720\n\t" \
        "v_pk_max_u16 v154, v154, v155\n\t" \
        "s_waitcnt lgkmcnt(10)\n\t" \
        "v_mfma_f32_16x16x32_bf16 v[32:35], a[240:243], v[116:119], v[32:35]\n\t" \
        "ds_read_b64_tr_b16 v[190:191], v7 offset:31488\n\t" \
        "v_pk_max_u16 v156, v156, v23\n\t" \
        "s_waitcnt lgkmcnt(10)\n\t" \
        "v_mfma_f32_16x16x32_bf16 v[36:39], a[244:247], v[116:119], v[36:39]\n\t" \
        "ds_read_b64_tr_b16 v[192:193], v8 offset:30720\n\t" \
        "v_pk_max_u16 v148, v148, v150\n\t" \
        "s_waitcnt lgkmcnt(9)\n\t" \
        "v_mfma_f32_16x16x32_bf16 v[40:43], a[248:251], v[116:119], v[40:43]\n\t" \
        "ds_read_b64_tr_b16 v[194:195], v8 offset:31488\n\t" \
        "v_pk_max_u16 v152, v152, v154\n\t" \
        "s_waitcnt lgkmcnt(9)\n\t" \
        "v_mfma_f32_16x16x32_bf16 v[44:47], a[240:243], v[120:123], v[44:47]\n\t" \
        "ds_read_b128 v[136:139], v15 offset:15872\n\t" \
        "v_pk_max_u16 v148, v148, v156\n\t" \
        "v_mfma_f32_16x16x32_bf16 v[48:51], a[244:247], v[120:123], v[48:51]\n\t" \
        "ds_read_b128 v[140:143], v15 offset:23808\n\t" \
        "v_pk_max_u16 v148, v148, v152\n\t" \
        "v_mfma_f32_16x16x32_bf16 v[52:55], a[248:251], v[120:123], v[52:55]\n\t" \
        "ds_read_b128 v[144:147], v15 offset:31744\n\t" \
        "v_cmp_ne_u32 vcc, v148, v23\n\t" \
        "s_nop 0\n\t" \
        "s_or_b32 s56, vcc_lo, vcc_hi\n\t" \
        "s_waitcnt lgkmcnt(10)\n\t" \
        "v_mfma_f32_16x16x32_bf16 v[56:59], a[240:243], v[124:127], v[56:59]\n\t" \
        "ds_read_b128 v[116:119], v15 offset:8000\n\t" \
        "v_mov_b32 v149, s56\n\t" \
        "v_mfma_f32_16x16x32_bf16 v[60:63], a[244:247], v[124:127], v[60:63]\n\t" \
        "ds_read_b128 v[120:123], v15 offset:15936\n\t" \
        "v_mfma_f32_16x16x32_bf16 v[64:67], a[248:251], v[124:127], v[64:67]\n\t" \
        "ds_read_b128 v[124:127], v15 offset:23872\n\t" \
        "v_mfma_f32_16x16x32_bf16 v[68:71], a[240:243], v[128:131], v[68:71]\n\t" \
        "ds_write_b32 v24, v149\n\t" \
        "v_mfma_f32_16x16x32_bf16 v[72:75], a[244:247], v[128:131], v[72:75]\n\t" \
        "v_mfma_f32_16x16x32_bf16 v[76:79], a[248:251], v[128:131], v[76:79]\n\t" \
        "s_waitcnt vmcnt(14)\n\t" \
        "s_barrier\n\t" \
        "ds_read_b64_tr_b16 v[196:197], v6 offset:36864\n\t" \
        "s_waitcnt lgkmcnt(9)\n\t" \
        "v_mfma_f32_16x16x32_bf16 v[32:35], a[252:255], v[132:135], v[32:35]\n\t" \
        "ds_read_b64_tr_b16 v[198:199], v6 offset:37632\n\t" \
        "v_mfma_f32_16x16x32_bf16 v[36:39], v[188:191], v[132:135], v[36:39]\n\t" \
        "ds_read_b64_tr_b16 v[200:201], v7 offset:36864\n\t" \
        "s_waitcnt lgkmcnt(9)\n\t" \
        "v_mfma_f32_16x16x32_bf16 v[40:43], v[192:195], v[132:135], v[40:43]\n\t" \
        "ds_read_b64_tr_b16 v[202:203], v7 offset:37632\n\t" \
        "v_mfma_f32_16x16x32_bf16 v[44:47], a[252:255], v[136:139], v[44:47]\n\t" \
        "ds_read_b64_tr_b16 v[204:205], v8 offset:36864\n\t" \
        "v_mov_b32 v184, 0\n\t" \
        "v_mfma_f32_16x16x32_bf16 v[48:51], v[188:191], v[136:139], v[48:51]\n\t" \
        "ds_read_b64_tr_b16 v[206:207], v8 offset:37632\n\t" \
        "v_mfma_f32_16x16x32_bf16 v[52:55], v[192:195], v[136:139], v[52:55]\n\t" \
        "ds_read_b128 v[128:131], v15 offset:31808\n\t" \
        "s_waitcnt lgkmcnt(9)\n\t" \
        "v_mfma_f32_16x16x32_bf16 v[56:59], a[252:255], v[140:143], v[56:59]\n\t" \
        "ds_read_b128 v[132:135], v15 offset:8064\n\t" \
        "v_mfma_f32_16x16x32_bf16 v[60:63], v[188:191], v[140:143], v[60:63]\n\t" \
        "ds_read_b64_tr_b16 v[208:209], v6 offset:43008\n\t" \
        "v_mfma_f32_16x16x32_bf16 v[64:67], v[192:195], v[140:143], v[64:67]\n\t" \
        "ds_read_b64_tr_b16 v[210:211], v6 offset:43776\n\t" \
        "v_mfma_f32_16x16x32_bf16 v[68:71], a[252:255], v[144:147], v[68:71]\n\t" \
        "ds_read_b64_tr_b16 v[212:213], v7 offset:43008\n\t" \
        "v_mfma_f32_16x16x32_bf16 v[72:75], v[188:191], v[144:147], v[72:75]\n\t" \
        "ds_read_b64_tr_b16 v[214:215], v7 offset:43776\n\t" \
        "v_mfma_f32_16x16x32_bf16 v[76:79], v[192:195], v[144:147], v[76:79]\n\t" \
        "ds_read_b64_tr_b16 v[216:217], v8 offset:43008\n\t" \
        "s_waitcnt lgkmcnt(10)\n\t" \
        "v_mfma_f32_16x16x32_bf16 v[32:35], v[196:199], v[116:119], v[32:35]\n\t" \
        "ds_read_b64_tr_b16 v[218:219], v8 offset:43776\n\t" \
        "s_waitcnt lgkmcnt(10)\n\t" \
        "v_mfma_f32_16x16x32_bf16 v[36:39], v[200:203], v[116:119], v[36:39]\n\t" \
        "ds_read_b128 v[136:139], v15 offset:16000\n\t" \
        "s_waitcnt lgkmcnt(9)\n\t" \
        "v_mfma_f32_16x16x32_bf16 v[40:43], v[204:207], v[116:119], v[40:43]\n\t" \
        "ds_read_b128 v[140:143], v15 offset:23936\n\t" \
        "v_mfma_f32_16x16x32_bf16 v[44:47], v[196:199], v[120:123], v[44:47]\n\t" \
        "ds_read_b128 v[144:147], v15 offset:31872\n\t" \
        "v_mfma_f32_16x16x32_bf16 v[48:51], v[200:203], v[120:123], v[48:51]\n\t" \
        "ds_read_b128 v[116:119], v16 offset:7936\n\t" \
        "v_mfma_f32_16x16x32_bf16 v[52:55], v[204:207], v[120:123], v[52:55]\n\t" \
        "ds_read_b128 v[120:123], v16 offset:15872\n\t" \
        "v_mfma_f32_16x16x32_bf16 v[56:59], v[196:199], v[124:127], v[56:59]\n\t" \
        "v_mfma_f32_16x16x32_bf16 v[60:63], v[200:203], v[124:127], v[60:63]\n\t" \
        "v_mfma_f32_16x16x32_bf16 v[64:67], v[204:207], v[124:127], v[64:67]\n\t" \
        "ds_read_b128 v[124:127], v16 offset:23808\n\t" \
        "s_waitcnt lgkmcnt(8)\n\t" \
        "v_mfma_f32_16x16x32_bf16 v[68:71], v[196:199], v[128:131], v[68:71]\n\t" \
        "v_mfma_f32_16x16x32_bf16 v[72:75], v[200:203], v[128:131], v[72:75]\n\t" \
        "v_mfma_f32_16x16x32_bf16 v[76:79], v[204:207], v[128:131], v[76:79]\n\t" \
        "s_waitcnt vmcnt(11)\n\t" \
        "s_barrier\n\t" \
        "ds_read_b64_tr_b16 v[220:221], v6 offset:49152\n\t" \
        "v_mfma_f32_16x16x32_bf16 v[32:35], v[208:211], v[132:135], v[32:35]\n\t" \
        "ds_read_b64_tr_b16 v[222:223], v6 offset:49920\n\t" \
        "v_mfma_f32_16x16x32_bf16 v[36:39], v[212:215], v[132:135], v[36:39]\n\t" \
        "ds_read_b64_tr_b16 v[224:225], v7 offset:49152\n\t" \
        "s_waitcnt lgkmcnt(6)\n\t" \
        "v_mfma_f32_16x16x32_bf16 v[40:43], v[216:219], v[132:135], v[40:43]\n\t" \
        "ds_read_b64_tr_b16 v[226:227], v7 offset:49920\n\t" \
        "v_mfma_f32_16x16x32_bf16 v[44:47], v[208:211], v[136:139], v[44:47]\n\t" \
        "ds_read_b64_tr_b16 v[228:229], v8 offset:49152\n\t" \
        "v_mfma_f32_16x16x32_bf16 v[48:51], v[212:215], v[136:139], v[48:51]\n\t" \
        "ds_read_b64_tr_b16 v[230:231], v8 offset:49920\n\t" \
        "v_mfma_f32_16x16x32_bf16 v[52:55], v[216:219], v[136:139], v[52:55]\n\t" \
        "ds_read_b128 v[128:131], v16 offset:31744\n\t" \
        "v_mfma_f32_16x16x32_bf16 v[56:59], v[208:211], v[140:143], v[56:59]\n\t" \
        "ds_read_b128 v[184:187], v184\n\t" \
        "v_mfma_f32_16x16x32_bf16 v[60:63], v[212:215], v[140:143], v[60:63]\n\t" \
        "ds_read_b128 v[132:135], v16 offset:8000\n\t" \
        "v_mfma_f32_16x16x32_bf16 v[64:67], v[216:219], v[140:143], v[64:67]\n\t" \
        "ds_read_b64_tr_b16 v[232:233], v6 offset:55296\n\t" \
        "v_mfma_f32_16x16x32_bf16 v[68:71], v[208:211], v[144:147], v[68:71]\n\t" \
        "ds_read_b64_tr_b16 v[234:235], v6 offset:56064\n\t" \
        "v_mfma_f32_16x16x32_bf16 v[72:75], v[212:215], v[144:147], v[72:75]\n\t" \
        "ds_read_b64_tr_b16 v[236:237], v7 offset:55296\n\t" \
        "v_mfma_f32_16x16x32_bf16 v[76:79], v[216:219], v[144:147], v[76:79]\n\t" \
        "ds_read_b64_tr_b16 v[238:239], v7 offset:56064\n\t" \
        "s_waitcnt lgkmcnt(10)\n\t" \
        "v_mfma_f32_16x16x32_bf16 v[32:35], v[220:223], v[116:119], v[32:35]\n\t" \
        "ds_read_b64_tr_b16 v[240:241], v8 offset:55296\n\t" \
        "s_waitcnt lgkmcnt(10)\n\t" \
        "v_mfma_f32_16x16x32_bf16 v[36:39], v[224:227], v[116:119], v[36:39]\n\t" \
        "ds_read_b64_tr_b16 v[242:243], v8 offset:56064\n\t" \
        "s_waitcnt lgkmcnt(9)\n\t" \
        "v_mfma_f32_16x16x32_bf16 v[40:43], v[228:231], v[116:119], v[40:43]\n\t" \
        "ds_read_b128 v[136:139], v16 offset:15936\n\t" \
        "s_waitcnt lgkmcnt(8)\n\t" \
        "v_or3_b32 v184, v184, v185, v186\n\t" \
        "v_mfma_f32_16x16x32_bf16 v[44:47], v[220:223], v[120:123], v[44:47]\n\t" \
        "ds_read_b128 v[140:143], v16 offset:23872\n\t" \
        "v_or_b32 v184, v184, v187\n\t" \
        "v_mfma_f32_16x16x32_bf16 v[48:51], v[224:227], v[120:123], v[48:51]\n\t" \
        "ds_read_b128 v[144:147], v16 offset:31808\n\t" \
        "v_readfirstlane_b32 s56, v184\n\t" \
        "v_mfma_f32_16x16x32_bf16 v[52:55], v[228:231], v[120:123], v[52:55]\n\t" \
        "ds_read_b128 v[116:119], v16 offset:8064\n\t" \
        "s_cmp_lg_u32 s56, 0\n\t" \
        "s_cbranch_scc1 .LR_abort%=\n\t" \
        "v_mfma_f32_16x16x32_bf16 v[56:59], v[220:223], v[124:127], v[56:59]\n\t" \
        "ds_read_b128 v[120:123], v16 offset:16000\n\t" \
        "v_mfma_f32_16x16x32_bf16 v[60:63], v[224:227], v[124:127], v[60:63]\n\t" \
        "v_mfma_f32_16x16x32_bf16 v[64:67], v[228:231], v[124:127], v[64:67]\n\t" \
        "ds_read_b128 v[124:127], v16 offset:23936\n\t" \
        "v_mfma_f32_16x16x32_bf16 v[68:71], v[220:223], v[128:131], v[68:71]\n\t" \
        "v_mfma_f32_16x16x32_bf16 v[72:75], v[224:227], v[128:131], v[72:75]\n\t" \
        "v_mfma_f32_16x16x32_bf16 v[76:79], v[228:231], v[128:131], v[76:79]\n\t" \
        "s_waitcnt vmcnt(8)\n\t" \
        "s_barrier\n\t" \
        "ds_read_b64_tr_b16 v[244:245], v20 offset:0\n\t" \
        "s_waitcnt lgkmcnt(7)\n\t" \
        "v_mfma_f32_16x16x32_bf16 v[32:35], v[232:235], v[132:135], v[32:35]\n\t" \
        "ds_read_b64_tr_b16 v[246:247], v20 offset:768\n\t" \
        "v_mfma_f32_16x16x32_bf16 v[36:39], v[236:239], v[132:135], v[36:39]\n\t" \
        "ds_read_b64_tr_b16 v[248:249], v21 offset:0\n\t" \
        "v_mfma_f32_16x16x32_bf16 v[40:43], v[240:243], v[132:135], v[40:43]\n\t" \
        "ds_read_b64_tr_b16 v[250:251], v21 offset:768\n\t" \
        "s_waitcnt lgkmcnt(7)\n\t" \
        "v_mfma_f32_16x16x32_bf16 v[44:47], v[232:235], v[136:139], v[44:47]\n\t" \
        "ds_read_b64_tr_b16 v[252:253], v22 offset:0\n\t" \
        "v_mfma_f32_16x16x32_bf16 v[48:51], v[236:239], v[136:139], v[48:51]\n\t" \
        "ds_read_b64_tr_b16 v[254:255], v22 offset:768\n\t" \
        "v_mfma_f32_16x16x32_bf16 v[52:55], v[240:243], v[136:139], v[52:55]\n\t" \
        "ds_read_b128 v[128:131], v16 offset:31872\n\t" \
        "v_mfma_f32_16x16x32_bf16 v[56:59], v[232:235], v[140:143], v[56:59]\n\t" \
        "ds_read_b128 v[132:135], v14 offset:31744\n\t" \
        "v_mfma_f32_16x16x32_bf16 v[60:63], v[236:239], v[140:143], v[60:63]\n\t" \
        "ds_read_b128 v[136:139], v14 offset:31808\n\t" \
        "v_mfma_f32_16x16x32_bf16 v[64:67], v[240:243], v[140:143], v[64:67]\n\t" \
        "ds_read_b128 v[140:143], v14 offset:31872\n\t" \
        "v_mfma_f32_16x16x32_bf16 v[68:71], v[232:235], v[144:147], v[68:71]\n\t" \
        "v_mfma_f32_16x16x32_bf16 v[72:75], v[236:239], v[144:147], v[72:75]\n\t" \
        "v_mfma_f32_16x16x32_bf16 v[76:79], v[240:243], v[144:147], v[76:79]\n\t" \
        "ds_read_b128 v[144:147], v15 offset:31744\n\t" \
        "s_waitcnt lgkmcnt(8)\n\t" \
        "v_mfma_f32_16x16x32_bf16 v[32:35], v[244:247], v[116:119], v[32:35]\n\t" \
        "s_waitcnt lgkmcnt(7)\n\t" \
        "v_mfma_f32_16x16x32_bf16 v[36:39], v[248:251], v[116:119], v[36:39]\n\t" \
        "s_waitcnt lgkmcnt(5)\n\t" \
        "v_mfma_f32_16x16x32_bf16 v[40:43], v[252:255], v[116:119], v[40:43]\n\t" \
        "ds_read_b128 v[116:119], v15 offset:31808\n\t" \
        "v_mfma_f32_16x16x32_bf16 v[44:47], v[244:247], v[120:123], v[44:47]\n\t" \
        "v_mfma_f32_16x16x32_bf16 v[48:51], v[248:251], v[120:123], v[48:51]\n\t" \
        "v_mfma_f32_16x16x32_bf16 v[52:55], v[252:255], v[120:123], v[52:55]\n\t" \
        "ds_read_b128 v[120:123], v15 offset:31872\n\t" \
        "v_mfma_f32_16x16x32_bf16 v[56:59], v[244:247], v[124:127], v[56:59]\n\t" \
        "v_mfma_f32_16x16x32_bf16 v[60:63], v[248:251], v[124:127], v[60:63]\n\t" \
        "v_mfma_f32_16x16x32_bf16 v[64:67], v[252:255], v[124:127], v[64:67]\n\t" \
        "ds_read_b128 v[124:127], v16 offset:31744\n\t" \
        "s_waitcnt lgkmcnt(4)\n\t" \
        "v_mfma_f32_16x16x32_bf16 v[68:71], v[244:247], v[128:131], v[68:71]\n\t" \
        "v_mfma_f32_16x16x32_bf16 v[72:75], v[248:251], v[128:131], v[72:75]\n\t" \
        "v_mfma_f32_16x16x32_bf16 v[76:79], v[252:255], v[128:131], v[76:79]\n\t" \
        "ds_read_b128 v[128:131], v16 offset:31808\n\t" \
        "v_mfma_f32_16x16x32_bf16 v[80:83], a[0:3], v[132:135], 0\n\t" \
        "v_mfma_f32_16x16x32_bf16 v[84:87], a[4:7], v[132:135], 0\n\t" \
        "v_mfma_f32_16x16x32_bf16 v[88:91], a[8:11], v[132:135], 0\n\t" \
        "v_cvt_pk_bf16_f32 v40, v40, v41\n\t" \
        "v_cvt_pk_bf16_f32 v41, v42, v43\n\t" \
        "v_mfma_f32_16x16x32_bf16 v[80:83], a[12:15], v[136:139], v[80:83]\n\t" \
        "ds_write_b64 v11, v[40:41] offset:0\n\t" \
        "v_cvt_pk_bf16_f32 v52, v52, v53\n\t" \
        "v_mfma_f32_16x16x32_bf16 v[84:87], a[16:19], v[136:139], v[84:87]\n\t" \
        "v_cvt_pk_bf16_f32 v53, v54, v55\n\t" \
        "v_cvt_pk_bf16_f32 v64, v64, v65\n\t" \
        "v_mfma_f32_16x16x32_bf16 v[88:91], a[20:23], v[136:139], v[88:91]\n\t" \
        "ds_write_b64 v11, v[52:53] offset:2048\n\t" \
        "v_cvt_pk_bf16_f32 v65, v66, v67\n\t" \
        "v_mfma_f32_16x16x32_bf16 v[80:83], a[24:27], v[140:143], v[80:83]\n\t" \
        "ds_write_b64 v11, v[64:65] offset:4096\n\t" \
        "v_cvt_pk_bf16_f32 v76, v76, v77\n\t" \
        "v_mfma_f32_16x16x32_bf16 v[84:87], a[28:31], v[140:143], v[84:87]\n\t" \
        "ds_read_b128 v[132:135], v16 offset:31872\n\t" \
        "v_cvt_pk_bf16_f32 v77, v78, v79\n\t" \
        "v_mfma_f32_16x16x32_bf16 v[88:91], a[32:35], v[140:143], v[88:91]\n\t" \
        "ds_write_b64 v11, v[76:77] offset:6144\n\t" \
        "v_cvt_pk_bf16_f32 v32, v32, v33\n\t" \
        "s_waitcnt lgkmcnt(6)\n\t" \
        "v_mfma_f32_16x16x32_bf16 v[80:83], a[36:39], v[144:147], v[80:83]\n\t" \
        "v_cvt_pk_bf16_f32 v33, v34, v35\n\t" \
        "v_cvt_pk_bf16_f32 v34, v36, v37\n\t" \
        "v_mfma_f32_16x16x32_bf16 v[84:87], a[40:43], v[144:147], v[84:87]\n\t" \
        "v_cvt_pk_bf16_f32 v35, v38, v39\n\t" \
        "v_permlane16_swap_b32 v32, v34\n\t" \
        "v_mfma_f32_16x16x32_bf16 v[88:91], a[44:47], v[144:147], v[88:91]\n\t" \
        "v_permlane16_swap_b32 v33, v35\n\t" \
        "v_mfma_f32_16x16x32_bf16 v[80:83], a[48:51], v[116:119], v[80:83]\n\t" \
        "v_mfma_f32_16x16x32_bf16 v[84:87], a[52:55], v[116:119], v[84:87]\n\t" \
        "v_mfma_f32_16x16x32_bf16 v[88:91], a[56:59], v[116:119], v[88:91]\n\t" \
        "v_mfma_f32_16x16x32_bf16 v[80:83], a[60:63], v[120:123], v[80:83]\n\t" \
        "v_mfma_f32_16x16x32_bf16 v[84:87], a[64:67], v[120:123], v[84:87]\n\t" \
        "v_mfma_f32_16x16x32_bf16 v[88:91], a[68:71], v[120:123], v[88:91]\n\t" \
        "v_mfma_f32_16x16x32_bf16 v[80:83], a[72:75], v[124:127], v[80:83]\n\t" \
        "v_mfma_f32_16x16x32_bf16 v[84:87], a[76:79], v[124:127], v[84:87]\n\t" \
        "s_waitcnt vmcnt(0) lgkmcnt(0)\n\t" \
        "s_barrier\n\t" \
        "ds_read_b128 v[136:139], v14 offset:35712\n\t" \
        "v_mfma_f32_16x16x32_bf16 v[88:91], a[80:83], v[124:127], v[88:91]\n\t" \
        "ds_read_b128 v[140:143], v14 offset:35776\n\t" \
        "v_mfma_f32_16x16x32_bf16 v[80:83], a[84:87], v[128:131], v[80:83]\n\t" \
        "ds_read_b128 v[144:147], v14 offset:35840\n\t" \
        "v_mfma_f32_16x16x32_bf16 v[84:87], a[88:91], v[128:131], v[84:87]\n\t" \
        "ds_read_b128 v[116:119], v15 offset:35712\n\t" \
        "v_mfma_f32_16x16x32_bf16 v[88:91], a[92:95], v[128:131], v[88:91]\n\t" \
        "ds_read_b128 v[120:123], v15 offset:35776\n\t" \
        "v_mfma_f32_16x16x32_bf16 v[80:83], a[96:99], v[132:135], v[80:83]\n\t" \
        "s_mov_b32 s56, s64\n\t" \
        "buffer_store_dwordx4 v[32:35], v9, s[28:31], s56 offen sc1\n\t" \
        "v_mfma_f32_16x16x32_bf16 v[84:87], a[100:103], v[132:135], v[84:87]\n\t" \
        "ds_read_b64 v[42:43], v12 offset:0\n\t" \
        "v_mfma_f32_16x16x32_bf16 v[88:91], a[104:107], v[132:135], v[88:91]\n\t" \
        "ds_read_b128 v[124:127], v15 offset:35840\n\t" \
        "s_waitcnt lgkmcnt(6)\n\t" \
        "v_mfma_f32_16x16x32_bf16 v[80:83], a[108:111], v[136:139], v[80:83]\n\t" \
        "ds_read_b128 v[128:131], v16 offset:35712\n\t" \
        "v_mfma_f32_16x16x32_bf16 v[84:87], a[112:115], v[136:139], v[84:87]\n\t" \
        "ds_read_b128 v[132:135], v16 offset:35776\n\t" \
        "v_mfma_f32_16x16x32_bf16 v[88:91], a[116:119], v[136:139], v[88:91]\n\t" \
        "ds_read_b128 v[136:139], v16 offset:35840\n\t" \
        "s_waitcnt lgkmcnt(8)\n\t" \
        "v_mfma_f32_16x16x32_bf16 v[80:83], a[120:123], v[140:143], v[80:83]\n\t" \
        "v_mfma_f32_16x16x32_bf16 v[84:87], a[124:127], v[140:143], v[84:87]\n\t" \
        "v_mfma_f32_16x16x32_bf16 v[88:91], a[128:131], v[140:143], v[88:91]\n\t" \
        "ds_read_b128 v[140:143], v14 offset:39680\n\t" \
        "v_cvt_pk_bf16_f32 v44, v44, v45\n\t" \
        "s_waitcnt lgkmcnt(7)\n\t" \
        "v_mfma_f32_16x16x32_bf16 v[80:83], a[132:135], v[144:147], v[80:83]\n\t" \
        "s_waitcnt lgkmcnt(5)\n\t" \
        "v_permlane16_swap_b32 v40, v42\n\t" \
        "v_permlane16_swap_b32 v41, v43\n\t" \
        "v_mfma_f32_16x16x32_bf16 v[84:87], a[136:139], v[144:147], v[84:87]\n\t" \
        "v_cvt_pk_bf16_f32 v45, v46, v47\n\t" \
        "v_cvt_pk_bf16_f32 v46, v48, v49\n\t" \
        "v_mfma_f32_16x16x32_bf16 v[88:91], a[140:143], v[144:147], v[88:91]\n\t" \
        "s_mov_b32 s56, s64\n\t" \
        "buffer_store_dwordx4 v[40:43], v10, s[28:31], s56 offen sc1\n\t" \
        "v_mfma_f32_16x16x32_bf16 v[80:83], a[144:147], v[116:119], v[80:83]\n\t" \
        "ds_read_b128 v[144:147], v14 offset:39744\n\t" \
        "v_cvt_pk_bf16_f32 v47, v50, v51\n\t" \
        "v_mfma_f32_16x16x32_bf16 v[84:87], a[148:151], v[116:119], v[84:87]\n\t" \
        "v_permlane16_swap_b32 v44, v46\n\t" \
        "v_permlane16_swap_b32 v45, v47\n\t" \
        "v_mfma_f32_16x16x32_bf16 v[88:91], a[152:155], v[116:119], v[88:91]\n\t" \
        "ds_read_b128 v[116:119], v14 offset:39808\n\t" \
        "v_mfma_f32_16x16x32_bf16 v[80:83], a[156:159], v[120:123], v[80:83]\n\t" \
        "ds_read_b64 v[54:55], v12 offset:2048\n\t" \
        "v_mfma_f32_16x16x32_bf16 v[84:87], a[160:163], v[120:123], v[84:87]\n\t" \
        "v_mfma_f32_16x16x32_bf16 v[88:91], a[164:167], v[120:123], v[88:91]\n\t" \
        "ds_read_b128 v[120:123], v15 offset:39680\n\t" \
        "s_waitcnt lgkmcnt(5)\n\t" \
        "v_mfma_f32_16x16x32_bf16 v[80:83], a[168:171], v[124:127], v[80:83]\n\t" \
        "v_mfma_f32_16x16x32_bf16 v[84:87], a[172:175], v[124:127], v[84:87]\n\t" \
        "v_mfma_f32_16x16x32_bf16 v[88:91], a[176:179], v[124:127], v[88:91]\n\t" \
        "ds_read_b128 v[124:127], v15 offset:39744\n\t" \
        "v_mfma_f32_16x16x32_bf16 v[80:83], a[180:183], v[128:131], v[80:83]\n\t" \
        "v_mfma_f32_16x16x32_bf16 v[84:87], a[184:187], v[128:131], v[84:87]\n\t" \
        "v_mfma_f32_16x16x32_bf16 v[88:91], a[188:191], v[128:131], v[88:91]\n\t" \
        "ds_read_b128 v[128:131], v15 offset:39808\n\t" \
        "s_waitcnt lgkmcnt(3)\n\t" \
        "v_permlane16_swap_b32 v52, v54\n\t" \
        "v_mfma_f32_16x16x32_bf16 v[80:83], a[192:195], v[132:135], v[80:83]\n\t" \
        "v_permlane16_swap_b32 v53, v55\n\t" \
        "v_mfma_f32_16x16x32_bf16 v[84:87], a[196:199], v[132:135], v[84:87]\n\t" \
        "v_mfma_f32_16x16x32_bf16 v[88:91], a[200:203], v[132:135], v[88:91]\n\t" \
        "ds_read_b128 v[132:135], v16 offset:39680\n\t" \
        "v_mfma_f32_16x16x32_bf16 v[80:83], a[204:207], v[136:139], v[80:83]\n\t" \
        "v_mfma_f32_16x16x32_bf16 v[84:87], a[208:211], v[136:139], v[84:87]\n\t" \
        "v_mfma_f32_16x16x32_bf16 v[88:91], a[212:215], v[136:139], v[88:91]\n\t" \
        "ds_read_b128 v[136:139], v16 offset:39744\n\t" \
        "v_mfma_f32_16x16x32_bf16 v[80:83], a[216:219], v[140:143], v[80:83]\n\t" \
        "s_add_u32 s56, s64, 0x15000\n\t" \
        "buffer_store_dwordx4 v[44:47], v9, s[28:31], s56 offen sc1\n\t" \
        "v_mfma_f32_16x16x32_bf16 v[84:87], a[220:223], v[140:143], v[84:87]\n\t" \
        "v_mfma_f32_16x16x32_bf16 v[88:91], a[224:227], v[140:143], v[88:91]\n\t" \
        "ds_read_b128 v[140:143], v16 offset:39808\n\t" \
        "v_mfma_f32_16x16x32_bf16 v[80:83], a[228:231], v[144:147], v[80:83]\n\t" \
        "v_mfma_f32_16x16x32_bf16 v[84:87], a[232:235], v[144:147], v[84:87]\n\t" \
        "v_mfma_f32_16x16x32_bf16 v[88:91], a[236:239], v[144:147], v[88:91]\n\t" \
        "ds_read_b128 v[144:147], v14 offset:39680\n\t" \
        "v_mfma_f32_16x16x32_bf16 v[80:83], a[240:243], v[116:119], v[80:83]\n\t" \
        "v_mfma_f32_16x16x32_bf16 v[84:87], a[244:247], v[116:119], v[84:87]\n\t" \
        "s_add_u32 s56, s64, 0x15000\n\t" \
        "buffer_store_dwordx4 v[52:55], v10, s[28:31], s56 offen sc1\n\t" \
        "v_mfma_f32_16x16x32_bf16 v[88:91], a[248:251], v[116:119], v[88:91]\n\t" \
        "ds_read_b128 v[116:119], v14 offset:39744\n\t" \
        "v_cvt_pk_bf16_f32 v56, v56, v57\n\t" \
        "s_waitcnt lgkmcnt(4)\n\t" \
        "v_mfma_f32_16x16x32_bf16 v[80:83], a[252:255], v[120:123], v[80:83]\n\t" \
        "v_cvt_pk_bf16_f32 v57, v58, v59\n\t" \
        "v_cvt_pk_bf16_f32 v58, v60, v61\n\t" \
        "v_mfma_f32_16x16x32_bf16 v[84:87], v[188:191], v[120:123], v[84:87]\n\t" \
        "v_cvt_pk_bf16_f32 v59, v62, v63\n\t" \
        "v_permlane16_swap_b32 v56, v58\n\t" \
        "v_mfma_f32_16x16x32_bf16 v[88:91], v[192:195], v[120:123], v[88:91]\n\t" \
        "ds_read_b128 v[120:123], v14 offset:39808\n\t" \
        "v_permlane16_swap_b32 v57, v59\n\t" \
        "v_mfma_f32_16x16x32_bf16 v[80:83], v[196:199], v[124:127], v[80:83]\n\t" \
        "ds_read_b64 v[66:67], v12 offset:4096\n\t" \
        "v_mfma_f32_16x16x32_bf16 v[84:87], v[200:203], v[124:127], v[84:87]\n\t" \
        "v_mfma_f32_16x16x32_bf16 v[88:91], v[204:207], v[124:127], v[88:91]\n\t" \
        "ds_read_b128 v[124:127], v15 offset:39680\n\t" \
        "v_mfma_f32_16x16x32_bf16 v[80:83], v[208:211], v[128:131], v[80:83]\n\t" \
        "v_mfma_f32_16x16x32_bf16 v[84:87], v[212:215], v[128:131], v[84:87]\n\t" \
        "v_mfma_f32_16x16x32_bf16 v[88:91], v[216:219], v[128:131], v[88:91]\n\t" \
        "ds_read_b128 v[128:131], v15 offset:39744\n\t" \
        "v_mfma_f32_16x16x32_bf16 v[80:83], v[220:223], v[132:135], v[80:83]\n\t" \
        "v_mfma_f32_16x16x32_bf16 v[84:87], v[224:227], v[132:135], v[84:87]\n\t" \
        "v_mfma_f32_16x16x32_bf16 v[88:91], v[228:231], v[132:135], v[88:91]\n\t" \
        "ds_read_b128 v[132:135], v15 offset:39808\n\t" \
        "s_waitcnt lgkmcnt(3)\n\t" \
        "v_permlane16_swap_b32 v64, v66\n\t" \
        "v_mfma_f32_16x16x32_bf16 v[80:83], v[232:235], v[136:139], v[80:83]\n\t" \
        "v_permlane16_swap_b32 v65, v67\n\t" \
        "v_mfma_f32_16x16x32_bf16 v[84:87], v[236:239], v[136:139], v[84:87]\n\t" \
        "v_mfma_f32_16x16x32_bf16 v[88:91], v[240:243], v[136:139], v[88:91]\n\t" \
        "ds_read_b128 v[136:139], v16 offset:39680\n\t" \
        "v_mfma_f32_16x16x32_bf16 v[80:83], v[244:247], v[140:143], v[80:83]\n\t" \
        "v_mfma_f32_16x16x32_bf16 v[84:87], v[248:251], v[140:143], v[84:87]\n\t" \
        "v_mfma_f32_16x16x32_bf16 v[88:91], v[252:255], v[140:143], v[88:91]\n\t" \
        "ds_read_b128 v[140:143], v16 offset:39744\n\t" \
        "v_mfma_f32_16x16x32_bf16 v[92:95], a[0:3], v[144:147], 0\n\t" \
        "v_mfma_f32_16x16x32_bf16 v[96:99], a[4:7], v[144:147], 0\n\t" \
        "v_mfma_f32_16x16x32_bf16 v[100:103], a[8:11], v[144:147], 0\n\t" \
        "s_nop 5\n\t" \
        "v_cvt_pk_bf16_f32 v88, v88, v89\n\t" \
        "v_cvt_pk_bf16_f32 v89, v90, v91\n\t" \
        "v_mfma_f32_16x16x32_bf16 v[92:95], a[12:15], v[116:119], v[92:95]\n\t" \
        "s_add_u32 s56, s64, 0x2a000\n\t" \
        "buffer_store_dwordx4 v[56:59], v9, s[28:31], s56 offen sc1\n\t" \
        "v_mfma_f32_16x16x32_bf16 v[96:99], a[16:19], v[116:119], v[96:99]\n\t" \
        "ds_write_b64 v11, v[88:89] offset:8192\n\t" \
        "v_cvt_pk_bf16_f32 v80, v80, v81\n\t" \
        "v_mfma_f32_16x16x32_bf16 v[100:103], a[20:23], v[116:119], v[100:103]\n\t" \
        "ds_read_b128 v[144:147], v16 offset:39808\n\t" \
        "v_cvt_pk_bf16_f32 v81, v82, v83\n\t" \
        "v_mfma_f32_16x16x32_bf16 v[92:95], a[24:27], v[120:123], v[92:95]\n\t" \
        "ds_read_b128 v[116:119], v14 offset:43648\n\t" \
        "v_cvt_pk_bf16_f32 v82, v84, v85\n\t" \
        "v_mfma_f32_16x16x32_bf16 v[96:99], a[28:31], v[120:123], v[96:99]\n\t" \
        "v_cvt_pk_bf16_f32 v83, v86, v87\n\t" \
        "v_permlane16_swap_b32 v80, v82\n\t" \
        "v_mfma_f32_16x16x32_bf16 v[100:103], a[32:35], v[120:123], v[100:103]\n\t" \
        "ds_read_b128 v[120:123], v14 offset:43712\n\t" \
        "v_permlane16_swap_b32 v81, v83\n\t" \
        "s_waitcnt lgkmcnt(5)\n\t" \
        "v_mfma_f32_16x16x32_bf16 v[92:95], a[36:39], v[124:127], v[92:95]\n\t" \
        "v_mfma_f32_16x16x32_bf16 v[96:99], a[40:43], v[124:127], v[96:99]\n\t" \
        "s_add_u32 s56, s64, 0x2a000\n\t" \
        "buffer_store_dwordx4 v[64:67], v10, s[28:31], s56 offen sc1\n\t" \
        "v_mfma_f32_16x16x32_bf16 v[100:103], a[44:47], v[124:127], v[100:103]\n\t" \
        "ds_read_b128 v[124:127], v14 offset:43776\n\t" \
        "v_cvt_pk_bf16_f32 v68, v68, v69\n\t" \
        "v_mfma_f32_16x16x32_bf16 v[92:95], a[48:51], v[128:131], v[92:95]\n\t" \
        "v_cvt_pk_bf16_f32 v69, v70, v71\n\t" \
        "v_cvt_pk_bf16_f32 v70, v72, v73\n\t" \
        "v_mfma_f32_16x16x32_bf16 v[96:99], a[52:55], v[128:131], v[96:99]\n\t" \
        "s_waitcnt lgkmcnt(4)\n\t" \
        "s_barrier\n\t" \
        "ds_read_b64 v[90:91], v12 offset:8192\n\t" \
        "v_cvt_pk_bf16_f32 v71, v74, v75\n\t" \
        "v_mfma_f32_16x16x32_bf16 v[100:103], a[56:59], v[128:131], v[100:103]\n\t" \
        "ds_read_b128 v[128:131], v15 offset:43648\n\t" \
        "v_permlane16_swap_b32 v68, v70\n\t" \
        "v_mfma_f32_16x16x32_bf16 v[92:95], a[60:63], v[132:135], v[92:95]\n\t" \
        "ds_read_b64 v[78:79], v12 offset:6144\n\t" \
        "v_permlane16_swap_b32 v69, v71\n\t" \
        "v_mfma_f32_16x16x32_bf16 v[96:99], a[64:67], v[132:135], v[96:99]\n\t" \
        "v_mfma_f32_16x16x32_bf16 v[100:103], a[68:71], v[132:135], v[100:103]\n\t" \
        "ds_read_b128 v[132:135], v15 offset:43712\n\t" \
        "v_mfma_f32_16x16x32_bf16 v[92:95], a[72:75], v[136:139], v[92:95]\n\t" \
        "v_mfma_f32_16x16x32_bf16 v[96:99], a[76:79], v[136:139], v[96:99]\n\t" \
        "v_mfma_f32_16x16x32_bf16 v[100:103], a[80:83], v[136:139], v[100:103]\n\t" \
        "ds_read_b128 v[136:139], v15 offset:43776\n\t" \
        "v_mfma_f32_16x16x32_bf16 v[92:95], a[84:87], v[140:143], v[92:95]\n\t" \
        "s_waitcnt lgkmcnt(4)\n\t" \
        "v_permlane16_swap_b32 v88, v90\n\t" \
        "v_permlane16_swap_b32 v89, v91\n\t" \
        "v_mfma_f32_16x16x32_bf16 v[96:99], a[88:91], v[140:143], v[96:99]\n\t" \
        "v_mfma_f32_16x16x32_bf16 v[100:103], a[92:95], v[140:143], v[100:103]\n\t" \
        "ds_read_b128 v[140:143], v16 offset:43648\n\t" \
        "s_waitcnt lgkmcnt(3)\n\t" \
        "v_permlane16_swap_b32 v76, v78\n\t" \
        "v_mfma_f32_16x16x32_bf16 v[92:95], a[96:99], v[144:147], v[92:95]\n\t" \
        "s_add_u32 s56, s64, 0x54000\n\t" \
        "buffer_store_dwordx4 v[80:83], v9, s[28:31], s56 offen sc1\n\t" \
        "v_mfma_f32_16x16x32_bf16 v[96:99], a[100:103], v[144:147], v[96:99]\n\t" \
        "v_permlane16_swap_b32 v77, v79\n\t" \
        "v_mfma_f32_16x16x32_bf16 v[100:103], a[104:107], v[144:147], v[100:103]\n\t" \
        "ds_read_b128 v[144:147], v16 offset:43712\n\t" \
        "v_mfma_f32_16x16x32_bf16 v[92:95], a[108:111], v[116:119], v[92:95]\n\t" \
        "v_mfma_f32_16x16x32_bf16 v[96:99], a[112:115], v[116:119], v[96:99]\n\t" \
        "v_mfma_f32_16x16x32_bf16 v[100:103], a[116:119], v[116:119], v[100:103]\n\t" \
        "ds_read_b128 v[116:119], v16 offset:43776\n\t" \
        "v_mfma_f32_16x16x32_bf16 v[92:95], a[120:123], v[120:123], v[92:95]\n\t" \
        "v_mfma_f32_16x16x32_bf16 v[96:99], a[124:127], v[120:123], v[96:99]\n\t" \
        "s_add_u32 s56, s64, 0x54000\n\t" \
        "buffer_store_dwordx4 v[88:91], v10, s[28:31], s56 offen sc1\n\t" \
        "v_mfma_f32_16x16x32_bf16 v[100:103], a[128:131], v[120:123], v[100:103]\n\t" \
        "ds_read_b128 v[120:123], v14 offset:47616\n\t" \
        "v_mfma_f32_16x16x32_bf16 v[92:95], a[132:135], v[124:127], v[92:95]\n\t" \
        "s_add_u32 s56, s64, 0x3f000\n\t" \
        "buffer_store_dwordx4 v[68:71], v9, s[28:31], s56 offen sc1\n\t" \
        "v_mfma_f32_16x16x32_bf16 v[96:99], a[136:139], v[124:127], v[96:99]\n\t" \
        "v_mfma_f32_16x16x32_bf16 v[100:103], a[140:143], v[124:127], v[100:103]\n\t" \
        "ds_read_b128 v[124:127], v14 offset:47680\n\t" \
        "v_mfma_f32_16x16x32_bf16 v[92:95], a[144:147], v[128:131], v[92:95]\n\t" \
        "v_mfma_f32_16x16x32_bf16 v[96:99], a[148:151], v[128:131], v[96:99]\n\t" \
        "v_mfma_f32_16x16x32_bf16 v[100:103], a[152:155], v[128:131], v[100:103]\n\t" \
        "ds_read_b128 v[128:131], v14 offset:47744\n\t" \
        "s_waitcnt lgkmcnt(4)\n\t" \
        "v_mfma_f32_16x16x32_bf16 v[92:95], a[156:159], v[132:135], v[92:95]\n\t" \
        "v_mfma_f32_16x16x32_bf16 v[96:99], a[160:163], v[132:135], v[96:99]\n\t" \
        "s_add_u32 s56, s64, 0x3f000\n\t" \
        "buffer_store_dwordx4 v[76:79], v10, s[28:31], s56 offen sc1\n\t" \
        "v_mfma_f32_16x16x32_bf16 v[100:103], a[164:167], v[132:135], v[100:103]\n\t" \
        "ds_read_b128 v[132:135], v15 offset:47616\n\t" \
        "v_mfma_f32_16x16x32_bf16 v[92:95], a[168:171], v[136:139], v[92:95]\n\t" \
        "v_mfma_f32_16x16x32_bf16 v[96:99], a[172:175], v[136:139], v[96:99]\n\t" \
        "v_mfma_f32_16x16x32_bf16 v[100:103], a[176:179], v[136:139], v[100:103]\n\t" \
        "ds_read_b128 v[136:139], v15 offset:47680\n\t" \
        "v_mfma_f32_16x16x32_bf16 v[92:95], a[180:183], v[140:143], v[92:95]\n\t" \
        "v_mfma_f32_16x16x32_bf16 v[96:99], a[184:187], v[140:143], v[96:99]\n\t" \
        "v_mfma_f32_16x16x32_bf16 v[100:103], a[188:191], v[140:143], v[100:103]\n\t" \
        "ds_read_b128 v[140:143], v15 offset:47744\n\t" \
        "v_mfma_f32_16x16x32_bf16 v[92:95], a[192:195], v[144:147], v[92:95]\n\t" \
        "v_mfma_f32_16x16x32_bf16 v[96:99], a[196:199], v[144:147], v[96:99]\n\t" \
        "v_mfma_f32_16x16x32_bf16 v[100:103], a[200:203], v[144:147], v[100:103]\n\t" \
        "ds_read_b128 v[144:147], v16 offset:47616\n\t" \
        "s_waitcnt lgkmcnt(4)\n\t" \
        "v_mfma_f32_16x16x32_bf16 v[92:95], a[204:207], v[116:119], v[92:95]\n\t" \
        "v_mfma_f32_16x16x32_bf16 v[96:99], a[208:211], v[116:119], v[96:99]\n\t" \
        "v_mfma_f32_16x16x32_bf16 v[100:103], a[212:215], v[116:119], v[100:103]\n\t" \
        "ds_read_b128 v[116:119], v16 offset:47680\n\t" \
        "v_mfma_f32_16x16x32_bf16 v[92:95], a[216:219], v[120:123], v[92:95]\n\t" \
        "v_mfma_f32_16x16x32_bf16 v[96:99], a[220:223], v[120:123], v[96:99]\n\t" \
        "v_mfma_f32_16x16x32_bf16 v[100:103], a[224:227], v[120:123], v[100:103]\n\t" \
        "ds_read_b128 v[120:123], v16 offset:47744\n\t" \
        "v_mfma_f32_16x16x32_bf16 v[92:95], a[228:231], v[124:127], v[92:95]\n\t" \
        "v_mfma_f32_16x16x32_bf16 v[96:99], a[232:235], v[124:127], v[96:99]\n\t" \
        "v_mfma_f32_16x16x32_bf16 v[100:103], a[236:239], v[124:127], v[100:103]\n\t" \
        "ds_read_b128 v[124:127], v14 offset:47616\n\t" \
        "v_mfma_f32_16x16x32_bf16 v[92:95], a[240:243], v[128:131], v[92:95]\n\t" \
        "v_mfma_f32_16x16x32_bf16 v[96:99], a[244:247], v[128:131], v[96:99]\n\t" \
        "v_mfma_f32_16x16x32_bf16 v[100:103], a[248:251], v[128:131], v[100:103]\n\t" \
        "ds_read_b128 v[128:131], v14 offset:47680\n\t" \
        "s_waitcnt lgkmcnt(4)\n\t" \
        "v_mfma_f32_16x16x32_bf16 v[92:95], a[252:255], v[132:135], v[92:95]\n\t" \
        "v_mfma_f32_16x16x32_bf16 v[96:99], v[188:191], v[132:135], v[96:99]\n\t" \
        "v_mfma_f32_16x16x32_bf16 v[100:103], v[192:195], v[132:135], v[100:103]\n\t" \
        "ds_read_b128 v[132:135], v14 offset:47744\n\t" \
        "v_mfma_f32_16x16x32_bf16 v[92:95], v[196:199], v[136:139], v[92:95]\n\t" \
        "v_mfma_f32_16x16x32_bf16 v[96:99], v[200:203], v[136:139], v[96:99]\n\t" \
        "v_mfma_f32_16x16x32_bf16 v[100:103], v[204:207], v[136:139], v[100:103]\n\t" \
        "ds_read_b128 v[136:139], v15 offset:47616\n\t" \
        "v_mfma_f32_16x16x32_bf16 v[92:95], v[208:211], v[140:143], v[92:95]\n\t" \
        "v_mfma_f32_16x16x32_bf16 v[96:99], v[212:215], v[140:143], v[96:99]\n\t" \
        "v_mfma_f32_16x16x32_bf16 v[100:103], v[216:219], v[140:143], v[100:103]\n\t" \
        "ds_read_b128 v[140:143], v15 offset:47680\n\t" \
        "v_mfma_f32_16x16x32_bf16 v[92:95], v[220:223], v[144:147], v[92:95]\n\t" \
        "v_mfma_f32_16x16x32_bf16 v[96:99], v[224:227], v[144:147], v[96:99]\n\t" \
        "v_mfma_f32_16x16x32_bf16 v[100:103], v[228:231], v[144:147], v[100:103]\n\t" \
        "ds_read_b128 v[144:147], v15 offset:47744\n\t" \
        "s_waitcnt lgkmcnt(4)\n\t" \
        "v_mfma_f32_16x16x32_bf16 v[92:95], v[232:235], v[116:119], v[92:95]\n\t" \
        "v_mfma_f32_16x16x32_bf16 v[96:99], v[236:239], v[116:119], v[96:99]\n\t" \
        "v_mfma_f32_16x16x32_bf16 v[100:103], v[240:243], v[116:119], v[100:103]\n\t" \
        "ds_read_b128 v[116:119], v16 offset:47616\n\t" \
        "v_mfma_f32_16x16x32_bf16 v[92:95], v[244:247], v[120:123], v[92:95]\n\t" \
        "v_mfma_f32_16x16x32_bf16 v[96:99], v[248:251], v[120:123], v[96:99]\n\t" \
        "v_mfma_f32_16x16x32_bf16 v[100:103], v[252:255], v[120:123], v[100:103]\n\t" \
        "s_bitcmp0_b32 s34, 4\n\t" \
        "s_cbranch_scc1 .LR14%=\n\t" \
        "ds_read_b128 v[120:123], v16 offset:47680\n\t" \
        "v_mfma_f32_16x16x32_bf16 v[104:107], a[0:3], v[124:127], 0\n\t" \
        "v_mfma_f32_16x16x32_bf16 v[108:111], a[4:7], v[124:127], 0\n\t" \
        "v_mfma_f32_16x16x32_bf16 v[112:115], a[8:11], v[124:127], 0\n\t" \
        "s_nop 3\n\t" \
        "v_cvt_pk_bf16_f32 v100, v100, v101\n\t" \
        "v_cvt_pk_bf16_f32 v101, v102, v103\n\t" \
        "v_mfma_f32_16x16x32_bf16 v[104:107], a[12:15], v[128:131], v[104:107]\n\t" \
        "ds_write_b64 v11, v[100:101] offset:10240\n\t" \
        "v_cvt_pk_bf16_f32 v92, v92, v93\n\t" \
        "v_mfma_f32_16x16x32_bf16 v[108:111], a[16:19], v[128:131], v[108:111]\n\t" \
        "ds_read_b128 v[124:127], v16 offset:47744\n\t" \
        "v_cvt_pk_bf16_f32 v93, v94, v95\n\t" \
        "v_mfma_f32_16x16x32_bf16 v[112:115], a[20:23], v[128:131], v[112:115]\n\t" \
        "ds_read_b128 v[128:131], v14 offset:51584\n\t" \
        "v_cvt_pk_bf16_f32 v94, v96, v97\n\t" \
        "s_waitcnt lgkmcnt(5)\n\t" \
        "v_mfma_f32_16x16x32_bf16 v[104:107], a[24:27], v[132:135], v[104:107]\n\t" \
        "v_cvt_pk_bf16_f32 v95, v98, v99\n\t" \
        "v_permlane16_swap_b32 v92, v94\n\t" \
        "v_mfma_f32_16x16x32_bf16 v[108:111], a[28:31], v[132:135], v[108:111]\n\t" \
        "v_permlane16_swap_b32 v93, v95\n\t" \
        "v_mfma_f32_16x16x32_bf16 v[112:115], a[32:35], v[132:135], v[112:115]\n\t" \
        "ds_read_b128 v[132:135], v14 offset:51648\n\t" \
        "v_mfma_f32_16x16x32_bf16 v[104:107], a[36:39], v[136:139], v[104:107]\n\t" \
        "v_mfma_f32_16x16x32_bf16 v[108:111], a[40:43], v[136:139], v[108:111]\n\t" \
        "v_mfma_f32_16x16x32_bf16 v[112:115], a[44:47], v[136:139], v[112:115]\n\t" \
        "ds_read_b128 v[136:139], v14 offset:51712\n\t" \
        "v_mfma_f32_16x16x32_bf16 v[104:107], a[48:51], v[140:143], v[104:107]\n\t" \
        "v_mfma_f32_16x16x32_bf16 v[108:111], a[52:55], v[140:143], v[108:111]\n\t" \
        "s_waitcnt lgkmcnt(4)\n\t" \
        "s_barrier\n\t" \
        "ds_read_b64 v[102:103], v12 offset:10240\n\t" \
        "v_mfma_f32_16x16x32_bf16 v[112:115], a[56:59], v[140:143], v[112:115]\n\t" \
        "ds_read_b128 v[140:143], v15 offset:51584\n\t" \
        "v_mfma_f32_16x16x32_bf16 v[104:107], a[60:63], v[144:147], v[104:107]\n\t" \
        "v_mfma_f32_16x16x32_bf16 v[108:111], a[64:67], v[144:147], v[108:111]\n\t" \
        "v_mfma_f32_16x16x32_bf16 v[112:115], a[68:71], v[144:147], v[112:115]\n\t" \
        "ds_read_b128 v[144:147], v15 offset:51648\n\t" \
        "v_mfma_f32_16x16x32_bf16 v[104:107], a[72:75], v[116:119], v[104:107]\n\t" \
        "v_mfma_f32_16x16x32_bf16 v[108:111], a[76:79], v[116:119], v[108:111]\n\t" \
        "v_mfma_f32_16x16x32_bf16 v[112:115], a[80:83], v[116:119], v[112:115]\n\t" \
        "ds_read_b128 v[116:119], v15 offset:51712\n\t" \
        "v_mfma_f32_16x16x32_bf16 v[104:107], a[84:87], v[120:123], v[104:107]\n\t" \
        "s_waitcnt lgkmcnt(3)\n\t" \
        "v_permlane16_swap_b32 v100, v102\n\t" \
        "v_permlane16_swap_b32 v101, v103\n\t" \
        "v_mfma_f32_16x16x32_bf16 v[108:111], a[88:91], v[120:123], v[108:111]\n\t" \
        "v_mfma_f32_16x16x32_bf16 v[112:115], a[92:95], v[120:123], v[112:115]\n\t" \
        "ds_read_b128 v[120:123], v16 offset:51584\n\t" \
        "v_mfma_f32_16x16x32_bf16 v[104:107], a[96:99], v[124:127], v[104:107]\n\t" \
        "s_add_u32 s56, s64, 0x69000\n\t" \
        "buffer_store_dwordx4 v[92:95], v9, s[28:31], s56 offen sc1\n\t" \
        "v_mfma_f32_16x16x32_bf16 v[108:111], a[100:103], v[124:127], v[108:111]\n\t" \
        "v_mfma_f32_16x16x32_bf16 v[112:115], a[104:107], v[124:127], v[112:115]\n\t" \
        "ds_read_b128 v[124:127], v16 offset:51648\n\t" \
        "v_mfma_f32_16x16x32_bf16 v[104:107], a[108:111], v[128:131], v[104:107]\n\t" \
        "v_mfma_f32_16x16x32_bf16 v[108:111], a[112:115], v[128:131], v[108:111]\n\t" \
        "v_mfma_f32_16x16x32_bf16 v[112:115], a[116:119], v[128:131], v[112:115]\n\t" \
        "ds_read_b128 v[128:131], v16 offset:51712\n\t" \
        "v_mfma_f32_16x16x32_bf16 v[104:107], a[120:123], v[132:135], v[104:107]\n\t" \
        "v_mfma_f32_16x16x32_bf16 v[108:111], a[124:127], v[132:135], v[108:111]\n\t" \
        "s_add_u32 s56, s64, 0x69000\n\t" \
        "buffer_store_dwordx4 v[100:103], v10, s[28:31], s56 offen sc1\n\t" \
        "v_mfma_f32_16x16x32_bf16 v[112:115], a[128:131], v[132:135], v[112:115]\n\t" \
        "ds_read_b128 v[132:135], v14 offset:55552\n\t" \
        "v_mfma_f32_16x16x32_bf16 v[104:107], a[132:135], v[136:139], v[104:107]\n\t" \
        "v_mfma_f32_16x16x32_bf16 v[108:111], a[136:139], v[136:139], v[108:111]\n\t" \
        "v_mfma_f32_16x16x32_bf16 v[112:115], a[140:143], v[136:139], v[112:115]\n\t" \
        "ds_read_b128 v[136:139], v14 offset:55616\n\t" \
        "s_waitcnt lgkmcnt(4)\n\t" \
        "v_mfma_f32_16x16x32_bf16 v[104:107], a[144:147], v[140:143], v[104:107]\n\t" \
        "v_mfma_f32_16x16x32_bf16 v[108:111], a[148:151], v[140:143], v[108:111]\n\t" \
        "v_mfma_f32_16x16x32_bf16 v[112:115], a[152:155], v[140:143], v[112:115]\n\t" \
        "ds_read_b128 v[140:143], v14 offset:55680\n\t" \
        "v_mfma_f32_16x16x32_bf16 v[104:107], a[156:159], v[144:147], v[104:107]\n\t" \
        "v_mfma_f32_16x16x32_bf16 v[108:111], a[160:163], v[144:147], v[108:111]\n\t" \
        "v_mfma_f32_16x16x32_bf16 v[112:115], a[164:167], v[144:147], v[112:115]\n\t" \
        "ds_read_b128 v[144:147], v15 offset:55552\n\t" \
        "v_mfma_f32_16x16x32_bf16 v[104:107], a[168:171], v[116:119], v[104:107]\n\t" \
        "v_mfma_f32_16x16x32_bf16 v[108:111], a[172:175], v[116:119], v[108:111]\n\t" \
        "v_mfma_f32_16x16x32_bf16 v[112:115], a[176:179], v[116:119], v[112:115]\n\t" \
        "ds_read_b128 v[116:119], v15 offset:55616\n\t" \
        "v_mfma_f32_16x16x32_bf16 v[104:107], a[180:183], v[120:123], v[104:107]\n\t" \
        "v_mfma_f32_16x16x32_bf16 v[108:111], a[184:187], v[120:123], v[108:111]\n\t" \
        "v_mfma_f32_16x16x32_bf16 v[112:115], a[188:191], v[120:123], v[112:115]\n\t" \
        "ds_read_b128 v[120:123], v15 offset:55680\n\t" \
        "s_waitcnt lgkmcnt(4)\n\t" \
        "v_mfma_f32_16x16x32_bf16 v[104:107], a[192:195], v[124:127], v[104:107]\n\t" \
        "v_mfma_f32_16x16x32_bf16 v[108:111], a[196:199], v[124:127], v[108:111]\n\t" \
        "v_mfma_f32_16x16x32_bf16 v[112:115], a[200:203], v[124:127], v[112:115]\n\t" \
        "ds_read_b128 v[124:127], v16 offset:55552\n\t" \
        "v_mfma_f32_16x16x32_bf16 v[104:107], a[204:207], v[128:131], v[104:107]\n\t" \
        "v_mfma_f32_16x16x32_bf16 v[108:111], a[208:211], v[128:131], v[108:111]\n\t" \
        "v_mfma_f32_16x16x32_bf16 v[112:115], a[212:215], v[128:131], v[112:115]\n\t" \
        "ds_read_b128 v[128:131], v16 offset:55616\n\t" \
        "v_mfma_f32_16x16x32_bf16 v[104:107], a[216:219], v[132:135], v[104:107]\n\t" \
        "v_mfma_f32_16x16x32_bf16 v[108:111], a[220:223], v[132:135], v[108:111]\n\t" \
        "v_mfma_f32_16x16x32_bf16 v[112:115], a[224:227], v[132:135], v[112:115]\n\t" \
        "ds_read_b128 v[132:135], v16 offset:55680\n\t" \
        "v_mfma_f32_16x16x32_bf16 v[104:107], a[228:231], v[136:139], v[104:107]\n\t" \
        "v_mfma_f32_16x16x32_bf16 v[108:111], a[232:235], v[136:139], v[108:111]\n\t" \
        "v_mfma_f32_16x16x32_bf16 v[112:115], a[236:239], v[136:139], v[112:115]\n\t" \
        "ds_read_b128 v[136:139], v14 offset:55552\n\t" \
        "s_waitcnt lgkmcnt(4)\n\t" \
        "v_mfma_f32_16x16x32_bf16 v[104:107], a[240:243], v[140:143], v[104:107]\n\t" \
        "v_mfma_f32_16x16x32_bf16 v[108:111], a[244:247], v[140:143], v[108:111]\n\t" \
        "v_mfma_f32_16x16x32_bf16 v[112:115], a[248:251], v[140:143], v[112:115]\n\t" \
        "ds_read_b128 v[140:143], v14 offset:55616\n\t" \
        "v_mfma_f32_16x16x32_bf16 v[104:107], a[252:255], v[144:147], v[104:107]\n\t" \
        "v_mfma_f32_16x16x32_bf16 v[108:111], v[188:191], v[144:147], v[108:111]\n\t" \
        "v_mfma_f32_16x16x32_bf16 v[112:115], v[192:195], v[144:147], v[112:115]\n\t" \
        "ds_read_b128 v[144:147], v14 offset:55680\n\t" \
        "v_mfma_f32_16x16x32_bf16 v[104:107], v[196:199], v[116:119], v[104:107]\n\t" \
        "v_mfma_f32_16x16x32_bf16 v[108:111], v[200:203], v[116:119], v[108:111]\n\t" \
        "v_mfma_f32_16x16x32_bf16 v[112:115], v[204:207], v[116:119], v[112:115]\n\t" \
        "ds_read_b128 v[116:119], v15 offset:55552\n\t" \
        "v_mfma_f32_16x16x32_bf16 v[104:107], v[208:211], v[120:123], v[104:107]\n\t" \
        "v_mfma_f32_16x16x32_bf16 v[108:111], v[212:215], v[120:123], v[108:111]\n\t" \
        "v_mfma_f32_16x16x32_bf16 v[112:115], v[216:219], v[120:123], v[112:115]\n\t" \
        "ds_read_b128 v[120:123], v15 offset:55616\n\t" \
        "s_waitcnt lgkmcnt(4)\n\t" \
        "v_mfma_f32_16x16x32_bf16 v[104:107], v[220:223], v[124:127], v[104:107]\n\t" \
        "v_mfma_f32_16x16x32_bf16 v[108:111], v[224:227], v[124:127], v[108:111]\n\t" \
        "v_mfma_f32_16x16x32_bf16 v[112:115], v[228:231], v[124:127], v[112:115]\n\t" \
        "ds_read_b128 v[124:127], v15 offset:55680\n\t" \
        "v_mfma_f32_16x16x32_bf16 v[104:107], v[232:235], v[128:131], v[104:107]\n\t" \
        "v_mfma_f32_16x16x32_bf16 v[108:111], v[236:239], v[128:131], v[108:111]\n\t" \
        "v_mfma_f32_16x16x32_bf16 v[112:115], v[240:243], v[128:131], v[112:115]\n\t" \
        "ds_read_b128 v[128:131], v16 offset:55552\n\t" \
        "v_mfma_f32_16x16x32_bf16 v[104:107], v[244:247], v[132:135], v[104:107]\n\t" \
        "v_mfma_f32_16x16x32_bf16 v[108:111], v[248:251], v[132:135], v[108:111]\n\t" \
        "v_mfma_f32_16x16x32_bf16 v[112:115], v[252:255], v[132:135], v[112:115]\n\t" \
        "ds_read_b128 v[132:135], v16 offset:55616\n\t" \
        "v_mfma_f32_16x16x32_bf16 v[32:35], a[0:3], v[136:139], 0\n\t" \
        "v_mfma_f32_16x16x32_bf16 v[36:39], a[4:7], v[136:139], 0\n\t" \
        "v_mfma_f32_16x16x32_bf16 v[40:43], a[8:11], v[136:139], 0\n\t" \
        "s_nop 5\n\t" \
        "v_cvt_pk_bf16_f32 v112, v112, v113\n\t" \
        "v_cvt_pk_bf16_f32 v113, v114, v115\n\t" \
        "s_waitcnt lgkmcnt(3)\n\t" \
        "v_mfma_f32_16x16x32_bf16 v[32:35], a[12:15], v[140:143], v[32:35]\n\t" \
        "ds_write_b64 v11, v[112:113] offset:12288\n\t" \
        "v_cvt_pk_bf16_f32 v104, v104, v105\n\t" \
        "v_mfma_f32_16x16x32_bf16 v[36:39], a[16:19], v[140:143], v[36:39]\n\t" \
        "ds_read_b128 v[136:139], v16 offset:55680\n\t" \
        "v_cvt_pk_bf16_f32 v105, v106, v107\n\t" \
        "v_mfma_f32_16x16x32_bf16 v[40:43], a[20:23], v[140:143], v[40:43]\n\t" \
        "ds_read_b128 v[140:143], v14 offset:59520\n\t" \
        "v_cvt_pk_bf16_f32 v106, v108, v109\n\t" \
        "v_mfma_f32_16x16x32_bf16 v[32:35], a[24:27], v[144:147], v[32:35]\n\t" \
        "v_cvt_pk_bf16_f32 v107, v110, v111\n\t" \
        "v_permlane16_swap_b32 v104, v106\n\t" \
        "v_mfma_f32_16x16x32_bf16 v[36:39], a[28:31], v[144:147], v[36:39]\n\t" \
        "v_permlane16_swap_b32 v105, v107\n\t" \
        "v_mfma_f32_16x16x32_bf16 v[40:43], a[32:35], v[144:147], v[40:43]\n\t" \
        "ds_read_b128 v[144:147], v14 offset:59584\n\t" \
        "v_mfma_f32_16x16x32_bf16 v[32:35], a[36:39], v[116:119], v[32:35]\n\t" \
        "v_mfma_f32_16x16x32_bf16 v[36:39], a[40:43], v[116:119], v[36:39]\n\t" \
        "v_mfma_f32_16x16x32_bf16 v[40:43], a[44:47], v[116:119], v[40:43]\n\t" \
        "ds_read_b128 v[116:119], v14 offset:59648\n\t" \
        "v_mfma_f32_16x16x32_bf16 v[32:35], a[48:51], v[120:123], v[32:35]\n\t" \
        "v_mfma_f32_16x16x32_bf16 v[36:39], a[52:55], v[120:123], v[36:39]\n\t" \
        "s_waitcnt lgkmcnt(4)\n\t" \
        "s_barrier\n\t" \
        "ds_read_b64 v[114:115], v12 offset:12288\n\t" \
        "v_mfma_f32_16x16x32_bf16 v[40:43], a[56:59], v[120:123], v[40:43]\n\t" \
        "ds_read_b128 v[120:123], v15 offset:59520\n\t" \
        "v_mfma_f32_16x16x32_bf16 v[32:35], a[60:63], v[124:127], v[32:35]\n\t" \
        "v_mfma_f32_16x16x32_bf16 v[36:39], a[64:67], v[124:127], v[36:39]\n\t" \
        "v_mfma_f32_16x16x32_bf16 v[40:43], a[68:71], v[124:127], v[40:43]\n\t" \
        "ds_read_b128 v[124:127], v15 offset:59584\n\t" \
        "v_mfma_f32_16x16x32_bf16 v[32:35], a[72:75], v[128:131], v[32:35]\n\t" \
        "v_mfma_f32_16x16x32_bf16 v[36:39], a[76:79], v[128:131], v[36:39]\n\t" \
        "v_mfma_f32_16x16x32_bf16 v[40:43], a[80:83], v[128:131], v[40:43]\n\t" \
        "ds_read_b128 v[128:131], v15 offset:59648\n\t" \
        "v_mfma_f32_16x16x32_bf16 v[32:35], a[84:87], v[132:135], v[32:35]\n\t" \
        "s_waitcnt lgkmcnt(3)\n\t" \
        "v_permlane16_swap_b32 v112, v114\n\t" \
        "v_permlane16_swap_b32 v113, v115\n\t" \
        "v_mfma_f32_16x16x32_bf16 v[36:39], a[88:91], v[132:135], v[36:39]\n\t" \
        "v_mfma_f32_16x16x32_bf16 v[40:43], a[92:95], v[132:135], v[40:43]\n\t" \
        "ds_read_b128 v[132:135], v16 offset:59520\n\t" \
        "v_mfma_f32_16x16x32_bf16 v[32:35], a[96:99], v[136:139], v[32:35]\n\t" \
        "s_add_u32 s56, s64, 0x7e000\n\t" \
        "buffer_store_dwordx4 v[104:107], v9, s[28:31], s56 offen sc1\n\t" \
        "v_mfma_f32_16x16x32_bf16 v[36:39], a[100:103], v[136:139], v[36:39]\n\t" \
        "v_mfma_f32_16x16x32_bf16 v[40:43], a[104:107], v[136:139], v[40:43]\n\t" \
        "ds_read_b128 v[136:139], v16 offset:59584\n\t" \
        "v_mfma_f32_16x16x32_bf16 v[32:35], a[108:111], v[140:143], v[32:35]\n\t" \
        "v_mfma_f32_16x16x32_bf16 v[36:39], a[112:115], v[140:143], v[36:39]\n\t" \
        "v_mfma_f32_16x16x32_bf16 v[40:43], a[116:119], v[140:143], v[40:43]\n\t" \
        "ds_read_b128 v[140:143], v16 offset:59648\n\t" \
        "v_mfma_f32_16x16x32_bf16 v[32:35], a[120:123], v[144:147], v[32:35]\n\t" \
        "v_mfma_f32_16x16x32_bf16 v[36:39], a[124:127], v[144:147], v[36:39]\n\t" \
        "s_add_u32 s56, s64, 0x7e000\n\t" \
        "buffer_store_dwordx4 v[112:115], v10, s[28:31], s56 offen sc1\n\t" \
        "v_mfma_f32_16x16x32_bf16 v[40:43], a[128:131], v[144:147], v[40:43]\n\t" \
        "ds_read_b128 v[144:147], v14 offset:63488\n\t" \
        "v_mfma_f32_16x16x32_bf16 v[32:35], a[132:135], v[116:119], v[32:35]\n\t" \
        "v_mfma_f32_16x16x32_bf16 v[36:39], a[136:139], v[116:119], v[36:39]\n\t" \
        "v_mfma_f32_16x16x32_bf16 v[40:43], a[140:143], v[116:119], v[40:43]\n\t" \
        "ds_read_b128 v[116:119], v14 offset:63552\n\t" \
        "s_waitcnt lgkmcnt(4)\n\t" \
        "v_mfma_f32_16x16x32_bf16 v[32:35], a[144:147], v[120:123], v[32:35]\n\t" \
        "v_mfma_f32_16x16x32_bf16 v[36:39], a[148:151], v[120:123], v[36:39]\n\t" \
        "v_mfma_f32_16x16x32_bf16 v[40:43], a[152:155], v[120:123], v[40:43]\n\t" \
        "ds_read_b128 v[120:123], v14 offset:63616\n\t" \
        "v_mfma_f32_16x16x32_bf16 v[32:35], a[156:159], v[124:127], v[32:35]\n\t" \
        "v_mfma_f32_16x16x32_bf16 v[36:39], a[160:163], v[124:127], v[36:39]\n\t" \
        "v_mfma_f32_16x16x32_bf16 v[40:43], a[164:167], v[124:127], v[40:43]\n\t" \
        "ds_read_b128 v[124:127], v15 offset:63488\n\t" \
        "v_mfma_f32_16x16x32_bf16 v[32:35], a[168:171], v[128:131], v[32:35]\n\t" \
        "v_mfma_f32_16x16x32_bf16 v[36:39], a[172:175], v[128:131], v[36:39]\n\t" \
        "v_mfma_f32_16x16x32_bf16 v[40:43], a[176:179], v[128:131], v[40:43]\n\t" \
        "ds_read_b128 v[128:131], v15 offset:63552\n\t" \
        "v_mfma_f32_16x16x32_bf16 v[32:35], a[180:183], v[132:135], v[32:35]\n\t" \
        "v_mfma_f32_16x16x32_bf16 v[36:39], a[184:187], v[132:135], v[36:39]\n\t" \
        "v_mfma_f32_16x16x32_bf16 v[40:43], a[188:191], v[132:135], v[40:43]\n\t" \
        "ds_read_b128 v[132:135], v15 offset:63616\n\t" \
        "s_waitcnt lgkmcnt(4)\n\t" \
        "v_mfma_f32_16x16x32_bf16 v[32:35], a[192:195], v[136:139], v[32:35]\n\t" \
        "v_mfma_f32_16x16x32_bf16 v[36:39], a[196:199], v[136:139], v[36:39]\n\t" \
        "v_mfma_f32_16x16x32_bf16 v[40:43], a[200:203], v[136:139], v[40:43]\n\t" \
        "ds_read_b128 v[136:139], v16 offset:63488\n\t" \
        "v_mfma_f32_16x16x32_bf16 v[32:35], a[204:207], v[140:143], v[32:35]\n\t" \
        "v_mfma_f32_16x16x32_bf16 v[36:39], a[208:211], v[140:143], v[36:39]\n\t" \
        "v_mfma_f32_16x16x32_bf16 v[40:43], a[212:215], v[140:143], v[40:43]\n\t" \
        "ds_read_b128 v[140:143], v16 offset:63552\n\t" \
        "v_mfma_f32_16x16x32_bf16 v[32:35], a[216:219], v[144:147], v[32:35]\n\t" \
        "v_mfma_f32_16x16x32_bf16 v[36:39], a[220:223], v[144:147], v[36:39]\n\t" \
        "v_mfma_f32_16x16x32_bf16 v[40:43], a[224:227], v[144:147], v[40:43]\n\t" \
        "ds_read_b128 v[144:147], v16 offset:63616\n\t" \
        "v_mfma_f32_16x16x32_bf16 v[32:35], a[228:231], v[116:119], v[32:35]\n\t" \
        "v_mfma_f32_16x16x32_bf16 v[36:39], a[232:235], v[116:119], v[36:39]\n\t" \
        "v_mfma_f32_16x16x32_bf16 v[40:43], a[236:239], v[116:119], v[40:43]\n\t" \
        "s_waitcnt lgkmcnt(3)\n\t" \
        "v_mfma_f32_16x16x32_bf16 v[32:35], a[240:243], v[120:123], v[32:35]\n\t" \
        "v_mfma_f32_16x16x32_bf16 v[36:39], a[244:247], v[120:123], v[36:39]\n\t" \
        "v_mfma_f32_16x16x32_bf16 v[40:43], a[248:251], v[120:123], v[40:43]\n\t" \
        "v_mfma_f32_16x16x32_bf16 v[32:35], a[252:255], v[124:127], v[32:35]\n\t" \
        "v_mfma_f32_16x16x32_bf16 v[36:39], v[188:191], v[124:127], v[36:39]\n\t" \
        "v_mfma_f32_16x16x32_bf16 v[40:43], v[192:195], v[124:127], v[40:43]\n\t" \
        "v_mfma_f32_16x16x32_bf16 v[32:35], v[196:199], v[128:131], v[32:35]\n\t" \
        "v_mfma_f32_16x16x32_bf16 v[36:39], v[200:203], v[128:131], v[36:39]\n\t" \
        "v_mfma_f32_16x16x32_bf16 v[40:43], v[204:207], v[128:131], v[40:43]\n\t" \
        "v_mfma_f32_16x16x32_bf16 v[32:35], v[208:211], v[132:135], v[32:35]\n\t" \
        "v_mfma_f32_16x16x32_bf16 v[36:39], v[212:215], v[132:135], v[36:39]\n\t" \
        "v_mfma_f32_16x16x32_bf16 v[40:43], v[216:219], v[132:135], v[40:43]\n\t" \
        "s_waitcnt lgkmcnt(0)\n\t" \
        "v_mfma_f32_16x16x32_bf16 v[32:35], v[220:223], v[136:139], v[32:35]\n\t" \
        "v_mfma_f32_16x16x32_bf16 v[36:39], v[224:227], v[136:139], v[36:39]\n\t" \
        "v_mfma_f32_16x16x32_bf16 v[40:43], v[228:231], v[136:139], v[40:43]\n\t" \
        "v_mfma_f32_16x16x32_bf16 v[32:35], v[232:235], v[140:143], v[32:35]\n\t" \
        "v_mfma_f32_16x16x32_bf16 v[36:39], v[236:239], v[140:143], v[36:39]\n\t" \
        "v_mfma_f32_16x16x32_bf16 v[40:43], v[240:243], v[140:143], v[40:43]\n\t" \
        "v_mfma_f32_16x16x32_bf16 v[32:35], v[244:247], v[144:147], v[32:35]\n\t" \
        "v_mfma_f32_16x16x32_bf16 v[36:39], v[248:251], v[144:147], v[36:39]\n\t" \
        "v_mfma_f32_16x16x32_bf16 v[40:43], v[252:255], v[144:147], v[40:43]\n\t" \
        "s_nop 9\n\t" \
        "v_cvt_pk_bf16_f32 v40, v40, v41\n\t" \
        "v_cvt_pk_bf16_f32 v41, v42, v43\n\t" \
        "v_cvt_pk_bf16_f32 v32, v32, v33\n\t" \
        "v_cvt_pk_bf16_f32 v33, v34, v35\n\t" \
        "v_cvt_pk_bf16_f32 v34, v36, v37\n\t" \
        "ds_write_b64 v11, v[40:41] offset:14336\n\t" \
        "v_cvt_pk_bf16_f32 v35, v38, v39\n\t" \
        "v_permlane16_swap_b32 v32, v34\n\t" \
        "s_nop 0\n\t" \
        "v_permlane16_swap_b32 v33, v35\n\t" \
        "s_add_u32 s56, s64, 0x93000\n\t" \
        "buffer_store_dwordx4 v[32:35], v9, s[28:31], s56 offen sc1\n\t" \
        "s_waitcnt lgkmcnt(0)\n\t" \
        "s_barrier\n\t" \
        "ds_read_b64 v[42:43], v12 offset:14336\n\t" \
        "s_waitcnt lgkmcnt(0)\n\t" \
        "v_permlane16_swap_b32 v40, v42\n\t" \
        "v_permlane16_swap_b32 v41, v43\n\t" \
        "s_add_u32 s56, s64, 0x93000\n\t" \
        "buffer_store_dwordx4 v[40:43], v10, s[28:31], s56 offen sc1\n\t" \
        "s_endpgm\n\t" \
        ".LR14%=:\n\t" \
        "ds_read_b128 v[120:123], v16 offset:47680\n\t" \
        "v_mfma_f32_16x16x32_bf16 v[104:107], a[0:3], v[124:127], 0\n\t" \
        "v_mfma_f32_16x16x32_bf16 v[108:111], a[4:7], v[124:127], 0\n\t" \
        "v_mfma_f32_16x16x32_bf16 v[112:115], a[8:11], v[124:127], 0\n\t" \
        "s_nop 3\n\t" \
        "v_cvt_pk_bf16_f32 v100, v100, v101\n\t" \
        "v_cvt_pk_bf16_f32 v101, v102, v103\n\t" \
        "v_mfma_f32_16x16x32_bf16 v[104:107], a[12:15], v[128:131], v[104:107]\n\t" \
        "ds_write_b64 v11, v[100:101] offset:10240\n\t" \
        "v_cvt_pk_bf16_f32 v92, v92, v93\n\t" \
        "v_mfma_f32_16x16x32_bf16 v[108:111], a[16:19], v[128:131], v[108:111]\n\t" \
        "ds_read_b128 v[124:127], v16 offset:47744\n\t" \
        "v_cvt_pk_bf16_f32 v93, v94, v95\n\t" \
        "v_mfma_f32_16x16x32_bf16 v[112:115], a[20:23], v[128:131], v[112:115]\n\t" \
        "ds_read_b128 v[128:131], v14 offset:51584\n\t" \
        "v_cvt_pk_bf16_f32 v94, v96, v97\n\t" \
        "s_waitcnt lgkmcnt(5)\n\t" \
        "v_mfma_f32_16x16x32_bf16 v[104:107], a[24:27], v[132:135], v[104:107]\n\t" \
        "v_cvt_pk_bf16_f32 v95, v98, v99\n\t" \
        "v_permlane16_swap_b32 v92, v94\n\t" \
        "v_mfma_f32_16x16x32_bf16 v[108:111], a[28:31], v[132:135], v[108:111]\n\t" \
        "v_permlane16_swap_b32 v93, v95\n\t" \
        "v_mfma_f32_16x16x32_bf16 v[112:115], a[32:35], v[132:135], v[112:115]\n\t" \
        "ds_read_b128 v[132:135], v14 offset:51648\n\t" \
        "v_mfma_f32_16x16x32_bf16 v[104:107], a[36:39], v[136:139], v[104:107]\n\t" \
        "v_mfma_f32_16x16x32_bf16 v[108:111], a[40:43], v[136:139], v[108:111]\n\t" \
        "v_mfma_f32_16x16x32_bf16 v[112:115], a[44:47], v[136:139], v[112:115]\n\t" \
        "ds_read_b128 v[136:139], v14 offset:51712\n\t" \
        "v_mfma_f32_16x16x32_bf16 v[104:107], a[48:51], v[140:143], v[104:107]\n\t" \
        "v_mfma_f32_16x16x32_bf16 v[108:111], a[52:55], v[140:143], v[108:111]\n\t" \
        "s_waitcnt lgkmcnt(4)\n\t" \
        "s_barrier\n\t" \
        "ds_read_b64 v[102:103], v12 offset:10240\n\t" \
        "v_mfma_f32_16x16x32_bf16 v[112:115], a[56:59], v[140:143], v[112:115]\n\t" \
        "ds_read_b128 v[140:143], v15 offset:51584\n\t" \
        "v_mfma_f32_16x16x32_bf16 v[104:107], a[60:63], v[144:147], v[104:107]\n\t" \
        "v_mfma_f32_16x16x32_bf16 v[108:111], a[64:67], v[144:147], v[108:111]\n\t" \
        "v_mfma_f32_16x16x32_bf16 v[112:115], a[68:71], v[144:147], v[112:115]\n\t" \
        "ds_read_b128 v[144:147], v15 offset:51648\n\t" \
        "v_mfma_f32_16x16x32_bf16 v[104:107], a[72:75], v[116:119], v[104:107]\n\t" \
        "v_mfma_f32_16x16x32_bf16 v[108:111], a[76:79], v[116:119], v[108:111]\n\t" \
        "v_mfma_f32_16x16x32_bf16 v[112:115], a[80:83], v[116:119], v[112:115]\n\t" \
        "ds_read_b128 v[116:119], v15 offset:51712\n\t" \
        "v_mfma_f32_16x16x32_bf16 v[104:107], a[84:87], v[120:123], v[104:107]\n\t" \
        "s_waitcnt lgkmcnt(3)\n\t" \
        "v_permlane16_swap_b32 v100, v102\n\t" \
        "v_permlane16_swap_b32 v101, v103\n\t" \
        "v_mfma_f32_16x16x32_bf16 v[108:111], a[88:91], v[120:123], v[108:111]\n\t" \
        "v_mfma_f32_16x16x32_bf16 v[112:115], a[92:95], v[120:123], v[112:115]\n\t" \
        "ds_read_b128 v[120:123], v16 offset:51584\n\t" \
        "v_mfma_f32_16x16x32_bf16 v[104:107], a[96:99], v[124:127], v[104:107]\n\t" \
        "s_add_u32 s56, s64, 0x69000\n\t" \
        "buffer_store_dwordx4 v[92:95], v9, s[28:31], s56 offen sc1\n\t" \
        "v_mfma_f32_16x16x32_bf16 v[108:111], a[100:103], v[124:127], v[108:111]\n\t" \
        "v_mfma_f32_16x16x32_bf16 v[112:115], a[104:107], v[124:127], v[112:115]\n\t" \
        "ds_read_b128 v[124:127], v16 offset:51648\n\t" \
        "v_mfma_f32_16x16x32_bf16 v[104:107], a[108:111], v[128:131], v[104:107]\n\t" \
        "v_mfma_f32_16x16x32_bf16 v[108:111], a[112:115], v[128:131], v[108:111]\n\t" \
        "v_mfma_f32_16x16x32_bf16 v[112:115], a[116:119], v[128:131], v[112:115]\n\t" \
        "ds_read_b128 v[128:131], v16 offset:51712\n\t" \
        "v_mfma_f32_16x16x32_bf16 v[104:107], a[120:123], v[132:135], v[104:107]\n\t" \
        "v_mfma_f32_16x16x32_bf16 v[108:111], a[124:127], v[132:135], v[108:111]\n\t" \
        "s_add_u32 s56, s64, 0x69000\n\t" \
        "buffer_store_dwordx4 v[100:103], v10, s[28:31], s56 offen sc1\n\t" \
        "v_mfma_f32_16x16x32_bf16 v[112:115], a[128:131], v[132:135], v[112:115]\n\t" \
        "ds_read_b128 v[132:135], v14 offset:55552\n\t" \
        "v_mfma_f32_16x16x32_bf16 v[104:107], a[132:135], v[136:139], v[104:107]\n\t" \
        "v_mfma_f32_16x16x32_bf16 v[108:111], a[136:139], v[136:139], v[108:111]\n\t" \
        "v_mfma_f32_16x16x32_bf16 v[112:115], a[140:143], v[136:139], v[112:115]\n\t" \
        "ds_read_b128 v[136:139], v14 offset:55616\n\t" \
        "s_waitcnt lgkmcnt(4)\n\t" \
        "v_mfma_f32_16x16x32_bf16 v[104:107], a[144:147], v[140:143], v[104:107]\n\t" \
        "v_mfma_f32_16x16x32_bf16 v[108:111], a[148:151], v[140:143], v[108:111]\n\t" \
        "v_mfma_f32_16x16x32_bf16 v[112:115], a[152:155], v[140:143], v[112:115]\n\t" \
        "ds_read_b128 v[140:143], v14 offset:55680\n\t" \
        "v_mfma_f32_16x16x32_bf16 v[104:107], a[156:159], v[144:147], v[104:107]\n\t" \
        "v_mfma_f32_16x16x32_bf16 v[108:111], a[160:163], v[144:147], v[108:111]\n\t" \
        "v_mfma_f32_16x16x32_bf16 v[112:115], a[164:167], v[144:147], v[112:115]\n\t" \
        "ds_read_b128 v[144:147], v15 offset:55552\n\t" \
        "v_mfma_f32_16x16x32_bf16 v[104:107], a[168:171], v[116:119], v[104:107]\n\t" \
        "v_mfma_f32_16x16x32_bf16 v[108:111], a[172:175], v[116:119], v[108:111]\n\t" \
        "v_mfma_f32_16x16x32_bf16 v[112:115], a[176:179], v[116:119], v[112:115]\n\t" \
        "ds_read_b128 v[116:119], v15 offset:55616\n\t" \
        "v_mfma_f32_16x16x32_bf16 v[104:107], a[180:183], v[120:123], v[104:107]\n\t" \
        "v_mfma_f32_16x16x32_bf16 v[108:111], a[184:187], v[120:123], v[108:111]\n\t" \
        "v_mfma_f32_16x16x32_bf16 v[112:115], a[188:191], v[120:123], v[112:115]\n\t" \
        "ds_read_b128 v[120:123], v15 offset:55680\n\t" \
        "s_waitcnt lgkmcnt(4)\n\t" \
        "v_mfma_f32_16x16x32_bf16 v[104:107], a[192:195], v[124:127], v[104:107]\n\t" \
        "v_mfma_f32_16x16x32_bf16 v[108:111], a[196:199], v[124:127], v[108:111]\n\t" \
        "v_mfma_f32_16x16x32_bf16 v[112:115], a[200:203], v[124:127], v[112:115]\n\t" \
        "ds_read_b128 v[124:127], v16 offset:55552\n\t" \
        "v_mfma_f32_16x16x32_bf16 v[104:107], a[204:207], v[128:131], v[104:107]\n\t" \
        "v_mfma_f32_16x16x32_bf16 v[108:111], a[208:211], v[128:131], v[108:111]\n\t" \
        "v_mfma_f32_16x16x32_bf16 v[112:115], a[212:215], v[128:131], v[112:115]\n\t" \
        "ds_read_b128 v[128:131], v16 offset:55616\n\t" \
        "v_mfma_f32_16x16x32_bf16 v[104:107], a[216:219], v[132:135], v[104:107]\n\t" \
        "v_mfma_f32_16x16x32_bf16 v[108:111], a[220:223], v[132:135], v[108:111]\n\t" \
        "v_mfma_f32_16x16x32_bf16 v[112:115], a[224:227], v[132:135], v[112:115]\n\t" \
        "ds_read_b128 v[132:135], v16 offset:55680\n\t" \
        "v_mfma_f32_16x16x32_bf16 v[104:107], a[228:231], v[136:139], v[104:107]\n\t" \
        "v_mfma_f32_16x16x32_bf16 v[108:111], a[232:235], v[136:139], v[108:111]\n\t" \
        "v_mfma_f32_16x16x32_bf16 v[112:115], a[236:239], v[136:139], v[112:115]\n\t" \
        "s_waitcnt lgkmcnt(3)\n\t" \
        "v_mfma_f32_16x16x32_bf16 v[104:107], a[240:243], v[140:143], v[104:107]\n\t" \
        "v_mfma_f32_16x16x32_bf16 v[108:111], a[244:247], v[140:143], v[108:111]\n\t" \
        "v_mfma_f32_16x16x32_bf16 v[112:115], a[248:251], v[140:143], v[112:115]\n\t" \
        "v_mfma_f32_16x16x32_bf16 v[104:107], a[252:255], v[144:147], v[104:107]\n\t" \
        "v_mfma_f32_16x16x32_bf16 v[108:111], v[188:191], v[144:147], v[108:111]\n\t" \
        "v_mfma_f32_16x16x32_bf16 v[112:115], v[192:195], v[144:147], v[112:115]\n\t" \
        "v_mfma_f32_16x16x32_bf16 v[104:107], v[196:199], v[116:119], v[104:107]\n\t" \
        "v_mfma_f32_16x16x32_bf16 v[108:111], v[200:203], v[116:119], v[108:111]\n\t" \
        "v_mfma_f32_16x16x32_bf16 v[112:115], v[204:207], v[116:119], v[112:115]\n\t" \
        "v_mfma_f32_16x16x32_bf16 v[104:107], v[208:211], v[120:123], v[104:107]\n\t" \
        "v_mfma_f32_16x16x32_bf16 v[108:111], v[212:215], v[120:123], v[108:111]\n\t" \
        "v_mfma_f32_16x16x32_bf16 v[112:115], v[216:219], v[120:123], v[112:115]\n\t" \
        "s_waitcnt lgkmcnt(0)\n\t" \
        "v_mfma_f32_16x16x32_bf16 v[104:107], v[220:223], v[124:127], v[104:107]\n\t" \
        "v_mfma_f32_16x16x32_bf16 v[108:111], v[224:227], v[124:127], v[108:111]\n\t" \
        "v_mfma_f32_16x16x32_bf16 v[112:115], v[228:231], v[124:127], v[112:115]\n\t" \
        "v_mfma_f32_16x16x32_bf16 v[104:107], v[232:235], v[128:131], v[104:107]\n\t" \
        "v_mfma_f32_16x16x32_bf16 v[108:111], v[236:239], v[128:131], v[108:111]\n\t" \
        "v_mfma_f32_16x16x32_bf16 v[112:115], v[240:243], v[128:131], v[112:115]\n\t" \
        "v_mfma_f32_16x16x32_bf16 v[104:107], v[244:247], v[132:135], v[104:107]\n\t" \
        "v_mfma_f32_16x16x32_bf16 v[108:111], v[248:251], v[132:135], v[108:111]\n\t" \
        "v_mfma_f32_16x16x32_bf16 v[112:115], v[252:255], v[132:135], v[112:115]\n\t" \
        "s_nop 9\n\t" \
        "v_cvt_pk_bf16_f32 v112, v112, v113\n\t" \
        "v_cvt_pk_bf16_f32 v113, v114, v115\n\t" \
        "v_cvt_pk_bf16_f32 v104, v104, v105\n\t" \
        "v_cvt_pk_bf16_f32 v105, v106, v107\n\t" \
        "v_cvt_pk_bf16_f32 v106, v108, v109\n\t" \
        "ds_write_b64 v11, v[112:113] offset:12288\n\t" \
        "v_cvt_pk_bf16_f32 v107, v110, v111\n\t" \
        "v_permlane16_swap_b32 v104, v106\n\t" \
        "s_nop 0\n\t" \
        "v_permlane16_swap_b32 v105, v107\n\t" \
        "s_add_u32 s56, s64, 0x7e000\n\t" \
        "buffer_store_dwordx4 v[104:107], v9, s[28:31], s56 offen sc1\n\t" \
        "s_waitcnt lgkmcnt(0)\n\t" \
        "s_barrier\n\t" \
        "ds_read_b64 v[114:115], v12 offset:12288\n\t" \
        "s_waitcnt lgkmcnt(0)\n\t" \
        "v_permlane16_swap_b32 v112, v114\n\t" \
        "v_permlane16_swap_b32 v113, v115\n\t" \
        "s_add_u32 s56, s64, 0x7e000\n\t" \
        "buffer_store_dwordx4 v[112:115], v10, s[28:31], s56 offen sc1\n\t" \
        "s_endpgm\n\t" \
        ".LH8_start%=:\n\t" \
        "s_add_u32 s57, s36, 0x15000\n\t" \
        "s_add_u32 m0, s38, 0x3e00\n\t" \
        "s_nop 0\n\t" \
        "buffer_load_dwordx4 v13, s[16:19], s57 offen sc1 lds\n\t" \
        "s_add_u32 s57, s36, 0x1f800\n\t" \
        "s_add_u32 m0, s38, 0x4d80\n\t" \
        "s_nop 0\n\t" \
        "buffer_load_dwordx4 v13, s[16:19], s57 offen sc1 lds\n\t" \
        "s_mov_b32 s62, 0\n\t" \
        "s_mov_b32 s63, -1\n\t" \
        "v_cmp_lt_u32 vcc, 47, v2\n\t" \
        "v_lshlrev_b32 v162, 4, v2\n\t" \
        "v_xor_b32 v163, 32, v162\n\t" \
        "v_cndmask_b32 v3, v162, v163, vcc\n\t" \
        "s_add_u32 s57, s36, 0x2a000\n\t" \
        "s_min_u32 s57, s57, 0x925800\n\t" \
        "s_add_u32 m0, s38, 0x5d00\n\t" \
        "s_nop 0\n\t" \
        "buffer_load_dwordx4 v13, s[16:19], s57 offen sc1 lds\n\t" \
        "v_cndmask_b32 v4, v163, v162, s[62:63]\n\t" \
        "v_cmp_lt_u32 vcc, 15, v2\n\t" \
        "s_nop 1\n\t" \
        "v_cndmask_b32 v5, v162, v163, vcc\n\t" \
        "s_mul_i32 s40, s35, 0xc00\n\t" \
        "s_add_u32 s39, s40, 0xba40\n\t" \
        "s_add_u32 s57, s36, 0x34800\n\t" \
        "s_min_u32 s57, s57, 0x925800\n\t" \
        "s_add_u32 m0, s38, 0x6c80\n\t" \
        "s_nop 0\n\t" \
        "buffer_load_dwordx4 v13, s[16:19], s57 offen sc1 lds\n\t" \
        "s_mov_b32 s42, 0xffff0000\n\t" \
        "s_mov_b32 s43, 0xc0\n\t" \
        "s_mov_b32 s44, 0xf80\n\t" \
        "s_mov_b32 s45, 0x435f435f\n\t" \
        "v_add_u32 v162, s32, v18\n\t" \
        "v_add_u32 v163, s33, v19\n\t" \
        "s_add_u32 s57, s36, 0x3f000\n\t" \
        "s_min_u32 s57, s57, 0x925800\n\t" \
        "s_add_u32 m0, s38, 0x7c00\n\t" \
        "s_nop 0\n\t" \
        "buffer_load_dwordx4 v13, s[16:19], s57 offen sc1 lds\n\t" \
        "v_add_u32 v162, -1, v162\n\t" \
        "v_add_u32 v163, -1, v163\n\t" \
        "v_cndmask_b32 v161, 0, 1.0, s[62:63]\n\t" \
        "v_cvt_f32_i32 v55, v162\n\t" \
        "v_cvt_f32_i32 v62, v163\n\t" \
        "v_add_f32 v56, v55, v161\n\t" \
        "s_add_u32 s57, s36, 0x49800\n\t" \
        "s_min_u32 s57, s57, 0x925800\n\t" \
        "s_add_u32 m0, s38, 0x8b80\n\t" \
        "s_nop 0\n\t" \
        "buffer_load_dwordx4 v13, s[16:19], s57 offen sc1 lds\n\t" \
        "v_add_f32 v59, v62, v161\n\t" \
        "v_add_f32 v57, 1.0, v55\n\t" \
        "v_add_f32 v62, 2.0, v62\n\t" \
        "v_add_f32 v58, 2.0, v55\n\t" \
        "v_add_f32 v61, 1.0, v59\n\t" \
        "v_fma_f32 v60, v161, -2.0, v62\n\t" \
        "s_add_u32 s57, s36, 0x54000\n\t" \
        "s_min_u32 s57, s57, 0x925800\n\t" \
        "s_add_u32 m0, s38, 0x9b00\n\t" \
        "s_nop 0\n\t" \
        "buffer_load_dwordx4 v13, s[16:19], s57 offen sc1 lds\n\t" \
        "v_bfe_u32 v160, v2, 4, 1\n\t" \
        "v_lshrrev_b32 v162, 2, v19\n\t" \
        "v_and_b32 v163, 1, v19\n\t" \
        "v_lshlrev_b32 v162, 5, v162\n\t" \
        "v_lshl_add_u32 v162, v163, 4, v162\n\t" \
        "v_and_b32 v163, 2, v19\n\t" \
        "s_add_u32 s57, s36, 0x5e800\n\t" \
        "s_min_u32 s57, s57, 0x925800\n\t" \
        "s_add_u32 m0, s38, 0xaa80\n\t" \
        "s_nop 0\n\t" \
        "buffer_load_dwordx4 v13, s[16:19], s57 offen sc1 lds\n\t" \
        "v_lshl_add_u32 v162, v160, 3, v162\n\t" \
        "v_lshlrev_b32 v161, 2, v2\n\t" \
        "v_lshl_add_u32 v162, v163, 1, v162\n\t" \
        "v_and_b32 v161, 0x80, v161\n\t" \
        "s_mul_i32 s55, s35, 2560\n\t" \
        "s_add_u32 s55, s55, 109120\n\t" \
        "s_mov_b32 s57, s40\n\t" \
        "s_mov_b32 m0, s39\n\t" \
        "s_nop 0\n\t" \
        "buffer_load_dwordx4 v3, s[24:27], s57 offen offset:0 lds\n\t" \
        "v_add3_u32 v63, v162, v161, s55\n\t" \
        "s_mul_i32 s56, s35, 1280\n\t" \
        "s_add_u32 s56, s56, 119360\n\t" \
        "v_lshl_add_u32 v10, v19, 3, s56\n\t" \
        "v_lshl_add_u32 v162, v160, 2, v161\n\t" \
        "v_add_u32 v64, v10, v162\n\t" \
        "buffer_load_dwordx4 v4, s[24:27], s57 offen offset:1024 lds\n\t" \
        "s_mul_i32 s55, s32, 0xf80\n\t" \
        "s_mul_i32 s56, s33, 0xc0\n\t" \
        "s_add_u32 s55, s55, s56\n\t" \
        "s_sub_u32 s55, 0x1f40, s55\n\t" \
        "v_mov_b32 v65, s55\n\t" \
        "v_mov_b32 v29, 0x3f80\n\t" \
        "buffer_load_dwordx4 v5, s[24:27], s57 offen offset:2048 lds\n\t" \
        "s_sub_i32 s55, s32, 2\n\t" \
        "s_sub_i32 s56, s33, 2\n\t" \
        "s_max_i32 s55, s55, 0\n\t" \
        "s_max_i32 s56, s56, 0\n\t" \
        "v_cvt_f32_u32 v162, s55\n\t" \
        "v_cvt_f32_u32 v163, s56\n\t" \
        "s_add_u32 s55, s32, 9\n\t" \
        "s_add_u32 s56, s33, 17\n\t" \
        "v_cvt_pk_bf16_f32 v14, v162, v163\n\t" \
        "v_cvt_f32_u32 v162, s55\n\t" \
        "v_cvt_f32_u32 v163, s56\n\t" \
        "v_cvt_pk_bf16_f32 v15, v162, v163\n\t" \
        "s_waitcnt vmcnt(15)\n\t" \
        "s_add_u32 s57, s40, 0x3000\n\t" \
        "s_add_u32 m0, s39, 0x3000\n\t" \
        "s_nop 0\n\t" \
        "buffer_load_dwordx4 v3, s[24:27], s57 offen offset:0 lds\n\t" \
        "v_cndmask_b32 v140, v40, v41, s[62:63]\n\t" \
        "v_lshlrev_b32 v141, 16, v140\n\t" \
        "v_and_b32 v142, s42, v140\n\t" \
        "v_add_f32 v141, v55, v141\n\t" \
        "v_add_f32 v142, v59, v142\n\t" \
        "v_cvt_pk_bf16_f32 v50, v141, v142\n\t" \
        "v_pk_max_i16 v50, v50, 0\n\t" \
        "v_pk_min_i16 v50, v50, s45\n\t" \
        "v_lshlrev_b32 v141, 16, v50\n\t" \
        "v_and_b32 v142, s42, v50\n\t" \
        "v_pk_min_i16 v66, v50, v50\n\t" \
        "v_fract_f32 v143, v141\n\t" \
        "v_fract_f32 v144, v142\n\t" \
        "v_cvt_u32_f32 v145, v141\n\t" \
        "v_cvt_u32_f32 v146, v142\n\t" \
        "v_cmp_neq_f32 s[10:11], 0, v143\n\t" \
        "v_cmp_neq_f32 vcc, 0, v144\n\t" \
        "v_add_u32 v146, 2, v146\n\t" \
        "v_pk_max_i16 v67, v50, v50\n\t" \
        "v_mad_u32_u24 v148, v146, s43, v65\n\t" \
        "v_bfe_u32 v147, v146, 2, 1\n\t" \
        "v_mad_u32_u24 v148, v145, s44, v148\n\t" \
        "v_add_u32 v146, 1, v146\n\t" \
        "v_lshl_add_u32 v149, v147, 5, v148\n\t" \
        "v_bfe_u32 v147, v146, 2, 1\n\t" \
        "v_add_u32 v148, s43, v148\n\t" \
        "v_add_u32 v150, s44, v149\n\t" \
        "v_lshl_add_u32 v148, v147, 5, v148\n\t" \
        "v_cndmask_b32 v150, v149, v150, s[10:11]\n\t" \
        "v_add_f32 v144, v144, v143\n\t" \
        "v_cndmask_b32 v150, v150, v148, vcc\n\t" \
        "v_sub_f32 v152, 1.0, v144\n\t" \
        "buffer_load_dwordx4 v4, s[24:27], s57 offen offset:1024 lds\n\t" \
        "s_and_b64 s[4:5], s[10:11], vcc\n\t" \
        "ds_write2st64_b32 v63, v149, v150 offset0:0 offset1:5\n\t" \
        "v_cvt_pk_bf16_f32 v151, v152, v144\n\t" \
        "v_cndmask_b32 v151, v151, v29, s[4:5]\n\t" \
        "ds_write_b32 v64, v151 offset:0\n\t" \
        "v_cndmask_b32 v140, v42, v43, s[62:63]\n\t" \
        "v_lshlrev_b32 v141, 16, v140\n\t" \
        "v_and_b32 v142, s42, v140\n\t" \
        "v_add_f32 v141, v56, v141\n\t" \
        "v_add_f32 v142, v60, v142\n\t" \
        "v_cvt_pk_bf16_f32 v51, v141, v142\n\t" \
        "v_pk_max_i16 v51, v51, 0\n\t" \
        "v_pk_min_i16 v51, v51, s45\n\t" \
        "v_lshlrev_b32 v141, 16, v51\n\t" \
        "v_and_b32 v142, s42, v51\n\t" \
        "v_pk_min_i16 v66, v66, v51\n\t" \
        "v_fract_f32 v143, v141\n\t" \
        "v_fract_f32 v144, v142\n\t" \
        "v_cvt_u32_f32 v145, v141\n\t" \
        "v_cvt_u32_f32 v146, v142\n\t" \
        "v_cmp_neq_f32 s[10:11], 0, v143\n\t" \
        "v_cmp_neq_f32 vcc, 0, v144\n\t" \
        "v_add_u32 v146, 2, v146\n\t" \
        "v_pk_max_i16 v67, v67, v51\n\t" \
        "v_mad_u32_u24 v148, v146, s43, v65\n\t" \
        "v_bfe_u32 v147, v146, 2, 1\n\t" \
        "v_mad_u32_u24 v148, v145, s44, v148\n\t" \
        "v_add_u32 v146, 1, v146\n\t" \
        "v_lshl_add_u32 v149, v147, 5, v148\n\t" \
        "v_bfe_u32 v147, v146, 2, 1\n\t" \
        "v_add_u32 v148, s43, v148\n\t" \
        "v_add_u32 v150, s44, v149\n\t" \
        "buffer_load_dwordx4 v5, s[24:27], s57 offen offset:2048 lds\n\t" \
        "v_lshl_add_u32 v148, v147, 5, v148\n\t" \
        "v_cndmask_b32 v150, v149, v150, s[10:11]\n\t" \
        "v_add_f32 v144, v144, v143\n\t" \
        "v_cndmask_b32 v150, v150, v148, vcc\n\t" \
        "v_sub_f32 v152, 1.0, v144\n\t" \
        "s_and_b64 s[6:7], s[10:11], vcc\n\t" \
        "ds_write2st64_b32 v63, v149, v150 offset0:1 offset1:6\n\t" \
        "v_cvt_pk_bf16_f32 v151, v152, v144\n\t" \
        "v_cndmask_b32 v151, v151, v29, s[6:7]\n\t" \
        "ds_write_b32 v64, v151 offset:256\n\t" \
        "v_cndmask_b32 v140, v44, v45, s[62:63]\n\t" \
        "v_lshlrev_b32 v141, 16, v140\n\t" \
        "v_and_b32 v142, s42, v140\n\t" \
        "v_add_f32 v141, v57, v141\n\t" \
        "v_add_f32 v142, v61, v142\n\t" \
        "v_cvt_pk_bf16_f32 v52, v141, v142\n\t" \
        "v_pk_max_i16 v52, v52, 0\n\t" \
        "v_pk_min_i16 v52, v52, s45\n\t" \
        "v_lshlrev_b32 v141, 16, v52\n\t" \
        "v_and_b32 v142, s42, v52\n\t" \
        "v_pk_min_i16 v66, v66, v52\n\t" \
        "v_fract_f32 v143, v141\n\t" \
        "v_fract_f32 v144, v142\n\t" \
        "v_cvt_u32_f32 v145, v141\n\t" \
        "v_cvt_u32_f32 v146, v142\n\t" \
        "v_cmp_neq_f32 s[10:11], 0, v143\n\t" \
        "v_cmp_neq_f32 vcc, 0, v144\n\t" \
        "v_add_u32 v146, 2, v146\n\t" \
        "v_pk_max_i16 v67, v67, v52\n\t" \
        "v_mad_u32_u24 v148, v146, s43, v65\n\t" \
        "v_bfe_u32 v147, v146, 2, 1\n\t" \
        "v_mad_u32_u24 v148, v145, s44, v148\n\t" \
        "s_add_u32 s57, s40, 0x6000\n\t" \
        "s_add_u32 m0, s39, 0x6000\n\t" \
        "s_nop 0\n\t" \
        "buffer_load_dwordx4 v3, s[24:27], s57 offen offset:0 lds\n\t" \
        "v_add_u32 v146, 1, v146\n\t" \
        "v_lshl_add_u32 v149, v147, 5, v148\n\t" \
        "v_bfe_u32 v147, v146, 2, 1\n\t" \
        "v_add_u32 v148, s43, v148\n\t" \
        "v_add_u32 v150, s44, v149\n\t" \
        "v_lshl_add_u32 v148, v147, 5, v148\n\t" \
        "v_cndmask_b32 v150, v149, v150, s[10:11]\n\t" \
        "v_add_f32 v144, v144, v143\n\t" \
        "v_cndmask_b32 v150, v150, v148, vcc\n\t" \
        "v_sub_f32 v152, 1.0, v144\n\t" \
        "s_and_b64 s[8:9], s[10:11], vcc\n\t" \
        "ds_write2st64_b32 v63, v149, v150 offset0:2 offset1:7\n\t" \
        "v_cvt_pk_bf16_f32 v151, v152, v144\n\t" \
        "v_cndmask_b32 v151, v151, v29, s[8:9]\n\t" \
        "ds_write_b32 v64, v151 offset:512\n\t" \
        "v_cndmask_b32 v140, v46, v47, s[62:63]\n\t" \
        "v_lshlrev_b32 v141, 16, v140\n\t" \
        "v_and_b32 v142, s42, v140\n\t" \
        "v_add_f32 v141, v58, v141\n\t" \
        "v_add_f32 v142, v59, v142\n\t" \
        "v_cvt_pk_bf16_f32 v53, v141, v142\n\t" \
        "v_pk_max_i16 v53, v53, 0\n\t" \
        "v_pk_min_i16 v53, v53, s45\n\t" \
        "v_lshlrev_b32 v141, 16, v53\n\t" \
        "v_and_b32 v142, s42, v53\n\t" \
        "v_pk_min_i16 v66, v66, v53\n\t" \
        "v_fract_f32 v143, v141\n\t" \
        "v_fract_f32 v144, v142\n\t" \
        "v_cvt_u32_f32 v145, v141\n\t" \
        "v_cvt_u32_f32 v146, v142\n\t" \
        "v_cmp_neq_f32 s[10:11], 0, v143\n\t" \
        "v_cmp_neq_f32 vcc, 0, v144\n\t" \
        "buffer_load_dwordx4 v4, s[24:27], s57 offen offset:1024 lds\n\t" \
        "v_add_u32 v146, 2, v146\n\t" \
        "v_pk_max_i16 v67, v67, v53\n\t" \
        "v_mad_u32_u24 v148, v146, s43, v65\n\t" \
        "v_bfe_u32 v147, v146, 2, 1\n\t" \
        "v_mad_u32_u24 v148, v145, s44, v148\n\t" \
        "v_add_u32 v146, 1, v146\n\t" \
        "v_lshl_add_u32 v149, v147, 5, v148\n\t" \
        "v_bfe_u32 v147, v146, 2, 1\n\t" \
        "v_add_u32 v148, s43, v148\n\t" \
        "v_add_u32 v150, s44, v149\n\t" \
        "v_lshl_add_u32 v148, v147, 5, v148\n\t" \
        "v_cndmask_b32 v150, v149, v150, s[10:11]\n\t" \
        "v_add_f32 v144, v144, v143\n\t" \
        "v_cndmask_b32 v150, v150, v148, vcc\n\t" \
        "v_sub_f32 v152, 1.0, v144\n\t" \
        "s_and_b64 s[12:13], s[10:11], vcc\n\t" \
        "ds_write2st64_b32 v63, v149, v150 offset0:3 offset1:8\n\t" \
        "v_cvt_pk_bf16_f32 v151, v152, v144\n\t" \
        "v_cndmask_b32 v151, v151, v29, s[12:13]\n\t" \
        "ds_write_b32 v64, v151 offset:768\n\t" \
        "v_lshlrev_b32 v141, 16, v48\n\t" \
        "v_and_b32 v142, s42, v48\n\t" \
        "v_add_f32 v141, v58, v141\n\t" \
        "v_add_f32 v142, v62, v142\n\t" \
        "v_cvt_pk_bf16_f32 v54, v141, v142\n\t" \
        "v_pk_max_i16 v54, v54, 0\n\t" \
        "v_pk_min_i16 v54, v54, s45\n\t" \
        "v_lshlrev_b32 v141, 16, v54\n\t" \
        "v_and_b32 v142, s42, v54\n\t" \
        "v_pk_min_i16 v66, v66, v54\n\t" \
        "v_fract_f32 v143, v141\n\t" \
        "v_fract_f32 v144, v142\n\t" \
        "buffer_load_dwordx4 v5, s[24:27], s57 offen offset:2048 lds\n\t" \
        "v_cvt_u32_f32 v145, v141\n\t" \
        "v_cvt_u32_f32 v146, v142\n\t" \
        "v_cmp_neq_f32 s[10:11], 0, v143\n\t" \
        "v_cmp_neq_f32 vcc, 0, v144\n\t" \
        "v_add_u32 v146, 2, v146\n\t" \
        "v_pk_max_i16 v67, v67, v54\n\t" \
        "v_mad_u32_u24 v148, v146, s43, v65\n\t" \
        "v_bfe_u32 v147, v146, 2, 1\n\t" \
        "v_mad_u32_u24 v148, v145, s44, v148\n\t" \
        "v_add_u32 v146, 1, v146\n\t" \
        "v_lshl_add_u32 v149, v147, 5, v148\n\t" \
        "v_bfe_u32 v147, v146, 2, 1\n\t" \
        "v_add_u32 v148, s43, v148\n\t" \
        "v_add_u32 v150, s44, v149\n\t" \
        "v_lshl_add_u32 v148, v147, 5, v148\n\t" \
        "v_cndmask_b32 v150, v149, v150, s[10:11]\n\t" \
        "v_add_f32 v144, v144, v143\n\t" \
        "v_cndmask_b32 v150, v150, v148, vcc\n\t" \
        "v_sub_f32 v152, 1.0, v144\n\t" \
        "s_and_b64 s[14:15], s[10:11], vcc\n\t" \
        "ds_write2st64_b32 v63, v149, v150 offset0:4 offset1:9\n\t" \
        "v_cvt_pk_bf16_f32 v151, v152, v144\n\t" \
        "v_cndmask_b32 v151, v151, v29, s[14:15]\n\t" \
        "ds_write_b32 v64, v151 offset:1024\n\t" \
        "v_pk_max_i16 v140, v66, v14\n\t" \
        "v_pk_min_i16 v141, v67, v15\n\t" \
        "v_cmp_ne_u32 s[10:11], v140, v66\n\t" \
        "v_cmp_ne_u32 vcc, v141, v67\n\t" \
        "s_nop 0\n\t" \
        "s_or_b64 s[10:11], s[10:11], vcc\n\t" \
        "s_cselect_b32 s54, 1, 0\n\t" \
        "s_or_b64 s[10:11], s[4:5], s[6:7]\n\t" \
        "s_or_b64 s[56:57], s[8:9], s[12:13]\n\t" \
        "s_or_b64 s[10:11], s[10:11], s[56:57]\n\t" \
        "s_or_b64 s[10:11], s[10:11], s[14:15]\n\t" \
        "s_cbranch_scc1 .LH_ent%=\n\t" \
        "s_lshl_b32 s55, s35, 2\n\t" \
        "s_cmp_lg_u32 s54, 0\n\t" \
        "v_mov_b32 v162, s55\n\t" \
        "s_cselect_b32 s55, 0x10000, 0\n\t" \
        "v_mov_b32 v163, s55\n\t" \
        "ds_write_b32 v162, v163\n\t" \
        "v_bfe_u32 v162, v2, 2, 1\n\t" \
        "v_lshrrev_b32 v163, 3, v2\n\t" \
        "s_mul_i32 s55, s35, 2560\n\t" \
        "s_add_u32 s55, s55, 109120\n\t" \
        "v_mul_u32_u24 v162, 0x500, v162\n\t" \
        "v_lshlrev_b32 v163, 4, v163\n\t" \
        "v_add3_u32 v9, v162, v163, s55\n\t" \
        "v_lshrrev_b32 v164, 4, v2\n\t" \
        "v_bfe_u32 v162, v2, 2, 2\n\t" \
        "v_and_b32 v163, 3, v2\n\t" \
        "v_lshl_add_u32 v162, v164, 2, v162\n\t" \
        "v_lshlrev_b32 v11, 3, v163\n\t" \
        "v_and_b32 v165, 1, v164\n\t" \
        "v_mul_u32_u24 v162, 0xc0, v162\n\t" \
        "v_xor_b32 v12, 32, v11\n\t" \
        "v_lshl_add_u32 v162, v165, 5, v162\n\t" \
        "v_lshlrev_b32 v163, 6, v165\n\t" \
        "s_mov_b32 s55, 0xba40\n\t" \
        "v_add3_u32 v6, v162, v11, s55\n\t" \
        "v_sub_u32 v7, v6, v163\n\t" \
        "v_mul_u32_u24 v162, 0xc0, v19\n\t" \
        "v_lshrrev_b32 v164, 1, v164\n\t" \
        "v_lshl_add_u32 v162, v165, 5, v162\n\t" \
        "v_lshl_add_u32 v8, v164, 4, v162\n\t" \
        "s_add_u32 s55, s32, s35\n\t" \
        "s_mul_i32 s56, s33, 0xc0\n\t" \
        "s_mul_i32 s55, s55, 0xa800\n\t" \
        "s_add_u32 s59, s55, s56\n\t" \
        "s_add_u32 s60, s59, 0x2a000\n\t" \
        "v_lshrrev_b32 v166, 4, v2\n\t" \
        "v_bfe_u32 v168, v2, 2, 2\n\t" \
        "v_and_b32 v167, 3, v2\n\t" \
        "v_cmp_eq_u32 vcc, v166, v168\n\t" \
        "v_lshlrev_b32 v167, v167, 1\n\t" \
        "s_nop 0\n\t" \
        "v_cndmask_b32 v167, 0, v167, vcc\n\t" \
        "v_bfe_i32 v25, v167, 0, 1\n\t" \
        "v_bfe_i32 v26, v167, 1, 1\n\t" \
        "v_bfe_i32 v27, v167, 2, 1\n\t" \
        "v_bfe_i32 v28, v167, 3, 1\n\t" \
        "v_mov_b32 v163, 0\n\t" \
        "s_waitcnt vmcnt(6) lgkmcnt(0)\n\t" \
        "s_barrier\n\t" \
        "ds_read_b128 v[152:155], v163\n\t" \
        "s_waitcnt lgkmcnt(0)\n\t" \
        "v_add_u32 v152, v152, v154\n\t" \
        "v_add_u32 v153, v153, v155\n\t" \
        "v_max_u32 v152, v152, v153\n\t" \
        "s_nop 0\n\t" \
        "v_readfirstlane_b32 s55, v152\n\t" \
        "s_nop 0\n\t" \
        "s_cmp_gt_u32 s55, 56\n\t" \
        "s_cbranch_scc1 .Lfail%=\n\t" \
        ".LH_join%=:\n\t" \
        "ds_read_b128 v[170:173], v9 offset:0\n\t" \
        "ds_read_b64 v[150:151], v10 offset:0\n\t" \
        "ds_read_b64_tr_b16 a[0:1], v6 offset:0\n\t" \
        "ds_read_b64_tr_b16 a[2:3], v6 offset:3072\n\t" \
        "ds_read_b64_tr_b16 a[4:5], v7 offset:32\n\t" \
        "ds_read_b64_tr_b16 a[6:7], v7 offset:3104\n\t" \
        "ds_read_b64_tr_b16 a[8:9], v6 offset:64\n\t" \
        "ds_read_b64_tr_b16 a[10:11], v6 offset:3136\n\t" \
        "s_waitcnt lgkmcnt(7)\n\t" \
        "v_xor_b32 v134, v170, v11\n\t" \
        "v_xor_b32 v135, v171, v11\n\t" \
        "v_xor_b32 v136, v172, v11\n\t" \
        "v_xor_b32 v138, v170, v12\n\t" \
        "v_xor_b32 v137, v173, v11\n\t" \
        "v_xor_b32 v139, v171, v12\n\t" \
        "v_xor_b32 v140, v172, v12\n\t" \
        "v_xor_b32 v141, v173, v12\n\t" \
        "ds_read_b128 v[174:177], v9 offset:128\n\t" \
        "ds_read_b64_tr_b16 a[96:97], v134 offset:0\n\t" \
        "ds_read_b64_tr_b16 a[98:99], v135 offset:0\n\t" \
        "ds_read_b64_tr_b16 a[100:101], v138 offset:0\n\t" \
        "s_waitcnt lgkmcnt(10)\n\t" \
        "v_and_b32 v154, v150, v25\n\t" \
        "ds_read_b64_tr_b16 a[102:103], v139 offset:0\n\t" \
        "ds_read_b64_tr_b16 a[104:105], v136 offset:0\n\t" \
        "v_and_b32 v155, v150, v26\n\t" \
        "ds_read_b64_tr_b16 a[106:107], v137 offset:0\n\t" \
        "ds_read_b64_tr_b16 a[108:109], v140 offset:0\n\t" \
        "v_and_b32 v156, v150, v27\n\t" \
        "ds_read_b64_tr_b16 a[110:111], v141 offset:0\n\t" \
        "ds_read_b64_tr_b16 a[112:113], v134 offset:64\n\t" \
        "v_and_b32 v157, v150, v28\n\t" \
        "ds_read_b64_tr_b16 a[114:115], v135 offset:64\n\t" \
        "ds_read_b64_tr_b16 a[116:117], v138 offset:64\n\t" \
        "ds_read_b64_tr_b16 a[118:119], v139 offset:64\n\t" \
        "ds_read_b64_tr_b16 a[120:121], v136 offset:64\n\t" \
        "ds_read_b64_tr_b16 a[122:123], v137 offset:64\n\t" \
        "ds_read_b64_tr_b16 a[124:125], v140 offset:64\n\t" \
        "v_and_b32 v158, v151, v25\n\t" \
        "ds_read_b64_tr_b16 a[126:127], v141 offset:64\n\t" \
        "ds_read_b64_tr_b16 a[128:129], v134 offset:128\n\t" \
        "v_and_b32 v159, v151, v26\n\t" \
        "ds_read_b64_tr_b16 a[130:131], v135 offset:128\n\t" \
        "ds_read_b64_tr_b16 a[132:133], v138 offset:128\n\t" \
        "v_and_b32 v160, v151, v27\n\t" \
        "ds_read_b64_tr_b16 a[134:135], v139 offset:128\n\t" \
        "ds_read_b64_tr_b16 a[136:137], v136 offset:128\n\t" \
        "s_waitcnt lgkmcnt(15)\n\t" \
        "v_mfma_f32_16x16x32_bf16 v[102:105], a[96:99], v[154:157], 0\n\t" \
        "ds_read_b64_tr_b16 a[138:139], v137 offset:128\n\t" \
        "ds_read_b64_tr_b16 a[140:141], v140 offset:128\n\t" \
        "v_and_b32 v161, v151, v28\n\t" \
        "v_xor_b32 v142, v174, v11\n\t" \
        "v_xor_b32 v143, v175, v11\n\t" \
        "v_xor_b32 v144, v176, v11\n\t" \
        "v_xor_b32 v146, v174, v12\n\t" \
        "v_xor_b32 v145, v177, v11\n\t" \
        "v_xor_b32 v147, v175, v12\n\t" \
        "v_mfma_f32_16x16x32_bf16 v[106:109], a[100:103], v[154:157], 0\n\t" \
        "s_add_u32 m0, s39, 36864\n\t" \
        "s_add_u32 s57, s40, 36864\n\t" \
        "buffer_load_dwordx4 v3, s[24:27], s57 offen offset:0 lds\n\t" \
        "ds_read_b64_tr_b16 a[142:143], v141 offset:128\n\t" \
        "v_mfma_f32_16x16x32_bf16 v[110:113], a[104:107], v[158:161], 0\n\t" \
        "ds_read_b64 v[152:153], v10 offset:128\n\t" \
        "ds_read_b64_tr_b16 a[12:13], v7 offset:96\n\t" \
        "v_xor_b32 v148, v176, v12\n\t" \
        "v_xor_b32 v149, v177, v12\n\t" \
        "s_waitcnt lgkmcnt(15)\n\t" \
        "v_mfma_f32_16x16x32_bf16 v[114:117], a[108:111], v[158:161], 0\n\t" \
        "ds_read_b64_tr_b16 a[14:15], v7 offset:3168\n\t" \
        "v_mfma_f32_16x16x32_bf16 v[118:121], a[112:115], v[154:157], 0\n\t" \
        "ds_read_b64_tr_b16 a[16:17], v6 offset:128\n\t" \
        "v_cvt_pk_bf16_f32 v78, v102, v103\n\t" \
        "v_cvt_pk_bf16_f32 v79, v104, v105\n\t" \
        "s_waitcnt lgkmcnt(15)\n\t" \
        "v_mfma_f32_16x16x32_bf16 v[122:125], a[116:119], v[154:157], 0\n\t" \
        "ds_read_b64_tr_b16 a[18:19], v6 offset:3200\n\t" \
        "ds_read_b64_tr_b16 a[20:21], v7 offset:160\n\t" \
        "v_cvt_pk_bf16_f32 v80, v106, v107\n\t" \
        "v_cvt_pk_bf16_f32 v81, v108, v109\n\t" \
        "s_waitcnt lgkmcnt(15)\n\t" \
        "v_mfma_f32_16x16x32_bf16 v[126:129], a[120:123], v[158:161], 0\n\t" \
        "buffer_load_dwordx4 v4, s[24:27], s57 offen offset:1024 lds\n\t" \
        "v_cvt_pk_bf16_f32 v82, v110, v111\n\t" \
        "s_waitcnt lgkmcnt(14)\n\t" \
        "v_mfma_f32_16x16x32_bf16 v[130:133], a[124:127], v[158:161], 0\n\t" \
        "ds_read_b64_tr_b16 a[22:23], v7 offset:3232\n\t" \
        "v_cvt_pk_bf16_f32 v83, v112, v113\n\t" \
        "v_cvt_pk_bf16_f32 v84, v114, v115\n\t" \
        "s_waitcnt lgkmcnt(13)\n\t" \
        "v_mfma_f32_16x16x32_bf16 v[102:105], a[128:131], v[154:157], 0\n\t" \
        "ds_read_b64_tr_b16 a[96:97], v142 offset:0\n\t" \
        "v_cvt_pk_bf16_f32 v85, v116, v117\n\t" \
        "s_waitcnt lgkmcnt(12)\n\t" \
        "v_mfma_f32_16x16x32_bf16 v[106:109], a[132:135], v[154:157], 0\n\t" \
        "ds_read_b64_tr_b16 a[98:99], v143 offset:0\n\t" \
        "ds_read_b64_tr_b16 a[100:101], v146 offset:0\n\t" \
        "s_waitcnt lgkmcnt(12)\n\t" \
        "v_mfma_f32_16x16x32_bf16 v[110:113], a[136:139], v[158:161], 0\n\t" \
        "ds_read_b64_tr_b16 a[102:103], v147 offset:0\n\t" \
        "v_cvt_pk_bf16_f32 v86, v118, v119\n\t" \
        "s_waitcnt lgkmcnt(11)\n\t" \
        "v_mfma_f32_16x16x32_bf16 v[114:117], a[140:143], v[158:161], 0\n\t" \
        "buffer_load_dwordx4 v5, s[24:27], s57 offen offset:2048 lds\n\t" \
        "v_mfma_f32_16x16x32_bf16 v[30:33], a[0:3], v[78:81], 0\n\t" \
        "ds_read_b64_tr_b16 a[104:105], v144 offset:0\n\t" \
        "s_waitcnt lgkmcnt(10)\n\t" \
        "v_and_b32 v162, v152, v25\n\t" \
        "v_mfma_f32_16x16x32_bf16 v[54:57], a[0:3], v[82:85], 0\n\t" \
        "ds_read_b64_tr_b16 a[106:107], v145 offset:0\n\t" \
        "ds_read_b64_tr_b16 a[108:109], v148 offset:0\n\t" \
        "v_mfma_f32_16x16x32_bf16 v[34:37], a[4:7], v[78:81], 0\n\t" \
        "ds_read_b64_tr_b16 a[110:111], v149 offset:0\n\t" \
        "v_and_b32 v163, v152, v26\n\t" \
        "v_mfma_f32_16x16x32_bf16 v[58:61], a[4:7], v[82:85], 0\n\t" \
        "ds_read_b64_tr_b16 a[24:25], v6 offset:6144\n\t" \
        "v_and_b32 v164, v152, v27\n\t" \
        "v_mfma_f32_16x16x32_bf16 v[38:41], a[8:11], v[78:81], 0\n\t" \
        "s_add_u32 m0, s39, 49152\n\t" \
        "s_add_u32 s57, s40, 49152\n\t" \
        "buffer_load_dwordx4 v3, s[24:27], s57 offen offset:0 lds\n\t" \
        "v_mfma_f32_16x16x32_bf16 v[62:65], a[8:11], v[82:85], 0\n\t" \
        "ds_read_b64_tr_b16 a[26:27], v6 offset:9216\n\t" \
        "ds_read_b64_tr_b16 a[28:29], v7 offset:6176\n\t" \
        "v_and_b32 v165, v152, v28\n\t" \
        "v_and_b32 v166, v153, v25\n\t" \
        "s_waitcnt lgkmcnt(11)\n\t" \
        "v_mfma_f32_16x16x32_bf16 v[42:45], a[12:15], v[78:81], 0\n\t" \
        "ds_read_b64_tr_b16 a[30:31], v7 offset:9248\n\t" \
        "v_cvt_pk_bf16_f32 v87, v120, v121\n\t" \
        "v_mfma_f32_16x16x32_bf16 v[66:69], a[12:15], v[82:85], 0\n\t" \
        "ds_read_b64_tr_b16 a[32:33], v6 offset:6208\n\t" \
        "ds_read_b64_tr_b16 a[34:35], v6 offset:9280\n\t" \
        "v_cvt_pk_bf16_f32 v88, v122, v123\n\t" \
        "v_and_b32 v167, v153, v26\n\t" \
        "v_mfma_f32_16x16x32_bf16 v[46:49], a[16:19], v[78:81], 0\n\t" \
        "ds_read_b64_tr_b16 a[36:37], v7 offset:6240\n\t" \
        "v_cvt_pk_bf16_f32 v89, v124, v125\n\t" \
        "v_cvt_pk_bf16_f32 v90, v126, v127\n\t" \
        "v_mfma_f32_16x16x32_bf16 v[70:73], a[16:19], v[82:85], 0\n\t" \
        "buffer_load_dwordx4 v4, s[24:27], s57 offen offset:1024 lds\n\t" \
        "v_mfma_f32_16x16x32_bf16 v[50:53], a[20:23], v[78:81], 0\n\t" \
        "ds_read_b64_tr_b16 a[38:39], v7 offset:9312\n\t" \
        "v_and_b32 v168, v153, v27\n\t" \
        "v_mfma_f32_16x16x32_bf16 v[74:77], a[20:23], v[82:85], 0\n\t" \
        "ds_read_b64_tr_b16 a[40:41], v6 offset:6272\n\t" \
        "ds_read_b64_tr_b16 a[42:43], v6 offset:9344\n\t" \
        "v_cvt_pk_bf16_f32 v91, v128, v129\n\t" \
        "v_cvt_pk_bf16_f32 v92, v130, v131\n\t" \
        "s_waitcnt lgkmcnt(11)\n\t" \
        "v_mfma_f32_16x16x32_bf16 v[118:121], a[96:99], v[162:165], 0\n\t" \
        "s_waitcnt vmcnt(8)\n\t" \
        "s_barrier\n\t" \
        "ds_read_b64_tr_b16 a[44:45], v7 offset:6304\n\t" \
        "v_and_b32 v169, v153, v28\n\t" \
        "v_cvt_pk_bf16_f32 v93, v132, v133\n\t" \
        "v_mfma_f32_16x16x32_bf16 v[122:125], a[100:103], v[162:165], 0\n\t" \
        "ds_read_b64_tr_b16 a[46:47], v7 offset:9376\n\t" \
        "v_cvt_pk_bf16_f32 v94, v102, v103\n\t" \
        "v_mfma_f32_16x16x32_bf16 v[126:129], a[104:107], v[166:169], 0\n\t" \
        "buffer_load_dwordx4 v5, s[24:27], s57 offen offset:2048 lds\n\t" \
        "s_waitcnt lgkmcnt(11)\n\t" \
        "v_mfma_f32_16x16x32_bf16 v[130:133], a[108:111], v[166:169], 0\n\t" \
        "ds_read_b64_tr_b16 a[112:113], v142 offset:64\n\t" \
        "ds_read_b64_tr_b16 a[114:115], v143 offset:64\n\t" \
        "s_waitcnt lgkmcnt(11)\n\t" \
        "v_mfma_f32_16x16x32_bf16 v[30:33], a[24:27], v[86:89], v[30:33]\n\t" \
        "ds_read_b64_tr_b16 a[116:117], v146 offset:64\n\t" \
        "v_cvt_pk_bf16_f32 v95, v104, v105\n\t" \
        "v_mfma_f32_16x16x32_bf16 v[54:57], a[24:27], v[90:93], v[54:57]\n\t" \
        "ds_read_b64_tr_b16 a[118:119], v147 offset:64\n\t" \
        "ds_read_b64_tr_b16 a[120:121], v144 offset:64\n\t" \
        "s_waitcnt lgkmcnt(11)\n\t" \
        "v_mfma_f32_16x16x32_bf16 v[34:37], a[28:31], v[86:89], v[34:37]\n\t" \
        "ds_read_b64_tr_b16 a[122:123], v145 offset:64\n\t" \
        "v_cvt_pk_bf16_f32 v96, v106, v107\n\t" \
        "v_mfma_f32_16x16x32_bf16 v[58:61], a[28:31], v[90:93], v[58:61]\n\t" \
        "ds_read_b64_tr_b16 a[124:125], v148 offset:64\n\t" \
        "v_cvt_pk_bf16_f32 v97, v108, v109\n\t" \
        "v_mfma_f32_16x16x32_bf16 v[38:41], a[32:35], v[86:89], v[38:41]\n\t" \
        "ds_read_b64_tr_b16 a[126:127], v149 offset:64\n\t" \
        "ds_read_b64_tr_b16 a[48:49], v6 offset:12288\n\t" \
        "v_mfma_f32_16x16x32_bf16 v[62:65], a[32:35], v[90:93], v[62:65]\n\t" \
        "ds_read_b64_tr_b16 a[50:51], v6 offset:15360\n\t" \
        "v_cvt_pk_bf16_f32 v98, v110, v111\n\t" \
        "s_waitcnt lgkmcnt(12)\n\t" \
        "v_mfma_f32_16x16x32_bf16 v[42:45], a[36:39], v[86:89], v[42:45]\n\t" \
        "ds_read_b128 v[170:173], v9 offset:256\n\t" \
        "v_cvt_pk_bf16_f32 v99, v112, v113\n\t" \
        "v_mfma_f32_16x16x32_bf16 v[66:69], a[36:39], v[90:93], v[66:69]\n\t" \
        "ds_read_b64_tr_b16 a[52:53], v7 offset:12320\n\t" \
        "ds_read_b64_tr_b16 a[54:55], v7 offset:15392\n\t" \
        "v_mfma_f32_16x16x32_bf16 v[46:49], a[40:43], v[86:89], v[46:49]\n\t" \
        "ds_read_b64_tr_b16 a[56:57], v6 offset:12352\n\t" \
        "v_cvt_pk_bf16_f32 v100, v114, v115\n\t" \
        "v_mfma_f32_16x16x32_bf16 v[70:73], a[40:43], v[90:93], v[70:73]\n\t" \
        "ds_read_b64_tr_b16 a[58:59], v6 offset:15424\n\t" \
        "v_cvt_pk_bf16_f32 v101, v116, v117\n\t" \
        "s_waitcnt lgkmcnt(13)\n\t" \
        "v_mfma_f32_16x16x32_bf16 v[50:53], a[44:47], v[86:89], v[50:53]\n\t" \
        "ds_read_b64_tr_b16 a[60:61], v7 offset:12384\n\t" \
        "ds_read_b64_tr_b16 a[62:63], v7 offset:15456\n\t" \
        "v_mfma_f32_16x16x32_bf16 v[74:77], a[44:47], v[90:93], v[74:77]\n\t" \
        "ds_read_b64_tr_b16 a[64:65], v6 offset:12416\n\t" \
        "v_cvt_pk_bf16_f32 v78, v118, v119\n\t" \
        "v_mfma_f32_16x16x32_bf16 v[102:105], a[112:115], v[162:165], 0\n\t" \
        "ds_read_b64_tr_b16 a[66:67], v6 offset:15488\n\t" \
        "ds_read_b64_tr_b16 a[68:69], v7 offset:12448\n\t" \
        "s_waitcnt lgkmcnt(14)\n\t" \
        "v_mfma_f32_16x16x32_bf16 v[106:109], a[116:119], v[162:165], 0\n\t" \
        "ds_read_b64_tr_b16 a[70:71], v7 offset:15520\n\t" \
        "v_cvt_pk_bf16_f32 v79, v120, v121\n\t" \
        "v_mfma_f32_16x16x32_bf16 v[110:113], a[120:123], v[166:169], 0\n\t" \
        "ds_read_b64_tr_b16 a[128:129], v142 offset:128\n\t" \
        "v_cvt_pk_bf16_f32 v80, v122, v123\n\t" \
        "s_waitcnt lgkmcnt(13)\n\t" \
        "v_mfma_f32_16x16x32_bf16 v[114:117], a[124:127], v[166:169], 0\n\t" \
        "ds_read_b64_tr_b16 a[130:131], v143 offset:128\n\t" \
        "ds_read_b64_tr_b16 a[132:133], v146 offset:128\n\t" \
        "s_waitcnt lgkmcnt(14)\n\t" \
        "v_mfma_f32_16x16x32_bf16 v[30:33], a[48:51], v[94:97], v[30:33]\n\t" \
        "ds_read_b64_tr_b16 a[134:135], v147 offset:128\n\t" \
        "s_waitcnt lgkmcnt(14)\n\t" \
        "v_xor_b32 v134, v170, v11\n\t" \
        "v_mfma_f32_16x16x32_bf16 v[54:57], a[48:51], v[98:101], v[54:57]\n\t" \
        "ds_read_b64_tr_b16 a[136:137], v144 offset:128\n\t" \
        "v_xor_b32 v135, v171, v11\n\t" \
        "s_waitcnt lgkmcnt(13)\n\t" \
        "v_mfma_f32_16x16x32_bf16 v[34:37], a[52:55], v[94:97], v[34:37]\n\t" \
        "ds_read_b64_tr_b16 a[138:139], v145 offset:128\n\t" \
        "ds_read_b64_tr_b16 a[140:141], v148 offset:128\n\t" \
        "v_mfma_f32_16x16x32_bf16 v[58:61], a[52:55], v[98:101], v[58:61]\n\t" \
        "ds_read_b64_tr_b16 a[142:143], v149 offset:128\n\t" \
        "v_xor_b32 v136, v172, v11\n\t" \
        "s_waitcnt lgkmcnt(14)\n\t" \
        "v_mfma_f32_16x16x32_bf16 v[38:41], a[56:59], v[94:97], v[38:41]\n\t" \
        "ds_read_b64_tr_b16 a[72:73], v6 offset:18432\n\t" \
        "ds_read_b64_tr_b16 a[74:75], v6 offset:21504\n\t" \
        "v_mfma_f32_16x16x32_bf16 v[62:65], a[56:59], v[98:101], v[62:65]\n\t" \
        "ds_read_b64_tr_b16 a[76:77], v7 offset:18464\n\t" \
        "v_xor_b32 v138, v170, v12\n\t" \
        "s_waitcnt lgkmcnt(14)\n\t" \
        "v_mfma_f32_16x16x32_bf16 v[42:45], a[60:63], v[94:97], v[42:45]\n\t" \
        "ds_read_b64 v[150:151], v10 offset:256\n\t" \
        "v_xor_b32 v137, v173, v11\n\t" \
        "v_mfma_f32_16x16x32_bf16 v[66:69], a[60:63], v[98:101], v[66:69]\n\t" \
        "ds_read_b64_tr_b16 a[78:79], v7 offset:21536\n\t" \
        "ds_read_b64_tr_b16 a[80:81], v6 offset:18496\n\t" \
        "v_xor_b32 v139, v171, v12\n\t" \
        "s_waitcnt lgkmcnt(14)\n\t" \
        "v_mfma_f32_16x16x32_bf16 v[46:49], a[64:67], v[94:97], v[46:49]\n\t" \
        "ds_read_b64_tr_b16 a[82:83], v6 offset:21568\n\t" \
        "v_xor_b32 v140, v172, v12\n\t" \
        "v_mfma_f32_16x16x32_bf16 v[70:73], a[64:67], v[98:101], v[70:73]\n\t" \
        "ds_read_b64_tr_b16 a[84:85], v7 offset:18528\n\t" \
        "v_xor_b32 v141, v173, v12\n\t" \
        "v_mfma_f32_16x16x32_bf16 v[50:53], a[68:71], v[94:97], v[50:53]\n\t" \
        "ds_read_b64_tr_b16 a[86:87], v7 offset:21600\n\t" \
        "ds_read_b64_tr_b16 a[88:89], v6 offset:18560\n\t" \
        "v_cvt_pk_bf16_f32 v81, v124, v125\n\t" \
        "v_cvt_pk_bf16_f32 v82, v126, v127\n\t" \
        "v_mfma_f32_16x16x32_bf16 v[74:77], a[68:71], v[98:101], v[74:77]\n\t" \
        "ds_read_b64_tr_b16 a[90:91], v6 offset:21632\n\t" \
        "v_cvt_pk_bf16_f32 v83, v128, v129\n\t" \
        "v_cvt_pk_bf16_f32 v84, v130, v131\n\t" \
        "s_waitcnt lgkmcnt(14)\n\t" \
        "v_mfma_f32_16x16x32_bf16 v[118:121], a[128:131], v[162:165], 0\n\t" \
        "s_waitcnt vmcnt(6)\n\t" \
        "s_barrier\n\t" \
        "ds_read_b64_tr_b16 a[92:93], v7 offset:18592\n\t" \
        "ds_read_b64_tr_b16 a[94:95], v7 offset:21664\n\t" \
        "v_cvt_pk_bf16_f32 v85, v132, v133\n\t" \
        "v_mfma_f32_16x16x32_bf16 v[122:125], a[132:135], v[162:165], 0\n\t" \
        "ds_read_b64_tr_b16 a[96:97], v134 offset:0\n\t" \
        "v_cvt_pk_bf16_f32 v86, v102, v103\n\t" \
        "s_waitcnt lgkmcnt(14)\n\t" \
        "v_mfma_f32_16x16x32_bf16 v[126:129], a[136:139], v[166:169], 0\n\t" \
        "ds_read_b64_tr_b16 a[98:99], v135 offset:0\n\t" \
        "v_cvt_pk_bf16_f32 v87, v104, v105\n\t" \
        "v_mfma_f32_16x16x32_bf16 v[130:133], a[140:143], v[166:169], 0\n\t" \
        "s_add_u32 m0, s39, 0\n\t" \
        "s_add_u32 s57, s40, 61440\n\t" \
        "buffer_load_dwordx4 v3, s[24:27], s57 offen offset:0 lds\n\t" \
        "s_waitcnt lgkmcnt(12)\n\t" \
        "v_mfma_f32_16x16x32_bf16 v[30:33], a[72:75], v[78:81], v[30:33]\n\t" \
        "ds_read_b64_tr_b16 a[100:101], v138 offset:0\n\t" \
        "ds_read_b64_tr_b16 a[102:103], v139 offset:0\n\t" \
        "v_mfma_f32_16x16x32_bf16 v[54:57], a[72:75], v[82:85], v[54:57]\n\t" \
        "ds_read_b64_tr_b16 a[104:105], v136 offset:0\n\t" \
        "s_waitcnt lgkmcnt(12)\n\t" \
        "v_and_b32 v154, v150, v25\n\t" \
        "v_mfma_f32_16x16x32_bf16 v[34:37], a[76:79], v[78:81], v[34:37]\n\t" \
        "ds_read_b64_tr_b16 a[106:107], v137 offset:0\n\t" \
        "ds_read_b64_tr_b16 a[108:109], v140 offset:0\n\t" \
        "v_mfma_f32_16x16x32_bf16 v[58:61], a[76:79], v[82:85], v[58:61]\n\t" \
        "ds_read_b64_tr_b16 a[110:111], v141 offset:0\n\t" \
        "v_and_b32 v155, v150, v26\n\t" \
        "s_waitcnt lgkmcnt(13)\n\t" \
        "v_mfma_f32_16x16x32_bf16 v[38:41], a[80:83], v[78:81], v[38:41]\n\t" \
        "ds_read_b64_tr_b16 a[0:1], v6 offset:24576\n\t" \
        "v_and_b32 v156, v150, v27\n\t" \
        "v_and_b32 v157, v150, v28\n\t" \
        "v_mfma_f32_16x16x32_bf16 v[62:65], a[80:83], v[82:85], v[62:65]\n\t" \
        "buffer_load_dwordx4 v4, s[24:27], s57 offen offset:1024 lds\n\t" \
        "s_waitcnt lgkmcnt(11)\n\t" \
        "v_mfma_f32_16x16x32_bf16 v[42:45], a[84:87], v[78:81], v[42:45]\n\t" \
        "ds_read_b64_tr_b16 a[2:3], v6 offset:27648\n\t" \
        "ds_read_b64_tr_b16 a[4:5], v7 offset:24608\n\t" \
        "v_and_b32 v158, v151, v25\n\t" \
        "v_mfma_f32_16x16x32_bf16 v[66:69], a[84:87], v[82:85], v[66:69]\n\t" \
        "ds_read_b64_tr_b16 a[6:7], v7 offset:27680\n\t" \
        "v_cvt_pk_bf16_f32 v88, v106, v107\n\t" \
        "v_and_b32 v159, v151, v26\n\t" \
        "v_mfma_f32_16x16x32_bf16 v[46:49], a[88:91], v[78:81], v[46:49]\n\t" \
        "ds_read_b64_tr_b16 a[8:9], v6 offset:24640\n\t" \
        "ds_read_b64_tr_b16 a[10:11], v6 offset:27712\n\t" \
        "v_cvt_pk_bf16_f32 v89, v108, v109\n\t" \
        "v_cvt_pk_bf16_f32 v90, v110, v111\n\t" \
        "v_mfma_f32_16x16x32_bf16 v[70:73], a[88:91], v[82:85], v[70:73]\n\t" \
        "ds_read_b64_tr_b16 a[12:13], v7 offset:24672\n\t" \
        "v_and_b32 v160, v151, v27\n\t" \
        "s_waitcnt lgkmcnt(13)\n\t" \
        "v_mfma_f32_16x16x32_bf16 v[50:53], a[92:95], v[78:81], v[50:53]\n\t" \
        "ds_read_b64_tr_b16 a[14:15], v7 offset:27744\n\t" \
        "ds_read_b64_tr_b16 a[16:17], v6 offset:24704\n\t" \
        "v_cvt_pk_bf16_f32 v91, v112, v113\n\t" \
        "v_cvt_pk_bf16_f32 v92, v114, v115\n\t" \
        "v_mfma_f32_16x16x32_bf16 v[74:77], a[92:95], v[82:85], v[74:77]\n\t" \
        "buffer_load_dwordx4 v5, s[24:27], s57 offen offset:2048 lds\n\t" \
        "v_mfma_f32_16x16x32_bf16 v[102:105], a[96:99], v[154:157], 0\n\t" \
        "ds_read_b64_tr_b16 a[18:19], v6 offset:27776\n\t" \
        "v_and_b32 v161, v151, v28\n\t" \
        "v_cvt_pk_bf16_f32 v93, v116, v117\n\t" \
        "s_waitcnt lgkmcnt(11)\n\t" \
        "v_mfma_f32_16x16x32_bf16 v[106:109], a[100:103], v[154:157], 0\n\t" \
        "ds_read_b64_tr_b16 a[20:21], v7 offset:24736\n\t" \
        "v_cvt_pk_bf16_f32 v94, v118, v119\n\t" \
        "v_mfma_f32_16x16x32_bf16 v[110:113], a[104:107], v[158:161], 0\n\t" \
        "ds_read_b64_tr_b16 a[22:23], v7 offset:27808\n\t" \
        "ds_read_b64_tr_b16 a[112:113], v134 offset:64\n\t" \
        "s_waitcnt lgkmcnt(12)\n\t" \
        "v_mfma_f32_16x16x32_bf16 v[114:117], a[108:111], v[158:161], 0\n\t" \
        "ds_read_b64_tr_b16 a[114:115], v135 offset:64\n\t" \
        "v_cvt_pk_bf16_f32 v95, v120, v121\n\t" \
        "s_waitcnt lgkmcnt(12)\n\t" \
        "v_mfma_f32_16x16x32_bf16 v[30:33], a[0:3], v[86:89], v[30:33]\n\t" \
        "ds_read_b64_tr_b16 a[116:117], v138 offset:64\n\t" \
        "v_cvt_pk_bf16_f32 v96, v122, v123\n\t" \
        "v_mfma_f32_16x16x32_bf16 v[54:57], a[0:3], v[90:93], v[54:57]\n\t" \
        "ds_read_b64_tr_b16 a[118:119], v139 offset:64\n\t" \
        "ds_read_b64_tr_b16 a[120:121], v136 offset:64\n\t" \
        "s_waitcnt lgkmcnt(13)\n\t" \
        "v_mfma_f32_16x16x32_bf16 v[34:37], a[4:7], v[86:89], v[34:37]\n\t" \
        "ds_read_b64_tr_b16 a[122:123], v137 offset:64\n\t" \
        "v_cvt_pk_bf16_f32 v97, v124, v125\n\t" \
        "v_mfma_f32_16x16x32_bf16 v[58:61], a[4:7], v[90:93], v[58:61]\n\t" \
        "ds_read_b64_tr_b16 a[124:125], v140 offset:64\n\t" \
        "ds_read_b64_tr_b16 a[126:127], v141 offset:64\n\t" \
        "s_waitcnt lgkmcnt(13)\n\t" \
        "v_mfma_f32_16x16x32_bf16 v[38:41], a[8:11], v[86:89], v[38:41]\n\t" \
        "ds_read_b64_tr_b16 a[24:25], v6 offset:30720\n\t" \
        "v_cvt_pk_bf16_f32 v98, v126, v127\n\t" \
        "v_mfma_f32_16x16x32_bf16 v[62:65], a[8:11], v[90:93], v[62:65]\n\t" \
        "ds_read_b64_tr_b16 a[26:27], v6 offset:33792\n\t" \
        "ds_read_b128 v[174:177], v9 offset:384\n\t" \
        "s_waitcnt lgkmcnt(14)\n\t" \
        "v_mfma_f32_16x16x32_bf16 v[42:45], a[12:15], v[86:89], v[42:45]\n\t" \
        "ds_read_b64_tr_b16 a[28:29], v7 offset:30752\n\t" \
        "v_cvt_pk_bf16_f32 v99, v128, v129\n\t" \
        "v_mfma_f32_16x16x32_bf16 v[66:69], a[12:15], v[90:93], v[66:69]\n\t" \
        "ds_read_b64_tr_b16 a[30:31], v7 offset:33824\n\t" \
        "v_cvt_pk_bf16_f32 v100, v130, v131\n\t" \
        "s_waitcnt lgkmcnt(14)\n\t" \
        "v_mfma_f32_16x16x32_bf16 v[46:49], a[16:19], v[86:89], v[46:49]\n\t" \
        "ds_read_b64_tr_b16 a[32:33], v6 offset:30784\n\t" \
        "ds_read_b64_tr_b16 a[34:35], v6 offset:33856\n\t" \
        "v_mfma_f32_16x16x32_bf16 v[70:73], a[16:19], v[90:93], v[70:73]\n\t" \
        "ds_read_b64_tr_b16 a[36:37], v7 offset:30816\n\t" \
        "v_cvt_pk_bf16_f32 v101, v132, v133\n\t" \
        "s_waitcnt lgkmcnt(14)\n\t" \
        "v_mfma_f32_16x16x32_bf16 v[50:53], a[20:23], v[86:89], v[50:53]\n\t" \
        "ds_read_b64_tr_b16 a[38:39], v7 offset:33888\n\t" \
        "ds_read_b64_tr_b16 a[40:41], v6 offset:30848\n\t" \
        "v_mfma_f32_16x16x32_bf16 v[74:77], a[20:23], v[90:93], v[74:77]\n\t" \
        "ds_read_b64_tr_b16 a[42:43], v6 offset:33920\n\t" \
        "v_cvt_pk_bf16_f32 v78, v102, v103\n\t" \
        "v_mfma_f32_16x16x32_bf16 v[118:121], a[112:115], v[154:157], 0\n\t" \
        "s_waitcnt vmcnt(6)\n\t" \
        "s_barrier\n\t" \
        "ds_read_b64_tr_b16 a[44:45], v7 offset:30880\n\t" \
        "v_cvt_pk_bf16_f32 v79, v104, v105\n\t" \
        "s_waitcnt lgkmcnt(14)\n\t" \
        "v_mfma_f32_16x16x32_bf16 v[122:125], a[116:119], v[154:157], 0\n\t" \
        "ds_read_b64_tr_b16 a[46:47], v7 offset:33952\n\t" \
        "ds_read_b64_tr_b16 a[128:129], v134 offset:128\n\t" \
        "v_mfma_f32_16x16x32_bf16 v[126:129], a[120:123], v[158:161], 0\n\t" \
        "ds_read_b64_tr_b16 a[130:131], v135 offset:128\n\t" \
        "s_waitcnt lgkmcnt(12)\n\t" \
        "v_xor_b32 v142, v174, v11\n\t" \
        "v_mfma_f32_16x16x32_bf16 v[130:133], a[124:127], v[158:161], 0\n\t" \
        "s_add_u32 m0, s39, 12288\n\t" \
        "s_add_u32 s57, s40, 73728\n\t" \
        "buffer_load_dwordx4 v3, s[24:27], s57 offen offset:0 lds\n\t" \
        "v_mfma_f32_16x16x32_bf16 v[30:33], a[24:27], v[94:97], v[30:33]\n\t" \
        "ds_read_b64_tr_b16 a[132:133], v138 offset:128\n\t" \
        "ds_read_b64_tr_b16 a[134:135], v139 offset:128\n\t" \
        "v_mfma_f32_16x16x32_bf16 v[54:57], a[24:27], v[98:101], v[54:57]\n\t" \
        "ds_read_b64_tr_b16 a[136:137], v136 offset:128\n\t" \
        "v_xor_b32 v143, v175, v11\n\t" \
        "s_waitcnt lgkmcnt(13)\n\t" \
        "v_mfma_f32_16x16x32_bf16 v[34:37], a[28:31], v[94:97], v[34:37]\n\t" \
        "ds_read_b64_tr_b16 a[138:139], v137 offset:128\n\t" \
        "v_xor_b32 v144, v176, v11\n\t" \
        "v_mfma_f32_16x16x32_bf16 v[58:61], a[28:31], v[98:101], v[58:61]\n\t" \
        "ds_read_b64_tr_b16 a[140:141], v140 offset:128\n\t" \
        "ds_read_b64_tr_b16 a[142:143], v141 offset:128\n\t" \
        "s_waitcnt lgkmcnt(13)\n\t" \
        "v_mfma_f32_16x16x32_bf16 v[38:41], a[32:35], v[94:97], v[38:41]\n\t" \
        "ds_read_b64_tr_b16 a[48:49], v6 offset:36864\n\t" \
        "v_xor_b32 v146, v174, v12\n\t" \
        "v_mfma_f32_16x16x32_bf16 v[62:65], a[32:35], v[98:101], v[62:65]\n\t" \
        "buffer_load_dwordx4 v4, s[24:27], s57 offen offset:1024 lds\n\t" \
        "s_waitcnt lgkmcnt(11)\n\t" \
        "v_mfma_f32_16x16x32_bf16 v[42:45], a[36:39], v[94:97], v[42:45]\n\t" \
        "ds_read_b64_tr_b16 a[50:51], v6 offset:39936\n\t" \
        "ds_read_b64_tr_b16 a[52:53], v7 offset:36896\n\t" \
        "v_xor_b32 v145, v177, v11\n\t" \
        "v_xor_b32 v147, v175, v12\n\t" \
        "v_mfma_f32_16x16x32_bf16 v[66:69], a[36:39], v[98:101], v[66:69]\n\t" \
        "ds_read_b64 v[152:153], v10 offset:384\n\t" \
        "v_xor_b32 v148, v176, v12\n\t" \
        "v_xor_b32 v149, v177, v12\n\t" \
        "v_mfma_f32_16x16x32_bf16 v[46:49], a[40:43], v[94:97], v[46:49]\n\t" \
        "ds_read_b64_tr_b16 a[54:55], v7 offset:39968\n\t" \
        "ds_read_b64_tr_b16 a[56:57], v6 offset:36928\n\t" \
        "v_cvt_pk_bf16_f32 v80, v106, v107\n\t" \
        "v_mfma_f32_16x16x32_bf16 v[70:73], a[40:43], v[98:101], v[70:73]\n\t" \
        "ds_read_b64_tr_b16 a[58:59], v6 offset:40000\n\t" \
        "v_cvt_pk_bf16_f32 v81, v108, v109\n\t" \
        "v_cvt_pk_bf16_f32 v82, v110, v111\n\t" \
        "s_waitcnt lgkmcnt(13)\n\t" \
        "v_mfma_f32_16x16x32_bf16 v[50:53], a[44:47], v[94:97], v[50:53]\n\t" \
        "ds_read_b64_tr_b16 a[60:61], v7 offset:36960\n\t" \
        "v_cvt_pk_bf16_f32 v83, v112, v113\n\t" \
        "v_cvt_pk_bf16_f32 v84, v114, v115\n\t" \
        "v_mfma_f32_16x16x32_bf16 v[74:77], a[44:47], v[98:101], v[74:77]\n\t" \
        "buffer_load_dwordx4 v5, s[24:27], s57 offen offset:2048 lds\n\t" \
        "v_mfma_f32_16x16x32_bf16 v[102:105], a[128:131], v[154:157], 0\n\t" \
        "ds_read_b64_tr_b16 a[62:63], v7 offset:40032\n\t" \
        "ds_read_b64_tr_b16 a[64:65], v6 offset:36992\n\t" \
        "v_cvt_pk_bf16_f32 v85, v116, v117\n\t" \
        "s_waitcnt lgkmcnt(12)\n\t" \
        "v_mfma_f32_16x16x32_bf16 v[106:109], a[132:135], v[154:157], 0\n\t" \
        "ds_read_b64_tr_b16 a[66:67], v6 offset:40064\n\t" \
        "v_cvt_pk_bf16_f32 v86, v118, v119\n\t" \
        "v_mfma_f32_16x16x32_bf16 v[110:113], a[136:139], v[158:161], 0\n\t" \
        "ds_read_b64_tr_b16 a[68:69], v7 offset:37024\n\t" \
        "ds_read_b64_tr_b16 a[70:71], v7 offset:40096\n\t" \
        "s_waitcnt lgkmcnt(12)\n\t" \
        "v_mfma_f32_16x16x32_bf16 v[114:117], a[140:143], v[158:161], 0\n\t" \
        "ds_read_b64_tr_b16 a[96:97], v142 offset:0\n\t" \
        "v_cvt_pk_bf16_f32 v87, v120, v121\n\t" \
        "s_waitcnt lgkmcnt(12)\n\t" \
        "v_mfma_f32_16x16x32_bf16 v[30:33], a[48:51], v[78:81], v[30:33]\n\t" \
        "ds_read_b64_tr_b16 a[98:99], v143 offset:0\n\t" \
        "s_waitcnt lgkmcnt(11)\n\t" \
        "v_and_b32 v162, v152, v25\n\t" \
        "v_mfma_f32_16x16x32_bf16 v[54:57], a[48:51], v[82:85], v[54:57]\n\t" \
        "ds_read_b64_tr_b16 a[100:101], v146 offset:0\n\t" \
        "ds_read_b64_tr_b16 a[102:103], v147 offset:0\n\t" \
        "s_waitcnt lgkmcnt(12)\n\t" \
        "v_mfma_f32_16x16x32_bf16 v[34:37], a[52:55], v[78:81], v[34:37]\n\t" \
        "ds_read_b64_tr_b16 a[104:105], v144 offset:0\n\t" \
        "v_and_b32 v163, v152, v26\n\t" \
        "v_mfma_f32_16x16x32_bf16 v[58:61], a[52:55], v[82:85], v[58:61]\n\t" \
        "ds_read_b64_tr_b16 a[106:107], v145 offset:0\n\t" \
        "ds_read_b64_tr_b16 a[108:109], v148 offset:0\n\t" \
        "s_waitcnt lgkmcnt(13)\n\t" \
        "v_mfma_f32_16x16x32_bf16 v[38:41], a[56:59], v[78:81], v[38:41]\n\t" \
        "ds_read_b64_tr_b16 a[110:111], v149 offset:0\n\t" \
        "v_and_b32 v164, v152, v27\n\t" \
        "v_mfma_f32_16x16x32_bf16 v[62:65], a[56:59], v[82:85], v[62:65]\n\t" \
        "ds_read_b64_tr_b16 a[72:73], v6 offset:43008\n\t" \
        "v_and_b32 v165, v152, v28\n\t" \
        "s_waitcnt lgkmcnt(13)\n\t" \
        "v_mfma_f32_16x16x32_bf16 v[42:45], a[60:63], v[78:81], v[42:45]\n\t" \
        "ds_read_b64_tr_b16 a[74:75], v6 offset:46080\n\t" \
        "ds_read_b64_tr_b16 a[76:77], v7 offset:43040\n\t" \
        "v_mfma_f32_16x16x32_bf16 v[66:69], a[60:63], v[82:85], v[66:69]\n\t" \
        "ds_read_b64_tr_b16 a[78:79], v7 offset:46112\n\t" \
        "v_and_b32 v166, v153, v25\n\t" \
        "s_waitcnt lgkmcnt(14)\n\t" \
        "v_mfma_f32_16x16x32_bf16 v[46:49], a[64:67], v[78:81], v[46:49]\n\t" \
        "ds_read_b64_tr_b16 a[80:81], v6 offset:43072\n\t" \
        "ds_read_b64_tr_b16 a[82:83], v6 offset:46144\n\t" \
        "v_cvt_pk_bf16_f32 v88, v122, v123\n\t" \
        "v_and_b32 v167, v153, v26\n\t" \
        "v_mfma_f32_16x16x32_bf16 v[70:73], a[64:67], v[82:85], v[70:73]\n\t" \
        "ds_read_b64_tr_b16 a[84:85], v7 offset:43104\n\t" \
        "v_cvt_pk_bf16_f32 v89, v124, v125\n\t" \
        "v_cvt_pk_bf16_f32 v90, v126, v127\n\t" \
        "s_waitcnt lgkmcnt(14)\n\t" \
        "v_mfma_f32_16x16x32_bf16 v[50:53], a[68:71], v[78:81], v[50:53]\n\t" \
        "ds_read_b64_tr_b16 a[86:87], v7 offset:46176\n\t" \
        "ds_read_b64_tr_b16 a[88:89], v6 offset:43136\n\t" \
        "v_and_b32 v168, v153, v27\n\t" \
        "v_mfma_f32_16x16x32_bf16 v[74:77], a[68:71], v[82:85], v[74:77]\n\t" \
        "ds_read_b64_tr_b16 a[90:91], v6 offset:46208\n\t" \
        "v_cvt_pk_bf16_f32 v91, v128, v129\n\t" \
        "v_cvt_pk_bf16_f32 v92, v130, v131\n\t" \
        "s_waitcnt lgkmcnt(14)\n\t" \
        "v_mfma_f32_16x16x32_bf16 v[118:121], a[96:99], v[162:165], 0\n\t" \
        "s_waitcnt vmcnt(6)\n\t" \
        "s_barrier\n\t" \
        "ds_read_b64_tr_b16 a[92:93], v7 offset:43168\n\t" \
        "v_and_b32 v169, v153, v28\n\t" \
        "v_cvt_pk_bf16_f32 v93, v132, v133\n\t" \
        "v_mfma_f32_16x16x32_bf16 v[122:125], a[100:103], v[162:165], 0\n\t" \
        "ds_read_b64_tr_b16 a[94:95], v7 offset:46240\n\t" \
        "ds_read_b64_tr_b16 a[112:113], v142 offset:64\n\t" \
        "s_waitcnt lgkmcnt(14)\n\t" \
        "v_mfma_f32_16x16x32_bf16 v[126:129], a[104:107], v[166:169], 0\n\t" \
        "ds_read_b64_tr_b16 a[114:115], v143 offset:64\n\t" \
        "v_cvt_pk_bf16_f32 v94, v102, v103\n\t" \
        "s_waitcnt lgkmcnt(14)\n\t" \
        "v_mfma_f32_16x16x32_bf16 v[130:133], a[108:111], v[166:169], 0\n\t" \
        "s_add_u32 m0, s39, 24576\n\t" \
        "s_add_u32 s57, s40, 86016\n\t" \
        "buffer_load_dwordx4 v3, s[24:27], s57 offen offset:0 lds\n\t" \
        "s_waitcnt lgkmcnt(12)\n\t" \
        "v_mfma_f32_16x16x32_bf16 v[30:33], a[72:75], v[86:89], v[30:33]\n\t" \
        "ds_read_b64_tr_b16 a[116:117], v146 offset:64\n\t" \
        "ds_read_b64_tr_b16 a[118:119], v147 offset:64\n\t" \
        "v_mfma_f32_16x16x32_bf16 v[54:57], a[72:75], v[90:93], v[54:57]\n\t" \
        "ds_read_b64_tr_b16 a[120:121], v144 offset:64\n\t" \
        "v_cvt_pk_bf16_f32 v95, v104, v105\n\t" \
        "s_waitcnt lgkmcnt(13)\n\t" \
        "v_mfma_f32_16x16x32_bf16 v[34:37], a[76:79], v[86:89], v[34:37]\n\t" \
        "ds_read_b64_tr_b16 a[122:123], v145 offset:64\n\t" \
        "ds_read_b64_tr_b16 a[124:125], v148 offset:64\n\t" \
        "v_mfma_f32_16x16x32_bf16 v[58:61], a[76:79], v[90:93], v[58:61]\n\t" \
        "ds_read_b64_tr_b16 a[126:127], v149 offset:64\n\t" \
        "v_cvt_pk_bf16_f32 v96, v106, v107\n\t" \
        "s_waitcnt lgkmcnt(13)\n\t" \
        "v_mfma_f32_16x16x32_bf16 v[38:41], a[80:83], v[86:89], v[38:41]\n\t" \
        "ds_read_b64_tr_b16 a[0:1], v6 offset:49152\n\t" \
        "ds_read_b64_tr_b16 a[2:3], v6 offset:52224\n\t" \
        "v_mfma_f32_16x16x32_bf16 v[62:65], a[80:83], v[90:93], v[62:65]\n\t" \
        "buffer_load_dwordx4 v4, s[24:27], s57 offen offset:1024 lds\n\t" \
        "s_waitcnt lgkmcnt(12)\n\t" \
        "v_mfma_f32_16x16x32_bf16 v[42:45], a[84:87], v[86:89], v[42:45]\n\t" \
        "ds_read_b128 v[170:173], v9 offset:512\n\t" \
        "v_cvt_pk_bf16_f32 v97, v108, v109\n\t" \
        "v_mfma_f32_16x16x32_bf16 v[66:69], a[84:87], v[90:93], v[66:69]\n\t" \
        "ds_read_b64_tr_b16 a[4:5], v7 offset:49184\n\t" \
        "v_cvt_pk_bf16_f32 v98, v110, v111\n\t" \
        "v_mfma_f32_16x16x32_bf16 v[46:49], a[88:91], v[86:89], v[46:49]\n\t" \
        "ds_read_b64_tr_b16 a[6:7], v7 offset:52256\n\t" \
        "ds_read_b64_tr_b16 a[8:9], v6 offset:49216\n\t" \
        "v_mfma_f32_16x16x32_bf16 v[70:73], a[88:91], v[90:93], v[70:73]\n\t" \
        "ds_read_b64_tr_b16 a[10:11], v6 offset:52288\n\t" \
        "v_cvt_pk_bf16_f32 v99, v112, v113\n\t" \
        "s_waitcnt lgkmcnt(13)\n\t" \
        "v_mfma_f32_16x16x32_bf16 v[50:53], a[92:95], v[86:89], v[50:53]\n\t" \
        "ds_read_b64_tr_b16 a[12:13], v7 offset:49248\n\t" \
        "ds_read_b64_tr_b16 a[14:15], v7 offset:52320\n\t" \
        "v_cvt_pk_bf16_f32 v100, v114, v115\n\t" \
        "v_mfma_f32_16x16x32_bf16 v[74:77], a[92:95], v[90:93], v[74:77]\n\t" \
        "buffer_load_dwordx4 v5, s[24:27], s57 offen offset:2048 lds\n\t" \
        "v_mfma_f32_16x16x32_bf16 v[102:105], a[112:115], v[162:165], 0\n\t" \
        "ds_read_b64_tr_b16 a[16:17], v6 offset:49280\n\t" \
        "v_cvt_pk_bf16_f32 v101, v116, v117\n\t" \
        "s_waitcnt lgkmcnt(11)\n\t" \
        "v_mfma_f32_16x16x32_bf16 v[106:109], a[116:119], v[162:165], 0\n\t" \
        "ds_read_b64_tr_b16 a[18:19], v6 offset:52352\n\t" \
        "ds_read_b64_tr_b16 a[20:21], v7 offset:49312\n\t" \
        "v_mfma_f32_16x16x32_bf16 v[110:113], a[120:123], v[166:169], 0\n\t" \
        "ds_read_b64_tr_b16 a[22:23], v7 offset:52384\n\t" \
        "v_cvt_pk_bf16_f32 v78, v118, v119\n\t" \
        "s_waitcnt lgkmcnt(11)\n\t" \
        "v_mfma_f32_16x16x32_bf16 v[114:117], a[124:127], v[166:169], 0\n\t" \
        "ds_read_b64_tr_b16 a[128:129], v142 offset:128\n\t" \
        "s_waitcnt lgkmcnt(11)\n\t" \
        "v_xor_b32 v134, v170, v11\n\t" \
        "v_mfma_f32_16x16x32_bf16 v[30:33], a[0:3], v[94:97], v[30:33]\n\t" \
        "ds_read_b64_tr_b16 a[130:131], v143 offset:128\n\t" \
        "ds_read_b64_tr_b16 a[132:133], v146 offset:128\n\t" \
        "v_mfma_f32_16x16x32_bf16 v[54:57], a[0:3], v[98:101], v[54:57]\n\t" \
        "ds_read_b64_tr_b16 a[134:135], v147 offset:128\n\t" \
        "v_xor_b32 v135, v171, v11\n\t" \
        "s_waitcnt lgkmcnt(12)\n\t" \
        "v_mfma_f32_16x16x32_bf16 v[34:37], a[4:7], v[94:97], v[34:37]\n\t" \
        "ds_read_b64_tr_b16 a[136:137], v144 offset:128\n\t" \
        "ds_read_b64_tr_b16 a[138:139], v145 offset:128\n\t" \
        "v_mfma_f32_16x16x32_bf16 v[58:61], a[4:7], v[98:101], v[58:61]\n\t" \
        "ds_read_b64_tr_b16 a[140:141], v148 offset:128\n\t" \
        "v_xor_b32 v136, v172, v11\n\t" \
        "s_waitcnt lgkmcnt(13)\n\t" \
        "v_mfma_f32_16x16x32_bf16 v[38:41], a[8:11], v[94:97], v[38:41]\n\t" \
        "ds_read_b64_tr_b16 a[142:143], v149 offset:128\n\t" \
        "ds_read_b64_tr_b16 a[24:25], v6 offset:55296\n\t" \
        "v_mfma_f32_16x16x32_bf16 v[62:65], a[8:11], v[98:101], v[62:65]\n\t" \
        "ds_read_b64_tr_b16 a[26:27], v6 offset:58368\n\t" \
        "v_xor_b32 v138, v170, v12\n\t" \
        "s_waitcnt lgkmcnt(14)\n\t" \
        "v_mfma_f32_16x16x32_bf16 v[42:45], a[12:15], v[94:97], v[42:45]\n\t" \
        "ds_read_b64_tr_b16 a[28:29], v7 offset:55328\n\t" \
        "v_xor_b32 v137, v173, v11\n\t" \
        "v_mfma_f32_16x16x32_bf16 v[66:69], a[12:15], v[98:101], v[66:69]\n\t" \
        "ds_read_b64 v[150:151], v10 offset:512\n\t" \
        "ds_read_b64_tr_b16 a[30:31], v7 offset:58400\n\t" \
        "v_xor_b32 v139, v171, v12\n\t" \
        "v_xor_b32 v140, v172, v12\n\t" \
        "s_waitcnt lgkmcnt(14)\n\t" \
        "v_mfma_f32_16x16x32_bf16 v[46:49], a[16:19], v[94:97], v[46:49]\n\t" \
        "ds_read_b64_tr_b16 a[32:33], v6 offset:55360\n\t" \
        "v_xor_b32 v141, v173, v12\n\t" \
        "v_cvt_pk_bf16_f32 v79, v120, v121\n\t" \
        "v_mfma_f32_16x16x32_bf16 v[70:73], a[16:19], v[98:101], v[70:73]\n\t" \
        "ds_read_b64_tr_b16 a[34:35], v6 offset:58432\n\t" \
        "ds_read_b64_tr_b16 a[36:37], v7 offset:55392\n\t" \
        "v_cvt_pk_bf16_f32 v80, v122, v123\n\t" \
        "s_waitcnt lgkmcnt(15)\n\t" \
        "v_mfma_f32_16x16x32_bf16 v[50:53], a[20:23], v[94:97], v[50:53]\n\t" \
        "ds_read_b64_tr_b16 a[38:39], v7 offset:58464\n\t" \
        "v_cvt_pk_bf16_f32 v81, v124, v125\n\t" \
        "v_cvt_pk_bf16_f32 v82, v126, v127\n\t" \
        "v_mfma_f32_16x16x32_bf16 v[74:77], a[20:23], v[98:101], v[74:77]\n\t" \
        "ds_read_b64_tr_b16 a[40:41], v6 offset:55424\n\t" \
        "ds_read_b64_tr_b16 a[42:43], v6 offset:58496\n\t" \
        "v_cvt_pk_bf16_f32 v83, v128, v129\n\t" \
        "v_cvt_pk_bf16_f32 v84, v130, v131\n\t" \
        "s_waitcnt lgkmcnt(15)\n\t" \
        "v_mfma_f32_16x16x32_bf16 v[118:121], a[128:131], v[162:165], 0\n\t" \
        "s_waitcnt vmcnt(6)\n\t" \
        "s_barrier\n\t" \
        "ds_read_b64_tr_b16 a[44:45], v7 offset:55456\n\t" \
        "v_cvt_pk_bf16_f32 v85, v132, v133\n\t" \
        "v_mfma_f32_16x16x32_bf16 v[122:125], a[132:135], v[162:165], 0\n\t" \
        "ds_read_b64_tr_b16 a[46:47], v7 offset:58528\n\t" \
        "v_cvt_pk_bf16_f32 v86, v102, v103\n\t" \
        "s_waitcnt lgkmcnt(14)\n\t" \
        "v_mfma_f32_16x16x32_bf16 v[126:129], a[136:139], v[166:169], 0\n\t" \
        "ds_read_b64_tr_b16 a[96:97], v134 offset:0\n\t" \
        "ds_read_b64_tr_b16 a[98:99], v135 offset:0\n\t" \
        "s_waitcnt lgkmcnt(14)\n\t" \
        "v_mfma_f32_16x16x32_bf16 v[130:133], a[140:143], v[166:169], 0\n\t" \
        "s_add_u32 m0, s39, 36864\n\t" \
        "s_add_u32 s57, s40, 98304\n\t" \
        "buffer_load_dwordx4 v3, s[24:27], s57 offen offset:0 lds\n\t" \
        "s_waitcnt lgkmcnt(13)\n\t" \
        "v_mfma_f32_16x16x32_bf16 v[30:33], a[24:27], v[78:81], v[30:33]\n\t" \
        "ds_read_b64_tr_b16 a[100:101], v138 offset:0\n\t" \
        "s_waitcnt lgkmcnt(12)\n\t" \
        "v_and_b32 v154, v150, v25\n\t" \
        "v_mfma_f32_16x16x32_bf16 v[54:57], a[24:27], v[82:85], v[54:57]\n\t" \
        "ds_read_b64_tr_b16 a[102:103], v139 offset:0\n\t" \
        "ds_read_b64_tr_b16 a[104:105], v136 offset:0\n\t" \
        "s_waitcnt lgkmcnt(13)\n\t" \
        "v_mfma_f32_16x16x32_bf16 v[34:37], a[28:31], v[78:81], v[34:37]\n\t" \
        "ds_read_b64_tr_b16 a[106:107], v137 offset:0\n\t" \
        "v_and_b32 v155, v150, v26\n\t" \
        "v_mfma_f32_16x16x32_bf16 v[58:61], a[28:31], v[82:85], v[58:61]\n\t" \
        "ds_read_b64_tr_b16 a[108:109], v140 offset:0\n\t" \
        "ds_read_b64_tr_b16 a[110:111], v141 offset:0\n\t" \
        "v_and_b32 v156, v150, v27\n\t" \
        "s_waitcnt lgkmcnt(13)\n\t" \
        "v_mfma_f32_16x16x32_bf16 v[38:41], a[32:35], v[78:81], v[38:41]\n\t" \
        "ds_read_b64_tr_b16 a[48:49], v6 offset:0\n\t" \
        "v_and_b32 v157, v150, v28\n\t" \
        "v_and_b32 v158, v151, v25\n\t" \
        "v_mfma_f32_16x16x32_bf16 v[62:65], a[32:35], v[82:85], v[62:65]\n\t" \
        "buffer_load_dwordx4 v4, s[24:27], s57 offen offset:1024 lds\n\t" \
        "s_waitcnt lgkmcnt(11)\n\t" \
        "v_mfma_f32_16x16x32_bf16 v[42:45], a[36:39], v[78:81], v[42:45]\n\t" \
        "ds_read_b64_tr_b16 a[50:51], v6 offset:3072\n\t" \
        "ds_read_b64_tr_b16 a[52:53], v7 offset:32\n\t" \
        "v_cvt_pk_bf16_f32 v87, v104, v105\n\t" \
        "v_mfma_f32_16x16x32_bf16 v[66:69], a[36:39], v[82:85], v[66:69]\n\t" \
        "ds_read_b64_tr_b16 a[54:55], v7 offset:3104\n\t" \
        "v_cvt_pk_bf16_f32 v88, v106, v107\n\t" \
        "v_and_b32 v159, v151, v26\n\t" \
        "v_mfma_f32_16x16x32_bf16 v[46:49], a[40:43], v[78:81], v[46:49]\n\t" \
        "ds_read_b64_tr_b16 a[56:57], v6 offset:64\n\t" \
        "v_cvt_pk_bf16_f32 v89, v108, v109\n\t" \
        "v_cvt_pk_bf16_f32 v90, v110, v111\n\t" \
        "v_mfma_f32_16x16x32_bf16 v[70:73], a[40:43], v[82:85], v[70:73]\n\t" \
        "ds_read_b64_tr_b16 a[58:59], v6 offset:3136\n\t" \
        "ds_read_b64_tr_b16 a[60:61], v7 offset:96\n\t" \
        "v_and_b32 v160, v151, v27\n\t" \
        "s_waitcnt lgkmcnt(13)\n\t" \
        "v_mfma_f32_16x16x32_bf16 v[50:53], a[44:47], v[78:81], v[50:53]\n\t" \
        "ds_read_b64_tr_b16 a[62:63], v7 offset:3168\n\t" \
        "v_cvt_pk_bf16_f32 v91, v112, v113\n\t" \
        "v_cvt_pk_bf16_f32 v92, v114, v115\n\t" \
        "v_mfma_f32_16x16x32_bf16 v[74:77], a[44:47], v[82:85], v[74:77]\n\t" \
        "buffer_load_dwordx4 v5, s[24:27], s57 offen offset:2048 lds\n\t" \
        "v_mfma_f32_16x16x32_bf16 v[102:105], a[96:99], v[154:157], 0\n\t" \
        "ds_read_b64_tr_b16 a[64:65], v6 offset:128\n\t" \
        "ds_read_b64_tr_b16 a[66:67], v6 offset:3200\n\t" \
        "v_and_b32 v161, v151, v28\n\t" \
        "v_cvt_pk_bf16_f32 v93, v116, v117\n\t" \
        "s_waitcnt lgkmcnt(12)\n\t" \
        "v_mfma_f32_16x16x32_bf16 v[106:109], a[100:103], v[154:157], 0\n\t" \
        "ds_read_b64_tr_b16 a[68:69], v7 offset:160\n\t" \
        "v_cvt_pk_bf16_f32 v94, v118, v119\n\t" \
        "v_mfma_f32_16x16x32_bf16 v[110:113], a[104:107], v[158:161], 0\n\t" \
        "ds_read_b64_tr_b16 a[70:71], v7 offset:3232\n\t" \
        "v_cvt_pk_bf16_f32 v95, v120, v121\n\t" \
        "s_waitcnt lgkmcnt(11)\n\t" \
        "v_mfma_f32_16x16x32_bf16 v[114:117], a[108:111], v[158:161], 0\n\t" \
        "ds_read_b64_tr_b16 a[112:113], v134 offset:64\n\t" \
        "ds_read_b64_tr_b16 a[114:115], v135 offset:64\n\t" \
        "s_waitcnt lgkmcnt(12)\n\t" \
        "v_mfma_f32_16x16x32_bf16 v[30:33], a[48:51], v[86:89], v[30:33]\n\t" \
        "ds_read_b64_tr_b16 a[116:117], v138 offset:64\n\t" \
        "v_cvt_pk_bf16_f32 v96, v122, v123\n\t" \
        "v_mfma_f32_16x16x32_bf16 v[54:57], a[48:51], v[90:93], v[54:57]\n\t" \
        "ds_read_b64_tr_b16 a[118:119], v139 offset:64\n\t" \
        "ds_read_b64_tr_b16 a[120:121], v136 offset:64\n\t" \
        "s_waitcnt lgkmcnt(13)\n\t" \
        "v_mfma_f32_16x16x32_bf16 v[34:37], a[52:55], v[86:89], v[34:37]\n\t" \
        "ds_read_b64_tr_b16 a[122:123], v137 offset:64\n\t" \
        "v_cvt_pk_bf16_f32 v97, v124, v125\n\t" \
        "v_mfma_f32_16x16x32_bf16 v[58:61], a[52:55], v[90:93], v[58:61]\n\t" \
        "ds_read_b64_tr_b16 a[124:125], v140 offset:64\n\t" \
        "v_cvt_pk_bf16_f32 v98, v126, v127\n\t" \
        "s_waitcnt lgkmcnt(12)\n\t" \
        "v_mfma_f32_16x16x32_bf16 v[38:41], a[56:59], v[86:89], v[38:41]\n\t" \
        "ds_read_b64_tr_b16 a[126:127], v141 offset:64\n\t" \
        "ds_read_b64_tr_b16 a[72:73], v6 offset:6144\n\t" \
        "v_mfma_f32_16x16x32_bf16 v[62:65], a[56:59], v[90:93], v[62:65]\n\t" \
        "ds_read_b64_tr_b16 a[74:75], v6 offset:9216\n\t" \
        "v_cvt_pk_bf16_f32 v99, v128, v129\n\t" \
        "s_waitcnt lgkmcnt(14)\n\t" \
        "v_mfma_f32_16x16x32_bf16 v[42:45], a[60:63], v[86:89], v[42:45]\n\t" \
        "ds_read_b128 v[174:177], v9 offset:640\n\t" \
        "ds_read_b64_tr_b16 a[76:77], v7 offset:6176\n\t" \
        "v_mfma_f32_16x16x32_bf16 v[66:69], a[60:63], v[90:93], v[66:69]\n\t" \
        "ds_read_b64_tr_b16 a[78:79], v7 offset:9248\n\t" \
        "v_cvt_pk_bf16_f32 v100, v130, v131\n\t" \
        "s_waitcnt lgkmcnt(14)\n\t" \
        "v_mfma_f32_16x16x32_bf16 v[46:49], a[64:67], v[86:89], v[46:49]\n\t" \
        "ds_read_b64_tr_b16 a[80:81], v6 offset:6208\n\t" \
        "ds_read_b64_tr_b16 a[82:83], v6 offset:9280\n\t" \
        "v_mfma_f32_16x16x32_bf16 v[70:73], a[64:67], v[90:93], v[70:73]\n\t" \
        "ds_read_b64_tr_b16 a[84:85], v7 offset:6240\n\t" \
        "v_cvt_pk_bf16_f32 v101, v132, v133\n\t" \
        "s_waitcnt lgkmcnt(14)\n\t" \
        "v_mfma_f32_16x16x32_bf16 v[50:53], a[68:71], v[86:89], v[50:53]\n\t" \
        "ds_read_b64_tr_b16 a[86:87], v7 offset:9312\n\t" \
        "v_cvt_pk_bf16_f32 v78, v102, v103\n\t" \
        "v_mfma_f32_16x16x32_bf16 v[74:77], a[68:71], v[90:93], v[74:77]\n\t" \
        "ds_read_b64_tr_b16 a[88:89], v6 offset:6272\n\t" \
        "ds_read_b64_tr_b16 a[90:91], v6 offset:9344\n\t" \
        "v_mfma_f32_16x16x32_bf16 v[118:121], a[112:115], v[154:157], 0\n\t" \
        "s_waitcnt vmcnt(6)\n\t" \
        "s_barrier\n\t" \
        "ds_read_b64_tr_b16 a[92:93], v7 offset:6304\n\t" \
        "v_cvt_pk_bf16_f32 v79, v104, v105\n\t" \
        "s_waitcnt lgkmcnt(14)\n\t" \
        "v_mfma_f32_16x16x32_bf16 v[122:125], a[116:119], v[154:157], 0\n\t" \
        "ds_read_b64_tr_b16 a[94:95], v7 offset:9376\n\t" \
        "ds_read_b64_tr_b16 a[128:129], v134 offset:128\n\t" \
        "v_mfma_f32_16x16x32_bf16 v[126:129], a[120:123], v[158:161], 0\n\t" \
        "ds_read_b64_tr_b16 a[130:131], v135 offset:128\n\t" \
        "v_cvt_pk_bf16_f32 v80, v106, v107\n\t" \
        "s_waitcnt lgkmcnt(14)\n\t" \
        "v_mfma_f32_16x16x32_bf16 v[130:133], a[124:127], v[158:161], 0\n\t" \
        "s_add_u32 m0, s39, 49152\n\t" \
        "s_add_u32 s57, s40, 110592\n\t" \
        "buffer_load_dwordx4 v3, s[24:27], s57 offen offset:0 lds\n\t" \
        "s_waitcnt lgkmcnt(13)\n\t" \
        "v_mfma_f32_16x16x32_bf16 v[30:33], a[72:75], v[94:97], v[30:33]\n\t" \
        "ds_read_b64_tr_b16 a[132:133], v138 offset:128\n\t" \
        "s_waitcnt lgkmcnt(12)\n\t" \
        "v_xor_b32 v142, v174, v11\n\t" \
        "v_mfma_f32_16x16x32_bf16 v[54:57], a[72:75], v[98:101], v[54:57]\n\t" \
        "ds_read_b64_tr_b16 a[134:135], v139 offset:128\n\t" \
        "ds_read_b64_tr_b16 a[136:137], v136 offset:128\n\t" \
        "s_waitcnt lgkmcnt(13)\n\t" \
        "v_mfma_f32_16x16x32_bf16 v[34:37], a[76:79], v[94:97], v[34:37]\n\t" \
        "ds_read_b64_tr_b16 a[138:139], v137 offset:128\n\t" \
        "v_xor_b32 v143, v175, v11\n\t" \
        "v_mfma_f32_16x16x32_bf16 v[58:61], a[76:79], v[98:101], v[58:61]\n\t" \
        "ds_read_b64_tr_b16 a[140:141], v140 offset:128\n\t" \
        "ds_read_b64_tr_b16 a[142:143], v141 offset:128\n\t" \
        "s_waitcnt lgkmcnt(13)\n\t" \
        "v_mfma_f32_16x16x32_bf16 v[38:41], a[80:83], v[94:97], v[38:41]\n\t" \
        "ds_read_b64_tr_b16 a[0:1], v6 offset:12288\n\t" \
        "v_xor_b32 v144, v176, v11\n\t" \
        "v_mfma_f32_16x16x32_bf16 v[62:65], a[80:83], v[98:101], v[62:65]\n\t" \
        "buffer_load_dwordx4 v4, s[24:27], s57 offen offset:1024 lds\n\t" \
        "s_waitcnt lgkmcnt(11)\n\t" \
        "v_mfma_f32_16x16x32_bf16 v[42:45], a[84:87], v[94:97], v[42:45]\n\t" \
        "ds_read_b64_tr_b16 a[2:3], v6 offset:15360\n\t" \
        "ds_read_b64_tr_b16 a[4:5], v7 offset:12320\n\t" \
        "v_xor_b32 v146, v174, v12\n\t" \
        "v_xor_b32 v145, v177, v11\n\t" \
        "v_mfma_f32_16x16x32_bf16 v[66:69], a[84:87], v[98:101], v[66:69]\n\t" \
        "ds_read_b64 v[152:153], v10 offset:640\n\t" \
        "v_xor_b32 v147, v175, v12\n\t" \
        "v_xor_b32 v148, v176, v12\n\t" \
        "v_mfma_f32_16x16x32_bf16 v[46:49], a[88:91], v[94:97], v[46:49]\n\t" \
        "ds_read_b64_tr_b16 a[6:7], v7 offset:15392\n\t" \
        "v_xor_b32 v149, v177, v12\n\t" \
        "v_mfma_f32_16x16x32_bf16 v[70:73], a[88:91], v[98:101], v[70:73]\n\t" \
        "ds_read_b64_tr_b16 a[8:9], v6 offset:12352\n\t" \
        "ds_read_b64_tr_b16 a[10:11], v6 offset:15424\n\t" \
        "v_cvt_pk_bf16_f32 v81, v108, v109\n\t" \
        "v_cvt_pk_bf16_f32 v82, v110, v111\n\t" \
        "s_waitcnt lgkmcnt(13)\n\t" \
        "v_mfma_f32_16x16x32_bf16 v[50:53], a[92:95], v[94:97], v[50:53]\n\t" \
        "ds_read_b64_tr_b16 a[12:13], v7 offset:12384\n\t" \
        "v_cvt_pk_bf16_f32 v83, v112, v113\n\t" \
        "v_cvt_pk_bf16_f32 v84, v114, v115\n\t" \
        "v_mfma_f32_16x16x32_bf16 v[74:77], a[92:95], v[98:101], v[74:77]\n\t" \
        "buffer_load_dwordx4 v5, s[24:27], s57 offen offset:2048 lds\n\t" \
        "v_mfma_f32_16x16x32_bf16 v[102:105], a[128:131], v[154:157], 0\n\t" \
        "ds_read_b64_tr_b16 a[14:15], v7 offset:15456\n\t" \
        "ds_read_b64_tr_b16 a[16:17], v6 offset:12416\n\t" \
        "v_cvt_pk_bf16_f32 v85, v116, v117\n\t" \
        "s_waitcnt lgkmcnt(12)\n\t" \
        "v_mfma_f32_16x16x32_bf16 v[106:109], a[132:135], v[154:157], 0\n\t" \
        "ds_read_b64_tr_b16 a[18:19], v6 offset:15488\n\t" \
        "v_cvt_pk_bf16_f32 v86, v118, v119\n\t" \
        "v_mfma_f32_16x16x32_bf16 v[110:113], a[136:139], v[158:161], 0\n\t" \
        "ds_read_b64_tr_b16 a[20:21], v7 offset:12448\n\t" \
        "ds_read_b64_tr_b16 a[22:23], v7 offset:15520\n\t" \
        "s_waitcnt lgkmcnt(12)\n\t" \
        "v_mfma_f32_16x16x32_bf16 v[114:117], a[140:143], v[158:161], 0\n\t" \
        "ds_read_b64_tr_b16 a[96:97], v142 offset:0\n\t" \
        "v_cvt_pk_bf16_f32 v87, v120, v121\n\t" \
        "s_waitcnt lgkmcnt(12)\n\t" \
        "v_mfma_f32_16x16x32_bf16 v[30:33], a[0:3], v[78:81], v[30:33]\n\t" \
        "ds_read_b64_tr_b16 a[98:99], v143 offset:0\n\t" \
        "s_waitcnt lgkmcnt(11)\n\t" \
        "v_and_b32 v162, v152, v25\n\t" \
        "v_mfma_f32_16x16x32_bf16 v[54:57], a[0:3], v[82:85], v[54:57]\n\t" \
        "ds_read_b64_tr_b16 a[100:101], v146 offset:0\n\t" \
        "ds_read_b64_tr_b16 a[102:103], v147 offset:0\n\t" \
        "s_waitcnt lgkmcnt(12)\n\t" \
        "v_mfma_f32_16x16x32_bf16 v[34:37], a[4:7], v[78:81], v[34:37]\n\t" \
        "ds_read_b64_tr_b16 a[104:105], v144 offset:0\n\t" \
        "v_and_b32 v163, v152, v26\n\t" \
        "v_mfma_f32_16x16x32_bf16 v[58:61], a[4:7], v[82:85], v[58:61]\n\t" \
        "ds_read_b64_tr_b16 a[106:107], v145 offset:0\n\t" \
        "ds_read_b64_tr_b16 a[108:109], v148 offset:0\n\t" \
        "s_waitcnt lgkmcnt(13)\n\t" \
        "v_mfma_f32_16x16x32_bf16 v[38:41], a[8:11], v[78:81], v[38:41]\n\t" \
        "ds_read_b64_tr_b16 a[110:111], v149 offset:0\n\t" \
        "v_and_b32 v164, v152, v27\n\t" \
        "v_mfma_f32_16x16x32_bf16 v[62:65], a[8:11], v[82:85], v[62:65]\n\t" \
        "ds_read_b64_tr_b16 a[24:25], v6 offset:18432\n\t" \
        "ds_read_b64_tr_b16 a[26:27], v6 offset:21504\n\t" \
        "s_waitcnt lgkmcnt(14)\n\t" \
        "v_mfma_f32_16x16x32_bf16 v[42:45], a[12:15], v[78:81], v[42:45]\n\t" \
        "ds_read_b64_tr_b16 a[28:29], v7 offset:18464\n\t" \
        "v_and_b32 v165, v152, v28\n\t" \
        "v_mfma_f32_16x16x32_bf16 v[66:69], a[12:15], v[82:85], v[66:69]\n\t" \
        "ds_read_b64_tr_b16 a[30:31], v7 offset:21536\n\t" \
        "v_and_b32 v166, v153, v25\n\t" \
        "s_waitcnt lgkmcnt(14)\n\t" \
        "v_mfma_f32_16x16x32_bf16 v[46:49], a[16:19], v[78:81], v[46:49]\n\t" \
        "ds_read_b64_tr_b16 a[32:33], v6 offset:18496\n\t" \
        "ds_read_b64_tr_b16 a[34:35], v6 offset:21568\n\t" \
        "v_cvt_pk_bf16_f32 v88, v122, v123\n\t" \
        "v_and_b32 v167, v153, v26\n\t" \
        "v_mfma_f32_16x16x32_bf16 v[70:73], a[16:19], v[82:85], v[70:73]\n\t" \
        "ds_read_b64_tr_b16 a[36:37], v7 offset:18528\n\t" \
        "v_cvt_pk_bf16_f32 v89, v124, v125\n\t" \
        "v_cvt_pk_bf16_f32 v90, v126, v127\n\t" \
        "s_waitcnt lgkmcnt(14)\n\t" \
        "v_mfma_f32_16x16x32_bf16 v[50:53], a[20:23], v[78:81], v[50:53]\n\t" \
        "ds_read_b64_tr_b16 a[38:39], v7 offset:21600\n\t" \
        "ds_read_b64_tr_b16 a[40:41], v6 offset:18560\n\t" \
        "v_and_b32 v168, v153, v27\n\t" \
        "v_mfma_f32_16x16x32_bf16 v[74:77], a[20:23], v[82:85], v[74:77]\n\t" \
        "ds_read_b64_tr_b16 a[42:43], v6 offset:21632\n\t" \
        "v_cvt_pk_bf16_f32 v91, v128, v129\n\t" \
        "v_cvt_pk_bf16_f32 v92, v130, v131\n\t" \
        "s_waitcnt lgkmcnt(14)\n\t" \
        "v_mfma_f32_16x16x32_bf16 v[118:121], a[96:99], v[162:165], 0\n\t" \
        "s_waitcnt vmcnt(6)\n\t" \
        "s_barrier\n\t" \
        "ds_read_b64_tr_b16 a[44:45], v7 offset:18592\n\t" \
        "ds_read_b64_tr_b16 a[46:47], v7 offset:21664\n\t" \
        "v_and_b32 v169, v153, v28\n\t" \
        "v_cvt_pk_bf16_f32 v93, v132, v133\n\t" \
        "v_mfma_f32_16x16x32_bf16 v[122:125], a[100:103], v[162:165], 0\n\t" \
        "ds_read_b64_tr_b16 a[112:113], v142 offset:64\n\t" \
        "v_cvt_pk_bf16_f32 v94, v102, v103\n\t" \
        "s_waitcnt lgkmcnt(14)\n\t" \
        "v_mfma_f32_16x16x32_bf16 v[126:129], a[104:107], v[166:169], 0\n\t" \
        "ds_read_b64_tr_b16 a[114:115], v143 offset:64\n\t" \
        "v_cvt_pk_bf16_f32 v95, v104, v105\n\t" \
        "s_waitcnt lgkmcnt(14)\n\t" \
        "v_mfma_f32_16x16x32_bf16 v[130:133], a[108:111], v[166:169], 0\n\t" \
        "s_add_u32 m0, s39, 0\n\t" \
        "s_add_u32 s57, s40, 122880\n\t" \
        "buffer_load_dwordx4 v3, s[24:27], s57 offen offset:0 lds\n\t" \
        "s_waitcnt lgkmcnt(12)\n\t" \
        "v_mfma_f32_16x16x32_bf16 v[30:33], a[24:27], v[86:89], v[30:33]\n\t" \
        "ds_read_b64_tr_b16 a[116:117], v146 offset:64\n\t" \
        "ds_read_b64_tr_b16 a[118:119], v147 offset:64\n\t" \
        "v_mfma_f32_16x16x32_bf16 v[54:57], a[24:27], v[90:93], v[54:57]\n\t" \
        "ds_read_b64_tr_b16 a[120:121], v144 offset:64\n\t" \
        "v_cvt_pk_bf16_f32 v96, v106, v107\n\t" \
        "s_waitcnt lgkmcnt(13)\n\t" \
        "v_mfma_f32_16x16x32_bf16 v[34:37], a[28:31], v[86:89], v[34:37]\n\t" \
        "ds_read_b64_tr_b16 a[122:123], v145 offset:64\n\t" \
        "ds_read_b64_tr_b16 a[124:125], v148 offset:64\n\t" \
        "v_mfma_f32_16x16x32_bf16 v[58:61], a[28:31], v[90:93], v[58:61]\n\t" \
        "ds_read_b64_tr_b16 a[126:127], v149 offset:64\n\t" \
        "v_cvt_pk_bf16_f32 v97, v108, v109\n\t" \
        "s_waitcnt lgkmcnt(13)\n\t" \
        "v_mfma_f32_16x16x32_bf16 v[38:41], a[32:35], v[86:89], v[38:41]\n\t" \
        "ds_read_b64_tr_b16 a[48:49], v6 offset:24576\n\t" \
        "ds_read_b64_tr_b16 a[50:51], v6 offset:27648\n\t" \
        "v_mfma_f32_16x16x32_bf16 v[62:65], a[32:35], v[90:93], v[62:65]\n\t" \
        "buffer_load_dwordx4 v4, s[24:27], s57 offen offset:1024 lds\n\t" \
        "s_waitcnt lgkmcnt(12)\n\t" \
        "v_mfma_f32_16x16x32_bf16 v[42:45], a[36:39], v[86:89], v[42:45]\n\t" \
        "ds_read_b128 v[170:173], v9 offset:768\n\t" \
        "v_cvt_pk_bf16_f32 v98, v110, v111\n\t" \
        "v_mfma_f32_16x16x32_bf16 v[66:69], a[36:39], v[90:93], v[66:69]\n\t" \
        "ds_read_b64_tr_b16 a[52:53], v7 offset:24608\n\t" \
        "v_cvt_pk_bf16_f32 v99, v112, v113\n\t" \
        "v_mfma_f32_16x16x32_bf16 v[46:49], a[40:43], v[86:89], v[46:49]\n\t" \
        "ds_read_b64_tr_b16 a[54:55], v7 offset:27680\n\t" \
        "ds_read_b64_tr_b16 a[56:57], v6 offset:24640\n\t" \
        "v_mfma_f32_16x16x32_bf16 v[70:73], a[40:43], v[90:93], v[70:73]\n\t" \
        "ds_read_b64_tr_b16 a[58:59], v6 offset:27712\n\t" \
        "v_cvt_pk_bf16_f32 v100, v114, v115\n\t" \
        "s_waitcnt lgkmcnt(13)\n\t" \
        "v_mfma_f32_16x16x32_bf16 v[50:53], a[44:47], v[86:89], v[50:53]\n\t" \
        "ds_read_b64_tr_b16 a[60:61], v7 offset:24672\n\t" \
        "ds_read_b64_tr_b16 a[62:63], v7 offset:27744\n\t" \
        "v_mfma_f32_16x16x32_bf16 v[74:77], a[44:47], v[90:93], v[74:77]\n\t" \
        "buffer_load_dwordx4 v5, s[24:27], s57 offen offset:2048 lds\n\t" \
        "v_mfma_f32_16x16x32_bf16 v[102:105], a[112:115], v[162:165], 0\n\t" \
        "ds_read_b64_tr_b16 a[64:65], v6 offset:24704\n\t" \
        "v_cvt_pk_bf16_f32 v101, v116, v117\n\t" \
        "s_waitcnt lgkmcnt(11)\n\t" \
        "v_mfma_f32_16x16x32_bf16 v[106:109], a[116:119], v[162:165], 0\n\t" \
        "ds_read_b64_tr_b16 a[66:67], v6 offset:27776\n\t" \
        "ds_read_b64_tr_b16 a[68:69], v7 offset:24736\n\t" \
        "v_mfma_f32_16x16x32_bf16 v[110:113], a[120:123], v[166:169], 0\n\t" \
        "ds_read_b64_tr_b16 a[70:71], v7 offset:27808\n\t" \
        "v_cvt_pk_bf16_f32 v78, v118, v119\n\t" \
        "s_waitcnt lgkmcnt(11)\n\t" \
        "v_mfma_f32_16x16x32_bf16 v[114:117], a[124:127], v[166:169], 0\n\t" \
        "ds_read_b64_tr_b16 a[128:129], v142 offset:128\n\t" \
        "s_waitcnt lgkmcnt(11)\n\t" \
        "v_xor_b32 v134, v170, v11\n\t" \
        "v_mfma_f32_16x16x32_bf16 v[30:33], a[48:51], v[94:97], v[30:33]\n\t" \
        "ds_read_b64_tr_b16 a[130:131], v143 offset:128\n\t" \
        "ds_read_b64_tr_b16 a[132:133], v146 offset:128\n\t" \
        "v_mfma_f32_16x16x32_bf16 v[54:57], a[48:51], v[98:101], v[54:57]\n\t" \
        "ds_read_b64_tr_b16 a[134:135], v147 offset:128\n\t" \
        "v_xor_b32 v135, v171, v11\n\t" \
        "s_waitcnt lgkmcnt(12)\n\t" \
        "v_mfma_f32_16x16x32_bf16 v[34:37], a[52:55], v[94:97], v[34:37]\n\t" \
        "ds_read_b64_tr_b16 a[136:137], v144 offset:128\n\t" \
        "ds_read_b64_tr_b16 a[138:139], v145 offset:128\n\t" \
        "v_mfma_f32_16x16x32_bf16 v[58:61], a[52:55], v[98:101], v[58:61]\n\t" \
        "ds_read_b64_tr_b16 a[140:141], v148 offset:128\n\t" \
        "v_xor_b32 v136, v172, v11\n\t" \
        "s_waitcnt lgkmcnt(13)\n\t" \
        "v_mfma_f32_16x16x32_bf16 v[38:41], a[56:59], v[94:97], v[38:41]\n\t" \
        "ds_read_b64_tr_b16 a[142:143], v149 offset:128\n\t" \
        "ds_read_b64_tr_b16 a[72:73], v6 offset:30720\n\t" \
        "v_mfma_f32_16x16x32_bf16 v[62:65], a[56:59], v[98:101], v[62:65]\n\t" \
        "ds_read_b64_tr_b16 a[74:75], v6 offset:33792\n\t" \
        "v_xor_b32 v138, v170, v12\n\t" \
        "s_waitcnt lgkmcnt(14)\n\t" \
        "v_mfma_f32_16x16x32_bf16 v[42:45], a[60:63], v[94:97], v[42:45]\n\t" \
        "ds_read_b64_tr_b16 a[76:77], v7 offset:30752\n\t" \
        "v_xor_b32 v137, v173, v11\n\t" \
        "v_mfma_f32_16x16x32_bf16 v[66:69], a[60:63], v[98:101], v[66:69]\n\t" \
        "ds_read_b64 v[150:151], v10 offset:768\n\t" \
        "ds_read_b64_tr_b16 a[78:79], v7 offset:33824\n\t" \
        "v_xor_b32 v139, v171, v12\n\t" \
        "v_xor_b32 v140, v172, v12\n\t" \
        "s_waitcnt lgkmcnt(14)\n\t" \
        "v_mfma_f32_16x16x32_bf16 v[46:49], a[64:67], v[94:97], v[46:49]\n\t" \
        "ds_read_b64_tr_b16 a[80:81], v6 offset:30784\n\t" \
        "v_xor_b32 v141, v173, v12\n\t" \
        "v_cvt_pk_bf16_f32 v79, v120, v121\n\t" \
        "v_mfma_f32_16x16x32_bf16 v[70:73], a[64:67], v[98:101], v[70:73]\n\t" \
        "ds_read_b64_tr_b16 a[82:83], v6 offset:33856\n\t" \
        "ds_read_b64_tr_b16 a[84:85], v7 offset:30816\n\t" \
        "v_cvt_pk_bf16_f32 v80, v122, v123\n\t" \
        "s_waitcnt lgkmcnt(15)\n\t" \
        "v_mfma_f32_16x16x32_bf16 v[50:53], a[68:71], v[94:97], v[50:53]\n\t" \
        "ds_read_b64_tr_b16 a[86:87], v7 offset:33888\n\t" \
        "v_cvt_pk_bf16_f32 v81, v124, v125\n\t" \
        "v_cvt_pk_bf16_f32 v82, v126, v127\n\t" \
        "v_mfma_f32_16x16x32_bf16 v[74:77], a[68:71], v[98:101], v[74:77]\n\t" \
        "ds_read_b64_tr_b16 a[88:89], v6 offset:30848\n\t" \
        "ds_read_b64_tr_b16 a[90:91], v6 offset:33920\n\t" \
        "v_cvt_pk_bf16_f32 v83, v128, v129\n\t" \
        "v_cvt_pk_bf16_f32 v84, v130, v131\n\t" \
        "s_waitcnt lgkmcnt(15)\n\t" \
        "v_mfma_f32_16x16x32_bf16 v[118:121], a[128:131], v[162:165], 0\n\t" \
        "s_waitcnt vmcnt(6)\n\t" \
        "s_barrier\n\t" \
        "ds_read_b64_tr_b16 a[92:93], v7 offset:30880\n\t" \
        "v_cvt_pk_bf16_f32 v85, v132, v133\n\t" \
        "v_mfma_f32_16x16x32_bf16 v[122:125], a[132:135], v[162:165], 0\n\t" \
        "ds_read_b64_tr_b16 a[94:95], v7 offset:33952\n\t" \
        "v_cvt_pk_bf16_f32 v86, v102, v103\n\t" \
        "s_waitcnt lgkmcnt(14)\n\t" \
        "v_mfma_f32_16x16x32_bf16 v[126:129], a[136:139], v[166:169], 0\n\t" \
        "ds_read_b64_tr_b16 a[96:97], v134 offset:0\n\t" \
        "ds_read_b64_tr_b16 a[98:99], v135 offset:0\n\t" \
        "s_waitcnt lgkmcnt(14)\n\t" \
        "v_mfma_f32_16x16x32_bf16 v[130:133], a[140:143], v[166:169], 0\n\t" \
        "s_add_u32 m0, s39, 12288\n\t" \
        "s_add_u32 s57, s40, 135168\n\t" \
        "buffer_load_dwordx4 v3, s[24:27], s57 offen offset:0 lds\n\t" \
        "s_waitcnt lgkmcnt(13)\n\t" \
        "v_mfma_f32_16x16x32_bf16 v[30:33], a[72:75], v[78:81], v[30:33]\n\t" \
        "ds_read_b64_tr_b16 a[100:101], v138 offset:0\n\t" \
        "s_waitcnt lgkmcnt(12)\n\t" \
        "v_and_b32 v154, v150, v25\n\t" \
        "v_mfma_f32_16x16x32_bf16 v[54:57], a[72:75], v[82:85], v[54:57]\n\t" \
        "ds_read_b64_tr_b16 a[102:103], v139 offset:0\n\t" \
        "ds_read_b64_tr_b16 a[104:105], v136 offset:0\n\t" \
        "s_waitcnt lgkmcnt(13)\n\t" \
        "v_mfma_f32_16x16x32_bf16 v[34:37], a[76:79], v[78:81], v[34:37]\n\t" \
        "ds_read_b64_tr_b16 a[106:107], v137 offset:0\n\t" \
        "v_and_b32 v155, v150, v26\n\t" \
        "v_mfma_f32_16x16x32_bf16 v[58:61], a[76:79], v[82:85], v[58:61]\n\t" \
        "ds_read_b64_tr_b16 a[108:109], v140 offset:0\n\t" \
        "ds_read_b64_tr_b16 a[110:111], v141 offset:0\n\t" \
        "v_and_b32 v156, v150, v27\n\t" \
        "s_waitcnt lgkmcnt(13)\n\t" \
        "v_mfma_f32_16x16x32_bf16 v[38:41], a[80:83], v[78:81], v[38:41]\n\t" \
        "ds_read_b64_tr_b16 a[0:1], v6 offset:36864\n\t" \
        "v_and_b32 v157, v150, v28\n\t" \
        "v_and_b32 v158, v151, v25\n\t" \
        "v_mfma_f32_16x16x32_bf16 v[62:65], a[80:83], v[82:85], v[62:65]\n\t" \
        "buffer_load_dwordx4 v4, s[24:27], s57 offen offset:1024 lds\n\t" \
        "s_waitcnt lgkmcnt(11)\n\t" \
        "v_mfma_f32_16x16x32_bf16 v[42:45], a[84:87], v[78:81], v[42:45]\n\t" \
        "ds_read_b64_tr_b16 a[2:3], v6 offset:39936\n\t" \
        "ds_read_b64_tr_b16 a[4:5], v7 offset:36896\n\t" \
        "v_cvt_pk_bf16_f32 v87, v104, v105\n\t" \
        "v_mfma_f32_16x16x32_bf16 v[66:69], a[84:87], v[82:85], v[66:69]\n\t" \
        "ds_read_b64_tr_b16 a[6:7], v7 offset:39968\n\t" \
        "v_cvt_pk_bf16_f32 v88, v106, v107\n\t" \
        "v_and_b32 v159, v151, v26\n\t" \
        "v_mfma_f32_16x16x32_bf16 v[46:49], a[88:91], v[78:81], v[46:49]\n\t" \
        "ds_read_b64_tr_b16 a[8:9], v6 offset:36928\n\t" \
        "v_cvt_pk_bf16_f32 v89, v108, v109\n\t" \
        "v_cvt_pk_bf16_f32 v90, v110, v111\n\t" \
        "v_mfma_f32_16x16x32_bf16 v[70:73], a[88:91], v[82:85], v[70:73]\n\t" \
        "ds_read_b64_tr_b16 a[10:11], v6 offset:40000\n\t" \
        "ds_read_b64_tr_b16 a[12:13], v7 offset:36960\n\t" \
        "v_and_b32 v160, v151, v27\n\t" \
        "s_waitcnt lgkmcnt(13)\n\t" \
        "v_mfma_f32_16x16x32_bf16 v[50:53], a[92:95], v[78:81], v[50:53]\n\t" \
        "ds_read_b64_tr_b16 a[14:15], v7 offset:40032\n\t" \
        "v_cvt_pk_bf16_f32 v91, v112, v113\n\t" \
        "v_cvt_pk_bf16_f32 v92, v114, v115\n\t" \
        "v_mfma_f32_16x16x32_bf16 v[74:77], a[92:95], v[82:85], v[74:77]\n\t" \
        "buffer_load_dwordx4 v5, s[24:27], s57 offen offset:2048 lds\n\t" \
        "v_mfma_f32_16x16x32_bf16 v[102:105], a[96:99], v[154:157], 0\n\t" \
        "ds_read_b64_tr_b16 a[16:17], v6 offset:36992\n\t" \
        "ds_read_b64_tr_b16 a[18:19], v6 offset:40064\n\t" \
        "v_and_b32 v161, v151, v28\n\t" \
        "v_cvt_pk_bf16_f32 v93, v116, v117\n\t" \
        "s_waitcnt lgkmcnt(12)\n\t" \
        "v_mfma_f32_16x16x32_bf16 v[106:109], a[100:103], v[154:157], 0\n\t" \
        "ds_read_b64_tr_b16 a[20:21], v7 offset:37024\n\t" \
        "v_cvt_pk_bf16_f32 v94, v118, v119\n\t" \
        "v_mfma_f32_16x16x32_bf16 v[110:113], a[104:107], v[158:161], 0\n\t" \
        "ds_read_b64_tr_b16 a[22:23], v7 offset:40096\n\t" \
        "v_cvt_pk_bf16_f32 v95, v120, v121\n\t" \
        "s_waitcnt lgkmcnt(11)\n\t" \
        "v_mfma_f32_16x16x32_bf16 v[114:117], a[108:111], v[158:161], 0\n\t" \
        "ds_read_b64_tr_b16 a[112:113], v134 offset:64\n\t" \
        "ds_read_b64_tr_b16 a[114:115], v135 offset:64\n\t" \
        "s_waitcnt lgkmcnt(12)\n\t" \
        "v_mfma_f32_16x16x32_bf16 v[30:33], a[0:3], v[86:89], v[30:33]\n\t" \
        "ds_read_b64_tr_b16 a[116:117], v138 offset:64\n\t" \
        "v_cvt_pk_bf16_f32 v96, v122, v123\n\t" \
        "v_mfma_f32_16x16x32_bf16 v[54:57], a[0:3], v[90:93], v[54:57]\n\t" \
        "ds_read_b64_tr_b16 a[118:119], v139 offset:64\n\t" \
        "ds_read_b64_tr_b16 a[120:121], v136 offset:64\n\t" \
        "s_waitcnt lgkmcnt(13)\n\t" \
        "v_mfma_f32_16x16x32_bf16 v[34:37], a[4:7], v[86:89], v[34:37]\n\t" \
        "ds_read_b64_tr_b16 a[122:123], v137 offset:64\n\t" \
        "v_cvt_pk_bf16_f32 v97, v124, v125\n\t" \
        "v_mfma_f32_16x16x32_bf16 v[58:61], a[4:7], v[90:93], v[58:61]\n\t" \
        "ds_read_b64_tr_b16 a[124:125], v140 offset:64\n\t" \
        "v_cvt_pk_bf16_f32 v98, v126, v127\n\t" \
        "s_waitcnt lgkmcnt(12)\n\t" \
        "v_mfma_f32_16x16x32_bf16 v[38:41], a[8:11], v[86:89], v[38:41]\n\t" \
        "ds_read_b64_tr_b16 a[126:127], v141 offset:64\n\t" \
        "ds_read_b64_tr_b16 a[24:25], v6 offset:43008\n\t" \
        "v_mfma_f32_16x16x32_bf16 v[62:65], a[8:11], v[90:93], v[62:65]\n\t" \
        "ds_read_b64_tr_b16 a[26:27], v6 offset:46080\n\t" \
        "v_cvt_pk_bf16_f32 v99, v128, v129\n\t" \
        "s_waitcnt lgkmcnt(14)\n\t" \
        "v_mfma_f32_16x16x32_bf16 v[42:45], a[12:15], v[86:89], v[42:45]\n\t" \
        "ds_read_b128 v[174:177], v9 offset:896\n\t" \
        "ds_read_b64_tr_b16 a[28:29], v7 offset:43040\n\t" \
        "v_mfma_f32_16x16x32_bf16 v[66:69], a[12:15], v[90:93], v[66:69]\n\t" \
        "ds_read_b64_tr_b16 a[30:31], v7 offset:46112\n\t" \
        "v_cvt_pk_bf16_f32 v100, v130, v131\n\t" \
        "s_waitcnt lgkmcnt(14)\n\t" \
        "v_mfma_f32_16x16x32_bf16 v[46:49], a[16:19], v[86:89], v[46:49]\n\t" \
        "ds_read_b64_tr_b16 a[32:33], v6 offset:43072\n\t" \
        "ds_read_b64_tr_b16 a[34:35], v6 offset:46144\n\t" \
        "v_mfma_f32_16x16x32_bf16 v[70:73], a[16:19], v[90:93], v[70:73]\n\t" \
        "ds_read_b64_tr_b16 a[36:37], v7 offset:43104\n\t" \
        "v_cvt_pk_bf16_f32 v101, v132, v133\n\t" \
        "s_waitcnt lgkmcnt(14)\n\t" \
        "v_mfma_f32_16x16x32_bf16 v[50:53], a[20:23], v[86:89], v[50:53]\n\t" \
        "ds_read_b64_tr_b16 a[38:39], v7 offset:46176\n\t" \
        "v_cvt_pk_bf16_f32 v78, v102, v103\n\t" \
        "v_mfma_f32_16x16x32_bf16 v[74:77], a[20:23], v[90:93], v[74:77]\n\t" \
        "ds_read_b64_tr_b16 a[40:41], v6 offset:43136\n\t" \
        "ds_read_b64_tr_b16 a[42:43], v6 offset:46208\n\t" \
        "v_mfma_f32_16x16x32_bf16 v[118:121], a[112:115], v[154:157], 0\n\t" \
        "s_waitcnt vmcnt(6)\n\t" \
        "s_barrier\n\t" \
        "ds_read_b64_tr_b16 a[44:45], v7 offset:43168\n\t" \
        "v_cvt_pk_bf16_f32 v79, v104, v105\n\t" \
        "s_waitcnt lgkmcnt(14)\n\t" \
        "v_mfma_f32_16x16x32_bf16 v[122:125], a[116:119], v[154:157], 0\n\t" \
        "ds_read_b64_tr_b16 a[46:47], v7 offset:46240\n\t" \
        "ds_read_b64_tr_b16 a[128:129], v134 offset:128\n\t" \
        "v_mfma_f32_16x16x32_bf16 v[126:129], a[120:123], v[158:161], 0\n\t" \
        "ds_read_b64_tr_b16 a[130:131], v135 offset:128\n\t" \
        "v_cvt_pk_bf16_f32 v80, v106, v107\n\t" \
        "s_waitcnt lgkmcnt(14)\n\t" \
        "v_mfma_f32_16x16x32_bf16 v[130:133], a[124:127], v[158:161], 0\n\t" \
        "s_add_u32 m0, s39, 24576\n\t" \
        "s_add_u32 s57, s40, 147456\n\t" \
        "buffer_load_dwordx4 v3, s[24:27], s57 offen offset:0 lds\n\t" \
        "s_waitcnt lgkmcnt(13)\n\t" \
        "v_mfma_f32_16x16x32_bf16 v[30:33], a[24:27], v[94:97], v[30:33]\n\t" \
        "ds_read_b64_tr_b16 a[132:133], v138 offset:128\n\t" \
        "s_waitcnt lgkmcnt(12)\n\t" \
        "v_xor_b32 v142, v174, v11\n\t" \
        "v_mfma_f32_16x16x32_bf16 v[54:57], a[24:27], v[98:101], v[54:57]\n\t" \
        "ds_read_b64_tr_b16 a[134:135], v139 offset:128\n\t" \
        "ds_read_b64_tr_b16 a[136:137], v136 offset:128\n\t" \
        "s_waitcnt lgkmcnt(13)\n\t" \
        "v_mfma_f32_16x16x32_bf16 v[34:37], a[28:31], v[94:97], v[34:37]\n\t" \
        "ds_read_b64_tr_b16 a[138:139], v137 offset:128\n\t" \
        "v_xor_b32 v143, v175, v11\n\t" \
        "v_mfma_f32_16x16x32_bf16 v[58:61], a[28:31], v[98:101], v[58:61]\n\t" \
        "ds_read_b64_tr_b16 a[140:141], v140 offset:128\n\t" \
        "ds_read_b64_tr_b16 a[142:143], v141 offset:128\n\t" \
        "s_waitcnt lgkmcnt(13)\n\t" \
        "v_mfma_f32_16x16x32_bf16 v[38:41], a[32:35], v[94:97], v[38:41]\n\t" \
        "ds_read_b64_tr_b16 a[48:49], v6 offset:49152\n\t" \
        "v_xor_b32 v144, v176, v11\n\t" \
        "v_mfma_f32_16x16x32_bf16 v[62:65], a[32:35], v[98:101], v[62:65]\n\t" \
        "buffer_load_dwordx4 v4, s[24:27], s57 offen offset:1024 lds\n\t" \
        "s_waitcnt lgkmcnt(11)\n\t" \
        "v_mfma_f32_16x16x32_bf16 v[42:45], a[36:39], v[94:97], v[42:45]\n\t" \
        "ds_read_b64_tr_b16 a[50:51], v6 offset:52224\n\t" \
        "ds_read_b64_tr_b16 a[52:53], v7 offset:49184\n\t" \
        "v_xor_b32 v146, v174, v12\n\t" \
        "v_xor_b32 v145, v177, v11\n\t" \
        "v_mfma_f32_16x16x32_bf16 v[66:69], a[36:39], v[98:101], v[66:69]\n\t" \
        "ds_read_b64 v[152:153], v10 offset:896\n\t" \
        "v_xor_b32 v147, v175, v12\n\t" \
        "v_xor_b32 v148, v176, v12\n\t" \
        "v_mfma_f32_16x16x32_bf16 v[46:49], a[40:43], v[94:97], v[46:49]\n\t" \
        "ds_read_b64_tr_b16 a[54:55], v7 offset:52256\n\t" \
        "v_xor_b32 v149, v177, v12\n\t" \
        "v_mfma_f32_16x16x32_bf16 v[70:73], a[40:43], v[98:101], v[70:73]\n\t" \
        "ds_read_b64_tr_b16 a[56:57], v6 offset:49216\n\t" \
        "ds_read_b64_tr_b16 a[58:59], v6 offset:52288\n\t" \
        "v_cvt_pk_bf16_f32 v81, v108, v109\n\t" \
        "v_cvt_pk_bf16_f32 v82, v110, v111\n\t" \
        "s_waitcnt lgkmcnt(13)\n\t" \
        "v_mfma_f32_16x16x32_bf16 v[50:53], a[44:47], v[94:97], v[50:53]\n\t" \
        "ds_read_b64_tr_b16 a[60:61], v7 offset:49248\n\t" \
        "v_cvt_pk_bf16_f32 v83, v112, v113\n\t" \
        "v_cvt_pk_bf16_f32 v84, v114, v115\n\t" \
        "v_mfma_f32_16x16x32_bf16 v[74:77], a[44:47], v[98:101], v[74:77]\n\t" \
        "buffer_load_dwordx4 v5, s[24:27], s57 offen offset:2048 lds\n\t" \
        "v_mfma_f32_16x16x32_bf16 v[102:105], a[128:131], v[154:157], 0\n\t" \
        "ds_read_b64_tr_b16 a[62:63], v7 offset:52320\n\t" \
        "ds_read_b64_tr_b16 a[64:65], v6 offset:49280\n\t" \
        "v_cvt_pk_bf16_f32 v85, v116, v117\n\t" \
        "s_waitcnt lgkmcnt(12)\n\t" \
        "v_mfma_f32_16x16x32_bf16 v[106:109], a[132:135], v[154:157], 0\n\t" \
        "ds_read_b64_tr_b16 a[66:67], v6 offset:52352\n\t" \
        "v_cvt_pk_bf16_f32 v86, v118, v119\n\t" \
        "v_mfma_f32_16x16x32_bf16 v[110:113], a[136:139], v[158:161], 0\n\t" \
        "ds_read_b64_tr_b16 a[68:69], v7 offset:49312\n\t" \
        "ds_read_b64_tr_b16 a[70:71], v7 offset:52384\n\t" \
        "s_waitcnt lgkmcnt(12)\n\t" \
        "v_mfma_f32_16x16x32_bf16 v[114:117], a[140:143], v[158:161], 0\n\t" \
        "ds_read_b64_tr_b16 a[96:97], v142 offset:0\n\t" \
        "v_cvt_pk_bf16_f32 v87, v120, v121\n\t" \
        "s_waitcnt lgkmcnt(12)\n\t" \
        "v_mfma_f32_16x16x32_bf16 v[30:33], a[48:51], v[78:81], v[30:33]\n\t" \
        "ds_read_b64_tr_b16 a[98:99], v143 offset:0\n\t" \
        "s_waitcnt lgkmcnt(11)\n\t" \
        "v_and_b32 v162, v152, v25\n\t" \
        "v_mfma_f32_16x16x32_bf16 v[54:57], a[48:51], v[82:85], v[54:57]\n\t" \
        "ds_read_b64_tr_b16 a[100:101], v146 offset:0\n\t" \
        "ds_read_b64_tr_b16 a[102:103], v147 offset:0\n\t" \
        "s_waitcnt lgkmcnt(12)\n\t" \
        "v_mfma_f32_16x16x32_bf16 v[34:37], a[52:55], v[78:81], v[34:37]\n\t" \
        "ds_read_b64_tr_b16 a[104:105], v144 offset:0\n\t" \
        "v_and_b32 v163, v152, v26\n\t" \
        "v_mfma_f32_16x16x32_bf16 v[58:61], a[52:55], v[82:85], v[58:61]\n\t" \
        "ds_read_b64_tr_b16 a[106:107], v145 offset:0\n\t" \
        "ds_read_b64_tr_b16 a[108:109], v148 offset:0\n\t" \
        "s_waitcnt lgkmcnt(13)\n\t" \
        "v_mfma_f32_16x16x32_bf16 v[38:41], a[56:59], v[78:81], v[38:41]\n\t" \
        "ds_read_b64_tr_b16 a[110:111], v149 offset:0\n\t" \
        "v_and_b32 v164, v152, v27\n\t" \
        "v_mfma_f32_16x16x32_bf16 v[62:65], a[56:59], v[82:85], v[62:65]\n\t" \
        "ds_read_b64_tr_b16 a[72:73], v6 offset:55296\n\t" \
        "v_and_b32 v165, v152, v28\n\t" \
        "s_waitcnt lgkmcnt(13)\n\t" \
        "v_mfma_f32_16x16x32_bf16 v[42:45], a[60:63], v[78:81], v[42:45]\n\t" \
        "ds_read_b64_tr_b16 a[74:75], v6 offset:58368\n\t" \
        "ds_read_b64_tr_b16 a[76:77], v7 offset:55328\n\t" \
        "v_mfma_f32_16x16x32_bf16 v[66:69], a[60:63], v[82:85], v[66:69]\n\t" \
        "ds_read_b64_tr_b16 a[78:79], v7 offset:58400\n\t" \
        "v_and_b32 v166, v153, v25\n\t" \
        "s_waitcnt lgkmcnt(14)\n\t" \
        "v_mfma_f32_16x16x32_bf16 v[46:49], a[64:67], v[78:81], v[46:49]\n\t" \
        "ds_read_b64_tr_b16 a[80:81], v6 offset:55360\n\t" \
        "ds_read_b64_tr_b16 a[82:83], v6 offset:58432\n\t" \
        "v_cvt_pk_bf16_f32 v88, v122, v123\n\t" \
        "v_and_b32 v167, v153, v26\n\t" \
        "v_mfma_f32_16x16x32_bf16 v[70:73], a[64:67], v[82:85], v[70:73]\n\t" \
        "ds_read_b64_tr_b16 a[84:85], v7 offset:55392\n\t" \
        "v_cvt_pk_bf16_f32 v89, v124, v125\n\t" \
        "v_cvt_pk_bf16_f32 v90, v126, v127\n\t" \
        "s_waitcnt lgkmcnt(14)\n\t" \
        "v_mfma_f32_16x16x32_bf16 v[50:53], a[68:71], v[78:81], v[50:53]\n\t" \
        "ds_read_b64_tr_b16 a[86:87], v7 offset:58464\n\t" \
        "ds_read_b64_tr_b16 a[88:89], v6 offset:55424\n\t" \
        "v_and_b32 v168, v153, v27\n\t" \
        "v_mfma_f32_16x16x32_bf16 v[74:77], a[68:71], v[82:85], v[74:77]\n\t" \
        "ds_read_b64_tr_b16 a[90:91], v6 offset:58496\n\t" \
        "v_cvt_pk_bf16_f32 v91, v128, v129\n\t" \
        "v_cvt_pk_bf16_f32 v92, v130, v131\n\t" \
        "s_waitcnt lgkmcnt(14)\n\t" \
        "v_mfma_f32_16x16x32_bf16 v[118:121], a[96:99], v[162:165], 0\n\t" \
        "s_waitcnt vmcnt(6)\n\t" \
        "s_barrier\n\t" \
        "ds_read_b64_tr_b16 a[92:93], v7 offset:55456\n\t" \
        "v_and_b32 v169, v153, v28\n\t" \
        "v_cvt_pk_bf16_f32 v93, v132, v133\n\t" \
        "v_mfma_f32_16x16x32_bf16 v[122:125], a[100:103], v[162:165], 0\n\t" \
        "ds_read_b64_tr_b16 a[94:95], v7 offset:58528\n\t" \
        "ds_read_b64_tr_b16 a[112:113], v142 offset:64\n\t" \
        "s_waitcnt lgkmcnt(14)\n\t" \
        "v_mfma_f32_16x16x32_bf16 v[126:129], a[104:107], v[166:169], 0\n\t" \
        "ds_read_b64_tr_b16 a[114:115], v143 offset:64\n\t" \
        "v_cvt_pk_bf16_f32 v94, v102, v103\n\t" \
        "s_waitcnt lgkmcnt(14)\n\t" \
        "v_mfma_f32_16x16x32_bf16 v[130:133], a[108:111], v[166:169], 0\n\t" \
        "s_add_u32 m0, s39, 36864\n\t" \
        "s_add_u32 s57, s40, 159744\n\t" \
        "buffer_load_dwordx4 v3, s[24:27], s57 offen offset:0 lds\n\t" \
        "s_waitcnt lgkmcnt(12)\n\t" \
        "v_mfma_f32_16x16x32_bf16 v[30:33], a[72:75], v[86:89], v[30:33]\n\t" \
        "ds_read_b64_tr_b16 a[116:117], v146 offset:64\n\t" \
        "ds_read_b64_tr_b16 a[118:119], v147 offset:64\n\t" \
        "v_mfma_f32_16x16x32_bf16 v[54:57], a[72:75], v[90:93], v[54:57]\n\t" \
        "ds_read_b64_tr_b16 a[120:121], v144 offset:64\n\t" \
        "v_cvt_pk_bf16_f32 v95, v104, v105\n\t" \
        "s_waitcnt lgkmcnt(13)\n\t" \
        "v_mfma_f32_16x16x32_bf16 v[34:37], a[76:79], v[86:89], v[34:37]\n\t" \
        "ds_read_b64_tr_b16 a[122:123], v145 offset:64\n\t" \
        "v_cvt_pk_bf16_f32 v96, v106, v107\n\t" \
        "v_mfma_f32_16x16x32_bf16 v[58:61], a[76:79], v[90:93], v[58:61]\n\t" \
        "ds_read_b64_tr_b16 a[124:125], v148 offset:64\n\t" \
        "ds_read_b64_tr_b16 a[126:127], v149 offset:64\n\t" \
        "s_waitcnt lgkmcnt(13)\n\t" \
        "v_mfma_f32_16x16x32_bf16 v[38:41], a[80:83], v[86:89], v[38:41]\n\t" \
        "ds_read_b64_tr_b16 a[0:1], v6 offset:0\n\t" \
        "v_cvt_pk_bf16_f32 v97, v108, v109\n\t" \
        "v_mfma_f32_16x16x32_bf16 v[62:65], a[80:83], v[90:93], v[62:65]\n\t" \
        "buffer_load_dwordx4 v4, s[24:27], s57 offen offset:1024 lds\n\t" \
        "s_waitcnt lgkmcnt(11)\n\t" \
        "v_mfma_f32_16x16x32_bf16 v[42:45], a[84:87], v[86:89], v[42:45]\n\t" \
        "ds_read_b64_tr_b16 a[2:3], v6 offset:3072\n\t" \
        "ds_read_b128 v[170:173], v9 offset:1024\n\t" \
        "v_mfma_f32_16x16x32_bf16 v[66:69], a[84:87], v[90:93], v[66:69]\n\t" \
        "ds_read_b64_tr_b16 a[4:5], v7 offset:32\n\t" \
        "v_cvt_pk_bf16_f32 v98, v110, v111\n\t" \
        "v_mfma_f32_16x16x32_bf16 v[46:49], a[88:91], v[86:89], v[46:49]\n\t" \
        "ds_read_b64_tr_b16 a[6:7], v7 offset:3104\n\t" \
        "v_cvt_pk_bf16_f32 v99, v112, v113\n\t" \
        "v_mfma_f32_16x16x32_bf16 v[70:73], a[88:91], v[90:93], v[70:73]\n\t" \
        "ds_read_b64_tr_b16 a[8:9], v6 offset:64\n\t" \
        "ds_read_b64_tr_b16 a[10:11], v6 offset:3136\n\t" \
        "s_waitcnt lgkmcnt(13)\n\t" \
        "v_mfma_f32_16x16x32_bf16 v[50:53], a[92:95], v[86:89], v[50:53]\n\t" \
        "ds_read_b64_tr_b16 a[12:13], v7 offset:96\n\t" \
        "v_cvt_pk_bf16_f32 v100, v114, v115\n\t" \
        "v_mfma_f32_16x16x32_bf16 v[74:77], a[92:95], v[90:93], v[74:77]\n\t" \
        "buffer_load_dwordx4 v5, s[24:27], s57 offen offset:2048 lds\n\t" \
        "v_mfma_f32_16x16x32_bf16 v[102:105], a[112:115], v[162:165], 0\n\t" \
        "ds_read_b64_tr_b16 a[14:15], v7 offset:3168\n\t" \
        "ds_read_b64_tr_b16 a[16:17], v6 offset:128\n\t" \
        "v_cvt_pk_bf16_f32 v101, v116, v117\n\t" \
        "s_waitcnt lgkmcnt(12)\n\t" \
        "v_mfma_f32_16x16x32_bf16 v[106:109], a[116:119], v[162:165], 0\n\t" \
        "ds_read_b64_tr_b16 a[18:19], v6 offset:3200\n\t" \
        "v_cvt_pk_bf16_f32 v78, v118, v119\n\t" \
        "v_mfma_f32_16x16x32_bf16 v[110:113], a[120:123], v[166:169], 0\n\t" \
        "ds_read_b64_tr_b16 a[20:21], v7 offset:160\n\t" \
        "v_cvt_pk_bf16_f32 v79, v120, v121\n\t" \
        "s_waitcnt lgkmcnt(11)\n\t" \
        "v_mfma_f32_16x16x32_bf16 v[114:117], a[124:127], v[166:169], 0\n\t" \
        "ds_read_b64_tr_b16 a[22:23], v7 offset:3232\n\t" \
        "ds_read_b64_tr_b16 a[128:129], v142 offset:128\n\t" \
        "s_waitcnt lgkmcnt(12)\n\t" \
        "v_mfma_f32_16x16x32_bf16 v[30:33], a[0:3], v[94:97], v[30:33]\n\t" \
        "ds_read_b64_tr_b16 a[130:131], v143 offset:128\n\t" \
        "s_waitcnt lgkmcnt(12)\n\t" \
        "v_xor_b32 v134, v170, v11\n\t" \
        "v_mfma_f32_16x16x32_bf16 v[54:57], a[0:3], v[98:101], v[54:57]\n\t" \
        "ds_read_b64_tr_b16 a[132:133], v146 offset:128\n\t" \
        "ds_read_b64_tr_b16 a[134:135], v147 offset:128\n\t" \
        "s_waitcnt lgkmcnt(12)\n\t" \
        "v_mfma_f32_16x16x32_bf16 v[34:37], a[4:7], v[94:97], v[34:37]\n\t" \
        "ds_read_b64_tr_b16 a[136:137], v144 offset:128\n\t" \
        "v_xor_b32 v135, v171, v11\n\t" \
        "v_mfma_f32_16x16x32_bf16 v[58:61], a[4:7], v[98:101], v[58:61]\n\t" \
        "ds_read_b64_tr_b16 a[138:139], v145 offset:128\n\t" \
        "v_xor_b32 v136, v172, v11\n\t" \
        "s_waitcnt lgkmcnt(12)\n\t" \
        "v_mfma_f32_16x16x32_bf16 v[38:41], a[8:11], v[94:97], v[38:41]\n\t" \
        "ds_read_b64_tr_b16 a[140:141], v148 offset:128\n\t" \
        "ds_read_b64_tr_b16 a[142:143], v149 offset:128\n\t" \
        "v_mfma_f32_16x16x32_bf16 v[62:65], a[8:11], v[98:101], v[62:65]\n\t" \
        "ds_read_b64_tr_b16 a[24:25], v6 offset:6144\n\t" \
        "v_xor_b32 v138, v170, v12\n\t" \
        "s_waitcnt lgkmcnt(13)\n\t" \
        "v_mfma_f32_16x16x32_bf16 v[42:45], a[12:15], v[94:97], v[42:45]\n\t" \
        "ds_read_b64_tr_b16 a[26:27], v6 offset:9216\n\t" \
        "v_xor_b32 v137, v173, v11\n\t" \
        "v_mfma_f32_16x16x32_bf16 v[66:69], a[12:15], v[98:101], v[66:69]\n\t" \
        "ds_read_b64_tr_b16 a[28:29], v7 offset:6176\n\t" \
        "ds_read_b64 v[150:151], v10 offset:1024\n\t" \
        "v_xor_b32 v139, v171, v12\n\t" \
        "s_waitcnt lgkmcnt(14)\n\t" \
        "v_mfma_f32_16x16x32_bf16 v[46:49], a[16:19], v[94:97], v[46:49]\n\t" \
        "ds_read_b64_tr_b16 a[30:31], v7 offset:9248\n\t" \
        "v_xor_b32 v140, v172, v12\n\t" \
        "v_xor_b32 v141, v173, v12\n\t" \
        "v_mfma_f32_16x16x32_bf16 v[70:73], a[16:19], v[98:101], v[70:73]\n\t" \
        "ds_read_b64_tr_b16 a[32:33], v6 offset:6208\n\t" \
        "v_cvt_pk_bf16_f32 v80, v122, v123\n\t" \
        "s_waitcnt lgkmcnt(13)\n\t" \
        "v_mfma_f32_16x16x32_bf16 v[50:53], a[20:23], v[94:97], v[50:53]\n\t" \
        "ds_read_b64_tr_b16 a[34:35], v6 offset:9280\n\t" \
        "ds_read_b64_tr_b16 a[36:37], v7 offset:6240\n\t" \
        "v_cvt_pk_bf16_f32 v81, v124, v125\n\t" \
        "v_cvt_pk_bf16_f32 v82, v126, v127\n\t" \
        "v_mfma_f32_16x16x32_bf16 v[74:77], a[20:23], v[98:101], v[74:77]\n\t" \
        "ds_read_b64_tr_b16 a[38:39], v7 offset:9312\n\t" \
        "v_cvt_pk_bf16_f32 v83, v128, v129\n\t" \
        "v_cvt_pk_bf16_f32 v84, v130, v131\n\t" \
        "s_waitcnt lgkmcnt(13)\n\t" \
        "v_mfma_f32_16x16x32_bf16 v[118:121], a[128:131], v[162:165], 0\n\t" \
        "s_waitcnt vmcnt(6)\n\t" \
        "s_barrier\n\t" \
        "ds_read_b64_tr_b16 a[40:41], v6 offset:6272\n\t" \
        "ds_read_b64_tr_b16 a[42:43], v6 offset:9344\n\t" \
        "v_cvt_pk_bf16_f32 v85, v132, v133\n\t" \
        "v_mfma_f32_16x16x32_bf16 v[122:125], a[132:135], v[162:165], 0\n\t" \
        "ds_read_b64_tr_b16 a[44:45], v7 offset:6304\n\t" \
        "v_cvt_pk_bf16_f32 v86, v102, v103\n\t" \
        "s_waitcnt lgkmcnt(14)\n\t" \
        "v_mfma_f32_16x16x32_bf16 v[126:129], a[136:139], v[166:169], 0\n\t" \
        "ds_read_b64_tr_b16 a[46:47], v7 offset:9376\n\t" \
        "v_cvt_pk_bf16_f32 v87, v104, v105\n\t" \
        "s_waitcnt lgkmcnt(13)\n\t" \
        "v_mfma_f32_16x16x32_bf16 v[130:133], a[140:143], v[166:169], 0\n\t" \
        "ds_read_b64_tr_b16 a[96:97], v134 offset:0\n\t" \
        "ds_read_b64_tr_b16 a[98:99], v135 offset:0\n\t" \
        "s_waitcnt lgkmcnt(13)\n\t" \
        "v_mfma_f32_16x16x32_bf16 v[30:33], a[24:27], v[78:81], v[30:33]\n\t" \
        "ds_read_b64_tr_b16 a[100:101], v138 offset:0\n\t" \
        "s_waitcnt lgkmcnt(12)\n\t" \
        "v_and_b32 v154, v150, v25\n\t" \
        "v_mfma_f32_16x16x32_bf16 v[54:57], a[24:27], v[82:85], v[54:57]\n\t" \
        "ds_read_b64_tr_b16 a[102:103], v139 offset:0\n\t" \
        "ds_read_b64_tr_b16 a[104:105], v136 offset:0\n\t" \
        "s_waitcnt lgkmcnt(13)\n\t" \
        "v_mfma_f32_16x16x32_bf16 v[34:37], a[28:31], v[78:81], v[34:37]\n\t" \
        "ds_read_b64_tr_b16 a[106:107], v137 offset:0\n\t" \
        "v_and_b32 v155, v150, v26\n\t" \
        "v_mfma_f32_16x16x32_bf16 v[58:61], a[28:31], v[82:85], v[58:61]\n\t" \
        "ds_read_b64_tr_b16 a[108:109], v140 offset:0\n\t" \
        "v_and_b32 v156, v150, v27\n\t" \
        "s_waitcnt lgkmcnt(13)\n\t" \
        "v_mfma_f32_16x16x32_bf16 v[38:41], a[32:35], v[78:81], v[38:41]\n\t" \
        "ds_read_b64_tr_b16 a[110:111], v141 offset:0\n\t" \
        "ds_read_b64_tr_b16 a[48:49], v6 offset:12288\n\t" \
        "v_mfma_f32_16x16x32_bf16 v[62:65], a[32:35], v[82:85], v[62:65]\n\t" \
        "ds_read_b64_tr_b16 a[50:51], v6 offset:15360\n\t" \
        "v_and_b32 v157, v150, v28\n\t" \
        "s_waitcnt lgkmcnt(14)\n\t" \
        "v_mfma_f32_16x16x32_bf16 v[42:45], a[36:39], v[78:81], v[42:45]\n\t" \
        "ds_read_b64_tr_b16 a[52:53], v7 offset:12320\n\t" \
        "ds_read_b64_tr_b16 a[54:55], v7 offset:15392\n\t" \
        "v_mfma_f32_16x16x32_bf16 v[66:69], a[36:39], v[82:85], v[66:69]\n\t" \
        "ds_read_b64_tr_b16 a[56:57], v6 offset:12352\n\t" \
        "v_and_b32 v158, v151, v25\n\t" \
        "s_waitcnt lgkmcnt(14)\n\t" \
        "v_mfma_f32_16x16x32_bf16 v[46:49], a[40:43], v[78:81], v[46:49]\n\t" \
        "ds_read_b64_tr_b16 a[58:59], v6 offset:15424\n\t" \
        "ds_read_b64_tr_b16 a[60:61], v7 offset:12384\n\t" \
        "v_cvt_pk_bf16_f32 v88, v106, v107\n\t" \
        "v_and_b32 v159, v151, v26\n\t" \
        "v_mfma_f32_16x16x32_bf16 v[70:73], a[40:43], v[82:85], v[70:73]\n\t" \
        "ds_read_b64_tr_b16 a[62:63], v7 offset:15456\n\t" \
        "v_cvt_pk_bf16_f32 v89, v108, v109\n\t" \
        "v_cvt_pk_bf16_f32 v90, v110, v111\n\t" \
        "s_waitcnt lgkmcnt(14)\n\t" \
        "v_mfma_f32_16x16x32_bf16 v[50:53], a[44:47], v[78:81], v[50:53]\n\t" \
        "ds_read_b64_tr_b16 a[64:65], v6 offset:12416\n\t" \
        "ds_read_b64_tr_b16 a[66:67], v6 offset:15488\n\t" \
        "v_and_b32 v160, v151, v27\n\t" \
        "v_mfma_f32_16x16x32_bf16 v[74:77], a[44:47], v[82:85], v[74:77]\n\t" \
        "ds_read_b64_tr_b16 a[68:69], v7 offset:12448\n\t" \
        "v_cvt_pk_bf16_f32 v91, v112, v113\n\t" \
        "v_cvt_pk_bf16_f32 v92, v114, v115\n\t" \
        "v_mfma_f32_16x16x32_bf16 v[102:105], a[96:99], v[154:157], 0\n\t" \
        "ds_read_b64_tr_b16 a[70:71], v7 offset:15520\n\t" \
        "ds_read_b64_tr_b16 a[112:113], v134 offset:64\n\t" \
        "v_and_b32 v161, v151, v28\n\t" \
        "v_cvt_pk_bf16_f32 v93, v116, v117\n\t" \
        "s_waitcnt lgkmcnt(15)\n\t" \
        "v_mfma_f32_16x16x32_bf16 v[106:109], a[100:103], v[154:157], 0\n\t" \
        "ds_read_b64_tr_b16 a[114:115], v135 offset:64\n\t" \
        "v_cvt_pk_bf16_f32 v94, v118, v119\n\t" \
        "v_mfma_f32_16x16x32_bf16 v[110:113], a[104:107], v[158:161], 0\n\t" \
        "ds_read_b64_tr_b16 a[116:117], v138 offset:64\n\t" \
        "ds_read_b64_tr_b16 a[118:119], v139 offset:64\n\t" \
        "s_waitcnt lgkmcnt(15)\n\t" \
        "v_mfma_f32_16x16x32_bf16 v[114:117], a[108:111], v[158:161], 0\n\t" \
        "ds_read_b64_tr_b16 a[120:121], v136 offset:64\n\t" \
        "v_cvt_pk_bf16_f32 v95, v120, v121\n\t" \
        "s_waitcnt lgkmcnt(15)\n\t" \
        "v_mfma_f32_16x16x32_bf16 v[30:33], a[48:51], v[86:89], v[30:33]\n\t" \
        "ds_read_b64_tr_b16 a[122:123], v137 offset:64\n\t" \
        "ds_read_b64_tr_b16 a[124:125], v140 offset:64\n\t" \
        "v_mfma_f32_16x16x32_bf16 v[54:57], a[48:51], v[90:93], v[54:57]\n\t" \
        "ds_read_b64_tr_b16 a[126:127], v141 offset:64\n\t" \
        "v_cvt_pk_bf16_f32 v96, v122, v123\n\t" \
        "s_waitcnt lgkmcnt(15)\n\t" \
        "v_mfma_f32_16x16x32_bf16 v[34:37], a[52:55], v[86:89], v[34:37]\n\t" \
        "ds_read_b64_tr_b16 a[72:73], v6 offset:18432\n\t" \
        "ds_read_b64_tr_b16 a[74:75], v6 offset:21504\n\t" \
        "v_mfma_f32_16x16x32_bf16 v[58:61], a[52:55], v[90:93], v[58:61]\n\t" \
        "ds_read_b64_tr_b16 a[76:77], v7 offset:18464\n\t" \
        "v_cvt_pk_bf16_f32 v97, v124, v125\n\t" \
        "s_waitcnt lgkmcnt(15)\n\t" \
        "v_mfma_f32_16x16x32_bf16 v[38:41], a[56:59], v[86:89], v[38:41]\n\t" \
        "ds_read_b64_tr_b16 a[78:79], v7 offset:21536\n\t" \
        "ds_read_b64_tr_b16 a[80:81], v6 offset:18496\n\t" \
        "v_mfma_f32_16x16x32_bf16 v[62:65], a[56:59], v[90:93], v[62:65]\n\t" \
        "ds_read_b64_tr_b16 a[82:83], v6 offset:21568\n\t" \
        "v_cvt_pk_bf16_f32 v98, v126, v127\n\t" \
        "v_mfma_f32_16x16x32_bf16 v[42:45], a[60:63], v[86:89], v[42:45]\n\t" \
        "ds_read_b64_tr_b16 a[84:85], v7 offset:18528\n\t" \
        "ds_read_b64_tr_b16 a[86:87], v7 offset:21600\n\t" \
        "v_mfma_f32_16x16x32_bf16 v[66:69], a[60:63], v[90:93], v[66:69]\n\t" \
        "ds_read_b64_tr_b16 a[88:89], v6 offset:18560\n\t" \
        "v_cvt_pk_bf16_f32 v99, v128, v129\n\t" \
        "s_waitcnt lgkmcnt(15)\n\t" \
        "v_mfma_f32_16x16x32_bf16 v[46:49], a[64:67], v[86:89], v[46:49]\n\t" \
        "ds_read_b64_tr_b16 a[90:91], v6 offset:21632\n\t" \
        "ds_read_b64_tr_b16 a[92:93], v7 offset:18592\n\t" \
        "v_mfma_f32_16x16x32_bf16 v[70:73], a[64:67], v[90:93], v[70:73]\n\t" \
        "ds_read_b64_tr_b16 a[94:95], v7 offset:21664\n\t" \
        "v_cvt_pk_bf16_f32 v100, v130, v131\n\t" \
        "v_mfma_f32_16x16x32_bf16 v[50:53], a[68:71], v[86:89], v[50:53]\n\t" \
        "ds_read_b64_tr_b16 a[128:129], v134 offset:128\n\t" \
        "v_cvt_pk_bf16_f32 v101, v132, v133\n\t" \
        "v_mfma_f32_16x16x32_bf16 v[74:77], a[68:71], v[90:93], v[74:77]\n\t" \
        "ds_read_b64_tr_b16 a[130:131], v135 offset:128\n\t" \
        "ds_read_b64_tr_b16 a[132:133], v138 offset:128\n\t" \
        "v_mfma_f32_16x16x32_bf16 v[118:121], a[112:115], v[154:157], 0\n\t" \
        "s_waitcnt vmcnt(3)\n\t" \
        "s_barrier\n\t" \
        "ds_read_b64_tr_b16 a[134:135], v139 offset:128\n\t" \
        "v_cvt_pk_bf16_f32 v78, v102, v103\n\t" \
        "s_waitcnt lgkmcnt(14)\n\t" \
        "v_mfma_f32_16x16x32_bf16 v[122:125], a[116:119], v[154:157], 0\n\t" \
        "ds_read_b64_tr_b16 a[136:137], v136 offset:128\n\t" \
        "ds_read_b64_tr_b16 a[138:139], v137 offset:128\n\t" \
        "v_mfma_f32_16x16x32_bf16 v[126:129], a[120:123], v[158:161], 0\n\t" \
        "ds_read_b64_tr_b16 a[140:141], v140 offset:128\n\t" \
        "v_cvt_pk_bf16_f32 v79, v104, v105\n\t" \
        "v_mfma_f32_16x16x32_bf16 v[130:133], a[124:127], v[158:161], 0\n\t" \
        "ds_read_b64_tr_b16 a[142:143], v141 offset:128\n\t" \
        "ds_read_b64_tr_b16 a[0:1], v6 offset:24576\n\t" \
        "v_mfma_f32_16x16x32_bf16 v[30:33], a[72:75], v[94:97], v[30:33]\n\t" \
        "ds_read_b64_tr_b16 a[2:3], v6 offset:27648\n\t" \
        "v_cvt_pk_bf16_f32 v80, v106, v107\n\t" \
        "v_mfma_f32_16x16x32_bf16 v[54:57], a[72:75], v[98:101], v[54:57]\n\t" \
        "ds_read_b64_tr_b16 a[4:5], v7 offset:24608\n\t" \
        "ds_read_b64_tr_b16 a[6:7], v7 offset:27680\n\t" \
        "s_waitcnt lgkmcnt(15)\n\t" \
        "v_mfma_f32_16x16x32_bf16 v[34:37], a[76:79], v[94:97], v[34:37]\n\t" \
        "ds_read_b64_tr_b16 a[8:9], v6 offset:24640\n\t" \
        "v_cvt_pk_bf16_f32 v81, v108, v109\n\t" \
        "v_mfma_f32_16x16x32_bf16 v[58:61], a[76:79], v[98:101], v[58:61]\n\t" \
        "ds_read_b64_tr_b16 a[10:11], v6 offset:27712\n\t" \
        "ds_read_b64_tr_b16 a[12:13], v7 offset:24672\n\t" \
        "v_mfma_f32_16x16x32_bf16 v[38:41], a[80:83], v[94:97], v[38:41]\n\t" \
        "ds_read_b64_tr_b16 a[14:15], v7 offset:27744\n\t" \
        "v_cvt_pk_bf16_f32 v82, v110, v111\n\t" \
        "v_mfma_f32_16x16x32_bf16 v[62:65], a[80:83], v[98:101], v[62:65]\n\t" \
        "ds_read_b64_tr_b16 a[16:17], v6 offset:24704\n\t" \
        "ds_read_b64_tr_b16 a[18:19], v6 offset:27776\n\t" \
        "v_mfma_f32_16x16x32_bf16 v[42:45], a[84:87], v[94:97], v[42:45]\n\t" \
        "ds_read_b64_tr_b16 a[20:21], v7 offset:24736\n\t" \
        "v_cvt_pk_bf16_f32 v83, v112, v113\n\t" \
        "v_mfma_f32_16x16x32_bf16 v[66:69], a[84:87], v[98:101], v[66:69]\n\t" \
        "ds_read_b64_tr_b16 a[22:23], v7 offset:27808\n\t" \
        "ds_read_b64_tr_b16 a[24:25], v6 offset:30720\n\t" \
        "s_waitcnt lgkmcnt(15)\n\t" \
        "v_mfma_f32_16x16x32_bf16 v[46:49], a[88:91], v[94:97], v[46:49]\n\t" \
        "s_waitcnt vmcnt(0)\n\t" \
        "s_barrier\n\t" \
        "ds_read_b64_tr_b16 a[26:27], v6 offset:33792\n\t" \
        "v_cvt_pk_bf16_f32 v84, v114, v115\n\t" \
        "v_mfma_f32_16x16x32_bf16 v[70:73], a[88:91], v[98:101], v[70:73]\n\t" \
        "ds_read_b64_tr_b16 a[28:29], v7 offset:30752\n\t" \
        "ds_read_b64_tr_b16 a[30:31], v7 offset:33824\n\t" \
        "v_mfma_f32_16x16x32_bf16 v[50:53], a[92:95], v[94:97], v[50:53]\n\t" \
        "ds_read_b64_tr_b16 a[32:33], v6 offset:30784\n\t" \
        "v_cvt_pk_bf16_f32 v85, v116, v117\n\t" \
        "v_mfma_f32_16x16x32_bf16 v[74:77], a[92:95], v[98:101], v[74:77]\n\t" \
        "ds_read_b64_tr_b16 a[34:35], v6 offset:33856\n\t" \
        "ds_read_b64_tr_b16 a[36:37], v7 offset:30816\n\t" \
        "v_mfma_f32_16x16x32_bf16 v[102:105], a[128:131], v[154:157], 0\n\t" \
        "ds_read_b64_tr_b16 a[38:39], v7 offset:33888\n\t" \
        "v_cvt_pk_bf16_f32 v86, v118, v119\n\t" \
        "v_mfma_f32_16x16x32_bf16 v[106:109], a[132:135], v[154:157], 0\n\t" \
        "ds_read_b64_tr_b16 a[40:41], v6 offset:30848\n\t" \
        "ds_read_b64_tr_b16 a[42:43], v6 offset:33920\n\t" \
        "v_mfma_f32_16x16x32_bf16 v[110:113], a[136:139], v[158:161], 0\n\t" \
        "ds_read_b64_tr_b16 a[44:45], v7 offset:30880\n\t" \
        "v_cvt_pk_bf16_f32 v88, v122, v123\n\t" \
        "s_waitcnt lgkmcnt(15)\n\t" \
        "v_mfma_f32_16x16x32_bf16 v[114:117], a[140:143], v[158:161], 0\n\t" \
        "ds_read_b64_tr_b16 a[46:47], v7 offset:33952\n\t" \
        "ds_read_b64_tr_b16 a[48:49], v6 offset:36864\n\t" \
        "v_mfma_f32_16x16x32_bf16 v[30:33], a[0:3], v[78:81], v[30:33]\n\t" \
        "ds_read_b64_tr_b16 a[50:51], v6 offset:39936\n\t" \
        "v_cvt_pk_bf16_f32 v87, v120, v121\n\t" \
        "v_mfma_f32_16x16x32_bf16 v[34:37], a[4:7], v[78:81], v[34:37]\n\t" \
        "ds_read_b64_tr_b16 a[52:53], v7 offset:36896\n\t" \
        "ds_read_b64_tr_b16 a[54:55], v7 offset:39968\n\t" \
        "v_mfma_f32_16x16x32_bf16 v[38:41], a[8:11], v[78:81], v[38:41]\n\t" \
        "ds_read_b64_tr_b16 a[56:57], v6 offset:36928\n\t" \
        "v_cvt_pk_bf16_f32 v89, v124, v125\n\t" \
        "v_mfma_f32_16x16x32_bf16 v[42:45], a[12:15], v[78:81], v[42:45]\n\t" \
        "ds_read_b64_tr_b16 a[58:59], v6 offset:40000\n\t" \
        "v_cvt_pk_bf16_f32 v94, v102, v103\n\t" \
        "s_waitcnt lgkmcnt(14)\n\t" \
        "v_mfma_f32_16x16x32_bf16 v[46:49], a[16:19], v[78:81], v[46:49]\n\t" \
        "ds_read_b64_tr_b16 a[60:61], v7 offset:36960\n\t" \
        "ds_read_b64_tr_b16 a[62:63], v7 offset:40032\n\t" \
        "v_mfma_f32_16x16x32_bf16 v[50:53], a[20:23], v[78:81], v[50:53]\n\t" \
        "ds_read_b64_tr_b16 a[64:65], v6 offset:36992\n\t" \
        "ds_read_b64_tr_b16 a[66:67], v6 offset:40064\n\t" \
        "v_mfma_f32_16x16x32_bf16 v[30:33], a[24:27], v[86:89], v[30:33]\n\t" \
        "ds_read_b64_tr_b16 a[68:69], v7 offset:37024\n\t" \
        "v_cvt_pk_bf16_f32 v96, v106, v107\n\t" \
        "v_mfma_f32_16x16x32_bf16 v[34:37], a[28:31], v[86:89], v[34:37]\n\t" \
        "ds_read_b64_tr_b16 a[70:71], v7 offset:40096\n\t" \
        "v_cvt_pk_bf16_f32 v95, v104, v105\n\t" \
        "s_waitcnt lgkmcnt(14)\n\t" \
        "v_mfma_f32_16x16x32_bf16 v[38:41], a[32:35], v[86:89], v[38:41]\n\t" \
        "v_cvt_pk_bf16_f32 v97, v108, v109\n\t" \
        "v_cvt_pk_bf16_f32 v90, v126, v127\n\t" \
        "v_mfma_f32_16x16x32_bf16 v[42:45], a[36:39], v[86:89], v[42:45]\n\t" \
        "v_cvt_pk_bf16_f32 v92, v130, v131\n\t" \
        "v_cvt_pk_bf16_f32 v91, v128, v129\n\t" \
        "v_mfma_f32_16x16x32_bf16 v[46:49], a[40:43], v[86:89], v[46:49]\n\t" \
        "v_cvt_pk_bf16_f32 v93, v132, v133\n\t" \
        "v_cvt_pk_bf16_f32 v98, v110, v111\n\t" \
        "s_waitcnt lgkmcnt(10)\n\t" \
        "v_mfma_f32_16x16x32_bf16 v[50:53], a[44:47], v[86:89], v[50:53]\n\t" \
        "v_cvt_pk_bf16_f32 v100, v114, v115\n\t" \
        "v_cvt_pk_bf16_f32 v99, v112, v113\n\t" \
        "v_mfma_f32_16x16x32_bf16 v[30:33], a[48:51], v[94:97], v[30:33]\n\t" \
        "v_cvt_pk_bf16_f32 v101, v116, v117\n\t" \
        "s_waitcnt lgkmcnt(7)\n\t" \
        "v_mfma_f32_16x16x32_bf16 v[34:37], a[52:55], v[94:97], v[34:37]\n\t" \
        "s_waitcnt lgkmcnt(6)\n\t" \
        "v_mfma_f32_16x16x32_bf16 v[38:41], a[56:59], v[94:97], v[38:41]\n\t" \
        "s_waitcnt lgkmcnt(4)\n\t" \
        "v_mfma_f32_16x16x32_bf16 v[42:45], a[60:63], v[94:97], v[42:45]\n\t" \
        "s_waitcnt lgkmcnt(2)\n\t" \
        "v_mfma_f32_16x16x32_bf16 v[46:49], a[64:67], v[94:97], v[46:49]\n\t" \
        "s_waitcnt lgkmcnt(0)\n\t" \
        "v_mfma_f32_16x16x32_bf16 v[50:53], a[68:71], v[94:97], v[50:53]\n\t" \
        "v_cvt_pk_bf16_f32 v30, v30, v31\n\t" \
        "v_mfma_f32_16x16x32_bf16 v[54:57], a[0:3], v[82:85], v[54:57]\n\t" \
        "v_cvt_pk_bf16_f32 v31, v32, v33\n\t" \
        "v_cvt_pk_bf16_f32 v32, v34, v35\n\t" \
        "v_mfma_f32_16x16x32_bf16 v[58:61], a[4:7], v[82:85], v[58:61]\n\t" \
        "v_cvt_pk_bf16_f32 v33, v36, v37\n\t" \
        "v_mfma_f32_16x16x32_bf16 v[62:65], a[8:11], v[82:85], v[62:65]\n\t" \
        "v_permlane16_swap_b32 v30, v32\n\t" \
        "v_mfma_f32_16x16x32_bf16 v[66:69], a[12:15], v[82:85], v[66:69]\n\t" \
        "v_permlane16_swap_b32 v31, v33\n\t" \
        "v_mfma_f32_16x16x32_bf16 v[70:73], a[16:19], v[82:85], v[70:73]\n\t" \
        "v_mfma_f32_16x16x32_bf16 v[74:77], a[20:23], v[82:85], v[74:77]\n\t" \
        "buffer_store_dwordx4 v[30:33], v8, s[28:31], s59 offen offset:0 sc1\n\t" \
        "v_mfma_f32_16x16x32_bf16 v[54:57], a[24:27], v[90:93], v[54:57]\n\t" \
        "v_cvt_pk_bf16_f32 v38, v38, v39\n\t" \
        "v_mfma_f32_16x16x32_bf16 v[58:61], a[28:31], v[90:93], v[58:61]\n\t" \
        "v_cvt_pk_bf16_f32 v39, v40, v41\n\t" \
        "v_cvt_pk_bf16_f32 v40, v42, v43\n\t" \
        "v_mfma_f32_16x16x32_bf16 v[62:65], a[32:35], v[90:93], v[62:65]\n\t" \
        "v_cvt_pk_bf16_f32 v41, v44, v45\n\t" \
        "v_mfma_f32_16x16x32_bf16 v[66:69], a[36:39], v[90:93], v[66:69]\n\t" \
        "v_permlane16_swap_b32 v38, v40\n\t" \
        "v_mfma_f32_16x16x32_bf16 v[70:73], a[40:43], v[90:93], v[70:73]\n\t" \
        "v_permlane16_swap_b32 v39, v41\n\t" \
        "v_mfma_f32_16x16x32_bf16 v[74:77], a[44:47], v[90:93], v[74:77]\n\t" \
        "v_mfma_f32_16x16x32_bf16 v[54:57], a[48:51], v[98:101], v[54:57]\n\t" \
        "buffer_store_dwordx4 v[38:41], v8, s[28:31], s59 offen offset:64 sc1\n\t" \
        "v_mfma_f32_16x16x32_bf16 v[58:61], a[52:55], v[98:101], v[58:61]\n\t" \
        "v_cvt_pk_bf16_f32 v46, v46, v47\n\t" \
        "v_mfma_f32_16x16x32_bf16 v[62:65], a[56:59], v[98:101], v[62:65]\n\t" \
        "v_cvt_pk_bf16_f32 v47, v48, v49\n\t" \
        "v_cvt_pk_bf16_f32 v48, v50, v51\n\t" \
        "v_mfma_f32_16x16x32_bf16 v[66:69], a[60:63], v[98:101], v[66:69]\n\t" \
        "v_cvt_pk_bf16_f32 v49, v52, v53\n\t" \
        "v_mfma_f32_16x16x32_bf16 v[70:73], a[64:67], v[98:101], v[70:73]\n\t" \
        "v_permlane16_swap_b32 v46, v48\n\t" \
        "v_mfma_f32_16x16x32_bf16 v[74:77], a[68:71], v[98:101], v[74:77]\n\t" \
        "v_permlane16_swap_b32 v47, v49\n\t" \
        "buffer_store_dwordx4 v[46:49], v8, s[28:31], s59 offen offset:128 sc1\n\t" \
        "v_cvt_pk_bf16_f32 v54, v54, v55\n\t" \
        "v_cvt_pk_bf16_f32 v55, v56, v57\n\t" \
        "v_cvt_pk_bf16_f32 v56, v58, v59\n\t" \
        "v_cvt_pk_bf16_f32 v57, v60, v61\n\t" \
        "s_nop 0\n\t" \
        "v_permlane16_swap_b32 v54, v56\n\t" \
        "v_permlane16_swap_b32 v55, v57\n\t" \
        "buffer_store_dwordx4 v[54:57], v8, s[28:31], s60 offen offset:0 sc1\n\t" \
        "v_cvt_pk_bf16_f32 v62, v62, v63\n\t" \
        "v_cvt_pk_bf16_f32 v63, v64, v65\n\t" \
        "v_cvt_pk_bf16_f32 v64, v66, v67\n\t" \
        "v_cvt_pk_bf16_f32 v65, v68, v69\n\t" \
        "s_nop 0\n\t" \
        "v_permlane16_swap_b32 v62, v64\n\t" \
        "v_permlane16_swap_b32 v63, v65\n\t" \
        "buffer_store_dwordx4 v[62:65], v8, s[28:31], s60 offen offset:64 sc1\n\t" \
        "v_cvt_pk_bf16_f32 v70, v70, v71\n\t" \
        "v_cvt_pk_bf16_f32 v71, v72, v73\n\t" \
        "v_cvt_pk_bf16_f32 v72, v74, v75\n\t" \
        "v_cvt_pk_bf16_f32 v73, v76, v77\n\t" \
        "s_nop 0\n\t" \
        "v_permlane16_swap_b32 v70, v72\n\t" \
        "v_permlane16_swap_b32 v71, v73\n\t" \
        "buffer_store_dwordx4 v[70:73], v8, s[28:31], s60 offen offset:128 sc1\n\t" \
        "s_endpgm\n\t" \
        ".LH4_start%=:\n\t" \
        "s_add_u32 s57, s36, 0x15000\n\t" \
        "s_add_u32 m0, s38, 0x3e00\n\t" \
        "s_nop 0\n\t" \
        "buffer_load_dwordx4 v13, s[16:19], s57 offen sc1 lds\n\t" \
        "s_add_u32 s57, s36, 0x1f800\n\t" \
        "s_add_u32 m0, s38, 0x4d80\n\t" \
        "s_nop 0\n\t" \
        "buffer_load_dwordx4 v13, s[16:19], s57 offen sc1 lds\n\t" \
        "s_mov_b32 s62, 0\n\t" \
        "s_mov_b32 s63, -1\n\t" \
        "v_cmp_lt_u32 vcc, 47, v2\n\t" \
        "v_lshlrev_b32 v162, 4, v2\n\t" \
        "v_xor_b32 v163, 32, v162\n\t" \
        "v_cndmask_b32 v3, v162, v163, vcc\n\t" \
        "v_cndmask_b32 v4, v163, v162, s[62:63]\n\t" \
        "v_cmp_lt_u32 vcc, 15, v2\n\t" \
        "s_nop 1\n\t" \
        "v_cndmask_b32 v5, v162, v163, vcc\n\t" \
        "s_add_u32 s57, s36, 0x2a000\n\t" \
        "s_min_u32 s57, s57, 0x925800\n\t" \
        "s_add_u32 m0, s38, 0x5d00\n\t" \
        "s_nop 0\n\t" \
        "buffer_load_dwordx4 v13, s[16:19], s57 offen sc1 lds\n\t" \
        "s_mul_i32 s40, s35, 0xc00\n\t" \
        "s_add_u32 s39, s40, 0xba40\n\t" \
        "s_mov_b32 s42, 0xffff0000\n\t" \
        "s_mov_b32 s43, 0xc0\n\t" \
        "s_mov_b32 s44, 0xf80\n\t" \
        "s_mov_b32 s45, 0x435f435f\n\t" \
        "v_add_u32 v162, s32, v18\n\t" \
        "v_add_u32 v163, s33, v19\n\t" \
        "v_add_u32 v162, -1, v162\n\t" \
        "v_add_u32 v163, -1, v163\n\t" \
        "s_add_u32 s57, s36, 0x34800\n\t" \
        "s_min_u32 s57, s57, 0x925800\n\t" \
        "s_add_u32 m0, s38, 0x6c80\n\t" \
        "s_nop 0\n\t" \
        "buffer_load_dwordx4 v13, s[16:19], s57 offen sc1 lds\n\t" \
        "v_cndmask_b32 v161, 0, 1.0, s[62:63]\n\t" \
        "v_cvt_f32_i32 v55, v162\n\t" \
        "v_cvt_f32_i32 v62, v163\n\t" \
        "v_add_f32 v56, v55, v161\n\t" \
        "v_add_f32 v59, v62, v161\n\t" \
        "v_add_f32 v57, 1.0, v55\n\t" \
        "v_add_f32 v62, 2.0, v62\n\t" \
        "v_add_f32 v58, 2.0, v55\n\t" \
        "v_add_f32 v61, 1.0, v59\n\t" \
        "v_fma_f32 v60, v161, -2.0, v62\n\t" \
        "s_mov_b32 s57, s40\n\t" \
        "s_mov_b32 m0, s39\n\t" \
        "s_nop 0\n\t" \
        "buffer_load_dwordx4 v3, s[24:27], s57 offen offset:0 lds\n\t" \
        "v_bfe_u32 v160, v2, 4, 1\n\t" \
        "v_lshrrev_b32 v162, 2, v19\n\t" \
        "v_and_b32 v163, 1, v19\n\t" \
        "v_lshlrev_b32 v162, 5, v162\n\t" \
        "v_lshl_add_u32 v162, v163, 4, v162\n\t" \
        "v_and_b32 v163, 2, v19\n\t" \
        "v_lshl_add_u32 v162, v160, 3, v162\n\t" \
        "v_lshlrev_b32 v161, 2, v2\n\t" \
        "v_lshl_add_u32 v162, v163, 1, v162\n\t" \
        "v_and_b32 v161, 0x80, v161\n\t" \
        "buffer_load_dwordx4 v4, s[24:27], s57 offen offset:1024 lds\n\t" \
        "s_mul_i32 s55, s35, 2560\n\t" \
        "s_add_u32 s55, s55, 109120\n\t" \
        "v_add3_u32 v63, v162, v161, s55\n\t" \
        "s_mul_i32 s56, s35, 1280\n\t" \
        "s_add_u32 s56, s56, 119360\n\t" \
        "v_lshl_add_u32 v10, v19, 3, s56\n\t" \
        "v_lshl_add_u32 v162, v160, 2, v161\n\t" \
        "v_add_u32 v64, v10, v162\n\t" \
        "s_mul_i32 s55, s32, 0xf80\n\t" \
        "s_mul_i32 s56, s33, 0xc0\n\t" \
        "buffer_load_dwordx4 v5, s[24:27], s57 offen offset:2048 lds\n\t" \
        "s_add_u32 s55, s55, s56\n\t" \
        "s_sub_u32 s55, 0x1f40, s55\n\t" \
        "v_mov_b32 v65, s55\n\t" \
        "s_mov_b32 s64, 0xffff0000\n\t" \
        "s_mov_b32 s65, s64\n\t" \
        "s_sub_i32 s55, s32, 2\n\t" \
        "s_sub_i32 s56, s33, 2\n\t" \
        "s_max_i32 s55, s55, 0\n\t" \
        "s_max_i32 s56, s56, 0\n\t" \
        "v_cvt_f32_u32 v162, s55\n\t" \
        "v_cvt_f32_u32 v163, s56\n\t" \
        "s_add_u32 s55, s32, 5\n\t" \
        "s_add_u32 s56, s33, 17\n\t" \
        "v_cvt_pk_bf16_f32 v14, v162, v163\n\t" \
        "v_cvt_f32_u32 v162, s55\n\t" \
        "v_cvt_f32_u32 v163, s56\n\t" \
        "v_cvt_pk_bf16_f32 v15, v162, v163\n\t" \
        "s_waitcnt vmcnt(11)\n\t" \
        "s_add_u32 s57, s40, 0x3000\n\t" \
        "s_add_u32 m0, s39, 0x3000\n\t" \
        "s_nop 0\n\t" \
        "buffer_load_dwordx4 v3, s[24:27], s57 offen offset:0 lds\n\t" \
        "v_cndmask_b32 v140, v40, v41, s[62:63]\n\t" \
        "v_lshlrev_b32 v141, 16, v140\n\t" \
        "v_and_b32 v142, s42, v140\n\t" \
        "v_add_f32 v141, v55, v141\n\t" \
        "v_add_f32 v142, v59, v142\n\t" \
        "v_cvt_pk_bf16_f32 v50, v141, v142\n\t" \
        "v_pk_max_i16 v50, v50, 0\n\t" \
        "v_pk_min_i16 v50, v50, s45\n\t" \
        "v_lshlrev_b32 v141, 16, v50\n\t" \
        "v_and_b32 v142, s42, v50\n\t" \
        "v_pk_min_i16 v66, v50, v50\n\t" \
        "v_fract_f32 v143, v141\n\t" \
        "v_fract_f32 v144, v142\n\t" \
        "v_cvt_u32_f32 v145, v141\n\t" \
        "v_cvt_u32_f32 v146, v142\n\t" \
        "v_cmp_neq_f32 s[10:11], 0, v143\n\t" \
        "v_cmp_neq_f32 vcc, 0, v144\n\t" \
        "v_add_u32 v146, 2, v146\n\t" \
        "v_pk_max_i16 v67, v50, v50\n\t" \
        "s_and_b64 s[10:11], s[10:11], s[64:65]\n\t" \
        "v_mad_u32_u24 v148, v146, s43, v65\n\t" \
        "v_bfe_u32 v147, v146, 2, 1\n\t" \
        "v_sub_f32 v141, 1.0, v143\n\t" \
        "v_addc_co_u32 v145, s[4:5], 0, v145, s[10:11]\n\t" \
        "v_sub_f32 v142, 1.0, v144\n\t" \
        "v_add_u32 v146, 1, v146\n\t" \
        "v_mad_u32_u24 v148, v145, s44, v148\n\t" \
        "v_cvt_pk_bf16_f32 v152, v141, v142\n\t" \
        "v_lshl_add_u32 v149, v147, 5, v148\n\t" \
        "v_bfe_u32 v147, v146, 2, 1\n\t" \
        "v_add_u32 v148, s43, v148\n\t" \
        "v_lshlrev_b32 v141, 16, v152\n\t" \
        "v_lshl_add_u32 v148, v147, 5, v148\n\t" \
        "v_and_b32 v152, s42, v152\n\t" \
        "v_cndmask_b32 v141, v141, v143, s[64:65]\n\t" \
        "v_cndmask_b32 v150, v149, v148, vcc\n\t" \
        "buffer_load_dwordx4 v4, s[24:27], s57 offen offset:1024 lds\n\t" \
        "v_mul_f32 v152, v141, v152\n\t" \
        "v_mul_f32 v144, v141, v144\n\t" \
        "ds_write2st64_b32 v63, v149, v150 offset0:0 offset1:5\n\t" \
        "v_cvt_pk_bf16_f32 v151, v152, v144\n\t" \
        "ds_write_b32 v64, v151 offset:0\n\t" \
        "v_cndmask_b32 v140, v42, v43, s[62:63]\n\t" \
        "v_lshlrev_b32 v141, 16, v140\n\t" \
        "v_and_b32 v142, s42, v140\n\t" \
        "v_add_f32 v141, v56, v141\n\t" \
        "v_add_f32 v142, v60, v142\n\t" \
        "v_cvt_pk_bf16_f32 v51, v141, v142\n\t" \
        "v_pk_max_i16 v51, v51, 0\n\t" \
        "v_pk_min_i16 v51, v51, s45\n\t" \
        "v_lshlrev_b32 v141, 16, v51\n\t" \
        "v_and_b32 v142, s42, v51\n\t" \
        "v_pk_min_i16 v66, v66, v51\n\t" \
        "v_fract_f32 v143, v141\n\t" \
        "v_fract_f32 v144, v142\n\t" \
        "v_cvt_u32_f32 v145, v141\n\t" \
        "v_cvt_u32_f32 v146, v142\n\t" \
        "v_cmp_neq_f32 s[10:11], 0, v143\n\t" \
        "v_cmp_neq_f32 vcc, 0, v144\n\t" \
        "v_add_u32 v146, 2, v146\n\t" \
        "v_pk_max_i16 v67, v67, v51\n\t" \
        "s_and_b64 s[10:11], s[10:11], s[64:65]\n\t" \
        "v_mad_u32_u24 v148, v146, s43, v65\n\t" \
        "v_bfe_u32 v147, v146, 2, 1\n\t" \
        "v_sub_f32 v141, 1.0, v143\n\t" \
        "v_addc_co_u32 v145, s[4:5], 0, v145, s[10:11]\n\t" \
        "v_sub_f32 v142, 1.0, v144\n\t" \
        "v_add_u32 v146, 1, v146\n\t" \
        "v_mad_u32_u24 v148, v145, s44, v148\n\t" \
        "v_cvt_pk_bf16_f32 v152, v141, v142\n\t" \
        "v_lshl_add_u32 v149, v147, 5, v148\n\t" \
        "v_bfe_u32 v147, v146, 2, 1\n\t" \
        "v_add_u32 v148, s43, v148\n\t" \
        "buffer_load_dwordx4 v5, s[24:27], s57 offen offset:2048 lds\n\t" \
        "v_lshlrev_b32 v141, 16, v152\n\t" \
        "v_lshl_add_u32 v148, v147, 5, v148\n\t" \
        "v_and_b32 v152, s42, v152\n\t" \
        "v_cndmask_b32 v141, v141, v143, s[64:65]\n\t" \
        "v_cndmask_b32 v150, v149, v148, vcc\n\t" \
        "v_mul_f32 v152, v141, v152\n\t" \
        "v_mul_f32 v144, v141, v144\n\t" \
        "ds_write2st64_b32 v63, v149, v150 offset0:1 offset1:6\n\t" \
        "v_cvt_pk_bf16_f32 v151, v152, v144\n\t" \
        "ds_write_b32 v64, v151 offset:256\n\t" \
        "v_cndmask_b32 v140, v44, v45, s[62:63]\n\t" \
        "v_lshlrev_b32 v141, 16, v140\n\t" \
        "v_and_b32 v142, s42, v140\n\t" \
        "v_add_f32 v141, v57, v141\n\t" \
        "v_add_f32 v142, v61, v142\n\t" \
        "v_cvt_pk_bf16_f32 v52, v141, v142\n\t" \
        "v_pk_max_i16 v52, v52, 0\n\t" \
        "v_pk_min_i16 v52, v52, s45\n\t" \
        "v_lshlrev_b32 v141, 16, v52\n\t" \
        "v_and_b32 v142, s42, v52\n\t" \
        "v_pk_min_i16 v66, v66, v52\n\t" \
        "v_fract_f32 v143, v141\n\t" \
        "v_fract_f32 v144, v142\n\t" \
        "v_cvt_u32_f32 v145, v141\n\t" \
        "v_cvt_u32_f32 v146, v142\n\t" \
        "v_cmp_neq_f32 s[10:11], 0, v143\n\t" \
        "v_cmp_neq_f32 vcc, 0, v144\n\t" \
        "v_add_u32 v146, 2, v146\n\t" \
        "v_pk_max_i16 v67, v67, v52\n\t" \
        "s_and_b64 s[10:11], s[10:11], s[64:65]\n\t" \
        "v_mad_u32_u24 v148, v146, s43, v65\n\t" \
        "v_bfe_u32 v147, v146, 2, 1\n\t" \
        "v_sub_f32 v141, 1.0, v143\n\t" \
        "v_addc_co_u32 v145, s[4:5], 0, v145, s[10:11]\n\t" \
        "v_sub_f32 v142, 1.0, v144\n\t" \
        "v_add_u32 v146, 1, v146\n\t" \
        "s_add_u32 s57, s40, 0x6000\n\t" \
        "s_add_u32 m0, s39, 0x6000\n\t" \
        "s_nop 0\n\t" \
        "buffer_load_dwordx4 v3, s[24:27], s57 offen offset:0 lds\n\t" \
        "v_mad_u32_u24 v148, v145, s44, v148\n\t" \
        "v_cvt_pk_bf16_f32 v152, v141, v142\n\t" \
        "v_lshl_add_u32 v149, v147, 5, v148\n\t" \
        "v_bfe_u32 v147, v146, 2, 1\n\t" \
        "v_add_u32 v148, s43, v148\n\t" \
        "v_lshlrev_b32 v141, 16, v152\n\t" \
        "v_lshl_add_u32 v148, v147, 5, v148\n\t" \
        "v_and_b32 v152, s42, v152\n\t" \
        "v_cndmask_b32 v141, v141, v143, s[64:65]\n\t" \
        "v_cndmask_b32 v150, v149, v148, vcc\n\t" \
        "v_mul_f32 v152, v141, v152\n\t" \
        "v_mul_f32 v144, v141, v144\n\t" \
        "ds_write2st64_b32 v63, v149, v150 offset0:2 offset1:7\n\t" \
        "v_cvt_pk_bf16_f32 v151, v152, v144\n\t" \
        "ds_write_b32 v64, v151 offset:512\n\t" \
        "v_cndmask_b32 v140, v46, v47, s[62:63]\n\t" \
        "v_lshlrev_b32 v141, 16, v140\n\t" \
        "v_and_b32 v142, s42, v140\n\t" \
        "v_add_f32 v141, v58, v141\n\t" \
        "v_add_f32 v142, v59, v142\n\t" \
        "v_cvt_pk_bf16_f32 v53, v141, v142\n\t" \
        "v_pk_max_i16 v53, v53, 0\n\t" \
        "v_pk_min_i16 v53, v53, s45\n\t" \
        "v_lshlrev_b32 v141, 16, v53\n\t" \
        "v_and_b32 v142, s42, v53\n\t" \
        "v_pk_min_i16 v66, v66, v53\n\t" \
        "v_fract_f32 v143, v141\n\t" \
        "v_fract_f32 v144, v142\n\t" \
        "v_cvt_u32_f32 v145, v141\n\t" \
        "v_cvt_u32_f32 v146, v142\n\t" \
        "v_cmp_neq_f32 s[10:11], 0, v143\n\t" \
        "v_cmp_neq_f32 vcc, 0, v144\n\t" \
        "v_add_u32 v146, 2, v146\n\t" \
        "v_pk_max_i16 v67, v67, v53\n\t" \
        "s_and_b64 s[10:11], s[10:11], s[64:65]\n\t" \
        "v_mad_u32_u24 v148, v146, s43, v65\n\t" \
        "buffer_load_dwordx4 v4, s[24:27], s57 offen offset:1024 lds\n\t" \
        "v_bfe_u32 v147, v146, 2, 1\n\t" \
        "v_sub_f32 v141, 1.0, v143\n\t" \
        "v_addc_co_u32 v145, s[4:5], 0, v145, s[10:11]\n\t" \
        "v_sub_f32 v142, 1.0, v144\n\t" \
        "v_add_u32 v146, 1, v146\n\t" \
        "v_mad_u32_u24 v148, v145, s44, v148\n\t" \
        "v_cvt_pk_bf16_f32 v152, v141, v142\n\t" \
        "v_lshl_add_u32 v149, v147, 5, v148\n\t" \
        "v_bfe_u32 v147, v146, 2, 1\n\t" \
        "v_add_u32 v148, s43, v148\n\t" \
        "v_lshlrev_b32 v141, 16, v152\n\t" \
        "v_lshl_add_u32 v148, v147, 5, v148\n\t" \
        "v_and_b32 v152, s42, v152\n\t" \
        "v_cndmask_b32 v141, v141, v143, s[64:65]\n\t" \
        "v_cndmask_b32 v150, v149, v148, vcc\n\t" \
        "v_mul_f32 v152, v141, v152\n\t" \
        "v_mul_f32 v144, v141, v144\n\t" \
        "ds_write2st64_b32 v63, v149, v150 offset0:3 offset1:8\n\t" \
        "v_cvt_pk_bf16_f32 v151, v152, v144\n\t" \
        "ds_write_b32 v64, v151 offset:768\n\t" \
        "v_lshlrev_b32 v141, 16, v48\n\t" \
        "v_and_b32 v142, s42, v48\n\t" \
        "v_add_f32 v141, v58, v141\n\t" \
        "v_add_f32 v142, v62, v142\n\t" \
        "v_cvt_pk_bf16_f32 v54, v141, v142\n\t" \
        "v_pk_max_i16 v54, v54, 0\n\t" \
        "v_pk_min_i16 v54, v54, s45\n\t" \
        "v_lshlrev_b32 v141, 16, v54\n\t" \
        "v_and_b32 v142, s42, v54\n\t" \
        "v_pk_min_i16 v66, v66, v54\n\t" \
        "v_fract_f32 v143, v141\n\t" \
        "v_fract_f32 v144, v142\n\t" \
        "v_cvt_u32_f32 v145, v141\n\t" \
        "v_cvt_u32_f32 v146, v142\n\t" \
        "v_cmp_neq_f32 s[10:11], 0, v143\n\t" \
        "v_cmp_neq_f32 vcc, 0, v144\n\t" \
        "buffer_load_dwordx4 v5, s[24:27], s57 offen offset:2048 lds\n\t" \
        "v_add_u32 v146, 2, v146\n\t" \
        "v_pk_max_i16 v67, v67, v54\n\t" \
        "s_and_b64 s[10:11], s[10:11], s[64:65]\n\t" \
        "v_mad_u32_u24 v148, v146, s43, v65\n\t" \
        "v_bfe_u32 v147, v146, 2, 1\n\t" \
        "v_sub_f32 v141, 1.0, v143\n\t" \
        "v_addc_co_u32 v145, s[4:5], 0, v145, s[10:11]\n\t" \
        "v_sub_f32 v142, 1.0, v144\n\t" \
        "v_add_u32 v146, 1, v146\n\t" \
        "v_mad_u32_u24 v148, v145, s44, v148\n\t" \
        "v_cvt_pk_bf16_f32 v152, v141, v142\n\t" \
        "v_lshl_add_u32 v149, v147, 5, v148\n\t" \
        "v_bfe_u32 v147, v146, 2, 1\n\t" \
        "v_add_u32 v148, s43, v148\n\t" \
        "v_lshlrev_b32 v141, 16, v152\n\t" \
        "v_lshl_add_u32 v148, v147, 5, v148\n\t" \
        "v_and_b32 v152, s42, v152\n\t" \
        "v_cndmask_b32 v141, v141, v143, s[64:65]\n\t" \
        "v_cndmask_b32 v150, v149, v148, vcc\n\t" \
        "v_mul_f32 v152, v141, v152\n\t" \
        "v_mul_f32 v144, v141, v144\n\t" \
        "ds_write2st64_b32 v63, v149, v150 offset0:4 offset1:9\n\t" \
        "v_cvt_pk_bf16_f32 v151, v152, v144\n\t" \
        "ds_write_b32 v64, v151 offset:1024\n\t" \
        "v_pk_max_i16 v140, v66, v14\n\t" \
        "v_pk_min_i16 v141, v67, v15\n\t" \
        "v_cmp_ne_u32 s[10:11], v140, v66\n\t" \
        "v_cmp_ne_u32 vcc, v141, v67\n\t" \
        "s_nop 0\n\t" \
        "s_or_b64 s[10:11], s[10:11], vcc\n\t" \
        "s_cselect_b32 s54, 1, 0\n\t" \
        "s_lshl_b32 s55, s35, 2\n\t" \
        "s_cmp_lg_u32 s54, 0\n\t" \
        "v_mov_b32 v162, s55\n\t" \
        "s_cselect_b32 s55, 0x10000, 0\n\t" \
        "v_mov_b32 v163, s55\n\t" \
        "ds_write_b32 v162, v163\n\t" \
        "v_bfe_u32 v162, v2, 2, 1\n\t" \
        "v_lshrrev_b32 v163, 3, v2\n\t" \
        "s_mul_i32 s55, s35, 2560\n\t" \
        "s_add_u32 s55, s55, 109120\n\t" \
        "v_mul_u32_u24 v162, 0x500, v162\n\t" \
        "v_lshlrev_b32 v163, 4, v163\n\t" \
        "v_add3_u32 v9, v162, v163, s55\n\t" \
        "v_lshrrev_b32 v164, 4, v2\n\t" \
        "v_bfe_u32 v162, v2, 2, 2\n\t" \
        "v_and_b32 v163, 3, v2\n\t" \
        "v_lshl_add_u32 v162, v164, 2, v162\n\t" \
        "v_lshlrev_b32 v11, 3, v163\n\t" \
        "v_and_b32 v165, 1, v164\n\t" \
        "v_mul_u32_u24 v162, 0xc0, v162\n\t" \
        "v_xor_b32 v12, 32, v11\n\t" \
        "v_lshl_add_u32 v162, v165, 5, v162\n\t" \
        "v_lshlrev_b32 v163, 6, v165\n\t" \
        "s_mov_b32 s55, 0xba40\n\t" \
        "v_add3_u32 v6, v162, v11, s55\n\t" \
        "v_sub_u32 v7, v6, v163\n\t" \
        "v_mul_u32_u24 v162, 0xc0, v19\n\t" \
        "v_lshrrev_b32 v164, 1, v164\n\t" \
        "v_lshl_add_u32 v162, v165, 5, v162\n\t" \
        "v_lshl_add_u32 v8, v164, 4, v162\n\t" \
        "s_add_u32 s55, s32, s35\n\t" \
        "s_mul_i32 s56, s33, 0xc0\n\t" \
        "s_mul_i32 s55, s55, 0xa800\n\t" \
        "s_add_u32 s59, s55, s56\n\t" \
        "s_add_u32 s60, s59, 0x2a000\n\t" \
        "v_lshrrev_b32 v166, 4, v2\n\t" \
        "v_bfe_u32 v168, v2, 2, 2\n\t" \
        "v_and_b32 v167, 3, v2\n\t" \
        "v_cmp_eq_u32 vcc, v166, v168\n\t" \
        "v_lshlrev_b32 v167, v167, 1\n\t" \
        "s_nop 0\n\t" \
        "v_cndmask_b32 v167, 0, v167, vcc\n\t" \
        "v_bfe_i32 v25, v167, 0, 1\n\t" \
        "v_bfe_i32 v26, v167, 1, 1\n\t" \
        "v_bfe_i32 v27, v167, 2, 1\n\t" \
        "v_bfe_i32 v28, v167, 3, 1\n\t" \
        "v_mov_b32 v163, 0\n\t" \
        "s_waitcnt vmcnt(6) lgkmcnt(0)\n\t" \
        "s_barrier\n\t" \
        "ds_read_b128 v[152:155], v163\n\t" \
        "s_waitcnt lgkmcnt(0)\n\t" \
        "v_add_u32 v152, v152, v154\n\t" \
        "v_add_u32 v153, v153, v155\n\t" \
        "v_max_u32 v152, v152, v153\n\t" \
        "s_nop 0\n\t" \
        "v_readfirstlane_b32 s55, v152\n\t" \
        "s_nop 0\n\t" \
        "s_cmp_gt_u32 s55, 56\n\t" \
        "s_cbranch_scc1 .Lfail%=\n\t" \
        "ds_read_b128 v[170:173], v9 offset:0\n\t" \
        "ds_read_b64 v[150:151], v10 offset:0\n\t" \
        "ds_read_b64_tr_b16 a[0:1], v6 offset:0\n\t" \
        "ds_read_b64_tr_b16 a[2:3], v6 offset:3072\n\t" \
        "ds_read_b64_tr_b16 a[4:5], v7 offset:32\n\t" \
        "ds_read_b64_tr_b16 a[6:7], v7 offset:3104\n\t" \
        "ds_read_b64_tr_b16 a[8:9], v6 offset:64\n\t" \
        "ds_read_b64_tr_b16 a[10:11], v6 offset:3136\n\t" \
        "s_waitcnt lgkmcnt(7)\n\t" \
        "v_xor_b32 v134, v170, v11\n\t" \
        "v_xor_b32 v135, v171, v11\n\t" \
        "v_xor_b32 v136, v172, v11\n\t" \
        "v_xor_b32 v138, v170, v12\n\t" \
        "v_xor_b32 v137, v173, v11\n\t" \
        "v_xor_b32 v139, v171, v12\n\t" \
        "v_xor_b32 v140, v172, v12\n\t" \
        "v_xor_b32 v141, v173, v12\n\t" \
        "ds_read_b128 v[174:177], v9 offset:128\n\t" \
        "ds_read_b64_tr_b16 a[96:97], v134 offset:0\n\t" \
        "ds_read_b64_tr_b16 a[98:99], v135 offset:0\n\t" \
        "ds_read_b64_tr_b16 a[100:101], v138 offset:0\n\t" \
        "s_waitcnt lgkmcnt(10)\n\t" \
        "v_and_b32 v154, v150, v25\n\t" \
        "ds_read_b64_tr_b16 a[102:103], v139 offset:0\n\t" \
        "ds_read_b64_tr_b16 a[104:105], v136 offset:0\n\t" \
        "v_and_b32 v155, v150, v26\n\t" \
        "ds_read_b64_tr_b16 a[106:107], v137 offset:0\n\t" \
        "ds_read_b64_tr_b16 a[108:109], v140 offset:0\n\t" \
        "v_and_b32 v156, v150, v27\n\t" \
        "ds_read_b64_tr_b16 a[110:111], v141 offset:0\n\t" \
        "ds_read_b64_tr_b16 a[112:113], v134 offset:64\n\t" \
        "v_and_b32 v157, v150, v28\n\t" \
        "ds_read_b64_tr_b16 a[114:115], v135 offset:64\n\t" \
        "ds_read_b64_tr_b16 a[116:117], v138 offset:64\n\t" \
        "ds_read_b64_tr_b16 a[118:119], v139 offset:64\n\t" \
        "ds_read_b64_tr_b16 a[120:121], v136 offset:64\n\t" \
        "ds_read_b64_tr_b16 a[122:123], v137 offset:64\n\t" \
        "ds_read_b64_tr_b16 a[124:125], v140 offset:64\n\t" \
        "v_and_b32 v158, v151, v25\n\t" \
        "ds_read_b64_tr_b16 a[126:127], v141 offset:64\n\t" \
        "ds_read_b64_tr_b16 a[128:129], v134 offset:128\n\t" \
        "v_and_b32 v159, v151, v26\n\t" \
        "ds_read_b64_tr_b16 a[130:131], v135 offset:128\n\t" \
        "ds_read_b64_tr_b16 a[132:133], v138 offset:128\n\t" \
        "v_and_b32 v160, v151, v27\n\t" \
        "ds_read_b64_tr_b16 a[134:135], v139 offset:128\n\t" \
        "ds_read_b64_tr_b16 a[136:137], v136 offset:128\n\t" \
        "s_waitcnt lgkmcnt(15)\n\t" \
        "v_mfma_f32_16x16x32_bf16 v[102:105], a[96:99], v[154:157], 0\n\t" \
        "ds_read_b64_tr_b16 a[138:139], v137 offset:128\n\t" \
        "ds_read_b64_tr_b16 a[140:141], v140 offset:128\n\t" \
        "v_and_b32 v161, v151, v28\n\t" \
        "v_xor_b32 v142, v174, v11\n\t" \
        "v_xor_b32 v143, v175, v11\n\t" \
        "v_xor_b32 v144, v176, v11\n\t" \
        "v_xor_b32 v146, v174, v12\n\t" \
        "v_xor_b32 v145, v177, v11\n\t" \
        "v_xor_b32 v147, v175, v12\n\t" \
        "v_mfma_f32_16x16x32_bf16 v[106:109], a[100:103], v[154:157], 0\n\t" \
        "s_add_u32 m0, s39, 36864\n\t" \
        "s_add_u32 s57, s40, 36864\n\t" \
        "buffer_load_dwordx4 v3, s[24:27], s57 offen offset:0 lds\n\t" \
        "ds_read_b64_tr_b16 a[142:143], v141 offset:128\n\t" \
        "v_mfma_f32_16x16x32_bf16 v[110:113], a[104:107], v[158:161], 0\n\t" \
        "ds_read_b64 v[152:153], v10 offset:128\n\t" \
        "ds_read_b64_tr_b16 a[12:13], v7 offset:96\n\t" \
        "v_xor_b32 v148, v176, v12\n\t" \
        "v_xor_b32 v149, v177, v12\n\t" \
        "s_waitcnt lgkmcnt(15)\n\t" \
        "v_mfma_f32_16x16x32_bf16 v[114:117], a[108:111], v[158:161], 0\n\t" \
        "ds_read_b64_tr_b16 a[14:15], v7 offset:3168\n\t" \
        "v_mfma_f32_16x16x32_bf16 v[118:121], a[112:115], v[154:157], 0\n\t" \
        "ds_read_b64_tr_b16 a[16:17], v6 offset:128\n\t" \
        "v_cvt_pk_bf16_f32 v78, v102, v103\n\t" \
        "v_cvt_pk_bf16_f32 v79, v104, v105\n\t" \
        "s_waitcnt lgkmcnt(15)\n\t" \
        "v_mfma_f32_16x16x32_bf16 v[122:125], a[116:119], v[154:157], 0\n\t" \
        "ds_read_b64_tr_b16 a[18:19], v6 offset:3200\n\t" \
        "ds_read_b64_tr_b16 a[20:21], v7 offset:160\n\t" \
        "v_cvt_pk_bf16_f32 v80, v106, v107\n\t" \
        "v_cvt_pk_bf16_f32 v81, v108, v109\n\t" \
        "s_waitcnt lgkmcnt(15)\n\t" \
        "v_mfma_f32_16x16x32_bf16 v[126:129], a[120:123], v[158:161], 0\n\t" \
        "buffer_load_dwordx4 v4, s[24:27], s57 offen offset:1024 lds\n\t" \
        "v_cvt_pk_bf16_f32 v82, v110, v111\n\t" \
        "s_waitcnt lgkmcnt(14)\n\t" \
        "v_mfma_f32_16x16x32_bf16 v[130:133], a[124:127], v[158:161], 0\n\t" \
        "ds_read_b64_tr_b16 a[22:23], v7 offset:3232\n\t" \
        "v_cvt_pk_bf16_f32 v83, v112, v113\n\t" \
        "v_cvt_pk_bf16_f32 v84, v114, v115\n\t" \
        "s_waitcnt lgkmcnt(13)\n\t" \
        "v_mfma_f32_16x16x32_bf16 v[102:105], a[128:131], v[154:157], 0\n\t" \
        "ds_read_b64_tr_b16 a[96:97], v142 offset:0\n\t" \
        "v_cvt_pk_bf16_f32 v85, v116, v117\n\t" \
        "s_waitcnt lgkmcnt(12)\n\t" \
        "v_mfma_f32_16x16x32_bf16 v[106:109], a[132:135], v[154:157], 0\n\t" \
        "ds_read_b64_tr_b16 a[98:99], v143 offset:0\n\t" \
        "ds_read_b64_tr_b16 a[100:101], v146 offset:0\n\t" \
        "s_waitcnt lgkmcnt(12)\n\t" \
        "v_mfma_f32_16x16x32_bf16 v[110:113], a[136:139], v[158:161], 0\n\t" \
        "ds_read_b64_tr_b16 a[102:103], v147 offset:0\n\t" \
        "v_cvt_pk_bf16_f32 v86, v118, v119\n\t" \
        "s_waitcnt lgkmcnt(11)\n\t" \
        "v_mfma_f32_16x16x32_bf16 v[114:117], a[140:143], v[158:161], 0\n\t" \
        "buffer_load_dwordx4 v5, s[24:27], s57 offen offset:2048 lds\n\t" \
        "v_mfma_f32_16x16x32_bf16 v[30:33], a[0:3], v[78:81], 0\n\t" \
        "ds_read_b64_tr_b16 a[104:105], v144 offset:0\n\t" \
        "s_waitcnt lgkmcnt(10)\n\t" \
        "v_and_b32 v162, v152, v25\n\t" \
        "v_mfma_f32_16x16x32_bf16 v[54:57], a[0:3], v[82:85], 0\n\t" \
        "ds_read_b64_tr_b16 a[106:107], v145 offset:0\n\t" \
        "ds_read_b64_tr_b16 a[108:109], v148 offset:0\n\t" \
        "v_mfma_f32_16x16x32_bf16 v[34:37], a[4:7], v[78:81], 0\n\t" \
        "ds_read_b64_tr_b16 a[110:111], v149 offset:0\n\t" \
        "v_and_b32 v163, v152, v26\n\t" \
        "v_mfma_f32_16x16x32_bf16 v[58:61], a[4:7], v[82:85], 0\n\t" \
        "ds_read_b64_tr_b16 a[24:25], v6 offset:6144\n\t" \
        "v_and_b32 v164, v152, v27\n\t" \
        "v_mfma_f32_16x16x32_bf16 v[38:41], a[8:11], v[78:81], 0\n\t" \
        "s_add_u32 m0, s39, 49152\n\t" \
        "s_add_u32 s57, s40, 49152\n\t" \
        "buffer_load_dwordx4 v3, s[24:27], s57 offen offset:0 lds\n\t" \
        "v_mfma_f32_16x16x32_bf16 v[62:65], a[8:11], v[82:85], 0\n\t" \
        "ds_read_b64_tr_b16 a[26:27], v6 offset:9216\n\t" \
        "ds_read_b64_tr_b16 a[28:29], v7 offset:6176\n\t" \
        "v_and_b32 v165, v152, v28\n\t" \
        "v_and_b32 v166, v153, v25\n\t" \
        "s_waitcnt lgkmcnt(11)\n\t" \
        "v_mfma_f32_16x16x32_bf16 v[42:45], a[12:15], v[78:81], 0\n\t" \
        "ds_read_b64_tr_b16 a[30:31], v7 offset:9248\n\t" \
        "v_cvt_pk_bf16_f32 v87, v120, v121\n\t" \
        "v_mfma_f32_16x16x32_bf16 v[66:69], a[12:15], v[82:85], 0\n\t" \
        "ds_read_b64_tr_b16 a[32:33], v6 offset:6208\n\t" \
        "ds_read_b64_tr_b16 a[34:35], v6 offset:9280\n\t" \
        "v_cvt_pk_bf16_f32 v88, v122, v123\n\t" \
        "v_and_b32 v167, v153, v26\n\t" \
        "v_mfma_f32_16x16x32_bf16 v[46:49], a[16:19], v[78:81], 0\n\t" \
        "ds_read_b64_tr_b16 a[36:37], v7 offset:6240\n\t" \
        "v_cvt_pk_bf16_f32 v89, v124, v125\n\t" \
        "v_cvt_pk_bf16_f32 v90, v126, v127\n\t" \
        "v_mfma_f32_16x16x32_bf16 v[70:73], a[16:19], v[82:85], 0\n\t" \
        "buffer_load_dwordx4 v4, s[24:27], s57 offen offset:1024 lds\n\t" \
        "v_mfma_f32_16x16x32_bf16 v[50:53], a[20:23], v[78:81], 0\n\t" \
        "ds_read_b64_tr_b16 a[38:39], v7 offset:9312\n\t" \
        "v_and_b32 v168, v153, v27\n\t" \
        "v_mfma_f32_16x16x32_bf16 v[74:77], a[20:23], v[82:85], 0\n\t" \
        "ds_read_b64_tr_b16 a[40:41], v6 offset:6272\n\t" \
        "ds_read_b64_tr_b16 a[42:43], v6 offset:9344\n\t" \
        "v_cvt_pk_bf16_f32 v91, v128, v129\n\t" \
        "v_cvt_pk_bf16_f32 v92, v130, v131\n\t" \
        "s_waitcnt lgkmcnt(11)\n\t" \
        "v_mfma_f32_16x16x32_bf16 v[118:121], a[96:99], v[162:165], 0\n\t" \
        "s_waitcnt vmcnt(8)\n\t" \
        "s_barrier\n\t" \
        "ds_read_b64_tr_b16 a[44:45], v7 offset:6304\n\t" \
        "v_and_b32 v169, v153, v28\n\t" \
        "v_cvt_pk_bf16_f32 v93, v132, v133\n\t" \
        "v_mfma_f32_16x16x32_bf16 v[122:125], a[100:103], v[162:165], 0\n\t" \
        "ds_read_b64_tr_b16 a[46:47], v7 offset:9376\n\t" \
        "v_cvt_pk_bf16_f32 v94, v102, v103\n\t" \
        "v_mfma_f32_16x16x32_bf16 v[126:129], a[104:107], v[166:169], 0\n\t" \
        "buffer_load_dwordx4 v5, s[24:27], s57 offen offset:2048 lds\n\t" \
        "s_waitcnt lgkmcnt(11)\n\t" \
        "v_mfma_f32_16x16x32_bf16 v[130:133], a[108:111], v[166:169], 0\n\t" \
        "ds_read_b64_tr_b16 a[112:113], v142 offset:64\n\t" \
        "ds_read_b64_tr_b16 a[114:115], v143 offset:64\n\t" \
        "s_waitcnt lgkmcnt(11)\n\t" \
        "v_mfma_f32_16x16x32_bf16 v[30:33], a[24:27], v[86:89], v[30:33]\n\t" \
        "ds_read_b64_tr_b16 a[116:117], v146 offset:64\n\t" \
        "v_cvt_pk_bf16_f32 v95, v104, v105\n\t" \
        "v_mfma_f32_16x16x32_bf16 v[54:57], a[24:27], v[90:93], v[54:57]\n\t" \
        "ds_read_b64_tr_b16 a[118:119], v147 offset:64\n\t" \
        "ds_read_b64_tr_b16 a[120:121], v144 offset:64\n\t" \
        "s_waitcnt lgkmcnt(11)\n\t" \
        "v_mfma_f32_16x16x32_bf16 v[34:37], a[28:31], v[86:89], v[34:37]\n\t" \
        "ds_read_b64_tr_b16 a[122:123], v145 offset:64\n\t" \
        "v_cvt_pk_bf16_f32 v96, v106, v107\n\t" \
        "v_mfma_f32_16x16x32_bf16 v[58:61], a[28:31], v[90:93], v[58:61]\n\t" \
        "ds_read_b64_tr_b16 a[124:125], v148 offset:64\n\t" \
        "v_cvt_pk_bf16_f32 v97, v108, v109\n\t" \
        "v_mfma_f32_16x16x32_bf16 v[38:41], a[32:35], v[86:89], v[38:41]\n\t" \
        "ds_read_b64_tr_b16 a[126:127], v149 offset:64\n\t" \
        "ds_read_b64_tr_b16 a[48:49], v6 offset:12288\n\t" \
        "v_mfma_f32_16x16x32_bf16 v[62:65], a[32:35], v[90:93], v[62:65]\n\t" \
        "ds_read_b64_tr_b16 a[50:51], v6 offset:15360\n\t" \
        "v_cvt_pk_bf16_f32 v98, v110, v111\n\t" \
        "s_waitcnt lgkmcnt(12)\n\t" \
        "v_mfma_f32_16x16x32_bf16 v[42:45], a[36:39], v[86:89], v[42:45]\n\t" \
        "ds_read_b128 v[170:173], v9 offset:256\n\t" \
        "v_cvt_pk_bf16_f32 v99, v112, v113\n\t" \
        "v_mfma_f32_16x16x32_bf16 v[66:69], a[36:39], v[90:93], v[66:69]\n\t" \
        "ds_read_b64_tr_b16 a[52:53], v7 offset:12320\n\t" \
        "ds_read_b64_tr_b16 a[54:55], v7 offset:15392\n\t" \
        "v_mfma_f32_16x16x32_bf16 v[46:49], a[40:43], v[86:89], v[46:49]\n\t" \
        "ds_read_b64_tr_b16 a[56:57], v6 offset:12352\n\t" \
        "v_cvt_pk_bf16_f32 v100, v114, v115\n\t" \
        "v_mfma_f32_16x16x32_bf16 v[70:73], a[40:43], v[90:93], v[70:73]\n\t" \
        "ds_read_b64_tr_b16 a[58:59], v6 offset:15424\n\t" \
        "v_cvt_pk_bf16_f32 v101, v116, v117\n\t" \
        "s_waitcnt lgkmcnt(13)\n\t" \
        "v_mfma_f32_16x16x32_bf16 v[50:53], a[44:47], v[86:89], v[50:53]\n\t" \
        "ds_read_b64_tr_b16 a[60:61], v7 offset:12384\n\t" \
        "ds_read_b64_tr_b16 a[62:63], v7 offset:15456\n\t" \
        "v_mfma_f32_16x16x32_bf16 v[74:77], a[44:47], v[90:93], v[74:77]\n\t" \
        "ds_read_b64_tr_b16 a[64:65], v6 offset:12416\n\t" \
        "v_cvt_pk_bf16_f32 v78, v118, v119\n\t" \
        "v_mfma_f32_16x16x32_bf16 v[102:105], a[112:115], v[162:165], 0\n\t" \
        "ds_read_b64_tr_b16 a[66:67], v6 offset:15488\n\t" \
        "ds_read_b64_tr_b16 a[68:69], v7 offset:12448\n\t" \
        "s_waitcnt lgkmcnt(14)\n\t" \
        "v_mfma_f32_16x16x32_bf16 v[106:109], a[116:119], v[162:165], 0\n\t" \
        "ds_read_b64_tr_b16 a[70:71], v7 offset:15520\n\t" \
        "v_cvt_pk_bf16_f32 v79, v120, v121\n\t" \
        "v_mfma_f32_16x16x32_bf16 v[110:113], a[120:123], v[166:169], 0\n\t" \
        "ds_read_b64_tr_b16 a[128:129], v142 offset:128\n\t" \
        "v_cvt_pk_bf16_f32 v80, v122, v123\n\t" \
        "s_waitcnt lgkmcnt(13)\n\t" \
        "v_mfma_f32_16x16x32_bf16 v[114:117], a[124:127], v[166:169], 0\n\t" \
        "ds_read_b64_tr_b16 a[130:131], v143 offset:128\n\t" \
        "ds_read_b64_tr_b16 a[132:133], v146 offset:128\n\t" \
        "s_waitcnt lgkmcnt(14)\n\t" \
        "v_mfma_f32_16x16x32_bf16 v[30:33], a[48:51], v[94:97], v[30:33]\n\t" \
        "ds_read_b64_tr_b16 a[134:135], v147 offset:128\n\t" \
        "s_waitcnt lgkmcnt(14)\n\t" \
        "v_xor_b32 v134, v170, v11\n\t" \
        "v_mfma_f32_16x16x32_bf16 v[54:57], a[48:51], v[98:101], v[54:57]\n\t" \
        "ds_read_b64_tr_b16 a[136:137], v144 offset:128\n\t" \
        "v_xor_b32 v135, v171, v11\n\t" \
        "s_waitcnt lgkmcnt(13)\n\t" \
        "v_mfma_f32_16x16x32_bf16 v[34:37], a[52:55], v[94:97], v[34:37]\n\t" \
        "ds_read_b64_tr_b16 a[138:139], v145 offset:128\n\t" \
        "ds_read_b64_tr_b16 a[140:141], v148 offset:128\n\t" \
        "v_mfma_f32_16x16x32_bf16 v[58:61], a[52:55], v[98:101], v[58:61]\n\t" \
        "ds_read_b64_tr_b16 a[142:143], v149 offset:128\n\t" \
        "v_xor_b32 v136, v172, v11\n\t" \
        "s_waitcnt lgkmcnt(14)\n\t" \
        "v_mfma_f32_16x16x32_bf16 v[38:41], a[56:59], v[94:97], v[38:41]\n\t" \
        "ds_read_b64_tr_b16 a[72:73], v6 offset:18432\n\t" \
        "ds_read_b64_tr_b16 a[74:75], v6 offset:21504\n\t" \
        "v_mfma_f32_16x16x32_bf16 v[62:65], a[56:59], v[98:101], v[62:65]\n\t" \
        "ds_read_b64_tr_b16 a[76:77], v7 offset:18464\n\t" \
        "v_xor_b32 v138, v170, v12\n\t" \
        "s_waitcnt lgkmcnt(14)\n\t" \
        "v_mfma_f32_16x16x32_bf16 v[42:45], a[60:63], v[94:97], v[42:45]\n\t" \
        "ds_read_b64 v[150:151], v10 offset:256\n\t" \
        "v_xor_b32 v137, v173, v11\n\t" \
        "v_mfma_f32_16x16x32_bf16 v[66:69], a[60:63], v[98:101], v[66:69]\n\t" \
        "ds_read_b64_tr_b16 a[78:79], v7 offset:21536\n\t" \
        "ds_read_b64_tr_b16 a[80:81], v6 offset:18496\n\t" \
        "v_xor_b32 v139, v171, v12\n\t" \
        "s_waitcnt lgkmcnt(14)\n\t" \
        "v_mfma_f32_16x16x32_bf16 v[46:49], a[64:67], v[94:97], v[46:49]\n\t" \
        "ds_read_b64_tr_b16 a[82:83], v6 offset:21568\n\t" \
        "v_xor_b32 v140, v172, v12\n\t" \
        "v_mfma_f32_16x16x32_bf16 v[70:73], a[64:67], v[98:101], v[70:73]\n\t" \
        "ds_read_b64_tr_b16 a[84:85], v7 offset:18528\n\t" \
        "v_xor_b32 v141, v173, v12\n\t" \
        "v_mfma_f32_16x16x32_bf16 v[50:53], a[68:71], v[94:97], v[50:53]\n\t" \
        "ds_read_b64_tr_b16 a[86:87], v7 offset:21600\n\t" \
        "ds_read_b64_tr_b16 a[88:89], v6 offset:18560\n\t" \
        "v_cvt_pk_bf16_f32 v81, v124, v125\n\t" \
        "v_cvt_pk_bf16_f32 v82, v126, v127\n\t" \
        "v_mfma_f32_16x16x32_bf16 v[74:77], a[68:71], v[98:101], v[74:77]\n\t" \
        "ds_read_b64_tr_b16 a[90:91], v6 offset:21632\n\t" \
        "v_cvt_pk_bf16_f32 v83, v128, v129\n\t" \
        "v_cvt_pk_bf16_f32 v84, v130, v131\n\t" \
        "s_waitcnt lgkmcnt(14)\n\t" \
        "v_mfma_f32_16x16x32_bf16 v[118:121], a[128:131], v[162:165], 0\n\t" \
        "s_waitcnt vmcnt(6)\n\t" \
        "s_barrier\n\t" \
        "ds_read_b64_tr_b16 a[92:93], v7 offset:18592\n\t" \
        "ds_read_b64_tr_b16 a[94:95], v7 offset:21664\n\t" \
        "v_cvt_pk_bf16_f32 v85, v132, v133\n\t" \
        "v_mfma_f32_16x16x32_bf16 v[122:125], a[132:135], v[162:165], 0\n\t" \
        "ds_read_b64_tr_b16 a[96:97], v134 offset:0\n\t" \
        "v_cvt_pk_bf16_f32 v86, v102, v103\n\t" \
        "s_waitcnt lgkmcnt(14)\n\t" \
        "v_mfma_f32_16x16x32_bf16 v[126:129], a[136:139], v[166:169], 0\n\t" \
        "ds_read_b64_tr_b16 a[98:99], v135 offset:0\n\t" \
        "v_cvt_pk_bf16_f32 v87, v104, v105\n\t" \
        "v_mfma_f32_16x16x32_bf16 v[130:133], a[140:143], v[166:169], 0\n\t" \
        "s_add_u32 m0, s39, 0\n\t" \
        "s_add_u32 s57, s40, 61440\n\t" \
        "buffer_load_dwordx4 v3, s[24:27], s57 offen offset:0 lds\n\t" \
        "s_waitcnt lgkmcnt(12)\n\t" \
        "v_mfma_f32_16x16x32_bf16 v[30:33], a[72:75], v[78:81], v[30:33]\n\t" \
        "ds_read_b64_tr_b16 a[100:101], v138 offset:0\n\t" \
        "ds_read_b64_tr_b16 a[102:103], v139 offset:0\n\t" \
        "v_mfma_f32_16x16x32_bf16 v[54:57], a[72:75], v[82:85], v[54:57]\n\t" \
        "ds_read_b64_tr_b16 a[104:105], v136 offset:0\n\t" \
        "s_waitcnt lgkmcnt(12)\n\t" \
        "v_and_b32 v154, v150, v25\n\t" \
        "v_mfma_f32_16x16x32_bf16 v[34:37], a[76:79], v[78:81], v[34:37]\n\t" \
        "ds_read_b64_tr_b16 a[106:107], v137 offset:0\n\t" \
        "ds_read_b64_tr_b16 a[108:109], v140 offset:0\n\t" \
        "v_mfma_f32_16x16x32_bf16 v[58:61], a[76:79], v[82:85], v[58:61]\n\t" \
        "ds_read_b64_tr_b16 a[110:111], v141 offset:0\n\t" \
        "v_and_b32 v155, v150, v26\n\t" \
        "s_waitcnt lgkmcnt(13)\n\t" \
        "v_mfma_f32_16x16x32_bf16 v[38:41], a[80:83], v[78:81], v[38:41]\n\t" \
        "ds_read_b64_tr_b16 a[0:1], v6 offset:24576\n\t" \
        "v_and_b32 v156, v150, v27\n\t" \
        "v_and_b32 v157, v150, v28\n\t" \
        "v_mfma_f32_16x16x32_bf16 v[62:65], a[80:83], v[82:85], v[62:65]\n\t" \
        "buffer_load_dwordx4 v4, s[24:27], s57 offen offset:1024 lds\n\t" \
        "s_waitcnt lgkmcnt(11)\n\t" \
        "v_mfma_f32_16x16x32_bf16 v[42:45], a[84:87], v[78:81], v[42:45]\n\t" \
        "ds_read_b64_tr_b16 a[2:3], v6 offset:27648\n\t" \
        "ds_read_b64_tr_b16 a[4:5], v7 offset:24608\n\t" \
        "v_and_b32 v158, v151, v25\n\t" \
        "v_mfma_f32_16x16x32_bf16 v[66:69], a[84:87], v[82:85], v[66:69]\n\t" \
        "ds_read_b64_tr_b16 a[6:7], v7 offset:27680\n\t" \
        "v_cvt_pk_bf16_f32 v88, v106, v107\n\t" \
        "v_and_b32 v159, v151, v26\n\t" \
        "v_mfma_f32_16x16x32_bf16 v[46:49], a[88:91], v[78:81], v[46:49]\n\t" \
        "ds_read_b64_tr_b16 a[8:9], v6 offset:24640\n\t" \
        "ds_read_b64_tr_b16 a[10:11], v6 offset:27712\n\t" \
        "v_cvt_pk_bf16_f32 v89, v108, v109\n\t" \
        "v_cvt_pk_bf16_f32 v90, v110, v111\n\t" \
        "v_mfma_f32_16x16x32_bf16 v[70:73], a[88:91], v[82:85], v[70:73]\n\t" \
        "ds_read_b64_tr_b16 a[12:13], v7 offset:24672\n\t" \
        "v_and_b32 v160, v151, v27\n\t" \
        "s_waitcnt lgkmcnt(13)\n\t" \
        "v_mfma_f32_16x16x32_bf16 v[50:53], a[92:95], v[78:81], v[50:53]\n\t" \
        "ds_read_b64_tr_b16 a[14:15], v7 offset:27744\n\t" \
        "ds_read_b64_tr_b16 a[16:17], v6 offset:24704\n\t" \
        "v_cvt_pk_bf16_f32 v91, v112, v113\n\t" \
        "v_cvt_pk_bf16_f32 v92, v114, v115\n\t" \
        "v_mfma_f32_16x16x32_bf16 v[74:77], a[92:95], v[82:85], v[74:77]\n\t" \
        "buffer_load_dwordx4 v5, s[24:27], s57 offen offset:2048 lds\n\t" \
        "v_mfma_f32_16x16x32_bf16 v[102:105], a[96:99], v[154:157], 0\n\t" \
        "ds_read_b64_tr_b16 a[18:19], v6 offset:27776\n\t" \
        "v_and_b32 v161, v151, v28\n\t" \
        "v_cvt_pk_bf16_f32 v93, v116, v117\n\t" \
        "s_waitcnt lgkmcnt(11)\n\t" \
        "v_mfma_f32_16x16x32_bf16 v[106:109], a[100:103], v[154:157], 0\n\t" \
        "ds_read_b64_tr_b16 a[20:21], v7 offset:24736\n\t" \
        "v_cvt_pk_bf16_f32 v94, v118, v119\n\t" \
        "v_mfma_f32_16x16x32_bf16 v[110:113], a[104:107], v[158:161], 0\n\t" \
        "ds_read_b64_tr_b16 a[22:23], v7 offset:27808\n\t" \
        "ds_read_b64_tr_b16 a[112:113], v134 offset:64\n\t" \
        "s_waitcnt lgkmcnt(12)\n\t" \
        "v_mfma_f32_16x16x32_bf16 v[114:117], a[108:111], v[158:161], 0\n\t" \
        "ds_read_b64_tr_b16 a[114:115], v135 offset:64\n\t" \
        "v_cvt_pk_bf16_f32 v95, v120, v121\n\t" \
        "s_waitcnt lgkmcnt(12)\n\t" \
        "v_mfma_f32_16x16x32_bf16 v[30:33], a[0:3], v[86:89], v[30:33]\n\t" \
        "ds_read_b64_tr_b16 a[116:117], v138 offset:64\n\t" \
        "v_cvt_pk_bf16_f32 v96, v122, v123\n\t" \
        "v_mfma_f32_16x16x32_bf16 v[54:57], a[0:3], v[90:93], v[54:57]\n\t" \
        "ds_read_b64_tr_b16 a[118:119], v139 offset:64\n\t" \
        "ds_read_b64_tr_b16 a[120:121], v136 offset:64\n\t" \
        "s_waitcnt lgkmcnt(13)\n\t" \
        "v_mfma_f32_16x16x32_bf16 v[34:37], a[4:7], v[86:89], v[34:37]\n\t" \
        "ds_read_b64_tr_b16 a[122:123], v137 offset:64\n\t" \
        "v_cvt_pk_bf16_f32 v97, v124, v125\n\t" \
        "v_mfma_f32_16x16x32_bf16 v[58:61], a[4:7], v[90:93], v[58:61]\n\t" \
        "ds_read_b64_tr_b16 a[124:125], v140 offset:64\n\t" \
        "ds_read_b64_tr_b16 a[126:127], v141 offset:64\n\t" \
        "s_waitcnt lgkmcnt(13)\n\t" \
        "v_mfma_f32_16x16x32_bf16 v[38:41], a[8:11], v[86:89], v[38:41]\n\t" \
        "ds_read_b64_tr_b16 a[24:25], v6 offset:30720\n\t" \
        "v_cvt_pk_bf16_f32 v98, v126, v127\n\t" \
        "v_mfma_f32_16x16x32_bf16 v[62:65], a[8:11], v[90:93], v[62:65]\n\t" \
        "ds_read_b64_tr_b16 a[26:27], v6 offset:33792\n\t" \
        "ds_read_b128 v[174:177], v9 offset:384\n\t" \
        "s_waitcnt lgkmcnt(14)\n\t" \
        "v_mfma_f32_16x16x32_bf16 v[42:45], a[12:15], v[86:89], v[42:45]\n\t" \
        "ds_read_b64_tr_b16 a[28:29], v7 offset:30752\n\t" \
        "v_cvt_pk_bf16_f32 v99, v128, v129\n\t" \
        "v_mfma_f32_16x16x32_bf16 v[66:69], a[12:15], v[90:93], v[66:69]\n\t" \
        "ds_read_b64_tr_b16 a[30:31], v7 offset:33824\n\t" \
        "v_cvt_pk_bf16_f32 v100, v130, v131\n\t" \
        "s_waitcnt lgkmcnt(14)\n\t" \
        "v_mfma_f32_16x16x32_bf16 v[46:49], a[16:19], v[86:89], v[46:49]\n\t" \
        "ds_read_b64_tr_b16 a[32:33], v6 offset:30784\n\t" \
        "ds_read_b64_tr_b16 a[34:35], v6 offset:33856\n\t" \
        "v_mfma_f32_16x16x32_bf16 v[70:73], a[16:19], v[90:93], v[70:73]\n\t" \
        "ds_read_b64_tr_b16 a[36:37], v7 offset:30816\n\t" \
        "v_cvt_pk_bf16_f32 v101, v132, v133\n\t" \
        "s_waitcnt lgkmcnt(14)\n\t" \
        "v_mfma_f32_16x16x32_bf16 v[50:53], a[20:23], v[86:89], v[50:53]\n\t" \
        "ds_read_b64_tr_b16 a[38:39], v7 offset:33888\n\t" \
        "ds_read_b64_tr_b16 a[40:41], v6 offset:30848\n\t" \
        "v_mfma_f32_16x16x32_bf16 v[74:77], a[20:23], v[90:93], v[74:77]\n\t" \
        "ds_read_b64_tr_b16 a[42:43], v6 offset:33920\n\t" \
        "v_cvt_pk_bf16_f32 v78, v102, v103\n\t" \
        "v_mfma_f32_16x16x32_bf16 v[118:121], a[112:115], v[154:157], 0\n\t" \
        "s_waitcnt vmcnt(6)\n\t" \
        "s_barrier\n\t" \
        "ds_read_b64_tr_b16 a[44:45], v7 offset:30880\n\t" \
        "v_cvt_pk_bf16_f32 v79, v104, v105\n\t" \
        "s_waitcnt lgkmcnt(14)\n\t" \
        "v_mfma_f32_16x16x32_bf16 v[122:125], a[116:119], v[154:157], 0\n\t" \
        "ds_read_b64_tr_b16 a[46:47], v7 offset:33952\n\t" \
        "ds_read_b64_tr_b16 a[128:129], v134 offset:128\n\t" \
        "v_mfma_f32_16x16x32_bf16 v[126:129], a[120:123], v[158:161], 0\n\t" \
        "ds_read_b64_tr_b16 a[130:131], v135 offset:128\n\t" \
        "s_waitcnt lgkmcnt(12)\n\t" \
        "v_xor_b32 v142, v174, v11\n\t" \
        "v_mfma_f32_16x16x32_bf16 v[130:133], a[124:127], v[158:161], 0\n\t" \
        "s_add_u32 m0, s39, 12288\n\t" \
        "s_add_u32 s57, s40, 73728\n\t" \
        "buffer_load_dwordx4 v3, s[24:27], s57 offen offset:0 lds\n\t" \
        "v_mfma_f32_16x16x32_bf16 v[30:33], a[24:27], v[94:97], v[30:33]\n\t" \
        "ds_read_b64_tr_b16 a[132:133], v138 offset:128\n\t" \
        "ds_read_b64_tr_b16 a[134:135], v139 offset:128\n\t" \
        "v_mfma_f32_16x16x32_bf16 v[54:57], a[24:27], v[98:101], v[54:57]\n\t" \
        "ds_read_b64_tr_b16 a[136:137], v136 offset:128\n\t" \
        "v_xor_b32 v143, v175, v11\n\t" \
        "s_waitcnt lgkmcnt(13)\n\t" \
        "v_mfma_f32_16x16x32_bf16 v[34:37], a[28:31], v[94:97], v[34:37]\n\t" \
        "ds_read_b64_tr_b16 a[138:139], v137 offset:128\n\t" \
        "v_xor_b32 v144, v176, v11\n\t" \
        "v_mfma_f32_16x16x32_bf16 v[58:61], a[28:31], v[98:101], v[58:61]\n\t" \
        "ds_read_b64_tr_b16 a[140:141], v140 offset:128\n\t" \
        "ds_read_b64_tr_b16 a[142:143], v141 offset:128\n\t" \
        "s_waitcnt lgkmcnt(13)\n\t" \
        "v_mfma_f32_16x16x32_bf16 v[38:41], a[32:35], v[94:97], v[38:41]\n\t" \
        "ds_read_b64_tr_b16 a[48:49], v6 offset:36864\n\t" \
        "v_xor_b32 v146, v174, v12\n\t" \
        "v_mfma_f32_16x16x32_bf16 v[62:65], a[32:35], v[98:101], v[62:65]\n\t" \
        "buffer_load_dwordx4 v4, s[24:27], s57 offen offset:1024 lds\n\t" \
        "s_waitcnt lgkmcnt(11)\n\t" \
        "v_mfma_f32_16x16x32_bf16 v[42:45], a[36:39], v[94:97], v[42:45]\n\t" \
        "ds_read_b64_tr_b16 a[50:51], v6 offset:39936\n\t" \
        "ds_read_b64_tr_b16 a[52:53], v7 offset:36896\n\t" \
        "v_xor_b32 v145, v177, v11\n\t" \
        "v_xor_b32 v147, v175, v12\n\t" \
        "v_mfma_f32_16x16x32_bf16 v[66:69], a[36:39], v[98:101], v[66:69]\n\t" \
        "ds_read_b64 v[152:153], v10 offset:384\n\t" \
        "v_xor_b32 v148, v176, v12\n\t" \
        "v_xor_b32 v149, v177, v12\n\t" \
        "v_mfma_f32_16x16x32_bf16 v[46:49], a[40:43], v[94:97], v[46:49]\n\t" \
        "ds_read_b64_tr_b16 a[54:55], v7 offset:39968\n\t" \
        "ds_read_b64_tr_b16 a[56:57], v6 offset:36928\n\t" \
        "v_cvt_pk_bf16_f32 v80, v106, v107\n\t" \
        "v_mfma_f32_16x16x32_bf16 v[70:73], a[40:43], v[98:101], v[70:73]\n\t" \
        "ds_read_b64_tr_b16 a[58:59], v6 offset:40000\n\t" \
        "v_cvt_pk_bf16_f32 v81, v108, v109\n\t" \
        "v_cvt_pk_bf16_f32 v82, v110, v111\n\t" \
        "s_waitcnt lgkmcnt(13)\n\t" \
        "v_mfma_f32_16x16x32_bf16 v[50:53], a[44:47], v[94:97], v[50:53]\n\t" \
        "ds_read_b64_tr_b16 a[60:61], v7 offset:36960\n\t" \
        "v_cvt_pk_bf16_f32 v83, v112, v113\n\t" \
        "v_cvt_pk_bf16_f32 v84, v114, v115\n\t" \
        "v_mfma_f32_16x16x32_bf16 v[74:77], a[44:47], v[98:101], v[74:77]\n\t" \
        "buffer_load_dwordx4 v5, s[24:27], s57 offen offset:2048 lds\n\t" \
        "v_mfma_f32_16x16x32_bf16 v[102:105], a[128:131], v[154:157], 0\n\t" \
        "ds_read_b64_tr_b16 a[62:63], v7 offset:40032\n\t" \
        "ds_read_b64_tr_b16 a[64:65], v6 offset:36992\n\t" \
        "v_cvt_pk_bf16_f32 v85, v116, v117\n\t" \
        "s_waitcnt lgkmcnt(12)\n\t" \
        "v_mfma_f32_16x16x32_bf16 v[106:109], a[132:135], v[154:157], 0\n\t" \
        "ds_read_b64_tr_b16 a[66:67], v6 offset:40064\n\t" \
        "v_cvt_pk_bf16_f32 v86, v118, v119\n\t" \
        "v_mfma_f32_16x16x32_bf16 v[110:113], a[136:139], v[158:161], 0\n\t" \
        "ds_read_b64_tr_b16 a[68:69], v7 offset:37024\n\t" \
        "ds_read_b64_tr_b16 a[70:71], v7 offset:40096\n\t" \
        "s_waitcnt lgkmcnt(12)\n\t" \
        "v_mfma_f32_16x16x32_bf16 v[114:117], a[140:143], v[158:161], 0\n\t" \
        "ds_read_b64_tr_b16 a[96:97], v142 offset:0\n\t" \
        "v_cvt_pk_bf16_f32 v87, v120, v121\n\t" \
        "s_waitcnt lgkmcnt(12)\n\t" \
        "v_mfma_f32_16x16x32_bf16 v[30:33], a[48:51], v[78:81], v[30:33]\n\t" \
        "ds_read_b64_tr_b16 a[98:99], v143 offset:0\n\t" \
        "s_waitcnt lgkmcnt(11)\n\t" \
        "v_and_b32 v162, v152, v25\n\t" \
        "v_mfma_f32_16x16x32_bf16 v[54:57], a[48:51], v[82:85], v[54:57]\n\t" \
        "ds_read_b64_tr_b16 a[100:101], v146 offset:0\n\t" \
        "ds_read_b64_tr_b16 a[102:103], v147 offset:0\n\t" \
        "s_waitcnt lgkmcnt(12)\n\t" \
        "v_mfma_f32_16x16x32_bf16 v[34:37], a[52:55], v[78:81], v[34:37]\n\t" \
        "ds_read_b64_tr_b16 a[104:105], v144 offset:0\n\t" \
        "v_and_b32 v163, v152, v26\n\t" \
        "v_mfma_f32_16x16x32_bf16 v[58:61], a[52:55], v[82:85], v[58:61]\n\t" \
        "ds_read_b64_tr_b16 a[106:107], v145 offset:0\n\t" \
        "ds_read_b64_tr_b16 a[108:109], v148 offset:0\n\t" \
        "s_waitcnt lgkmcnt(13)\n\t" \
        "v_mfma_f32_16x16x32_bf16 v[38:41], a[56:59], v[78:81], v[38:41]\n\t" \
        "ds_read_b64_tr_b16 a[110:111], v149 offset:0\n\t" \
        "v_and_b32 v164, v152, v27\n\t" \
        "v_mfma_f32_16x16x32_bf16 v[62:65], a[56:59], v[82:85], v[62:65]\n\t" \
        "ds_read_b64_tr_b16 a[72:73], v6 offset:43008\n\t" \
        "v_and_b32 v165, v152, v28\n\t" \
        "s_waitcnt lgkmcnt(13)\n\t" \
        "v_mfma_f32_16x16x32_bf16 v[42:45], a[60:63], v[78:81], v[42:45]\n\t" \
        "ds_read_b64_tr_b16 a[74:75], v6 offset:46080\n\t" \
        "ds_read_b64_tr_b16 a[76:77], v7 offset:43040\n\t" \
        "v_mfma_f32_16x16x32_bf16 v[66:69], a[60:63], v[82:85], v[66:69]\n\t" \
        "ds_read_b64_tr_b16 a[78:79], v7 offset:46112\n\t" \
        "v_and_b32 v166, v153, v25\n\t" \
        "s_waitcnt lgkmcnt(14)\n\t" \
        "v_mfma_f32_16x16x32_bf16 v[46:49], a[64:67], v[78:81], v[46:49]\n\t" \
        "ds_read_b64_tr_b16 a[80:81], v6 offset:43072\n\t" \
        "ds_read_b64_tr_b16 a[82:83], v6 offset:46144\n\t" \
        "v_cvt_pk_bf16_f32 v88, v122, v123\n\t" \
        "v_and_b32 v167, v153, v26\n\t" \
        "v_mfma_f32_16x16x32_bf16 v[70:73], a[64:67], v[82:85], v[70:73]\n\t" \
        "ds_read_b64_tr_b16 a[84:85], v7 offset:43104\n\t" \
        "v_cvt_pk_bf16_f32 v89, v124, v125\n\t" \
        "v_cvt_pk_bf16_f32 v90, v126, v127\n\t" \
        "s_waitcnt lgkmcnt(14)\n\t" \
        "v_mfma_f32_16x16x32_bf16 v[50:53], a[68:71], v[78:81], v[50:53]\n\t" \
        "ds_read_b64_tr_b16 a[86:87], v7 offset:46176\n\t" \
        "ds_read_b64_tr_b16 a[88:89], v6 offset:43136\n\t" \
        "v_and_b32 v168, v153, v27\n\t" \
        "v_mfma_f32_16x16x32_bf16 v[74:77], a[68:71], v[82:85], v[74:77]\n\t" \
        "ds_read_b64_tr_b16 a[90:91], v6 offset:46208\n\t" \
        "v_cvt_pk_bf16_f32 v91, v128, v129\n\t" \
        "v_cvt_pk_bf16_f32 v92, v130, v131\n\t" \
        "s_waitcnt lgkmcnt(14)\n\t" \
        "v_mfma_f32_16x16x32_bf16 v[118:121], a[96:99], v[162:165], 0\n\t" \
        "s_waitcnt vmcnt(6)\n\t" \
        "s_barrier\n\t" \
        "ds_read_b64_tr_b16 a[92:93], v7 offset:43168\n\t" \
        "v_and_b32 v169, v153, v28\n\t" \
        "v_cvt_pk_bf16_f32 v93, v132, v133\n\t" \
        "v_mfma_f32_16x16x32_bf16 v[122:125], a[100:103], v[162:165], 0\n\t" \
        "ds_read_b64_tr_b16 a[94:95], v7 offset:46240\n\t" \
        "ds_read_b64_tr_b16 a[112:113], v142 offset:64\n\t" \
        "s_waitcnt lgkmcnt(14)\n\t" \
        "v_mfma_f32_16x16x32_bf16 v[126:129], a[104:107], v[166:169], 0\n\t" \
        "ds_read_b64_tr_b16 a[114:115], v143 offset:64\n\t" \
        "v_cvt_pk_bf16_f32 v94, v102, v103\n\t" \
        "s_waitcnt lgkmcnt(14)\n\t" \
        "v_mfma_f32_16x16x32_bf16 v[130:133], a[108:111], v[166:169], 0\n\t" \
        "s_add_u32 m0, s39, 24576\n\t" \
        "s_add_u32 s57, s40, 86016\n\t" \
        "buffer_load_dwordx4 v3, s[24:27], s57 offen offset:0 lds\n\t" \
        "s_waitcnt lgkmcnt(12)\n\t" \
        "v_mfma_f32_16x16x32_bf16 v[30:33], a[72:75], v[86:89], v[30:33]\n\t" \
        "ds_read_b64_tr_b16 a[116:117], v146 offset:64\n\t" \
        "ds_read_b64_tr_b16 a[118:119], v147 offset:64\n\t" \
        "v_mfma_f32_16x16x32_bf16 v[54:57], a[72:75], v[90:93], v[54:57]\n\t" \
        "ds_read_b64_tr_b16 a[120:121], v144 offset:64\n\t" \
        "v_cvt_pk_bf16_f32 v95, v104, v105\n\t" \
        "s_waitcnt lgkmcnt(13)\n\t" \
        "v_mfma_f32_16x16x32_bf16 v[34:37], a[76:79], v[86:89], v[34:37]\n\t" \
        "ds_read_b64_tr_b16 a[122:123], v145 offset:64\n\t" \
        "ds_read_b64_tr_b16 a[124:125], v148 offset:64\n\t" \
        "v_mfma_f32_16x16x32_bf16 v[58:61], a[76:79], v[90:93], v[58:61]\n\t" \
        "ds_read_b64_tr_b16 a[126:127], v149 offset:64\n\t" \
        "v_cvt_pk_bf16_f32 v96, v106, v107\n\t" \
        "s_waitcnt lgkmcnt(13)\n\t" \
        "v_mfma_f32_16x16x32_bf16 v[38:41], a[80:83], v[86:89], v[38:41]\n\t" \
        "ds_read_b64_tr_b16 a[0:1], v6 offset:49152\n\t" \
        "ds_read_b64_tr_b16 a[2:3], v6 offset:52224\n\t" \
        "v_mfma_f32_16x16x32_bf16 v[62:65], a[80:83], v[90:93], v[62:65]\n\t" \
        "buffer_load_dwordx4 v4, s[24:27], s57 offen offset:1024 lds\n\t" \
        "s_waitcnt lgkmcnt(12)\n\t" \
        "v_mfma_f32_16x16x32_bf16 v[42:45], a[84:87], v[86:89], v[42:45]\n\t" \
        "ds_read_b128 v[170:173], v9 offset:512\n\t" \
        "v_cvt_pk_bf16_f32 v97, v108, v109\n\t" \
        "v_mfma_f32_16x16x32_bf16 v[66:69], a[84:87], v[90:93], v[66:69]\n\t" \
        "ds_read_b64_tr_b16 a[4:5], v7 offset:49184\n\t" \
        "v_cvt_pk_bf16_f32 v98, v110, v111\n\t" \
        "v_mfma_f32_16x16x32_bf16 v[46:49], a[88:91], v[86:89], v[46:49]\n\t" \
        "ds_read_b64_tr_b16 a[6:7], v7 offset:52256\n\t" \
        "ds_read_b64_tr_b16 a[8:9], v6 offset:49216\n\t" \
        "v_mfma_f32_16x16x32_bf16 v[70:73], a[88:91], v[90:93], v[70:73]\n\t" \
        "ds_read_b64_tr_b16 a[10:11], v6 offset:52288\n\t" \
        "v_cvt_pk_bf16_f32 v99, v112, v113\n\t" \
        "s_waitcnt lgkmcnt(13)\n\t" \
        "v_mfma_f32_16x16x32_bf16 v[50:53], a[92:95], v[86:89], v[50:53]\n\t" \
        "ds_read_b64_tr_b16 a[12:13], v7 offset:49248\n\t" \
        "ds_read_b64_tr_b16 a[14:15], v7 offset:52320\n\t" \
        "v_cvt_pk_bf16_f32 v100, v114, v115\n\t" \
        "v_mfma_f32_16x16x32_bf16 v[74:77], a[92:95], v[90:93], v[74:77]\n\t" \
        "buffer_load_dwordx4 v5, s[24:27], s57 offen offset:2048 lds\n\t" \
        "v_mfma_f32_16x16x32_bf16 v[102:105], a[112:115], v[162:165], 0\n\t" \
        "ds_read_b64_tr_b16 a[16:17], v6 offset:49280\n\t" \
        "v_cvt_pk_bf16_f32 v101, v116, v117\n\t" \
        "s_waitcnt lgkmcnt(11)\n\t" \
        "v_mfma_f32_16x16x32_bf16 v[106:109], a[116:119], v[162:165], 0\n\t" \
        "ds_read_b64_tr_b16 a[18:19], v6 offset:52352\n\t" \
        "ds_read_b64_tr_b16 a[20:21], v7 offset:49312\n\t" \
        "v_mfma_f32_16x16x32_bf16 v[110:113], a[120:123], v[166:169], 0\n\t" \
        "ds_read_b64_tr_b16 a[22:23], v7 offset:52384\n\t" \
        "v_cvt_pk_bf16_f32 v78, v118, v119\n\t" \
        "s_waitcnt lgkmcnt(11)\n\t" \
        "v_mfma_f32_16x16x32_bf16 v[114:117], a[124:127], v[166:169], 0\n\t" \
        "ds_read_b64_tr_b16 a[128:129], v142 offset:128\n\t" \
        "s_waitcnt lgkmcnt(11)\n\t" \
        "v_xor_b32 v134, v170, v11\n\t" \
        "v_mfma_f32_16x16x32_bf16 v[30:33], a[0:3], v[94:97], v[30:33]\n\t" \
        "ds_read_b64_tr_b16 a[130:131], v143 offset:128\n\t" \
        "ds_read_b64_tr_b16 a[132:133], v146 offset:128\n\t" \
        "v_mfma_f32_16x16x32_bf16 v[54:57], a[0:3], v[98:101], v[54:57]\n\t" \
        "ds_read_b64_tr_b16 a[134:135], v147 offset:128\n\t" \
        "v_xor_b32 v135, v171, v11\n\t" \
        "s_waitcnt lgkmcnt(12)\n\t" \
        "v_mfma_f32_16x16x32_bf16 v[34:37], a[4:7], v[94:97], v[34:37]\n\t" \
        "ds_read_b64_tr_b16 a[136:137], v144 offset:128\n\t" \
        "ds_read_b64_tr_b16 a[138:139], v145 offset:128\n\t" \
        "v_mfma_f32_16x16x32_bf16 v[58:61], a[4:7], v[98:101], v[58:61]\n\t" \
        "ds_read_b64_tr_b16 a[140:141], v148 offset:128\n\t" \
        "v_xor_b32 v136, v172, v11\n\t" \
        "s_waitcnt lgkmcnt(13)\n\t" \
        "v_mfma_f32_16x16x32_bf16 v[38:41], a[8:11], v[94:97], v[38:41]\n\t" \
        "ds_read_b64_tr_b16 a[142:143], v149 offset:128\n\t" \
        "ds_read_b64_tr_b16 a[24:25], v6 offset:55296\n\t" \
        "v_mfma_f32_16x16x32_bf16 v[62:65], a[8:11], v[98:101], v[62:65]\n\t" \
        "ds_read_b64_tr_b16 a[26:27], v6 offset:58368\n\t" \
        "v_xor_b32 v138, v170, v12\n\t" \
        "s_waitcnt lgkmcnt(14)\n\t" \
        "v_mfma_f32_16x16x32_bf16 v[42:45], a[12:15], v[94:97], v[42:45]\n\t" \
        "ds_read_b64_tr_b16 a[28:29], v7 offset:55328\n\t" \
        "v_xor_b32 v137, v173, v11\n\t" \
        "v_mfma_f32_16x16x32_bf16 v[66:69], a[12:15], v[98:101], v[66:69]\n\t" \
        "ds_read_b64 v[150:151], v10 offset:512\n\t" \
        "ds_read_b64_tr_b16 a[30:31], v7 offset:58400\n\t" \
        "v_xor_b32 v139, v171, v12\n\t" \
        "v_xor_b32 v140, v172, v12\n\t" \
        "s_waitcnt lgkmcnt(14)\n\t" \
        "v_mfma_f32_16x16x32_bf16 v[46:49], a[16:19], v[94:97], v[46:49]\n\t" \
        "ds_read_b64_tr_b16 a[32:33], v6 offset:55360\n\t" \
        "v_xor_b32 v141, v173, v12\n\t" \
        "v_cvt_pk_bf16_f32 v79, v120, v121\n\t" \
        "v_mfma_f32_16x16x32_bf16 v[70:73], a[16:19], v[98:101], v[70:73]\n\t" \
        "ds_read_b64_tr_b16 a[34:35], v6 offset:58432\n\t" \
        "ds_read_b64_tr_b16 a[36:37], v7 offset:55392\n\t" \
        "v_cvt_pk_bf16_f32 v80, v122, v123\n\t" \
        "s_waitcnt lgkmcnt(15)\n\t" \
        "v_mfma_f32_16x16x32_bf16 v[50:53], a[20:23], v[94:97], v[50:53]\n\t" \
        "ds_read_b64_tr_b16 a[38:39], v7 offset:58464\n\t" \
        "v_cvt_pk_bf16_f32 v81, v124, v125\n\t" \
        "v_cvt_pk_bf16_f32 v82, v126, v127\n\t" \
        "v_mfma_f32_16x16x32_bf16 v[74:77], a[20:23], v[98:101], v[74:77]\n\t" \
        "ds_read_b64_tr_b16 a[40:41], v6 offset:55424\n\t" \
        "ds_read_b64_tr_b16 a[42:43], v6 offset:58496\n\t" \
        "v_cvt_pk_bf16_f32 v83, v128, v129\n\t" \
        "v_cvt_pk_bf16_f32 v84, v130, v131\n\t" \
        "s_waitcnt lgkmcnt(15)\n\t" \
        "v_mfma_f32_16x16x32_bf16 v[118:121], a[128:131], v[162:165], 0\n\t" \
        "s_waitcnt vmcnt(6)\n\t" \
        "s_barrier\n\t" \
        "ds_read_b64_tr_b16 a[44:45], v7 offset:55456\n\t" \
        "v_cvt_pk_bf16_f32 v85, v132, v133\n\t" \
        "v_mfma_f32_16x16x32_bf16 v[122:125], a[132:135], v[162:165], 0\n\t" \
        "ds_read_b64_tr_b16 a[46:47], v7 offset:58528\n\t" \
        "v_cvt_pk_bf16_f32 v86, v102, v103\n\t" \
        "s_waitcnt lgkmcnt(14)\n\t" \
        "v_mfma_f32_16x16x32_bf16 v[126:129], a[136:139], v[166:169], 0\n\t" \
        "ds_read_b64_tr_b16 a[96:97], v134 offset:0\n\t" \
        "ds_read_b64_tr_b16 a[98:99], v135 offset:0\n\t" \
        "s_waitcnt lgkmcnt(14)\n\t" \
        "v_mfma_f32_16x16x32_bf16 v[130:133], a[140:143], v[166:169], 0\n\t" \
        "s_add_u32 m0, s39, 36864\n\t" \
        "s_add_u32 s57, s40, 98304\n\t" \
        "buffer_load_dwordx4 v3, s[24:27], s57 offen offset:0 lds\n\t" \
        "s_waitcnt lgkmcnt(13)\n\t" \
        "v_mfma_f32_16x16x32_bf16 v[30:33], a[24:27], v[78:81], v[30:33]\n\t" \
        "ds_read_b64_tr_b16 a[100:101], v138 offset:0\n\t" \
        "s_waitcnt lgkmcnt(12)\n\t" \
        "v_and_b32 v154, v150, v25\n\t" \
        "v_mfma_f32_16x16x32_bf16 v[54:57], a[24:27], v[82:85], v[54:57]\n\t" \
        "ds_read_b64_tr_b16 a[102:103], v139 offset:0\n\t" \
        "ds_read_b64_tr_b16 a[104:105], v136 offset:0\n\t" \
        "s_waitcnt lgkmcnt(13)\n\t" \
        "v_mfma_f32_16x16x32_bf16 v[34:37], a[28:31], v[78:81], v[34:37]\n\t" \
        "ds_read_b64_tr_b16 a[106:107], v137 offset:0\n\t" \
        "v_and_b32 v155, v150, v26\n\t" \
        "v_mfma_f32_16x16x32_bf16 v[58:61], a[28:31], v[82:85], v[58:61]\n\t" \
        "ds_read_b64_tr_b16 a[108:109], v140 offset:0\n\t" \
        "ds_read_b64_tr_b16 a[110:111], v141 offset:0\n\t" \
        "v_and_b32 v156, v150, v27\n\t" \
        "s_waitcnt lgkmcnt(13)\n\t" \
        "v_mfma_f32_16x16x32_bf16 v[38:41], a[32:35], v[78:81], v[38:41]\n\t" \
        "ds_read_b64_tr_b16 a[48:49], v6 offset:0\n\t" \
        "v_and_b32 v157, v150, v28\n\t" \
        "v_and_b32 v158, v151, v25\n\t" \
        "v_mfma_f32_16x16x32_bf16 v[62:65], a[32:35], v[82:85], v[62:65]\n\t" \
        "buffer_load_dwordx4 v4, s[24:27], s57 offen offset:1024 lds\n\t" \
        "s_waitcnt lgkmcnt(11)\n\t" \
        "v_mfma_f32_16x16x32_bf16 v[42:45], a[36:39], v[78:81], v[42:45]\n\t" \
        "ds_read_b64_tr_b16 a[50:51], v6 offset:3072\n\t" \
        "ds_read_b64_tr_b16 a[52:53], v7 offset:32\n\t" \
        "v_cvt_pk_bf16_f32 v87, v104, v105\n\t" \
        "v_mfma_f32_16x16x32_bf16 v[66:69], a[36:39], v[82:85], v[66:69]\n\t" \
        "ds_read_b64_tr_b16 a[54:55], v7 offset:3104\n\t" \
        "v_cvt_pk_bf16_f32 v88, v106, v107\n\t" \
        "v_and_b32 v159, v151, v26\n\t" \
        "v_mfma_f32_16x16x32_bf16 v[46:49], a[40:43], v[78:81], v[46:49]\n\t" \
        "ds_read_b64_tr_b16 a[56:57], v6 offset:64\n\t" \
        "v_cvt_pk_bf16_f32 v89, v108, v109\n\t" \
        "v_cvt_pk_bf16_f32 v90, v110, v111\n\t" \
        "v_mfma_f32_16x16x32_bf16 v[70:73], a[40:43], v[82:85], v[70:73]\n\t" \
        "ds_read_b64_tr_b16 a[58:59], v6 offset:3136\n\t" \
        "ds_read_b64_tr_b16 a[60:61], v7 offset:96\n\t" \
        "v_and_b32 v160, v151, v27\n\t" \
        "s_waitcnt lgkmcnt(13)\n\t" \
        "v_mfma_f32_16x16x32_bf16 v[50:53], a[44:47], v[78:81], v[50:53]\n\t" \
        "ds_read_b64_tr_b16 a[62:63], v7 offset:3168\n\t" \
        "v_cvt_pk_bf16_f32 v91, v112, v113\n\t" \
        "v_cvt_pk_bf16_f32 v92, v114, v115\n\t" \
        "v_mfma_f32_16x16x32_bf16 v[74:77], a[44:47], v[82:85], v[74:77]\n\t" \
        "buffer_load_dwordx4 v5, s[24:27], s57 offen offset:2048 lds\n\t" \
        "v_mfma_f32_16x16x32_bf16 v[102:105], a[96:99], v[154:157], 0\n\t" \
        "ds_read_b64_tr_b16 a[64:65], v6 offset:128\n\t" \
        "ds_read_b64_tr_b16 a[66:67], v6 offset:3200\n\t" \
        "v_and_b32 v161, v151, v28\n\t" \
        "v_cvt_pk_bf16_f32 v93, v116, v117\n\t" \
        "s_waitcnt lgkmcnt(12)\n\t" \
        "v_mfma_f32_16x16x32_bf16 v[106:109], a[100:103], v[154:157], 0\n\t" \
        "ds_read_b64_tr_b16 a[68:69], v7 offset:160\n\t" \
        "v_cvt_pk_bf16_f32 v94, v118, v119\n\t" \
        "v_mfma_f32_16x16x32_bf16 v[110:113], a[104:107], v[158:161], 0\n\t" \
        "ds_read_b64_tr_b16 a[70:71], v7 offset:3232\n\t" \
        "v_cvt_pk_bf16_f32 v95, v120, v121\n\t" \
        "s_waitcnt lgkmcnt(11)\n\t" \
        "v_mfma_f32_16x16x32_bf16 v[114:117], a[108:111], v[158:161], 0\n\t" \
        "ds_read_b64_tr_b16 a[112:113], v134 offset:64\n\t" \
        "ds_read_b64_tr_b16 a[114:115], v135 offset:64\n\t" \
        "s_waitcnt lgkmcnt(12)\n\t" \
        "v_mfma_f32_16x16x32_bf16 v[30:33], a[48:51], v[86:89], v[30:33]\n\t" \
        "ds_read_b64_tr_b16 a[116:117], v138 offset:64\n\t" \
        "v_cvt_pk_bf16_f32 v96, v122, v123\n\t" \
        "v_mfma_f32_16x16x32_bf16 v[54:57], a[48:51], v[90:93], v[54:57]\n\t" \
        "ds_read_b64_tr_b16 a[118:119], v139 offset:64\n\t" \
        "ds_read_b64_tr_b16 a[120:121], v136 offset:64\n\t" \
        "s_waitcnt lgkmcnt(13)\n\t" \
        "v_mfma_f32_16x16x32_bf16 v[34:37], a[52:55], v[86:89], v[34:37]\n\t" \
        "ds_read_b64_tr_b16 a[122:123], v137 offset:64\n\t" \
        "v_cvt_pk_bf16_f32 v97, v124, v125\n\t" \
        "v_mfma_f32_16x16x32_bf16 v[58:61], a[52:55], v[90:93], v[58:61]\n\t" \
        "ds_read_b64_tr_b16 a[124:125], v140 offset:64\n\t" \
        "v_cvt_pk_bf16_f32 v98, v126, v127\n\t" \
        "s_waitcnt lgkmcnt(12)\n\t" \
        "v_mfma_f32_16x16x32_bf16 v[38:41], a[56:59], v[86:89], v[38:41]\n\t" \
        "ds_read_b64_tr_b16 a[126:127], v141 offset:64\n\t" \
        "ds_read_b64_tr_b16 a[72:73], v6 offset:6144\n\t" \
        "v_mfma_f32_16x16x32_bf16 v[62:65], a[56:59], v[90:93], v[62:65]\n\t" \
        "ds_read_b64_tr_b16 a[74:75], v6 offset:9216\n\t" \
        "v_cvt_pk_bf16_f32 v99, v128, v129\n\t" \
        "s_waitcnt lgkmcnt(14)\n\t" \
        "v_mfma_f32_16x16x32_bf16 v[42:45], a[60:63], v[86:89], v[42:45]\n\t" \
        "ds_read_b128 v[174:177], v9 offset:640\n\t" \
        "ds_read_b64_tr_b16 a[76:77], v7 offset:6176\n\t" \
        "v_mfma_f32_16x16x32_bf16 v[66:69], a[60:63], v[90:93], v[66:69]\n\t" \
        "ds_read_b64_tr_b16 a[78:79], v7 offset:9248\n\t" \
        "v_cvt_pk_bf16_f32 v100, v130, v131\n\t" \
        "s_waitcnt lgkmcnt(14)\n\t" \
        "v_mfma_f32_16x16x32_bf16 v[46:49], a[64:67], v[86:89], v[46:49]\n\t" \
        "ds_read_b64_tr_b16 a[80:81], v6 offset:6208\n\t" \
        "ds_read_b64_tr_b16 a[82:83], v6 offset:9280\n\t" \
        "v_mfma_f32_16x16x32_bf16 v[70:73], a[64:67], v[90:93], v[70:73]\n\t" \
        "ds_read_b64_tr_b16 a[84:85], v7 offset:6240\n\t" \
        "v_cvt_pk_bf16_f32 v101, v132, v133\n\t" \
        "s_waitcnt lgkmcnt(14)\n\t" \
        "v_mfma_f32_16x16x32_bf16 v[50:53], a[68:71], v[86:89], v[50:53]\n\t" \
        "ds_read_b64_tr_b16 a[86:87], v7 offset:9312\n\t" \
        "v_cvt_pk_bf16_f32 v78, v102, v103\n\t" \
        "v_mfma_f32_16x16x32_bf16 v[74:77], a[68:71], v[90:93], v[74:77]\n\t" \
        "ds_read_b64_tr_b16 a[88:89], v6 offset:6272\n\t" \
        "ds_read_b64_tr_b16 a[90:91], v6 offset:9344\n\t" \
        "v_mfma_f32_16x16x32_bf16 v[118:121], a[112:115], v[154:157], 0\n\t" \
        "s_waitcnt vmcnt(6)\n\t" \
        "s_barrier\n\t" \
        "ds_read_b64_tr_b16 a[92:93], v7 offset:6304\n\t" \
        "v_cvt_pk_bf16_f32 v79, v104, v105\n\t" \
        "s_waitcnt lgkmcnt(14)\n\t" \
        "v_mfma_f32_16x16x32_bf16 v[122:125], a[116:119], v[154:157], 0\n\t" \
        "ds_read_b64_tr_b16 a[94:95], v7 offset:9376\n\t" \
        "ds_read_b64_tr_b16 a[128:129], v134 offset:128\n\t" \
        "v_mfma_f32_16x16x32_bf16 v[126:129], a[120:123], v[158:161], 0\n\t" \
        "ds_read_b64_tr_b16 a[130:131], v135 offset:128\n\t" \
        "v_cvt_pk_bf16_f32 v80, v106, v107\n\t" \
        "s_waitcnt lgkmcnt(14)\n\t" \
        "v_mfma_f32_16x16x32_bf16 v[130:133], a[124:127], v[158:161], 0\n\t" \
        "s_add_u32 m0, s39, 49152\n\t" \
        "s_add_u32 s57, s40, 110592\n\t" \
        "buffer_load_dwordx4 v3, s[24:27], s57 offen offset:0 lds\n\t" \
        "s_waitcnt lgkmcnt(13)\n\t" \
        "v_mfma_f32_16x16x32_bf16 v[30:33], a[72:75], v[94:97], v[30:33]\n\t" \
        "ds_read_b64_tr_b16 a[132:133], v138 offset:128\n\t" \
        "s_waitcnt lgkmcnt(12)\n\t" \
        "v_xor_b32 v142, v174, v11\n\t" \
        "v_mfma_f32_16x16x32_bf16 v[54:57], a[72:75], v[98:101], v[54:57]\n\t" \
        "ds_read_b64_tr_b16 a[134:135], v139 offset:128\n\t" \
        "ds_read_b64_tr_b16 a[136:137], v136 offset:128\n\t" \
        "s_waitcnt lgkmcnt(13)\n\t" \
        "v_mfma_f32_16x16x32_bf16 v[34:37], a[76:79], v[94:97], v[34:37]\n\t" \
        "ds_read_b64_tr_b16 a[138:139], v137 offset:128\n\t" \
        "v_xor_b32 v143, v175, v11\n\t" \
        "v_mfma_f32_16x16x32_bf16 v[58:61], a[76:79], v[98:101], v[58:61]\n\t" \
        "ds_read_b64_tr_b16 a[140:141], v140 offset:128\n\t" \
        "ds_read_b64_tr_b16 a[142:143], v141 offset:128\n\t" \
        "s_waitcnt lgkmcnt(13)\n\t" \
        "v_mfma_f32_16x16x32_bf16 v[38:41], a[80:83], v[94:97], v[38:41]\n\t" \
        "ds_read_b64_tr_b16 a[0:1], v6 offset:12288\n\t" \
        "v_xor_b32 v144, v176, v11\n\t" \
        "v_mfma_f32_16x16x32_bf16 v[62:65], a[80:83], v[98:101], v[62:65]\n\t" \
        "buffer_load_dwordx4 v4, s[24:27], s57 offen offset:1024 lds\n\t" \
        "s_waitcnt lgkmcnt(11)\n\t" \
        "v_mfma_f32_16x16x32_bf16 v[42:45], a[84:87], v[94:97], v[42:45]\n\t" \
        "ds_read_b64_tr_b16 a[2:3], v6 offset:15360\n\t" \
        "ds_read_b64_tr_b16 a[4:5], v7 offset:12320\n\t" \
        "v_xor_b32 v146, v174, v12\n\t" \
        "v_xor_b32 v145, v177, v11\n\t" \
        "v_mfma_f32_16x16x32_bf16 v[66:69], a[84:87], v[98:101], v[66:69]\n\t" \
        "ds_read_b64 v[152:153], v10 offset:640\n\t" \
        "v_xor_b32 v147, v175, v12\n\t" \
        "v_xor_b32 v148, v176, v12\n\t" \
        "v_mfma_f32_16x16x32_bf16 v[46:49], a[88:91], v[94:97], v[46:49]\n\t" \
        "ds_read_b64_tr_b16 a[6:7], v7 offset:15392\n\t" \
        "v_xor_b32 v149, v177, v12\n\t" \
        "v_mfma_f32_16x16x32_bf16 v[70:73], a[88:91], v[98:101], v[70:73]\n\t" \
        "ds_read_b64_tr_b16 a[8:9], v6 offset:12352\n\t" \
        "ds_read_b64_tr_b16 a[10:11], v6 offset:15424\n\t" \
        "v_cvt_pk_bf16_f32 v81, v108, v109\n\t" \
        "v_cvt_pk_bf16_f32 v82, v110, v111\n\t" \
        "s_waitcnt lgkmcnt(13)\n\t" \
        "v_mfma_f32_16x16x32_bf16 v[50:53], a[92:95], v[94:97], v[50:53]\n\t" \
        "ds_read_b64_tr_b16 a[12:13], v7 offset:12384\n\t" \
        "v_cvt_pk_bf16_f32 v83, v112, v113\n\t" \
        "v_cvt_pk_bf16_f32 v84, v114, v115\n\t" \
        "v_mfma_f32_16x16x32_bf16 v[74:77], a[92:95], v[98:101], v[74:77]\n\t" \
        "buffer_load_dwordx4 v5, s[24:27], s57 offen offset:2048 lds\n\t" \
        "v_mfma_f32_16x16x32_bf16 v[102:105], a[128:131], v[154:157], 0\n\t" \
        "ds_read_b64_tr_b16 a[14:15], v7 offset:15456\n\t" \
        "ds_read_b64_tr_b16 a[16:17], v6 offset:12416\n\t" \
        "v_cvt_pk_bf16_f32 v85, v116, v117\n\t" \
        "s_waitcnt lgkmcnt(12)\n\t" \
        "v_mfma_f32_16x16x32_bf16 v[106:109], a[132:135], v[154:157], 0\n\t" \
        "ds_read_b64_tr_b16 a[18:19], v6 offset:15488\n\t" \
        "v_cvt_pk_bf16_f32 v86, v118, v119\n\t" \
        "v_mfma_f32_16x16x32_bf16 v[110:113], a[136:139], v[158:161], 0\n\t" \
        "ds_read_b64_tr_b16 a[20:21], v7 offset:12448\n\t" \
        "ds_read_b64_tr_b16 a[22:23], v7 offset:15520\n\t" \
        "s_waitcnt lgkmcnt(12)\n\t" \
        "v_mfma_f32_16x16x32_bf16 v[114:117], a[140:143], v[158:161], 0\n\t" \
        "ds_read_b64_tr_b16 a[96:97], v142 offset:0\n\t" \
        "v_cvt_pk_bf16_f32 v87, v120, v121\n\t" \
        "s_waitcnt lgkmcnt(12)\n\t" \
        "v_mfma_f32_16x16x32_bf16 v[30:33], a[0:3], v[78:81], v[30:33]\n\t" \
        "ds_read_b64_tr_b16 a[98:99], v143 offset:0\n\t" \
        "s_waitcnt lgkmcnt(11)\n\t" \
        "v_and_b32 v162, v152, v25\n\t" \
        "v_mfma_f32_16x16x32_bf16 v[54:57], a[0:3], v[82:85], v[54:57]\n\t" \
        "ds_read_b64_tr_b16 a[100:101], v146 offset:0\n\t" \
        "ds_read_b64_tr_b16 a[102:103], v147 offset:0\n\t" \
        "s_waitcnt lgkmcnt(12)\n\t" \
        "v_mfma_f32_16x16x32_bf16 v[34:37], a[4:7], v[78:81], v[34:37]\n\t" \
        "ds_read_b64_tr_b16 a[104:105], v144 offset:0\n\t" \
        "v_and_b32 v163, v152, v26\n\t" \
        "v_mfma_f32_16x16x32_bf16 v[58:61], a[4:7], v[82:85], v[58:61]\n\t" \
        "ds_read_b64_tr_b16 a[106:107], v145 offset:0\n\t" \
        "ds_read_b64_tr_b16 a[108:109], v148 offset:0\n\t" \
        "s_waitcnt lgkmcnt(13)\n\t" \
        "v_mfma_f32_16x16x32_bf16 v[38:41], a[8:11], v[78:81], v[38:41]\n\t" \
        "ds_read_b64_tr_b16 a[110:111], v149 offset:0\n\t" \
        "v_and_b32 v164, v152, v27\n\t" \
        "v_mfma_f32_16x16x32_bf16 v[62:65], a[8:11], v[82:85], v[62:65]\n\t" \
        "ds_read_b64_tr_b16 a[24:25], v6 offset:18432\n\t" \
        "ds_read_b64_tr_b16 a[26:27], v6 offset:21504\n\t" \
        "s_waitcnt lgkmcnt(14)\n\t" \
        "v_mfma_f32_16x16x32_bf16 v[42:45], a[12:15], v[78:81], v[42:45]\n\t" \
        "ds_read_b64_tr_b16 a[28:29], v7 offset:18464\n\t" \
        "v_and_b32 v165, v152, v28\n\t" \
        "v_mfma_f32_16x16x32_bf16 v[66:69], a[12:15], v[82:85], v[66:69]\n\t" \
        "ds_read_b64_tr_b16 a[30:31], v7 offset:21536\n\t" \
        "v_and_b32 v166, v153, v25\n\t" \
        "s_waitcnt lgkmcnt(14)\n\t" \
        "v_mfma_f32_16x16x32_bf16 v[46:49], a[16:19], v[78:81], v[46:49]\n\t" \
        "ds_read_b64_tr_b16 a[32:33], v6 offset:18496\n\t" \
        "ds_read_b64_tr_b16 a[34:35], v6 offset:21568\n\t" \
        "v_cvt_pk_bf16_f32 v88, v122, v123\n\t" \
        "v_and_b32 v167, v153, v26\n\t" \
        "v_mfma_f32_16x16x32_bf16 v[70:73], a[16:19], v[82:85], v[70:73]\n\t" \
        "ds_read_b64_tr_b16 a[36:37], v7 offset:18528\n\t" \
        "v_cvt_pk_bf16_f32 v89, v124, v125\n\t" \
        "v_cvt_pk_bf16_f32 v90, v126, v127\n\t" \
        "s_waitcnt lgkmcnt(14)\n\t" \
        "v_mfma_f32_16x16x32_bf16 v[50:53], a[20:23], v[78:81], v[50:53]\n\t" \
        "ds_read_b64_tr_b16 a[38:39], v7 offset:21600\n\t" \
        "ds_read_b64_tr_b16 a[40:41], v6 offset:18560\n\t" \
        "v_and_b32 v168, v153, v27\n\t" \
        "v_mfma_f32_16x16x32_bf16 v[74:77], a[20:23], v[82:85], v[74:77]\n\t" \
        "ds_read_b64_tr_b16 a[42:43], v6 offset:21632\n\t" \
        "v_cvt_pk_bf16_f32 v91, v128, v129\n\t" \
        "v_cvt_pk_bf16_f32 v92, v130, v131\n\t" \
        "s_waitcnt lgkmcnt(14)\n\t" \
        "v_mfma_f32_16x16x32_bf16 v[118:121], a[96:99], v[162:165], 0\n\t" \
        "s_waitcnt vmcnt(6)\n\t" \
        "s_barrier\n\t" \
        "ds_read_b64_tr_b16 a[44:45], v7 offset:18592\n\t" \
        "ds_read_b64_tr_b16 a[46:47], v7 offset:21664\n\t" \
        "v_and_b32 v169, v153, v28\n\t" \
        "v_cvt_pk_bf16_f32 v93, v132, v133\n\t" \
        "v_mfma_f32_16x16x32_bf16 v[122:125], a[100:103], v[162:165], 0\n\t" \
        "ds_read_b64_tr_b16 a[112:113], v142 offset:64\n\t" \
        "v_cvt_pk_bf16_f32 v94, v102, v103\n\t" \
        "s_waitcnt lgkmcnt(14)\n\t" \
        "v_mfma_f32_16x16x32_bf16 v[126:129], a[104:107], v[166:169], 0\n\t" \
        "ds_read_b64_tr_b16 a[114:115], v143 offset:64\n\t" \
        "v_cvt_pk_bf16_f32 v95, v104, v105\n\t" \
        "s_waitcnt lgkmcnt(14)\n\t" \
        "v_mfma_f32_16x16x32_bf16 v[130:133], a[108:111], v[166:169], 0\n\t" \
        "s_add_u32 m0, s39, 0\n\t" \
        "s_add_u32 s57, s40, 122880\n\t" \
        "buffer_load_dwordx4 v3, s[24:27], s57 offen offset:0 lds\n\t" \
        "s_waitcnt lgkmcnt(12)\n\t" \
        "v_mfma_f32_16x16x32_bf16 v[30:33], a[24:27], v[86:89], v[30:33]\n\t" \
        "ds_read_b64_tr_b16 a[116:117], v146 offset:64\n\t" \
        "ds_read_b64_tr_b16 a[118:119], v147 offset:64\n\t" \
        "v_mfma_f32_16x16x32_bf16 v[54:57], a[24:27], v[90:93], v[54:57]\n\t" \
        "ds_read_b64_tr_b16 a[120:121], v144 offset:64\n\t" \
        "v_cvt_pk_bf16_f32 v96, v106, v107\n\t" \
        "s_waitcnt lgkmcnt(13)\n\t" \
        "v_mfma_f32_16x16x32_bf16 v[34:37], a[28:31], v[86:89], v[34:37]\n\t" \
        "ds_read_b64_tr_b16 a[122:123], v145 offset:64\n\t" \
        "ds_read_b64_tr_b16 a[124:125], v148 offset:64\n\t" \
        "v_mfma_f32_16x16x32_bf16 v[58:61], a[28:31], v[90:93], v[58:61]\n\t" \
        "ds_read_b64_tr_b16 a[126:127], v149 offset:64\n\t" \
        "v_cvt_pk_bf16_f32 v97, v108, v109\n\t" \
        "s_waitcnt lgkmcnt(13)\n\t" \
        "v_mfma_f32_16x16x32_bf16 v[38:41], a[32:35], v[86:89], v[38:41]\n\t" \
        "ds_read_b64_tr_b16 a[48:49], v6 offset:24576\n\t" \
        "ds_read_b64_tr_b16 a[50:51], v6 offset:27648\n\t" \
        "v_mfma_f32_16x16x32_bf16 v[62:65], a[32:35], v[90:93], v[62:65]\n\t" \
        "buffer_load_dwordx4 v4, s[24:27], s57 offen offset:1024 lds\n\t" \
        "s_waitcnt lgkmcnt(12)\n\t" \
        "v_mfma_f32_16x16x32_bf16 v[42:45], a[36:39], v[86:89], v[42:45]\n\t" \
        "ds_read_b128 v[170:173], v9 offset:768\n\t" \
        "v_cvt_pk_bf16_f32 v98, v110, v111\n\t" \
        "v_mfma_f32_16x16x32_bf16 v[66:69], a[36:39], v[90:93], v[66:69]\n\t" \
        "ds_read_b64_tr_b16 a[52:53], v7 offset:24608\n\t" \
        "v_cvt_pk_bf16_f32 v99, v112, v113\n\t" \
        "v_mfma_f32_16x16x32_bf16 v[46:49], a[40:43], v[86:89], v[46:49]\n\t" \
        "ds_read_b64_tr_b16 a[54:55], v7 offset:27680\n\t" \
        "ds_read_b64_tr_b16 a[56:57], v6 offset:24640\n\t" \
        "v_mfma_f32_16x16x32_bf16 v[70:73], a[40:43], v[90:93], v[70:73]\n\t" \
        "ds_read_b64_tr_b16 a[58:59], v6 offset:27712\n\t" \
        "v_cvt_pk_bf16_f32 v100, v114, v115\n\t" \
        "s_waitcnt lgkmcnt(13)\n\t" \
        "v_mfma_f32_16x16x32_bf16 v[50:53], a[44:47], v[86:89], v[50:53]\n\t" \
        "ds_read_b64_tr_b16 a[60:61], v7 offset:24672\n\t" \
        "ds_read_b64_tr_b16 a[62:63], v7 offset:27744\n\t" \
        "v_mfma_f32_16x16x32_bf16 v[74:77], a[44:47], v[90:93], v[74:77]\n\t" \
        "buffer_load_dwordx4 v5, s[24:27], s57 offen offset:2048 lds\n\t" \
        "v_mfma_f32_16x16x32_bf16 v[102:105], a[112:115], v[162:165], 0\n\t" \
        "ds_read_b64_tr_b16 a[64:65], v6 offset:24704\n\t" \
        "v_cvt_pk_bf16_f32 v101, v116, v117\n\t" \
        "s_waitcnt lgkmcnt(11)\n\t" \
        "v_mfma_f32_16x16x32_bf16 v[106:109], a[116:119], v[162:165], 0\n\t" \
        "ds_read_b64_tr_b16 a[66:67], v6 offset:27776\n\t" \
        "ds_read_b64_tr_b16 a[68:69], v7 offset:24736\n\t" \
        "v_mfma_f32_16x16x32_bf16 v[110:113], a[120:123], v[166:169], 0\n\t" \
        "ds_read_b64_tr_b16 a[70:71], v7 offset:27808\n\t" \
        "v_cvt_pk_bf16_f32 v78, v118, v119\n\t" \
        "s_waitcnt lgkmcnt(11)\n\t" \
        "v_mfma_f32_16x16x32_bf16 v[114:117], a[124:127], v[166:169], 0\n\t" \
        "ds_read_b64_tr_b16 a[128:129], v142 offset:128\n\t" \
        "s_waitcnt lgkmcnt(11)\n\t" \
        "v_xor_b32 v134, v170, v11\n\t" \
        "v_mfma_f32_16x16x32_bf16 v[30:33], a[48:51], v[94:97], v[30:33]\n\t" \
        "ds_read_b64_tr_b16 a[130:131], v143 offset:128\n\t" \
        "ds_read_b64_tr_b16 a[132:133], v146 offset:128\n\t" \
        "v_mfma_f32_16x16x32_bf16 v[54:57], a[48:51], v[98:101], v[54:57]\n\t" \
        "ds_read_b64_tr_b16 a[134:135], v147 offset:128\n\t" \
        "v_xor_b32 v135, v171, v11\n\t" \
        "s_waitcnt lgkmcnt(12)\n\t" \
        "v_mfma_f32_16x16x32_bf16 v[34:37], a[52:55], v[94:97], v[34:37]\n\t" \
        "ds_read_b64_tr_b16 a[136:137], v144 offset:128\n\t" \
        "ds_read_b64_tr_b16 a[138:139], v145 offset:128\n\t" \
        "v_mfma_f32_16x16x32_bf16 v[58:61], a[52:55], v[98:101], v[58:61]\n\t" \
        "ds_read_b64_tr_b16 a[140:141], v148 offset:128\n\t" \
        "v_xor_b32 v136, v172, v11\n\t" \
        "s_waitcnt lgkmcnt(13)\n\t" \
        "v_mfma_f32_16x16x32_bf16 v[38:41], a[56:59], v[94:97], v[38:41]\n\t" \
        "ds_read_b64_tr_b16 a[142:143], v149 offset:128\n\t" \
        "ds_read_b64_tr_b16 a[72:73], v6 offset:30720\n\t" \
        "v_mfma_f32_16x16x32_bf16 v[62:65], a[56:59], v[98:101], v[62:65]\n\t" \
        "ds_read_b64_tr_b16 a[74:75], v6 offset:33792\n\t" \
        "v_xor_b32 v138, v170, v12\n\t" \
        "s_waitcnt lgkmcnt(14)\n\t" \
        "v_mfma_f32_16x16x32_bf16 v[42:45], a[60:63], v[94:97], v[42:45]\n\t" \
        "ds_read_b64_tr_b16 a[76:77], v7 offset:30752\n\t" \
        "v_xor_b32 v137, v173, v11\n\t" \
        "v_mfma_f32_16x16x32_bf16 v[66:69], a[60:63], v[98:101], v[66:69]\n\t" \
        "ds_read_b64 v[150:151], v10 offset:768\n\t" \
        "ds_read_b64_tr_b16 a[78:79], v7 offset:33824\n\t" \
        "v_xor_b32 v139, v171, v12\n\t" \
        "v_xor_b32 v140, v172, v12\n\t" \
        "s_waitcnt lgkmcnt(14)\n\t" \
        "v_mfma_f32_16x16x32_bf16 v[46:49], a[64:67], v[94:97], v[46:49]\n\t" \
        "ds_read_b64_tr_b16 a[80:81], v6 offset:30784\n\t" \
        "v_xor_b32 v141, v173, v12\n\t" \
        "v_cvt_pk_bf16_f32 v79, v120, v121\n\t" \
        "v_mfma_f32_16x16x32_bf16 v[70:73], a[64:67], v[98:101], v[70:73]\n\t" \
        "ds_read_b64_tr_b16 a[82:83], v6 offset:33856\n\t" \
        "ds_read_b64_tr_b16 a[84:85], v7 offset:30816\n\t" \
        "v_cvt_pk_bf16_f32 v80, v122, v123\n\t" \
        "s_waitcnt lgkmcnt(15)\n\t" \
        "v_mfma_f32_16x16x32_bf16 v[50:53], a[68:71], v[94:97], v[50:53]\n\t" \
        "ds_read_b64_tr_b16 a[86:87], v7 offset:33888\n\t" \
        "v_cvt_pk_bf16_f32 v81, v124, v125\n\t" \
        "v_cvt_pk_bf16_f32 v82, v126, v127\n\t" \
        "v_mfma_f32_16x16x32_bf16 v[74:77], a[68:71], v[98:101], v[74:77]\n\t" \
        "ds_read_b64_tr_b16 a[88:89], v6 offset:30848\n\t" \
        "ds_read_b64_tr_b16 a[90:91], v6 offset:33920\n\t" \
        "v_cvt_pk_bf16_f32 v83, v128, v129\n\t" \
        "v_cvt_pk_bf16_f32 v84, v130, v131\n\t" \
        "s_waitcnt lgkmcnt(15)\n\t" \
        "v_mfma_f32_16x16x32_bf16 v[118:121], a[128:131], v[162:165], 0\n\t" \
        "s_waitcnt vmcnt(6)\n\t" \
        "s_barrier\n\t" \
        "ds_read_b64_tr_b16 a[92:93], v7 offset:30880\n\t" \
        "v_cvt_pk_bf16_f32 v85, v132, v133\n\t" \
        "v_mfma_f32_16x16x32_bf16 v[122:125], a[132:135], v[162:165], 0\n\t" \
        "ds_read_b64_tr_b16 a[94:95], v7 offset:33952\n\t" \
        "v_cvt_pk_bf16_f32 v86, v102, v103\n\t" \
        "s_waitcnt lgkmcnt(14)\n\t" \
        "v_mfma_f32_16x16x32_bf16 v[126:129], a[136:139], v[166:169], 0\n\t" \
        "ds_read_b64_tr_b16 a[96:97], v134 offset:0\n\t" \
        "ds_read_b64_tr_b16 a[98:99], v135 offset:0\n\t" \
        "s_waitcnt lgkmcnt(14)\n\t" \
        "v_mfma_f32_16x16x32_bf16 v[130:133], a[140:143], v[166:169], 0\n\t" \
        "s_add_u32 m0, s39, 12288\n\t" \
        "s_add_u32 s57, s40, 135168\n\t" \
        "buffer_load_dwordx4 v3, s[24:27], s57 offen offset:0 lds\n\t" \
        "s_waitcnt lgkmcnt(13)\n\t" \
        "v_mfma_f32_16x16x32_bf16 v[30:33], a[72:75], v[78:81], v[30:33]\n\t" \
        "ds_read_b64_tr_b16 a[100:101], v138 offset:0\n\t" \
        "s_waitcnt lgkmcnt(12)\n\t" \
        "v_and_b32 v154, v150, v25\n\t" \
        "v_mfma_f32_16x16x32_bf16 v[54:57], a[72:75], v[82:85], v[54:57]\n\t" \
        "ds_read_b64_tr_b16 a[102:103], v139 offset:0\n\t" \
        "ds_read_b64_tr_b16 a[104:105], v136 offset:0\n\t" \
        "s_waitcnt lgkmcnt(13)\n\t" \
        "v_mfma_f32_16x16x32_bf16 v[34:37], a[76:79], v[78:81], v[34:37]\n\t" \
        "ds_read_b64_tr_b16 a[106:107], v137 offset:0\n\t" \
        "v_and_b32 v155, v150, v26\n\t" \
        "v_mfma_f32_16x16x32_bf16 v[58:61], a[76:79], v[82:85], v[58:61]\n\t" \
        "ds_read_b64_tr_b16 a[108:109], v140 offset:0\n\t" \
        "ds_read_b64_tr_b16 a[110:111], v141 offset:0\n\t" \
        "v_and_b32 v156, v150, v27\n\t" \
        "s_waitcnt lgkmcnt(13)\n\t" \
        "v_mfma_f32_16x16x32_bf16 v[38:41], a[80:83], v[78:81], v[38:41]\n\t" \
        "ds_read_b64_tr_b16 a[0:1], v6 offset:36864\n\t" \
        "v_and_b32 v157, v150, v28\n\t" \
        "v_and_b32 v158, v151, v25\n\t" \
        "v_mfma_f32_16x16x32_bf16 v[62:65], a[80:83], v[82:85], v[62:65]\n\t" \
        "buffer_load_dwordx4 v4, s[24:27], s57 offen offset:1024 lds\n\t" \
        "s_waitcnt lgkmcnt(11)\n\t" \
        "v_mfma_f32_16x16x32_bf16 v[42:45], a[84:87], v[78:81], v[42:45]\n\t" \
        "ds_read_b64_tr_b16 a[2:3], v6 offset:39936\n\t" \
        "ds_read_b64_tr_b16 a[4:5], v7 offset:36896\n\t" \
        "v_cvt_pk_bf16_f32 v87, v104, v105\n\t" \
        "v_mfma_f32_16x16x32_bf16 v[66:69], a[84:87], v[82:85], v[66:69]\n\t" \
        "ds_read_b64_tr_b16 a[6:7], v7 offset:39968\n\t" \
        "v_cvt_pk_bf16_f32 v88, v106, v107\n\t" \
        "v_and_b32 v159, v151, v26\n\t" \
        "v_mfma_f32_16x16x32_bf16 v[46:49], a[88:91], v[78:81], v[46:49]\n\t" \
        "ds_read_b64_tr_b16 a[8:9], v6 offset:36928\n\t" \
        "v_cvt_pk_bf16_f32 v89, v108, v109\n\t" \
        "v_cvt_pk_bf16_f32 v90, v110, v111\n\t" \
        "v_mfma_f32_16x16x32_bf16 v[70:73], a[88:91], v[82:85], v[70:73]\n\t" \
        "ds_read_b64_tr_b16 a[10:11], v6 offset:40000\n\t" \
        "ds_read_b64_tr_b16 a[12:13], v7 offset:36960\n\t" \
        "v_and_b32 v160, v151, v27\n\t" \
        "s_waitcnt lgkmcnt(13)\n\t" \
        "v_mfma_f32_16x16x32_bf16 v[50:53], a[92:95], v[78:81], v[50:53]\n\t" \
        "ds_read_b64_tr_b16 a[14:15], v7 offset:40032\n\t" \
        "v_cvt_pk_bf16_f32 v91, v112, v113\n\t" \
        "v_cvt_pk_bf16_f32 v92, v114, v115\n\t" \
        "v_mfma_f32_16x16x32_bf16 v[74:77], a[92:95], v[82:85], v[74:77]\n\t" \
        "buffer_load_dwordx4 v5, s[24:27], s57 offen offset:2048 lds\n\t" \
        "v_mfma_f32_16x16x32_bf16 v[102:105], a[96:99], v[154:157], 0\n\t" \
        "ds_read_b64_tr_b16 a[16:17], v6 offset:36992\n\t" \
        "ds_read_b64_tr_b16 a[18:19], v6 offset:40064\n\t" \
        "v_and_b32 v161, v151, v28\n\t" \
        "v_cvt_pk_bf16_f32 v93, v116, v117\n\t" \
        "s_waitcnt lgkmcnt(12)\n\t" \
        "v_mfma_f32_16x16x32_bf16 v[106:109], a[100:103], v[154:157], 0\n\t" \
        "ds_read_b64_tr_b16 a[20:21], v7 offset:37024\n\t" \
        "v_cvt_pk_bf16_f32 v94, v118, v119\n\t" \
        "v_mfma_f32_16x16x32_bf16 v[110:113], a[104:107], v[158:161], 0\n\t" \
        "ds_read_b64_tr_b16 a[22:23], v7 offset:40096\n\t" \
        "v_cvt_pk_bf16_f32 v95, v120, v121\n\t" \
        "s_waitcnt lgkmcnt(11)\n\t" \
        "v_mfma_f32_16x16x32_bf16 v[114:117], a[108:111], v[158:161], 0\n\t" \
        "ds_read_b64_tr_b16 a[112:113], v134 offset:64\n\t" \
        "ds_read_b64_tr_b16 a[114:115], v135 offset:64\n\t" \
        "s_waitcnt lgkmcnt(12)\n\t" \
        "v_mfma_f32_16x16x32_bf16 v[30:33], a[0:3], v[86:89], v[30:33]\n\t" \
        "ds_read_b64_tr_b16 a[116:117], v138 offset:64\n\t" \
        "v_cvt_pk_bf16_f32 v96, v122, v123\n\t" \
        "v_mfma_f32_16x16x32_bf16 v[54:57], a[0:3], v[90:93], v[54:57]\n\t" \
        "ds_read_b64_tr_b16 a[118:119], v139 offset:64\n\t" \
        "ds_read_b64_tr_b16 a[120:121], v136 offset:64\n\t" \
        "s_waitcnt lgkmcnt(13)\n\t" \
        "v_mfma_f32_16x16x32_bf16 v[34:37], a[4:7], v[86:89], v[34:37]\n\t" \
        "ds_read_b64_tr_b16 a[122:123], v137 offset:64\n\t" \
        "v_cvt_pk_bf16_f32 v97, v124, v125\n\t" \
        "v_mfma_f32_16x16x32_bf16 v[58:61], a[4:7], v[90:93], v[58:61]\n\t" \
        "ds_read_b64_tr_b16 a[124:125], v140 offset:64\n\t" \
        "v_cvt_pk_bf16_f32 v98, v126, v127\n\t" \
        "s_waitcnt lgkmcnt(12)\n\t" \
        "v_mfma_f32_16x16x32_bf16 v[38:41], a[8:11], v[86:89], v[38:41]\n\t" \
        "ds_read_b64_tr_b16 a[126:127], v141 offset:64\n\t" \
        "ds_read_b64_tr_b16 a[24:25], v6 offset:43008\n\t" \
        "v_mfma_f32_16x16x32_bf16 v[62:65], a[8:11], v[90:93], v[62:65]\n\t" \
        "ds_read_b64_tr_b16 a[26:27], v6 offset:46080\n\t" \
        "v_cvt_pk_bf16_f32 v99, v128, v129\n\t" \
        "s_waitcnt lgkmcnt(14)\n\t" \
        "v_mfma_f32_16x16x32_bf16 v[42:45], a[12:15], v[86:89], v[42:45]\n\t" \
        "ds_read_b128 v[174:177], v9 offset:896\n\t" \
        "ds_read_b64_tr_b16 a[28:29], v7 offset:43040\n\t" \
        "v_mfma_f32_16x16x32_bf16 v[66:69], a[12:15], v[90:93], v[66:69]\n\t" \
        "ds_read_b64_tr_b16 a[30:31], v7 offset:46112\n\t" \
        "v_cvt_pk_bf16_f32 v100, v130, v131\n\t" \
        "s_waitcnt lgkmcnt(14)\n\t" \
        "v_mfma_f32_16x16x32_bf16 v[46:49], a[16:19], v[86:89], v[46:49]\n\t" \
        "ds_read_b64_tr_b16 a[32:33], v6 offset:43072\n\t" \
        "ds_read_b64_tr_b16 a[34:35], v6 offset:46144\n\t" \
        "v_mfma_f32_16x16x32_bf16 v[70:73], a[16:19], v[90:93], v[70:73]\n\t" \
        "ds_read_b64_tr_b16 a[36:37], v7 offset:43104\n\t" \
        "v_cvt_pk_bf16_f32 v101, v132, v133\n\t" \
        "s_waitcnt lgkmcnt(14)\n\t" \
        "v_mfma_f32_16x16x32_bf16 v[50:53], a[20:23], v[86:89], v[50:53]\n\t" \
        "ds_read_b64_tr_b16 a[38:39], v7 offset:46176\n\t" \
        "v_cvt_pk_bf16_f32 v78, v102, v103\n\t" \
        "v_mfma_f32_16x16x32_bf16 v[74:77], a[20:23], v[90:93], v[74:77]\n\t" \
        "ds_read_b64_tr_b16 a[40:41], v6 offset:43136\n\t" \
        "ds_read_b64_tr_b16 a[42:43], v6 offset:46208\n\t" \
        "v_mfma_f32_16x16x32_bf16 v[118:121], a[112:115], v[154:157], 0\n\t" \
        "s_waitcnt vmcnt(6)\n\t" \
        "s_barrier\n\t" \
        "ds_read_b64_tr_b16 a[44:45], v7 offset:43168\n\t" \
        "v_cvt_pk_bf16_f32 v79, v104, v105\n\t" \
        "s_waitcnt lgkmcnt(14)\n\t" \
        "v_mfma_f32_16x16x32_bf16 v[122:125], a[116:119], v[154:157], 0\n\t" \
        "ds_read_b64_tr_b16 a[46:47], v7 offset:46240\n\t" \
        "ds_read_b64_tr_b16 a[128:129], v134 offset:128\n\t" \
        "v_mfma_f32_16x16x32_bf16 v[126:129], a[120:123], v[158:161], 0\n\t" \
        "ds_read_b64_tr_b16 a[130:131], v135 offset:128\n\t" \
        "v_cvt_pk_bf16_f32 v80, v106, v107\n\t" \
        "s_waitcnt lgkmcnt(14)\n\t" \
        "v_mfma_f32_16x16x32_bf16 v[130:133], a[124:127], v[158:161], 0\n\t" \
        "s_add_u32 m0, s39, 24576\n\t" \
        "s_add_u32 s57, s40, 147456\n\t" \
        "buffer_load_dwordx4 v3, s[24:27], s57 offen offset:0 lds\n\t" \
        "s_waitcnt lgkmcnt(13)\n\t" \
        "v_mfma_f32_16x16x32_bf16 v[30:33], a[24:27], v[94:97], v[30:33]\n\t" \
        "ds_read_b64_tr_b16 a[132:133], v138 offset:128\n\t" \
        "s_waitcnt lgkmcnt(12)\n\t" \
        "v_xor_b32 v142, v174, v11\n\t" \
        "v_mfma_f32_16x16x32_bf16 v[54:57], a[24:27], v[98:101], v[54:57]\n\t" \
        "ds_read_b64_tr_b16 a[134:135], v139 offset:128\n\t" \
        "ds_read_b64_tr_b16 a[136:137], v136 offset:128\n\t" \
        "s_waitcnt lgkmcnt(13)\n\t" \
        "v_mfma_f32_16x16x32_bf16 v[34:37], a[28:31], v[94:97], v[34:37]\n\t" \
        "ds_read_b64_tr_b16 a[138:139], v137 offset:128\n\t" \
        "v_xor_b32 v143, v175, v11\n\t" \
        "v_mfma_f32_16x16x32_bf16 v[58:61], a[28:31], v[98:101], v[58:61]\n\t" \
        "ds_read_b64_tr_b16 a[140:141], v140 offset:128\n\t" \
        "ds_read_b64_tr_b16 a[142:143], v141 offset:128\n\t" \
        "s_waitcnt lgkmcnt(13)\n\t" \
        "v_mfma_f32_16x16x32_bf16 v[38:41], a[32:35], v[94:97], v[38:41]\n\t" \
        "ds_read_b64_tr_b16 a[48:49], v6 offset:49152\n\t" \
        "v_xor_b32 v144, v176, v11\n\t" \
        "v_mfma_f32_16x16x32_bf16 v[62:65], a[32:35], v[98:101], v[62:65]\n\t" \
        "buffer_load_dwordx4 v4, s[24:27], s57 offen offset:1024 lds\n\t" \
        "s_waitcnt lgkmcnt(11)\n\t" \
        "v_mfma_f32_16x16x32_bf16 v[42:45], a[36:39], v[94:97], v[42:45]\n\t" \
        "ds_read_b64_tr_b16 a[50:51], v6 offset:52224\n\t" \
        "ds_read_b64_tr_b16 a[52:53], v7 offset:49184\n\t" \
        "v_xor_b32 v146, v174, v12\n\t" \
        "v_xor_b32 v145, v177, v11\n\t" \
        "v_mfma_f32_16x16x32_bf16 v[66:69], a[36:39], v[98:101], v[66:69]\n\t" \
        "ds_read_b64 v[152:153], v10 offset:896\n\t" \
        "v_xor_b32 v147, v175, v12\n\t" \
        "v_xor_b32 v148, v176, v12\n\t" \
        "v_mfma_f32_16x16x32_bf16 v[46:49], a[40:43], v[94:97], v[46:49]\n\t" \
        "ds_read_b64_tr_b16 a[54:55], v7 offset:52256\n\t" \
        "v_xor_b32 v149, v177, v12\n\t" \
        "v_mfma_f32_16x16x32_bf16 v[70:73], a[40:43], v[98:101], v[70:73]\n\t" \
        "ds_read_b64_tr_b16 a[56:57], v6 offset:49216\n\t" \
        "ds_read_b64_tr_b16 a[58:59], v6 offset:52288\n\t" \
        "v_cvt_pk_bf16_f32 v81, v108, v109\n\t" \
        "v_cvt_pk_bf16_f32 v82, v110, v111\n\t" \
        "s_waitcnt lgkmcnt(13)\n\t" \
        "v_mfma_f32_16x16x32_bf16 v[50:53], a[44:47], v[94:97], v[50:53]\n\t" \
        "ds_read_b64_tr_b16 a[60:61], v7 offset:49248\n\t" \
        "v_cvt_pk_bf16_f32 v83, v112, v113\n\t" \
        "v_cvt_pk_bf16_f32 v84, v114, v115\n\t" \
        "v_mfma_f32_16x16x32_bf16 v[74:77], a[44:47], v[98:101], v[74:77]\n\t" \
        "buffer_load_dwordx4 v5, s[24:27], s57 offen offset:2048 lds\n\t" \
        "v_mfma_f32_16x16x32_bf16 v[102:105], a[128:131], v[154:157], 0\n\t" \
        "ds_read_b64_tr_b16 a[62:63], v7 offset:52320\n\t" \
        "ds_read_b64_tr_b16 a[64:65], v6 offset:49280\n\t" \
        "v_cvt_pk_bf16_f32 v85, v116, v117\n\t" \
        "s_waitcnt lgkmcnt(12)\n\t" \
        "v_mfma_f32_16x16x32_bf16 v[106:109], a[132:135], v[154:157], 0\n\t" \
        "ds_read_b64_tr_b16 a[66:67], v6 offset:52352\n\t" \
        "v_cvt_pk_bf16_f32 v86, v118, v119\n\t" \
        "v_mfma_f32_16x16x32_bf16 v[110:113], a[136:139], v[158:161], 0\n\t" \
        "ds_read_b64_tr_b16 a[68:69], v7 offset:49312\n\t" \
        "ds_read_b64_tr_b16 a[70:71], v7 offset:52384\n\t" \
        "s_waitcnt lgkmcnt(12)\n\t" \
        "v_mfma_f32_16x16x32_bf16 v[114:117], a[140:143], v[158:161], 0\n\t" \
        "ds_read_b64_tr_b16 a[96:97], v142 offset:0\n\t" \
        "v_cvt_pk_bf16_f32 v87, v120, v121\n\t" \
        "s_waitcnt lgkmcnt(12)\n\t" \
        "v_mfma_f32_16x16x32_bf16 v[30:33], a[48:51], v[78:81], v[30:33]\n\t" \
        "ds_read_b64_tr_b16 a[98:99], v143 offset:0\n\t" \
        "s_waitcnt lgkmcnt(11)\n\t" \
        "v_and_b32 v162, v152, v25\n\t" \
        "v_mfma_f32_16x16x32_bf16 v[54:57], a[48:51], v[82:85], v[54:57]\n\t" \
        "ds_read_b64_tr_b16 a[100:101], v146 offset:0\n\t" \
        "ds_read_b64_tr_b16 a[102:103], v147 offset:0\n\t" \
        "s_waitcnt lgkmcnt(12)\n\t" \
        "v_mfma_f32_16x16x32_bf16 v[34:37], a[52:55], v[78:81], v[34:37]\n\t" \
        "ds_read_b64_tr_b16 a[104:105], v144 offset:0\n\t" \
        "v_and_b32 v163, v152, v26\n\t" \
        "v_mfma_f32_16x16x32_bf16 v[58:61], a[52:55], v[82:85], v[58:61]\n\t" \
        "ds_read_b64_tr_b16 a[106:107], v145 offset:0\n\t" \
        "ds_read_b64_tr_b16 a[108:109], v148 offset:0\n\t" \
        "s_waitcnt lgkmcnt(13)\n\t" \
        "v_mfma_f32_16x16x32_bf16 v[38:41], a[56:59], v[78:81], v[38:41]\n\t" \
        "ds_read_b64_tr_b16 a[110:111], v149 offset:0\n\t" \
        "v_and_b32 v164, v152, v27\n\t" \
        "v_mfma_f32_16x16x32_bf16 v[62:65], a[56:59], v[82:85], v[62:65]\n\t" \
        "ds_read_b64_tr_b16 a[72:73], v6 offset:55296\n\t" \
        "v_and_b32 v165, v152, v28\n\t" \
        "s_waitcnt lgkmcnt(13)\n\t" \
        "v_mfma_f32_16x16x32_bf16 v[42:45], a[60:63], v[78:81], v[42:45]\n\t" \
        "ds_read_b64_tr_b16 a[74:75], v6 offset:58368\n\t" \
        "ds_read_b64_tr_b16 a[76:77], v7 offset:55328\n\t" \
        "v_mfma_f32_16x16x32_bf16 v[66:69], a[60:63], v[82:85], v[66:69]\n\t" \
        "ds_read_b64_tr_b16 a[78:79], v7 offset:58400\n\t" \
        "v_and_b32 v166, v153, v25\n\t" \
        "s_waitcnt lgkmcnt(14)\n\t" \
        "v_mfma_f32_16x16x32_bf16 v[46:49], a[64:67], v[78:81], v[46:49]\n\t" \
        "ds_read_b64_tr_b16 a[80:81], v6 offset:55360\n\t" \
        "ds_read_b64_tr_b16 a[82:83], v6 offset:58432\n\t" \
        "v_cvt_pk_bf16_f32 v88, v122, v123\n\t" \
        "v_and_b32 v167, v153, v26\n\t" \
        "v_mfma_f32_16x16x32_bf16 v[70:73], a[64:67], v[82:85], v[70:73]\n\t" \
        "ds_read_b64_tr_b16 a[84:85], v7 offset:55392\n\t" \
        "v_cvt_pk_bf16_f32 v89, v124, v125\n\t" \
        "v_cvt_pk_bf16_f32 v90, v126, v127\n\t" \
        "s_waitcnt lgkmcnt(14)\n\t" \
        "v_mfma_f32_16x16x32_bf16 v[50:53], a[68:71], v[78:81], v[50:53]\n\t" \
        "ds_read_b64_tr_b16 a[86:87], v7 offset:58464\n\t" \
        "ds_read_b64_tr_b16 a[88:89], v6 offset:55424\n\t" \
        "v_and_b32 v168, v153, v27\n\t" \
        "v_mfma_f32_16x16x32_bf16 v[74:77], a[68:71], v[82:85], v[74:77]\n\t" \
        "ds_read_b64_tr_b16 a[90:91], v6 offset:58496\n\t" \
        "v_cvt_pk_bf16_f32 v91, v128, v129\n\t" \
        "v_cvt_pk_bf16_f32 v92, v130, v131\n\t" \
        "s_waitcnt lgkmcnt(14)\n\t" \
        "v_mfma_f32_16x16x32_bf16 v[118:121], a[96:99], v[162:165], 0\n\t" \
        "s_waitcnt vmcnt(6)\n\t" \
        "s_barrier\n\t" \
        "ds_read_b64_tr_b16 a[92:93], v7 offset:55456\n\t" \
        "v_and_b32 v169, v153, v28\n\t" \
        "v_cvt_pk_bf16_f32 v93, v132, v133\n\t" \
        "v_mfma_f32_16x16x32_bf16 v[122:125], a[100:103], v[162:165], 0\n\t" \
        "ds_read_b64_tr_b16 a[94:95], v7 offset:58528\n\t" \
        "ds_read_b64_tr_b16 a[112:113], v142 offset:64\n\t" \
        "s_waitcnt lgkmcnt(14)\n\t" \
        "v_mfma_f32_16x16x32_bf16 v[126:129], a[104:107], v[166:169], 0\n\t" \
        "ds_read_b64_tr_b16 a[114:115], v143 offset:64\n\t" \
        "v_cvt_pk_bf16_f32 v94, v102, v103\n\t" \
        "s_waitcnt lgkmcnt(14)\n\t" \
        "v_mfma_f32_16x16x32_bf16 v[130:133], a[108:111], v[166:169], 0\n\t" \
        "s_add_u32 m0, s39, 36864\n\t" \
        "s_add_u32 s57, s40, 159744\n\t" \
        "buffer_load_dwordx4 v3, s[24:27], s57 offen offset:0 lds\n\t" \
        "s_waitcnt lgkmcnt(12)\n\t" \
        "v_mfma_f32_16x16x32_bf16 v[30:33], a[72:75], v[86:89], v[30:33]\n\t" \
        "ds_read_b64_tr_b16 a[116:117], v146 offset:64\n\t" \
        "ds_read_b64_tr_b16 a[118:119], v147 offset:64\n\t" \
        "v_mfma_f32_16x16x32_bf16 v[54:57], a[72:75], v[90:93], v[54:57]\n\t" \
        "ds_read_b64_tr_b16 a[120:121], v144 offset:64\n\t" \
        "v_cvt_pk_bf16_f32 v95, v104, v105\n\t" \
        "s_waitcnt lgkmcnt(13)\n\t" \
        "v_mfma_f32_16x16x32_bf16 v[34:37], a[76:79], v[86:89], v[34:37]\n\t" \
        "ds_read_b64_tr_b16 a[122:123], v145 offset:64\n\t" \
        "v_cvt_pk_bf16_f32 v96, v106, v107\n\t" \
        "v_mfma_f32_16x16x32_bf16 v[58:61], a[76:79], v[90:93], v[58:61]\n\t" \
        "ds_read_b64_tr_b16 a[124:125], v148 offset:64\n\t" \
        "ds_read_b64_tr_b16 a[126:127], v149 offset:64\n\t" \
        "s_waitcnt lgkmcnt(13)\n\t" \
        "v_mfma_f32_16x16x32_bf16 v[38:41], a[80:83], v[86:89], v[38:41]\n\t" \
        "ds_read_b64_tr_b16 a[0:1], v6 offset:0\n\t" \
        "v_cvt_pk_bf16_f32 v97, v108, v109\n\t" \
        "v_mfma_f32_16x16x32_bf16 v[62:65], a[80:83], v[90:93], v[62:65]\n\t" \
        "buffer_load_dwordx4 v4, s[24:27], s57 offen offset:1024 lds\n\t" \
        "s_waitcnt lgkmcnt(11)\n\t" \
        "v_mfma_f32_16x16x32_bf16 v[42:45], a[84:87], v[86:89], v[42:45]\n\t" \
        "ds_read_b64_tr_b16 a[2:3], v6 offset:3072\n\t" \
        "ds_read_b128 v[170:173], v9 offset:1024\n\t" \
        "v_mfma_f32_16x16x32_bf16 v[66:69], a[84:87], v[90:93], v[66:69]\n\t" \
        "ds_read_b64_tr_b16 a[4:5], v7 offset:32\n\t" \
        "v_cvt_pk_bf16_f32 v98, v110, v111\n\t" \
        "v_mfma_f32_16x16x32_bf16 v[46:49], a[88:91], v[86:89], v[46:49]\n\t" \
        "ds_read_b64_tr_b16 a[6:7], v7 offset:3104\n\t" \
        "v_cvt_pk_bf16_f32 v99, v112, v113\n\t" \
        "v_mfma_f32_16x16x32_bf16 v[70:73], a[88:91], v[90:93], v[70:73]\n\t" \
        "ds_read_b64_tr_b16 a[8:9], v6 offset:64\n\t" \
        "ds_read_b64_tr_b16 a[10:11], v6 offset:3136\n\t" \
        "s_waitcnt lgkmcnt(13)\n\t" \
        "v_mfma_f32_16x16x32_bf16 v[50:53], a[92:95], v[86:89], v[50:53]\n\t" \
        "ds_read_b64_tr_b16 a[12:13], v7 offset:96\n\t" \
        "v_cvt_pk_bf16_f32 v100, v114, v115\n\t" \
        "v_mfma_f32_16x16x32_bf16 v[74:77], a[92:95], v[90:93], v[74:77]\n\t" \
        "buffer_load_dwordx4 v5, s[24:27], s57 offen offset:2048 lds\n\t" \
        "v_mfma_f32_16x16x32_bf16 v[102:105], a[112:115], v[162:165], 0\n\t" \
        "ds_read_b64_tr_b16 a[14:15], v7 offset:3168\n\t" \
        "ds_read_b64_tr_b16 a[16:17], v6 offset:128\n\t" \
        "v_cvt_pk_bf16_f32 v101, v116, v117\n\t" \
        "s_waitcnt lgkmcnt(12)\n\t" \
        "v_mfma_f32_16x16x32_bf16 v[106:109], a[116:119], v[162:165], 0\n\t" \
        "ds_read_b64_tr_b16 a[18:19], v6 offset:3200\n\t" \
        "v_cvt_pk_bf16_f32 v78, v118, v119\n\t" \
        "v_mfma_f32_16x16x32_bf16 v[110:113], a[120:123], v[166:169], 0\n\t" \
        "ds_read_b64_tr_b16 a[20:21], v7 offset:160\n\t" \
        "v_cvt_pk_bf16_f32 v79, v120, v121\n\t" \
        "s_waitcnt lgkmcnt(11)\n\t" \
        "v_mfma_f32_16x16x32_bf16 v[114:117], a[124:127], v[166:169], 0\n\t" \
        "ds_read_b64_tr_b16 a[22:23], v7 offset:3232\n\t" \
        "ds_read_b64_tr_b16 a[128:129], v142 offset:128\n\t" \
        "s_waitcnt lgkmcnt(12)\n\t" \
        "v_mfma_f32_16x16x32_bf16 v[30:33], a[0:3], v[94:97], v[30:33]\n\t" \
        "ds_read_b64_tr_b16 a[130:131], v143 offset:128\n\t" \
        "s_waitcnt lgkmcnt(12)\n\t" \
        "v_xor_b32 v134, v170, v11\n\t" \
        "v_mfma_f32_16x16x32_bf16 v[54:57], a[0:3], v[98:101], v[54:57]\n\t" \
        "ds_read_b64_tr_b16 a[132:133], v146 offset:128\n\t" \
        "ds_read_b64_tr_b16 a[134:135], v147 offset:128\n\t" \
        "s_waitcnt lgkmcnt(12)\n\t" \
        "v_mfma_f32_16x16x32_bf16 v[34:37], a[4:7], v[94:97], v[34:37]\n\t" \
        "ds_read_b64_tr_b16 a[136:137], v144 offset:128\n\t" \
        "v_xor_b32 v135, v171, v11\n\t" \
        "v_mfma_f32_16x16x32_bf16 v[58:61], a[4:7], v[98:101], v[58:61]\n\t" \
        "ds_read_b64_tr_b16 a[138:139], v145 offset:128\n\t" \
        "v_xor_b32 v136, v172, v11\n\t" \
        "s_waitcnt lgkmcnt(12)\n\t" \
        "v_mfma_f32_16x16x32_bf16 v[38:41], a[8:11], v[94:97], v[38:41]\n\t" \
        "ds_read_b64_tr_b16 a[140:141], v148 offset:128\n\t" \
        "ds_read_b64_tr_b16 a[142:143], v149 offset:128\n\t" \
        "v_mfma_f32_16x16x32_bf16 v[62:65], a[8:11], v[98:101], v[62:65]\n\t" \
        "ds_read_b64_tr_b16 a[24:25], v6 offset:6144\n\t" \
        "v_xor_b32 v138, v170, v12\n\t" \
        "s_waitcnt lgkmcnt(13)\n\t" \
        "v_mfma_f32_16x16x32_bf16 v[42:45], a[12:15], v[94:97], v[42:45]\n\t" \
        "ds_read_b64_tr_b16 a[26:27], v6 offset:9216\n\t" \
        "v_xor_b32 v137, v173, v11\n\t" \
        "v_mfma_f32_16x16x32_bf16 v[66:69], a[12:15], v[98:101], v[66:69]\n\t" \
        "ds_read_b64_tr_b16 a[28:29], v7 offset:6176\n\t" \
        "ds_read_b64 v[150:151], v10 offset:1024\n\t" \
        "v_xor_b32 v139, v171, v12\n\t" \
        "s_waitcnt lgkmcnt(14)\n\t" \
        "v_mfma_f32_16x16x32_bf16 v[46:49], a[16:19], v[94:97], v[46:49]\n\t" \
        "ds_read_b64_tr_b16 a[30:31], v7 offset:9248\n\t" \
        "v_xor_b32 v140, v172, v12\n\t" \
        "v_xor_b32 v141, v173, v12\n\t" \
        "v_mfma_f32_16x16x32_bf16 v[70:73], a[16:19], v[98:101], v[70:73]\n\t" \
        "ds_read_b64_tr_b16 a[32:33], v6 offset:6208\n\t" \
        "v_cvt_pk_bf16_f32 v80, v122, v123\n\t" \
        "s_waitcnt lgkmcnt(13)\n\t" \
        "v_mfma_f32_16x16x32_bf16 v[50:53], a[20:23], v[94:97], v[50:53]\n\t" \
        "ds_read_b64_tr_b16 a[34:35], v6 offset:9280\n\t" \
        "ds_read_b64_tr_b16 a[36:37], v7 offset:6240\n\t" \
        "v_cvt_pk_bf16_f32 v81, v124, v125\n\t" \
        "v_cvt_pk_bf16_f32 v82, v126, v127\n\t" \
        "v_mfma_f32_16x16x32_bf16 v[74:77], a[20:23], v[98:101], v[74:77]\n\t" \
        "ds_read_b64_tr_b16 a[38:39], v7 offset:9312\n\t" \
        "v_cvt_pk_bf16_f32 v83, v128, v129\n\t" \
        "v_cvt_pk_bf16_f32 v84, v130, v131\n\t" \
        "s_waitcnt lgkmcnt(13)\n\t" \
        "v_mfma_f32_16x16x32_bf16 v[118:121], a[128:131], v[162:165], 0\n\t" \
        "s_waitcnt vmcnt(6)\n\t" \
        "s_barrier\n\t" \
        "ds_read_b64_tr_b16 a[40:41], v6 offset:6272\n\t" \
        "ds_read_b64_tr_b16 a[42:43], v6 offset:9344\n\t" \
        "v_cvt_pk_bf16_f32 v85, v132, v133\n\t" \
        "v_mfma_f32_16x16x32_bf16 v[122:125], a[132:135], v[162:165], 0\n\t" \
        "ds_read_b64_tr_b16 a[44:45], v7 offset:6304\n\t" \
        "v_cvt_pk_bf16_f32 v86, v102, v103\n\t" \
        "s_waitcnt lgkmcnt(14)\n\t" \
        "v_mfma_f32_16x16x32_bf16 v[126:129], a[136:139], v[166:169], 0\n\t" \
        "ds_read_b64_tr_b16 a[46:47], v7 offset:9376\n\t" \
        "v_cvt_pk_bf16_f32 v87, v104, v105\n\t" \
        "s_waitcnt lgkmcnt(13)\n\t" \
        "v_mfma_f32_16x16x32_bf16 v[130:133], a[140:143], v[166:169], 0\n\t" \
        "ds_read_b64_tr_b16 a[96:97], v134 offset:0\n\t" \
        "ds_read_b64_tr_b16 a[98:99], v135 offset:0\n\t" \
        "s_waitcnt lgkmcnt(13)\n\t" \
        "v_mfma_f32_16x16x32_bf16 v[30:33], a[24:27], v[78:81], v[30:33]\n\t" \
        "ds_read_b64_tr_b16 a[100:101], v138 offset:0\n\t" \
        "s_waitcnt lgkmcnt(12)\n\t" \
        "v_and_b32 v154, v150, v25\n\t" \
        "v_mfma_f32_16x16x32_bf16 v[54:57], a[24:27], v[82:85], v[54:57]\n\t" \
        "ds_read_b64_tr_b16 a[102:103], v139 offset:0\n\t" \
        "ds_read_b64_tr_b16 a[104:105], v136 offset:0\n\t" \
        "s_waitcnt lgkmcnt(13)\n\t" \
        "v_mfma_f32_16x16x32_bf16 v[34:37], a[28:31], v[78:81], v[34:37]\n\t" \
        "ds_read_b64_tr_b16 a[106:107], v137 offset:0\n\t" \
        "v_and_b32 v155, v150, v26\n\t" \
        "v_mfma_f32_16x16x32_bf16 v[58:61], a[28:31], v[82:85], v[58:61]\n\t" \
        "ds_read_b64_tr_b16 a[108:109], v140 offset:0\n\t" \
        "v_and_b32 v156, v150, v27\n\t" \
        "s_waitcnt lgkmcnt(13)\n\t" \
        "v_mfma_f32_16x16x32_bf16 v[38:41], a[32:35], v[78:81], v[38:41]\n\t" \
        "ds_read_b64_tr_b16 a[110:111], v141 offset:0\n\t" \
        "ds_read_b64_tr_b16 a[48:49], v6 offset:12288\n\t" \
        "v_mfma_f32_16x16x32_bf16 v[62:65], a[32:35], v[82:85], v[62:65]\n\t" \
        "ds_read_b64_tr_b16 a[50:51], v6 offset:15360\n\t" \
        "v_and_b32 v157, v150, v28\n\t" \
        "s_waitcnt lgkmcnt(14)\n\t" \
        "v_mfma_f32_16x16x32_bf16 v[42:45], a[36:39], v[78:81], v[42:45]\n\t" \
        "ds_read_b64_tr_b16 a[52:53], v7 offset:12320\n\t" \
        "ds_read_b64_tr_b16 a[54:55], v7 offset:15392\n\t" \
        "v_mfma_f32_16x16x32_bf16 v[66:69], a[36:39], v[82:85], v[66:69]\n\t" \
        "ds_read_b64_tr_b16 a[56:57], v6 offset:12352\n\t" \
        "v_and_b32 v158, v151, v25\n\t" \
        "s_waitcnt lgkmcnt(14)\n\t" \
        "v_mfma_f32_16x16x32_bf16 v[46:49], a[40:43], v[78:81], v[46:49]\n\t" \
        "ds_read_b64_tr_b16 a[58:59], v6 offset:15424\n\t" \
        "ds_read_b64_tr_b16 a[60:61], v7 offset:12384\n\t" \
        "v_cvt_pk_bf16_f32 v88, v106, v107\n\t" \
        "v_and_b32 v159, v151, v26\n\t" \
        "v_mfma_f32_16x16x32_bf16 v[70:73], a[40:43], v[82:85], v[70:73]\n\t" \
        "ds_read_b64_tr_b16 a[62:63], v7 offset:15456\n\t" \
        "v_cvt_pk_bf16_f32 v89, v108, v109\n\t" \
        "v_cvt_pk_bf16_f32 v90, v110, v111\n\t" \
        "s_waitcnt lgkmcnt(14)\n\t" \
        "v_mfma_f32_16x16x32_bf16 v[50:53], a[44:47], v[78:81], v[50:53]\n\t" \
        "ds_read_b64_tr_b16 a[64:65], v6 offset:12416\n\t" \
        "ds_read_b64_tr_b16 a[66:67], v6 offset:15488\n\t" \
        "v_and_b32 v160, v151, v27\n\t" \
        "v_mfma_f32_16x16x32_bf16 v[74:77], a[44:47], v[82:85], v[74:77]\n\t" \
        "ds_read_b64_tr_b16 a[68:69], v7 offset:12448\n\t" \
        "v_cvt_pk_bf16_f32 v91, v112, v113\n\t" \
        "v_cvt_pk_bf16_f32 v92, v114, v115\n\t" \
        "v_mfma_f32_16x16x32_bf16 v[102:105], a[96:99], v[154:157], 0\n\t" \
        "ds_read_b64_tr_b16 a[70:71], v7 offset:15520\n\t" \
        "ds_read_b64_tr_b16 a[112:113], v134 offset:64\n\t" \
        "v_and_b32 v161, v151, v28\n\t" \
        "v_cvt_pk_bf16_f32 v93, v116, v117\n\t" \
        "s_waitcnt lgkmcnt(15)\n\t" \
        "v_mfma_f32_16x16x32_bf16 v[106:109], a[100:103], v[154:157], 0\n\t" \
        "ds_read_b64_tr_b16 a[114:115], v135 offset:64\n\t" \
        "v_cvt_pk_bf16_f32 v94, v118, v119\n\t" \
        "v_mfma_f32_16x16x32_bf16 v[110:113], a[104:107], v[158:161], 0\n\t" \
        "ds_read_b64_tr_b16 a[116:117], v138 offset:64\n\t" \
        "ds_read_b64_tr_b16 a[118:119], v139 offset:64\n\t" \
        "s_waitcnt lgkmcnt(15)\n\t" \
        "v_mfma_f32_16x16x32_bf16 v[114:117], a[108:111], v[158:161], 0\n\t" \
        "ds_read_b64_tr_b16 a[120:121], v136 offset:64\n\t" \
        "v_cvt_pk_bf16_f32 v95, v120, v121\n\t" \
        "s_waitcnt lgkmcnt(15)\n\t" \
        "v_mfma_f32_16x16x32_bf16 v[30:33], a[48:51], v[86:89], v[30:33]\n\t" \
        "ds_read_b64_tr_b16 a[122:123], v137 offset:64\n\t" \
        "ds_read_b64_tr_b16 a[124:125], v140 offset:64\n\t" \
        "v_mfma_f32_16x16x32_bf16 v[54:57], a[48:51], v[90:93], v[54:57]\n\t" \
        "ds_read_b64_tr_b16 a[126:127], v141 offset:64\n\t" \
        "v_cvt_pk_bf16_f32 v96, v122, v123\n\t" \
        "s_waitcnt lgkmcnt(15)\n\t" \
        "v_mfma_f32_16x16x32_bf16 v[34:37], a[52:55], v[86:89], v[34:37]\n\t" \
        "ds_read_b64_tr_b16 a[72:73], v6 offset:18432\n\t" \
        "ds_read_b64_tr_b16 a[74:75], v6 offset:21504\n\t" \
        "v_mfma_f32_16x16x32_bf16 v[58:61], a[52:55], v[90:93], v[58:61]\n\t" \
        "ds_read_b64_tr_b16 a[76:77], v7 offset:18464\n\t" \
        "v_cvt_pk_bf16_f32 v97, v124, v125\n\t" \
        "s_waitcnt lgkmcnt(15)\n\t" \
        "v_mfma_f32_16x16x32_bf16 v[38:41], a[56:59], v[86:89], v[38:41]\n\t" \
        "ds_read_b64_tr_b16 a[78:79], v7 offset:21536\n\t" \
        "ds_read_b64_tr_b16 a[80:81], v6 offset:18496\n\t" \
        "v_mfma_f32_16x16x32_bf16 v[62:65], a[56:59], v[90:93], v[62:65]\n\t" \
        "ds_read_b64_tr_b16 a[82:83], v6 offset:21568\n\t" \
        "v_cvt_pk_bf16_f32 v98, v126, v127\n\t" \
        "v_mfma_f32_16x16x32_bf16 v[42:45], a[60:63], v[86:89], v[42:45]\n\t" \
        "ds_read_b64_tr_b16 a[84:85], v7 offset:18528\n\t" \
        "ds_read_b64_tr_b16 a[86:87], v7 offset:21600\n\t" \
        "v_mfma_f32_16x16x32_bf16 v[66:69], a[60:63], v[90:93], v[66:69]\n\t" \
        "ds_read_b64_tr_b16 a[88:89], v6 offset:18560\n\t" \
        "v_cvt_pk_bf16_f32 v99, v128, v129\n\t" \
        "s_waitcnt lgkmcnt(15)\n\t" \
        "v_mfma_f32_16x16x32_bf16 v[46:49], a[64:67], v[86:89], v[46:49]\n\t" \
        "ds_read_b64_tr_b16 a[90:91], v6 offset:21632\n\t" \
        "ds_read_b64_tr_b16 a[92:93], v7 offset:18592\n\t" \
        "v_mfma_f32_16x16x32_bf16 v[70:73], a[64:67], v[90:93], v[70:73]\n\t" \
        "ds_read_b64_tr_b16 a[94:95], v7 offset:21664\n\t" \
        "v_cvt_pk_bf16_f32 v100, v130, v131\n\t" \
        "v_mfma_f32_16x16x32_bf16 v[50:53], a[68:71], v[86:89], v[50:53]\n\t" \
        "ds_read_b64_tr_b16 a[128:129], v134 offset:128\n\t" \
        "v_cvt_pk_bf16_f32 v101, v132, v133\n\t" \
        "v_mfma_f32_16x16x32_bf16 v[74:77], a[68:71], v[90:93], v[74:77]\n\t" \
        "ds_read_b64_tr_b16 a[130:131], v135 offset:128\n\t" \
        "ds_read_b64_tr_b16 a[132:133], v138 offset:128\n\t" \
        "v_mfma_f32_16x16x32_bf16 v[118:121], a[112:115], v[154:157], 0\n\t" \
        "s_waitcnt vmcnt(3)\n\t" \
        "s_barrier\n\t" \
        "ds_read_b64_tr_b16 a[134:135], v139 offset:128\n\t" \
        "v_cvt_pk_bf16_f32 v78, v102, v103\n\t" \
        "s_waitcnt lgkmcnt(14)\n\t" \
        "v_mfma_f32_16x16x32_bf16 v[122:125], a[116:119], v[154:157], 0\n\t" \
        "ds_read_b64_tr_b16 a[136:137], v136 offset:128\n\t" \
        "ds_read_b64_tr_b16 a[138:139], v137 offset:128\n\t" \
        "v_mfma_f32_16x16x32_bf16 v[126:129], a[120:123], v[158:161], 0\n\t" \
        "ds_read_b64_tr_b16 a[140:141], v140 offset:128\n\t" \
        "v_cvt_pk_bf16_f32 v79, v104, v105\n\t" \
        "v_mfma_f32_16x16x32_bf16 v[130:133], a[124:127], v[158:161], 0\n\t" \
        "ds_read_b64_tr_b16 a[142:143], v141 offset:128\n\t" \
        "ds_read_b64_tr_b16 a[0:1], v6 offset:24576\n\t" \
        "v_mfma_f32_16x16x32_bf16 v[30:33], a[72:75], v[94:97], v[30:33]\n\t" \
        "ds_read_b64_tr_b16 a[2:3], v6 offset:27648\n\t" \
        "v_cvt_pk_bf16_f32 v80, v106, v107\n\t" \
        "v_mfma_f32_16x16x32_bf16 v[54:57], a[72:75], v[98:101], v[54:57]\n\t" \
        "ds_read_b64_tr_b16 a[4:5], v7 offset:24608\n\t" \
        "ds_read_b64_tr_b16 a[6:7], v7 offset:27680\n\t" \
        "s_waitcnt lgkmcnt(15)\n\t" \
        "v_mfma_f32_16x16x32_bf16 v[34:37], a[76:79], v[94:97], v[34:37]\n\t" \
        "ds_read_b64_tr_b16 a[8:9], v6 offset:24640\n\t" \
        "v_cvt_pk_bf16_f32 v81, v108, v109\n\t" \
        "v_mfma_f32_16x16x32_bf16 v[58:61], a[76:79], v[98:101], v[58:61]\n\t" \
        "ds_read_b64_tr_b16 a[10:11], v6 offset:27712\n\t" \
        "ds_read_b64_tr_b16 a[12:13], v7 offset:24672\n\t" \
        "v_mfma_f32_16x16x32_bf16 v[38:41], a[80:83], v[94:97], v[38:41]\n\t" \
        "ds_read_b64_tr_b16 a[14:15], v7 offset:27744\n\t" \
        "v_cvt_pk_bf16_f32 v82, v110, v111\n\t" \
        "v_mfma_f32_16x16x32_bf16 v[62:65], a[80:83], v[98:101], v[62:65]\n\t" \
        "ds_read_b64_tr_b16 a[16:17], v6 offset:24704\n\t" \
        "ds_read_b64_tr_b16 a[18:19], v6 offset:27776\n\t" \
        "v_mfma_f32_16x16x32_bf16 v[42:45], a[84:87], v[94:97], v[42:45]\n\t" \
        "ds_read_b64_tr_b16 a[20:21], v7 offset:24736\n\t" \
        "v_cvt_pk_bf16_f32 v83, v112, v113\n\t" \
        "v_mfma_f32_16x16x32_bf16 v[66:69], a[84:87], v[98:101], v[66:69]\n\t" \
        "ds_read_b64_tr_b16 a[22:23], v7 offset:27808\n\t" \
        "ds_read_b64_tr_b16 a[24:25], v6 offset:30720\n\t" \
        "s_waitcnt lgkmcnt(15)\n\t" \
        "v_mfma_f32_16x16x32_bf16 v[46:49], a[88:91], v[94:97], v[46:49]\n\t" \
        "s_waitcnt vmcnt(0)\n\t" \
        "s_barrier\n\t" \
        "ds_read_b64_tr_b16 a[26:27], v6 offset:33792\n\t" \
        "v_cvt_pk_bf16_f32 v84, v114, v115\n\t" \
        "v_mfma_f32_16x16x32_bf16 v[70:73], a[88:91], v[98:101], v[70:73]\n\t" \
        "ds_read_b64_tr_b16 a[28:29], v7 offset:30752\n\t" \
        "ds_read_b64_tr_b16 a[30:31], v7 offset:33824\n\t" \
        "v_mfma_f32_16x16x32_bf16 v[50:53], a[92:95], v[94:97], v[50:53]\n\t" \
        "ds_read_b64_tr_b16 a[32:33], v6 offset:30784\n\t" \
        "v_cvt_pk_bf16_f32 v85, v116, v117\n\t" \
        "v_mfma_f32_16x16x32_bf16 v[74:77], a[92:95], v[98:101], v[74:77]\n\t" \
        "ds_read_b64_tr_b16 a[34:35], v6 offset:33856\n\t" \
        "ds_read_b64_tr_b16 a[36:37], v7 offset:30816\n\t" \
        "v_mfma_f32_16x16x32_bf16 v[102:105], a[128:131], v[154:157], 0\n\t" \
        "ds_read_b64_tr_b16 a[38:39], v7 offset:33888\n\t" \
        "v_cvt_pk_bf16_f32 v86, v118, v119\n\t" \
        "v_mfma_f32_16x16x32_bf16 v[106:109], a[132:135], v[154:157], 0\n\t" \
        "ds_read_b64_tr_b16 a[40:41], v6 offset:30848\n\t" \
        "ds_read_b64_tr_b16 a[42:43], v6 offset:33920\n\t" \
        "v_mfma_f32_16x16x32_bf16 v[110:113], a[136:139], v[158:161], 0\n\t" \
        "ds_read_b64_tr_b16 a[44:45], v7 offset:30880\n\t" \
        "v_cvt_pk_bf16_f32 v88, v122, v123\n\t" \
        "s_waitcnt lgkmcnt(15)\n\t" \
        "v_mfma_f32_16x16x32_bf16 v[114:117], a[140:143], v[158:161], 0\n\t" \
        "ds_read_b64_tr_b16 a[46:47], v7 offset:33952\n\t" \
        "ds_read_b64_tr_b16 a[48:49], v6 offset:36864\n\t" \
        "v_mfma_f32_16x16x32_bf16 v[30:33], a[0:3], v[78:81], v[30:33]\n\t" \
        "ds_read_b64_tr_b16 a[50:51], v6 offset:39936\n\t" \
        "v_cvt_pk_bf16_f32 v87, v120, v121\n\t" \
        "v_mfma_f32_16x16x32_bf16 v[34:37], a[4:7], v[78:81], v[34:37]\n\t" \
        "ds_read_b64_tr_b16 a[52:53], v7 offset:36896\n\t" \
        "ds_read_b64_tr_b16 a[54:55], v7 offset:39968\n\t" \
        "v_mfma_f32_16x16x32_bf16 v[38:41], a[8:11], v[78:81], v[38:41]\n\t" \
        "ds_read_b64_tr_b16 a[56:57], v6 offset:36928\n\t" \
        "v_cvt_pk_bf16_f32 v89, v124, v125\n\t" \
        "v_mfma_f32_16x16x32_bf16 v[42:45], a[12:15], v[78:81], v[42:45]\n\t" \
        "ds_read_b64_tr_b16 a[58:59], v6 offset:40000\n\t" \
        "v_cvt_pk_bf16_f32 v94, v102, v103\n\t" \
        "s_waitcnt lgkmcnt(14)\n\t" \
        "v_mfma_f32_16x16x32_bf16 v[46:49], a[16:19], v[78:81], v[46:49]\n\t" \
        "ds_read_b64_tr_b16 a[60:61], v7 offset:36960\n\t" \
        "ds_read_b64_tr_b16 a[62:63], v7 offset:40032\n\t" \
        "v_mfma_f32_16x16x32_bf16 v[50:53], a[20:23], v[78:81], v[50:53]\n\t" \
        "ds_read_b64_tr_b16 a[64:65], v6 offset:36992\n\t" \
        "ds_read_b64_tr_b16 a[66:67], v6 offset:40064\n\t" \
        "v_mfma_f32_16x16x32_bf16 v[30:33], a[24:27], v[86:89], v[30:33]\n\t" \
        "ds_read_b64_tr_b16 a[68:69], v7 offset:37024\n\t" \
        "v_cvt_pk_bf16_f32 v96, v106, v107\n\t" \
        "v_mfma_f32_16x16x32_bf16 v[34:37], a[28:31], v[86:89], v[34:37]\n\t" \
        "ds_read_b64_tr_b16 a[70:71], v7 offset:40096\n\t" \
        "v_cvt_pk_bf16_f32 v95, v104, v105\n\t" \
        "s_waitcnt lgkmcnt(14)\n\t" \
        "v_mfma_f32_16x16x32_bf16 v[38:41], a[32:35], v[86:89], v[38:41]\n\t" \
        "v_cvt_pk_bf16_f32 v97, v108, v109\n\t" \
        "v_cvt_pk_bf16_f32 v90, v126, v127\n\t" \
        "v_mfma_f32_16x16x32_bf16 v[42:45], a[36:39], v[86:89], v[42:45]\n\t" \
        "v_cvt_pk_bf16_f32 v92, v130, v131\n\t" \
        "v_cvt_pk_bf16_f32 v91, v128, v129\n\t" \
        "v_mfma_f32_16x16x32_bf16 v[46:49], a[40:43], v[86:89], v[46:49]\n\t" \
        "v_cvt_pk_bf16_f32 v93, v132, v133\n\t" \
        "v_cvt_pk_bf16_f32 v98, v110, v111\n\t" \
        "s_waitcnt lgkmcnt(10)\n\t" \
        "v_mfma_f32_16x16x32_bf16 v[50:53], a[44:47], v[86:89], v[50:53]\n\t" \
        "v_cvt_pk_bf16_f32 v100, v114, v115\n\t" \
        "v_cvt_pk_bf16_f32 v99, v112, v113\n\t" \
        "v_mfma_f32_16x16x32_bf16 v[30:33], a[48:51], v[94:97], v[30:33]\n\t" \
        "v_cvt_pk_bf16_f32 v101, v116, v117\n\t" \
        "s_waitcnt lgkmcnt(7)\n\t" \
        "v_mfma_f32_16x16x32_bf16 v[34:37], a[52:55], v[94:97], v[34:37]\n\t" \
        "s_waitcnt lgkmcnt(6)\n\t" \
        "v_mfma_f32_16x16x32_bf16 v[38:41], a[56:59], v[94:97], v[38:41]\n\t" \
        "s_waitcnt lgkmcnt(4)\n\t" \
        "v_mfma_f32_16x16x32_bf16 v[42:45], a[60:63], v[94:97], v[42:45]\n\t" \
        "s_waitcnt lgkmcnt(2)\n\t" \
        "v_mfma_f32_16x16x32_bf16 v[46:49], a[64:67], v[94:97], v[46:49]\n\t" \
        "s_waitcnt lgkmcnt(0)\n\t" \
        "v_mfma_f32_16x16x32_bf16 v[50:53], a[68:71], v[94:97], v[50:53]\n\t" \
        "v_mfma_f32_16x16x32_bf16 v[54:57], a[0:3], v[82:85], v[54:57]\n\t" \
        "v_mfma_f32_16x16x32_bf16 v[58:61], a[4:7], v[82:85], v[58:61]\n\t" \
        "v_mfma_f32_16x16x32_bf16 v[62:65], a[8:11], v[82:85], v[62:65]\n\t" \
        "v_mfma_f32_16x16x32_bf16 v[66:69], a[12:15], v[82:85], v[66:69]\n\t" \
        "v_mfma_f32_16x16x32_bf16 v[70:73], a[16:19], v[82:85], v[70:73]\n\t" \
        "v_mfma_f32_16x16x32_bf16 v[74:77], a[20:23], v[82:85], v[74:77]\n\t" \
        "v_mfma_f32_16x16x32_bf16 v[54:57], a[24:27], v[90:93], v[54:57]\n\t" \
        "v_mfma_f32_16x16x32_bf16 v[58:61], a[28:31], v[90:93], v[58:61]\n\t" \
        "v_mfma_f32_16x16x32_bf16 v[62:65], a[32:35], v[90:93], v[62:65]\n\t" \
        "v_mfma_f32_16x16x32_bf16 v[66:69], a[36:39], v[90:93], v[66:69]\n\t" \
        "v_mfma_f32_16x16x32_bf16 v[70:73], a[40:43], v[90:93], v[70:73]\n\t" \
        "v_mfma_f32_16x16x32_bf16 v[74:77], a[44:47], v[90:93], v[74:77]\n\t" \
        "v_mfma_f32_16x16x32_bf16 v[54:57], a[48:51], v[98:101], v[54:57]\n\t" \
        "v_mfma_f32_16x16x32_bf16 v[58:61], a[52:55], v[98:101], v[58:61]\n\t" \
        "v_mfma_f32_16x16x32_bf16 v[62:65], a[56:59], v[98:101], v[62:65]\n\t" \
        "v_mfma_f32_16x16x32_bf16 v[66:69], a[60:63], v[98:101], v[66:69]\n\t" \
        "v_mfma_f32_16x16x32_bf16 v[70:73], a[64:67], v[98:101], v[70:73]\n\t" \
        "v_mfma_f32_16x16x32_bf16 v[74:77], a[68:71], v[98:101], v[74:77]\n\t" \
        "s_nop 4\n\t" \
        "v_add_f32 v30, v30, v54\n\t" \
        "v_add_f32 v31, v31, v55\n\t" \
        "v_add_f32 v32, v32, v56\n\t" \
        "v_add_f32 v33, v33, v57\n\t" \
        "v_add_f32 v34, v34, v58\n\t" \
        "v_add_f32 v35, v35, v59\n\t" \
        "v_add_f32 v36, v36, v60\n\t" \
        "v_add_f32 v37, v37, v61\n\t" \
        "v_add_f32 v38, v38, v62\n\t" \
        "v_add_f32 v39, v39, v63\n\t" \
        "v_add_f32 v40, v40, v64\n\t" \
        "v_add_f32 v41, v41, v65\n\t" \
        "v_add_f32 v42, v42, v66\n\t" \
        "v_add_f32 v43, v43, v67\n\t" \
        "v_add_f32 v44, v44, v68\n\t" \
        "v_add_f32 v45, v45, v69\n\t" \
        "v_add_f32 v46, v46, v70\n\t" \
        "v_add_f32 v47, v47, v71\n\t" \
        "v_add_f32 v48, v48, v72\n\t" \
        "v_add_f32 v49, v49, v73\n\t" \
        "v_add_f32 v50, v50, v74\n\t" \
        "v_add_f32 v51, v51, v75\n\t" \
        "v_add_f32 v52, v52, v76\n\t" \
        "v_add_f32 v53, v53, v77\n\t" \
        "v_cvt_pk_bf16_f32 v30, v30, v31\n\t" \
        "v_cvt_pk_bf16_f32 v31, v32, v33\n\t" \
        "v_cvt_pk_bf16_f32 v32, v34, v35\n\t" \
        "v_cvt_pk_bf16_f32 v33, v36, v37\n\t" \
        "s_nop 0\n\t" \
        "v_permlane16_swap_b32 v30, v32\n\t" \
        "v_permlane16_swap_b32 v31, v33\n\t" \
        "buffer_store_dwordx4 v[30:33], v8, s[28:31], s59 offen offset:0 sc1\n\t" \
        "v_cvt_pk_bf16_f32 v38, v38, v39\n\t" \
        "v_cvt_pk_bf16_f32 v39, v40, v41\n\t" \
        "v_cvt_pk_bf16_f32 v40, v42, v43\n\t" \
        "v_cvt_pk_bf16_f32 v41, v44, v45\n\t" \
        "s_nop 0\n\t" \
        "v_permlane16_swap_b32 v38, v40\n\t" \
        "v_permlane16_swap_b32 v39, v41\n\t" \
        "buffer_store_dwordx4 v[38:41], v8, s[28:31], s59 offen offset:64 sc1\n\t" \
        "v_cvt_pk_bf16_f32 v46, v46, v47\n\t" \
        "v_cvt_pk_bf16_f32 v47, v48, v49\n\t" \
        "v_cvt_pk_bf16_f32 v48, v50, v51\n\t" \
        "v_cvt_pk_bf16_f32 v49, v52, v53\n\t" \
        "s_nop 0\n\t" \
        "v_permlane16_swap_b32 v46, v48\n\t" \
        "v_permlane16_swap_b32 v47, v49\n\t" \
        "buffer_store_dwordx4 v[46:49], v8, s[28:31], s59 offen offset:128 sc1\n\t" \
        "s_endpgm\n\t" \
        ".LTeprod%=:\n\t" \
        "s_waitcnt lgkmcnt(10)\n\t" \
        "v_mfma_f32_16x16x32_bf16 v[88:91], v[64:67], v[112:115], 0\n\t" \
        "v_lshlrev_b32 v116, 5, v120\n\t" \
        "v_bitop3_b32 v116, v116, s52, v23 bitop3:0x6a\n\t" \
        "v_xor_b32 v117, 8, v116\n\t" \
        "v_lshlrev_b32 v118, 5, v121\n\t" \
        "v_bitop3_b32 v118, v118, s52, v23 bitop3:0x6a\n\t" \
        "v_xor_b32 v119, 8, v118\n\t" \
        "ds_read_u16 v120, v20 offset:128\n\t" \
        "ds_read_u16 v121, v20 offset:136\n\t" \
        "ds_read_b64_tr_b16 v[64:65], v116 offset:0\n\t" \
        "ds_read_b64_tr_b16 v[66:67], v118 offset:0\n\t" \
        "s_waitcnt lgkmcnt(12)\n\t" \
        "v_mfma_f32_16x16x32_bf16 v[92:95], v[68:71], v[112:115], 0\n\t" \
        "ds_read_b64_tr_b16 v[68:69], v117 offset:0\n\t" \
        "ds_read_b64_tr_b16 v[70:71], v119 offset:0\n\t" \
        "s_waitcnt lgkmcnt(12)\n\t" \
        "v_mfma_f32_16x16x32_bf16 v[96:99], v[72:75], v[112:115], 0\n\t" \
        "ds_read_b64_tr_b16 v[72:73], v116 offset:64\n\t" \
        "ds_read_b64_tr_b16 v[74:75], v118 offset:64\n\t" \
        "s_waitcnt lgkmcnt(12)\n\t" \
        "v_mfma_f32_16x16x32_bf16 v[100:103], v[76:79], v[112:115], 0\n\t" \
        "ds_read_b64_tr_b16 v[76:77], v117 offset:64\n\t" \
        "ds_read_b64_tr_b16 v[78:79], v119 offset:64\n\t" \
        "s_waitcnt lgkmcnt(12)\n\t" \
        "v_mfma_f32_16x16x32_bf16 v[104:107], v[80:83], v[112:115], 0\n\t" \
        "ds_read_b64_tr_b16 v[80:81], v116 offset:128\n\t" \
        "ds_read_b64_tr_b16 v[82:83], v118 offset:128\n\t" \
        "s_waitcnt lgkmcnt(12)\n\t" \
        "v_mfma_f32_16x16x32_bf16 v[108:111], v[84:87], v[112:115], 0\n\t" \
        "ds_read_b64_tr_b16 v[84:85], v117 offset:128\n\t" \
        "ds_read_b64_tr_b16 v[86:87], v119 offset:128\n\t" \
        "v_and_b32 v112, v122, v25\n\t" \
        "v_and_b32 v113, v122, v26\n\t" \
        "v_and_b32 v114, v122, v27\n\t" \
        "v_and_b32 v115, v122, v28\n\t" \
        "ds_read_b32 v122, v21 offset:256\n\t" \
        "s_and_b32 s55, s50, 0xffff\n\t" \
        "s_cbranch_scc1 .LTetwo1%=\n\t" \
        ".LTeback1%=:\n\t" \
        "s_sub_u32 s55, s46, s47\n\t" \
        "v_xor_b32 v156, 8, v22\n\t" \
        "v_cmp_gt_i32 vcc, s55, v24\n\t" \
        "s_mov_b64 exec, vcc\n\t" \
        "v_cvt_pk_bf16_f32 v88, v88, v89\n\t" \
        "v_cvt_pk_bf16_f32 v89, v90, v91\n\t" \
        "ds_write_b64 v22, v[88:89] offset:0\n\t" \
        "v_cvt_pk_bf16_f32 v92, v92, v93\n\t" \
        "v_cvt_pk_bf16_f32 v93, v94, v95\n\t" \
        "ds_write_b64 v156, v[92:93] offset:0\n\t" \
        "v_cvt_pk_bf16_f32 v96, v96, v97\n\t" \
        "v_cvt_pk_bf16_f32 v97, v98, v99\n\t" \
        "ds_write_b64 v22, v[96:97] offset:64\n\t" \
        "v_cvt_pk_bf16_f32 v100, v100, v101\n\t" \
        "v_cvt_pk_bf16_f32 v101, v102, v103\n\t" \
        "ds_write_b64 v156, v[100:101] offset:64\n\t" \
        "v_cvt_pk_bf16_f32 v104, v104, v105\n\t" \
        "v_cvt_pk_bf16_f32 v105, v106, v107\n\t" \
        "ds_write_b64 v22, v[104:105] offset:128\n\t" \
        "v_cvt_pk_bf16_f32 v108, v108, v109\n\t" \
        "v_cvt_pk_bf16_f32 v109, v110, v111\n\t" \
        "ds_write_b64 v156, v[108:109] offset:128\n\t" \
        "s_mov_b64 exec, -1\n\t" \
        "s_lshl_b32 s55, s53, 4\n\t" \
        "s_add_u32 s47, s47, 16\n\t" \
        "v_add_u32 v22, s55, v22\n\t" \
        "s_lshr_b64 s[50:51], s[50:51], 16\n\t" \
        "v_add_u32 v20, 64, v20\n\t" \
        "v_add_u32 v21, 0x80, v21\n\t" \
        "s_add_u32 s55, s47, 16\n\t" \
        "s_cmp_lt_u32 s55, s46\n\t" \
        "s_cbranch_scc1 .LTeprod%=\n\t" \
        "s_branch .LTelast%=\n\t" \
        ".LTetwo1%=:\n\t" \
        "s_nop 4\n\t" \
        "ds_read_u16 v156, v20\n\t" \
        "ds_read_u16 v158, v20 offset:8\n\t" \
        "ds_read_b32 v127, v21 offset:4\n\t" \
        "s_waitcnt lgkmcnt(0)\n\t" \
        "v_lshrrev_b32 v124, 15, v156\n\t" \
        "v_lshlrev_b32 v156, 5, v156\n\t" \
        "v_bitop3_b32 v156, v156, s52, v23 bitop3:0x6a\n\t" \
        "v_mul_u32_u24 v124, 3968, v124\n\t" \
        "v_add_u32 v156, v156, v124\n\t" \
        "v_xor_b32 v157, 8, v156\n\t" \
        "v_lshrrev_b32 v125, 15, v158\n\t" \
        "v_lshlrev_b32 v158, 5, v158\n\t" \
        "v_bitop3_b32 v158, v158, s52, v23 bitop3:0x6a\n\t" \
        "v_mul_u32_u24 v125, 3968, v125\n\t" \
        "v_add_u32 v158, v158, v125\n\t" \
        "v_xor_b32 v159, 8, v158\n\t" \
        "ds_read_b64_tr_b16 v[128:129], v156 offset:0\n\t" \
        "ds_read_b64_tr_b16 v[130:131], v158 offset:0\n\t" \
        "ds_read_b64_tr_b16 v[132:133], v157 offset:0\n\t" \
        "ds_read_b64_tr_b16 v[134:135], v159 offset:0\n\t" \
        "ds_read_b64_tr_b16 v[136:137], v156 offset:64\n\t" \
        "ds_read_b64_tr_b16 v[138:139], v158 offset:64\n\t" \
        "ds_read_b64_tr_b16 v[140:141], v157 offset:64\n\t" \
        "ds_read_b64_tr_b16 v[142:143], v159 offset:64\n\t" \
        "ds_read_b64_tr_b16 v[144:145], v156 offset:128\n\t" \
        "ds_read_b64_tr_b16 v[146:147], v158 offset:128\n\t" \
        "ds_read_b64_tr_b16 v[148:149], v157 offset:128\n\t" \
        "ds_read_b64_tr_b16 v[150:151], v159 offset:128\n\t" \
        "v_and_b32 v124, v127, v25\n\t" \
        "v_and_b32 v125, v127, v26\n\t" \
        "v_and_b32 v126, v127, v27\n\t" \
        "v_and_b32 v127, v127, v28\n\t" \
        "s_nop 1\n\t" \
        "s_waitcnt lgkmcnt(10)\n\t" \
        "v_mfma_f32_16x16x32_bf16 v[88:91], v[128:131], v[124:127], v[88:91]\n\t" \
        "s_waitcnt lgkmcnt(8)\n\t" \
        "v_mfma_f32_16x16x32_bf16 v[92:95], v[132:135], v[124:127], v[92:95]\n\t" \
        "s_waitcnt lgkmcnt(6)\n\t" \
        "v_mfma_f32_16x16x32_bf16 v[96:99], v[136:139], v[124:127], v[96:99]\n\t" \
        "s_waitcnt lgkmcnt(4)\n\t" \
        "v_mfma_f32_16x16x32_bf16 v[100:103], v[140:143], v[124:127], v[100:103]\n\t" \
        "s_waitcnt lgkmcnt(2)\n\t" \
        "v_mfma_f32_16x16x32_bf16 v[104:107], v[144:147], v[124:127], v[104:107]\n\t" \
        "s_waitcnt lgkmcnt(0)\n\t" \
        "v_mfma_f32_16x16x32_bf16 v[108:111], v[148:151], v[124:127], v[108:111]\n\t" \
        "s_nop 7\n\t" \
        "s_branch .LTeback1%=\n\t" \
        ".LTetwo2%=:\n\t" \
        "s_nop 4\n\t" \
        "ds_read_u16 v156, v20\n\t" \
        "ds_read_u16 v158, v20 offset:8\n\t" \
        "ds_read_b32 v127, v21 offset:4\n\t" \
        "s_waitcnt lgkmcnt(0)\n\t" \
        "v_lshrrev_b32 v124, 15, v156\n\t" \
        "v_lshlrev_b32 v156, 5, v156\n\t" \
        "v_bitop3_b32 v156, v156, s52, v23 bitop3:0x6a\n\t" \
        "v_mul_u32_u24 v124, 3968, v124\n\t" \
        "v_add_u32 v156, v156, v124\n\t" \
        "v_xor_b32 v157, 8, v156\n\t" \
        "v_lshrrev_b32 v125, 15, v158\n\t" \
        "v_lshlrev_b32 v158, 5, v158\n\t" \
        "v_bitop3_b32 v158, v158, s52, v23 bitop3:0x6a\n\t" \
        "v_mul_u32_u24 v125, 3968, v125\n\t" \
        "v_add_u32 v158, v158, v125\n\t" \
        "v_xor_b32 v159, 8, v158\n\t" \
        "ds_read_b64_tr_b16 v[128:129], v156 offset:0\n\t" \
        "ds_read_b64_tr_b16 v[130:131], v158 offset:0\n\t" \
        "ds_read_b64_tr_b16 v[132:133], v157 offset:0\n\t" \
        "ds_read_b64_tr_b16 v[134:135], v159 offset:0\n\t" \
        "ds_read_b64_tr_b16 v[136:137], v156 offset:64\n\t" \
        "ds_read_b64_tr_b16 v[138:139], v158 offset:64\n\t" \
        "ds_read_b64_tr_b16 v[140:141], v157 offset:64\n\t" \
        "ds_read_b64_tr_b16 v[142:143], v159 offset:64\n\t" \
        "ds_read_b64_tr_b16 v[144:145], v156 offset:128\n\t" \
        "ds_read_b64_tr_b16 v[146:147], v158 offset:128\n\t" \
        "ds_read_b64_tr_b16 v[148:149], v157 offset:128\n\t" \
        "ds_read_b64_tr_b16 v[150:151], v159 offset:128\n\t" \
        "v_and_b32 v124, v127, v25\n\t" \
        "v_and_b32 v125, v127, v26\n\t" \
        "v_and_b32 v126, v127, v27\n\t" \
        "v_and_b32 v127, v127, v28\n\t" \
        "s_nop 1\n\t" \
        "s_waitcnt lgkmcnt(10)\n\t" \
        "v_mfma_f32_16x16x32_bf16 v[88:91], v[128:131], v[124:127], v[88:91]\n\t" \
        "s_waitcnt lgkmcnt(8)\n\t" \
        "v_mfma_f32_16x16x32_bf16 v[92:95], v[132:135], v[124:127], v[92:95]\n\t" \
        "s_waitcnt lgkmcnt(6)\n\t" \
        "v_mfma_f32_16x16x32_bf16 v[96:99], v[136:139], v[124:127], v[96:99]\n\t" \
        "s_waitcnt lgkmcnt(4)\n\t" \
        "v_mfma_f32_16x16x32_bf16 v[100:103], v[140:143], v[124:127], v[100:103]\n\t" \
        "s_waitcnt lgkmcnt(2)\n\t" \
        "v_mfma_f32_16x16x32_bf16 v[104:107], v[144:147], v[124:127], v[104:107]\n\t" \
        "s_waitcnt lgkmcnt(0)\n\t" \
        "v_mfma_f32_16x16x32_bf16 v[108:111], v[148:151], v[124:127], v[108:111]\n\t" \
        "s_nop 7\n\t" \
        "s_branch .LTeback2%=\n\t" \
        ".LT_again%=:\n\t" \
        "s_waitcnt vmcnt(0) lgkmcnt(0)\n\t" \
        "s_barrier\n\t" \
        "s_add_u32 s32, s32, s3\n\t" \
        "s_mov_b32 s3, 0\n\t" \
        "s_mov_b32 s11, 3\n\t" \
        "s_branch .LT_entry%=\n\t" \
        ".LR_abort%=:\n\t" \
        "s_waitcnt vmcnt(0) lgkmcnt(0)\n\t" \
        "s_barrier\n\t" \
        "s_nop 7\n\t" \
        "s_nop 7\n\t" \
        "s_sub_u32 s3, s34, 12\n\t" \
        "s_mov_b32 s34, 12\n\t" \
        "s_mov_b32 s11, 3\n\t" \
        "s_branch .LT_entry%=\n\t" \
        ".LH_ent%=:\n\t" \
        "s_nop 4\n\t" \
        "v_bfe_u32 v162, v2, 2, 1\n\t" \
        "v_lshrrev_b32 v163, 3, v2\n\t" \
        "s_mul_i32 s55, s35, 2560\n\t" \
        "s_add_u32 s55, s55, 109120\n\t" \
        "v_mul_u32_u24 v162, 0x500, v162\n\t" \
        "v_lshlrev_b32 v163, 4, v163\n\t" \
        "v_add3_u32 v9, v162, v163, s55\n\t" \
        "v_lshrrev_b32 v164, 4, v2\n\t" \
        "v_bfe_u32 v162, v2, 2, 2\n\t" \
        "v_and_b32 v163, 3, v2\n\t" \
        "v_lshl_add_u32 v162, v164, 2, v162\n\t" \
        "v_lshlrev_b32 v11, 3, v163\n\t" \
        "v_and_b32 v165, 1, v164\n\t" \
        "v_mul_u32_u24 v162, 0xc0, v162\n\t" \
        "v_xor_b32 v12, 32, v11\n\t" \
        "v_lshl_add_u32 v162, v165, 5, v162\n\t" \
        "v_lshlrev_b32 v163, 6, v165\n\t" \
        "s_mov_b32 s55, 0xba40\n\t" \
        "v_add3_u32 v6, v162, v11, s55\n\t" \
        "v_sub_u32 v7, v6, v163\n\t" \
        "v_mul_u32_u24 v162, 0xc0, v19\n\t" \
        "v_lshrrev_b32 v164, 1, v164\n\t" \
        "v_lshl_add_u32 v162, v165, 5, v162\n\t" \
        "v_lshl_add_u32 v8, v164, 4, v162\n\t" \
        "s_add_u32 s55, s32, s35\n\t" \
        "s_mul_i32 s56, s33, 0xc0\n\t" \
        "s_mul_i32 s55, s55, 0xa800\n\t" \
        "s_add_u32 s59, s55, s56\n\t" \
        "s_add_u32 s60, s59, 0x2a000\n\t" \
        "v_lshrrev_b32 v166, 4, v2\n\t" \
        "v_bfe_u32 v168, v2, 2, 2\n\t" \
        "v_and_b32 v167, 3, v2\n\t" \
        "v_cmp_eq_u32 vcc, v166, v168\n\t" \
        "v_lshlrev_b32 v167, v167, 1\n\t" \
        "s_nop 0\n\t" \
        "v_cndmask_b32 v167, 0, v167, vcc\n\t" \
        "v_bfe_i32 v25, v167, 0, 1\n\t" \
        "v_bfe_i32 v26, v167, 1, 1\n\t" \
        "v_bfe_i32 v27, v167, 2, 1\n\t" \
        "v_bfe_i32 v28, v167, 3, 1\n\t" \
        "s_lshl_b32 s48, s35, 10\n\t" \
        "s_and_b32 s55, s35, 1\n\t" \
        "s_add_u32 s48, s48, 0x1e640\n\t" \
        "s_mul_i32 s55, s55, 0x2a00\n\t" \
        "s_cmp_lt_u32 s35, 2\n\t" \
        "s_cselect_b32 s49, 0, 0x2940\n\t" \
        "s_cselect_b32 s53, 1, -1\n\t" \
        "s_add_u32 s49, s49, s55\n\t" \
        "s_mul_i32 s53, s53, 192\n\t" \
        "s_mov_b32 s52, 0x3ffe0\n\t" \
        "s_add_u32 s49, s49, 0x1f640\n\t" \
        "v_lshrrev_b32 v166, 4, v2\n\t" \
        "v_and_b32 v24, 15, v2\n\t" \
        "v_bfe_u32 v167, v2, 3, 1\n\t" \
        "v_lshl_add_u32 v167, v166, 2, v167\n\t" \
        "v_lshrrev_b32 v168, 1, v2\n\t" \
        "v_and_b32 v168, 2, v168\n\t" \
        "v_lshl_add_u32 v167, v167, 2, v168\n\t" \
        "s_add_u32 s55, s48, 512\n\t" \
        "v_add_u32 v20, s55, v167\n\t" \
        "v_lshl_add_u32 v21, v24, 3, s48\n\t" \
        "v_bfe_u32 v167, v2, 1, 1\n\t" \
        "v_xor_b32 v167, v167, v166\n\t" \
        "v_lshrrev_b32 v168, 2, v24\n\t" \
        "v_and_b32 v169, 2, v168\n\t" \
        "v_xor_b32 v167, v167, v169\n\t" \
        "v_and_b32 v169, 4, v2\n\t" \
        "v_lshlrev_b32 v167, 4, v167\n\t" \
        "v_lshl_add_u32 v167, v169, 1, v167\n\t" \
        "v_mul_i32_i24 v22, s53, v24\n\t" \
        "v_add3_u32 v22, v22, v167, s49\n\t" \
        "v_and_b32 v167, 3, v2\n\t" \
        "v_and_b32 v169, 1, v166\n\t" \
        "v_lshlrev_b32 v169, 3, v169\n\t" \
        "v_lshl_or_b32 v23, v167, 4, v169\n\t" \
        "s_mov_b32 s45, s48\n\t" \
        "v_mbcnt_lo_u32_b32 v140, s4, 0\n\t" \
        "v_mbcnt_hi_u32_b32 v140, s5, v140\n\t" \
        "s_bcnt1_i32_b64 s55, s[4:5]\n\t" \
        "v_add_u32 v141, 0, v63\n\t" \
        "v_lshl_add_u32 v140, v140, 3, s45\n\t" \
        "s_lshl3_add_u32 s45, s55, s45\n\t" \
        "s_mov_b64 exec, s[4:5]\n\t" \
        "ds_write2_b32 v140, v50, v141 offset1:1\n\t" \
        "s_mov_b64 exec, -1\n\t" \
        "v_mbcnt_lo_u32_b32 v140, s6, 0\n\t" \
        "v_mbcnt_hi_u32_b32 v140, s7, v140\n\t" \
        "s_bcnt1_i32_b64 s55, s[6:7]\n\t" \
        "v_add_u32 v141, 256, v63\n\t" \
        "v_lshl_add_u32 v140, v140, 3, s45\n\t" \
        "s_lshl3_add_u32 s45, s55, s45\n\t" \
        "s_mov_b64 exec, s[6:7]\n\t" \
        "ds_write2_b32 v140, v51, v141 offset1:1\n\t" \
        "s_mov_b64 exec, -1\n\t" \
        "v_mbcnt_lo_u32_b32 v140, s8, 0\n\t" \
        "v_mbcnt_hi_u32_b32 v140, s9, v140\n\t" \
        "s_bcnt1_i32_b64 s55, s[8:9]\n\t" \
        "v_add_u32 v141, 512, v63\n\t" \
        "v_lshl_add_u32 v140, v140, 3, s45\n\t" \
        "s_lshl3_add_u32 s45, s55, s45\n\t" \
        "s_mov_b64 exec, s[8:9]\n\t" \
        "ds_write2_b32 v140, v52, v141 offset1:1\n\t" \
        "s_mov_b64 exec, -1\n\t" \
        "v_mbcnt_lo_u32_b32 v140, s12, 0\n\t" \
        "v_mbcnt_hi_u32_b32 v140, s13, v140\n\t" \
        "s_bcnt1_i32_b64 s55, s[12:13]\n\t" \
        "v_add_u32 v141, 768, v63\n\t" \
        "v_lshl_add_u32 v140, v140, 3, s45\n\t" \
        "s_lshl3_add_u32 s45, s55, s45\n\t" \
        "s_mov_b64 exec, s[12:13]\n\t" \
        "ds_write2_b32 v140, v53, v141 offset1:1\n\t" \
        "s_mov_b64 exec, -1\n\t" \
        "v_mbcnt_lo_u32_b32 v140, s14, 0\n\t" \
        "v_mbcnt_hi_u32_b32 v140, s15, v140\n\t" \
        "s_bcnt1_i32_b64 s55, s[14:15]\n\t" \
        "v_add_u32 v141, 1024, v63\n\t" \
        "v_lshl_add_u32 v140, v140, 3, s45\n\t" \
        "s_lshl3_add_u32 s45, s55, s45\n\t" \
        "s_mov_b64 exec, s[14:15]\n\t" \
        "ds_write2_b32 v140, v54, v141 offset1:1\n\t" \
        "s_mov_b64 exec, -1\n\t" \
        "s_sub_u32 s46, s45, s48\n\t" \
        "s_lshr_b32 s46, s46, 3\n\t" \
        "s_cmp_gt_u32 s46, 64\n\t" \
        "s_cselect_b32 s54, 1, s54\n\t" \
        "s_cselect_b32 s46, 64, s46\n\t" \
        "v_mul_i32_i24 v30, s53, v2\n\t" \
        "v_lshlrev_b32 v166, 3, v2\n\t" \
        "v_lshlrev_b32 v167, 2, v2\n\t" \
        "v_add_u32 v30, s49, v30\n\t" \
        "v_and_b32 v166, 16, v166\n\t" \
        "v_and_b32 v167, 32, v167\n\t" \
        "v_lshl_add_u32 v31, v2, 3, s48\n\t" \
        "v_or3_b32 v30, v30, v166, v167\n\t" \
        "v_lshlrev_b32 v166, 2, v2\n\t" \
        "s_add_u32 s55, s48, 512\n\t" \
        "v_add_u32 v32, s55, v166\n\t" \
        "v_cvt_f32_u32 v34, s32\n\t" \
        "v_cvt_f32_u32 v35, s33\n\t" \
        "v_mov_b32 v36, 124\n\t" \
        "v_add_f32 v34, -2.0, v34\n\t" \
        "v_add_f32 v35, -2.0, v35\n\t" \
        "v_cvt_f32_u32 v33, 11\n\t" \
        "v_mov_b32 v37, 0x05040100\n\t" \
        "v_mov_b32 v38, 0x80008000\n\t" \
        "v_mov_b32 v39, 0x435f435f\n\t" \
        "ds_read_b64 v[68:69], v31\n\t" \
        "v_cmp_gt_u32 s[12:13], s46, v2\n\t" \
        "s_mov_b32 s14, 0x2\n\t" \
        "s_mov_b32 s15, 0x8\n\t" \
        "v_mov_b32 v83, 0x41980000\n\t" \
        "v_mov_b32 v29, 0x20002\n\t" \
        "s_waitcnt lgkmcnt(0)\n\t" \
        "v_lshlrev_b32 v70, 16, v68\n\t" \
        "v_and_b32 v71, 0xffff0000, v68\n\t" \
        "s_mov_b64 exec, s[12:13]\n\t" \
        "ds_write_b32 v69, v30\n\t" \
        "ds_write_b32 v69, v30 offset:1280\n\t" \
        "s_mov_b64 exec, -1\n\t" \
        "v_fract_f32 v72, v70\n\t" \
        "v_fract_f32 v73, v71\n\t" \
        "v_sub_f32 v70, v70, v34\n\t" \
        "v_sub_f32 v71, v71, v35\n\t" \
        "v_sub_f32 v74, 1.0, v72\n\t" \
        "v_sub_f32 v75, 1.0, v73\n\t" \
        "v_med3_f32 v78, v70, 0, v33\n\t" \
        "v_cvt_pk_bf16_f32 v74, v74, v75\n\t" \
        "v_med3_f32 v79, v71, 0, v83\n\t" \
        "v_cmp_neq_f32 s[4:5], v78, v70\n\t" \
        "v_and_b32 v75, 0xffff0000, v74\n\t" \
        "v_cmp_neq_f32 s[6:7], v79, v71\n\t" \
        "v_lshlrev_b32 v74, 16, v74\n\t" \
        "v_cvt_u32_f32 v78, v78\n\t" \
        "v_cvt_u32_f32 v79, v79\n\t" \
        "s_or_b64 s[4:5], s[4:5], s[6:7]\n\t" \
        "v_mul_f32 v76, v74, v75\n\t" \
        "v_mul_f32 v74, v74, v73\n\t" \
        "s_and_b64 s[4:5], s[4:5], s[12:13]\n\t" \
        "s_cselect_b32 s54, 1, s54\n\t" \
        "v_mul_f32 v77, v72, v75\n\t" \
        "v_mul_f32 v75, v72, v73\n\t" \
        "v_cmp_neq_f32 s[4:5], 0, v73\n\t" \
        "v_cmp_neq_f32 s[6:7], 0, v72\n\t" \
        "v_cvt_pk_bf16_f32 v76, v76, v74\n\t" \
        "v_cvt_pk_bf16_f32 v77, v77, v75\n\t" \
        "v_mul_u32_u24 v80, 124, v78\n\t" \
        "v_add_u32 v83, 1, v79\n\t" \
        "v_mad_u32_u24 v80, v79, 6, v80\n\t" \
        "v_bfe_u32 v84, v79, 2, 1\n\t" \
        "v_bfe_u32 v83, v83, 2, 1\n\t" \
        "s_and_b64 s[50:51], s[4:5], s[6:7]\n\t" \
        "v_add3_u32 v81, v80, v84, s14\n\t" \
        "v_add3_u32 v82, v80, v83, s15\n\t" \
        "s_and_b64 s[50:51], s[50:51], s[12:13]\n\t" \
        "v_cndmask_b32 v83, 0, v36, s[6:7]\n\t" \
        "v_perm_b32 v74, v77, v76, v37\n\t" \
        "v_add_u32 v83, v81, v83\n\t" \
        "v_cndmask_b32 v68, v74, v76, s[4:5]\n\t" \
        "v_cndmask_b32 v83, v83, v82, s[4:5]\n\t" \
        "v_cndmask_b32 v75, 0, v38, s[50:51]\n\t" \
        "v_lshl_or_b32 v81, v83, 16, v81\n\t" \
        "v_cndmask_b32 v69, 0, v77, s[50:51]\n\t" \
        "v_or_b32 v81, v81, v75\n\t" \
        "ds_write_b64 v31, v[68:69]\n\t" \
        "v_cndmask_b32 v81, v29, v81, s[12:13]\n\t" \
        "ds_write_b32 v32, v81\n\t" \
        "s_lshl_b32 s55, s35, 2\n\t" \
        "s_cmp_lg_u32 s54, 0\n\t" \
        "v_mov_b32 v162, s55\n\t" \
        "s_cselect_b32 s55, 0x10000, s46\n\t" \
        "v_mov_b32 v163, s55\n\t" \
        "ds_write_b32 v162, v163\n\t" \
        "ds_read_u16 v120, v20\n\t" \
        "ds_read_u16 v121, v20 offset:8\n\t" \
        "ds_read_b32 v122, v21\n\t" \
        "v_mov_b32 v156, 0\n\t" \
        "s_mov_b32 s47, 0\n\t" \
        "s_waitcnt lgkmcnt(0)\n\t" \
        "v_lshlrev_b32 v116, 5, v120\n\t" \
        "v_bitop3_b32 v116, v116, s52, v23 bitop3:0x6a\n\t" \
        "v_xor_b32 v117, 8, v116\n\t" \
        "v_lshlrev_b32 v118, 5, v121\n\t" \
        "v_bitop3_b32 v118, v118, s52, v23 bitop3:0x6a\n\t" \
        "v_xor_b32 v119, 8, v118\n\t" \
        "v_and_b32 v112, v122, v25\n\t" \
        "v_and_b32 v113, v122, v26\n\t" \
        "v_and_b32 v114, v122, v27\n\t" \
        "v_and_b32 v115, v122, v28\n\t" \
        "ds_read_u16 v120, v20 offset:64\n\t" \
        "ds_read_u16 v121, v20 offset:72\n\t" \
        "ds_read_b32 v122, v21 offset:128\n\t" \
        "s_waitcnt vmcnt(6) lgkmcnt(0)\n\t" \
        "s_barrier\n\t" \
        "ds_read_b128 v[152:155], v156\n\t" \
        "ds_read_b64_tr_b16 v[64:65], v116 offset:0\n\t" \
        "ds_read_b64_tr_b16 v[66:67], v118 offset:0\n\t" \
        "ds_read_b64_tr_b16 v[68:69], v117 offset:0\n\t" \
        "ds_read_b64_tr_b16 v[70:71], v119 offset:0\n\t" \
        "ds_read_b64_tr_b16 v[72:73], v116 offset:64\n\t" \
        "ds_read_b64_tr_b16 v[74:75], v118 offset:64\n\t" \
        "ds_read_b64_tr_b16 v[76:77], v117 offset:64\n\t" \
        "ds_read_b64_tr_b16 v[78:79], v119 offset:64\n\t" \
        "ds_read_b64_tr_b16 v[80:81], v116 offset:128\n\t" \
        "ds_read_b64_tr_b16 v[82:83], v118 offset:128\n\t" \
        "ds_read_b64_tr_b16 v[84:85], v117 offset:128\n\t" \
        "ds_read_b64_tr_b16 v[86:87], v119 offset:128\n\t" \
        "s_waitcnt lgkmcnt(12)\n\t" \
        "v_add_u32 v152, v152, v154\n\t" \
        "v_add_u32 v153, v153, v155\n\t" \
        "v_max_u32 v152, v152, v153\n\t" \
        "s_nop 0\n\t" \
        "v_readfirstlane_b32 s55, v152\n\t" \
        "s_nop 0\n\t" \
        "s_cmp_gt_u32 s55, 56\n\t" \
        "s_cbranch_scc1 .Lfail%=\n\t" \
        "s_cmp_gt_u32 s46, 16\n\t" \
        "s_cbranch_scc1 .LHeprod%=\n\t" \
        ".LHelast%=:\n\t" \
        "s_waitcnt lgkmcnt(10)\n\t" \
        "v_mfma_f32_16x16x32_bf16 v[88:91], v[64:67], v[112:115], 0\n\t" \
        "s_waitcnt lgkmcnt(8)\n\t" \
        "v_mfma_f32_16x16x32_bf16 v[92:95], v[68:71], v[112:115], 0\n\t" \
        "s_waitcnt lgkmcnt(6)\n\t" \
        "v_mfma_f32_16x16x32_bf16 v[96:99], v[72:75], v[112:115], 0\n\t" \
        "s_waitcnt lgkmcnt(4)\n\t" \
        "v_mfma_f32_16x16x32_bf16 v[100:103], v[76:79], v[112:115], 0\n\t" \
        "s_waitcnt lgkmcnt(2)\n\t" \
        "v_mfma_f32_16x16x32_bf16 v[104:107], v[80:83], v[112:115], 0\n\t" \
        "s_waitcnt lgkmcnt(0)\n\t" \
        "v_mfma_f32_16x16x32_bf16 v[108:111], v[84:87], v[112:115], 0\n\t" \
        "s_and_b32 s55, s50, 0xffff\n\t" \
        "s_cbranch_scc1 .LHetwo2%=\n\t" \
        ".LHeback2%=:\n\t" \
        "s_sub_u32 s55, s46, s47\n\t" \
        "v_xor_b32 v156, 8, v22\n\t" \
        "v_cmp_gt_i32 vcc, s55, v24\n\t" \
        "s_mov_b64 exec, vcc\n\t" \
        "v_cvt_pk_bf16_f32 v88, v88, v89\n\t" \
        "v_cvt_pk_bf16_f32 v89, v90, v91\n\t" \
        "ds_write_b64 v22, v[88:89] offset:0\n\t" \
        "v_cvt_pk_bf16_f32 v92, v92, v93\n\t" \
        "v_cvt_pk_bf16_f32 v93, v94, v95\n\t" \
        "ds_write_b64 v156, v[92:93] offset:0\n\t" \
        "v_cvt_pk_bf16_f32 v96, v96, v97\n\t" \
        "v_cvt_pk_bf16_f32 v97, v98, v99\n\t" \
        "ds_write_b64 v22, v[96:97] offset:64\n\t" \
        "v_cvt_pk_bf16_f32 v100, v100, v101\n\t" \
        "v_cvt_pk_bf16_f32 v101, v102, v103\n\t" \
        "ds_write_b64 v156, v[100:101] offset:64\n\t" \
        "v_cvt_pk_bf16_f32 v104, v104, v105\n\t" \
        "v_cvt_pk_bf16_f32 v105, v106, v107\n\t" \
        "ds_write_b64 v22, v[104:105] offset:128\n\t" \
        "v_cvt_pk_bf16_f32 v108, v108, v109\n\t" \
        "v_cvt_pk_bf16_f32 v109, v110, v111\n\t" \
        "ds_write_b64 v156, v[108:109] offset:128\n\t" \
        "s_mov_b64 exec, -1\n\t" \
        "s_waitcnt lgkmcnt(0)\n\t" \
        "s_nop 7\n\t" \
        "s_branch .LH_join%=\n\t" \
        ".LHeprod%=:\n\t" \
        "s_waitcnt lgkmcnt(10)\n\t" \
        "v_mfma_f32_16x16x32_bf16 v[88:91], v[64:67], v[112:115], 0\n\t" \
        "v_lshlrev_b32 v116, 5, v120\n\t" \
        "v_bitop3_b32 v116, v116, s52, v23 bitop3:0x6a\n\t" \
        "v_xor_b32 v117, 8, v116\n\t" \
        "v_lshlrev_b32 v118, 5, v121\n\t" \
        "v_bitop3_b32 v118, v118, s52, v23 bitop3:0x6a\n\t" \
        "v_xor_b32 v119, 8, v118\n\t" \
        "ds_read_u16 v120, v20 offset:128\n\t" \
        "ds_read_u16 v121, v20 offset:136\n\t" \
        "ds_read_b64_tr_b16 v[64:65], v116 offset:0\n\t" \
        "ds_read_b64_tr_b16 v[66:67], v118 offset:0\n\t" \
        "s_waitcnt lgkmcnt(12)\n\t" \
        "v_mfma_f32_16x16x32_bf16 v[92:95], v[68:71], v[112:115], 0\n\t" \
        "ds_read_b64_tr_b16 v[68:69], v117 offset:0\n\t" \
        "ds_read_b64_tr_b16 v[70:71], v119 offset:0\n\t" \
        "s_waitcnt lgkmcnt(12)\n\t" \
        "v_mfma_f32_16x16x32_bf16 v[96:99], v[72:75], v[112:115], 0\n\t" \
        "ds_read_b64_tr_b16 v[72:73], v116 offset:64\n\t" \
        "ds_read_b64_tr_b16 v[74:75], v118 offset:64\n\t" \
        "s_waitcnt lgkmcnt(12)\n\t" \
        "v_mfma_f32_16x16x32_bf16 v[100:103], v[76:79], v[112:115], 0\n\t" \
        "ds_read_b64_tr_b16 v[76:77], v117 offset:64\n\t" \
        "ds_read_b64_tr_b16 v[78:79], v119 offset:64\n\t" \
        "s_waitcnt lgkmcnt(12)\n\t" \
        "v_mfma_f32_16x16x32_bf16 v[104:107], v[80:83], v[112:115], 0\n\t" \
        "ds_read_b64_tr_b16 v[80:81], v116 offset:128\n\t" \
        "ds_read_b64_tr_b16 v[82:83], v118 offset:128\n\t" \
        "s_waitcnt lgkmcnt(12)\n\t" \
        "v_mfma_f32_16x16x32_bf16 v[108:111], v[84:87], v[112:115], 0\n\t" \
        "ds_read_b64_tr_b16 v[84:85], v117 offset:128\n\t" \
        "ds_read_b64_tr_b16 v[86:87], v119 offset:128\n\t" \
        "v_and_b32 v112, v122, v25\n\t" \
        "v_and_b32 v113, v122, v26\n\t" \
        "v_and_b32 v114, v122, v27\n\t" \
        "v_and_b32 v115, v122, v28\n\t" \
        "ds_read_b32 v122, v21 offset:256\n\t" \
        "s_and_b32 s55, s50, 0xffff\n\t" \
        "s_cbranch_scc1 .LHetwo1%=\n\t" \
        ".LHeback1%=:\n\t" \
        "s_sub_u32 s55, s46, s47\n\t" \
        "v_xor_b32 v156, 8, v22\n\t" \
        "v_cmp_gt_i32 vcc, s55, v24\n\t" \
        "s_mov_b64 exec, vcc\n\t" \
        "v_cvt_pk_bf16_f32 v88, v88, v89\n\t" \
        "v_cvt_pk_bf16_f32 v89, v90, v91\n\t" \
        "ds_write_b64 v22, v[88:89] offset:0\n\t" \
        "v_cvt_pk_bf16_f32 v92, v92, v93\n\t" \
        "v_cvt_pk_bf16_f32 v93, v94, v95\n\t" \
        "ds_write_b64 v156, v[92:93] offset:0\n\t" \
        "v_cvt_pk_bf16_f32 v96, v96, v97\n\t" \
        "v_cvt_pk_bf16_f32 v97, v98, v99\n\t" \
        "ds_write_b64 v22, v[96:97] offset:64\n\t" \
        "v_cvt_pk_bf16_f32 v100, v100, v101\n\t" \
        "v_cvt_pk_bf16_f32 v101, v102, v103\n\t" \
        "ds_write_b64 v156, v[100:101] offset:64\n\t" \
        "v_cvt_pk_bf16_f32 v104, v104, v105\n\t" \
        "v_cvt_pk_bf16_f32 v105, v106, v107\n\t" \
        "ds_write_b64 v22, v[104:105] offset:128\n\t" \
        "v_cvt_pk_bf16_f32 v108, v108, v109\n\t" \
        "v_cvt_pk_bf16_f32 v109, v110, v111\n\t" \
        "ds_write_b64 v156, v[108:109] offset:128\n\t" \
        "s_mov_b64 exec, -1\n\t" \
        "s_lshl_b32 s55, s53, 4\n\t" \
        "s_add_u32 s47, s47, 16\n\t" \
        "v_add_u32 v22, s55, v22\n\t" \
        "s_lshr_b64 s[50:51], s[50:51], 16\n\t" \
        "v_add_u32 v20, 64, v20\n\t" \
        "v_add_u32 v21, 0x80, v21\n\t" \
        "s_add_u32 s55, s47, 16\n\t" \
        "s_cmp_lt_u32 s55, s46\n\t" \
        "s_cbranch_scc1 .LHeprod%=\n\t" \
        "s_branch .LHelast%=\n\t" \
        ".LHetwo1%=:\n\t" \
        "s_nop 4\n\t" \
        "ds_read_u16 v156, v20\n\t" \
        "ds_read_u16 v158, v20 offset:8\n\t" \
        "ds_read_b32 v127, v21 offset:4\n\t" \
        "s_waitcnt lgkmcnt(0)\n\t" \
        "v_lshrrev_b32 v124, 15, v156\n\t" \
        "v_lshlrev_b32 v156, 5, v156\n\t" \
        "v_bitop3_b32 v156, v156, s52, v23 bitop3:0x6a\n\t" \
        "v_mul_u32_u24 v124, 3968, v124\n\t" \
        "v_add_u32 v156, v156, v124\n\t" \
        "v_xor_b32 v157, 8, v156\n\t" \
        "v_lshrrev_b32 v125, 15, v158\n\t" \
        "v_lshlrev_b32 v158, 5, v158\n\t" \
        "v_bitop3_b32 v158, v158, s52, v23 bitop3:0x6a\n\t" \
        "v_mul_u32_u24 v125, 3968, v125\n\t" \
        "v_add_u32 v158, v158, v125\n\t" \
        "v_xor_b32 v159, 8, v158\n\t" \
        "ds_read_b64_tr_b16 v[128:129], v156 offset:0\n\t" \
        "ds_read_b64_tr_b16 v[130:131], v158 offset:0\n\t" \
        "ds_read_b64_tr_b16 v[132:133], v157 offset:0\n\t" \
        "ds_read_b64_tr_b16 v[134:135], v159 offset:0\n\t" \
        "ds_read_b64_tr_b16 v[136:137], v156 offset:64\n\t" \
        "ds_read_b64_tr_b16 v[138:139], v158 offset:64\n\t" \
        "ds_read_b64_tr_b16 v[140:141], v157 offset:64\n\t" \
        "ds_read_b64_tr_b16 v[142:143], v159 offset:64\n\t" \
        "ds_read_b64_tr_b16 v[144:145], v156 offset:128\n\t" \
        "ds_read_b64_tr_b16 v[146:147], v158 offset:128\n\t" \
        "ds_read_b64_tr_b16 v[148:149], v157 offset:128\n\t" \
        "ds_read_b64_tr_b16 v[150:151], v159 offset:128\n\t" \
        "v_and_b32 v124, v127, v25\n\t" \
        "v_and_b32 v125, v127, v26\n\t" \
        "v_and_b32 v126, v127, v27\n\t" \
        "v_and_b32 v127, v127, v28\n\t" \
        "s_nop 1\n\t" \
        "s_waitcnt lgkmcnt(10)\n\t" \
        "v_mfma_f32_16x16x32_bf16 v[88:91], v[128:131], v[124:127], v[88:91]\n\t" \
        "s_waitcnt lgkmcnt(8)\n\t" \
        "v_mfma_f32_16x16x32_bf16 v[92:95], v[132:135], v[124:127], v[92:95]\n\t" \
        "s_waitcnt lgkmcnt(6)\n\t" \
        "v_mfma_f32_16x16x32_bf16 v[96:99], v[136:139], v[124:127], v[96:99]\n\t" \
        "s_waitcnt lgkmcnt(4)\n\t" \
        "v_mfma_f32_16x16x32_bf16 v[100:103], v[140:143], v[124:127], v[100:103]\n\t" \
        "s_waitcnt lgkmcnt(2)\n\t" \
        "v_mfma_f32_16x16x32_bf16 v[104:107], v[144:147], v[124:127], v[104:107]\n\t" \
        "s_waitcnt lgkmcnt(0)\n\t" \
        "v_mfma_f32_16x16x32_bf16 v[108:111], v[148:151], v[124:127], v[108:111]\n\t" \
        "s_nop 7\n\t" \
        "s_branch .LHeback1%=\n\t" \
        ".LHetwo2%=:\n\t" \
        "s_nop 4\n\t" \
        "ds_read_u16 v156, v20\n\t" \
        "ds_read_u16 v158, v20 offset:8\n\t" \
        "ds_read_b32 v127, v21 offset:4\n\t" \
        "s_waitcnt lgkmcnt(0)\n\t" \
        "v_lshrrev_b32 v124, 15, v156\n\t" \
        "v_lshlrev_b32 v156, 5, v156\n\t" \
        "v_bitop3_b32 v156, v156, s52, v23 bitop3:0x6a\n\t" \
        "v_mul_u32_u24 v124, 3968, v124\n\t" \
        "v_add_u32 v156, v156, v124\n\t" \
        "v_xor_b32 v157, 8, v156\n\t" \
        "v_lshrrev_b32 v125, 15, v158\n\t" \
        "v_lshlrev_b32 v158, 5, v158\n\t" \
        "v_bitop3_b32 v158, v158, s52, v23 bitop3:0x6a\n\t" \
        "v_mul_u32_u24 v125, 3968, v125\n\t" \
        "v_add_u32 v158, v158, v125\n\t" \
        "v_xor_b32 v159, 8, v158\n\t" \
        "ds_read_b64_tr_b16 v[128:129], v156 offset:0\n\t" \
        "ds_read_b64_tr_b16 v[130:131], v158 offset:0\n\t" \
        "ds_read_b64_tr_b16 v[132:133], v157 offset:0\n\t" \
        "ds_read_b64_tr_b16 v[134:135], v159 offset:0\n\t" \
        "ds_read_b64_tr_b16 v[136:137], v156 offset:64\n\t" \
        "ds_read_b64_tr_b16 v[138:139], v158 offset:64\n\t" \
        "ds_read_b64_tr_b16 v[140:141], v157 offset:64\n\t" \
        "ds_read_b64_tr_b16 v[142:143], v159 offset:64\n\t" \
        "ds_read_b64_tr_b16 v[144:145], v156 offset:128\n\t" \
        "ds_read_b64_tr_b16 v[146:147], v158 offset:128\n\t" \
        "ds_read_b64_tr_b16 v[148:149], v157 offset:128\n\t" \
        "ds_read_b64_tr_b16 v[150:151], v159 offset:128\n\t" \
        "v_and_b32 v124, v127, v25\n\t" \
        "v_and_b32 v125, v127, v26\n\t" \
        "v_and_b32 v126, v127, v27\n\t" \
        "v_and_b32 v127, v127, v28\n\t" \
        "s_nop 1\n\t" \
        "s_waitcnt lgkmcnt(10)\n\t" \
        "v_mfma_f32_16x16x32_bf16 v[88:91], v[128:131], v[124:127], v[88:91]\n\t" \
        "s_waitcnt lgkmcnt(8)\n\t" \
        "v_mfma_f32_16x16x32_bf16 v[92:95], v[132:135], v[124:127], v[92:95]\n\t" \
        "s_waitcnt lgkmcnt(6)\n\t" \
        "v_mfma_f32_16x16x32_bf16 v[96:99], v[136:139], v[124:127], v[96:99]\n\t" \
        "s_waitcnt lgkmcnt(4)\n\t" \
        "v_mfma_f32_16x16x32_bf16 v[100:103], v[140:143], v[124:127], v[100:103]\n\t" \
        "s_waitcnt lgkmcnt(2)\n\t" \
        "v_mfma_f32_16x16x32_bf16 v[104:107], v[144:147], v[124:127], v[104:107]\n\t" \
        "s_waitcnt lgkmcnt(0)\n\t" \
        "v_mfma_f32_16x16x32_bf16 v[108:111], v[148:151], v[124:127], v[108:111]\n\t" \
        "s_nop 7\n\t" \
        "s_branch .LHeback2%=\n\t" \
        ".Lpf1%=:\n\t" \
        ".Lfail%=:\n\t" \
        "s_waitcnt vmcnt(0) lgkmcnt(0)\n\t" \
        "s_nop 7\n\t" \
        "s_nop 7\n\t" \
        "s_add_u32 s34, s34, s3\n\t" \
        "s_mov_b32 s41, 1\n\t" \
        ".Lexit%=:\n\t"
#define DCN_ASM_CLOBBERS "s3", "s4", "s5", "s6", "s7", "s8", "s9", "s10", "s11", "s12", "s13", "s14", "s15", "s36", "s37", "s38", "s39", "s40", "s42", "s43", "s44", "s45", "s46", "s47", "s48", "s49", "s50", "s51", "s52", "s53", "s54", "s55", "s56", "s57", "s58", "s59", "s60", "s61", "s62", "s63", "s64", "s65", "s66", "s67", "s68", "s69", "s70", "s71", "s72", "s73", "s74", "s75", "s76", "s77", "s78", "s79", "s80", "s81", "s82", "s83", "s84", "s85", "s86", "s87", "s88", "s89", "s90", "s91", "s92", "s93", "s94", "s95", "s96", "s97", "s98", "s99", "v2", "v3", "v4", "v5", "v6", "v7", "v8", "v9", "v10", "v11", "v12", "v13", "v14", "v15", "v16", "v17", "v18", "v19", "v20", "v21", "v22", "v23", "v24", "v25", "v26", "v27", "v28", "v29", "v30", "v31", "v32", "v33", "v34", "v35", "v36", "v37", "v38", "v39", "v40", "v41", "v42", "v43", "v44", "v45", "v46", "v47", "v48", "v49", "v50", "v51", "v52", "v53", "v54", "v55", "v56", "v57", "v58", "v59", "v60", "v61", "v62", "v63", "v64", "v65", "v66", "v67", "v68", "v69", "v70", "v71", "v72", "v73", "v74", "v75", "v76", "v77", "v78", "v79", "v80", "v81", "v82", "v83", "v84", "v85", "v86", "v87", "v88", "v89", "v90", "v91", "v92", "v93", "v94", "v95", "v96", "v97", "v98", "v99", "v100", "v101", "v102", "v103", "v104", "v105", "v106", "v107", "v108", "v109", "v110", "v111", "v112", "v113", "v114", "v115", "v116", "v117", "v118", "v119", "v120", "v121", "v122", "v123", "v124", "v125", "v126", "v127", "v128", "v129", "v130", "v131", "v132", "v133", "v134", "v135", "v136", "v137", "v138", "v139", "v140", "v141", "v142", "v143", "v144", "v145", "v146", "v147", "v148", "v149", "v150", "v151", "v152", "v153", "v154", "v155", "v156", "v157", "v158", "v159", "v160", "v161", "v162", "v163", "v164", "v165", "v166", "v167", "v168", "v169", "v170", "v171", "v172", "v173", "v174", "v175", "v176", "v177", "v178", "v179", "v180", "v181", "v182", "v183", "v184", "v185", "v186", "v187", "v188", "v189", "v190", "v191", "v192", "v193", "v194", "v195", "v196", "v197", "v198", "v199", "v200", "v201", "v202", "v203", "v204", "v205", "v206", "v207", "v208", "v209", "v210", "v211", "v212", "v213", "v214", "v215", "v216", "v217", "v218", "v219", "v220", "v221", "v222", "v223", "v224", "v225", "v226", "v227", "v228", "v229", "v230", "v231", "v232", "v233", "v234", "v235", "v236", "v237", "v238", "v239", "v240", "v241", "v242", "v243", "v244", "v245", "v246", "v247", "v248", "v249", "v250", "v251", "v252", "v253", "v254", "v255", "a0", "a1", "a2", "a3", "a4", "a5", "a6", "a7", "a8", "a9", "a10", "a11", "a12", "a13", "a14", "a15", "a16", "a17", "a18", "a19", "a20", "a21", "a22", "a23", "a24", "a25", "a26", "a27", "a28", "a29", "a30", "a31", "a32", "a33", "a34", "a35", "a36", "a37", "a38", "a39", "a40", "a41", "a42", "a43", "a44", "a45", "a46", "a47", "a48", "a49", "a50", "a51", "a52", "a53", "a54", "a55", "a56", "a57", "a58", "a59", "a60", "a61", "a62", "a63", "a64", "a65", "a66", "a67", "a68", "a69", "a70", "a71", "a72", "a73", "a74", "a75", "a76", "a77", "a78", "a79", "a80", "a81", "a82", "a83", "a84", "a85", "a86", "a87", "a88", "a89", "a90", "a91", "a92", "a93", "a94", "a95", "a96", "a97", "a98", "a99", "a100", "a101", "a102", "a103", "a104", "a105", "a106", "a107", "a108", "a109", "a110", "a111", "a112", "a113", "a114", "a115", "a116", "a117", "a118", "a119", "a120", "a121", "a122", "a123", "a124", "a125", "a126", "a127", "a128", "a129", "a130", "a131", "a132", "a133", "a134", "a135", "a136", "a137", "a138", "a139", "a140", "a141", "a142", "a143", "a144", "a145", "a146", "a147", "a148", "a149", "a150", "a151", "a152", "a153", "a154", "a155", "a156", "a157", "a158", "a159", "a160", "a161", "a162", "a163", "a164", "a165", "a166", "a167", "a168", "a169", "a170", "a171", "a172", "a173", "a174", "a175", "a176", "a177", "a178", "a179", "a180", "a181", "a182", "a183", "a184", "a185", "a186", "a187", "a188", "a189", "a190", "a191", "a192", "a193", "a194", "a195", "a196", "a197", "a198", "a199", "a200", "a201", "a202", "a203", "a204", "a205", "a206", "a207", "a208", "a209", "a210", "a211", "a212", "a213", "a214", "a215", "a216", "a217", "a218", "a219", "a220", "a221", "a222", "a223", "a224", "a225", "a226", "a227", "a228", "a229", "a230", "a231", "a232", "a233", "a234", "a235", "a236", "a237", "a238", "a239", "a240", "a241", "a242", "a243", "a244", "a245", "a246", "a247", "a248", "a249", "a250", "a251", "a252", "a253", "a254", "a255"
#define NWG 255
#define CYC 1

__device__ __forceinline__ float bf_lo(unsigned u) { return __uint_as_float(u << 16); }
__device__ __forceinline__ float bf_hi(unsigned u) { return __uint_as_float(u & 0xffff0000u); }
__device__ __forceinline__ unsigned cvt_pk(float lo, float hi) {
    bf16x2 r = {(__bf16)lo, (__bf16)hi};
    unsigned u;
    __builtin_memcpy(&u, &r, 4);
    return u;
}
__device__ __forceinline__ f32x4 mfma16(u32x4 a, u32x4 b, f32x4 c) {
    bf16x8 A, B;
    __builtin_memcpy(&A, &a, 16);
    __builtin_memcpy(&B, &b, 16);
    return __builtin_amdgcn_mfma_f32_16x16x32_bf16(A, B, c, 0, 0, 0);
}
__device__ __forceinline__ u32x2 tr_read(unsigned a) {
    s16x4 v = __builtin_amdgcn_ds_read_tr16_b64_v4i16((lds_s16x4*)(uintptr_t)a);
    u32x2 u;
    __builtin_memcpy(&u, &v, 8);
    return u;
}
__device__ __forceinline__ unsigned clip_pk(unsigned v) {
    typedef __attribute__((ext_vector_type(2))) short i16x2;
    i16x2 r;
    __builtin_memcpy(&r, &v, 4);
    r = __builtin_elementwise_min(__builtin_elementwise_max(r, (i16x2){0, 0}), (i16x2){0x435f, 0x435f});
    __builtin_memcpy(&v, &r, 4);
    return v;
}

__device__ __forceinline__ void slow_tile(const unsigned short* x, const unsigned short* off, const unsigned short* wgt, unsigned short* out,
                                          const int wv, const unsigned lane, const int y0, const int x0, const int nrows, const unsigned lb) {
    const unsigned px = lane & 15, q = lane >> 4;
#pragma unroll 1
    for (int r0 = 0; r0 < nrows; r0 += 4) {
        const int row = y0 + min(r0 + wv, nrows - 1), col = x0 + px;
        f32x4 acc[6];
#pragma unroll
        for (int i = 0; i < 6; ++i) acc[i] = (f32x4){0.f, 0.f, 0.f, 0.f};
#pragma unroll 1
        for (int t = 0; t < 9; ++t) {
            asm volatile("s_waitcnt vmcnt(0) lgkmcnt(0)\n\ts_barrier" ::: "memory");
            for (unsigned d = wv * 64 + lane; d < WTAP / 16; d += NTHREADS)
                *(lds_u32x4*)(uintptr_t)(lb + d * 16) = *(const glb_u32x4*)((uintptr_t)wgt + t * WTAP + d * 16);
            const unsigned od = *(const glb_u32*)((uintptr_t)off + (row * IMG + col) * 36 + t * 4);
            const unsigned pc = clip_pk(cvt_pk((float)(row + t / 3 - 1) + bf_lo(od), (float)(col + t % 3 - 1) + bf_hi(od)));
            const float py = bf_lo(pc), pxf = bf_hi(pc);
            const int gy = (int)py, gx = (int)pxf;
            const float wy = py - (float)gy, wx = pxf - (float)gx;
            const unsigned ab = cvt_pk(1.f - wy, 1.f - wx);
            const float a = bf_lo(ab), b = bf_hi(ab);
            const unsigned w01 = cvt_pk(a * b, a * wx), w23 = cvt_pk(wy * b, wy * wx);
            const unsigned o00 = (gy * IMG + gx) * (CH * 2) + q * 16;
            const unsigned dx = gx < IMG - 1 ? CH * 2 : 0, dy = gy < IMG - 1 ? ROWB : 0;
            const float wc[4] = {bf_lo(w01), bf_hi(w01), bf_lo(w23), bf_hi(w23)};
            u32x4 bfr[3];
#pragma unroll
            for (int j = 0; j < 3; ++j) {
                float vv[8];
#pragma unroll
                for (int c = 0; c < 4; ++c) {
                    const u32x4 gv = *(const glb_u32x4*)((uintptr_t)x + (o00 + ((c & 1) ? dx : 0u) + ((c & 2) ? dy : 0u) + j * 64));
#pragma unroll
                    for (int k = 0; k < 4; ++k) {
                        vv[2 * k] = c ? fmaf(wc[c], bf_lo(gv[k]), vv[2 * k]) : wc[0] * bf_lo(gv[k]);
                        vv[2 * k + 1] = c ? fmaf(wc[c], bf_hi(gv[k]), vv[2 * k + 1]) : wc[0] * bf_hi(gv[k]);
                    }
                }
                bfr[j] = (u32x4){cvt_pk(vv[0], vv[1]), cvt_pk(vv[2], vv[3]), cvt_pk(vv[4], vv[5]), cvt_pk(vv[6], vv[7])};
            }
            asm volatile("s_waitcnt vmcnt(0) lgkmcnt(0)\n\ts_barrier" ::: "memory");
            const unsigned wa = lb + (8 * q + ((lane >> 2) & 3)) * 192 + (lane & 3) * 8;
#pragma unroll
            for (int j = 0; j < 3; ++j)
#pragma unroll
                for (int i = 0; i < 6; ++i) {
                    const u32x2 lo = tr_read(wa + j * 6144 + i * 32), hi = tr_read(wa + j * 6144 + i * 32 + 768);
                    acc[i] = mfma16((u32x4){lo[0], lo[1], hi[0], hi[1]}, bfr[j], acc[i]);
                }
        }
        if (r0 + wv < nrows) {
#pragma unroll
            for (int i = 0; i < 6; ++i)
                *(glb_u32x2*)((uintptr_t)out + (row * IMG + col) * (CH * 2) + i * 32 + q * 8) =
                    (u32x2){cvt_pk(acc[i][0], acc[i][1]), cvt_pk(acc[i][2], acc[i][3])};
        }
    }
}

__global__ __launch_bounds__(NTHREADS, 1) void dcn_kernel(const unsigned short* x_, const unsigned short* off_,
                                                          const unsigned short* wgt_, unsigned short* out_) {
    __shared__ __attribute__((aligned(64))) unsigned char lds[LDS_BYTES];
    u32x4 xd, od, wd, odsc;
    unsigned y0, x0, rows, wv, status, stv;
    asm volatile(DCN_ASM_TEXT
                 : "={s[16:19]}"(xd), "={s[20:23]}"(od), "={s[24:27]}"(wd), "={s[28:31]}"(odsc),
                   "={s32}"(y0), "={s33}"(x0), "={s34}"(rows), "={s35}"(wv), "={s41}"(status), "={v1}"(stv)
                 : "{s[0:1]}"(__builtin_amdgcn_kernarg_segment_ptr()), "{s2}"(blockIdx.x), "{v0}"(threadIdx.x)
                 : "memory", "scc", "vcc", DCN_ASM_CLOBBERS);
    unsigned lane;
    asm volatile("v_mbcnt_lo_u32_b32 %0, -1, 0\n\tv_mbcnt_hi_u32_b32 %0, -1, %0" : "=v"(lane));
    slow_tile((const unsigned short*)(((unsigned long long)xd[1] << 32) | xd[0]), (const unsigned short*)(((unsigned long long)od[1] << 32) | od[0]),
              (const unsigned short*)(((unsigned long long)wd[1] << 32) | wd[0]), (unsigned short*)(((unsigned long long)odsc[1] << 32) | odsc[0]),
              wv, lane, y0, x0, rows, (unsigned)(uintptr_t)(__attribute__((address_space(3))) unsigned char*)lds);
}

extern "C" void kernel_launch(void* const* d_in, const int* in_sizes, int n_in,
                              void* d_out, int out_size, void* d_ws, size_t ws_size,
                              hipStream_t stream) {
    dcn_kernel<<<NWG, NTHREADS, 0, stream>>>((const unsigned short*)d_in[0], (const unsigned short*)d_in[1],
                                             (const unsigned short*)d_in[2], (unsigned short*)d_out);
}
```

```cpp
#include <hip/hip_runtime.h>
#include <stdint.h>

#define IMG 224
#define CH 96
#define ROWB (IMG * CH * 2)
#define NTHREADS 256
#define WTAP 18432
#define LDS_BYTES 163840

typedef __attribute__((ext_vector_type(2))) __bf16 bf16x2;
typedef __attribute__((ext_vector_type(8))) __bf16 bf16x8;
typedef __attribute__((ext_vector_type(4))) float f32x4;
typedef __attribute__((ext_vector_type(4))) unsigned u32x4;
typedef __attribute__((address_space(1))) u32x4 glb_u32x4;
typedef __attribute__((address_space(1))) unsigned glb_u32;
typedef __attribute__((ext_vector_type(2))) unsigned u32x2;
typedef __attribute__((address_space(1))) u32x2 glb_u32x2;
typedef __attribute__((ext_vector_type(4))) short s16x4;
typedef __attribute__((address_space(3))) s16x4 lds_s16x4;
typedef __attribute__((address_space(3))) u32x4 lds_u32x4;

#define DCN_ASM_TEXT \
        "s_load_dwordx2 s[24:25], s[0:1], 0x10\n\t" \
        "s_load_dwordx2 s[20:21], s[0:1], 0x8\n\t" \
        "s_load_dwordx2 s[16:17], s[0:1], 0x0\n\t" \
        "s_load_dwordx2 s[28:29], s[0:1], 0x18\n\t" \
        "s_getpc_b64 s[4:5]\n\t" \
        ".Lpf0%=:\n\t" \
        "v_lshrrev_b32 v3, 6, v0\n\t" \
        "s_bfe_u32 s15, s2, 0x20003\n\t" \
        "s_mov_b32 m0, 0x27f00\n\t" \
        "v_readfirstlane_b32 s35, v3\n\t" \
        "v_lshl_add_u32 v3, v0, 2, s15\n\t" \
        "s_lshr_b32 s8, s2, 3\n\t" \
        "v_lshlrev_b32 v3, 7, v3\n\t" \
        "s_and_b32 s7, s2, 7\n\t" \
        "v_min_u32 v3, .Lpf1%=-.Lpf0%=, v3\n\t" \
        "v_and_b32 v2, 63, v0\n\t" \
        "s_lshl_b32 s7, s7, 5\n\t" \
        "global_load_lds_dword v3, s[4:5]\n\t" \
        "s_add_u32 s6, s7, s8\n\t" \
        "s_mov_b32 s3, 0x1d028300\n\t" \
        "s_cmp_ge_u32 s6, 88\n\t" \
        "s_cselect_b32 s3, 0xec10258, s3\n\t" \
        "s_cmp_ge_u32 s6, 112\n\t" \
        "s_cselect_b32 s3, 0x3ec28270, s3\n\t" \
        "s_cmp_ge_u32 s6, 128\n\t" \
        "s_cselect_b32 s3, 0x38400080, s3\n\t" \
        "s_cmp_ge_u32 s6, 138\n\t" \
        "s_cselect_b32 s3, 0x1840018a, s3\n\t" \
        "s_cmp_ge_u32 s6, 150\n\t" \
        "s_cselect_b32 s3, 0x14ea8396, s3\n\t" \
        "s_cmp_ge_u32 s6, 194\n\t" \
        "s_cselect_b32 s3, 0xa8003c2, s3\n\t" \
        "s_cmp_ge_u32 s6, 216\n\t" \
        "s_cselect_b32 s3, 0x3a8280d8, s3\n\t" \
        "s_cmp_ge_u32 s6, 239\n\t" \
        "s_cselect_b32 s3, 0x3ec201ef, s3\n\t" \
        "s_and_b32 s4, s3, 0xff\n\t" \
        "s_lshr_b32 s5, s3, 27\n\t" \
        "s_sub_u32 s6, s6, s4\n\t" \
        "s_bfe_u32 s33, s3, 0x40008\n\t" \
        "s_lshr_b32 s7, s6, s5\n\t" \
        "s_bfe_u32 s34, s3, 0x50014\n\t" \
        "s_add_u32 s33, s33, s7\n\t" \
        "s_lshl_b32 s7, s7, s5\n\t" \
        "s_bfe_u32 s32, s3, 0x8000c\n\t" \
        "s_sub_u32 s6, s6, s7\n\t" \
        "s_bfe_u32 s11, s3, 0x20019\n\t" \
        "s_mul_i32 s6, s6, s34\n\t" \
        "s_lshl_b32 s33, s33, 4\n\t" \
        "s_add_u32 s32, s32, s6\n\t" \
        "s_sub_u32 s4, 224, s34\n\t" \
        "s_mov_b32 s3, 0\n\t" \
        "s_min_u32 s32, s32, s4\n\t" \
        "s_cmp_lg_u32 s11, 2\n\t" \
        "s_cbranch_scc1 .LT_entry%=\n\t" \
        "s_mul_i32 s40, s35, 0xc00\n\t" \
        "s_mov_b32 s9, 0xc0\n\t" \
        "s_mov_b32 s10, 18\n\t" \
        "s_mov_b32 s14, 0xffff5800\n\t" \
        "s_add_u32 s39, s40, 0xf840\n\t" \
        "v_lshlrev_b32 v3, 4, v2\n\t" \
        "v_and_b32 v32, 32, v2\n\t" \
        "v_xor_b32 v5, 32, v3\n\t" \
        "v_xor_b32 v4, v3, v32\n\t" \
        "s_branch .Lxv%=\n\t" \
        ".LT_entry%=:\n\t" \
        "s_lshl_b32 s4, s35, 2\n\t" \
        "s_cmp_eq_u32 s11, 3\n\t" \
        "s_cselect_b32 s5, 2, 4\n\t" \
        "s_cselect_b32 s4, s4, 0\n\t" \
        "v_lshrrev_b32 v36, s5, v2\n\t" \
        "s_cselect_b32 s5, 15, s11\n\t" \
        "v_add_u32 v19, s4, v2\n\t" \
        "s_cselect_b32 s12, 0, 2\n\t" \
        "v_and_b32 v36, s5, v36\n\t" \
        "s_cselect_b32 s13, 0, s35\n\t" \
        "v_and_b32 v19, 15, v19\n\t" \
        "s_mul_i32 s37, s32, 224\n\t" \
        "v_lshlrev_b32 v36, s12, v36\n\t" \
        "s_cmp_eq_u32 s33, 0\n\t" \
        "s_cselect_b32 s9, 0x180, 0\n\t" \
        "v_add_u32 v18, s13, v36\n\t" \
        "s_add_u32 s37, s37, s33\n\t" \
        "v_mul_u32_u24 v33, 0x1f80, v18\n\t" \
        "s_mul_i32 s37, s37, 36\n\t" \
        "s_mov_b32 s10, 19\n\t" \
        "v_mad_u32_u24 v17, v19, 36, v33\n\t" \
        "s_mov_b32 s14, 0xfffeb000\n\t" \
        ".Lxv%=:\n\t" \
        "s_mul_i32 s42, s35, 60\n\t" \
        "s_sub_u32 s43, 225, s33\n\t" \
        "v_add_u32 v32, s42, v2\n\t" \
        "s_min_u32 s43, s43, s10\n\t" \
        "v_mul_u32_u24 v33, 0x2ab, v32\n\t" \
        "s_mul_i32 s43, s43, 0xc0\n\t" \
        "v_lshrrev_b32 v33, 13, v33\n\t" \
        "s_mul_i32 s44, s33, 0xc0\n\t" \
        "v_mul_u32_u24 v34, 0xc0, v33\n\t" \
        "s_mul_i32 s36, s32, 0xa800\n\t" \
        "v_mad_i32_i24 v35, v33, -12, v32\n\t" \
        "s_mul_i32 s38, s35, 0x3c0\n\t" \
        "v_and_b32 v33, 4, v33\n\t" \
        "s_sub_u32 s44, s44, 0x180\n\t" \
        "v_min_u32 v34, s43, v34\n\t" \
        "s_add_u32 s38, s38, 64\n\t" \
        "v_lshrrev_b32 v33, 1, v33\n\t" \
        "s_add_i32 s58, s36, s14\n\t" \
        "v_max_u32 v34, s9, v34\n\t" \
        "s_mov_b32 s19, 0x20000\n\t" \
        "v_xor_b32 v35, v35, v33\n\t" \
        "s_mov_b32 s18, 0x930000\n\t" \
        "v_lshl_add_u32 v35, v35, 4, v34\n\t" \
        "s_mov_b32 s26, 0x28800\n\t" \
        "v_add_u32 v13, s44, v35\n\t" \
        "s_mov_b32 s22, 0x1b9000\n\t" \
        "s_mov_b32 s23, s19\n\t" \
        "s_mov_b32 s27, s19\n\t" \
        "s_mov_b64 s[30:31], s[18:19]\n\t" \
        "s_mov_b32 s59, 0xf80\n\t" \
        "s_mov_b32 s60, 0xa800\n\t" \
        "s_mov_b32 m0, s39\n\t" \
        "s_cmp_eq_u32 s11, 2\n\t" \
        "s_waitcnt lgkmcnt(0)\n\t" \
        "s_cbranch_scc1 .Lw0%=\n\t" \
        "buffer_load_dwordx4 v[40:43], v17, s[20:23], s37 offen \n\t" \
        "buffer_load_dwordx4 v[44:47], v17, s[20:23], s37 offen offset:16 \n\t" \
        "buffer_load_dword v48, v17, s[20:23], s37 offen offset:32 \n\t" \
        "s_branch .Lx%=\n\t" \
        ".Lw0%=:\n\t" \
        "buffer_load_dwordx4 v3, s[24:27], s40 offen offset:0  lds\n\t" \
        "buffer_load_dwordx4 v4, s[24:27], s40 offen offset:1024  lds\n\t" \
        "buffer_load_dwordx4 v5, s[24:27], s40 offen offset:2048  lds\n\t" \
        ".Lx%=:\n\t" \
        "s_mov_b32 m0, s38\n\t" \
        "s_max_i32 s57, s58, 0\n\t" \
        "buffer_load_dwordx4 v13, s[16:19], s57 offen nt lds\n\t" \
        "s_add_u32 m0, m0, s59\n\t" \
        "s_add_i32 s58, s58, s60\n\t" \
        "s_max_i32 s57, s58, 0\n\t" \
        "buffer_load_dwordx4 v13, s[16:19], s57 offen nt lds\n\t" \
        "s_cmp_eq_u32 s11, 2\n\t" \
        "s_cbranch_scc1 .LR_start%=\n\t" \
        "s_cmp_eq_u32 s11, 1\n\t" \
        "s_cbranch_scc1 .LH8_start%=\n\t" \
        "s_cmp_eq_u32 s11, 0\n\t" \
        "s_cbranch_scc1 .LH4_start%=\n\t" \
        "s_add_u32 s57, s36, 0x0\n\t" \
        "s_add_u32 m0, s38, 0x1f00\n\t" \
        "s_nop 0\n\t" \
        "buffer_load_dwordx4 v13, s[16:19], s57 offen nt lds\n\t" \
        "v_lshrrev_b32 v164, 2, v18\n\t" \
        "v_add_u32 v168, s32, v18\n\t" \
        "v_add_u32 v169, s33, v19\n\t" \
        "v_and_b32 v164, 2, v164\n\t" \
        "v_lshl_add_u32 v165, v168, 1, -3\n\t" \
        "v_lshl_add_u32 v166, v169, 1, -3\n\t" \
        "v_and_or_b32 v164, v18, 1, v164\n\t" \
        "v_bfe_u32 v167, v18, 1, 2\n\t" \
        "v_mul_u32_u24 v164, 0x900, v164\n\t" \
        "v_cmp_gt_u32 vcc, s34, v18\n\t" \
        "v_lshl_add_u32 v16, v19, 4, v164\n\t" \
        "v_lshl_add_u32 v16, v167, 2, v16\n\t" \
        "s_mov_b64 s[10:11], vcc\n\t" \
        "v_add_u32 v16, 0x1e840, v16\n\t" \
        "s_movk_i32 s42, -1152\n\t" \
        "v_cvt_f32_u32 v60, v165\n\t" \
        "v_cvt_f32_u32 v63, v166\n\t" \
        "v_add_u32 v165, 2, v165\n\t" \
        "v_add_u32 v166, 2, v166\n\t" \
        "v_cvt_f32_u32 v61, v165\n\t" \
        "v_cvt_f32_u32 v64, v166\n\t" \
        "v_add_u32 v165, 2, v165\n\t" \
        "v_add_u32 v166, 2, v166\n\t" \
        "v_cvt_f32_u32 v62, v165\n\t" \
        "v_cvt_f32_u32 v65, v166\n\t" \
        "v_mov_b32 v165, 0x7c00\n\t" \
        "v_mov_b32 v166, 0x7f800000\n\t" \
        "v_lshrrev_b32 v60, 23, v60\n\t" \
        "v_and_b32 v63, 0x7f800000, v63\n\t" \
        "v_lshl_add_u32 v60, v60, 7, s42\n\t" \
        "v_add_u32 v63, 0xfb800000, v63\n\t" \
        "v_cndmask_b32 v60, v165, v60, s[10:11]\n\t" \
        "v_cndmask_b32 v63, v166, v63, s[10:11]\n\t" \
        "v_lshrrev_b32 v61, 23, v61\n\t" \
        "v_and_b32 v64, 0x7f800000, v64\n\t" \
        "v_lshl_add_u32 v61, v61, 7, s42\n\t" \
        "v_add_u32 v64, 0xfb800000, v64\n\t" \
        "v_cndmask_b32 v61, v165, v61, s[10:11]\n\t" \
        "v_cndmask_b32 v64, v166, v64, s[10:11]\n\t" \
        "v_lshrrev_b32 v62, 23, v62\n\t" \
        "v_and_b32 v65, 0x7f800000, v65\n\t" \
        "v_lshl_add_u32 v62, v62, 7, s42\n\t" \
        "v_add_u32 v65, 0xfb800000, v65\n\t" \
        "v_cndmask_b32 v62, v165, v62, s[10:11]\n\t" \
        "v_cndmask_b32 v65, v166, v65, s[10:11]\n\t" \
        "v_add_u32 v168, -1, v168\n\t" \
        "v_add_u32 v169, -1, v169\n\t" \
        "v_lshrrev_b32 v165, 2, v16\n\t" \
        "v_lshl_or_b32 v50, v168, 16, v165\n\t" \
        "v_lshl_or_b32 v50, v169, 24, v50\n\t" \
        "v_add_u32 v51, 0x1000040, v50\n\t" \
        "v_add_u32 v52, 0x2000080, v50\n\t" \
        "v_add_u32 v53, 0x100c0, v50\n\t" \
        "v_add_u32 v54, 0x1010100, v50\n\t" \
        "v_add_u32 v55, 0x2010140, v50\n\t" \
        "v_add_u32 v56, 0x20180, v50\n\t" \
        "v_add_u32 v57, 0x10201c0, v50\n\t" \
        "v_add_u32 v58, 0x2020200, v50\n\t" \
        "v_lshlrev_b32 v66, 3, v2\n\t" \
        "v_add_u32 v66, 0x10840, v66\n\t" \
        "v_mul_u32_u24 v174, 0xf80, v18\n\t" \
        "v_mul_u32_u24 v177, 0xc0, v19\n\t" \
        "v_lshlrev_b32 v178, 3, v19\n\t" \
        "s_add_u32 s57, s36, 0xa800\n\t" \
        "s_add_u32 m0, s38, 0x2e80\n\t" \
        "s_nop 0\n\t" \
        "buffer_load_dwordx4 v13, s[16:19], s57 offen nt lds\n\t" \
        "v_add_u32 v170, 0x1080, v174\n\t" \
        "v_and_b32 v14, 48, v2\n\t" \
        "v_add_u32 v170, v170, v177\n\t" \
        "v_add_u32 v173, 8, v178\n\t" \
        "v_and_b32 v173, 32, v173\n\t" \
        "v_add_u32 v174, v170, v173\n\t" \
        "v_add_u32 v175, 0xf80, v174\n\t" \
        "v_add_u32 v176, 0x1f00, v174\n\t" \
        "ds_write2st64_b32 v16, v174, v175 offset0:0 offset1:3\n\t" \
        "ds_write_b32 v16, v176 offset:1536\n\t" \
        "v_add_u32 v170, 0xc0, v170\n\t" \
        "v_add_u32 v173, 16, v178\n\t" \
        "v_and_b32 v173, 32, v173\n\t" \
        "v_add_u32 v174, v170, v173\n\t" \
        "v_add_u32 v175, 0xf80, v174\n\t" \
        "v_add_u32 v176, 0x1f00, v174\n\t" \
        "ds_write2st64_b32 v16, v174, v175 offset0:1 offset1:4\n\t" \
        "ds_write_b32 v16, v176 offset:1792\n\t" \
        "v_add_u32 v170, 0xc0, v170\n\t" \
        "v_add_u32 v173, 24, v178\n\t" \
        "v_and_b32 v173, 32, v173\n\t" \
        "v_add_u32 v174, v170, v173\n\t" \
        "v_add_u32 v175, 0xf80, v174\n\t" \
        "v_add_u32 v176, 0x1f00, v174\n\t" \
        "ds_write2st64_b32 v16, v174, v175 offset0:2 offset1:5\n\t" \
        "ds_write_b32 v16, v176 offset:2048\n\t" \
        "s_lshl_b32 s48, s35, 10\n\t" \
        "s_and_b32 s55, s35, 1\n\t" \
        "s_add_u32 s48, s48, 0xf840\n\t" \
        "s_mul_i32 s55, s55, 0x3180\n\t" \
        "s_cmp_lt_u32 s35, 2\n\t" \
        "s_cselect_b32 s49, 0, 0x30c0\n\t" \
        "s_cselect_b32 s53, 1, -1\n\t" \
        "s_add_u32 s49, s49, s55\n\t" \
        "s_mul_i32 s53, s53, 192\n\t" \
        "s_mov_b32 s52, 0x3ffe0\n\t" \
        "s_add_u32 s49, s49, 0x20c40\n\t" \
        "v_mul_i32_i24 v30, s53, v2\n\t" \
        "v_lshlrev_b32 v170, 3, v2\n\t" \
        "v_lshlrev_b32 v171, 2, v2\n\t" \
        "v_add_u32 v30, s49, v30\n\t" \
        "v_and_b32 v170, 16, v170\n\t" \
        "v_and_b32 v171, 32, v171\n\t" \
        "v_lshl_add_u32 v31, v2, 3, s48\n\t" \
        "v_or3_b32 v30, v30, v170, v171\n\t" \
        "v_lshlrev_b32 v170, 2, v2\n\t" \
        "s_add_u32 s55, s48, 512\n\t" \
        "v_add_u32 v32, s55, v170\n\t" \
        "v_cvt_f32_u32 v34, s32\n\t" \
        "v_cvt_f32_u32 v35, s33\n\t" \
        "v_mov_b32 v36, 124\n\t" \
        "v_add_f32 v34, -2.0, v34\n\t" \
        "v_add_f32 v35, -2.0, v35\n\t" \
        "v_cvt_f32_u32 v33, 15\n\t" \
        "v_mov_b32 v37, 0x05040100\n\t" \
        "v_mov_b32 v38, 0x80008000\n\t" \
        "v_mov_b32 v39, 0x435f435f\n\t" \
        "s_mul_i32 s40, s35, 0xc00\n\t" \
        "v_lshlrev_b32 v3, 4, v2\n\t" \
        "v_and_b32 v170, 32, v2\n\t" \
        "v_xor_b32 v5, 32, v3\n\t" \
        "v_xor_b32 v4, v3, v170\n\t" \
        "s_add_u32 s39, s40, 0xf840\n\t" \
        "s_waitcnt vmcnt(4)\n\t" \
        "s_add_u32 s57, s36, 0x15000\n\t" \
        "s_add_u32 m0, s38, 0x3e00\n\t" \
        "s_nop 0\n\t" \
        "buffer_load_dwordx4 v13, s[16:19], s57 offen nt lds\n\t" \
        "s_lshl_b32 s45, s35, 10\n\t" \
        "s_mov_b32 s54, 0\n\t" \
        "s_add_u32 s45, s45, 0xf840\n\t" \
        "v_cmp_nlt_f32 s[4:5], |v40|, v63\n\t" \
        "v_cmp_nlt_f16 s[8:9], |v40|, v60\n\t" \
        "v_cmp_nlt_f32 s[6:7], |v41|, v64\n\t" \
        "v_cmp_nlt_f16 s[10:11], |v41|, v60\n\t" \
        "s_or_b64 s[12:13], s[4:5], s[8:9]\n\t" \
        "v_mbcnt_lo_u32_b32 v160, s12, 0\n\t" \
        "v_mbcnt_hi_u32_b32 v160, s13, v160\n\t" \
        "s_add_u32 s57, s36, 0x1f800\n\t" \
        "s_add_u32 m0, s38, 0x4d80\n\t" \
        "s_nop 0\n\t" \
        "buffer_load_dwordx4 v13, s[16:19], s57 offen nt lds\n\t" \
        "s_bcnt1_i32_b64 s55, s[12:13]\n\t" \
        "v_lshl_add_u32 v160, v160, 3, s45\n\t" \
        "s_lshl3_add_u32 s45, s55, s45\n\t" \
        "v_cndmask_b32 v160, v66, v160, s[12:13]\n\t" \
        "ds_write2_b32 v160, v40, v50 offset1:1\n\t" \
        "v_cmp_nlt_f32 s[4:5], |v42|, v65\n\t" \
        "v_cmp_nlt_f16 s[8:9], |v42|, v60\n\t" \
        "s_or_b64 s[12:13], s[6:7], s[10:11]\n\t" \
        "v_mbcnt_lo_u32_b32 v161, s12, 0\n\t" \
        "v_mbcnt_hi_u32_b32 v161, s13, v161\n\t" \
        "s_add_u32 s57, s36, 0x2a000\n\t" \
        "s_add_u32 m0, s38, 0x5d00\n\t" \
        "s_nop 0\n\t" \
        "buffer_load_dwordx4 v13, s[16:19], s57 offen nt lds\n\t" \
        "s_bcnt1_i32_b64 s55, s[12:13]\n\t" \
        "v_lshl_add_u32 v161, v161, 3, s45\n\t" \
        "s_lshl3_add_u32 s45, s55, s45\n\t" \
        "v_cndmask_b32 v161, v66, v161, s[12:13]\n\t" \
        "ds_write2_b32 v161, v41, v51 offset1:1\n\t" \
        "v_cmp_nlt_f32 s[6:7], |v43|, v63\n\t" \
        "v_cmp_nlt_f16 s[10:11], |v43|, v61\n\t" \
        "s_or_b64 s[12:13], s[4:5], s[8:9]\n\t" \
        "v_mbcnt_lo_u32_b32 v160, s12, 0\n\t" \
        "v_mbcnt_hi_u32_b32 v160, s13, v160\n\t" \
        "s_add_u32 s57, s36, 0x34800\n\t" \
        "s_add_u32 m0, s38, 0x6c80\n\t" \
        "s_nop 0\n\t" \
        "buffer_load_dwordx4 v13, s[16:19], s57 offen nt lds\n\t" \
        "s_bcnt1_i32_b64 s55, s[12:13]\n\t" \
        "v_lshl_add_u32 v160, v160, 3, s45\n\t" \
        "s_lshl3_add_u32 s45, s55, s45\n\t" \
        "v_cndmask_b32 v160, v66, v160, s[12:13]\n\t" \
        "ds_write2_b32 v160, v42, v52 offset1:1\n\t" \
        "v_cmp_nlt_f32 s[4:5], |v44|, v64\n\t" \
        "v_cmp_nlt_f16 s[8:9], |v44|, v61\n\t" \
        "s_or_b64 s[12:13], s[6:7], s[10:11]\n\t" \
        "v_mbcnt_lo_u32_b32 v161, s12, 0\n\t" \
        "v_mbcnt_hi_u32_b32 v161, s13, v161\n\t" \
        "s_add_u32 s57, s36, 0x3f000\n\t" \
        "s_add_u32 m0, s38, 0x7c00\n\t" \
        "s_nop 0\n\t" \
        "buffer_load_dwordx4 v13, s[16:19], s57 offen nt lds\n\t" \
        "s_bcnt1_i32_b64 s55, s[12:13]\n\t" \
        "v_lshl_add_u32 v161, v161, 3, s45\n\t" \
        "s_lshl3_add_u32 s45, s55, s45\n\t" \
        "v_cndmask_b32 v161, v66, v161, s[12:13]\n\t" \
        "ds_write2_b32 v161, v43, v53 offset1:1\n\t" \
        "v_cmp_nlt_f32 s[6:7], |v45|, v65\n\t" \
        "v_cmp_nlt_f16 s[10:11], |v45|, v61\n\t" \
        "s_or_b64 s[12:13], s[4:5], s[8:9]\n\t" \
        "v_mbcnt_lo_u32_b32 v160, s12, 0\n\t" \
        "v_mbcnt_hi_u32_b32 v160, s13, v160\n\t" \
        "s_add_u32 s57, s36, 0x49800\n\t" \
        "s_add_u32 m0, s38, 0x8b80\n\t" \
        "s_nop 0\n\t" \
        "buffer_load_dwordx4 v13, s[16:19], s57 offen nt lds\n\t" \
        "s_bcnt1_i32_b64 s55, s[12:13]\n\t" \
        "v_lshl_add_u32 v160, v160, 3, s45\n\t" \
        "s_lshl3_add_u32 s45, s55, s45\n\t" \
        "v_cndmask_b32 v160, v66, v160, s[12:13]\n\t" \
        "ds_write2_b32 v160, v44, v54 offset1:1\n\t" \
        "v_cmp_nlt_f32 s[4:5], |v46|, v63\n\t" \
        "v_cmp_nlt_f16 s[8:9], |v46|, v62\n\t" \
        "s_or_b64 s[12:13], s[6:7], s[10:11]\n\t" \
        "v_mbcnt_lo_u32_b32 v161, s12, 0\n\t" \
        "v_mbcnt_hi_u32_b32 v161, s13, v161\n\t" \
        "s_add_u32 s57, s36, 0x54000\n\t" \
        "s_add_u32 m0, s38, 0x9b00\n\t" \
        "s_nop 0\n\t" \
        "buffer_load_dwordx4 v13, s[16:19], s57 offen nt lds\n\t" \
        "s_bcnt1_i32_b64 s55, s[12:13]\n\t" \
        "v_lshl_add_u32 v161, v161, 3, s45\n\t" \
        "s_lshl3_add_u32 s45, s55, s45\n\t" \
        "v_cndmask_b32 v161, v66, v161, s[12:13]\n\t" \
        "ds_write2_b32 v161, v45, v55 offset1:1\n\t" \
        "v_cmp_nlt_f32 s[6:7], |v47|, v64\n\t" \
        "v_cmp_nlt_f16 s[10:11], |v47|, v62\n\t" \
        "s_or_b64 s[12:13], s[4:5], s[8:9]\n\t" \
        "v_mbcnt_lo_u32_b32 v160, s12, 0\n\t" \
        "v_mbcnt_hi_u32_b32 v160, s13, v160\n\t" \
        "s_add_u32 s57, s36, 0x5e800\n\t" \
        "s_add_u32 m0, s38, 0xaa80\n\t" \
        "s_nop 0\n\t" \
        "buffer_load_dwordx4 v13, s[16:19], s57 offen nt lds\n\t" \
        "s_bcnt1_i32_b64 s55, s[12:13]\n\t" \
        "v_lshl_add_u32 v160, v160, 3, s45\n\t" \
        "s_lshl3_add_u32 s45, s55, s45\n\t" \
        "v_cndmask_b32 v160, v66, v160, s[12:13]\n\t" \
        "ds_write2_b32 v160, v46, v56 offset1:1\n\t" \
        "v_cmp_nlt_f32 s[4:5], |v48|, v65\n\t" \
        "v_cmp_nlt_f16 s[8:9], |v48|, v62\n\t" \
        "s_or_b64 s[12:13], s[6:7], s[10:11]\n\t" \
        "v_mbcnt_lo_u32_b32 v161, s12, 0\n\t" \
        "v_mbcnt_hi_u32_b32 v161, s13, v161\n\t" \
        "s_add_u32 s57, s36, 0x69000\n\t" \
        "s_add_u32 m0, s38, 0xba00\n\t" \
        "s_nop 0\n\t" \
        "buffer_load_dwordx4 v13, s[16:19], s57 offen nt lds\n\t" \
        "s_bcnt1_i32_b64 s55, s[12:13]\n\t" \
        "v_lshl_add_u32 v161, v161, 3, s45\n\t" \
        "s_lshl3_add_u32 s45, s55, s45\n\t" \
        "v_cndmask_b32 v161, v66, v161, s[12:13]\n\t" \
        "ds_write2_b32 v161, v47, v57 offset1:1\n\t" \
        "s_or_b64 s[12:13], s[4:5], s[8:9]\n\t" \
        "v_mbcnt_lo_u32_b32 v160, s12, 0\n\t" \
        "v_mbcnt_hi_u32_b32 v160, s13, v160\n\t" \
        "s_bcnt1_i32_b64 s55, s[12:13]\n\t" \
        "v_lshl_add_u32 v160, v160, 3, s45\n\t" \
        "s_add_u32 s57, s36, 0x73800\n\t" \
        "s_add_u32 m0, s38, 0xc980\n\t" \
        "s_nop 0\n\t" \
        "buffer_load_dwordx4 v13, s[16:19], s57 offen nt lds\n\t" \
        "s_lshl3_add_u32 s45, s55, s45\n\t" \
        "v_cndmask_b32 v160, v66, v160, s[12:13]\n\t" \
        "ds_write2_b32 v160, v48, v58 offset1:1\n\t" \
        "s_sub_u32 s46, s45, s48\n\t" \
        "s_lshr_b32 s46, s46, 3\n\t" \
        "s_cmp_gt_u32 s46, 64\n\t" \
        "s_cselect_b32 s54, 1, 0\n\t" \
        "s_cselect_b32 s46, 64, s46\n\t" \
        "s_add_u32 s57, s36, 0x7e000\n\t" \
        "s_min_u32 s57, s57, 0x925800\n\t" \
        "s_add_u32 m0, s38, 0xd900\n\t" \
        "s_nop 0\n\t" \
        "buffer_load_dwordx4 v13, s[16:19], s57 offen nt lds\n\t" \
        "v_lshrrev_b32 v170, 4, v2\n\t" \
        "v_and_b32 v24, 15, v2\n\t" \
        "v_bfe_u32 v171, v2, 3, 1\n\t" \
        "v_lshl_add_u32 v171, v170, 2, v171\n\t" \
        "v_lshrrev_b32 v172, 1, v2\n\t" \
        "v_and_b32 v172, 2, v172\n\t" \
        "v_lshl_add_u32 v171, v171, 2, v172\n\t" \
        "s_add_u32 s55, s48, 512\n\t" \
        "v_add_u32 v20, s55, v171\n\t" \
        "v_lshl_add_u32 v21, v24, 3, s48\n\t" \
        "v_bfe_u32 v171, v2, 1, 1\n\t" \
        "v_xor_b32 v171, v171, v170\n\t" \
        "v_lshrrev_b32 v172, 2, v24\n\t" \
        "v_and_b32 v173, 2, v172\n\t" \
        "v_xor_b32 v171, v171, v173\n\t" \
        "v_and_b32 v173, 4, v2\n\t" \
        "v_lshlrev_b32 v171, 4, v171\n\t" \
        "v_lshl_add_u32 v171, v173, 1, v171\n\t" \
        "v_mul_i32_i24 v22, s53, v24\n\t" \
        "v_add3_u32 v22, v22, v171, s49\n\t" \
        "v_and_b32 v171, 3, v2\n\t" \
        "v_and_b32 v173, 1, v170\n\t" \
        "s_add_u32 s57, s36, 0x88800\n\t" \
        "s_min_u32 s57, s57, 0x925800\n\t" \
        "s_add_u32 m0, s38, 0xe880\n\t" \
        "s_nop 0\n\t" \
        "buffer_load_dwordx4 v13, s[16:19], s57 offen nt lds\n\t" \
        "v_lshlrev_b32 v173, 3, v173\n\t" \
        "v_lshl_or_b32 v23, v171, 4, v173\n\t" \
        "v_bfe_u32 v172, v2, 2, 2\n\t" \
        "v_and_b32 v171, 3, v2\n\t" \
        "v_cmp_eq_u32 vcc, v170, v172\n\t" \
        "v_lshlrev_b32 v171, v171, 1\n\t" \
        "s_nop 0\n\t" \
        "v_cndmask_b32 v171, 0, v171, vcc\n\t" \
        "v_bfe_i32 v25, v171, 0, 1\n\t" \
        "v_bfe_i32 v26, v171, 1, 1\n\t" \
        "v_bfe_i32 v27, v171, 2, 1\n\t" \
        "v_bfe_i32 v28, v171, 3, 1\n\t" \
        "ds_read_b64 v[68:69], v31\n\t" \
        "v_cmp_gt_u32 s[12:13], s46, v2\n\t" \
        "s_mov_b32 s14, 0x2\n\t" \
        "s_mov_b32 s15, 0x8\n\t" \
        "v_mov_b32 v83, 0x41980000\n\t" \
        "v_mov_b32 v29, 0x20002\n\t" \
        "s_waitcnt lgkmcnt(0)\n\t" \
        "v_lshlrev_b32 v70, 16, v68\n\t" \
        "v_and_b32 v71, 0xffff0000, v68\n\t" \
        "v_cvt_f32_ubyte2 v74, v69\n\t" \
        "v_cvt_f32_ubyte3 v75, v69\n\t" \
        "s_mov_b32 s57, s40\n\t" \
        "s_add_u32 m0, s39, 0x3000\n\t" \
        "s_nop 0\n\t" \
        "buffer_load_dwordx4 v3, s[24:27], s57 offen offset:0  lds\n\t" \
        "v_add_f32 v70, v70, v74\n\t" \
        "v_add_f32 v71, v71, v75\n\t" \
        "v_cvt_pk_bf16_f32 v68, v70, v71\n\t" \
        "v_pk_max_i16 v68, v68, 0\n\t" \
        "v_pk_min_i16 v68, v68, v39\n\t" \
        "v_and_b32 v69, 0xffff, v69\n\t" \
        "v_lshlrev_b32 v69, 2, v69\n\t" \
        "v_lshlrev_b32 v70, 16, v68\n\t" \
        "v_and_b32 v71, 0xffff0000, v68\n\t" \
        "v_cndmask_b32 v69, v66, v69, s[12:13]\n\t" \
        "ds_write_b32 v69, v30\n\t" \
        "v_fract_f32 v72, v70\n\t" \
        "v_fract_f32 v73, v71\n\t" \
        "v_sub_f32 v70, v70, v34\n\t" \
        "v_sub_f32 v71, v71, v35\n\t" \
        "v_sub_f32 v74, 1.0, v72\n\t" \
        "v_sub_f32 v75, 1.0, v73\n\t" \
        "v_med3_f32 v78, v70, 0, v33\n\t" \
        "v_cvt_pk_bf16_f32 v74, v74, v75\n\t" \
        "v_med3_f32 v79, v71, 0, v83\n\t" \
        "v_cmp_neq_f32 s[4:5], v78, v70\n\t" \
        "v_and_b32 v75, 0xffff0000, v74\n\t" \
        "buffer_load_dwordx4 v4, s[24:27], s57 offen offset:1024  lds\n\t" \
        "v_cmp_neq_f32 s[6:7], v79, v71\n\t" \
        "v_lshlrev_b32 v74, 16, v74\n\t" \
        "v_cvt_u32_f32 v78, v78\n\t" \
        "v_cvt_u32_f32 v79, v79\n\t" \
        "s_or_b64 s[4:5], s[4:5], s[6:7]\n\t" \
        "v_mul_f32 v76, v74, v75\n\t" \
        "v_mul_f32 v74, v74, v73\n\t" \
        "s_and_b64 s[4:5], s[4:5], s[12:13]\n\t" \
        "s_cselect_b32 s54, 1, s54\n\t" \
        "v_mul_f32 v77, v72, v75\n\t" \
        "v_mul_f32 v75, v72, v73\n\t" \
        "v_cmp_neq_f32 s[4:5], 0, v73\n\t" \
        "v_cmp_neq_f32 s[6:7], 0, v72\n\t" \
        "v_cvt_pk_bf16_f32 v76, v76, v74\n\t" \
        "v_cvt_pk_bf16_f32 v77, v77, v75\n\t" \
        "v_mul_u32_u24 v80, 124, v78\n\t" \
        "v_add_u32 v83, 1, v79\n\t" \
        "v_mad_u32_u24 v80, v79, 6, v80\n\t" \
        "v_bfe_u32 v84, v79, 2, 1\n\t" \
        "v_bfe_u32 v83, v83, 2, 1\n\t" \
        "s_and_b64 s[50:51], s[4:5], s[6:7]\n\t" \
        "v_add3_u32 v81, v80, v84, s14\n\t" \
        "buffer_load_dwordx4 v5, s[24:27], s57 offen offset:2048  lds\n\t" \
        "v_add3_u32 v82, v80, v83, s15\n\t" \
        "s_and_b64 s[50:51], s[50:51], s[12:13]\n\t" \
        "v_cndmask_b32 v83, 0, v36, s[6:7]\n\t" \
        "v_perm_b32 v74, v77, v76, v37\n\t" \
        "v_add_u32 v83, v81, v83\n\t" \
        "v_cndmask_b32 v68, v74, v76, s[4:5]\n\t" \
        "v_cndmask_b32 v83, v83, v82, s[4:5]\n\t" \
        "v_cndmask_b32 v75, 0, v38, s[50:51]\n\t" \
        "v_lshl_or_b32 v81, v83, 16, v81\n\t" \
        "v_cndmask_b32 v69, 0, v77, s[50:51]\n\t" \
        "v_or_b32 v81, v81, v75\n\t" \
        "ds_write_b64 v31, v[68:69]\n\t" \
        "v_cndmask_b32 v81, v29, v81, s[12:13]\n\t" \
        "ds_write_b32 v32, v81\n\t" \
        "s_lshl_b32 s55, s35, 2\n\t" \
        "s_cmp_lg_u32 s54, 0\n\t" \
        "v_mov_b32 v162, s55\n\t" \
        "s_cselect_b32 s55, 0x10000, s46\n\t" \
        "v_mov_b32 v163, s55\n\t" \
        "ds_write_b32 v162, v163\n\t" \
        "s_add_u32 s57, s40, 0x3000\n\t" \
        "s_add_u32 m0, s39, 0x6000\n\t" \
        "s_nop 0\n\t" \
        "buffer_load_dwordx4 v3, s[24:27], s57 offen offset:0  lds\n\t" \
        "s_and_b32 s61, s35, 1\n\t" \
        "v_lshrrev_b32 v170, 4, v2\n\t" \
        "v_and_b32 v171, 15, v2\n\t" \
        "v_and_b32 v172, 16, v2\n\t" \
        "s_lshr_b32 s62, s35, 1\n\t" \
        "v_lshlrev_b32 v172, 1, v172\n\t" \
        "v_bfe_u32 v173, v2, 2, 2\n\t" \
        "v_mul_u32_u24 v175, 0xc0, v171\n\t" \
        "v_lshl_or_b32 v173, v170, 3, v173\n\t" \
        "v_and_b32 v174, 3, v2\n\t" \
        "v_mul_u32_u24 v173, 0xc0, v173\n\t" \
        "s_lshl_b32 s63, s61, 7\n\t" \
        "v_lshl_add_u32 v173, v174, 3, v173\n\t" \
        "s_add_u32 s63, s63, 0xf840\n\t" \
        "v_add3_u32 v6, v173, v172, s63\n\t" \
        "buffer_load_dwordx4 v4, s[24:27], s57 offen offset:1024  lds\n\t" \
        "s_lshl_b32 s63, s61, 5\n\t" \
        "v_xor_b32 v174, s63, v172\n\t" \
        "v_xor_b32 v7, 32, v6\n\t" \
        "v_add_u32 v173, 0xf880, v173\n\t" \
        "s_lshl_b32 s63, s61, 7\n\t" \
        "v_add_u32 v8, v173, v174\n\t" \
        "v_lshrrev_b32 v173, 1, v2\n\t" \
        "v_and_b32 v173, 16, v173\n\t" \
        "v_add_u32 v175, v175, v173\n\t" \
        "v_bfe_u32 v173, v2, 3, 1\n\t" \
        "v_add3_u32 v9, v175, v172, s63\n\t" \
        "v_cmp_eq_u32 vcc, s61, v173\n\t" \
        "v_add3_u32 v10, v175, v174, 64\n\t" \
        "v_bfrev_b32 v174, 1\n\t" \
        "s_lshl_b32 s63, s35, 9\n\t" \
        "buffer_load_dwordx4 v5, s[24:27], s57 offen offset:2048  lds\n\t" \
        "v_cndmask_b32 v10, v174, v10, vcc\n\t" \
        "s_add_u32 s63, s63, 0xf840\n\t" \
        "v_lshl_add_u32 v11, v2, 3, s63\n\t" \
        "s_lshl_b32 s63, s61, 10\n\t" \
        "s_sub_u32 s63, 0x200, s63\n\t" \
        "v_add_u32 v12, s63, v11\n\t" \
        "s_add_u32 s64, s32, s62\n\t" \
        "s_mul_i32 s63, s33, 0xc0\n\t" \
        "s_mul_i32 s64, s64, 0xa800\n\t" \
        "s_add_u32 s64, s64, s63\n\t" \
        "v_and_b32 v170, 15, v2\n\t" \
        "s_mul_i32 s62, s62, 2304\n\t" \
        "s_add_u32 s62, s62, 124992\n\t" \
        "v_lshl_add_u32 v15, v170, 4, s62\n\t" \
        "ds_read_u16 v120, v20\n\t" \
        "ds_read_u16 v121, v20 offset:8\n\t" \
        "ds_read_b32 v122, v21\n\t" \
        "v_mov_b32 v156, 0\n\t" \
        "s_mov_b32 s47, 0\n\t" \
        "s_waitcnt lgkmcnt(0)\n\t" \
        "v_lshlrev_b32 v116, 5, v120\n\t" \
        "v_bitop3_b32 v116, v116, s52, v23 bitop3:0x6a\n\t" \
        "v_xor_b32 v117, 8, v116\n\t" \
        "v_lshlrev_b32 v118, 5, v121\n\t" \
        "v_bitop3_b32 v118, v118, s52, v23 bitop3:0x6a\n\t" \
        "v_xor_b32 v119, 8, v118\n\t" \
        "v_and_b32 v112, v122, v25\n\t" \
        "v_and_b32 v113, v122, v26\n\t" \
        "v_and_b32 v114, v122, v27\n\t" \
        "v_and_b32 v115, v122, v28\n\t" \
        "ds_read_u16 v120, v20 offset:64\n\t" \
        "ds_read_u16 v121, v20 offset:72\n\t" \
        "ds_read_b32 v122, v21 offset:128\n\t" \
        "s_waitcnt vmcnt(6) lgkmcnt(0)\n\t" \
        "s_barrier\n\t" \
        "ds_read_b128 v[152:155], v156\n\t" \
        "ds_read_b64_tr_b16 v[64:65], v116 offset:0\n\t" \
        "ds_read_b64_tr_b16 v[66:67], v118 offset:0\n\t" \
        "ds_read_b64_tr_b16 v[68:69], v117 offset:0\n\t" \
        "ds_read_b64_tr_b16 v[70:71], v119 offset:0\n\t" \
        "ds_read_b64_tr_b16 v[72:73], v116 offset:64\n\t" \
        "ds_read_b64_tr_b16 v[74:75], v118 offset:64\n\t" \
        "ds_read_b64_tr_b16 v[76:77], v117 offset:64\n\t" \
        "ds_read_b64_tr_b16 v[78:79], v119 offset:64\n\t" \
        "ds_read_b64_tr_b16 v[80:81], v116 offset:128\n\t" \
        "ds_read_b64_tr_b16 v[82:83], v118 offset:128\n\t" \
        "ds_read_b64_tr_b16 v[84:85], v117 offset:128\n\t" \
        "ds_read_b64_tr_b16 v[86:87], v119 offset:128\n\t" \
        "s_add_u32 s57, s40, 0x6000\n\t" \
        "s_add_u32 m0, s39, 0x9000\n\t" \
        "s_nop 0\n\t" \
        "buffer_load_dwordx4 v3, s[24:27], s57 offen offset:0  lds\n\t" \
        "buffer_load_dwordx4 v4, s[24:27], s57 offen offset:1024  lds\n\t" \
        "buffer_load_dwordx4 v5, s[24:27], s57 offen offset:2048  lds\n\t" \
        "s_waitcnt lgkmcnt(12)\n\t" \
        "v_add_u32 v152, v152, v154\n\t" \
        "v_add_u32 v153, v153, v155\n\t" \
        "v_max_u32 v152, v152, v153\n\t" \
        "s_nop 0\n\t" \
        "v_readfirstlane_b32 s55, v152\n\t" \
        "s_nop 0\n\t" \
        "s_cmp_gt_u32 s55, 66\n\t" \
        "s_cbranch_scc1 .Lfail%=\n\t" \
        "s_cmp_gt_u32 s46, 16\n\t" \
        "s_cbranch_scc1 .LTeprod%=\n\t" \
        ".LTelast%=:\n\t" \
        "s_waitcnt lgkmcnt(10)\n\t" \
        "v_mfma_f32_16x16x32_bf16 v[88:91], v[64:67], v[112:115], 0\n\t" \
        "s_waitcnt lgkmcnt(8)\n\t" \
        "v_mfma_f32_16x16x32_bf16 v[92:95], v[68:71], v[112:115], 0\n\t" \
        "s_waitcnt lgkmcnt(6)\n\t" \
        "v_mfma_f32_16x16x32_bf16 v[96:99], v[72:75], v[112:115], 0\n\t" \
        "s_waitcnt lgkmcnt(4)\n\t" \
        "v_mfma_f32_16x16x32_bf16 v[100:103], v[76:79], v[112:115], 0\n\t" \
        "s_waitcnt lgkmcnt(2)\n\t" \
        "v_mfma_f32_16x16x32_bf16 v[104:107], v[80:83], v[112:115], 0\n\t" \
        "s_waitcnt lgkmcnt(0)\n\t" \
        "v_mfma_f32_16x16x32_bf16 v[108:111], v[84:87], v[112:115], 0\n\t" \
        "s_and_b32 s55, s50, 0xffff\n\t" \
        "s_cbranch_scc1 .LTetwo2%=\n\t" \
        ".LTeback2%=:\n\t" \
        "s_sub_u32 s55, s46, s47\n\t" \
        "v_xor_b32 v156, 8, v22\n\t" \
        "v_cmp_gt_i32 vcc, s55, v24\n\t" \
        "s_mov_b64 exec, vcc\n\t" \
        "v_cvt_pk_bf16_f32 v88, v88, v89\n\t" \
        "v_cvt_pk_bf16_f32 v89, v90, v91\n\t" \
        "ds_write_b64 v22, v[88:89] offset:0\n\t" \
        "v_cvt_pk_bf16_f32 v92, v92, v93\n\t" \
        "v_cvt_pk_bf16_f32 v93, v94, v95\n\t" \
        "ds_write_b64 v156, v[92:93] offset:0\n\t" \
        "v_cvt_pk_bf16_f32 v96, v96, v97\n\t" \
        "v_cvt_pk_bf16_f32 v97, v98, v99\n\t" \
        "ds_write_b64 v22, v[96:97] offset:64\n\t" \
        "v_cvt_pk_bf16_f32 v100, v100, v101\n\t" \
        "v_cvt_pk_bf16_f32 v101, v102, v103\n\t" \
        "ds_write_b64 v156, v[100:101] offset:64\n\t" \
        "v_cvt_pk_bf16_f32 v104, v104, v105\n\t" \
        "v_cvt_pk_bf16_f32 v105, v106, v107\n\t" \
        "ds_write_b64 v22, v[104:105] offset:128\n\t" \
        "v_cvt_pk_bf16_f32 v108, v108, v109\n\t" \
        "v_cvt_pk_bf16_f32 v109, v110, v111\n\t" \
        "ds_write_b64 v156, v[108:109] offset:128\n\t" \
        "s_mov_b64 exec, -1\n\t" \
        "s_waitcnt vmcnt(6) lgkmcnt(0)\n\t" \
        "s_barrier\n\t" \
        "ds_read_b128 v[148:151], v15 offset:0\n\t" \
        "s_waitcnt lgkmcnt(0)\n\t" \
        "v_xor_b32 v148, v148, v14\n\t" \
        "ds_read_b128 v[116:119], v148 offset:0\n\t" \
        "ds_read_b64_tr_b16 a[0:1], v6 offset:12288\n\t" \
        "ds_read_b64_tr_b16 a[2:3], v6 offset:13056\n\t" \
        "v_xor_b32 v149, v149, v14\n\t" \
        "ds_read_b64_tr_b16 a[4:5], v7 offset:12288\n\t" \
        "ds_read_b64_tr_b16 a[6:7], v7 offset:13056\n\t" \
        "ds_read_b64_tr_b16 a[8:9], v8 offset:12288\n\t" \
        "ds_read_b64_tr_b16 a[10:11], v8 offset:13056\n\t" \
        "ds_read_b128 v[120:123], v149 offset:0\n\t" \
        "v_xor_b32 v150, v150, v14\n\t" \
        "ds_read_b128 v[124:127], v150 offset:0\n\t" \
        "v_xor_b32 v151, v151, v14\n\t" \
        "ds_read_b128 v[128:131], v151 offset:0\n\t" \
        "s_add_u32 s57, s40, 0x9000\n\t" \
        "s_add_u32 m0, s39, 0xc000\n\t" \
        "s_waitcnt lgkmcnt(7)\n\t" \
        "v_mfma_f32_16x16x32_bf16 v[32:35], a[0:3], v[116:119], 0\n\t" \
        "buffer_load_dwordx4 v3, s[24:27], s57 offen offset:0  lds\n\t" \
        "s_waitcnt lgkmcnt(5)\n\t" \
        "v_mfma_f32_16x16x32_bf16 v[36:39], a[4:7], v[116:119], 0\n\t" \
        "ds_read_b128 v[132:135], v148 offset:64\n\t" \
        "s_waitcnt lgkmcnt(4)\n\t" \
        "v_mfma_f32_16x16x32_bf16 v[40:43], a[8:11], v[116:119], 0\n\t" \
        "ds_read_b64_tr_b16 a[12:13], v6 offset:18432\n\t" \
        "s_waitcnt lgkmcnt(4)\n\t" \
        "v_mfma_f32_16x16x32_bf16 v[44:47], a[0:3], v[120:123], 0\n\t" \
        "ds_read_b64_tr_b16 a[14:15], v6 offset:19200\n\t" \
        "v_mfma_f32_16x16x32_bf16 v[48:51], a[4:7], v[120:123], 0\n\t" \
        "buffer_load_dwordx4 v4, s[24:27], s57 offen offset:1024  lds\n\t" \
        "v_mfma_f32_16x16x32_bf16 v[52:55], a[8:11], v[120:123], 0\n\t" \
        "ds_read_b64_tr_b16 a[16:17], v7 offset:18432\n\t" \
        "s_waitcnt lgkmcnt(5)\n\t" \
        "v_mfma_f32_16x16x32_bf16 v[56:59], a[0:3], v[124:127], 0\n\t" \
        "ds_read_b64_tr_b16 a[18:19], v7 offset:19200\n\t" \
        "ds_read_b64_tr_b16 a[20:21], v8 offset:18432\n\t" \
        "v_mfma_f32_16x16x32_bf16 v[60:63], a[4:7], v[124:127], 0\n\t" \
        "ds_read_b64_tr_b16 a[22:23], v8 offset:19200\n\t" \
        "v_mfma_f32_16x16x32_bf16 v[64:67], a[8:11], v[124:127], 0\n\t" \
        "ds_read_b128 v[136:139], v149 offset:64\n\t" \
        "s_waitcnt lgkmcnt(7)\n\t" \
        "v_mfma_f32_16x16x32_bf16 v[68:71], a[0:3], v[128:131], 0\n\t" \
        "buffer_load_dwordx4 v5, s[24:27], s57 offen offset:2048  lds\n\t" \
        "v_mfma_f32_16x16x32_bf16 v[72:75], a[4:7], v[128:131], 0\n\t" \
        "ds_read_b128 v[140:143], v150 offset:64\n\t" \
        "s_add_u32 s57, s40, 0xc000\n\t" \
        "s_mov_b32 m0, s39\n\t" \
        "v_mfma_f32_16x16x32_bf16 v[76:79], a[8:11], v[128:131], 0\n\t" \
        "s_waitcnt vmcnt(6) lgkmcnt(2)\n\t" \
        "s_barrier\n\t" \
        "ds_read_b128 v[144:147], v151 offset:64\n\t" \
        "v_mfma_f32_16x16x32_bf16 v[32:35], a[12:15], v[132:135], v[32:35]\n\t" \
        "buffer_load_dwordx4 v3, s[24:27], s57 offen offset:0  lds\n\t" \
        "v_mfma_f32_16x16x32_bf16 v[36:39], a[16:19], v[132:135], v[36:39]\n\t" \
        "ds_read_b128 v[116:119], v148 offset:128\n\t" \
        "v_mfma_f32_16x16x32_bf16 v[40:43], a[20:23], v[132:135], v[40:43]\n\t" \
        "ds_read_b128 v[152:155], v15 offset:256\n\t" \
        "s_waitcnt lgkmcnt(4)\n\t" \
        "v_mfma_f32_16x16x32_bf16 v[44:47], a[12:15], v[136:139], v[44:47]\n\t" \
        "ds_read_b64_tr_b16 a[24:25], v6 offset:24576\n\t" \
        "v_mfma_f32_16x16x32_bf16 v[48:51], a[16:19], v[136:139], v[48:51]\n\t" \
        "buffer_load_dwordx4 v4, s[24:27], s57 offen offset:1024  lds\n\t" \
        "v_mfma_f32_16x16x32_bf16 v[52:55], a[20:23], v[136:139], v[52:55]\n\t" \
        "ds_read_b64_tr_b16 a[26:27], v6 offset:25344\n\t" \
        "ds_read_b64_tr_b16 a[28:29], v7 offset:24576\n\t" \
        "s_waitcnt lgkmcnt(6)\n\t" \
        "v_mfma_f32_16x16x32_bf16 v[56:59], a[12:15], v[140:143], v[56:59]\n\t" \
        "ds_read_b64_tr_b16 a[30:31], v7 offset:25344\n\t" \
        "ds_read_b64_tr_b16 a[32:33], v8 offset:24576\n\t" \
        "v_mfma_f32_16x16x32_bf16 v[60:63], a[16:19], v[140:143], v[60:63]\n\t" \
        "ds_read_b64_tr_b16 a[34:35], v8 offset:25344\n\t" \
        "v_mfma_f32_16x16x32_bf16 v[64:67], a[20:23], v[140:143], v[64:67]\n\t" \
        "ds_read_b128 v[120:123], v149 offset:128\n\t" \
        "s_waitcnt lgkmcnt(7)\n\t" \
        "v_xor_b32 v152, v152, v14\n\t" \
        "v_mfma_f32_16x16x32_bf16 v[68:71], a[12:15], v[144:147], v[68:71]\n\t" \
        "buffer_load_dwordx4 v5, s[24:27], s57 offen offset:2048  lds\n\t" \
        "v_mfma_f32_16x16x32_bf16 v[72:75], a[16:19], v[144:147], v[72:75]\n\t" \
        "ds_read_b128 v[124:127], v150 offset:128\n\t" \
        "s_add_u32 s57, s40, 0xf000\n\t" \
        "s_add_u32 m0, s39, 0x3000\n\t" \
        "v_xor_b32 v153, v153, v14\n\t" \
        "v_mfma_f32_16x16x32_bf16 v[76:79], a[20:23], v[144:147], v[76:79]\n\t" \
        "ds_read_b128 v[128:131], v151 offset:128\n\t" \
        "v_xor_b32 v154, v154, v14\n\t" \
        "s_waitcnt lgkmcnt(7)\n\t" \
        "v_mfma_f32_16x16x32_bf16 v[32:35], a[24:27], v[116:119], v[32:35]\n\t" \
        "buffer_load_dwordx4 v3, s[24:27], s57 offen offset:0  lds\n\t" \
        "s_waitcnt lgkmcnt(5)\n\t" \
        "v_mfma_f32_16x16x32_bf16 v[36:39], a[28:31], v[116:119], v[36:39]\n\t" \
        "ds_read_b128 v[132:135], v152 offset:0\n\t" \
        "v_xor_b32 v155, v155, v14\n\t" \
        "s_waitcnt lgkmcnt(4)\n\t" \
        "v_mfma_f32_16x16x32_bf16 v[40:43], a[32:35], v[116:119], v[40:43]\n\t" \
        "ds_read_b64_tr_b16 a[36:37], v6 offset:30720\n\t" \
        "s_waitcnt lgkmcnt(4)\n\t" \
        "v_mfma_f32_16x16x32_bf16 v[44:47], a[24:27], v[120:123], v[44:47]\n\t" \
        "ds_read_b64_tr_b16 a[38:39], v6 offset:31488\n\t" \
        "v_mfma_f32_16x16x32_bf16 v[48:51], a[28:31], v[120:123], v[48:51]\n\t" \
        "buffer_load_dwordx4 v4, s[24:27], s57 offen offset:1024  lds\n\t" \
        "v_mfma_f32_16x16x32_bf16 v[52:55], a[32:35], v[120:123], v[52:55]\n\t" \
        "ds_read_b64_tr_b16 a[40:41], v7 offset:30720\n\t" \
        "s_waitcnt lgkmcnt(5)\n\t" \
        "v_mfma_f32_16x16x32_bf16 v[56:59], a[24:27], v[124:127], v[56:59]\n\t" \
        "ds_read_b64_tr_b16 a[42:43], v7 offset:31488\n\t" \
        "ds_read_b64_tr_b16 a[44:45], v8 offset:30720\n\t" \
        "v_mfma_f32_16x16x32_bf16 v[60:63], a[28:31], v[124:127], v[60:63]\n\t" \
        "ds_read_b64_tr_b16 a[46:47], v8 offset:31488\n\t" \
        "v_mfma_f32_16x16x32_bf16 v[64:67], a[32:35], v[124:127], v[64:67]\n\t" \
        "ds_read_b128 v[136:139], v153 offset:0\n\t" \
        "s_waitcnt lgkmcnt(7)\n\t" \
        "v_mfma_f32_16x16x32_bf16 v[68:71], a[24:27], v[128:131], v[68:71]\n\t" \
        "buffer_load_dwordx4 v5, s[24:27], s57 offen offset:2048  lds\n\t" \
        "v_mfma_f32_16x16x32_bf16 v[72:75], a[28:31], v[128:131], v[72:75]\n\t" \
        "ds_read_b128 v[140:143], v154 offset:0\n\t" \
        "v_mfma_f32_16x16x32_bf16 v[76:79], a[32:35], v[128:131], v[76:79]\n\t" \
        "s_waitcnt vmcnt(9) lgkmcnt(2)\n\t" \
        "s_barrier\n\t" \
        "ds_read_b128 v[144:147], v155 offset:0\n\t" \
        "s_add_u32 s57, s40, 0x12000\n\t" \
        "s_add_u32 m0, s39, 0x6000\n\t" \
        "v_mfma_f32_16x16x32_bf16 v[32:35], a[36:39], v[132:135], v[32:35]\n\t" \
        "ds_read_b128 v[116:119], v152 offset:64\n\t" \
        "v_mfma_f32_16x16x32_bf16 v[36:39], a[40:43], v[132:135], v[36:39]\n\t" \
        "buffer_load_dwordx4 v3, s[24:27], s57 offen offset:0  lds\n\t" \
        "v_mfma_f32_16x16x32_bf16 v[40:43], a[44:47], v[132:135], v[40:43]\n\t" \
        "ds_read_b64_tr_b16 a[48:49], v6 offset:36864\n\t" \
        "s_waitcnt lgkmcnt(4)\n\t" \
        "v_mfma_f32_16x16x32_bf16 v[44:47], a[36:39], v[136:139], v[44:47]\n\t" \
        "ds_read_b64_tr_b16 a[50:51], v6 offset:37632\n\t" \
        "v_mfma_f32_16x16x32_bf16 v[48:51], a[40:43], v[136:139], v[48:51]\n\t" \
        "ds_read_b64_tr_b16 a[52:53], v7 offset:36864\n\t" \
        "v_mfma_f32_16x16x32_bf16 v[52:55], a[44:47], v[136:139], v[52:55]\n\t" \
        "buffer_load_dwordx4 v4, s[24:27], s57 offen offset:1024  lds\n\t" \
        "s_waitcnt lgkmcnt(5)\n\t" \
        "v_mfma_f32_16x16x32_bf16 v[56:59], a[36:39], v[140:143], v[56:59]\n\t" \
        "ds_read_b64_tr_b16 a[54:55], v7 offset:37632\n\t" \
        "ds_read_b64_tr_b16 a[56:57], v8 offset:36864\n\t" \
        "v_mfma_f32_16x16x32_bf16 v[60:63], a[40:43], v[140:143], v[60:63]\n\t" \
        "ds_read_b64_tr_b16 a[58:59], v8 offset:37632\n\t" \
        "v_mfma_f32_16x16x32_bf16 v[64:67], a[44:47], v[140:143], v[64:67]\n\t" \
        "ds_read_b128 v[120:123], v153 offset:64\n\t" \
        "s_waitcnt lgkmcnt(7)\n\t" \
        "v_mfma_f32_16x16x32_bf16 v[68:71], a[36:39], v[144:147], v[68:71]\n\t" \
        "buffer_load_dwordx4 v5, s[24:27], s57 offen offset:2048  lds\n\t" \
        "v_mfma_f32_16x16x32_bf16 v[72:75], a[40:43], v[144:147], v[72:75]\n\t" \
        "ds_read_b128 v[124:127], v154 offset:64\n\t" \
        "v_mfma_f32_16x16x32_bf16 v[76:79], a[44:47], v[144:147], v[76:79]\n\t" \
        "ds_read_b128 v[128:131], v155 offset:64\n\t" \
        "s_waitcnt lgkmcnt(6)\n\t" \
        "v_mfma_f32_16x16x32_bf16 v[32:35], a[48:51], v[116:119], v[32:35]\n\t" \
        "ds_read_b128 v[132:135], v152 offset:128\n\t" \
        "s_waitcnt lgkmcnt(6)\n\t" \
        "v_mfma_f32_16x16x32_bf16 v[36:39], a[52:55], v[116:119], v[36:39]\n\t" \
        "ds_read_b128 v[156:159], v15 offset:512\n\t" \
        "s_waitcnt lgkmcnt(5)\n\t" \
        "v_mfma_f32_16x16x32_bf16 v[40:43], a[56:59], v[116:119], v[40:43]\n\t" \
        "ds_read_b64_tr_b16 a[60:61], v6 offset:43008\n\t" \
        "s_waitcnt lgkmcnt(5)\n\t" \
        "v_mfma_f32_16x16x32_bf16 v[44:47], a[48:51], v[120:123], v[44:47]\n\t" \
        "ds_read_b64_tr_b16 a[62:63], v6 offset:43776\n\t" \
        "v_mfma_f32_16x16x32_bf16 v[48:51], a[52:55], v[120:123], v[48:51]\n\t" \
        "ds_read_b64_tr_b16 a[64:65], v7 offset:43008\n\t" \
        "v_mfma_f32_16x16x32_bf16 v[52:55], a[56:59], v[120:123], v[52:55]\n\t" \
        "ds_read_b64_tr_b16 a[66:67], v7 offset:43776\n\t" \
        "s_waitcnt lgkmcnt(7)\n\t" \
        "v_mfma_f32_16x16x32_bf16 v[56:59], a[48:51], v[124:127], v[56:59]\n\t" \
        "ds_read_b64_tr_b16 a[68:69], v8 offset:43008\n\t" \
        "v_mfma_f32_16x16x32_bf16 v[60:63], a[52:55], v[124:127], v[60:63]\n\t" \
        "ds_read_b64_tr_b16 a[70:71], v8 offset:43776\n\t" \
        "s_waitcnt lgkmcnt(6)\n\t" \
        "v_xor_b32 v156, v156, v14\n\t" \
        "v_mfma_f32_16x16x32_bf16 v[64:67], a[56:59], v[124:127], v[64:67]\n\t" \
        "ds_read_b128 v[136:139], v153 offset:128\n\t" \
        "v_xor_b32 v157, v157, v14\n\t" \
        "v_mfma_f32_16x16x32_bf16 v[68:71], a[48:51], v[128:131], v[68:71]\n\t" \
        "ds_read_b128 v[140:143], v154 offset:128\n\t" \
        "v_xor_b32 v158, v158, v14\n\t" \
        "v_mfma_f32_16x16x32_bf16 v[72:75], a[52:55], v[128:131], v[72:75]\n\t" \
        "ds_read_b128 v[144:147], v155 offset:128\n\t" \
        "v_xor_b32 v159, v159, v14\n\t" \
        "v_mfma_f32_16x16x32_bf16 v[76:79], a[56:59], v[128:131], v[76:79]\n\t" \
        "s_waitcnt vmcnt(9) lgkmcnt(3)\n\t" \
        "s_barrier\n\t" \
        "ds_read_b128 v[116:119], v156 offset:0\n\t" \
        "s_add_u32 s57, s40, 0x15000\n\t" \
        "s_add_u32 m0, s39, 0x9000\n\t" \
        "v_mfma_f32_16x16x32_bf16 v[32:35], a[60:63], v[132:135], v[32:35]\n\t" \
        "ds_read_b64_tr_b16 a[72:73], v6 offset:49152\n\t" \
        "v_mfma_f32_16x16x32_bf16 v[36:39], a[64:67], v[132:135], v[36:39]\n\t" \
        "buffer_load_dwordx4 v3, s[24:27], s57 offen offset:0  lds\n\t" \
        "v_mfma_f32_16x16x32_bf16 v[40:43], a[68:71], v[132:135], v[40:43]\n\t" \
        "ds_read_b64_tr_b16 a[74:75], v6 offset:49920\n\t" \
        "s_waitcnt lgkmcnt(5)\n\t" \
        "v_mfma_f32_16x16x32_bf16 v[44:47], a[60:63], v[136:139], v[44:47]\n\t" \
        "ds_read_b64_tr_b16 a[76:77], v7 offset:49152\n\t" \
        "v_mfma_f32_16x16x32_bf16 v[48:51], a[64:67], v[136:139], v[48:51]\n\t" \
        "ds_read_b64_tr_b16 a[78:79], v7 offset:49920\n\t" \
        "v_mfma_f32_16x16x32_bf16 v[52:55], a[68:71], v[136:139], v[52:55]\n\t" \
        "ds_read_b64_tr_b16 a[80:81], v8 offset:49152\n\t" \
        "s_waitcnt lgkmcnt(6)\n\t" \
        "v_mfma_f32_16x16x32_bf16 v[56:59], a[60:63], v[140:143], v[56:59]\n\t" \
        "ds_read_b64_tr_b16 a[82:83], v8 offset:49920\n\t" \
        "v_mfma_f32_16x16x32_bf16 v[60:63], a[64:67], v[140:143], v[60:63]\n\t" \
        "buffer_load_dwordx4 v4, s[24:27], s57 offen offset:1024  lds\n\t" \
        "v_mfma_f32_16x16x32_bf16 v[64:67], a[68:71], v[140:143], v[64:67]\n\t" \
        "ds_read_b128 v[120:123], v157 offset:0\n\t" \
        "v_mfma_f32_16x16x32_bf16 v[68:71], a[60:63], v[144:147], v[68:71]\n\t" \
        "ds_read_b128 v[124:127], v158 offset:0\n\t" \
        "v_mfma_f32_16x16x32_bf16 v[72:75], a[64:67], v[144:147], v[72:75]\n\t" \
        "ds_read_b128 v[128:131], v159 offset:0\n\t" \
        "v_mfma_f32_16x16x32_bf16 v[76:79], a[68:71], v[144:147], v[76:79]\n\t" \
        "ds_read_b128 v[132:135], v156 offset:64\n\t" \
        "s_waitcnt lgkmcnt(6)\n\t" \
        "v_mfma_f32_16x16x32_bf16 v[32:35], a[72:75], v[116:119], v[32:35]\n\t" \
        "ds_read_b64_tr_b16 a[84:85], v6 offset:55296\n\t" \
        "v_mfma_f32_16x16x32_bf16 v[36:39], a[76:79], v[116:119], v[36:39]\n\t" \
        "buffer_load_dwordx4 v5, s[24:27], s57 offen offset:2048  lds\n\t" \
        "s_waitcnt lgkmcnt(5)\n\t" \
        "v_mfma_f32_16x16x32_bf16 v[40:43], a[80:83], v[116:119], v[40:43]\n\t" \
        "ds_read_b64_tr_b16 a[86:87], v6 offset:56064\n\t" \
        "s_waitcnt lgkmcnt(5)\n\t" \
        "v_mfma_f32_16x16x32_bf16 v[44:47], a[72:75], v[120:123], v[44:47]\n\t" \
        "ds_read_b64_tr_b16 a[88:89], v7 offset:55296\n\t" \
        "v_mfma_f32_16x16x32_bf16 v[48:51], a[76:79], v[120:123], v[48:51]\n\t" \
        "ds_read_b64_tr_b16 a[90:91], v7 offset:56064\n\t" \
        "v_mfma_f32_16x16x32_bf16 v[52:55], a[80:83], v[120:123], v[52:55]\n\t" \
        "ds_read_b64_tr_b16 a[92:93], v8 offset:55296\n\t" \
        "s_waitcnt lgkmcnt(6)\n\t" \
        "v_mfma_f32_16x16x32_bf16 v[56:59], a[72:75], v[124:127], v[56:59]\n\t" \
        "ds_read_b64_tr_b16 a[94:95], v8 offset:56064\n\t" \
        "v_mfma_f32_16x16x32_bf16 v[60:63], a[76:79], v[124:127], v[60:63]\n\t" \
        "ds_read_b128 v[136:139], v157 offset:64\n\t" \
        "v_mfma_f32_16x16x32_bf16 v[64:67], a[80:83], v[124:127], v[64:67]\n\t" \
        "ds_read_b128 v[140:143], v158 offset:64\n\t" \
        "v_mfma_f32_16x16x32_bf16 v[68:71], a[72:75], v[128:131], v[68:71]\n\t" \
        "ds_read_b128 v[144:147], v159 offset:64\n\t" \
        "v_mfma_f32_16x16x32_bf16 v[72:75], a[76:79], v[128:131], v[72:75]\n\t" \
        "ds_read_b128 v[116:119], v156 offset:128\n\t" \
        "v_mfma_f32_16x16x32_bf16 v[76:79], a[80:83], v[128:131], v[76:79]\n\t" \
        "s_waitcnt vmcnt(9) lgkmcnt(4)\n\t" \
        "s_barrier\n\t" \
        "ds_read_b128 v[160:163], v15 offset:768\n\t" \
        "s_add_u32 s57, s40, 0x18000\n\t" \
        "s_add_u32 m0, s39, 0xc000\n\t" \
        "v_mfma_f32_16x16x32_bf16 v[32:35], a[84:87], v[132:135], v[32:35]\n\t" \
        "ds_read_b64_tr_b16 a[96:97], v6 offset:0\n\t" \
        "v_mfma_f32_16x16x32_bf16 v[36:39], a[88:91], v[132:135], v[36:39]\n\t" \
        "buffer_load_dwordx4 v3, s[24:27], s57 offen offset:0  lds\n\t" \
        "v_mfma_f32_16x16x32_bf16 v[40:43], a[92:95], v[132:135], v[40:43]\n\t" \
        "ds_read_b64_tr_b16 a[98:99], v6 offset:768\n\t" \
        "s_waitcnt lgkmcnt(6)\n\t" \
        "v_mfma_f32_16x16x32_bf16 v[44:47], a[84:87], v[136:139], v[44:47]\n\t" \
        "ds_read_b64_tr_b16 a[100:101], v7 offset:0\n\t" \
        "v_mfma_f32_16x16x32_bf16 v[48:51], a[88:91], v[136:139], v[48:51]\n\t" \
        "ds_read_b64_tr_b16 a[102:103], v7 offset:768\n\t" \
        "v_mfma_f32_16x16x32_bf16 v[52:55], a[92:95], v[136:139], v[52:55]\n\t" \
        "ds_read_b64_tr_b16 a[104:105], v8 offset:0\n\t" \
        "s_waitcnt lgkmcnt(5)\n\t" \
        "v_xor_b32 v160, v160, v14\n\t" \
        "v_mfma_f32_16x16x32_bf16 v[56:59], a[84:87], v[140:143], v[56:59]\n\t" \
        "ds_read_b64_tr_b16 a[106:107], v8 offset:768\n\t" \
        "v_xor_b32 v161, v161, v14\n\t" \
        "v_mfma_f32_16x16x32_bf16 v[60:63], a[88:91], v[140:143], v[60:63]\n\t" \
        "buffer_load_dwordx4 v4, s[24:27], s57 offen offset:1024  lds\n\t" \
        "v_mfma_f32_16x16x32_bf16 v[64:67], a[92:95], v[140:143], v[64:67]\n\t" \
        "ds_read_b128 v[120:123], v157 offset:128\n\t" \
        "v_xor_b32 v162, v162, v14\n\t" \
        "v_mfma_f32_16x16x32_bf16 v[68:71], a[84:87], v[144:147], v[68:71]\n\t" \
        "ds_read_b128 v[124:127], v158 offset:128\n\t" \
        "v_xor_b32 v163, v163, v14\n\t" \
        "v_mfma_f32_16x16x32_bf16 v[72:75], a[88:91], v[144:147], v[72:75]\n\t" \
        "ds_read_b128 v[128:131], v159 offset:128\n\t" \
        "v_mfma_f32_16x16x32_bf16 v[76:79], a[92:95], v[144:147], v[76:79]\n\t" \
        "ds_read_b128 v[132:135], v160 offset:0\n\t" \
        "s_waitcnt lgkmcnt(6)\n\t" \
        "v_mfma_f32_16x16x32_bf16 v[32:35], a[96:99], v[116:119], v[32:35]\n\t" \
        "ds_read_b64_tr_b16 a[108:109], v6 offset:6144\n\t" \
        "v_mfma_f32_16x16x32_bf16 v[36:39], a[100:103], v[116:119], v[36:39]\n\t" \
        "buffer_load_dwordx4 v5, s[24:27], s57 offen offset:2048  lds\n\t" \
        "s_waitcnt lgkmcnt(5)\n\t" \
        "v_mfma_f32_16x16x32_bf16 v[40:43], a[104:107], v[116:119], v[40:43]\n\t" \
        "ds_read_b64_tr_b16 a[110:111], v6 offset:6912\n\t" \
        "s_waitcnt lgkmcnt(5)\n\t" \
        "v_mfma_f32_16x16x32_bf16 v[44:47], a[96:99], v[120:123], v[44:47]\n\t" \
        "ds_read_b64_tr_b16 a[112:113], v7 offset:6144\n\t" \
        "v_mfma_f32_16x16x32_bf16 v[48:51], a[100:103], v[120:123], v[48:51]\n\t" \
        "ds_read_b64_tr_b16 a[114:115], v7 offset:6912\n\t" \
        "v_mfma_f32_16x16x32_bf16 v[52:55], a[104:107], v[120:123], v[52:55]\n\t" \
        "ds_read_b64_tr_b16 a[116:117], v8 offset:6144\n\t" \
        "s_waitcnt lgkmcnt(6)\n\t" \
        "v_mfma_f32_16x16x32_bf16 v[56:59], a[96:99], v[124:127], v[56:59]\n\t" \
        "ds_read_b64_tr_b16 a[118:119], v8 offset:6912\n\t" \
        "v_mfma_f32_16x16x32_bf16 v[60:63], a[100:103], v[124:127], v[60:63]\n\t" \
        "ds_read_b128 v[136:139], v161 offset:0\n\t" \
        "v_mfma_f32_16x16x32_bf16 v[64:67], a[104:107], v[124:127], v[64:67]\n\t" \
        "ds_read_b128 v[140:143], v162 offset:0\n\t" \
        "v_mfma_f32_16x16x32_bf16 v[68:71], a[96:99], v[128:131], v[68:71]\n\t" \
        "ds_read_b128 v[144:147], v163 offset:0\n\t" \
        "v_mfma_f32_16x16x32_bf16 v[72:75], a[100:103], v[128:131], v[72:75]\n\t" \
        "ds_read_b128 v[116:119], v160 offset:64\n\t" \
        "v_mfma_f32_16x16x32_bf16 v[76:79], a[104:107], v[128:131], v[76:79]\n\t" \
        "s_waitcnt vmcnt(9) lgkmcnt(4)\n\t" \
        "s_barrier\n\t" \
        "ds_read_b64_tr_b16 a[120:121], v6 offset:12288\n\t" \
        "s_add_u32 s57, s40, 0x1b000\n\t" \
        "s_mov_b32 m0, s39\n\t" \
        "v_mfma_f32_16x16x32_bf16 v[32:35], a[108:111], v[132:135], v[32:35]\n\t" \
        "ds_read_b64_tr_b16 a[122:123], v6 offset:13056\n\t" \
        "v_mfma_f32_16x16x32_bf16 v[36:39], a[112:115], v[132:135], v[36:39]\n\t" \
        "buffer_load_dwordx4 v3, s[24:27], s57 offen offset:0  lds\n\t" \
        "v_mfma_f32_16x16x32_bf16 v[40:43], a[116:119], v[132:135], v[40:43]\n\t" \
        "ds_read_b64_tr_b16 a[124:125], v7 offset:12288\n\t" \
        "s_waitcnt lgkmcnt(6)\n\t" \
        "v_mfma_f32_16x16x32_bf16 v[44:47], a[108:111], v[136:139], v[44:47]\n\t" \
        "ds_read_b64_tr_b16 a[126:127], v7 offset:13056\n\t" \
        "v_mfma_f32_16x16x32_bf16 v[48:51], a[112:115], v[136:139], v[48:51]\n\t" \
        "ds_read_b64_tr_b16 a[128:129], v8 offset:12288\n\t" \
        "v_mfma_f32_16x16x32_bf16 v[52:55], a[116:119], v[136:139], v[52:55]\n\t" \
        "ds_read_b64_tr_b16 a[130:131], v8 offset:13056\n\t" \
        "s_waitcnt lgkmcnt(6)\n\t" \
        "v_mfma_f32_16x16x32_bf16 v[56:59], a[108:111], v[140:143], v[56:59]\n\t" \
        "ds_read_b128 v[120:123], v161 offset:64\n\t" \
        "v_mfma_f32_16x16x32_bf16 v[60:63], a[112:115], v[140:143], v[60:63]\n\t" \
        "buffer_load_dwordx4 v4, s[24:27], s57 offen offset:1024  lds\n\t" \
        "v_mfma_f32_16x16x32_bf16 v[64:67], a[116:119], v[140:143], v[64:67]\n\t" \
        "ds_read_b128 v[124:127], v162 offset:64\n\t" \
        "v_mfma_f32_16x16x32_bf16 v[68:71], a[108:111], v[144:147], v[68:71]\n\t" \
        "ds_read_b128 v[128:131], v163 offset:64\n\t" \
        "v_mfma_f32_16x16x32_bf16 v[72:75], a[112:115], v[144:147], v[72:75]\n\t" \
        "ds_read_b128 v[132:135], v160 offset:128\n\t" \
        "v_mfma_f32_16x16x32_bf16 v[76:79], a[116:119], v[144:147], v[76:79]\n\t" \
        "ds_read_b128 v[164:167], v15 offset:1024\n\t" \
        "s_waitcnt lgkmcnt(6)\n\t" \
        "v_mfma_f32_16x16x32_bf16 v[32:35], a[120:123], v[116:119], v[32:35]\n\t" \
        "ds_read_b64_tr_b16 a[132:133], v6 offset:18432\n\t" \
        "v_mfma_f32_16x16x32_bf16 v[36:39], a[124:127], v[116:119], v[36:39]\n\t" \
        "buffer_load_dwordx4 v5, s[24:27], s57 offen offset:2048  lds\n\t" \
        "s_waitcnt lgkmcnt(5)\n\t" \
        "v_mfma_f32_16x16x32_bf16 v[40:43], a[128:131], v[116:119], v[40:43]\n\t" \
        "ds_read_b64_tr_b16 a[134:135], v6 offset:19200\n\t" \
        "v_mfma_f32_16x16x32_bf16 v[44:47], a[120:123], v[120:123], v[44:47]\n\t" \
        "ds_read_b64_tr_b16 a[136:137], v7 offset:18432\n\t" \
        "v_mfma_f32_16x16x32_bf16 v[48:51], a[124:127], v[120:123], v[48:51]\n\t" \
        "ds_read_b64_tr_b16 a[138:139], v7 offset:19200\n\t" \
        "v_mfma_f32_16x16x32_bf16 v[52:55], a[128:131], v[120:123], v[52:55]\n\t" \
        "ds_read_b64_tr_b16 a[140:141], v8 offset:18432\n\t" \
        "s_waitcnt lgkmcnt(5)\n\t" \
        "v_xor_b32 v164, v164, v14\n\t" \
        "v_mfma_f32_16x16x32_bf16 v[56:59], a[120:123], v[124:127], v[56:59]\n\t" \
        "ds_read_b64_tr_b16 a[142:143], v8 offset:19200\n\t" \
        "v_xor_b32 v165, v165, v14\n\t" \
        "v_mfma_f32_16x16x32_bf16 v[60:63], a[124:127], v[124:127], v[60:63]\n\t" \
        "ds_read_b128 v[136:139], v161 offset:128\n\t" \
        "v_xor_b32 v166, v166, v14\n\t" \
        "v_mfma_f32_16x16x32_bf16 v[64:67], a[128:131], v[124:127], v[64:67]\n\t" \
        "ds_read_b128 v[140:143], v162 offset:128\n\t" \
        "v_xor_b32 v167, v167, v14\n\t" \
        "v_mfma_f32_16x16x32_bf16 v[68:71], a[120:123], v[128:131], v[68:71]\n\t" \
        "ds_read_b128 v[144:147], v163 offset:128\n\t" \
        "v_mfma_f32_16x16x32_bf16 v[72:75], a[124:127], v[128:131], v[72:75]\n\t" \
        "ds_read_b128 v[116:119], v164 offset:0\n\t" \
        "v_mfma_f32_16x16x32_bf16 v[76:79], a[128:131], v[128:131], v[76:79]\n\t" \
        "s_waitcnt vmcnt(9) lgkmcnt(4)\n\t" \
        "s_barrier\n\t" \
        "ds_read_b64_tr_b16 a[144:145], v6 offset:24576\n\t" \
        "s_add_u32 s57, s40, 0x1e000\n\t" \
        "s_add_u32 m0, s39, 0x3000\n\t" \
        "v_mfma_f32_16x16x32_bf16 v[32:35], a[132:135], v[132:135], v[32:35]\n\t" \
        "ds_read_b64_tr_b16 a[146:147], v6 offset:25344\n\t" \
        "v_mfma_f32_16x16x32_bf16 v[36:39], a[136:139], v[132:135], v[36:39]\n\t" \
        "buffer_load_dwordx4 v3, s[24:27], s57 offen offset:0  lds\n\t" \
        "v_mfma_f32_16x16x32_bf16 v[40:43], a[140:143], v[132:135], v[40:43]\n\t" \
        "ds_read_b64_tr_b16 a[148:149], v7 offset:24576\n\t" \
        "s_waitcnt lgkmcnt(6)\n\t" \
        "v_mfma_f32_16x16x32_bf16 v[44:47], a[132:135], v[136:139], v[44:47]\n\t" \
        "ds_read_b64_tr_b16 a[150:151], v7 offset:25344\n\t" \
        "v_mfma_f32_16x16x32_bf16 v[48:51], a[136:139], v[136:139], v[48:51]\n\t" \
        "ds_read_b64_tr_b16 a[152:153], v8 offset:24576\n\t" \
        "v_mfma_f32_16x16x32_bf16 v[52:55], a[140:143], v[136:139], v[52:55]\n\t" \
        "ds_read_b64_tr_b16 a[154:155], v8 offset:25344\n\t" \
        "s_waitcnt lgkmcnt(6)\n\t" \
        "v_mfma_f32_16x16x32_bf16 v[56:59], a[132:135], v[140:143], v[56:59]\n\t" \
        "ds_read_b128 v[120:123], v165 offset:0\n\t" \
        "v_mfma_f32_16x16x32_bf16 v[60:63], a[136:139], v[140:143], v[60:63]\n\t" \
        "buffer_load_dwordx4 v4, s[24:27], s57 offen offset:1024  lds\n\t" \
        "v_mfma_f32_16x16x32_bf16 v[64:67], a[140:143], v[140:143], v[64:67]\n\t" \
        "ds_read_b128 v[124:127], v166 offset:0\n\t" \
        "v_mfma_f32_16x16x32_bf16 v[68:71], a[132:135], v[144:147], v[68:71]\n\t" \
        "ds_read_b128 v[128:131], v167 offset:0\n\t" \
        "v_mfma_f32_16x16x32_bf16 v[72:75], a[136:139], v[144:147], v[72:75]\n\t" \
        "ds_read_b128 v[132:135], v164 offset:64\n\t" \
        "v_mfma_f32_16x16x32_bf16 v[76:79], a[140:143], v[144:147], v[76:79]\n\t" \
        "ds_read_b64_tr_b16 a[156:157], v6 offset:30720\n\t" \
        "s_waitcnt lgkmcnt(6)\n\t" \
        "v_mfma_f32_16x16x32_bf16 v[32:35], a[144:147], v[116:119], v[32:35]\n\t" \
        "ds_read_b64_tr_b16 a[158:159], v6 offset:31488\n\t" \
        "v_mfma_f32_16x16x32_bf16 v[36:39], a[148:151], v[116:119], v[36:39]\n\t" \
        "buffer_load_dwordx4 v5, s[24:27], s57 offen offset:2048  lds\n\t" \
        "s_waitcnt lgkmcnt(5)\n\t" \
        "v_mfma_f32_16x16x32_bf16 v[40:43], a[152:155], v[116:119], v[40:43]\n\t" \
        "ds_read_b64_tr_b16 a[160:161], v7 offset:30720\n\t" \
        "v_mfma_f32_16x16x32_bf16 v[44:47], a[144:147], v[120:123], v[44:47]\n\t" \
        "ds_read_b64_tr_b16 a[162:163], v7 offset:31488\n\t" \
        "v_mfma_f32_16x16x32_bf16 v[48:51], a[148:151], v[120:123], v[48:51]\n\t" \
        "ds_read_b64_tr_b16 a[164:165], v8 offset:30720\n\t" \
        "v_mfma_f32_16x16x32_bf16 v[52:55], a[152:155], v[120:123], v[52:55]\n\t" \
        "ds_read_b64_tr_b16 a[166:167], v8 offset:31488\n\t" \
        "s_waitcnt lgkmcnt(6)\n\t" \
        "v_mfma_f32_16x16x32_bf16 v[56:59], a[144:147], v[124:127], v[56:59]\n\t" \
        "ds_read_b128 v[136:139], v165 offset:64\n\t" \
        "v_mfma_f32_16x16x32_bf16 v[60:63], a[148:151], v[124:127], v[60:63]\n\t" \
        "ds_read_b128 v[140:143], v166 offset:64\n\t" \
        "v_mfma_f32_16x16x32_bf16 v[64:67], a[152:155], v[124:127], v[64:67]\n\t" \
        "ds_read_b128 v[144:147], v167 offset:64\n\t" \
        "v_mfma_f32_16x16x32_bf16 v[68:71], a[144:147], v[128:131], v[68:71]\n\t" \
        "ds_read_b128 v[116:119], v164 offset:128\n\t" \
        "v_mfma_f32_16x16x32_bf16 v[72:75], a[148:151], v[128:131], v[72:75]\n\t" \
        "ds_read_b128 v[168:171], v15 offset:1280\n\t" \
        "v_mfma_f32_16x16x32_bf16 v[76:79], a[152:155], v[128:131], v[76:79]\n\t" \
        "s_waitcnt vmcnt(9) lgkmcnt(5)\n\t" \
        "s_barrier\n\t" \
        "ds_read_b64_tr_b16 a[168:169], v6 offset:36864\n\t" \
        "s_add_u32 s57, s40, 0x21000\n\t" \
        "s_add_u32 m0, s39, 0x6000\n\t" \
        "v_mfma_f32_16x16x32_bf16 v[32:35], a[156:159], v[132:135], v[32:35]\n\t" \
        "ds_read_b64_tr_b16 a[170:171], v6 offset:37632\n\t" \
        "v_mfma_f32_16x16x32_bf16 v[36:39], a[160:163], v[132:135], v[36:39]\n\t" \
        "buffer_load_dwordx4 v3, s[24:27], s57 offen offset:0  lds\n\t" \
        "v_mfma_f32_16x16x32_bf16 v[40:43], a[164:167], v[132:135], v[40:43]\n\t" \
        "ds_read_b64_tr_b16 a[172:173], v7 offset:36864\n\t" \
        "s_waitcnt lgkmcnt(6)\n\t" \
        "v_mfma_f32_16x16x32_bf16 v[44:47], a[156:159], v[136:139], v[44:47]\n\t" \
        "ds_read_b64_tr_b16 a[174:175], v7 offset:37632\n\t" \
        "v_mfma_f32_16x16x32_bf16 v[48:51], a[160:163], v[136:139], v[48:51]\n\t" \
        "ds_read_b64_tr_b16 a[176:177], v8 offset:36864\n\t" \
        "s_waitcnt lgkmcnt(5)\n\t" \
        "v_xor_b32 v168, v168, v14\n\t" \
        "v_mfma_f32_16x16x32_bf16 v[52:55], a[164:167], v[136:139], v[52:55]\n\t" \
        "ds_read_b64_tr_b16 a[178:179], v8 offset:37632\n\t" \
        "v_xor_b32 v169, v169, v14\n\t" \
        "v_mfma_f32_16x16x32_bf16 v[56:59], a[156:159], v[140:143], v[56:59]\n\t" \
        "ds_read_b128 v[120:123], v165 offset:128\n\t" \
        "v_xor_b32 v170, v170, v14\n\t" \
        "v_mfma_f32_16x16x32_bf16 v[60:63], a[160:163], v[140:143], v[60:63]\n\t" \
        "buffer_load_dwordx4 v4, s[24:27], s57 offen offset:1024  lds\n\t" \
        "v_mfma_f32_16x16x32_bf16 v[64:67], a[164:167], v[140:143], v[64:67]\n\t" \
        "ds_read_b128 v[124:127], v166 offset:128\n\t" \
        "v_xor_b32 v171, v171, v14\n\t" \
        "v_mfma_f32_16x16x32_bf16 v[68:71], a[156:159], v[144:147], v[68:71]\n\t" \
        "ds_read_b128 v[128:131], v167 offset:128\n\t" \
        "v_mfma_f32_16x16x32_bf16 v[72:75], a[160:163], v[144:147], v[72:75]\n\t" \
        "ds_read_b128 v[132:135], v168 offset:0\n\t" \
        "v_mfma_f32_16x16x32_bf16 v[76:79], a[164:167], v[144:147], v[76:79]\n\t" \
        "ds_read_b64_tr_b16 a[180:181], v6 offset:43008\n\t" \
        "s_waitcnt lgkmcnt(6)\n\t" \
        "v_mfma_f32_16x16x32_bf16 v[32:35], a[168:171], v[116:119], v[32:35]\n\t" \
        "ds_read_b64_tr_b16 a[182:183], v6 offset:43776\n\t" \
        "v_mfma_f32_16x16x32_bf16 v[36:39], a[172:175], v[116:119], v[36:39]\n\t" \
        "buffer_load_dwordx4 v5, s[24:27], s57 offen offset:2048  lds\n\t" \
        "s_waitcnt lgkmcnt(5)\n\t" \
        "v_mfma_f32_16x16x32_bf16 v[40:43], a[176:179], v[116:119], v[40:43]\n\t" \
        "ds_read_b64_tr_b16 a[184:185], v7 offset:43008\n\t" \
        "v_mfma_f32_16x16x32_bf16 v[44:47], a[168:171], v[120:123], v[44:47]\n\t" \
        "ds_read_b64_tr_b16 a[186:187], v7 offset:43776\n\t" \
        "v_mfma_f32_16x16x32_bf16 v[48:51], a[172:175], v[120:123], v[48:51]\n\t" \
        "ds_read_b64_tr_b16 a[188:189], v8 offset:43008\n\t" \
        "v_mfma_f32_16x16x32_bf16 v[52:55], a[176:179], v[120:123], v[52:55]\n\t" \
        "ds_read_b64_tr_b16 a[190:191], v8 offset:43776\n\t" \
        "s_waitcnt lgkmcnt(6)\n\t" \
        "v_mfma_f32_16x16x32_bf16 v[56:59], a[168:171], v[124:127], v[56:59]\n\t" \
        "ds_read_b128 v[136:139], v169 offset:0\n\t" \
        "v_mfma_f32_16x16x32_bf16 v[60:63], a[172:175], v[124:127], v[60:63]\n\t" \
        "ds_read_b128 v[140:143], v170 offset:0\n\t" \
        "v_mfma_f32_16x16x32_bf16 v[64:67], a[176:179], v[124:127], v[64:67]\n\t" \
        "ds_read_b128 v[144:147], v171 offset:0\n\t" \
        "v_mfma_f32_16x16x32_bf16 v[68:71], a[168:171], v[128:131], v[68:71]\n\t" \
        "ds_read_b128 v[116:119], v168 offset:64\n\t" \
        "v_mfma_f32_16x16x32_bf16 v[72:75], a[172:175], v[128:131], v[72:75]\n\t" \
        "ds_read_b128 v[120:123], v169 offset:64\n\t" \
        "v_mfma_f32_16x16x32_bf16 v[76:79], a[176:179], v[128:131], v[76:79]\n\t" \
        "s_waitcnt vmcnt(9) lgkmcnt(5)\n\t" \
        "s_barrier\n\t" \
        "ds_read_b64_tr_b16 a[192:193], v6 offset:49152\n\t" \
        "s_add_u32 s57, s40, 0x24000\n\t" \
        "s_add_u32 m0, s39, 0x9000\n\t" \
        "v_mfma_f32_16x16x32_bf16 v[32:35], a[180:183], v[132:135], v[32:35]\n\t" \
        "ds_read_b64_tr_b16 a[194:195], v6 offset:49920\n\t" \
        "v_mfma_f32_16x16x32_bf16 v[36:39], a[184:187], v[132:135], v[36:39]\n\t" \
        "buffer_load_dwordx4 v3, s[24:27], s57 offen offset:0  lds\n\t" \
        "v_mfma_f32_16x16x32_bf16 v[40:43], a[188:191], v[132:135], v[40:43]\n\t" \
        "ds_read_b64_tr_b16 a[196:197], v7 offset:49152\n\t" \
        "s_waitcnt lgkmcnt(6)\n\t" \
        "v_mfma_f32_16x16x32_bf16 v[44:47], a[180:183], v[136:139], v[44:47]\n\t" \
        "ds_read_b64_tr_b16 a[198:199], v7 offset:49920\n\t" \
        "v_mfma_f32_16x16x32_bf16 v[48:51], a[184:187], v[136:139], v[48:51]\n\t" \
        "ds_read_b64_tr_b16 a[200:201], v8 offset:49152\n\t" \
        "v_mfma_f32_16x16x32_bf16 v[52:55], a[188:191], v[136:139], v[52:55]\n\t" \
        "ds_read_b64_tr_b16 a[202:203], v8 offset:49920\n\t" \
        "v_mfma_f32_16x16x32_bf16 v[56:59], a[180:183], v[140:143], v[56:59]\n\t" \
        "ds_read_b128 v[124:127], v170 offset:64\n\t" \
        "v_mfma_f32_16x16x32_bf16 v[60:63], a[184:187], v[140:143], v[60:63]\n\t" \
        "buffer_load_dwordx4 v4, s[24:27], s57 offen offset:1024  lds\n\t" \
        "v_mfma_f32_16x16x32_bf16 v[64:67], a[188:191], v[140:143], v[64:67]\n\t" \
        "ds_read_b128 v[128:131], v171 offset:64\n\t" \
        "s_waitcnt lgkmcnt(6)\n\t" \
        "v_mfma_f32_16x16x32_bf16 v[68:71], a[180:183], v[144:147], v[68:71]\n\t" \
        "ds_read_b128 v[132:135], v168 offset:128\n\t" \
        "v_mfma_f32_16x16x32_bf16 v[72:75], a[184:187], v[144:147], v[72:75]\n\t" \
        "ds_read_b128 v[172:175], v15 offset:1536\n\t" \
        "v_mfma_f32_16x16x32_bf16 v[76:79], a[188:191], v[144:147], v[76:79]\n\t" \
        "ds_read_b64_tr_b16 a[204:205], v6 offset:55296\n\t" \
        "v_mfma_f32_16x16x32_bf16 v[32:35], a[192:195], v[116:119], v[32:35]\n\t" \
        "ds_read_b64_tr_b16 a[206:207], v6 offset:56064\n\t" \
        "s_waitcnt lgkmcnt(6)\n\t" \
        "v_mfma_f32_16x16x32_bf16 v[36:39], a[196:199], v[116:119], v[36:39]\n\t" \
        "buffer_load_dwordx4 v5, s[24:27], s57 offen offset:2048  lds\n\t" \
        "v_mfma_f32_16x16x32_bf16 v[40:43], a[200:203], v[116:119], v[40:43]\n\t" \
        "ds_read_b64_tr_b16 a[208:209], v7 offset:55296\n\t" \
        "v_mfma_f32_16x16x32_bf16 v[44:47], a[192:195], v[120:123], v[44:47]\n\t" \
        "ds_read_b64_tr_b16 a[210:211], v7 offset:56064\n\t" \
        "v_mfma_f32_16x16x32_bf16 v[48:51], a[196:199], v[120:123], v[48:51]\n\t" \
        "ds_read_b64_tr_b16 a[212:213], v8 offset:55296\n\t" \
        "s_waitcnt lgkmcnt(5)\n\t" \
        "v_xor_b32 v172, v172, v14\n\t" \
        "v_mfma_f32_16x16x32_bf16 v[52:55], a[200:203], v[120:123], v[52:55]\n\t" \
        "ds_read_b64_tr_b16 a[214:215], v8 offset:56064\n\t" \
        "v_xor_b32 v173, v173, v14\n\t" \
        "v_mfma_f32_16x16x32_bf16 v[56:59], a[192:195], v[124:127], v[56:59]\n\t" \
        "ds_read_b128 v[136:139], v169 offset:128\n\t" \
        "v_xor_b32 v174, v174, v14\n\t" \
        "v_mfma_f32_16x16x32_bf16 v[60:63], a[196:199], v[124:127], v[60:63]\n\t" \
        "ds_read_b128 v[140:143], v170 offset:128\n\t" \
        "v_xor_b32 v175, v175, v14\n\t" \
        "v_mfma_f32_16x16x32_bf16 v[64:67], a[200:203], v[124:127], v[64:67]\n\t" \
        "ds_read_b128 v[144:147], v171 offset:128\n\t" \
        "v_mfma_f32_16x16x32_bf16 v[68:71], a[192:195], v[128:131], v[68:71]\n\t" \
        "ds_read_b128 v[116:119], v172 offset:0\n\t" \
        "v_mfma_f32_16x16x32_bf16 v[72:75], a[196:199], v[128:131], v[72:75]\n\t" \
        "ds_read_b128 v[120:123], v173 offset:0\n\t" \
        "v_mfma_f32_16x16x32_bf16 v[76:79], a[200:203], v[128:131], v[76:79]\n\t" \
        "s_waitcnt vmcnt(9) lgkmcnt(5)\n\t" \
        "s_barrier\n\t" \
        "ds_read_b64_tr_b16 a[216:217], v6 offset:0\n\t" \
        "s_add_u32 s57, s40, 0x27000\n\t" \
        "s_add_u32 m0, s39, 0xc000\n\t" \
        "v_mfma_f32_16x16x32_bf16 v[32:35], a[204:207], v[132:135], v[32:35]\n\t" \
        "ds_read_b64_tr_b16 a[218:219], v6 offset:768\n\t" \
        "v_mfma_f32_16x16x32_bf16 v[36:39], a[208:211], v[132:135], v[36:39]\n\t" \
        "buffer_load_dwordx4 v3, s[24:27], s57 offen offset:0  lds\n\t" \
        "v_mfma_f32_16x16x32_bf16 v[40:43], a[212:215], v[132:135], v[40:43]\n\t" \
        "ds_read_b64_tr_b16 a[220:221], v7 offset:0\n\t" \
        "s_waitcnt lgkmcnt(6)\n\t" \
        "v_mfma_f32_16x16x32_bf16 v[44:47], a[204:207], v[136:139], v[44:47]\n\t" \
        "ds_read_b64_tr_b16 a[222:223], v7 offset:768\n\t" \
        "v_mfma_f32_16x16x32_bf16 v[48:51], a[208:211], v[136:139], v[48:51]\n\t" \
        "ds_read_b64_tr_b16 a[224:225], v8 offset:0\n\t" \
        "v_mfma_f32_16x16x32_bf16 v[52:55], a[212:215], v[136:139], v[52:55]\n\t" \
        "ds_read_b64_tr_b16 a[226:227], v8 offset:768\n\t" \
        "v_mfma_f32_16x16x32_bf16 v[56:59], a[204:207], v[140:143], v[56:59]\n\t" \
        "ds_read_b128 v[124:127], v174 offset:0\n\t" \
        "v_mfma_f32_16x16x32_bf16 v[60:63], a[208:211], v[140:143], v[60:63]\n\t" \
        "buffer_load_dwordx4 v4, s[24:27], s57 offen offset:1024  lds\n\t" \
        "v_mfma_f32_16x16x32_bf16 v[64:67], a[212:215], v[140:143], v[64:67]\n\t" \
        "ds_read_b128 v[128:131], v175 offset:0\n\t" \
        "s_waitcnt lgkmcnt(6)\n\t" \
        "v_mfma_f32_16x16x32_bf16 v[68:71], a[204:207], v[144:147], v[68:71]\n\t" \
        "ds_read_b128 v[132:135], v172 offset:64\n\t" \
        "v_mfma_f32_16x16x32_bf16 v[72:75], a[208:211], v[144:147], v[72:75]\n\t" \
        "ds_read_b64_tr_b16 a[228:229], v6 offset:6144\n\t" \
        "v_mfma_f32_16x16x32_bf16 v[76:79], a[212:215], v[144:147], v[76:79]\n\t" \
        "ds_read_b64_tr_b16 a[230:231], v6 offset:6912\n\t" \
        "v_mfma_f32_16x16x32_bf16 v[32:35], a[216:219], v[116:119], v[32:35]\n\t" \
        "ds_read_b64_tr_b16 a[232:233], v7 offset:6144\n\t" \
        "s_waitcnt lgkmcnt(6)\n\t" \
        "v_mfma_f32_16x16x32_bf16 v[36:39], a[220:223], v[116:119], v[36:39]\n\t" \
        "buffer_load_dwordx4 v5, s[24:27], s57 offen offset:2048  lds\n\t" \
        "v_mfma_f32_16x16x32_bf16 v[40:43], a[224:227], v[116:119], v[40:43]\n\t" \
        "ds_read_b64_tr_b16 a[234:235], v7 offset:6912\n\t" \
        "v_mfma_f32_16x16x32_bf16 v[44:47], a[216:219], v[120:123], v[44:47]\n\t" \
        "ds_read_b64_tr_b16 a[236:237], v8 offset:6144\n\t" \
        "v_mfma_f32_16x16x32_bf16 v[48:51], a[220:223], v[120:123], v[48:51]\n\t" \
        "ds_read_b64_tr_b16 a[238:239], v8 offset:6912\n\t" \
        "v_mfma_f32_16x16x32_bf16 v[52:55], a[224:227], v[120:123], v[52:55]\n\t" \
        "ds_read_b128 v[136:139], v173 offset:64\n\t" \
        "s_waitcnt lgkmcnt(6)\n\t" \
        "v_mfma_f32_16x16x32_bf16 v[56:59], a[216:219], v[124:127], v[56:59]\n\t" \
        "ds_read_b128 v[140:143], v174 offset:64\n\t" \
        "v_mfma_f32_16x16x32_bf16 v[60:63], a[220:223], v[124:127], v[60:63]\n\t" \
        "ds_read_b128 v[144:147], v175 offset:64\n\t" \
        "v_mfma_f32_16x16x32_bf16 v[64:67], a[224:227], v[124:127], v[64:67]\n\t" \
        "ds_read_b128 v[116:119], v172 offset:128\n\t" \
        "v_mfma_f32_16x16x32_bf16 v[68:71], a[216:219], v[128:131], v[68:71]\n\t" \
        "ds_read_b128 v[176:179], v15 offset:1792\n\t" \
        "v_mfma_f32_16x16x32_bf16 v[72:75], a[220:223], v[128:131], v[72:75]\n\t" \
        "ds_read_b128 v[120:123], v173 offset:128\n\t" \
        "v_mfma_f32_16x16x32_bf16 v[76:79], a[224:227], v[128:131], v[76:79]\n\t" \
        "s_waitcnt vmcnt(9)\n\t" \
        "s_barrier\n\t" \
        "ds_read_b64_tr_b16 a[240:241], v6 offset:12288\n\t" \
        "s_waitcnt lgkmcnt(7)\n\t" \
        "v_mfma_f32_16x16x32_bf16 v[32:35], a[228:231], v[132:135], v[32:35]\n\t" \
        "ds_read_b64_tr_b16 a[242:243], v6 offset:13056\n\t" \
        "v_mfma_f32_16x16x32_bf16 v[36:39], a[232:235], v[132:135], v[36:39]\n\t" \
        "ds_read_b64_tr_b16 a[244:245], v7 offset:12288\n\t" \
        "v_mfma_f32_16x16x32_bf16 v[40:43], a[236:239], v[132:135], v[40:43]\n\t" \
        "ds_read_b64_tr_b16 a[246:247], v7 offset:13056\n\t" \
        "s_waitcnt lgkmcnt(7)\n\t" \
        "v_mfma_f32_16x16x32_bf16 v[44:47], a[228:231], v[136:139], v[44:47]\n\t" \
        "ds_read_b64_tr_b16 a[248:249], v8 offset:12288\n\t" \
        "s_waitcnt lgkmcnt(6)\n\t" \
        "v_xor_b32 v176, v176, v14\n\t" \
        "v_mfma_f32_16x16x32_bf16 v[48:51], a[232:235], v[136:139], v[48:51]\n\t" \
        "ds_read_b64_tr_b16 a[250:251], v8 offset:13056\n\t" \
        "v_xor_b32 v177, v177, v14\n\t" \
        "v_mfma_f32_16x16x32_bf16 v[52:55], a[236:239], v[136:139], v[52:55]\n\t" \
        "ds_read_b128 v[124:127], v174 offset:128\n\t" \
        "v_xor_b32 v178, v178, v14\n\t" \
        "v_mfma_f32_16x16x32_bf16 v[56:59], a[228:231], v[140:143], v[56:59]\n\t" \
        "ds_read_b128 v[128:131], v175 offset:128\n\t" \
        "v_xor_b32 v179, v179, v14\n\t" \
        "v_mfma_f32_16x16x32_bf16 v[60:63], a[232:235], v[140:143], v[60:63]\n\t" \
        "ds_read_b128 v[132:135], v176 offset:0\n\t" \
        "v_mfma_f32_16x16x32_bf16 v[64:67], a[236:239], v[140:143], v[64:67]\n\t" \
        "ds_read_b64_tr_b16 a[252:253], v6 offset:18432\n\t" \
        "v_mfma_f32_16x16x32_bf16 v[68:71], a[228:231], v[144:147], v[68:71]\n\t" \
        "ds_read_b64_tr_b16 a[254:255], v6 offset:19200\n\t" \
        "v_mfma_f32_16x16x32_bf16 v[72:75], a[232:235], v[144:147], v[72:75]\n\t" \
        "ds_read_b64_tr_b16 v[188:189], v7 offset:18432\n\t" \
        "v_mfma_f32_16x16x32_bf16 v[76:79], a[236:239], v[144:147], v[76:79]\n\t" \
        "ds_read_b64_tr_b16 v[190:191], v7 offset:19200\n\t" \
        "s_waitcnt lgkmcnt(7)\n\t" \
        "v_mfma_f32_16x16x32_bf16 v[32:35], a[240:243], v[116:119], v[32:35]\n\t" \
        "ds_read_b64_tr_b16 v[192:193], v8 offset:18432\n\t" \
        "v_mfma_f32_16x16x32_bf16 v[36:39], a[244:247], v[116:119], v[36:39]\n\t" \
        "ds_read_b64_tr_b16 v[194:195], v8 offset:19200\n\t" \
        "v_mfma_f32_16x16x32_bf16 v[40:43], a[248:251], v[116:119], v[40:43]\n\t" \
        "ds_read_b128 v[136:139], v177 offset:0\n\t" \
        "v_mfma_f32_16x16x32_bf16 v[44:47], a[240:243], v[120:123], v[44:47]\n\t" \
        "ds_read_b128 v[140:143], v178 offset:0\n\t" \
        "v_mfma_f32_16x16x32_bf16 v[48:51], a[244:247], v[120:123], v[48:51]\n\t" \
        "ds_read_b128 v[144:147], v179 offset:0\n\t" \
        "v_mfma_f32_16x16x32_bf16 v[52:55], a[248:251], v[120:123], v[52:55]\n\t" \
        "ds_read_b128 v[116:119], v176 offset:64\n\t" \
        "s_waitcnt lgkmcnt(7)\n\t" \
        "v_mfma_f32_16x16x32_bf16 v[56:59], a[240:243], v[124:127], v[56:59]\n\t" \
        "ds_read_b128 v[120:123], v177 offset:64\n\t" \
        "v_mfma_f32_16x16x32_bf16 v[60:63], a[244:247], v[124:127], v[60:63]\n\t" \
        "ds_read_b128 v[180:183], v15 offset:2048\n\t" \
        "v_mfma_f32_16x16x32_bf16 v[64:67], a[248:251], v[124:127], v[64:67]\n\t" \
        "ds_read_b128 v[124:127], v178 offset:64\n\t" \
        "v_mfma_f32_16x16x32_bf16 v[68:71], a[240:243], v[128:131], v[68:71]\n\t" \
        "ds_read_b64 v[148:149], v15 offset:4608\n\t" \
        "v_mfma_f32_16x16x32_bf16 v[72:75], a[244:247], v[128:131], v[72:75]\n\t" \
        "ds_read_b64 v[152:153], v15 offset:4864\n\t" \
        "v_mfma_f32_16x16x32_bf16 v[76:79], a[248:251], v[128:131], v[76:79]\n\t" \
        "s_waitcnt vmcnt(6)\n\t" \
        "s_barrier\n\t" \
        "ds_read_b64_tr_b16 v[196:197], v6 offset:24576\n\t" \
        "v_mfma_f32_16x16x32_bf16 v[32:35], a[252:255], v[132:135], v[32:35]\n\t" \
        "ds_read_b64_tr_b16 v[198:199], v6 offset:25344\n\t" \
        "s_waitcnt lgkmcnt(7)\n\t" \
        "v_mfma_f32_16x16x32_bf16 v[36:39], v[188:191], v[132:135], v[36:39]\n\t" \
        "ds_read_b64_tr_b16 v[200:201], v7 offset:24576\n\t" \
        "s_waitcnt lgkmcnt(6)\n\t" \
        "v_xor_b32 v180, v180, v14\n\t" \
        "v_mfma_f32_16x16x32_bf16 v[40:43], v[192:195], v[132:135], v[40:43]\n\t" \
        "ds_read_b64_tr_b16 v[202:203], v7 offset:25344\n\t" \
        "v_xor_b32 v181, v181, v14\n\t" \
        "v_mfma_f32_16x16x32_bf16 v[44:47], a[252:255], v[136:139], v[44:47]\n\t" \
        "ds_read_b64_tr_b16 v[204:205], v8 offset:24576\n\t" \
        "v_xor_b32 v182, v182, v14\n\t" \
        "v_mfma_f32_16x16x32_bf16 v[48:51], v[188:191], v[136:139], v[48:51]\n\t" \
        "ds_read_b64_tr_b16 v[206:207], v8 offset:25344\n\t" \
        "v_xor_b32 v183, v183, v14\n\t" \
        "v_mfma_f32_16x16x32_bf16 v[52:55], v[192:195], v[136:139], v[52:55]\n\t" \
        "ds_read_b128 v[128:131], v179 offset:64\n\t" \
        "s_waitcnt lgkmcnt(7)\n\t" \
        "v_xor_b32 v148, v148, v14\n\t" \
        "v_mfma_f32_16x16x32_bf16 v[56:59], a[252:255], v[140:143], v[56:59]\n\t" \
        "ds_read_b128 v[132:135], v176 offset:128\n\t" \
        "v_xor_b32 v152, v152, v14\n\t" \
        "v_mfma_f32_16x16x32_bf16 v[60:63], v[188:191], v[140:143], v[60:63]\n\t" \
        "ds_read_b64_tr_b16 v[208:209], v6 offset:30720\n\t" \
        "v_xor_b32 v149, v149, v14\n\t" \
        "v_mfma_f32_16x16x32_bf16 v[64:67], v[192:195], v[140:143], v[64:67]\n\t" \
        "ds_read_b64_tr_b16 v[210:211], v6 offset:31488\n\t" \
        "v_xor_b32 v153, v153, v14\n\t" \
        "v_mfma_f32_16x16x32_bf16 v[68:71], a[252:255], v[144:147], v[68:71]\n\t" \
        "ds_read_b64_tr_b16 v[212:213], v7 offset:30720\n\t" \
        "v_mfma_f32_16x16x32_bf16 v[72:75], v[188:191], v[144:147], v[72:75]\n\t" \
        "ds_read_b64_tr_b16 v[214:215], v7 offset:31488\n\t" \
        "v_mfma_f32_16x16x32_bf16 v[76:79], v[192:195], v[144:147], v[76:79]\n\t" \
        "ds_read_b64_tr_b16 v[216:217], v8 offset:30720\n\t" \
        "s_waitcnt lgkmcnt(7)\n\t" \
        "v_mfma_f32_16x16x32_bf16 v[32:35], v[196:199], v[116:119], v[32:35]\n\t" \
        "ds_read_b64_tr_b16 v[218:219], v8 offset:31488\n\t" \
        "v_mfma_f32_16x16x32_bf16 v[36:39], v[200:203], v[116:119], v[36:39]\n\t" \
        "ds_read_b128 v[136:139], v177 offset:128\n\t" \
        "v_mfma_f32_16x16x32_bf16 v[40:43], v[204:207], v[116:119], v[40:43]\n\t" \
        "ds_read_b128 v[140:143], v178 offset:128\n\t" \
        "v_mfma_f32_16x16x32_bf16 v[44:47], v[196:199], v[120:123], v[44:47]\n\t" \
        "ds_read_b128 v[144:147], v179 offset:128\n\t" \
        "v_mfma_f32_16x16x32_bf16 v[48:51], v[200:203], v[120:123], v[48:51]\n\t" \
        "ds_read_b128 v[116:119], v180 offset:0\n\t" \
        "v_mfma_f32_16x16x32_bf16 v[52:55], v[204:207], v[120:123], v[52:55]\n\t" \
        "ds_read_b128 v[120:123], v181 offset:0\n\t" \
        "v_mfma_f32_16x16x32_bf16 v[56:59], v[196:199], v[124:127], v[56:59]\n\t" \
        "ds_read_b64 v[156:157], v15 offset:5120\n\t" \
        "v_mfma_f32_16x16x32_bf16 v[60:63], v[200:203], v[124:127], v[60:63]\n\t" \
        "ds_read_b64 v[160:161], v15 offset:5376\n\t" \
        "v_mfma_f32_16x16x32_bf16 v[64:67], v[204:207], v[124:127], v[64:67]\n\t" \
        "ds_read_b128 v[124:127], v182 offset:0\n\t" \
        "s_waitcnt lgkmcnt(7)\n\t" \
        "v_mfma_f32_16x16x32_bf16 v[68:71], v[196:199], v[128:131], v[68:71]\n\t" \
        "ds_read_b64 v[164:165], v15 offset:5632\n\t" \
        "v_mfma_f32_16x16x32_bf16 v[72:75], v[200:203], v[128:131], v[72:75]\n\t" \
        "ds_read_b64 v[168:169], v15 offset:5888\n\t" \
        "v_mfma_f32_16x16x32_bf16 v[76:79], v[204:207], v[128:131], v[76:79]\n\t" \
        "s_waitcnt vmcnt(3)\n\t" \
        "s_barrier\n\t" \
        "ds_read_b64_tr_b16 v[220:221], v6 offset:36864\n\t" \
        "v_mfma_f32_16x16x32_bf16 v[32:35], v[208:211], v[132:135], v[32:35]\n\t" \
        "ds_read_b64_tr_b16 v[222:223], v6 offset:37632\n\t" \
        "s_waitcnt lgkmcnt(6)\n\t" \
        "v_xor_b32 v156, v156, v14\n\t" \
        "v_mfma_f32_16x16x32_bf16 v[36:39], v[212:215], v[132:135], v[36:39]\n\t" \
        "ds_read_b64_tr_b16 v[224:225], v7 offset:36864\n\t" \
        "s_waitcnt lgkmcnt(6)\n\t" \
        "v_xor_b32 v160, v160, v14\n\t" \
        "v_mfma_f32_16x16x32_bf16 v[40:43], v[216:219], v[132:135], v[40:43]\n\t" \
        "ds_read_b64_tr_b16 v[226:227], v7 offset:37632\n\t" \
        "v_xor_b32 v157, v157, v14\n\t" \
        "v_mfma_f32_16x16x32_bf16 v[44:47], v[208:211], v[136:139], v[44:47]\n\t" \
        "ds_read_b64_tr_b16 v[228:229], v8 offset:36864\n\t" \
        "s_waitcnt lgkmcnt(6)\n\t" \
        "v_xor_b32 v164, v164, v14\n\t" \
        "v_mfma_f32_16x16x32_bf16 v[48:51], v[212:215], v[136:139], v[48:51]\n\t" \
        "ds_read_b64_tr_b16 v[230:231], v8 offset:37632\n\t" \
        "s_waitcnt lgkmcnt(6)\n\t" \
        "v_xor_b32 v168, v168, v14\n\t" \
        "v_mfma_f32_16x16x32_bf16 v[52:55], v[216:219], v[136:139], v[52:55]\n\t" \
        "ds_read_b128 v[128:131], v183 offset:0\n\t" \
        "v_xor_b32 v161, v161, v14\n\t" \
        "v_mfma_f32_16x16x32_bf16 v[56:59], v[208:211], v[140:143], v[56:59]\n\t" \
        "ds_read_b128 v[132:135], v180 offset:64\n\t" \
        "v_xor_b32 v165, v165, v14\n\t" \
        "v_mfma_f32_16x16x32_bf16 v[60:63], v[212:215], v[140:143], v[60:63]\n\t" \
        "ds_read_b64_tr_b16 v[232:233], v6 offset:43008\n\t" \
        "v_xor_b32 v169, v169, v14\n\t" \
        "v_mfma_f32_16x16x32_bf16 v[64:67], v[216:219], v[140:143], v[64:67]\n\t" \
        "ds_read_b64_tr_b16 v[234:235], v6 offset:43776\n\t" \
        "v_mfma_f32_16x16x32_bf16 v[68:71], v[208:211], v[144:147], v[68:71]\n\t" \
        "ds_read_b64_tr_b16 v[236:237], v7 offset:43008\n\t" \
        "v_mfma_f32_16x16x32_bf16 v[72:75], v[212:215], v[144:147], v[72:75]\n\t" \
        "ds_read_b64_tr_b16 v[238:239], v7 offset:43776\n\t" \
        "v_mfma_f32_16x16x32_bf16 v[76:79], v[216:219], v[144:147], v[76:79]\n\t" \
        "ds_read_b64_tr_b16 v[240:241], v8 offset:43008\n\t" \
        "s_waitcnt lgkmcnt(7)\n\t" \
        "v_mfma_f32_16x16x32_bf16 v[32:35], v[220:223], v[116:119], v[32:35]\n\t" \
        "ds_read_b64_tr_b16 v[242:243], v8 offset:43776\n\t" \
        "v_mfma_f32_16x16x32_bf16 v[36:39], v[224:227], v[116:119], v[36:39]\n\t" \
        "ds_read_b128 v[136:139], v181 offset:64\n\t" \
        "v_mfma_f32_16x16x32_bf16 v[40:43], v[228:231], v[116:119], v[40:43]\n\t" \
        "ds_read_b128 v[140:143], v182 offset:64\n\t" \
        "v_mfma_f32_16x16x32_bf16 v[44:47], v[220:223], v[120:123], v[44:47]\n\t" \
        "ds_read_b128 v[144:147], v183 offset:64\n\t" \
        "v_mfma_f32_16x16x32_bf16 v[48:51], v[224:227], v[120:123], v[48:51]\n\t" \
        "ds_read_b128 v[116:119], v180 offset:128\n\t" \
        "v_mfma_f32_16x16x32_bf16 v[52:55], v[228:231], v[120:123], v[52:55]\n\t" \
        "ds_read_b128 v[120:123], v181 offset:128\n\t" \
        "v_mfma_f32_16x16x32_bf16 v[56:59], v[220:223], v[124:127], v[56:59]\n\t" \
        "ds_read_b64 v[172:173], v15 offset:6144\n\t" \
        "v_mfma_f32_16x16x32_bf16 v[60:63], v[224:227], v[124:127], v[60:63]\n\t" \
        "ds_read_b64 v[176:177], v15 offset:6400\n\t" \
        "v_mfma_f32_16x16x32_bf16 v[64:67], v[228:231], v[124:127], v[64:67]\n\t" \
        "ds_read_b128 v[124:127], v182 offset:128\n\t" \
        "s_waitcnt lgkmcnt(7)\n\t" \
        "v_mfma_f32_16x16x32_bf16 v[68:71], v[220:223], v[128:131], v[68:71]\n\t" \
        "v_mfma_f32_16x16x32_bf16 v[72:75], v[224:227], v[128:131], v[72:75]\n\t" \
        "v_mfma_f32_16x16x32_bf16 v[76:79], v[228:231], v[128:131], v[76:79]\n\t" \
        "s_waitcnt vmcnt(0)\n\t" \
        "s_barrier\n\t" \
        "ds_read_b64_tr_b16 v[244:245], v6 offset:49152\n\t" \
        "v_mfma_f32_16x16x32_bf16 v[32:35], v[232:235], v[132:135], v[32:35]\n\t" \
        "ds_read_b64_tr_b16 v[246:247], v6 offset:49920\n\t" \
        "s_waitcnt lgkmcnt(4)\n\t" \
        "v_xor_b32 v172, v172, v14\n\t" \
        "v_mfma_f32_16x16x32_bf16 v[36:39], v[236:239], v[132:135], v[36:39]\n\t" \
        "ds_read_b64_tr_b16 v[248:249], v7 offset:49152\n\t" \
        "s_waitcnt lgkmcnt(4)\n\t" \
        "v_xor_b32 v176, v176, v14\n\t" \
        "v_mfma_f32_16x16x32_bf16 v[40:43], v[240:243], v[132:135], v[40:43]\n\t" \
        "ds_read_b64_tr_b16 v[250:251], v7 offset:49920\n\t" \
        "v_xor_b32 v173, v173, v14\n\t" \
        "v_mfma_f32_16x16x32_bf16 v[44:47], v[232:235], v[136:139], v[44:47]\n\t" \
        "ds_read_b64_tr_b16 v[252:253], v8 offset:49152\n\t" \
        "v_xor_b32 v177, v177, v14\n\t" \
        "v_mfma_f32_16x16x32_bf16 v[48:51], v[236:239], v[136:139], v[48:51]\n\t" \
        "ds_read_b64_tr_b16 v[254:255], v8 offset:49920\n\t" \
        "v_mfma_f32_16x16x32_bf16 v[52:55], v[240:243], v[136:139], v[52:55]\n\t" \
        "ds_read_b128 v[128:131], v183 offset:128\n\t" \
        "v_mfma_f32_16x16x32_bf16 v[56:59], v[232:235], v[140:143], v[56:59]\n\t" \
        "ds_read_b128 v[132:135], v148 offset:0\n\t" \
        "v_mfma_f32_16x16x32_bf16 v[60:63], v[236:239], v[140:143], v[60:63]\n\t" \
        "ds_read_b128 v[136:139], v148 offset:64\n\t" \
        "v_mfma_f32_16x16x32_bf16 v[64:67], v[240:243], v[140:143], v[64:67]\n\t" \
        "ds_read_b128 v[140:143], v148 offset:128\n\t" \
        "v_mfma_f32_16x16x32_bf16 v[68:71], v[232:235], v[144:147], v[68:71]\n\t" \
        "ds_read_b64 v[180:181], v15 offset:6656\n\t" \
        "v_mfma_f32_16x16x32_bf16 v[72:75], v[236:239], v[144:147], v[72:75]\n\t" \
        "v_mfma_f32_16x16x32_bf16 v[76:79], v[240:243], v[144:147], v[76:79]\n\t" \
        "ds_read_b128 v[144:147], v152 offset:0\n\t" \
        "s_waitcnt lgkmcnt(6)\n\t" \
        "v_mfma_f32_16x16x32_bf16 v[32:35], v[244:247], v[116:119], v[32:35]\n\t" \
        "v_mfma_f32_16x16x32_bf16 v[36:39], v[248:251], v[116:119], v[36:39]\n\t" \
        "v_mfma_f32_16x16x32_bf16 v[40:43], v[252:255], v[116:119], v[40:43]\n\t" \
        "ds_read_b128 v[116:119], v152 offset:64\n\t" \
        "v_mfma_f32_16x16x32_bf16 v[44:47], v[244:247], v[120:123], v[44:47]\n\t" \
        "s_waitcnt lgkmcnt(2)\n\t" \
        "v_xor_b32 v180, v180, v14\n\t" \
        "v_xor_b32 v181, v181, v14\n\t" \
        "v_mfma_f32_16x16x32_bf16 v[48:51], v[248:251], v[120:123], v[48:51]\n\t" \
        "v_mfma_f32_16x16x32_bf16 v[52:55], v[252:255], v[120:123], v[52:55]\n\t" \
        "ds_read_b128 v[120:123], v152 offset:128\n\t" \
        "v_mfma_f32_16x16x32_bf16 v[56:59], v[244:247], v[124:127], v[56:59]\n\t" \
        "v_mfma_f32_16x16x32_bf16 v[60:63], v[248:251], v[124:127], v[60:63]\n\t" \
        "v_mfma_f32_16x16x32_bf16 v[64:67], v[252:255], v[124:127], v[64:67]\n\t" \
        "ds_read_b128 v[124:127], v156 offset:0\n\t" \
        "v_mfma_f32_16x16x32_bf16 v[68:71], v[244:247], v[128:131], v[68:71]\n\t" \
        "v_mfma_f32_16x16x32_bf16 v[72:75], v[248:251], v[128:131], v[72:75]\n\t" \
        "v_mfma_f32_16x16x32_bf16 v[76:79], v[252:255], v[128:131], v[76:79]\n\t" \
        "ds_read_b128 v[128:131], v156 offset:64\n\t" \
        "v_mfma_f32_16x16x32_bf16 v[80:83], a[0:3], v[132:135], 0\n\t" \
        "v_mfma_f32_16x16x32_bf16 v[84:87], a[4:7], v[132:135], 0\n\t" \
        "v_mfma_f32_16x16x32_bf16 v[88:91], a[8:11], v[132:135], 0\n\t" \
        "v_cvt_pk_bf16_f32 v40, v40, v41\n\t" \
        "v_cvt_pk_bf16_f32 v41, v42, v43\n\t" \
        "v_mfma_f32_16x16x32_bf16 v[80:83], a[12:15], v[136:139], v[80:83]\n\t" \
        "ds_write_b64 v11, v[40:41] offset:0\n\t" \
        "v_cvt_pk_bf16_f32 v52, v52, v53\n\t" \
        "v_mfma_f32_16x16x32_bf16 v[84:87], a[16:19], v[136:139], v[84:87]\n\t" \
        "v_cvt_pk_bf16_f32 v53, v54, v55\n\t" \
        "v_cvt_pk_bf16_f32 v64, v64, v65\n\t" \
        "v_mfma_f32_16x16x32_bf16 v[88:91], a[20:23], v[136:139], v[88:91]\n\t" \
        "ds_write_b64 v11, v[52:53] offset:2048\n\t" \
        "v_cvt_pk_bf16_f32 v65, v66, v67\n\t" \
        "v_mfma_f32_16x16x32_bf16 v[80:83], a[24:27], v[140:143], v[80:83]\n\t" \
        "ds_write_b64 v11, v[64:65] offset:4096\n\t" \
        "v_cvt_pk_bf16_f32 v76, v76, v77\n\t" \
        "v_mfma_f32_16x16x32_bf16 v[84:87], a[28:31], v[140:143], v[84:87]\n\t" \
        "ds_read_b128 v[132:135], v156 offset:128\n\t" \
        "v_cvt_pk_bf16_f32 v77, v78, v79\n\t" \
        "v_mfma_f32_16x16x32_bf16 v[88:91], a[32:35], v[140:143], v[88:91]\n\t" \
        "ds_write_b64 v11, v[76:77] offset:6144\n\t" \
        "v_cvt_pk_bf16_f32 v32, v32, v33\n\t" \
        "s_waitcnt lgkmcnt(5)\n\t" \
        "v_mfma_f32_16x16x32_bf16 v[80:83], a[36:39], v[144:147], v[80:83]\n\t" \
        "ds_read_b128 v[136:139], v160 offset:0\n\t" \
        "v_cvt_pk_bf16_f32 v33, v34, v35\n\t" \
        "v_mfma_f32_16x16x32_bf16 v[84:87], a[40:43], v[144:147], v[84:87]\n\t" \
        "ds_read_b128 v[140:143], v160 offset:64\n\t" \
        "v_cvt_pk_bf16_f32 v34, v36, v37\n\t" \
        "v_mfma_f32_16x16x32_bf16 v[88:91], a[44:47], v[144:147], v[88:91]\n\t" \
        "ds_read_b128 v[144:147], v160 offset:128\n\t" \
        "v_cvt_pk_bf16_f32 v35, v38, v39\n\t" \
        "v_mfma_f32_16x16x32_bf16 v[80:83], a[48:51], v[116:119], v[80:83]\n\t" \
        "v_permlane16_swap_b32 v32, v34\n\t" \
        "v_permlane16_swap_b32 v33, v35\n\t" \
        "v_mfma_f32_16x16x32_bf16 v[84:87], a[52:55], v[116:119], v[84:87]\n\t" \
        "v_mfma_f32_16x16x32_bf16 v[88:91], a[56:59], v[116:119], v[88:91]\n\t" \
        "ds_read_b128 v[116:119], v164 offset:0\n\t" \
        "v_mfma_f32_16x16x32_bf16 v[80:83], a[60:63], v[120:123], v[80:83]\n\t" \
        "v_mfma_f32_16x16x32_bf16 v[84:87], a[64:67], v[120:123], v[84:87]\n\t" \
        "v_mfma_f32_16x16x32_bf16 v[88:91], a[68:71], v[120:123], v[88:91]\n\t" \
        "ds_read_b128 v[120:123], v164 offset:64\n\t" \
        "v_mfma_f32_16x16x32_bf16 v[80:83], a[72:75], v[124:127], v[80:83]\n\t" \
        "v_mfma_f32_16x16x32_bf16 v[84:87], a[76:79], v[124:127], v[84:87]\n\t" \
        "s_waitcnt lgkmcnt(5)\n\t" \
        "s_barrier\n\t" \
        "ds_read_b64 v[42:43], v12 offset:0\n\t" \
        "v_mfma_f32_16x16x32_bf16 v[88:91], a[80:83], v[124:127], v[88:91]\n\t" \
        "ds_read_b128 v[124:127], v164 offset:128\n\t" \
        "v_mfma_f32_16x16x32_bf16 v[80:83], a[84:87], v[128:131], v[80:83]\n\t" \
        "v_mfma_f32_16x16x32_bf16 v[84:87], a[88:91], v[128:131], v[84:87]\n\t" \
        "v_mfma_f32_16x16x32_bf16 v[88:91], a[92:95], v[128:131], v[88:91]\n\t" \
        "ds_read_b128 v[128:131], v168 offset:0\n\t" \
        "v_mfma_f32_16x16x32_bf16 v[80:83], a[96:99], v[132:135], v[80:83]\n\t" \
        "s_mov_b32 s56, s64\n\t" \
        "buffer_store_dwordx4 v[32:35], v9, s[28:31], s56 offen sc1\n\t" \
        "v_mfma_f32_16x16x32_bf16 v[84:87], a[100:103], v[132:135], v[84:87]\n\t" \
        "v_mfma_f32_16x16x32_bf16 v[88:91], a[104:107], v[132:135], v[88:91]\n\t" \
        "ds_read_b128 v[132:135], v168 offset:64\n\t" \
        "s_waitcnt lgkmcnt(3)\n\t" \
        "v_mfma_f32_16x16x32_bf16 v[80:83], a[108:111], v[136:139], v[80:83]\n\t" \
        "v_permlane16_swap_b32 v40, v42\n\t" \
        "v_permlane16_swap_b32 v41, v43\n\t" \
        "v_mfma_f32_16x16x32_bf16 v[84:87], a[112:115], v[136:139], v[84:87]\n\t" \
        "v_mfma_f32_16x16x32_bf16 v[88:91], a[116:119], v[136:139], v[88:91]\n\t" \
        "ds_read_b128 v[136:139], v168 offset:128\n\t" \
        "v_mfma_f32_16x16x32_bf16 v[80:83], a[120:123], v[140:143], v[80:83]\n\t" \
        "v_mfma_f32_16x16x32_bf16 v[84:87], a[124:127], v[140:143], v[84:87]\n\t" \
        "s_mov_b32 s56, s64\n\t" \
        "buffer_store_dwordx4 v[40:43], v10, s[28:31], s56 offen sc1\n\t" \
        "v_mfma_f32_16x16x32_bf16 v[88:91], a[128:131], v[140:143], v[88:91]\n\t" \
        "ds_read_b128 v[140:143], v172 offset:0\n\t" \
        "v_cvt_pk_bf16_f32 v44, v44, v45\n\t" \
        "v_mfma_f32_16x16x32_bf16 v[80:83], a[132:135], v[144:147], v[80:83]\n\t" \
        "v_cvt_pk_bf16_f32 v45, v46, v47\n\t" \
        "v_cvt_pk_bf16_f32 v46, v48, v49\n\t" \
        "v_mfma_f32_16x16x32_bf16 v[84:87], a[136:139], v[144:147], v[84:87]\n\t" \
        "v_cvt_pk_bf16_f32 v47, v50, v51\n\t" \
        "v_permlane16_swap_b32 v44, v46\n\t" \
        "v_mfma_f32_16x16x32_bf16 v[88:91], a[140:143], v[144:147], v[88:91]\n\t" \
        "ds_read_b128 v[144:147], v172 offset:64\n\t" \
        "v_permlane16_swap_b32 v45, v47\n\t" \
        "v_mfma_f32_16x16x32_bf16 v[80:83], a[144:147], v[116:119], v[80:83]\n\t" \
        "ds_read_b64 v[54:55], v12 offset:2048\n\t" \
        "v_mfma_f32_16x16x32_bf16 v[84:87], a[148:151], v[116:119], v[84:87]\n\t" \
        "v_mfma_f32_16x16x32_bf16 v[88:91], a[152:155], v[116:119], v[88:91]\n\t" \
        "ds_read_b128 v[116:119], v172 offset:128\n\t" \
        "v_mfma_f32_16x16x32_bf16 v[80:83], a[156:159], v[120:123], v[80:83]\n\t" \
        "v_mfma_f32_16x16x32_bf16 v[84:87], a[160:163], v[120:123], v[84:87]\n\t" \
        "v_mfma_f32_16x16x32_bf16 v[88:91], a[164:167], v[120:123], v[88:91]\n\t" \
        "ds_read_b128 v[120:123], v176 offset:0\n\t" \
        "s_waitcnt lgkmcnt(4)\n\t" \
        "v_mfma_f32_16x16x32_bf16 v[80:83], a[168:171], v[124:127], v[80:83]\n\t" \
        "v_mfma_f32_16x16x32_bf16 v[84:87], a[172:175], v[124:127], v[84:87]\n\t" \
        "v_mfma_f32_16x16x32_bf16 v[88:91], a[176:179], v[124:127], v[88:91]\n\t" \
        "ds_read_b128 v[124:127], v176 offset:64\n\t" \
        "s_waitcnt lgkmcnt(3)\n\t" \
        "v_permlane16_swap_b32 v52, v54\n\t" \
        "v_mfma_f32_16x16x32_bf16 v[80:83], a[180:183], v[128:131], v[80:83]\n\t" \
        "v_permlane16_swap_b32 v53, v55\n\t" \
        "v_mfma_f32_16x16x32_bf16 v[84:87], a[184:187], v[128:131], v[84:87]\n\t" \
        "v_mfma_f32_16x16x32_bf16 v[88:91], a[188:191], v[128:131], v[88:91]\n\t" \
        "ds_read_b128 v[128:131], v176 offset:128\n\t" \
        "v_mfma_f32_16x16x32_bf16 v[80:83], a[192:195], v[132:135], v[80:83]\n\t" \
        "v_mfma_f32_16x16x32_bf16 v[84:87], a[196:199], v[132:135], v[84:87]\n\t" \
        "v_mfma_f32_16x16x32_bf16 v[88:91], a[200:203], v[132:135], v[88:91]\n\t" \
        "ds_read_b128 v[132:135], v180 offset:0\n\t" \
        "v_mfma_f32_16x16x32_bf16 v[80:83], a[204:207], v[136:139], v[80:83]\n\t" \
        "v_mfma_f32_16x16x32_bf16 v[84:87], a[208:211], v[136:139], v[84:87]\n\t" \
        "v_mfma_f32_16x16x32_bf16 v[88:91], a[212:215], v[136:139], v[88:91]\n\t" \
        "ds_read_b128 v[136:139], v180 offset:64\n\t" \
        "v_mfma_f32_16x16x32_bf16 v[80:83], a[216:219], v[140:143], v[80:83]\n\t" \
        "s_add_u32 s56, s64, 0x15000\n\t" \
        "buffer_store_dwordx4 v[44:47], v9, s[28:31], s56 offen sc1\n\t" \
        "v_mfma_f32_16x16x32_bf16 v[84:87], a[220:223], v[140:143], v[84:87]\n\t" \
        "v_mfma_f32_16x16x32_bf16 v[88:91], a[224:227], v[140:143], v[88:91]\n\t" \
        "ds_read_b128 v[140:143], v180 offset:128\n\t" \
        "v_mfma_f32_16x16x32_bf16 v[80:83], a[228:231], v[144:147], v[80:83]\n\t" \
        "v_mfma_f32_16x16x32_bf16 v[84:87], a[232:235], v[144:147], v[84:87]\n\t" \
        "v_mfma_f32_16x16x32_bf16 v[88:91], a[236:239], v[144:147], v[88:91]\n\t" \
        "ds_read_b128 v[144:147], v149 offset:0\n\t" \
        "s_waitcnt lgkmcnt(3)\n\t" \
        "v_mfma_f32_16x16x32_bf16 v[80:83], a[240:243], v[116:119], v[80:83]\n\t" \
        "v_mfma_f32_16x16x32_bf16 v[84:87], a[244:247], v[116:119], v[84:87]\n\t" \
        "s_add_u32 s56, s64, 0x15000\n\t" \
        "buffer_store_dwordx4 v[52:55], v10, s[28:31], s56 offen sc1\n\t" \
        "v_mfma_f32_16x16x32_bf16 v[88:91], a[248:251], v[116:119], v[88:91]\n\t" \
        "ds_read_b128 v[116:119], v149 offset:64\n\t" \
        "v_cvt_pk_bf16_f32 v56, v56, v57\n\t" \
        "v_mfma_f32_16x16x32_bf16 v[80:83], a[252:255], v[120:123], v[80:83]\n\t" \
        "v_cvt_pk_bf16_f32 v57, v58, v59\n\t" \
        "v_cvt_pk_bf16_f32 v58, v60, v61\n\t" \
        "v_mfma_f32_16x16x32_bf16 v[84:87], v[188:191], v[120:123], v[84:87]\n\t" \
        "v_cvt_pk_bf16_f32 v59, v62, v63\n\t" \
        "v_permlane16_swap_b32 v56, v58\n\t" \
        "v_mfma_f32_16x16x32_bf16 v[88:91], v[192:195], v[120:123], v[88:91]\n\t" \
        "ds_read_b128 v[120:123], v149 offset:128\n\t" \
        "v_permlane16_swap_b32 v57, v59\n\t" \
        "v_mfma_f32_16x16x32_bf16 v[80:83], v[196:199], v[124:127], v[80:83]\n\t" \
        "ds_read_b64 v[66:67], v12 offset:4096\n\t" \
        "v_mfma_f32_16x16x32_bf16 v[84:87], v[200:203], v[124:127], v[84:87]\n\t" \
        "v_mfma_f32_16x16x32_bf16 v[88:91], v[204:207], v[124:127], v[88:91]\n\t" \
        "ds_read_b128 v[124:127], v153 offset:0\n\t" \
        "v_mfma_f32_16x16x32_bf16 v[80:83], v[208:211], v[128:131], v[80:83]\n\t" \
        "v_mfma_f32_16x16x32_bf16 v[84:87], v[212:215], v[128:131], v[84:87]\n\t" \
        "v_mfma_f32_16x16x32_bf16 v[88:91], v[216:219], v[128:131], v[88:91]\n\t" \
        "ds_read_b128 v[128:131], v153 offset:64\n\t" \
        "v_mfma_f32_16x16x32_bf16 v[80:83], v[220:223], v[132:135], v[80:83]\n\t" \
        "v_mfma_f32_16x16x32_bf16 v[84:87], v[224:227], v[132:135], v[84:87]\n\t" \
        "v_mfma_f32_16x16x32_bf16 v[88:91], v[228:231], v[132:135], v[88:91]\n\t" \
        "ds_read_b128 v[132:135], v153 offset:128\n\t" \
        "s_waitcnt lgkmcnt(3)\n\t" \
        "v_permlane16_swap_b32 v64, v66\n\t" \
        "v_mfma_f32_16x16x32_bf16 v[80:83], v[232:235], v[136:139], v[80:83]\n\t" \
        "v_permlane16_swap_b32 v65, v67\n\t" \
        "v_mfma_f32_16x16x32_bf16 v[84:87], v[236:239], v[136:139], v[84:87]\n\t" \
        "v_mfma_f32_16x16x32_bf16 v[88:91], v[240:243], v[136:139], v[88:91]\n\t" \
        "ds_read_b128 v[136:139], v157 offset:0\n\t" \
        "v_mfma_f32_16x16x32_bf16 v[80:83], v[244:247], v[140:143], v[80:83]\n\t" \
        "v_mfma_f32_16x16x32_bf16 v[84:87], v[248:251], v[140:143], v[84:87]\n\t" \
        "v_mfma_f32_16x16x32_bf16 v[88:91], v[252:255], v[140:143], v[88:91]\n\t" \
        "ds_read_b128 v[140:143], v157 offset:64\n\t" \
        "v_mfma_f32_16x16x32_bf16 v[92:95], a[0:3], v[144:147], 0\n\t" \
        "v_mfma_f32_16x16x32_bf16 v[96:99], a[4:7], v[144:147], 0\n\t" \
        "v_mfma_f32_16x16x32_bf16 v[100:103], a[8:11], v[144:147], 0\n\t" \
        "s_nop 5\n\t" \
        "v_cvt_pk_bf16_f32 v88, v88, v89\n\t" \
        "v_cvt_pk_bf16_f32 v89, v90, v91\n\t" \
        "v_mfma_f32_16x16x32_bf16 v[92:95], a[12:15], v[116:119], v[92:95]\n\t" \
        "s_add_u32 s56, s64, 0x2a000\n\t" \
        "buffer_store_dwordx4 v[56:59], v9, s[28:31], s56 offen sc1\n\t" \
        "v_mfma_f32_16x16x32_bf16 v[96:99], a[16:19], v[116:119], v[96:99]\n\t" \
        "ds_write_b64 v11, v[88:89] offset:8192\n\t" \
        "v_cvt_pk_bf16_f32 v80, v80, v81\n\t" \
        "v_mfma_f32_16x16x32_bf16 v[100:103], a[20:23], v[116:119], v[100:103]\n\t" \
        "ds_read_b128 v[144:147], v157 offset:128\n\t" \
        "v_cvt_pk_bf16_f32 v81, v82, v83\n\t" \
        "v_mfma_f32_16x16x32_bf16 v[92:95], a[24:27], v[120:123], v[92:95]\n\t" \
        "ds_read_b128 v[116:119], v161 offset:0\n\t" \
        "v_cvt_pk_bf16_f32 v82, v84, v85\n\t" \
        "v_mfma_f32_16x16x32_bf16 v[96:99], a[28:31], v[120:123], v[96:99]\n\t" \
        "v_cvt_pk_bf16_f32 v83, v86, v87\n\t" \
        "v_permlane16_swap_b32 v80, v82\n\t" \
        "v_mfma_f32_16x16x32_bf16 v[100:103], a[32:35], v[120:123], v[100:103]\n\t" \
        "ds_read_b128 v[120:123], v161 offset:64\n\t" \
        "v_permlane16_swap_b32 v81, v83\n\t" \
        "s_waitcnt lgkmcnt(4)\n\t" \
        "v_mfma_f32_16x16x32_bf16 v[92:95], a[36:39], v[124:127], v[92:95]\n\t" \
        "v_mfma_f32_16x16x32_bf16 v[96:99], a[40:43], v[124:127], v[96:99]\n\t" \
        "s_add_u32 s56, s64, 0x2a000\n\t" \
        "buffer_store_dwordx4 v[64:67], v10, s[28:31], s56 offen sc1\n\t" \
        "v_mfma_f32_16x16x32_bf16 v[100:103], a[44:47], v[124:127], v[100:103]\n\t" \
        "ds_read_b128 v[124:127], v161 offset:128\n\t" \
        "v_cvt_pk_bf16_f32 v68, v68, v69\n\t" \
        "v_mfma_f32_16x16x32_bf16 v[92:95], a[48:51], v[128:131], v[92:95]\n\t" \
        "v_cvt_pk_bf16_f32 v69, v70, v71\n\t" \
        "v_cvt_pk_bf16_f32 v70, v72, v73\n\t" \
        "v_mfma_f32_16x16x32_bf16 v[96:99], a[52:55], v[128:131], v[96:99]\n\t" \
        "s_waitcnt lgkmcnt(4)\n\t" \
        "s_barrier\n\t" \
        "ds_read_b64 v[90:91], v12 offset:8192\n\t" \
        "v_cvt_pk_bf16_f32 v71, v74, v75\n\t" \
        "v_mfma_f32_16x16x32_bf16 v[100:103], a[56:59], v[128:131], v[100:103]\n\t" \
        "ds_read_b128 v[128:131], v165 offset:0\n\t" \
        "v_permlane16_swap_b32 v68, v70\n\t" \
        "v_mfma_f32_16x16x32_bf16 v[92:95], a[60:63], v[132:135], v[92:95]\n\t" \
        "ds_read_b64 v[78:79], v12 offset:6144\n\t" \
        "v_permlane16_swap_b32 v69, v71\n\t" \
        "v_mfma_f32_16x16x32_bf16 v[96:99], a[64:67], v[132:135], v[96:99]\n\t" \
        "v_mfma_f32_16x16x32_bf16 v[100:103], a[68:71], v[132:135], v[100:103]\n\t" \
        "ds_read_b128 v[132:135], v165 offset:64\n\t" \
        "v_mfma_f32_16x16x32_bf16 v[92:95], a[72:75], v[136:139], v[92:95]\n\t" \
        "v_mfma_f32_16x16x32_bf16 v[96:99], a[76:79], v[136:139], v[96:99]\n\t" \
        "v_mfma_f32_16x16x32_bf16 v[100:103], a[80:83], v[136:139], v[100:103]\n\t" \
        "ds_read_b128 v[136:139], v165 offset:128\n\t" \
        "v_mfma_f32_16x16x32_bf16 v[92:95], a[84:87], v[140:143], v[92:95]\n\t" \
        "s_waitcnt lgkmcnt(3)\n\t" \
        "v_permlane16_swap_b32 v88, v90\n\t" \
        "v_permlane16_swap_b32 v89, v91\n\t" \
        "v_mfma_f32_16x16x32_bf16 v[96:99], a[88:91], v[140:143], v[96:99]\n\t" \
        "v_mfma_f32_16x16x32_bf16 v[100:103], a[92:95], v[140:143], v[100:103]\n\t" \
        "ds_read_b128 v[140:143], v169 offset:0\n\t" \
        "s_waitcnt lgkmcnt(3)\n\t" \
        "v_permlane16_swap_b32 v76, v78\n\t" \
        "v_mfma_f32_16x16x32_bf16 v[92:95], a[96:99], v[144:147], v[92:95]\n\t" \
        "s_add_u32 s56, s64, 0x54000\n\t" \
        "buffer_store_dwordx4 v[80:83], v9, s[28:31], s56 offen sc1\n\t" \
        "v_mfma_f32_16x16x32_bf16 v[96:99], a[100:103], v[144:147], v[96:99]\n\t" \
        "v_permlane16_swap_b32 v77, v79\n\t" \
        "v_mfma_f32_16x16x32_bf16 v[100:103], a[104:107], v[144:147], v[100:103]\n\t" \
        "ds_read_b128 v[144:147], v169 offset:64\n\t" \
        "v_mfma_f32_16x16x32_bf16 v[92:95], a[108:111], v[116:119], v[92:95]\n\t" \
        "v_mfma_f32_16x16x32_bf16 v[96:99], a[112:115], v[116:119], v[96:99]\n\t" \
        "v_mfma_f32_16x16x32_bf16 v[100:103], a[116:119], v[116:119], v[100:103]\n\t" \
        "ds_read_b128 v[116:119], v169 offset:128\n\t" \
        "v_mfma_f32_16x16x32_bf16 v[92:95], a[120:123], v[120:123], v[92:95]\n\t" \
        "v_mfma_f32_16x16x32_bf16 v[96:99], a[124:127], v[120:123], v[96:99]\n\t" \
        "s_add_u32 s56, s64, 0x54000\n\t" \
        "buffer_store_dwordx4 v[88:91], v10, s[28:31], s56 offen sc1\n\t" \
        "v_mfma_f32_16x16x32_bf16 v[100:103], a[128:131], v[120:123], v[100:103]\n\t" \
        "ds_read_b128 v[120:123], v173 offset:0\n\t" \
        "v_mfma_f32_16x16x32_bf16 v[92:95], a[132:135], v[124:127], v[92:95]\n\t" \
        "s_add_u32 s56, s64, 0x3f000\n\t" \
        "buffer_store_dwordx4 v[68:71], v9, s[28:31], s56 offen sc1\n\t" \
        "v_mfma_f32_16x16x32_bf16 v[96:99], a[136:139], v[124:127], v[96:99]\n\t" \
        "v_mfma_f32_16x16x32_bf16 v[100:103], a[140:143], v[124:127], v[100:103]\n\t" \
        "ds_read_b128 v[124:127], v173 offset:64\n\t" \
        "v_mfma_f32_16x16x32_bf16 v[92:95], a[144:147], v[128:131], v[92:95]\n\t" \
        "v_mfma_f32_16x16x32_bf16 v[96:99], a[148:151], v[128:131], v[96:99]\n\t" \
        "v_mfma_f32_16x16x32_bf16 v[100:103], a[152:155], v[128:131], v[100:103]\n\t" \
        "ds_read_b128 v[128:131], v173 offset:128\n\t" \
        "s_waitcnt lgkmcnt(3)\n\t" \
        "v_mfma_f32_16x16x32_bf16 v[92:95], a[156:159], v[132:135], v[92:95]\n\t" \
        "v_mfma_f32_16x16x32_bf16 v[96:99], a[160:163], v[132:135], v[96:99]\n\t" \
        "s_add_u32 s56, s64, 0x3f000\n\t" \
        "buffer_store_dwordx4 v[76:79], v10, s[28:31], s56 offen sc1\n\t" \
        "v_mfma_f32_16x16x32_bf16 v[100:103], a[164:167], v[132:135], v[100:103]\n\t" \
        "ds_read_b128 v[132:135], v177 offset:0\n\t" \
        "v_mfma_f32_16x16x32_bf16 v[92:95], a[168:171], v[136:139], v[92:95]\n\t" \
        "v_mfma_f32_16x16x32_bf16 v[96:99], a[172:175], v[136:139], v[96:99]\n\t" \
        "v_mfma_f32_16x16x32_bf16 v[100:103], a[176:179], v[136:139], v[100:103]\n\t" \
        "ds_read_b128 v[136:139], v177 offset:64\n\t" \
        "v_mfma_f32_16x16x32_bf16 v[92:95], a[180:183], v[140:143], v[92:95]\n\t" \
        "v_mfma_f32_16x16x32_bf16 v[96:99], a[184:187], v[140:143], v[96:99]\n\t" \
        "v_mfma_f32_16x16x32_bf16 v[100:103], a[188:191], v[140:143], v[100:103]\n\t" \
        "ds_read_b128 v[140:143], v177 offset:128\n\t" \
        "v_mfma_f32_16x16x32_bf16 v[92:95], a[192:195], v[144:147], v[92:95]\n\t" \
        "v_mfma_f32_16x16x32_bf16 v[96:99], a[196:199], v[144:147], v[96:99]\n\t" \
        "v_mfma_f32_16x16x32_bf16 v[100:103], a[200:203], v[144:147], v[100:103]\n\t" \
        "ds_read_b128 v[144:147], v181 offset:0\n\t" \
        "v_mfma_f32_16x16x32_bf16 v[92:95], a[204:207], v[116:119], v[92:95]\n\t" \
        "v_mfma_f32_16x16x32_bf16 v[96:99], a[208:211], v[116:119], v[96:99]\n\t" \
        "v_mfma_f32_16x16x32_bf16 v[100:103], a[212:215], v[116:119], v[100:103]\n\t" \
        "ds_read_b128 v[116:119], v181 offset:64\n\t" \
        "s_waitcnt lgkmcnt(3)\n\t" \
        "v_mfma_f32_16x16x32_bf16 v[92:95], a[216:219], v[120:123], v[92:95]\n\t" \
        "v_mfma_f32_16x16x32_bf16 v[96:99], a[220:223], v[120:123], v[96:99]\n\t" \
        "v_mfma_f32_16x16x32_bf16 v[100:103], a[224:227], v[120:123], v[100:103]\n\t" \
        "ds_read_b128 v[120:123], v181 offset:128\n\t" \
        "v_mfma_f32_16x16x32_bf16 v[92:95], a[228:231], v[124:127], v[92:95]\n\t" \
        "v_mfma_f32_16x16x32_bf16 v[96:99], a[232:235], v[124:127], v[96:99]\n\t" \
        "v_mfma_f32_16x16x32_bf16 v[100:103], a[236:239], v[124:127], v[100:103]\n\t" \
        "v_mfma_f32_16x16x32_bf16 v[92:95], a[240:243], v[128:131], v[92:95]\n\t" \
        "v_mfma_f32_16x16x32_bf16 v[96:99], a[244:247], v[128:131], v[96:99]\n\t" \
        "v_mfma_f32_16x16x32_bf16 v[100:103], a[248:251], v[128:131], v[100:103]\n\t" \
        "v_mfma_f32_16x16x32_bf16 v[92:95], a[252:255], v[132:135], v[92:95]\n\t" \
        "v_mfma_f32_16x16x32_bf16 v[96:99], v[188:191], v[132:135], v[96:99]\n\t" \
        "v_mfma_f32_16x16x32_bf16 v[100:103], v[192:195], v[132:135], v[100:103]\n\t" \
        "v_mfma_f32_16x16x32_bf16 v[92:95], v[196:199], v[136:139], v[92:95]\n\t" \
        "v_mfma_f32_16x16x32_bf16 v[96:99], v[200:203], v[136:139], v[96:99]\n\t" \
        "v_mfma_f32_16x16x32_bf16 v[100:103], v[204:207], v[136:139], v[100:103]\n\t" \
        "s_waitcnt lgkmcnt(0)\n\t" \
        "v_mfma_f32_16x16x32_bf16 v[92:95], v[208:211], v[140:143], v[92:95]\n\t" \
        "v_mfma_f32_16x16x32_bf16 v[96:99], v[212:215], v[140:143], v[96:99]\n\t" \
        "v_mfma_f32_16x16x32_bf16 v[100:103], v[216:219], v[140:143], v[100:103]\n\t" \
        "v_mfma_f32_16x16x32_bf16 v[92:95], v[220:223], v[144:147], v[92:95]\n\t" \
        "v_mfma_f32_16x16x32_bf16 v[96:99], v[224:227], v[144:147], v[96:99]\n\t" \
        "v_mfma_f32_16x16x32_bf16 v[100:103], v[228:231], v[144:147], v[100:103]\n\t" \
        "v_mfma_f32_16x16x32_bf16 v[92:95], v[232:235], v[116:119], v[92:95]\n\t" \
        "v_mfma_f32_16x16x32_bf16 v[96:99], v[236:239], v[116:119], v[96:99]\n\t" \
        "v_mfma_f32_16x16x32_bf16 v[100:103], v[240:243], v[116:119], v[100:103]\n\t" \
        "v_mfma_f32_16x16x32_bf16 v[92:95], v[244:247], v[120:123], v[92:95]\n\t" \
        "v_mfma_f32_16x16x32_bf16 v[96:99], v[248:251], v[120:123], v[96:99]\n\t" \
        "v_mfma_f32_16x16x32_bf16 v[100:103], v[252:255], v[120:123], v[100:103]\n\t" \
        "s_nop 9\n\t" \
        "v_cvt_pk_bf16_f32 v100, v100, v101\n\t" \
        "v_cvt_pk_bf16_f32 v101, v102, v103\n\t" \
        "v_cvt_pk_bf16_f32 v92, v92, v93\n\t" \
        "v_cvt_pk_bf16_f32 v93, v94, v95\n\t" \
        "v_cvt_pk_bf16_f32 v94, v96, v97\n\t" \
        "ds_write_b64 v11, v[100:101] offset:10240\n\t" \
        "v_cvt_pk_bf16_f32 v95, v98, v99\n\t" \
        "v_permlane16_swap_b32 v92, v94\n\t" \
        "s_nop 0\n\t" \
        "v_permlane16_swap_b32 v93, v95\n\t" \
        "s_add_u32 s56, s64, 0x69000\n\t" \
        "buffer_store_dwordx4 v[92:95], v9, s[28:31], s56 offen sc1\n\t" \
        "s_waitcnt lgkmcnt(0)\n\t" \
        "s_barrier\n\t" \
        "ds_read_b64 v[102:103], v12 offset:10240\n\t" \
        "s_waitcnt lgkmcnt(0)\n\t" \
        "v_permlane16_swap_b32 v100, v102\n\t" \
        "v_permlane16_swap_b32 v101, v103\n\t" \
        "s_add_u32 s56, s64, 0x69000\n\t" \
        "buffer_store_dwordx4 v[100:103], v10, s[28:31], s56 offen sc1\n\t" \
        "s_cmp_lg_u32 s3, 0\n\t" \
        "s_cbranch_scc1 .LT_again%=\n\t" \
        "s_endpgm\n\t" \
        ".LR_start%=:\n\t" \
        "s_add_u32 s57, s40, 0x3000\n\t" \
        "s_add_u32 m0, s39, 0x3000\n\t" \
        "s_nop 0\n\t" \
        "buffer_load_dwordx4 v3, s[24:27], s57 offen offset:0  lds\n\t" \
        "v_lshrrev_b32 v38, 4, v2\n\t" \
        "s_lshr_b32 s42, s35, 1\n\t" \
        "s_and_b32 s43, s35, 1\n\t" \
        "v_and_b32 v19, 15, v2\n\t" \
        "s_lshl3_add_u32 s42, s42, s43\n\t" \
        "s_mul_i32 s37, s32, 224\n\t" \
        "v_lshl_add_u32 v18, v38, 1, s42\n\t" \
        "buffer_load_dwordx4 v4, s[24:27], s57 offen offset:1024  lds\n\t" \
        "s_add_u32 s37, s37, s33\n\t" \
        "v_mul_u32_u24 v38, 0x1f80, v18\n\t" \
        "s_mul_i32 s37, s37, 36\n\t" \
        "v_mad_u32_u24 v17, v19, 36, v38\n\t" \
        "s_and_b32 s61, s35, 1\n\t" \
        "v_lshrrev_b32 v40, 4, v2\n\t" \
        "v_and_b32 v41, 15, v2\n\t" \
        "buffer_load_dwordx4 v5, s[24:27], s57 offen offset:2048  lds\n\t" \
        "v_and_b32 v42, 16, v2\n\t" \
        "s_lshr_b32 s62, s35, 1\n\t" \
        "v_lshlrev_b32 v42, 1, v42\n\t" \
        "v_bfe_u32 v43, v2, 2, 2\n\t" \
        "v_mul_u32_u24 v45, 0xc0, v41\n\t" \
        "v_lshl_or_b32 v43, v40, 3, v43\n\t" \
        "v_and_b32 v44, 3, v2\n\t" \
        "s_add_u32 s57, s36, 0xa800\n\t" \
        "s_add_u32 m0, s38, 0x1f00\n\t" \
        "s_nop 0\n\t" \
        "buffer_load_dwordx4 v13, s[16:19], s57 offen nt lds\n\t" \
        "v_mul_u32_u24 v43, 0xc0, v43\n\t" \
        "s_lshl_b32 s63, s61, 7\n\t" \
        "v_lshl_add_u32 v43, v44, 3, v43\n\t" \
        "s_add_u32 s63, s63, 0xf840\n\t" \
        "v_add3_u32 v6, v43, v42, s63\n\t" \
        "s_lshl_b32 s63, s61, 5\n\t" \
        "v_xor_b32 v44, s63, v42\n\t" \
        "s_add_u32 s57, s36, 0x15000\n\t" \
        "s_add_u32 m0, s38, 0x2e80\n\t" \
        "s_nop 0\n\t" \
        "buffer_load_dwordx4 v13, s[16:19], s57 offen nt lds\n\t" \
        "v_xor_b32 v7, 32, v6\n\t" \
        "v_add_u32 v43, 0xf880, v43\n\t" \
        "s_lshl_b32 s63, s61, 7\n\t" \
        "v_add_u32 v8, v43, v44\n\t" \
        "v_lshrrev_b32 v43, 1, v2\n\t" \
        "v_and_b32 v43, 16, v43\n\t" \
        "v_add_u32 v45, v45, v43\n\t" \
        "s_add_u32 s57, s40, 0x6000\n\t" \
        "s_add_u32 m0, s39, 0x6000\n\t" \
        "s_nop 0\n\t" \
        "buffer_load_dwordx4 v3, s[24:27], s57 offen offset:0  lds\n\t" \
        "v_bfe_u32 v43, v2, 3, 1\n\t" \
        "v_add3_u32 v9, v45, v42, s63\n\t" \
        "v_cmp_eq_u32 vcc, s61, v43\n\t" \
        "v_add3_u32 v10, v45, v44, 64\n\t" \
        "v_bfrev_b32 v44, 1\n\t" \
        "s_lshl_b32 s63, s35, 9\n\t" \
        "v_cndmask_b32 v10, v44, v10, vcc\n\t" \
        "buffer_load_dwordx4 v4, s[24:27], s57 offen offset:1024  lds\n\t" \
        "s_add_u32 s63, s63, 0x21840\n\t" \
        "v_lshl_add_u32 v11, v2, 3, s63\n\t" \
        "s_lshl_b32 s63, s61, 10\n\t" \
        "s_sub_u32 s63, 0x200, s63\n\t" \
        "v_add_u32 v12, s63, v11\n\t" \
        "s_add_u32 s64, s32, s62\n\t" \
        "s_mul_i32 s63, s33, 0xc0\n\t" \
        "buffer_load_dwordx4 v5, s[24:27], s57 offen offset:2048  lds\n\t" \
        "s_mul_i32 s64, s64, 0xa800\n\t" \
        "s_add_u32 s64, s64, s63\n\t" \
        "v_and_b32 v38, 48, v2\n\t" \
        "v_mul_u32_u24 v37, 0xc0, v19\n\t" \
        "v_lshlrev_b32 v36, 3, v19\n\t" \
        "s_mul_i32 s63, s62, 0xf80\n\t" \
        "v_add_u32 v39, 8, v36\n\t" \
        "s_add_u32 s57, s36, 0x1f800\n\t" \
        "s_add_u32 m0, s38, 0x3e00\n\t" \
        "s_nop 0\n\t" \
        "buffer_load_dwordx4 v13, s[16:19], s57 offen nt lds\n\t" \
        "s_add_u32 s63, s63, 0x100\n\t" \
        "v_bitop3_b32 v39, v39, 32, v38 bitop3:0x6a\n\t" \
        "v_add3_u32 v14, v37, v39, s63\n\t" \
        "v_add_u32 v39, 16, v36\n\t" \
        "s_add_u32 s63, s63, 0xc0\n\t" \
        "v_bitop3_b32 v39, v39, 32, v38 bitop3:0x6a\n\t" \
        "v_add3_u32 v15, v37, v39, s63\n\t" \
        "s_add_u32 s57, s36, 0x2a000\n\t" \
        "s_add_u32 m0, s38, 0x4d80\n\t" \
        "s_nop 0\n\t" \
        "buffer_load_dwordx4 v13, s[16:19], s57 offen nt lds\n\t" \
        "v_add_u32 v39, 24, v36\n\t" \
        "s_add_u32 s63, s63, 0xc0\n\t" \
        "v_bitop3_b32 v39, v39, 32, v38 bitop3:0x6a\n\t" \
        "v_add3_u32 v16, v37, v39, s63\n\t" \
        "v_add_u32 v20, 0xf000, v6\n\t" \
        "v_add_u32 v21, 0xf000, v7\n\t" \
        "v_add_u32 v22, 0xf000, v8\n\t" \
        "s_add_u32 s57, s40, 0x9000\n\t" \
        "s_add_u32 m0, s39, 0x9000\n\t" \
        "s_nop 0\n\t" \
        "buffer_load_dwordx4 v3, s[24:27], s57 offen offset:0  lds\n\t" \
        "v_add_u32 v38, s32, v18\n\t" \
        "v_add_u32 v39, s33, v19\n\t" \
        "v_lshl_add_u32 v38, v38, 1, -3\n\t" \
        "v_lshl_add_u32 v39, v39, 1, -3\n\t" \
        "v_cvt_f32_u32 v38, v38\n\t" \
        "v_cvt_f32_u32 v39, v39\n\t" \
        "v_lshrrev_b32 v38, 16, v38\n\t" \
        "buffer_load_dwordx4 v4, s[24:27], s57 offen offset:1024  lds\n\t" \
        "v_and_b32 v39, 0xffff0000, v39\n\t" \
        "v_or_b32 v38, v38, v39\n\t" \
        "v_and_b32 v38, 0x7f807f80, v38\n\t" \
        "v_subrev_u32 v23, 0x04810481, v38\n\t" \
        "s_lshl_b32 s45, s35, 2\n\t" \
        "v_mov_b32 v24, s45\n\t" \
        "buffer_load_dwordx4 v5, s[24:27], s57 offen offset:2048  lds\n\t" \
        "s_waitcnt vmcnt(13)\n\t" \
        "s_barrier\n\t" \
        "ds_read_b128 v[116:119], v14 offset:0\n\t" \
        "ds_read_b64_tr_b16 a[0:1], v6 offset:0\n\t" \
        "ds_read_b64_tr_b16 a[2:3], v6 offset:768\n\t" \
        "ds_read_b64_tr_b16 a[4:5], v7 offset:0\n\t" \
        "ds_read_b64_tr_b16 a[6:7], v7 offset:768\n\t" \
        "ds_read_b64_tr_b16 a[8:9], v8 offset:0\n\t" \
        "ds_read_b64_tr_b16 a[10:11], v8 offset:768\n\t" \
        "ds_read_b128 v[120:123], v14 offset:64\n\t" \
        "ds_read_b64_tr_b16 a[12:13], v6 offset:6144\n\t" \
        "ds_read_b64_tr_b16 a[14:15], v6 offset:6912\n\t" \
        "ds_read_b64_tr_b16 a[16:17], v7 offset:6144\n\t" \
        "ds_read_b64_tr_b16 a[18:19], v7 offset:6912\n\t" \
        "ds_read_b64_tr_b16 a[20:21], v8 offset:6144\n\t" \
        "ds_read_b64_tr_b16 a[22:23], v8 offset:6912\n\t" \
        "ds_read_b128 v[124:127], v14 offset:128\n\t" \
        "ds_read_b128 v[128:131], v15 offset:0\n\t" \
        "s_add_u32 s57, s36, 0x34800\n\t" \
        "s_add_u32 m0, s38, 0x5d00\n\t" \
        "s_waitcnt lgkmcnt(13)\n\t" \
        "v_mfma_f32_16x16x32_bf16 v[32:35], a[0:3], v[116:119], 0\n\t" \
        "s_waitcnt vmcnt(10) lgkmcnt(2)\n\t" \
        "s_barrier\n\t" \
        "ds_read_b64_tr_b16 a[24:25], v6 offset:12288\n\t" \
        "ds_read_b64_tr_b16 a[26:27], v6 offset:13056\n\t" \
        "ds_read_b64_tr_b16 a[28:29], v7 offset:12288\n\t" \
        "ds_read_b64_tr_b16 a[30:31], v7 offset:13056\n\t" \
        "ds_read_b64_tr_b16 a[32:33], v8 offset:12288\n\t" \
        "v_mfma_f32_16x16x32_bf16 v[36:39], a[4:7], v[116:119], 0\n\t" \
        "ds_read_b64_tr_b16 a[34:35], v8 offset:13056\n\t" \
        "ds_read_b64_tr_b16 a[36:37], v6 offset:18432\n\t" \
        "v_mfma_f32_16x16x32_bf16 v[40:43], a[8:11], v[116:119], 0\n\t" \
        "ds_read_b64_tr_b16 a[38:39], v6 offset:19200\n\t" \
        "ds_read_b64_tr_b16 a[40:41], v7 offset:18432\n\t" \
        "ds_read_b64_tr_b16 a[42:43], v7 offset:19200\n\t" \
        "ds_read_b64_tr_b16 a[44:45], v8 offset:18432\n\t" \
        "ds_read_b64_tr_b16 a[46:47], v8 offset:19200\n\t" \
        "v_mfma_f32_16x16x32_bf16 v[32:35], a[12:15], v[120:123], v[32:35]\n\t" \
        "buffer_load_dwordx4 v13, s[16:19], s57 offen nt lds\n\t" \
        "s_add_u32 s57, s36, 0x3f000\n\t" \
        "s_add_u32 m0, s38, 0x6c80\n\t" \
        "v_mfma_f32_16x16x32_bf16 v[36:39], a[16:19], v[120:123], v[36:39]\n\t" \
        "buffer_load_dwordx4 v13, s[16:19], s57 offen nt lds\n\t" \
        "v_mfma_f32_16x16x32_bf16 v[40:43], a[20:23], v[120:123], v[40:43]\n\t" \
        "s_waitcnt vmcnt(10) lgkmcnt(0)\n\t" \
        "s_barrier\n\t" \
        "ds_read_b128 v[132:135], v14 offset:7936\n\t" \
        "v_mfma_f32_16x16x32_bf16 v[32:35], a[24:27], v[124:127], v[32:35]\n\t" \
        "ds_read_b128 v[136:139], v14 offset:8000\n\t" \
        "s_add_u32 s57, s40, 0xc000\n\t" \
        "s_add_u32 m0, s39, 0xc000\n\t" \
        "v_mfma_f32_16x16x32_bf16 v[36:39], a[28:31], v[124:127], v[36:39]\n\t" \
        "ds_read_b128 v[140:143], v14 offset:8064\n\t" \
        "v_mfma_f32_16x16x32_bf16 v[40:43], a[32:35], v[124:127], v[40:43]\n\t" \
        "buffer_load_dwordx4 v3, s[24:27], s57 offen offset:0  lds\n\t" \
        "v_mfma_f32_16x16x32_bf16 v[32:35], a[36:39], v[128:131], v[32:35]\n\t" \
        "ds_read_b128 v[144:147], v15 offset:7936\n\t" \
        "v_mfma_f32_16x16x32_bf16 v[36:39], a[40:43], v[128:131], v[36:39]\n\t" \
        "ds_read_b128 v[116:119], v15 offset:64\n\t" \
        "v_mfma_f32_16x16x32_bf16 v[40:43], a[44:47], v[128:131], v[40:43]\n\t" \
        "s_waitcnt vmcnt(8)\n\t" \
        "s_barrier\n\t" \
        "ds_read_b64_tr_b16 a[48:49], v6 offset:24576\n\t" \
        "s_waitcnt lgkmcnt(5)\n\t" \
        "v_mfma_f32_16x16x32_bf16 v[44:47], a[0:3], v[132:135], 0\n\t" \
        "buffer_load_dwordx4 v4, s[24:27], s57 offen offset:1024  lds\n\t" \
        "v_mfma_f32_16x16x32_bf16 v[48:51], a[4:7], v[132:135], 0\n\t" \
        "ds_read_b64_tr_b16 a[50:51], v6 offset:25344\n\t" \
        "v_mfma_f32_16x16x32_bf16 v[52:55], a[8:11], v[132:135], 0\n\t" \
        "ds_read_b64_tr_b16 a[52:53], v7 offset:24576\n\t" \
        "s_waitcnt lgkmcnt(6)\n\t" \
        "v_mfma_f32_16x16x32_bf16 v[44:47], a[12:15], v[136:139], v[44:47]\n\t" \
        "ds_read_b64_tr_b16 a[54:55], v7 offset:25344\n\t" \
        "v_mfma_f32_16x16x32_bf16 v[48:51], a[16:19], v[136:139], v[48:51]\n\t" \
        "buffer_load_dwordx4 v5, s[24:27], s57 offen offset:2048  lds\n\t" \
        "v_mfma_f32_16x16x32_bf16 v[52:55], a[20:23], v[136:139], v[52:55]\n\t" \
        "ds_read_b64_tr_b16 a[56:57], v8 offset:24576\n\t" \
        "s_waitcnt lgkmcnt(6)\n\t" \
        "v_mfma_f32_16x16x32_bf16 v[44:47], a[24:27], v[140:143], v[44:47]\n\t" \
        "ds_read_b64_tr_b16 a[58:59], v8 offset:25344\n\t" \
        "s_add_u32 s57, s40, 0xf000\n\t" \
        "s_add_u32 m0, s39, 0xf000\n\t" \
        "v_mfma_f32_16x16x32_bf16 v[48:51], a[28:31], v[140:143], v[48:51]\n\t" \
        "ds_read_b128 v[120:123], v15 offset:8000\n\t" \
        "v_mfma_f32_16x16x32_bf16 v[52:55], a[32:35], v[140:143], v[52:55]\n\t" \
        "ds_read_b64_tr_b16 a[60:61], v6 offset:30720\n\t" \
        "ds_read_b64_tr_b16 a[62:63], v6 offset:31488\n\t" \
        "ds_read_b64_tr_b16 a[64:65], v7 offset:30720\n\t" \
        "ds_read_b64_tr_b16 a[66:67], v7 offset:31488\n\t" \
        "ds_read_b64_tr_b16 a[68:69], v8 offset:30720\n\t" \
        "ds_read_b64_tr_b16 a[70:71], v8 offset:31488\n\t" \
        "v_mfma_f32_16x16x32_bf16 v[44:47], a[36:39], v[144:147], v[44:47]\n\t" \
        "buffer_load_dwordx4 v3, s[24:27], s57 offen offset:0  lds\n\t" \
        "v_mfma_f32_16x16x32_bf16 v[48:51], a[40:43], v[144:147], v[48:51]\n\t" \
        "ds_read_b128 v[124:127], v15 offset:128\n\t" \
        "v_mfma_f32_16x16x32_bf16 v[52:55], a[44:47], v[144:147], v[52:55]\n\t" \
        "s_waitcnt vmcnt(9) lgkmcnt(1)\n\t" \
        "s_barrier\n\t" \
        "ds_read_b128 v[128:131], v15 offset:8064\n\t" \
        "v_mfma_f32_16x16x32_bf16 v[32:35], a[48:51], v[116:119], v[32:35]\n\t" \
        "buffer_load_dwordx4 v4, s[24:27], s57 offen offset:1024  lds\n\t" \
        "v_mfma_f32_16x16x32_bf16 v[36:39], a[52:55], v[116:119], v[36:39]\n\t" \
        "ds_read_b128 v[132:135], v14 offset:15872\n\t" \
        "v_mfma_f32_16x16x32_bf16 v[40:43], a[56:59], v[116:119], v[40:43]\n\t" \
        "ds_read_b128 v[136:139], v14 offset:15936\n\t" \
        "v_mfma_f32_16x16x32_bf16 v[44:47], a[48:51], v[120:123], v[44:47]\n\t" \
        "ds_read_b128 v[140:143], v14 offset:16000\n\t" \
        "v_mfma_f32_16x16x32_bf16 v[48:51], a[52:55], v[120:123], v[48:51]\n\t" \
        "buffer_load_dwordx4 v5, s[24:27], s57 offen offset:2048  lds\n\t" \
        "v_mfma_f32_16x16x32_bf16 v[52:55], a[56:59], v[120:123], v[52:55]\n\t" \
        "ds_read_b128 v[144:147], v15 offset:15872\n\t" \
        "s_waitcnt lgkmcnt(5)\n\t" \
        "v_mfma_f32_16x16x32_bf16 v[32:35], a[60:63], v[124:127], v[32:35]\n\t" \
        "ds_read_b128 v[116:119], v15 offset:15936\n\t" \
        "s_add_u32 s57, s36, 0x49800\n\t" \
        "s_add_u32 m0, s38, 0x7c00\n\t" \
        "v_mfma_f32_16x16x32_bf16 v[36:39], a[64:67], v[124:127], v[36:39]\n\t" \
        "ds_read_b128 v[120:123], v15 offset:16000\n\t" \
        "v_mfma_f32_16x16x32_bf16 v[40:43], a[68:71], v[124:127], v[40:43]\n\t" \
        "buffer_load_dwordx4 v13, s[16:19], s57 offen nt lds\n\t" \
        "s_waitcnt lgkmcnt(6)\n\t" \
        "v_mfma_f32_16x16x32_bf16 v[44:47], a[60:63], v[128:131], v[44:47]\n\t" \
        "ds_read_b128 v[124:127], v16 offset:0\n\t" \
        "v_mfma_f32_16x16x32_bf16 v[48:51], a[64:67], v[128:131], v[48:51]\n\t" \
        "s_add_u32 s57, s36, 0x54000\n\t" \
        "s_add_u32 m0, s38, 0x8b80\n\t" \
        "v_mfma_f32_16x16x32_bf16 v[52:55], a[68:71], v[128:131], v[52:55]\n\t" \
        "s_waitcnt vmcnt(9)\n\t" \
        "s_barrier\n\t" \
        "ds_read_b64_tr_b16 a[72:73], v6 offset:36864\n\t" \
        "s_waitcnt lgkmcnt(5)\n\t" \
        "v_mfma_f32_16x16x32_bf16 v[56:59], a[0:3], v[132:135], 0\n\t" \
        "buffer_load_dwordx4 v13, s[16:19], s57 offen nt lds\n\t" \
        "v_mfma_f32_16x16x32_bf16 v[60:63], a[4:7], v[132:135], 0\n\t" \
        "ds_read_b64_tr_b16 a[74:75], v6 offset:37632\n\t" \
        "v_mfma_f32_16x16x32_bf16 v[64:67], a[8:11], v[132:135], 0\n\t" \
        "ds_read_b64_tr_b16 a[76:77], v7 offset:36864\n\t" \
        "s_add_u32 s57, s40, 0x12000\n\t" \
        "s_add_u32 m0, s39, 0x12000\n\t" \
        "v_mfma_f32_16x16x32_bf16 v[56:59], a[12:15], v[136:139], v[56:59]\n\t" \
        "ds_read_b64_tr_b16 a[78:79], v7 offset:37632\n\t" \
        "v_mfma_f32_16x16x32_bf16 v[60:63], a[16:19], v[136:139], v[60:63]\n\t" \
        "buffer_load_dwordx4 v3, s[24:27], s57 offen offset:0  lds\n\t" \
        "v_mfma_f32_16x16x32_bf16 v[64:67], a[20:23], v[136:139], v[64:67]\n\t" \
        "ds_read_b64_tr_b16 a[80:81], v8 offset:36864\n\t" \
        "v_mfma_f32_16x16x32_bf16 v[56:59], a[24:27], v[140:143], v[56:59]\n\t" \
        "ds_read_b64_tr_b16 a[82:83], v8 offset:37632\n\t" \
        "v_mfma_f32_16x16x32_bf16 v[60:63], a[28:31], v[140:143], v[60:63]\n\t" \
        "ds_read_b128 v[128:131], v16 offset:7936\n\t" \
        "v_mfma_f32_16x16x32_bf16 v[64:67], a[32:35], v[140:143], v[64:67]\n\t" \
        "buffer_load_dwordx4 v4, s[24:27], s57 offen offset:1024  lds\n\t" \
        "s_waitcnt lgkmcnt(6)\n\t" \
        "v_mfma_f32_16x16x32_bf16 v[56:59], a[36:39], v[144:147], v[56:59]\n\t" \
        "ds_read_b128 v[132:135], v16 offset:15872\n\t" \
        "v_mfma_f32_16x16x32_bf16 v[60:63], a[40:43], v[144:147], v[60:63]\n\t" \
        "ds_read_b128 v[136:139], v16 offset:64\n\t" \
        "v_mfma_f32_16x16x32_bf16 v[64:67], a[44:47], v[144:147], v[64:67]\n\t" \
        "ds_read_b64_tr_b16 a[84:85], v6 offset:43008\n\t" \
        "v_mfma_f32_16x16x32_bf16 v[56:59], a[48:51], v[116:119], v[56:59]\n\t" \
        "buffer_load_dwordx4 v5, s[24:27], s57 offen offset:2048  lds\n\t" \
        "v_mfma_f32_16x16x32_bf16 v[60:63], a[52:55], v[116:119], v[60:63]\n\t" \
        "ds_read_b64_tr_b16 a[86:87], v6 offset:43776\n\t" \
        "v_mfma_f32_16x16x32_bf16 v[64:67], a[56:59], v[116:119], v[64:67]\n\t" \
        "ds_read_b64_tr_b16 a[88:89], v7 offset:43008\n\t" \
        "ds_read_b64_tr_b16 a[90:91], v7 offset:43776\n\t" \
        "ds_read_b64_tr_b16 a[92:93], v8 offset:43008\n\t" \
        "ds_read_b64_tr_b16 a[94:95], v8 offset:43776\n\t" \
        "s_add_u32 s57, s40, 0x15000\n\t" \
        "s_add_u32 m0, s39, 0x15000\n\t" \
        "v_mfma_f32_16x16x32_bf16 v[56:59], a[60:63], v[120:123], v[56:59]\n\t" \
        "ds_read_b128 v[140:143], v16 offset:8000\n\t" \
        "v_mfma_f32_16x16x32_bf16 v[60:63], a[64:67], v[120:123], v[60:63]\n\t" \
        "buffer_load_dwordx4 v3, s[24:27], s57 offen offset:0  lds\n\t" \
        "v_mfma_f32_16x16x32_bf16 v[64:67], a[68:71], v[120:123], v[64:67]\n\t" \
        "s_waitcnt vmcnt(12) lgkmcnt(1)\n\t" \
        "s_barrier\n\t" \
        "ds_read_b128 v[144:147], v16 offset:15936\n\t" \
        "v_mfma_f32_16x16x32_bf16 v[32:35], a[72:75], v[124:127], v[32:35]\n\t" \
        "ds_read_b128 v[116:119], v14 offset:23808\n\t" \
        "v_mfma_f32_16x16x32_bf16 v[36:39], a[76:79], v[124:127], v[36:39]\n\t" \
        "ds_read_b128 v[120:123], v14 offset:23872\n\t" \
        "v_mfma_f32_16x16x32_bf16 v[40:43], a[80:83], v[124:127], v[40:43]\n\t" \
        "buffer_load_dwordx4 v4, s[24:27], s57 offen offset:1024  lds\n\t" \
        "v_mfma_f32_16x16x32_bf16 v[44:47], a[72:75], v[128:131], v[44:47]\n\t" \
        "ds_read_b128 v[124:127], v14 offset:23936\n\t" \
        "v_mfma_f32_16x16x32_bf16 v[48:51], a[76:79], v[128:131], v[48:51]\n\t" \
        "v_mfma_f32_16x16x32_bf16 v[52:55], a[80:83], v[128:131], v[52:55]\n\t" \
        "ds_read_b128 v[128:131], v15 offset:23808\n\t" \
        "v_mfma_f32_16x16x32_bf16 v[56:59], a[72:75], v[132:135], v[56:59]\n\t" \
        "buffer_load_dwordx4 v5, s[24:27], s57 offen offset:2048  lds\n\t" \
        "v_mfma_f32_16x16x32_bf16 v[60:63], a[76:79], v[132:135], v[60:63]\n\t" \
        "v_mfma_f32_16x16x32_bf16 v[64:67], a[80:83], v[132:135], v[64:67]\n\t" \
        "ds_read_b128 v[132:135], v15 offset:23872\n\t" \
        "v_mfma_f32_16x16x32_bf16 v[32:35], a[84:87], v[136:139], v[32:35]\n\t" \
        "v_mfma_f32_16x16x32_bf16 v[36:39], a[88:91], v[136:139], v[36:39]\n\t" \
        "s_add_u32 s57, s40, 0x18000\n\t" \
        "s_mov_b32 m0, s39\n\t" \
        "v_mfma_f32_16x16x32_bf16 v[40:43], a[92:95], v[136:139], v[40:43]\n\t" \
        "ds_read_b128 v[136:139], v15 offset:23936\n\t" \
        "s_waitcnt lgkmcnt(3)\n\t" \
        "v_mfma_f32_16x16x32_bf16 v[44:47], a[84:87], v[140:143], v[44:47]\n\t" \
        "buffer_load_dwordx4 v3, s[24:27], s57 offen offset:0  lds\n\t" \
        "v_mfma_f32_16x16x32_bf16 v[48:51], a[88:91], v[140:143], v[48:51]\n\t" \
        "v_mfma_f32_16x16x32_bf16 v[52:55], a[92:95], v[140:143], v[52:55]\n\t" \
        "ds_read_b128 v[140:143], v16 offset:23808\n\t" \
        "v_mfma_f32_16x16x32_bf16 v[56:59], a[84:87], v[144:147], v[56:59]\n\t" \
        "v_mfma_f32_16x16x32_bf16 v[60:63], a[88:91], v[144:147], v[60:63]\n\t" \
        "v_mfma_f32_16x16x32_bf16 v[64:67], a[92:95], v[144:147], v[64:67]\n\t" \
        "ds_read_b128 v[144:147], v16 offset:23872\n\t" \
        "v_mfma_f32_16x16x32_bf16 v[68:71], a[0:3], v[116:119], 0\n\t" \
        "buffer_load_dwordx4 v4, s[24:27], s57 offen offset:1024  lds\n\t" \
        "v_mfma_f32_16x16x32_bf16 v[72:75], a[4:7], v[116:119], 0\n\t" \
        "v_mfma_f32_16x16x32_bf16 v[76:79], a[8:11], v[116:119], 0\n\t" \
        "ds_read_b128 v[116:119], v16 offset:128\n\t" \
        "v_mfma_f32_16x16x32_bf16 v[68:71], a[12:15], v[120:123], v[68:71]\n\t" \
        "v_mfma_f32_16x16x32_bf16 v[72:75], a[16:19], v[120:123], v[72:75]\n\t" \
        "v_mfma_f32_16x16x32_bf16 v[76:79], a[20:23], v[120:123], v[76:79]\n\t" \
        "s_waitcnt vmcnt(13)\n\t" \
        "s_barrier\n\t" \
        "ds_read_b64_tr_b16 a[96:97], v6 offset:49152\n\t" \
        "v_mfma_f32_16x16x32_bf16 v[68:71], a[24:27], v[124:127], v[68:71]\n\t" \
        "buffer_load_dwordx4 v5, s[24:27], s57 offen offset:2048  lds\n\t" \
        "v_mfma_f32_16x16x32_bf16 v[72:75], a[28:31], v[124:127], v[72:75]\n\t" \
        "ds_read_b64_tr_b16 a[98:99], v6 offset:49920\n\t" \
        "v_mfma_f32_16x16x32_bf16 v[76:79], a[32:35], v[124:127], v[76:79]\n\t" \
        "ds_read_b64_tr_b16 a[100:101], v7 offset:49152\n\t" \
        "s_waitcnt lgkmcnt(4)\n\t" \
        "v_mfma_f32_16x16x32_bf16 v[68:71], a[36:39], v[128:131], v[68:71]\n\t" \
        "ds_read_b64_tr_b16 a[102:103], v7 offset:49920\n\t" \
        "v_mfma_f32_16x16x32_bf16 v[72:75], a[40:43], v[128:131], v[72:75]\n\t" \
        "ds_read_b64_tr_b16 a[104:105], v8 offset:49152\n\t" \
        "s_add_u32 s57, s40, 0x1b000\n\t" \
        "s_add_u32 m0, s39, 0x3000\n\t" \
        "v_mfma_f32_16x16x32_bf16 v[76:79], a[44:47], v[128:131], v[76:79]\n\t" \
        "ds_read_b64_tr_b16 a[106:107], v8 offset:49920\n\t" \
        "v_mfma_f32_16x16x32_bf16 v[68:71], a[48:51], v[132:135], v[68:71]\n\t" \
        "buffer_load_dwordx4 v3, s[24:27], s57 offen offset:0  lds\n\t" \
        "v_mfma_f32_16x16x32_bf16 v[72:75], a[52:55], v[132:135], v[72:75]\n\t" \
        "ds_read_b128 v[120:123], v16 offset:8064\n\t" \
        "v_mfma_f32_16x16x32_bf16 v[76:79], a[56:59], v[132:135], v[76:79]\n\t" \
        "ds_read_b128 v[124:127], v16 offset:16000\n\t" \
        "v_mfma_f32_16x16x32_bf16 v[68:71], a[60:63], v[136:139], v[68:71]\n\t" \
        "ds_read_b128 v[128:131], v16 offset:23936\n\t" \
        "v_mfma_f32_16x16x32_bf16 v[72:75], a[64:67], v[136:139], v[72:75]\n\t" \
        "ds_read_b128 v[132:135], v14 offset:3968\n\t" \
        "v_mfma_f32_16x16x32_bf16 v[76:79], a[68:71], v[136:139], v[76:79]\n\t" \
        "ds_read_b64_tr_b16 a[108:109], v6 offset:55296\n\t" \
        "v_mfma_f32_16x16x32_bf16 v[68:71], a[72:75], v[140:143], v[68:71]\n\t" \
        "buffer_load_dwordx4 v4, s[24:27], s57 offen offset:1024  lds\n\t" \
        "v_mfma_f32_16x16x32_bf16 v[72:75], a[76:79], v[140:143], v[72:75]\n\t" \
        "ds_read_b64_tr_b16 a[110:111], v6 offset:56064\n\t" \
        "v_mfma_f32_16x16x32_bf16 v[76:79], a[80:83], v[140:143], v[76:79]\n\t" \
        "ds_read_b64_tr_b16 a[112:113], v7 offset:55296\n\t" \
        "v_mfma_f32_16x16x32_bf16 v[68:71], a[84:87], v[144:147], v[68:71]\n\t" \
        "ds_read_b64_tr_b16 a[114:115], v7 offset:56064\n\t" \
        "v_mfma_f32_16x16x32_bf16 v[72:75], a[88:91], v[144:147], v[72:75]\n\t" \
        "ds_read_b64_tr_b16 a[116:117], v8 offset:55296\n\t" \
        "v_mfma_f32_16x16x32_bf16 v[76:79], a[92:95], v[144:147], v[76:79]\n\t" \
        "ds_read_b64_tr_b16 a[118:119], v8 offset:56064\n\t" \
        "s_waitcnt lgkmcnt(7)\n\t" \
        "v_mfma_f32_16x16x32_bf16 v[32:35], a[96:99], v[116:119], v[32:35]\n\t" \
        "buffer_load_dwordx4 v5, s[24:27], s57 offen offset:2048  lds\n\t" \
        "v_mfma_f32_16x16x32_bf16 v[36:39], a[100:103], v[116:119], v[36:39]\n\t" \
        "ds_read_b128 v[136:139], v14 offset:11904\n\t" \
        "v_mfma_f32_16x16x32_bf16 v[40:43], a[104:107], v[116:119], v[40:43]\n\t" \
        "s_waitcnt vmcnt(14) lgkmcnt(1)\n\t" \
        "s_barrier\n\t" \
        "ds_read_b128 v[140:143], v14 offset:19840\n\t" \
        "v_mfma_f32_16x16x32_bf16 v[44:47], a[96:99], v[120:123], v[44:47]\n\t" \
        "ds_read_b128 v[144:147], v14 offset:4032\n\t" \
        "v_mfma_f32_16x16x32_bf16 v[48:51], a[100:103], v[120:123], v[48:51]\n\t" \
        "ds_read_b64_tr_b16 a[120:121], v20 offset:0\n\t" \
        "v_mfma_f32_16x16x32_bf16 v[52:55], a[104:107], v[120:123], v[52:55]\n\t" \
        "ds_read_b64_tr_b16 a[122:123], v20 offset:768\n\t" \
        "v_mfma_f32_16x16x32_bf16 v[56:59], a[96:99], v[124:127], v[56:59]\n\t" \
        "buffer_load_dwordx4 v[148:151], v17, s[20:23], s37 offen \n\t" \
        "v_mfma_f32_16x16x32_bf16 v[60:63], a[100:103], v[124:127], v[60:63]\n\t" \
        "ds_read_b64_tr_b16 a[124:125], v21 offset:0\n\t" \
        "v_mfma_f32_16x16x32_bf16 v[64:67], a[104:107], v[124:127], v[64:67]\n\t" \
        "ds_read_b64_tr_b16 a[126:127], v21 offset:768\n\t" \
        "v_mfma_f32_16x16x32_bf16 v[68:71], a[96:99], v[128:131], v[68:71]\n\t" \
        "ds_read_b64_tr_b16 a[128:129], v22 offset:0\n\t" \
        "v_mfma_f32_16x16x32_bf16 v[72:75], a[100:103], v[128:131], v[72:75]\n\t" \
        "ds_read_b64_tr_b16 a[130:131], v22 offset:768\n\t" \
        "v_mfma_f32_16x16x32_bf16 v[76:79], a[104:107], v[128:131], v[76:79]\n\t" \
        "ds_read_b128 v[116:119], v14 offset:11968\n\t" \
        "v_mfma_f32_16x16x32_bf16 v[32:35], a[108:111], v[132:135], v[32:35]\n\t" \
        "buffer_load_dwordx4 v[152:155], v17, s[20:23], s37 offen offset:16 \n\t" \
        "v_mfma_f32_16x16x32_bf16 v[36:39], a[112:115], v[132:135], v[36:39]\n\t" \
        "ds_read_b128 v[120:123], v14 offset:19904\n\t" \
        "v_mfma_f32_16x16x32_bf16 v[40:43], a[116:119], v[132:135], v[40:43]\n\t" \
        "ds_read_b128 v[124:127], v14 offset:4096\n\t" \
        "s_waitcnt lgkmcnt(7)\n\t" \
        "v_mfma_f32_16x16x32_bf16 v[44:47], a[108:111], v[136:139], v[44:47]\n\t" \
        "ds_read_b64_tr_b16 a[132:133], v20 offset:6144\n\t" \
        "v_mfma_f32_16x16x32_bf16 v[48:51], a[112:115], v[136:139], v[48:51]\n\t" \
        "ds_read_b64_tr_b16 a[134:135], v20 offset:6912\n\t" \
        "v_mfma_f32_16x16x32_bf16 v[52:55], a[116:119], v[136:139], v[52:55]\n\t" \
        "ds_read_b64_tr_b16 a[136:137], v21 offset:6144\n\t" \
        "ds_read_b64_tr_b16 a[138:139], v21 offset:6912\n\t" \
        "ds_read_b64_tr_b16 a[140:141], v22 offset:6144\n\t" \
        "ds_read_b64_tr_b16 a[142:143], v22 offset:6912\n\t" \
        "v_mfma_f32_16x16x32_bf16 v[56:59], a[108:111], v[140:143], v[56:59]\n\t" \
        "buffer_load_dword v156, v17, s[20:23], s37 offen offset:32 \n\t" \
        "v_mfma_f32_16x16x32_bf16 v[60:63], a[112:115], v[140:143], v[60:63]\n\t" \
        "ds_read_b128 v[128:131], v14 offset:12032\n\t" \
        "v_mfma_f32_16x16x32_bf16 v[64:67], a[116:119], v[140:143], v[64:67]\n\t" \
        "s_waitcnt vmcnt(15) lgkmcnt(1)\n\t" \
        "s_barrier\n\t" \
        "ds_read_b128 v[132:135], v14 offset:19968\n\t" \
        "v_mfma_f32_16x16x32_bf16 v[32:35], a[120:123], v[144:147], v[32:35]\n\t" \
        "ds_read_b128 v[136:139], v14 offset:27776\n\t" \
        "v_mfma_f32_16x16x32_bf16 v[36:39], a[124:127], v[144:147], v[36:39]\n\t" \
        "ds_read_b128 v[140:143], v14 offset:27840\n\t" \
        "s_add_u32 s57, s40, 0x1e000\n\t" \
        "s_add_u32 m0, s39, 0x6000\n\t" \
        "v_mfma_f32_16x16x32_bf16 v[40:43], a[128:131], v[144:147], v[40:43]\n\t" \
        "ds_read_b128 v[144:147], v14 offset:27904\n\t" \
        "v_mfma_f32_16x16x32_bf16 v[44:47], a[120:123], v[116:119], v[44:47]\n\t" \
        "buffer_load_dwordx4 v3, s[24:27], s57 offen offset:0  lds\n\t" \
        "v_mfma_f32_16x16x32_bf16 v[48:51], a[124:127], v[116:119], v[48:51]\n\t" \
        "v_mfma_f32_16x16x32_bf16 v[52:55], a[128:131], v[116:119], v[52:55]\n\t" \
        "ds_read_b128 v[116:119], v15 offset:3968\n\t" \
        "v_mfma_f32_16x16x32_bf16 v[56:59], a[120:123], v[120:123], v[56:59]\n\t" \
        "v_mfma_f32_16x16x32_bf16 v[60:63], a[124:127], v[120:123], v[60:63]\n\t" \
        "v_mfma_f32_16x16x32_bf16 v[64:67], a[128:131], v[120:123], v[64:67]\n\t" \
        "ds_read_b128 v[120:123], v15 offset:11904\n\t" \
        "v_mfma_f32_16x16x32_bf16 v[32:35], a[132:135], v[124:127], v[32:35]\n\t" \
        "buffer_load_dwordx4 v4, s[24:27], s57 offen offset:1024  lds\n\t" \
        "v_mfma_f32_16x16x32_bf16 v[36:39], a[136:139], v[124:127], v[36:39]\n\t" \
        "v_mfma_f32_16x16x32_bf16 v[40:43], a[140:143], v[124:127], v[40:43]\n\t" \
        "ds_read_b128 v[124:127], v15 offset:19840\n\t" \
        "s_waitcnt lgkmcnt(3)\n\t" \
        "v_mfma_f32_16x16x32_bf16 v[44:47], a[132:135], v[128:131], v[44:47]\n\t" \
        "v_mfma_f32_16x16x32_bf16 v[48:51], a[136:139], v[128:131], v[48:51]\n\t" \
        "v_mfma_f32_16x16x32_bf16 v[52:55], a[140:143], v[128:131], v[52:55]\n\t" \
        "ds_read_b128 v[128:131], v15 offset:27776\n\t" \
        "v_mfma_f32_16x16x32_bf16 v[56:59], a[132:135], v[132:135], v[56:59]\n\t" \
        "buffer_load_dwordx4 v5, s[24:27], s57 offen offset:2048  lds\n\t" \
        "v_mfma_f32_16x16x32_bf16 v[60:63], a[136:139], v[132:135], v[60:63]\n\t" \
        "v_mfma_f32_16x16x32_bf16 v[64:67], a[140:143], v[132:135], v[64:67]\n\t" \
        "s_waitcnt vmcnt(15)\n\t" \
        "s_barrier\n\t" \
        "ds_read_b64_tr_b16 a[144:145], v20 offset:12288\n\t" \
        "v_mfma_f32_16x16x32_bf16 v[68:71], a[108:111], v[136:139], v[68:71]\n\t" \
        "ds_read_b64_tr_b16 a[146:147], v20 offset:13056\n\t" \
        "v_mfma_f32_16x16x32_bf16 v[72:75], a[112:115], v[136:139], v[72:75]\n\t" \
        "ds_read_b64_tr_b16 a[148:149], v21 offset:12288\n\t" \
        "s_add_u32 s57, s40, 0x21000\n\t" \
        "s_add_u32 m0, s39, 0x9000\n\t" \
        "v_mfma_f32_16x16x32_bf16 v[76:79], a[116:119], v[136:139], v[76:79]\n\t" \
        "ds_read_b64_tr_b16 a[150:151], v21 offset:13056\n\t" \
        "v_mfma_f32_16x16x32_bf16 v[68:71], a[120:123], v[140:143], v[68:71]\n\t" \
        "ds_read_b64_tr_b16 a[152:153], v22 offset:12288\n\t" \
        "v_mfma_f32_16x16x32_bf16 v[72:75], a[124:127], v[140:143], v[72:75]\n\t" \
        "ds_read_b64_tr_b16 a[154:155], v22 offset:13056\n\t" \
        "v_mfma_f32_16x16x32_bf16 v[76:79], a[128:131], v[140:143], v[76:79]\n\t" \
        "buffer_load_dwordx4 v3, s[24:27], s57 offen offset:0  lds\n\t" \
        "v_mfma_f32_16x16x32_bf16 v[68:71], a[132:135], v[144:147], v[68:71]\n\t" \
        "ds_read_b128 v[132:135], v15 offset:4032\n\t" \
        "v_mfma_f32_16x16x32_bf16 v[72:75], a[136:139], v[144:147], v[72:75]\n\t" \
        "ds_read_b64_tr_b16 a[156:157], v20 offset:18432\n\t" \
        "v_mfma_f32_16x16x32_bf16 v[76:79], a[140:143], v[144:147], v[76:79]\n\t" \
        "ds_read_b64_tr_b16 a[158:159], v20 offset:19200\n\t" \
        "s_waitcnt lgkmcnt(7)\n\t" \
        "v_mfma_f32_16x16x32_bf16 v[32:35], a[144:147], v[116:119], v[32:35]\n\t" \
        "buffer_load_dwordx4 v4, s[24:27], s57 offen offset:1024  lds\n\t" \
        "s_waitcnt lgkmcnt(5)\n\t" \
        "v_mfma_f32_16x16x32_bf16 v[36:39], a[148:151], v[116:119], v[36:39]\n\t" \
        "ds_read_b64_tr_b16 a[160:161], v21 offset:18432\n\t" \
        "s_waitcnt lgkmcnt(4)\n\t" \
        "v_mfma_f32_16x16x32_bf16 v[40:43], a[152:155], v[116:119], v[40:43]\n\t" \
        "ds_read_b64_tr_b16 a[162:163], v21 offset:19200\n\t" \
        "ds_read_b64_tr_b16 a[164:165], v22 offset:18432\n\t" \
        "ds_read_b64_tr_b16 a[166:167], v22 offset:19200\n\t" \
        "v_mfma_f32_16x16x32_bf16 v[44:47], a[144:147], v[120:123], v[44:47]\n\t" \
        "ds_read_b128 v[136:139], v15 offset:11968\n\t" \
        "v_mfma_f32_16x16x32_bf16 v[48:51], a[148:151], v[120:123], v[48:51]\n\t" \
        "ds_read_b128 v[140:143], v15 offset:19904\n\t" \
        "v_mfma_f32_16x16x32_bf16 v[52:55], a[152:155], v[120:123], v[52:55]\n\t" \
        "s_waitcnt vmcnt(14)\n\t" \
        "s_barrier\n\t" \
        "ds_read_b128 v[144:147], v15 offset:27840\n\t" \
        "v_mfma_f32_16x16x32_bf16 v[56:59], a[144:147], v[124:127], v[56:59]\n\t" \
        "buffer_load_dwordx4 v5, s[24:27], s57 offen offset:2048  lds\n\t" \
        "v_mfma_f32_16x16x32_bf16 v[60:63], a[148:151], v[124:127], v[60:63]\n\t" \
        "ds_read_b128 v[116:119], v15 offset:4096\n\t" \
        "v_mfma_f32_16x16x32_bf16 v[64:67], a[152:155], v[124:127], v[64:67]\n\t" \
        "ds_read_b64_tr_b16 a[168:169], v20 offset:24576\n\t" \
        "v_mfma_f32_16x16x32_bf16 v[68:71], a[144:147], v[128:131], v[68:71]\n\t" \
        "ds_read_b64_tr_b16 a[170:171], v20 offset:25344\n\t" \
        "v_mfma_f32_16x16x32_bf16 v[72:75], a[148:151], v[128:131], v[72:75]\n\t" \
        "ds_read_b64_tr_b16 a[172:173], v21 offset:24576\n\t" \
        "s_add_u32 s57, s40, 0x24000\n\t" \
        "s_add_u32 m0, s39, 0xc000\n\t" \
        "v_mfma_f32_16x16x32_bf16 v[76:79], a[152:155], v[128:131], v[76:79]\n\t" \
        "ds_read_b64_tr_b16 a[174:175], v21 offset:25344\n\t" \
        "s_waitcnt lgkmcnt(7)\n\t" \
        "v_mfma_f32_16x16x32_bf16 v[32:35], a[156:159], v[132:135], v[32:35]\n\t" \
        "buffer_load_dwordx4 v3, s[24:27], s57 offen offset:0  lds\n\t" \
        "v_mfma_f32_16x16x32_bf16 v[36:39], a[160:163], v[132:135], v[36:39]\n\t" \
        "ds_read_b64_tr_b16 a[176:177], v22 offset:24576\n\t" \
        "v_mfma_f32_16x16x32_bf16 v[40:43], a[164:167], v[132:135], v[40:43]\n\t" \
        "ds_read_b64_tr_b16 a[178:179], v22 offset:25344\n\t" \
        "v_mfma_f32_16x16x32_bf16 v[44:47], a[156:159], v[136:139], v[44:47]\n\t" \
        "ds_read_b128 v[120:123], v15 offset:12032\n\t" \
        "v_mfma_f32_16x16x32_bf16 v[48:51], a[160:163], v[136:139], v[48:51]\n\t" \
        "ds_read_b128 v[124:127], v15 offset:19968\n\t" \
        "v_mfma_f32_16x16x32_bf16 v[52:55], a[164:167], v[136:139], v[52:55]\n\t" \
        "ds_read_b128 v[128:131], v15 offset:27904\n\t" \
        "s_waitcnt lgkmcnt(7)\n\t" \
        "v_mfma_f32_16x16x32_bf16 v[56:59], a[156:159], v[140:143], v[56:59]\n\t" \
        "buffer_load_dwordx4 v4, s[24:27], s57 offen offset:1024  lds\n\t" \
        "v_mfma_f32_16x16x32_bf16 v[60:63], a[160:163], v[140:143], v[60:63]\n\t" \
        "ds_read_b128 v[132:135], v16 offset:3968\n\t" \
        "v_mfma_f32_16x16x32_bf16 v[64:67], a[164:167], v[140:143], v[64:67]\n\t" \
        "ds_read_b64_tr_b16 a[180:181], v20 offset:30720\n\t" \
        "v_mfma_f32_16x16x32_bf16 v[68:71], a[156:159], v[144:147], v[68:71]\n\t" \
        "ds_read_b64_tr_b16 a[182:183], v20 offset:31488\n\t" \
        "v_mfma_f32_16x16x32_bf16 v[72:75], a[160:163], v[144:147], v[72:75]\n\t" \
        "ds_read_b64_tr_b16 a[184:185], v21 offset:30720\n\t" \
        "v_mfma_f32_16x16x32_bf16 v[76:79], a[164:167], v[144:147], v[76:79]\n\t" \
        "ds_read_b64_tr_b16 a[186:187], v21 offset:31488\n\t" \
        "v_mfma_f32_16x16x32_bf16 v[32:35], a[168:171], v[116:119], v[32:35]\n\t" \
        "buffer_load_dwordx4 v5, s[24:27], s57 offen offset:2048  lds\n\t" \
        "s_waitcnt lgkmcnt(6)\n\t" \
        "v_mfma_f32_16x16x32_bf16 v[36:39], a[172:175], v[116:119], v[36:39]\n\t" \
        "ds_read_b64_tr_b16 a[188:189], v22 offset:30720\n\t" \
        "v_mfma_f32_16x16x32_bf16 v[40:43], a[176:179], v[116:119], v[40:43]\n\t" \
        "ds_read_b64_tr_b16 a[190:191], v22 offset:31488\n\t" \
        "v_mfma_f32_16x16x32_bf16 v[44:47], a[168:171], v[120:123], v[44:47]\n\t" \
        "ds_read_b128 v[136:139], v16 offset:11904\n\t" \
        "v_mfma_f32_16x16x32_bf16 v[48:51], a[172:175], v[120:123], v[48:51]\n\t" \
        "ds_read_b128 v[140:143], v16 offset:19840\n\t" \
        "s_add_u32 s57, s40, 0x27000\n\t" \
        "s_add_u32 m0, s39, 0xf000\n\t" \
        "v_mfma_f32_16x16x32_bf16 v[52:55], a[176:179], v[120:123], v[52:55]\n\t" \
        "s_waitcnt vmcnt(15)\n\t" \
        "s_barrier\n\t" \
        "ds_read_b128 v[144:147], v16 offset:27776\n\t" \
        "v_mfma_f32_16x16x32_bf16 v[56:59], a[168:171], v[124:127], v[56:59]\n\t" \
        "buffer_load_dwordx4 v3, s[24:27], s57 offen offset:0  lds\n\t" \
        "v_mfma_f32_16x16x32_bf16 v[60:63], a[172:175], v[124:127], v[60:63]\n\t" \
        "ds_read_b128 v[116:119], v16 offset:4032\n\t" \
        "v_mfma_f32_16x16x32_bf16 v[64:67], a[176:179], v[124:127], v[64:67]\n\t" \
        "ds_read_b64_tr_b16 a[192:193], v6 offset:0\n\t" \
        "s_waitcnt lgkmcnt(7)\n\t" \
        "v_mfma_f32_16x16x32_bf16 v[68:71], a[168:171], v[128:131], v[68:71]\n\t" \
        "ds_read_b64_tr_b16 a[194:195], v6 offset:768\n\t" \
        "v_mfma_f32_16x16x32_bf16 v[72:75], a[172:175], v[128:131], v[72:75]\n\t" \
        "ds_read_b64_tr_b16 a[196:197], v7 offset:0\n\t" \
        "v_mfma_f32_16x16x32_bf16 v[76:79], a[176:179], v[128:131], v[76:79]\n\t" \
        "ds_read_b64_tr_b16 a[198:199], v7 offset:768\n\t" \
        "v_mfma_f32_16x16x32_bf16 v[32:35], a[180:183], v[132:135], v[32:35]\n\t" \
        "buffer_load_dwordx4 v4, s[24:27], s57 offen offset:1024  lds\n\t" \
        "v_mfma_f32_16x16x32_bf16 v[36:39], a[184:187], v[132:135], v[36:39]\n\t" \
        "ds_read_b64_tr_b16 a[200:201], v8 offset:0\n\t" \
        "s_waitcnt lgkmcnt(6)\n\t" \
        "v_mfma_f32_16x16x32_bf16 v[40:43], a[188:191], v[132:135], v[40:43]\n\t" \
        "ds_read_b64_tr_b16 a[202:203], v8 offset:768\n\t" \
        "v_mfma_f32_16x16x32_bf16 v[44:47], a[180:183], v[136:139], v[44:47]\n\t" \
        "ds_read_b128 v[120:123], v16 offset:11968\n\t" \
        "v_mfma_f32_16x16x32_bf16 v[48:51], a[184:187], v[136:139], v[48:51]\n\t" \
        "ds_read_b128 v[124:127], v16 offset:19904\n\t" \
        "v_mfma_f32_16x16x32_bf16 v[52:55], a[188:191], v[136:139], v[52:55]\n\t" \
        "ds_read_b128 v[128:131], v16 offset:27840\n\t" \
        "v_mfma_f32_16x16x32_bf16 v[56:59], a[180:183], v[140:143], v[56:59]\n\t" \
        "buffer_load_dwordx4 v5, s[24:27], s57 offen offset:2048  lds\n\t" \
        "v_mfma_f32_16x16x32_bf16 v[60:63], a[184:187], v[140:143], v[60:63]\n\t" \
        "ds_read_b128 v[132:135], v16 offset:4096\n\t" \
        "v_mfma_f32_16x16x32_bf16 v[64:67], a[188:191], v[140:143], v[64:67]\n\t" \
        "ds_read_b64_tr_b16 a[204:205], v6 offset:6144\n\t" \
        "v_mfma_f32_16x16x32_bf16 v[68:71], a[180:183], v[144:147], v[68:71]\n\t" \
        "ds_read_b64_tr_b16 a[206:207], v6 offset:6912\n\t" \
        "v_mfma_f32_16x16x32_bf16 v[72:75], a[184:187], v[144:147], v[72:75]\n\t" \
        "ds_read_b64_tr_b16 a[208:209], v7 offset:6144\n\t" \
        "s_add_u32 s57, s36, 0x5e800\n\t" \
        "s_add_u32 m0, s38, 0x9b00\n\t" \
        "v_mfma_f32_16x16x32_bf16 v[76:79], a[188:191], v[144:147], v[76:79]\n\t" \
        "ds_read_b64_tr_b16 a[210:211], v7 offset:6912\n\t" \
        "s_waitcnt lgkmcnt(7)\n\t" \
        "v_mfma_f32_16x16x32_bf16 v[32:35], a[192:195], v[116:119], v[32:35]\n\t" \
        "buffer_load_dwordx4 v13, s[16:19], s57 offen nt lds\n\t" \
        "v_mfma_f32_16x16x32_bf16 v[36:39], a[196:199], v[116:119], v[36:39]\n\t" \
        "ds_read_b64_tr_b16 a[212:213], v8 offset:6144\n\t" \
        "v_mfma_f32_16x16x32_bf16 v[40:43], a[200:203], v[116:119], v[40:43]\n\t" \
        "ds_read_b64_tr_b16 a[214:215], v8 offset:6912\n\t" \
        "v_mfma_f32_16x16x32_bf16 v[44:47], a[192:195], v[120:123], v[44:47]\n\t" \
        "ds_read_b128 v[136:139], v16 offset:12032\n\t" \
        "v_mfma_f32_16x16x32_bf16 v[48:51], a[196:199], v[120:123], v[48:51]\n\t" \
        "ds_read_b128 v[140:143], v16 offset:19968\n\t" \
        "s_add_u32 s57, s36, 0x69000\n\t" \
        "s_add_u32 m0, s38, 0xaa80\n\t" \
        "v_mfma_f32_16x16x32_bf16 v[52:55], a[200:203], v[120:123], v[52:55]\n\t" \
        "s_waitcnt vmcnt(16) lgkmcnt(2)\n\t" \
        "s_barrier\n\t" \
        "ds_read_b128 v[144:147], v16 offset:27904\n\t" \
        "v_mfma_f32_16x16x32_bf16 v[56:59], a[192:195], v[124:127], v[56:59]\n\t" \
        "buffer_load_dwordx4 v13, s[16:19], s57 offen nt lds\n\t" \
        "v_mfma_f32_16x16x32_bf16 v[60:63], a[196:199], v[124:127], v[60:63]\n\t" \
        "ds_read_b128 v[116:119], v14 offset:7936\n\t" \
        "v_mfma_f32_16x16x32_bf16 v[64:67], a[200:203], v[124:127], v[64:67]\n\t" \
        "ds_read_b64_tr_b16 a[216:217], v6 offset:12288\n\t" \
        "v_mfma_f32_16x16x32_bf16 v[68:71], a[192:195], v[128:131], v[68:71]\n\t" \
        "ds_read_b64_tr_b16 a[218:219], v6 offset:13056\n\t" \
        "v_mfma_f32_16x16x32_bf16 v[72:75], a[196:199], v[128:131], v[72:75]\n\t" \
        "ds_read_b64_tr_b16 a[220:221], v7 offset:12288\n\t" \
        "s_add_u32 s57, s36, 0x73800\n\t" \
        "s_add_u32 m0, s38, 0xba00\n\t" \
        "v_mfma_f32_16x16x32_bf16 v[76:79], a[200:203], v[128:131], v[76:79]\n\t" \
        "ds_read_b64_tr_b16 a[222:223], v7 offset:13056\n\t" \
        "v_mfma_f32_16x16x32_bf16 v[32:35], a[204:207], v[132:135], v[32:35]\n\t" \
        "buffer_load_dwordx4 v13, s[16:19], s57 offen nt lds\n\t" \
        "v_mfma_f32_16x16x32_bf16 v[36:39], a[208:211], v[132:135], v[36:39]\n\t" \
        "ds_read_b64_tr_b16 a[224:225], v8 offset:12288\n\t" \
        "v_mfma_f32_16x16x32_bf16 v[40:43], a[212:215], v[132:135], v[40:43]\n\t" \
        "ds_read_b64_tr_b16 a[226:227], v8 offset:13056\n\t" \
        "s_waitcnt lgkmcnt(7)\n\t" \
        "v_mfma_f32_16x16x32_bf16 v[44:47], a[204:207], v[136:139], v[44:47]\n\t" \
        "ds_read_b128 v[120:123], v14 offset:15872\n\t" \
        "v_mfma_f32_16x16x32_bf16 v[48:51], a[208:211], v[136:139], v[48:51]\n\t" \
        "ds_read_b128 v[124:127], v14 offset:23808\n\t" \
        "s_add_u32 s57, s36, 0x7e000\n\t" \
        "s_add_u32 m0, s38, 0xc980\n\t" \
        "v_mfma_f32_16x16x32_bf16 v[52:55], a[212:215], v[136:139], v[52:55]\n\t" \
        "ds_read_b128 v[128:131], v14 offset:31744\n\t" \
        "v_mfma_f32_16x16x32_bf16 v[56:59], a[204:207], v[140:143], v[56:59]\n\t" \
        "buffer_load_dwordx4 v13, s[16:19], s57 offen nt lds\n\t" \
        "v_mfma_f32_16x16x32_bf16 v[60:63], a[208:211], v[140:143], v[60:63]\n\t" \
        "ds_read_b128 v[132:135], v14 offset:8000\n\t" \
        "v_mfma_f32_16x16x32_bf16 v[64:67], a[212:215], v[140:143], v[64:67]\n\t" \
        "ds_read_b64_tr_b16 a[228:229], v6 offset:18432\n\t" \
        "v_mfma_f32_16x16x32_bf16 v[68:71], a[204:207], v[144:147], v[68:71]\n\t" \
        "ds_read_b64_tr_b16 a[230:231], v6 offset:19200\n\t" \
        "v_mfma_f32_16x16x32_bf16 v[72:75], a[208:211], v[144:147], v[72:75]\n\t" \
        "ds_read_b64_tr_b16 a[232:233], v7 offset:18432\n\t" \
        "s_add_u32 s57, s36, 0x88800\n\t" \
        "s_add_u32 m0, s38, 0xd900\n\t" \
        "v_mfma_f32_16x16x32_bf16 v[76:79], a[212:215], v[144:147], v[76:79]\n\t" \
        "ds_read_b64_tr_b16 a[234:235], v7 offset:19200\n\t" \
        "s_waitcnt lgkmcnt(7)\n\t" \
        "v_mfma_f32_16x16x32_bf16 v[32:35], a[216:219], v[116:119], v[32:35]\n\t" \
        "buffer_load_dwordx4 v13, s[16:19], s57 offen nt lds\n\t" \
        "v_mfma_f32_16x16x32_bf16 v[36:39], a[220:223], v[116:119], v[36:39]\n\t" \
        "ds_read_b64_tr_b16 a[236:237], v8 offset:18432\n\t" \
        "v_mfma_f32_16x16x32_bf16 v[40:43], a[224:227], v[116:119], v[40:43]\n\t" \
        "ds_read_b64_tr_b16 a[238:239], v8 offset:19200\n\t" \
        "v_mfma_f32_16x16x32_bf16 v[44:47], a[216:219], v[120:123], v[44:47]\n\t" \
        "ds_read_b128 v[136:139], v14 offset:15936\n\t" \
        "v_mfma_f32_16x16x32_bf16 v[48:51], a[220:223], v[120:123], v[48:51]\n\t" \
        "ds_read_b128 v[140:143], v14 offset:23872\n\t" \
        "s_add_u32 s57, s36, 0x93000\n\t" \
        "s_min_u32 s57, s57, 0x925800\n\t" \
        "s_add_u32 m0, s38, 0xe880\n\t" \
        "v_mfma_f32_16x16x32_bf16 v[52:55], a[224:227], v[120:123], v[52:55]\n\t" \
        "s_waitcnt vmcnt(14)\n\t" \
        "s_barrier\n\t" \
        "ds_read_b128 v[144:147], v14 offset:31808\n\t" \
        "v_and_b32 v148, 0x7fff7fff, v148\n\t" \
        "s_waitcnt lgkmcnt(7)\n\t" \
        "v_mfma_f32_16x16x32_bf16 v[56:59], a[216:219], v[124:127], v[56:59]\n\t" \
        "buffer_load_dwordx4 v13, s[16:19], s57 offen nt lds\n\t" \
        "v_mfma_f32_16x16x32_bf16 v[60:63], a[220:223], v[124:127], v[60:63]\n\t" \
        "ds_read_b128 v[116:119], v14 offset:8064\n\t" \
        "v_and_b32 v149, 0x7fff7fff, v149\n\t" \
        "v_mfma_f32_16x16x32_bf16 v[64:67], a[224:227], v[124:127], v[64:67]\n\t" \
        "ds_read_b64_tr_b16 a[240:241], v6 offset:24576\n\t" \
        "v_and_b32 v150, 0x7fff7fff, v150\n\t" \
        "v_mfma_f32_16x16x32_bf16 v[68:71], a[216:219], v[128:131], v[68:71]\n\t" \
        "ds_read_b64_tr_b16 a[242:243], v6 offset:25344\n\t" \
        "v_and_b32 v151, 0x7fff7fff, v151\n\t" \
        "v_mfma_f32_16x16x32_bf16 v[72:75], a[220:223], v[128:131], v[72:75]\n\t" \
        "ds_read_b64_tr_b16 a[244:245], v7 offset:24576\n\t" \
        "s_add_u32 s57, s36, 0x9d800\n\t" \
        "s_min_u32 s57, s57, 0x925800\n\t" \
        "s_add_u32 m0, s38, 0xf800\n\t" \
        "v_and_b32 v152, 0x7fff7fff, v152\n\t" \
        "v_mfma_f32_16x16x32_bf16 v[76:79], a[224:227], v[128:131], v[76:79]\n\t" \
        "ds_read_b64_tr_b16 a[246:247], v7 offset:25344\n\t" \
        "v_and_b32 v153, 0x7fff7fff, v153\n\t" \
        "v_mfma_f32_16x16x32_bf16 v[32:35], a[228:231], v[132:135], v[32:35]\n\t" \
        "buffer_load_dwordx4 v13, s[16:19], s57 offen nt lds\n\t" \
        "s_waitcnt lgkmcnt(6)\n\t" \
        "v_mfma_f32_16x16x32_bf16 v[36:39], a[232:235], v[132:135], v[36:39]\n\t" \
        "ds_read_b64_tr_b16 a[248:249], v8 offset:24576\n\t" \
        "v_and_b32 v154, 0x7fff7fff, v154\n\t" \
        "v_mfma_f32_16x16x32_bf16 v[40:43], a[236:239], v[132:135], v[40:43]\n\t" \
        "ds_read_b64_tr_b16 a[250:251], v8 offset:25344\n\t" \
        "v_and_b32 v155, 0x7fff7fff, v155\n\t" \
        "v_mfma_f32_16x16x32_bf16 v[44:47], a[228:231], v[136:139], v[44:47]\n\t" \
        "ds_read_b128 v[120:123], v14 offset:16000\n\t" \
        "v_and_b32 v156, 0x7fff7fff, v156\n\t" \
        "v_mfma_f32_16x16x32_bf16 v[48:51], a[232:235], v[136:139], v[48:51]\n\t" \
        "ds_read_b128 v[124:127], v14 offset:23936\n\t" \
        "s_add_u32 s57, s36, 0xa8000\n\t" \
        "s_min_u32 s57, s57, 0x925800\n\t" \
        "s_add_u32 m0, s38, 0x10780\n\t" \
        "v_pk_max_u16 v148, v148, v149\n\t" \
        "v_mfma_f32_16x16x32_bf16 v[52:55], a[236:239], v[136:139], v[52:55]\n\t" \
        "ds_read_b128 v[128:131], v14 offset:31872\n\t" \
        "v_pk_max_u16 v150, v150, v151\n\t" \
        "v_mfma_f32_16x16x32_bf16 v[56:59], a[228:231], v[140:143], v[56:59]\n\t" \
        "buffer_load_dwordx4 v13, s[16:19], s57 offen nt lds\n\t" \
        "v_mfma_f32_16x16x32_bf16 v[60:63], a[232:235], v[140:143], v[60:63]\n\t" \
        "ds_read_b128 v[132:135], v15 offset:7936\n\t" \
        "v_pk_max_u16 v152, v152, v153\n\t" \
        "v_mfma_f32_16x16x32_bf16 v[64:67], a[236:239], v[140:143], v[64:67]\n\t" \
        "ds_read_b64_tr_b16 a[252:253], v6 offset:30720\n\t" \
        "v_pk_max_u16 v154, v154, v155\n\t" \
        "s_waitcnt lgkmcnt(7)\n\t" \
        "v_mfma_f32_16x16x32_bf16 v[68:71], a[228:231], v[144:147], v[68:71]\n\t" \
        "ds_read_b64_tr_b16 a[254:255], v6 offset:31488\n\t" \
        "v_pk_max_u16 v156, v156, v23\n\t" \
        "v_mfma_f32_16x16x32_bf16 v[72:75], a[232:235], v[144:147], v[72:75]\n\t" \
        "ds_read_b64_tr_b16 v[188:189], v7 offset:30720\n\t" \
        "v_pk_max_u16 v148, v148, v150\n\t" \
        "v_mfma_f32_16x16x32_bf16 v[76:79], a[236:239], v[144:147], v[76:79]\n\t" \
        "ds_read_b64_tr_b16 v[190:191], v7 offset:31488\n\t" \
        "v_pk_max_u16 v152, v152, v154\n\t" \
        "v_mfma_f32_16x16x32_bf16 v[32:35], a[240:243], v[116:119], v[32:35]\n\t" \
        "ds_read_b64_tr_b16 v[192:193], v8 offset:30720\n\t" \
        "v_pk_max_u16 v148, v148, v156\n\t" \
        "v_mfma_f32_16x16x32_bf16 v[36:39], a[244:247], v[116:119], v[36:39]\n\t" \
        "ds_read_b64_tr_b16 v[194:195], v8 offset:31488\n\t" \
        "v_pk_max_u16 v148, v148, v152\n\t" \
        "s_waitcnt lgkmcnt(7)\n\t" \
        "v_mfma_f32_16x16x32_bf16 v[40:43], a[248:251], v[116:119], v[40:43]\n\t" \
        "ds_read_b128 v[136:139], v15 offset:15872\n\t" \
        "v_cmp_ne_u32 vcc, v148, v23\n\t" \
        "s_nop 0\n\t" \
        "s_or_b32 s56, vcc_lo, vcc_hi\n\t" \
        "v_mfma_f32_16x16x32_bf16 v[44:47], a[240:243], v[120:123], v[44:47]\n\t" \
        "ds_read_b128 v[140:143], v15 offset:23808\n\t" \
        "v_mov_b32 v149, s56\n\t" \
        "v_mfma_f32_16x16x32_bf16 v[48:51], a[244:247], v[120:123], v[48:51]\n\t" \
        "ds_read_b128 v[144:147], v15 offset:31744\n\t" \
        "v_mfma_f32_16x16x32_bf16 v[52:55], a[248:251], v[120:123], v[52:55]\n\t" \
        "s_waitcnt vmcnt(14)\n\t" \
        "s_barrier\n\t" \
        "ds_read_b128 v[116:119], v15 offset:8000\n\t" \
        "v_mfma_f32_16x16x32_bf16 v[56:59], a[240:243], v[124:127], v[56:59]\n\t" \
        "ds_read_b64_tr_b16 v[196:197], v6 offset:36864\n\t" \
        "v_mfma_f32_16x16x32_bf16 v[60:63], a[244:247], v[124:127], v[60:63]\n\t" \
        "ds_read_b64_tr_b16 v[198:199], v6 offset:37632\n\t" \
        "v_mfma_f32_16x16x32_bf16 v[64:67], a[248:251], v[124:127], v[64:67]\n\t" \
        "ds_read_b64_tr_b16 v[200:201], v7 offset:36864\n\t" \
        "v_mfma_f32_16x16x32_bf16 v[68:71], a[240:243], v[128:131], v[68:71]\n\t" \
        "ds_read_b64_tr_b16 v[202:203], v7 offset:37632\n\t" \
        "v_mov_b32 v184, 0\n\t" \
        "v_mfma_f32_16x16x32_bf16 v[72:75], a[244:247], v[128:131], v[72:75]\n\t" \
        "ds_read_b64_tr_b16 v[204:205], v8 offset:36864\n\t" \
        "v_mfma_f32_16x16x32_bf16 v[76:79], a[248:251], v[128:131], v[76:79]\n\t" \
        "ds_read_b64_tr_b16 v[206:207], v8 offset:37632\n\t" \
        "s_waitcnt lgkmcnt(8)\n\t" \
        "v_mfma_f32_16x16x32_bf16 v[32:35], a[252:255], v[132:135], v[32:35]\n\t" \
        "ds_read_b128 v[120:123], v15 offset:15936\n\t" \
        "v_mfma_f32_16x16x32_bf16 v[36:39], v[188:191], v[132:135], v[36:39]\n\t" \
        "ds_read_b128 v[124:127], v15 offset:23872\n\t" \
        "v_mfma_f32_16x16x32_bf16 v[40:43], v[192:195], v[132:135], v[40:43]\n\t" \
        "ds_read_b128 v[128:131], v15 offset:31808\n\t" \
        "v_mfma_f32_16x16x32_bf16 v[44:47], a[252:255], v[136:139], v[44:47]\n\t" \
        "ds_read_b128 v[132:135], v15 offset:8064\n\t" \
        "v_mfma_f32_16x16x32_bf16 v[48:51], v[188:191], v[136:139], v[48:51]\n\t" \
        "ds_read_b64_tr_b16 v[208:209], v6 offset:43008\n\t" \
        "v_mfma_f32_16x16x32_bf16 v[52:55], v[192:195], v[136:139], v[52:55]\n\t" \
        "ds_read_b64_tr_b16 v[210:211], v6 offset:43776\n\t" \
        "v_mfma_f32_16x16x32_bf16 v[56:59], a[252:255], v[140:143], v[56:59]\n\t" \
        "ds_read_b64_tr_b16 v[212:213], v7 offset:43008\n\t" \
        "v_mfma_f32_16x16x32_bf16 v[60:63], v[188:191], v[140:143], v[60:63]\n\t" \
        "ds_read_b64_tr_b16 v[214:215], v7 offset:43776\n\t" \
        "v_mfma_f32_16x16x32_bf16 v[64:67], v[192:195], v[140:143], v[64:67]\n\t" \
        "ds_write_b32 v24, v149\n\t" \
        "s_waitcnt lgkmcnt(8)\n\t" \
        "v_mfma_f32_16x16x32_bf16 v[68:71], a[252:255], v[144:147], v[68:71]\n\t" \
        "ds_read_b64_tr_b16 v[216:217], v8 offset:43008\n\t" \
        "v_mfma_f32_16x16x32_bf16 v[72:75], v[188:191], v[144:147], v[72:75]\n\t" \
        "ds_read_b64_tr_b16 v[218:219], v8 offset:43776\n\t" \
        "v_mfma_f32_16x16x32_bf16 v[76:79], v[192:195], v[144:147], v[76:79]\n\t" \
        "ds_read_b128 v[136:139], v15 offset:16000\n\t" \
        "v_mfma_f32_16x16x32_bf16 v[32:35], v[196:199], v[116:119], v[32:35]\n\t" \
        "ds_read_b128 v[140:143], v15 offset:23936\n\t" \
        "v_mfma_f32_16x16x32_bf16 v[36:39], v[200:203], v[116:119], v[36:39]\n\t" \
        "ds_read_b128 v[144:147], v15 offset:31872\n\t" \
        "v_mfma_f32_16x16x32_bf16 v[40:43], v[204:207], v[116:119], v[40:43]\n\t" \
        "ds_read_b128 v[116:119], v16 offset:7936\n\t" \
        "v_mfma_f32_16x16x32_bf16 v[44:47], v[196:199], v[120:123], v[44:47]\n\t" \
        "v_mfma_f32_16x16x32_bf16 v[48:51], v[200:203], v[120:123], v[48:51]\n\t" \
        "v_mfma_f32_16x16x32_bf16 v[52:55], v[204:207], v[120:123], v[52:55]\n\t" \
        "s_waitcnt vmcnt(11) lgkmcnt(6)\n\t" \
        "s_barrier\n\t" \
        "ds_read_b64_tr_b16 v[220:221], v6 offset:49152\n\t" \
        "v_mfma_f32_16x16x32_bf16 v[56:59], v[196:199], v[124:127], v[56:59]\n\t" \
        "ds_read_b64_tr_b16 v[222:223], v6 offset:49920\n\t" \
        "v_mfma_f32_16x16x32_bf16 v[60:63], v[200:203], v[124:127], v[60:63]\n\t" \
        "ds_read_b64_tr_b16 v[224:225], v7 offset:49152\n\t" \
        "v_mfma_f32_16x16x32_bf16 v[64:67], v[204:207], v[124:127], v[64:67]\n\t" \
        "ds_read_b64_tr_b16 v[226:227], v7 offset:49920\n\t" \
        "v_mfma_f32_16x16x32_bf16 v[68:71], v[196:199], v[128:131], v[68:71]\n\t" \
        "ds_read_b64_tr_b16 v[228:229], v8 offset:49152\n\t" \
        "v_mfma_f32_16x16x32_bf16 v[72:75], v[200:203], v[128:131], v[72:75]\n\t" \
        "ds_read_b64_tr_b16 v[230:231], v8 offset:49920\n\t" \
        "v_mfma_f32_16x16x32_bf16 v[76:79], v[204:207], v[128:131], v[76:79]\n\t" \
        "ds_read_b128 v[120:123], v16 offset:15872\n\t" \
        "v_mfma_f32_16x16x32_bf16 v[32:35], v[208:211], v[132:135], v[32:35]\n\t" \
        "ds_read_b128 v[124:127], v16 offset:23808\n\t" \
        "v_mfma_f32_16x16x32_bf16 v[36:39], v[212:215], v[132:135], v[36:39]\n\t" \
        "ds_read_b128 v[184:187], v184\n\t" \
        "s_waitcnt lgkmcnt(8)\n\t" \
        "v_mfma_f32_16x16x32_bf16 v[40:43], v[216:219], v[132:135], v[40:43]\n\t" \
        "ds_read_b128 v[128:131], v16 offset:31744\n\t" \
        "v_mfma_f32_16x16x32_bf16 v[44:47], v[208:211], v[136:139], v[44:47]\n\t" \
        "ds_read_b128 v[132:135], v16 offset:8000\n\t" \
        "v_mfma_f32_16x16x32_bf16 v[48:51], v[212:215], v[136:139], v[48:51]\n\t" \
        "ds_read_b64_tr_b16 v[232:233], v6 offset:55296\n\t" \
        "v_mfma_f32_16x16x32_bf16 v[52:55], v[216:219], v[136:139], v[52:55]\n\t" \
        "ds_read_b64_tr_b16 v[234:235], v6 offset:56064\n\t" \
        "v_mfma_f32_16x16x32_bf16 v[56:59], v[208:211], v[140:143], v[56:59]\n\t" \
        "ds_read_b64_tr_b16 v[236:237], v7 offset:55296\n\t" \
        "v_mfma_f32_16x16x32_bf16 v[60:63], v[212:215], v[140:143], v[60:63]\n\t" \
        "ds_read_b64_tr_b16 v[238:239], v7 offset:56064\n\t" \
        "v_mfma_f32_16x16x32_bf16 v[64:67], v[216:219], v[140:143], v[64:67]\n\t" \
        "ds_read_b64_tr_b16 v[240:241], v8 offset:55296\n\t" \
        "v_mfma_f32_16x16x32_bf16 v[68:71], v[208:211], v[144:147], v[68:71]\n\t" \
        "ds_read_b64_tr_b16 v[242:243], v8 offset:56064\n\t" \
        "s_waitcnt lgkmcnt(8)\n\t" \
        "v_or3_b32 v184, v184, v185, v186\n\t" \
        "v_mfma_f32_16x16x32_bf16 v[72:75], v[212:215], v[144:147], v[72:75]\n\t" \
        "ds_read_b128 v[136:139], v16 offset:15936\n\t" \
        "v_or_b32 v184, v184, v187\n\t" \
        "v_mfma_f32_16x16x32_bf16 v[76:79], v[216:219], v[144:147], v[76:79]\n\t" \
        "ds_read_b128 v[140:143], v16 offset:23872\n\t" \
        "v_readfirstlane_b32 s56, v184\n\t" \
        "v_mfma_f32_16x16x32_bf16 v[32:35], v[220:223], v[116:119], v[32:35]\n\t" \
        "ds_read_b128 v[144:147], v16 offset:31808\n\t" \
        "s_cmp_lg_u32 s56, 0\n\t" \
        "s_cbranch_scc1 .LR_abort%=\n\t" \
        "v_mfma_f32_16x16x32_bf16 v[36:39], v[224:227], v[116:119], v[36:39]\n\t" \
        "v_mfma_f32_16x16x32_bf16 v[40:43], v[228:231], v[116:119], v[40:43]\n\t" \
        "ds_read_b128 v[116:119], v16 offset:8064\n\t" \
        "v_mfma_f32_16x16x32_bf16 v[44:47], v[220:223], v[120:123], v[44:47]\n\t" \
        "v_mfma_f32_16x16x32_bf16 v[48:51], v[224:227], v[120:123], v[48:51]\n\t" \
        "v_mfma_f32_16x16x32_bf16 v[52:55], v[228:231], v[120:123], v[52:55]\n\t" \
        "s_waitcnt vmcnt(8)\n\t" \
        "s_barrier\n\t" \
        "ds_read_b64_tr_b16 v[244:245], v20 offset:0\n\t" \
        "v_mfma_f32_16x16x32_bf16 v[56:59], v[220:223], v[124:127], v[56:59]\n\t" \
        "ds_read_b64_tr_b16 v[246:247], v20 offset:768\n\t" \
        "v_mfma_f32_16x16x32_bf16 v[60:63], v[224:227], v[124:127], v[60:63]\n\t" \
        "ds_read_b64_tr_b16 v[248:249], v21 offset:0\n\t" \
        "v_mfma_f32_16x16x32_bf16 v[64:67], v[228:231], v[124:127], v[64:67]\n\t" \
        "ds_read_b64_tr_b16 v[250:251], v21 offset:768\n\t" \
        "s_waitcnt lgkmcnt(5)\n\t" \
        "v_mfma_f32_16x16x32_bf16 v[68:71], v[220:223], v[128:131], v[68:71]\n\t" \
        "ds_read_b64_tr_b16 v[252:253], v22 offset:0\n\t" \
        "v_mfma_f32_16x16x32_bf16 v[72:75], v[224:227], v[128:131], v[72:75]\n\t" \
        "ds_read_b64_tr_b16 v[254:255], v22 offset:768\n\t" \
        "v_mfma_f32_16x16x32_bf16 v[76:79], v[228:231], v[128:131], v[76:79]\n\t" \
        "ds_read_b128 v[120:123], v16 offset:16000\n\t" \
        "v_mfma_f32_16x16x32_bf16 v[32:35], v[232:235], v[132:135], v[32:35]\n\t" \
        "ds_read_b128 v[124:127], v16 offset:23936\n\t" \
        "v_mfma_f32_16x16x32_bf16 v[36:39], v[236:239], v[132:135], v[36:39]\n\t" \
        "ds_read_b128 v[128:131], v16 offset:31872\n\t" \
        "v_mfma_f32_16x16x32_bf16 v[40:43], v[240:243], v[132:135], v[40:43]\n\t" \
        "ds_read_b128 v[132:135], v14 offset:31744\n\t" \
        "v_mfma_f32_16x16x32_bf16 v[44:47], v[232:235], v[136:139], v[44:47]\n\t" \
        "v_mfma_f32_16x16x32_bf16 v[48:51], v[236:239], v[136:139], v[48:51]\n\t" \
        "v_mfma_f32_16x16x32_bf16 v[52:55], v[240:243], v[136:139], v[52:55]\n\t" \
        "ds_read_b128 v[136:139], v14 offset:31808\n\t" \
        "v_mfma_f32_16x16x32_bf16 v[56:59], v[232:235], v[140:143], v[56:59]\n\t" \
        "v_mfma_f32_16x16x32_bf16 v[60:63], v[236:239], v[140:143], v[60:63]\n\t" \
        "v_mfma_f32_16x16x32_bf16 v[64:67], v[240:243], v[140:143], v[64:67]\n\t" \
        "ds_read_b128 v[140:143], v14 offset:31872\n\t" \
        "v_mfma_f32_16x16x32_bf16 v[68:71], v[232:235], v[144:147], v[68:71]\n\t" \
        "v_mfma_f32_16x16x32_bf16 v[72:75], v[236:239], v[144:147], v[72:75]\n\t" \
        "v_mfma_f32_16x16x32_bf16 v[76:79], v[240:243], v[144:147], v[76:79]\n\t" \
        "ds_read_b128 v[144:147], v15 offset:31744\n\t" \
        "s_waitcnt lgkmcnt(3)\n\t" \
        "v_mfma_f32_16x16x32_bf16 v[32:35], v[244:247], v[116:119], v[32:35]\n\t" \
        "v_mfma_f32_16x16x32_bf16 v[36:39], v[248:251], v[116:119], v[36:39]\n\t" \
        "v_mfma_f32_16x16x32_bf16 v[40:43], v[252:255], v[116:119], v[40:43]\n\t" \
        "ds_read_b128 v[116:119], v15 offset:31808\n\t" \
        "v_mfma_f32_16x16x32_bf16 v[44:47], v[244:247], v[120:123], v[44:47]\n\t" \
        "v_mfma_f32_16x16x32_bf16 v[48:51], v[248:251], v[120:123], v[48:51]\n\t" \
        "v_mfma_f32_16x16x32_bf16 v[52:55], v[252:255], v[120:123], v[52:55]\n\t" \
        "ds_read_b128 v[120:123], v15 offset:31872\n\t" \
        "v_mfma_f32_16x16x32_bf16 v[56:59], v[244:247], v[124:127], v[56:59]\n\t" \
        "v_mfma_f32_16x16x32_bf16 v[60:63], v[248:251], v[124:127], v[60:63]\n\t" \
        "v_mfma_f32_16x16x32_bf16 v[64:67], v[252:255], v[124:127], v[64:67]\n\t" \
        "ds_read_b128 v[124:127], v16 offset:31744\n\t" \
        "v_mfma_f32_16x16x32_bf16 v[68:71], v[244:247], v[128:131], v[68:71]\n\t" \
        "v_mfma_f32_16x16x32_bf16 v[72:75], v[248:251], v[128:131], v[72:75]\n\t" \
        "v_mfma_f32_16x16x32_bf16 v[76:79], v[252:255], v[128:131], v[76:79]\n\t" \
        "ds_read_b128 v[128:131], v16 offset:31808\n\t" \
        "v_mfma_f32_16x16x32_bf16 v[80:83], a[0:3], v[132:135], 0\n\t" \
        "v_mfma_f32_16x16x32_bf16 v[84:87], a[4:7], v[132:135], 0\n\t" \
        "v_mfma_f32_16x16x32_bf16 v[88:91], a[8:11], v[132:135], 0\n\t" \
        "v_cvt_pk_bf16_f32 v40, v40, v41\n\t" \
        "v_cvt_pk_bf16_f32 v41, v42, v43\n\t" \
        "s_waitcnt lgkmcnt(2)\n\t" \
        "v_mfma_f32_16x16x32_bf16 v[80:83], a[12:15], v[136:139], v[80:83]\n\t" \
        "ds_write_b64 v11, v[40:41] offset:0\n\t" \
        "v_cvt_pk_bf16_f32 v52, v52, v53\n\t" \
        "v_mfma_f32_16x16x32_bf16 v[84:87], a[16:19], v[136:139], v[84:87]\n\t" \
        "ds_read_b128 v[132:135], v16 offset:31872\n\t" \
        "v_cvt_pk_bf16_f32 v53, v54, v55\n\t" \
        "v_mfma_f32_16x16x32_bf16 v[88:91], a[20:23], v[136:139], v[88:91]\n\t" \
        "ds_write_b64 v11, v[52:53] offset:2048\n\t" \
        "v_cvt_pk_bf16_f32 v64, v64, v65\n\t" \
        "v_mfma_f32_16x16x32_bf16 v[80:83], a[24:27], v[140:143], v[80:83]\n\t" \
        "v_cvt_pk_bf16_f32 v65, v66, v67\n\t" \
        "v_cvt_pk_bf16_f32 v76, v76, v77\n\t" \
        "v_mfma_f32_16x16x32_bf16 v[84:87], a[28:31], v[140:143], v[84:87]\n\t" \
        "ds_write_b64 v11, v[64:65] offset:4096\n\t" \
        "v_cvt_pk_bf16_f32 v77, v78, v79\n\t" \
        "v_mfma_f32_16x16x32_bf16 v[88:91], a[32:35], v[140:143], v[88:91]\n\t" \
        "ds_write_b64 v11, v[76:77] offset:6144\n\t" \
        "v_cvt_pk_bf16_f32 v32, v32, v33\n\t" \
        "v_mfma_f32_16x16x32_bf16 v[80:83], a[36:39], v[144:147], v[80:83]\n\t" \
        "v_cvt_pk_bf16_f32 v33, v34, v35\n\t" \
        "v_cvt_pk_bf16_f32 v34, v36, v37\n\t" \
        "v_mfma_f32_16x16x32_bf16 v[84:87], a[40:43], v[144:147], v[84:87]\n\t" \
        "v_cvt_pk_bf16_f32 v35, v38, v39\n\t" \
        "v_permlane16_swap_b32 v32, v34\n\t" \
        "v_mfma_f32_16x16x32_bf16 v[88:91], a[44:47], v[144:147], v[88:91]\n\t" \
        "v_permlane16_swap_b32 v33, v35\n\t" \
        "v_mfma_f32_16x16x32_bf16 v[80:83], a[48:51], v[116:119], v[80:83]\n\t" \
        "v_mfma_f32_16x16x32_bf16 v[84:87], a[52:55], v[116:119], v[84:87]\n\t" \
        "v_mfma_f32_16x16x32_bf16 v[88:91], a[56:59], v[116:119], v[88:91]\n\t" \
        "v_mfma_f32_16x16x32_bf16 v[80:83], a[60:63], v[120:123], v[80:83]\n\t" \
        "v_mfma_f32_16x16x32_bf16 v[84:87], a[64:67], v[120:123], v[84:87]\n\t" \
        "v_mfma_f32_16x16x32_bf16 v[88:91], a[68:71], v[120:123], v[88:91]\n\t" \
        "s_waitcnt lgkmcnt(0)\n\t" \
        "v_mfma_f32_16x16x32_bf16 v[80:83], a[72:75], v[124:127], v[80:83]\n\t" \
        "v_mfma_f32_16x16x32_bf16 v[84:87], a[76:79], v[124:127], v[84:87]\n\t" \
        "s_waitcnt vmcnt(0)\n\t" \
        "s_barrier\n\t" \
        "ds_read_b128 v[136:139], v14 offset:35712\n\t" \
        "v_mfma_f32_16x16x32_bf16 v[88:91], a[80:83], v[124:127], v[88:91]\n\t" \
        "ds_read_b128 v[140:143], v14 offset:35776\n\t" \
        "v_mfma_f32_16x16x32_bf16 v[80:83], a[84:87], v[128:131], v[80:83]\n\t" \
        "ds_read_b128 v[144:147], v14 offset:35840\n\t" \
        "v_mfma_f32_16x16x32_bf16 v[84:87], a[88:91], v[128:131], v[84:87]\n\t" \
        "ds_read_b128 v[116:119], v15 offset:35712\n\t" \
        "v_mfma_f32_16x16x32_bf16 v[88:91], a[92:95], v[128:131], v[88:91]\n\t" \
        "ds_read_b128 v[120:123], v15 offset:35776\n\t" \
        "v_mfma_f32_16x16x32_bf16 v[80:83], a[96:99], v[132:135], v[80:83]\n\t" \
        "s_mov_b32 s56, s64\n\t" \
        "buffer_store_dwordx4 v[32:35], v9, s[28:31], s56 offen sc1\n\t" \
        "v_mfma_f32_16x16x32_bf16 v[84:87], a[100:103], v[132:135], v[84:87]\n\t" \
        "ds_read_b128 v[124:127], v15 offset:35840\n\t" \
        "v_mfma_f32_16x16x32_bf16 v[88:91], a[104:107], v[132:135], v[88:91]\n\t" \
        "ds_read_b128 v[128:131], v16 offset:35712\n\t" \
        "s_waitcnt lgkmcnt(6)\n\t" \
        "v_mfma_f32_16x16x32_bf16 v[80:83], a[108:111], v[136:139], v[80:83]\n\t" \
        "ds_read_b64 v[42:43], v12 offset:0\n\t" \
        "v_mfma_f32_16x16x32_bf16 v[84:87], a[112:115], v[136:139], v[84:87]\n\t" \
        "ds_read_b128 v[132:135], v16 offset:35776\n\t" \
        "v_mfma_f32_16x16x32_bf16 v[88:91], a[116:119], v[136:139], v[88:91]\n\t" \
        "ds_read_b128 v[136:139], v16 offset:35840\n\t" \
        "s_waitcnt lgkmcnt(7)\n\t" \
        "v_mfma_f32_16x16x32_bf16 v[80:83], a[120:123], v[140:143], v[80:83]\n\t" \
        "v_mfma_f32_16x16x32_bf16 v[84:87], a[124:127], v[140:143], v[84:87]\n\t" \
        "v_mfma_f32_16x16x32_bf16 v[88:91], a[128:131], v[140:143], v[88:91]\n\t" \
        "ds_read_b128 v[140:143], v14 offset:39680\n\t" \
        "v_cvt_pk_bf16_f32 v44, v44, v45\n\t" \
        "v_mfma_f32_16x16x32_bf16 v[80:83], a[132:135], v[144:147], v[80:83]\n\t" \
        "v_cvt_pk_bf16_f32 v45, v46, v47\n\t" \
        "v_cvt_pk_bf16_f32 v46, v48, v49\n\t" \
        "v_mfma_f32_16x16x32_bf16 v[84:87], a[136:139], v[144:147], v[84:87]\n\t" \
        "v_cvt_pk_bf16_f32 v47, v50, v51\n\t" \
        "v_permlane16_swap_b32 v44, v46\n\t" \
        "v_mfma_f32_16x16x32_bf16 v[88:91], a[140:143], v[144:147], v[88:91]\n\t" \
        "s_waitcnt lgkmcnt(3)\n\t" \
        "v_permlane16_swap_b32 v40, v42\n\t" \
        "v_permlane16_swap_b32 v41, v43\n\t" \
        "v_mfma_f32_16x16x32_bf16 v[80:83], a[144:147], v[116:119], v[80:83]\n\t" \
        "ds_read_b128 v[144:147], v14 offset:39744\n\t" \
        "v_permlane16_swap_b32 v45, v47\n\t" \
        "v_mfma_f32_16x16x32_bf16 v[84:87], a[148:151], v[116:119], v[84:87]\n\t" \
        "s_mov_b32 s56, s64\n\t" \
        "buffer_store_dwordx4 v[40:43], v10, s[28:31], s56 offen sc1\n\t" \
        "v_mfma_f32_16x16x32_bf16 v[88:91], a[152:155], v[116:119], v[88:91]\n\t" \
        "ds_read_b128 v[116:119], v14 offset:39808\n\t" \
        "v_mfma_f32_16x16x32_bf16 v[80:83], a[156:159], v[120:123], v[80:83]\n\t" \
        "ds_read_b64 v[54:55], v12 offset:2048\n\t" \
        "v_mfma_f32_16x16x32_bf16 v[84:87], a[160:163], v[120:123], v[84:87]\n\t" \
        "v_mfma_f32_16x16x32_bf16 v[88:91], a[164:167], v[120:123], v[88:91]\n\t" \
        "ds_read_b128 v[120:123], v15 offset:39680\n\t" \
        "v_mfma_f32_16x16x32_bf16 v[80:83], a[168:171], v[124:127], v[80:83]\n\t" \
        "v_mfma_f32_16x16x32_bf16 v[84:87], a[172:175], v[124:127], v[84:87]\n\t" \
        "v_mfma_f32_16x16x32_bf16 v[88:91], a[176:179], v[124:127], v[88:91]\n\t" \
        "ds_read_b128 v[124:127], v15 offset:39744\n\t" \
        "v_mfma_f32_16x16x32_bf16 v[80:83], a[180:183], v[128:131], v[80:83]\n\t" \
        "v_mfma_f32_16x16x32_bf16 v[84:87], a[184:187], v[128:131], v[84:87]\n\t" \
        "v_mfma_f32_16x16x32_bf16 v[88:91], a[188:191], v[128:131], v[88:91]\n\t" \
        "ds_read_b128 v[128:131], v15 offset:39808\n\t" \
        "s_waitcnt lgkmcnt(3)\n\t" \
        "v_permlane16_swap_b32 v52, v54\n\t" \
        "v_mfma_f32_16x16x32_bf16 v[80:83], a[192:195], v[132:135], v[80:83]\n\t" \
        "v_permlane16_swap_b32 v53, v55\n\t" \
        "v_mfma_f32_16x16x32_bf16 v[84:87], a[196:199], v[132:135], v[84:87]\n\t" \
        "v_mfma_f32_16x16x32_bf16 v[88:91], a[200:203], v[132:135], v[88:91]\n\t" \
        "ds_read_b128 v[132:135], v16 offset:39680\n\t" \
        "v_mfma_f32_16x16x32_bf16 v[80:83], a[204:207], v[136:139], v[80:83]\n\t" \
        "v_mfma_f32_16x16x32_bf16 v[84:87], a[208:211], v[136:139], v[84:87]\n\t" \
        "v_mfma_f32_16x16x32_bf16 v[88:91], a[212:215], v[136:139], v[88:91]\n\t" \
        "ds_read_b128 v[136:139], v16 offset:39744\n\t" \
        "v_mfma_f32_16x16x32_bf16 v[80:83], a[216:219], v[140:143], v[80:83]\n\t" \
        "s_add_u32 s56, s64, 0x15000\n\t" \
        "buffer_store_dwordx4 v[44:47], v9, s[28:31], s56 offen sc1\n\t" \
        "v_mfma_f32_16x16x32_bf16 v[84:87], a[220:223], v[140:143], v[84:87]\n\t" \
        "v_mfma_f32_16x16x32_bf16 v[88:91], a[224:227], v[140:143], v[88:91]\n\t" \
        "ds_read_b128 v[140:143], v16 offset:39808\n\t" \
        "v_mfma_f32_16x16x32_bf16 v[80:83], a[228:231], v[144:147], v[80:83]\n\t" \
        "v_mfma_f32_16x16x32_bf16 v[84:87], a[232:235], v[144:147], v[84:87]\n\t" \
        "v_mfma_f32_16x16x32_bf16 v[88:91], a[236:239], v[144:147], v[88:91]\n\t" \
        "ds_read_b128 v[144:147], v14 offset:39680\n\t" \
        "v_mfma_f32_16x16x32_bf16 v[80:83], a[240:243], v[116:119], v[80:83]\n\t" \
        "v_mfma_f32_16x16x32_bf16 v[84:87], a[244:247], v[116:119], v[84:87]\n\t" \
        "s_add_u32 s56, s64, 0x15000\n\t" \
        "buffer_store_dwordx4 v[52:55], v10, s[28:31], s56 offen sc1\n\t" \
        "v_mfma_f32_16x16x32_bf16 v[88:91], a[248:251], v[116:119], v[88:91]\n\t" \
        "ds_read_b128 v[116:119], v14 offset:39744\n\t" \
        "v_cvt_pk_bf16_f32 v56, v56, v57\n\t" \
        "s_waitcnt lgkmcnt(3)\n\t" \
        "v_mfma_f32_16x16x32_bf16 v[80:83], a[252:255], v[120:123], v[80:83]\n\t" \
        "v_cvt_pk_bf16_f32 v57, v58, v59\n\t" \
        "v_cvt_pk_bf16_f32 v58, v60, v61\n\t" \
        "v_mfma_f32_16x16x32_bf16 v[84:87], v[188:191], v[120:123], v[84:87]\n\t" \
        "v_cvt_pk_bf16_f32 v59, v62, v63\n\t" \
        "v_permlane16_swap_b32 v56, v58\n\t" \
        "v_mfma_f32_16x16x32_bf16 v[88:91], v[192:195], v[120:123], v[88:91]\n\t" \
        "ds_read_b128 v[120:123], v14 offset:39808\n\t" \
        "v_permlane16_swap_b32 v57, v59\n\t" \
        "v_mfma_f32_16x16x32_bf16 v[80:83], v[196:199], v[124:127], v[80:83]\n\t" \
        "ds_read_b64 v[66:67], v12 offset:4096\n\t" \
        "v_mfma_f32_16x16x32_bf16 v[84:87], v[200:203], v[124:127], v[84:87]\n\t" \
        "v_mfma_f32_16x16x32_bf16 v[88:91], v[204:207], v[124:127], v[88:91]\n\t" \
        "ds_read_b128 v[124:127], v15 offset:39680\n\t" \
        "v_mfma_f32_16x16x32_bf16 v[80:83], v[208:211], v[128:131], v[80:83]\n\t" \
        "v_mfma_f32_16x16x32_bf16 v[84:87], v[212:215], v[128:131], v[84:87]\n\t" \
        "v_mfma_f32_16x16x32_bf16 v[88:91], v[216:219], v[128:131], v[88:91]\n\t" \
        "ds_read_b128 v[128:131], v15 offset:39744\n\t" \
        "v_mfma_f32_16x16x32_bf16 v[80:83], v[220:223], v[132:135], v[80:83]\n\t" \
        "v_mfma_f32_16x16x32_bf16 v[84:87], v[224:227], v[132:135], v[84:87]\n\t" \
        "v_mfma_f32_16x16x32_bf16 v[88:91], v[228:231], v[132:135], v[88:91]\n\t" \
        "ds_read_b128 v[132:135], v15 offset:39808\n\t" \
        "s_waitcnt lgkmcnt(3)\n\t" \
        "v_permlane16_swap_b32 v64, v66\n\t" \
        "v_mfma_f32_16x16x32_bf16 v[80:83], v[232:235], v[136:139], v[80:83]\n\t" \
        "v_permlane16_swap_b32 v65, v67\n\t" \
        "v_mfma_f32_16x16x32_bf16 v[84:87], v[236:239], v[136:139], v[84:87]\n\t" \
        "v_mfma_f32_16x16x32_bf16 v[88:91], v[240:243], v[136:139], v[88:91]\n\t" \
        "ds_read_b128 v[136:139], v16 offset:39680\n\t" \
        "v_mfma_f32_16x16x32_bf16 v[80:83], v[244:247], v[140:143], v[80:83]\n\t" \
        "v_mfma_f32_16x16x32_bf16 v[84:87], v[248:251], v[140:143], v[84:87]\n\t" \
        "v_mfma_f32_16x16x32_bf16 v[88:91], v[252:255], v[140:143], v[88:91]\n\t" \
        "ds_read_b128 v[140:143], v16 offset:39744\n\t" \
        "v_mfma_f32_16x16x32_bf16 v[92:95], a[0:3], v[144:147], 0\n\t" \
        "v_mfma_f32_16x16x32_bf16 v[96:99], a[4:7], v[144:147], 0\n\t" \
        "v_mfma_f32_16x16x32_bf16 v[100:103], a[8:11], v[144:147], 0\n\t" \
        "s_nop 5\n\t" \
        "v_cvt_pk_bf16_f32 v88, v88, v89\n\t" \
        "v_cvt_pk_bf16_f32 v89, v90, v91\n\t" \
        "v_mfma_f32_16x16x32_bf16 v[92:95], a[12:15], v[116:119], v[92:95]\n\t" \
        "s_add_u32 s56, s64, 0x2a000\n\t" \
        "buffer_store_dwordx4 v[56:59], v9, s[28:31], s56 offen sc1\n\t" \
        "v_mfma_f32_16x16x32_bf16 v[96:99], a[16:19], v[116:119], v[96:99]\n\t" \
        "ds_write_b64 v11, v[88:89] offset:8192\n\t" \
        "v_cvt_pk_bf16_f32 v80, v80, v81\n\t" \
        "v_mfma_f32_16x16x32_bf16 v[100:103], a[20:23], v[116:119], v[100:103]\n\t" \
        "ds_read_b128 v[144:147], v16 offset:39808\n\t" \
        "v_cvt_pk_bf16_f32 v81, v82, v83\n\t" \
        "v_mfma_f32_16x16x32_bf16 v[92:95], a[24:27], v[120:123], v[92:95]\n\t" \
        "ds_read_b128 v[116:119], v14 offset:43648\n\t" \
        "v_cvt_pk_bf16_f32 v82, v84, v85\n\t" \
        "v_mfma_f32_16x16x32_bf16 v[96:99], a[28:31], v[120:123], v[96:99]\n\t" \
        "v_cvt_pk_bf16_f32 v83, v86, v87\n\t" \
        "v_permlane16_swap_b32 v80, v82\n\t" \
        "v_mfma_f32_16x16x32_bf16 v[100:103], a[32:35], v[120:123], v[100:103]\n\t" \
        "ds_read_b128 v[120:123], v14 offset:43712\n\t" \
        "v_permlane16_swap_b32 v81, v83\n\t" \
        "s_waitcnt lgkmcnt(4)\n\t" \
        "v_mfma_f32_16x16x32_bf16 v[92:95], a[36:39], v[124:127], v[92:95]\n\t" \
        "v_mfma_f32_16x16x32_bf16 v[96:99], a[40:43], v[124:127], v[96:99]\n\t" \
        "s_add_u32 s56, s64, 0x2a000\n\t" \
        "buffer_store_dwordx4 v[64:67], v10, s[28:31], s56 offen sc1\n\t" \
        "v_mfma_f32_16x16x32_bf16 v[100:103], a[44:47], v[124:127], v[100:103]\n\t" \
        "ds_read_b128 v[124:127], v14 offset:43776\n\t" \
        "v_cvt_pk_bf16_f32 v68, v68, v69\n\t" \
        "v_mfma_f32_16x16x32_bf16 v[92:95], a[48:51], v[128:131], v[92:95]\n\t" \
        "v_cvt_pk_bf16_f32 v69, v70, v71\n\t" \
        "v_cvt_pk_bf16_f32 v70, v72, v73\n\t" \
        "v_mfma_f32_16x16x32_bf16 v[96:99], a[52:55], v[128:131], v[96:99]\n\t" \
        "v_cvt_pk_bf16_f32 v71, v74, v75\n\t" \
        "v_permlane16_swap_b32 v68, v70\n\t" \
        "v_mfma_f32_16x16x32_bf16 v[100:103], a[56:59], v[128:131], v[100:103]\n\t" \
        "ds_read_b128 v[128:131], v15 offset:43648\n\t" \
        "v_permlane16_swap_b32 v69, v71\n\t" \
        "v_mfma_f32_16x16x32_bf16 v[92:95], a[60:63], v[132:135], v[92:95]\n\t" \
        "ds_read_b64 v[78:79], v12 offset:6144\n\t" \
        "v_mfma_f32_16x16x32_bf16 v[96:99], a[64:67], v[132:135], v[96:99]\n\t" \
        "v_mfma_f32_16x16x32_bf16 v[100:103], a[68:71], v[132:135], v[100:103]\n\t" \
        "ds_read_b128 v[132:135], v15 offset:43712\n\t" \
        "v_mfma_f32_16x16x32_bf16 v[92:95], a[72:75], v[136:139], v[92:95]\n\t" \
        "v_mfma_f32_16x16x32_bf16 v[96:99], a[76:79], v[136:139], v[96:99]\n\t" \
        "v_mfma_f32_16x16x32_bf16 v[100:103], a[80:83], v[136:139], v[100:103]\n\t" \
        "ds_read_b128 v[136:139], v15 offset:43776\n\t" \
        "v_mfma_f32_16x16x32_bf16 v[92:95], a[84:87], v[140:143], v[92:95]\n\t" \
        "v_mfma_f32_16x16x32_bf16 v[96:99], a[88:91], v[140:143], v[96:99]\n\t" \
        "v_mfma_f32_16x16x32_bf16 v[100:103], a[92:95], v[140:143], v[100:103]\n\t" \
        "ds_read_b128 v[140:143], v16 offset:43648\n\t" \
        "s_waitcnt lgkmcnt(3)\n\t" \
        "v_permlane16_swap_b32 v76, v78\n\t" \
        "v_mfma_f32_16x16x32_bf16 v[92:95], a[96:99], v[144:147], v[92:95]\n\t" \
        "s_add_u32 s56, s64, 0x54000\n\t" \
        "buffer_store_dwordx4 v[80:83], v9, s[28:31], s56 offen sc1\n\t" \
        "v_mfma_f32_16x16x32_bf16 v[96:99], a[100:103], v[144:147], v[96:99]\n\t" \
        "v_permlane16_swap_b32 v77, v79\n\t" \
        "v_mfma_f32_16x16x32_bf16 v[100:103], a[104:107], v[144:147], v[100:103]\n\t" \
        "ds_read_b128 v[144:147], v16 offset:43712\n\t" \
        "v_mfma_f32_16x16x32_bf16 v[92:95], a[108:111], v[116:119], v[92:95]\n\t" \
        "v_mfma_f32_16x16x32_bf16 v[96:99], a[112:115], v[116:119], v[96:99]\n\t" \
        "v_mfma_f32_16x16x32_bf16 v[100:103], a[116:119], v[116:119], v[100:103]\n\t" \
        "ds_read_b128 v[116:119], v16 offset:43776\n\t" \
        "v_mfma_f32_16x16x32_bf16 v[92:95], a[120:123], v[120:123], v[92:95]\n\t" \
        "v_mfma_f32_16x16x32_bf16 v[96:99], a[124:127], v[120:123], v[96:99]\n\t" \
        "v_mfma_f32_16x16x32_bf16 v[100:103], a[128:131], v[120:123], v[100:103]\n\t" \
        "ds_read_b128 v[120:123], v14 offset:47616\n\t" \
        "v_mfma_f32_16x16x32_bf16 v[92:95], a[132:135], v[124:127], v[92:95]\n\t" \
        "s_add_u32 s56, s64, 0x3f000\n\t" \
        "buffer_store_dwordx4 v[68:71], v9, s[28:31], s56 offen sc1\n\t" \
        "v_mfma_f32_16x16x32_bf16 v[96:99], a[136:139], v[124:127], v[96:99]\n\t" \
        "v_mfma_f32_16x16x32_bf16 v[100:103], a[140:143], v[124:127], v[100:103]\n\t" \
        "ds_read_b128 v[124:127], v14 offset:47680\n\t" \
        "v_mfma_f32_16x16x32_bf16 v[92:95], a[144:147], v[128:131], v[92:95]\n\t" \
        "v_mfma_f32_16x16x32_bf16 v[96:99], a[148:151], v[128:131], v[96:99]\n\t" \
        "v_mfma_f32_16x16x32_bf16 v[100:103], a[152:155], v[128:131], v[100:103]\n\t" \
        "ds_read_b128 v[128:131], v14 offset:47744\n\t" \
        "s_waitcnt lgkmcnt(3)\n\t" \
        "v_mfma_f32_16x16x32_bf16 v[92:95], a[156:159], v[132:135], v[92:95]\n\t" \
        "v_mfma_f32_16x16x32_bf16 v[96:99], a[160:163], v[132:135], v[96:99]\n\t" \
        "s_add_u32 s56, s64, 0x3f000\n\t" \
        "buffer_store_dwordx4 v[76:79], v10, s[28:31], s56 offen sc1\n\t" \
        "v_mfma_f32_16x16x32_bf16 v[100:103], a[164:167], v[132:135], v[100:103]\n\t" \
        "ds_read_b128 v[132:135], v15 offset:47616\n\t" \
        "v_mfma_f32_16x16x32_bf16 v[92:95], a[168:171], v[136:139], v[92:95]\n\t" \
        "v_mfma_f32_16x16x32_bf16 v[96:99], a[172:175], v[136:139], v[96:99]\n\t" \
        "v_mfma_f32_16x16x32_bf16 v[100:103], a[176:179], v[136:139], v[100:103]\n\t" \
        "ds_read_b128 v[136:139], v15 offset:47680\n\t" \
        "v_mfma_f32_16x16x32_bf16 v[92:95], a[180:183], v[140:143], v[92:95]\n\t" \
        "v_mfma_f32_16x16x32_bf16 v[96:99], a[184:187], v[140:143], v[96:99]\n\t" \
        "v_mfma_f32_16x16x32_bf16 v[100:103], a[188:191], v[140:143], v[100:103]\n\t" \
        "ds_read_b128 v[140:143], v15 offset:47744\n\t" \
        "v_mfma_f32_16x16x32_bf16 v[92:95], a[192:195], v[144:147], v[92:95]\n\t" \
        "v_mfma_f32_16x16x32_bf16 v[96:99], a[196:199], v[144:147], v[96:99]\n\t" \
        "v_mfma_f32_16x16x32_bf16 v[100:103], a[200:203], v[144:147], v[100:103]\n\t" \
        "ds_read_b128 v[144:147], v16 offset:47616\n\t" \
        "v_mfma_f32_16x16x32_bf16 v[92:95], a[204:207], v[116:119], v[92:95]\n\t" \
        "v_mfma_f32_16x16x32_bf16 v[96:99], a[208:211], v[116:119], v[96:99]\n\t" \
        "v_mfma_f32_16x16x32_bf16 v[100:103], a[212:215], v[116:119], v[100:103]\n\t" \
        "ds_read_b128 v[116:119], v16 offset:47680\n\t" \
        "s_waitcnt lgkmcnt(3)\n\t" \
        "v_mfma_f32_16x16x32_bf16 v[92:95], a[216:219], v[120:123], v[92:95]\n\t" \
        "v_mfma_f32_16x16x32_bf16 v[96:99], a[220:223], v[120:123], v[96:99]\n\t" \
        "v_mfma_f32_16x16x32_bf16 v[100:103], a[224:227], v[120:123], v[100:103]\n\t" \
        "ds_read_b128 v[120:123], v16 offset:47744\n\t" \
        "v_mfma_f32_16x16x32_bf16 v[92:95], a[228:231], v[124:127], v[92:95]\n\t" \
        "v_mfma_f32_16x16x32_bf16 v[96:99], a[232:235], v[124:127], v[96:99]\n\t" \
        "v_mfma_f32_16x16x32_bf16 v[100:103], a[236:239], v[124:127], v[100:103]\n\t" \
        "ds_read_b128 v[124:127], v14 offset:47616\n\t" \
        "v_mfma_f32_16x16x32_bf16 v[92:95], a[240:243], v[128:131], v[92:95]\n\t" \
        "v_mfma_f32_16x16x32_bf16 v[96:99], a[244:247], v[128:131], v[96:99]\n\t" \
        "v_mfma_f32_16x16x32_bf16 v[100:103], a[248:251], v[128:131], v[100:103]\n\t" \
        "ds_read_b128 v[128:131], v14 offset:47680\n\t" \
        "v_mfma_f32_16x16x32_bf16 v[92:95], a[252:255], v[132:135], v[92:95]\n\t" \
        "v_mfma_f32_16x16x32_bf16 v[96:99], v[188:191], v[132:135], v[96:99]\n\t" \
        "v_mfma_f32_16x16x32_bf16 v[100:103], v[192:195], v[132:135], v[100:103]\n\t" \
        "ds_read_b128 v[132:135], v14 offset:47744\n\t" \
        "v_mfma_f32_16x16x32_bf16 v[92:95], v[196:199], v[136:139], v[92:95]\n\t" \
        "v_mfma_f32_16x16x32_bf16 v[96:99], v[200:203], v[136:139], v[96:99]\n\t" \
        "v_mfma_f32_16x16x32_bf16 v[100:103], v[204:207], v[136:139], v[100:103]\n\t" \
        "ds_read_b128 v[136:139], v15 offset:47616\n\t" \
        "s_waitcnt lgkmcnt(3)\n\t" \
        "v_mfma_f32_16x16x32_bf16 v[92:95], v[208:211], v[140:143], v[92:95]\n\t" \
        "v_mfma_f32_16x16x32_bf16 v[96:99], v[212:215], v[140:143], v[96:99]\n\t" \
        "v_mfma_f32_16x16x32_bf16 v[100:103], v[216:219], v[140:143], v[100:103]\n\t" \
        "ds_read_b128 v[140:143], v15 offset:47680\n\t" \
        "v_mfma_f32_16x16x32_bf16 v[92:95], v[220:223], v[144:147], v[92:95]\n\t" \
        "v_mfma_f32_16x16x32_bf16 v[96:99], v[224:227], v[144:147], v[96:99]\n\t" \
        "v_mfma_f32_16x16x32_bf16 v[100:103], v[228:231], v[144:147], v[100:103]\n\t" \
        "ds_read_b128 v[144:147], v15 offset:47744\n\t" \
        "v_mfma_f32_16x16x32_bf16 v[92:95], v[232:235], v[116:119], v[92:95]\n\t" \
        "v_mfma_f32_16x16x32_bf16 v[96:99], v[236:239], v[116:119], v[96:99]\n\t" \
        "v_mfma_f32_16x16x32_bf16 v[100:103], v[240:243], v[116:119], v[100:103]\n\t" \
        "ds_read_b128 v[116:119], v16 offset:47616\n\t" \
        "v_mfma_f32_16x16x32_bf16 v[92:95], v[244:247], v[120:123], v[92:95]\n\t" \
        "v_mfma_f32_16x16x32_bf16 v[96:99], v[248:251], v[120:123], v[96:99]\n\t" \
        "v_mfma_f32_16x16x32_bf16 v[100:103], v[252:255], v[120:123], v[100:103]\n\t" \
        "s_bitcmp0_b32 s34, 4\n\t" \
        "s_cbranch_scc1 .LR14%=\n\t" \
        "ds_read_b128 v[120:123], v16 offset:47680\n\t" \
        "v_mfma_f32_16x16x32_bf16 v[104:107], a[0:3], v[124:127], 0\n\t" \
        "v_mfma_f32_16x16x32_bf16 v[108:111], a[4:7], v[124:127], 0\n\t" \
        "v_mfma_f32_16x16x32_bf16 v[112:115], a[8:11], v[124:127], 0\n\t" \
        "s_nop 3\n\t" \
        "v_cvt_pk_bf16_f32 v100, v100, v101\n\t" \
        "v_cvt_pk_bf16_f32 v101, v102, v103\n\t" \
        "s_waitcnt lgkmcnt(2)\n\t" \
        "v_mfma_f32_16x16x32_bf16 v[104:107], a[12:15], v[128:131], v[104:107]\n\t" \
        "ds_write_b64 v11, v[100:101] offset:10240\n\t" \
        "v_cvt_pk_bf16_f32 v92, v92, v93\n\t" \
        "v_mfma_f32_16x16x32_bf16 v[108:111], a[16:19], v[128:131], v[108:111]\n\t" \
        "ds_read_b128 v[124:127], v16 offset:47744\n\t" \
        "v_cvt_pk_bf16_f32 v93, v94, v95\n\t" \
        "v_mfma_f32_16x16x32_bf16 v[112:115], a[20:23], v[128:131], v[112:115]\n\t" \
        "ds_read_b128 v[128:131], v14 offset:51584\n\t" \
        "v_cvt_pk_bf16_f32 v94, v96, v97\n\t" \
        "v_mfma_f32_16x16x32_bf16 v[104:107], a[24:27], v[132:135], v[104:107]\n\t" \
        "v_cvt_pk_bf16_f32 v95, v98, v99\n\t" \
        "v_permlane16_swap_b32 v92, v94\n\t" \
        "v_mfma_f32_16x16x32_bf16 v[108:111], a[28:31], v[132:135], v[108:111]\n\t" \
        "v_permlane16_swap_b32 v93, v95\n\t" \
        "v_mfma_f32_16x16x32_bf16 v[112:115], a[32:35], v[132:135], v[112:115]\n\t" \
        "ds_read_b128 v[132:135], v14 offset:51648\n\t" \
        "v_mfma_f32_16x16x32_bf16 v[104:107], a[36:39], v[136:139], v[104:107]\n\t" \
        "v_mfma_f32_16x16x32_bf16 v[108:111], a[40:43], v[136:139], v[108:111]\n\t" \
        "v_mfma_f32_16x16x32_bf16 v[112:115], a[44:47], v[136:139], v[112:115]\n\t" \
        "ds_read_b128 v[136:139], v14 offset:51712\n\t" \
        "v_mfma_f32_16x16x32_bf16 v[104:107], a[48:51], v[140:143], v[104:107]\n\t" \
        "v_mfma_f32_16x16x32_bf16 v[108:111], a[52:55], v[140:143], v[108:111]\n\t" \
        "s_waitcnt lgkmcnt(4)\n\t" \
        "s_barrier\n\t" \
        "ds_read_b64 v[90:91], v12 offset:8192\n\t" \
        "v_mfma_f32_16x16x32_bf16 v[112:115], a[56:59], v[140:143], v[112:115]\n\t" \
        "ds_read_b128 v[140:143], v15 offset:51584\n\t" \
        "v_mfma_f32_16x16x32_bf16 v[104:107], a[60:63], v[144:147], v[104:107]\n\t" \
        "ds_read_b64 v[102:103], v12 offset:10240\n\t" \
        "v_mfma_f32_16x16x32_bf16 v[108:111], a[64:67], v[144:147], v[108:111]\n\t" \
        "v_mfma_f32_16x16x32_bf16 v[112:115], a[68:71], v[144:147], v[112:115]\n\t" \
        "ds_read_b128 v[144:147], v15 offset:51648\n\t" \
        "v_mfma_f32_16x16x32_bf16 v[104:107], a[72:75], v[116:119], v[104:107]\n\t" \
        "v_mfma_f32_16x16x32_bf16 v[108:111], a[76:79], v[116:119], v[108:111]\n\t" \
        "v_mfma_f32_16x16x32_bf16 v[112:115], a[80:83], v[116:119], v[112:115]\n\t" \
        "ds_read_b128 v[116:119], v15 offset:51712\n\t" \
        "v_mfma_f32_16x16x32_bf16 v[104:107], a[84:87], v[120:123], v[104:107]\n\t" \
        "s_waitcnt lgkmcnt(4)\n\t" \
        "v_permlane16_swap_b32 v88, v90\n\t" \
        "v_permlane16_swap_b32 v89, v91\n\t" \
        "v_mfma_f32_16x16x32_bf16 v[108:111], a[88:91], v[120:123], v[108:111]\n\t" \
        "v_mfma_f32_16x16x32_bf16 v[112:115], a[92:95], v[120:123], v[112:115]\n\t" \
        "s_add_u32 s56, s64, 0x54000\n\t" \
        "buffer_store_dwordx4 v[88:91], v10, s[28:31], s56 offen sc1\n\t" \
        "v_mfma_f32_16x16x32_bf16 v[104:107], a[96:99], v[124:127], v[104:107]\n\t" \
        "s_add_u32 s56, s64, 0x69000\n\t" \
        "buffer_store_dwordx4 v[92:95], v9, s[28:31], s56 offen sc1\n\t" \
        "v_mfma_f32_16x16x32_bf16 v[108:111], a[100:103], v[124:127], v[108:111]\n\t" \
        "ds_read_b128 v[120:123], v16 offset:51584\n\t" \
        "s_waitcnt lgkmcnt(2)\n\t" \
        "v_permlane16_swap_b32 v100, v102\n\t" \
        "v_mfma_f32_16x16x32_bf16 v[112:115], a[104:107], v[124:127], v[112:115]\n\t" \
        "ds_read_b128 v[124:127], v16 offset:51648\n\t" \
        "v_permlane16_swap_b32 v101, v103\n\t" \
        "v_mfma_f32_16x16x32_bf16 v[104:107], a[108:111], v[128:131], v[104:107]\n\t" \
        "v_mfma_f32_16x16x32_bf16 v[108:111], a[112:115], v[128:131], v[108:111]\n\t" \
        "v_mfma_f32_16x16x32_bf16 v[112:115], a[116:119], v[128:131], v[112:115]\n\t" \
        "ds_read_b128 v[128:131], v16 offset:51712\n\t" \
        "v_mfma_f32_16x16x32_bf16 v[104:107], a[120:123], v[132:135], v[104:107]\n\t" \
        "v_mfma_f32_16x16x32_bf16 v[108:111], a[124:127], v[132:135], v[108:111]\n\t" \
        "s_add_u32 s56, s64, 0x69000\n\t" \
        "buffer_store_dwordx4 v[100:103], v10, s[28:31], s56 offen sc1\n\t" \
        "v_mfma_f32_16x16x32_bf16 v[112:115], a[128:131], v[132:135], v[112:115]\n\t" \
        "ds_read_b128 v[132:135], v14 offset:55552\n\t" \
        "v_mfma_f32_16x16x32_bf16 v[104:107], a[132:135], v[136:139], v[104:107]\n\t" \
        "v_mfma_f32_16x16x32_bf16 v[108:111], a[136:139], v[136:139], v[108:111]\n\t" \
        "v_mfma_f32_16x16x32_bf16 v[112:115], a[140:143], v[136:139], v[112:115]\n\t" \
        "ds_read_b128 v[136:139], v14 offset:55616\n\t" \
        "v_mfma_f32_16x16x32_bf16 v[104:107], a[144:147], v[140:143], v[104:107]\n\t" \
        "v_mfma_f32_16x16x32_bf16 v[108:111], a[148:151], v[140:143], v[108:111]\n\t" \
        "v_mfma_f32_16x16x32_bf16 v[112:115], a[152:155], v[140:143], v[112:115]\n\t" \
        "ds_read_b128 v[140:143], v14 offset:55680\n\t" \
        "v_mfma_f32_16x16x32_bf16 v[104:107], a[156:159], v[144:147], v[104:107]\n\t" \
        "v_mfma_f32_16x16x32_bf16 v[108:111], a[160:163], v[144:147], v[108:111]\n\t" \
        "v_mfma_f32_16x16x32_bf16 v[112:115], a[164:167], v[144:147], v[112:115]\n\t" \
        "ds_read_b128 v[144:147], v15 offset:55552\n\t" \
        "s_waitcnt lgkmcnt(3)\n\t" \
        "v_mfma_f32_16x16x32_bf16 v[104:107], a[168:171], v[116:119], v[104:107]\n\t" \
        "v_mfma_f32_16x16x32_bf16 v[108:111], a[172:175], v[116:119], v[108:111]\n\t" \
        "v_mfma_f32_16x16x32_bf16 v[112:115], a[176:179], v[116:119], v[112:115]\n\t" \
        "ds_read_b128 v[116:119], v15 offset:55616\n\t" \
        "v_mfma_f32_16x16x32_bf16 v[104:107], a[180:183], v[120:123], v[104:107]\n\t" \
        "v_mfma_f32_16x16x32_bf16 v[108:111], a[184:187], v[120:123], v[108:111]\n\t" \
        "v_mfma_f32_16x16x32_bf16 v[112:115], a[188:191], v[120:123], v[112:115]\n\t" \
        "ds_read_b128 v[120:123], v15 offset:55680\n\t" \
        "v_mfma_f32_16x16x32_bf16 v[104:107], a[192:195], v[124:127], v[104:107]\n\t" \
        "v_mfma_f32_16x16x32_bf16 v[108:111], a[196:199], v[124:127], v[108:111]\n\t" \
        "v_mfma_f32_16x16x32_bf16 v[112:115], a[200:203], v[124:127], v[112:115]\n\t" \
        "ds_read_b128 v[124:127], v16 offset:55552\n\t" \
        "v_mfma_f32_16x16x32_bf16 v[104:107], a[204:207], v[128:131], v[104:107]\n\t" \
        "v_mfma_f32_16x16x32_bf16 v[108:111], a[208:211], v[128:131], v[108:111]\n\t" \
        "v_mfma_f32_16x16x32_bf16 v[112:115], a[212:215], v[128:131], v[112:115]\n\t" \
        "ds_read_b128 v[128:131], v16 offset:55616\n\t" \
        "v_mfma_f32_16x16x32_bf16 v[104:107], a[216:219], v[132:135], v[104:107]\n\t" \
        "v_mfma_f32_16x16x32_bf16 v[108:111], a[220:223], v[132:135], v[108:111]\n\t" \
        "v_mfma_f32_16x16x32_bf16 v[112:115], a[224:227], v[132:135], v[112:115]\n\t" \
        "ds_read_b128 v[132:135], v16 offset:55680\n\t" \
        "s_waitcnt lgkmcnt(3)\n\t" \
        "v_mfma_f32_16x16x32_bf16 v[104:107], a[228:231], v[136:139], v[104:107]\n\t" \
        "v_mfma_f32_16x16x32_bf16 v[108:111], a[232:235], v[136:139], v[108:111]\n\t" \
        "v_mfma_f32_16x16x32_bf16 v[112:115], a[236:239], v[136:139], v[112:115]\n\t" \
        "ds_read_b128 v[136:139], v14 offset:55552\n\t" \
        "v_mfma_f32_16x16x32_bf16 v[104:107], a[240:243], v[140:143], v[104:107]\n\t" \
        "v_mfma_f32_16x16x32_bf16 v[108:111], a[244:247], v[140:143], v[108:111]\n\t" \
        "v_mfma_f32_16x16x32_bf16 v[112:115], a[248:251], v[140:143], v[112:115]\n\t" \
        "ds_read_b128 v[140:143], v14 offset:55616\n\t" \
        "v_mfma_f32_16x16x32_bf16 v[104:107], a[252:255], v[144:147], v[104:107]\n\t" \
        "v_mfma_f32_16x16x32_bf16 v[108:111], v[188:191], v[144:147], v[108:111]\n\t" \
        "v_mfma_f32_16x16x32_bf16 v[112:115], v[192:195], v[144:147], v[112:115]\n\t" \
        "ds_read_b128 v[144:147], v14 offset:55680\n\t" \
        "v_mfma_f32_16x16x32_bf16 v[104:107], v[196:199], v[116:119], v[104:107]\n\t" \
        "v_mfma_f32_16x16x32_bf16 v[108:111], v[200:203], v[116:119], v[108:111]\n\t" \
        "v_mfma_f32_16x16x32_bf16 v[112:115], v[204:207], v[116:119], v[112:115]\n\t" \
        "ds_read_b128 v[116:119], v15 offset:55552\n\t" \
        "v_mfma_f32_16x16x32_bf16 v[104:107], v[208:211], v[120:123], v[104:107]\n\t" \
        "v_mfma_f32_16x16x32_bf16 v[108:111], v[212:215], v[120:123], v[108:111]\n\t" \
        "v_mfma_f32_16x16x32_bf16 v[112:115], v[216:219], v[120:123], v[112:115]\n\t" \
        "ds_read_b128 v[120:123], v15 offset:55616\n\t" \
        "s_waitcnt lgkmcnt(3)\n\t" \
        "v_mfma_f32_16x16x32_bf16 v[104:107], v[220:223], v[124:127], v[104:107]\n\t" \
        "v_mfma_f32_16x16x32_bf16 v[108:111], v[224:227], v[124:127], v[108:111]\n\t" \
        "v_mfma_f32_16x16x32_bf16 v[112:115], v[228:231], v[124:127], v[112:115]\n\t" \
        "ds_read_b128 v[124:127], v15 offset:55680\n\t" \
        "v_mfma_f32_16x16x32_bf16 v[104:107], v[232:235], v[128:131], v[104:107]\n\t" \
        "v_mfma_f32_16x16x32_bf16 v[108:111], v[236:239], v[128:131], v[108:111]\n\t" \
        "v_mfma_f32_16x16x32_bf16 v[112:115], v[240:243], v[128:131], v[112:115]\n\t" \
        "ds_read_b128 v[128:131], v16 offset:55552\n\t" \
        "v_mfma_f32_16x16x32_bf16 v[104:107], v[244:247], v[132:135], v[104:107]\n\t" \
        "v_mfma_f32_16x16x32_bf16 v[108:111], v[248:251], v[132:135], v[108:111]\n\t" \
        "v_mfma_f32_16x16x32_bf16 v[112:115], v[252:255], v[132:135], v[112:115]\n\t" \
        "ds_read_b128 v[132:135], v16 offset:55616\n\t" \
        "v_mfma_f32_16x16x32_bf16 v[32:35], a[0:3], v[136:139], 0\n\t" \
        "v_mfma_f32_16x16x32_bf16 v[36:39], a[4:7], v[136:139], 0\n\t" \
        "v_mfma_f32_16x16x32_bf16 v[40:43], a[8:11], v[136:139], 0\n\t" \
        "s_nop 5\n\t" \
        "v_cvt_pk_bf16_f32 v112, v112, v113\n\t" \
        "v_cvt_pk_bf16_f32 v113, v114, v115\n\t" \
        "v_mfma_f32_16x16x32_bf16 v[32:35], a[12:15], v[140:143], v[32:35]\n\t" \
        "ds_write_b64 v11, v[112:113] offset:12288\n\t" \
        "v_cvt_pk_bf16_f32 v104, v104, v105\n\t" \
        "v_mfma_f32_16x16x32_bf16 v[36:39], a[16:19], v[140:143], v[36:39]\n\t" \
        "ds_read_b128 v[136:139], v16 offset:55680\n\t" \
        "v_cvt_pk_bf16_f32 v105, v106, v107\n\t" \
        "v_mfma_f32_16x16x32_bf16 v[40:43], a[20:23], v[140:143], v[40:43]\n\t" \
        "ds_read_b128 v[140:143], v14 offset:59520\n\t" \
        "v_cvt_pk_bf16_f32 v106, v108, v109\n\t" \
        "s_waitcnt lgkmcnt(4)\n\t" \
        "v_mfma_f32_16x16x32_bf16 v[32:35], a[24:27], v[144:147], v[32:35]\n\t" \
        "v_cvt_pk_bf16_f32 v107, v110, v111\n\t" \
        "v_permlane16_swap_b32 v104, v106\n\t" \
        "v_mfma_f32_16x16x32_bf16 v[36:39], a[28:31], v[144:147], v[36:39]\n\t" \
        "v_permlane16_swap_b32 v105, v107\n\t" \
        "v_mfma_f32_16x16x32_bf16 v[40:43], a[32:35], v[144:147], v[40:43]\n\t" \
        "ds_read_b128 v[144:147], v14 offset:59584\n\t" \
        "v_mfma_f32_16x16x32_bf16 v[32:35], a[36:39], v[116:119], v[32:35]\n\t" \
        "v_mfma_f32_16x16x32_bf16 v[36:39], a[40:43], v[116:119], v[36:39]\n\t" \
        "v_mfma_f32_16x16x32_bf16 v[40:43], a[44:47], v[116:119], v[40:43]\n\t" \
        "ds_read_b128 v[116:119], v14 offset:59648\n\t" \
        "v_mfma_f32_16x16x32_bf16 v[32:35], a[48:51], v[120:123], v[32:35]\n\t" \
        "v_mfma_f32_16x16x32_bf16 v[36:39], a[52:55], v[120:123], v[36:39]\n\t" \
        "s_waitcnt lgkmcnt(4)\n\t" \
        "s_barrier\n\t" \
        "ds_read_b64 v[114:115], v12 offset:12288\n\t" \
        "v_mfma_f32_16x16x32_bf16 v[40:43], a[56:59], v[120:123], v[40:43]\n\t" \
        "ds_read_b128 v[120:123], v15 offset:59520\n\t" \
        "v_mfma_f32_16x16x32_bf16 v[32:35], a[60:63], v[124:127], v[32:35]\n\t" \
        "v_mfma_f32_16x16x32_bf16 v[36:39], a[64:67], v[124:127], v[36:39]\n\t" \
        "v_mfma_f32_16x16x32_bf16 v[40:43], a[68:71], v[124:127], v[40:43]\n\t" \
        "ds_read_b128 v[124:127], v15 offset:59584\n\t" \
        "v_mfma_f32_16x16x32_bf16 v[32:35], a[72:75], v[128:131], v[32:35]\n\t" \
        "v_mfma_f32_16x16x32_bf16 v[36:39], a[76:79], v[128:131], v[36:39]\n\t" \
        "v_mfma_f32_16x16x32_bf16 v[40:43], a[80:83], v[128:131], v[40:43]\n\t" \
        "ds_read_b128 v[128:131], v15 offset:59648\n\t" \
        "v_mfma_f32_16x16x32_bf16 v[32:35], a[84:87], v[132:135], v[32:35]\n\t" \
        "s_waitcnt lgkmcnt(3)\n\t" \
        "v_permlane16_swap_b32 v112, v114\n\t" \
        "v_permlane16_swap_b32 v113, v115\n\t" \
        "v_mfma_f32_16x16x32_bf16 v[36:39], a[88:91], v[132:135], v[36:39]\n\t" \
        "v_mfma_f32_16x16x32_bf16 v[40:43], a[92:95], v[132:135], v[40:43]\n\t" \
        "ds_read_b128 v[132:135], v16 offset:59520\n\t" \
        "v_mfma_f32_16x16x32_bf16 v[32:35], a[96:99], v[136:139], v[32:35]\n\t" \
        "s_add_u32 s56, s64, 0x7e000\n\t" \
        "buffer_store_dwordx4 v[104:107], v9, s[28:31], s56 offen sc1\n\t" \
        "v_mfma_f32_16x16x32_bf16 v[36:39], a[100:103], v[136:139], v[36:39]\n\t" \
        "v_mfma_f32_16x16x32_bf16 v[40:43], a[104:107], v[136:139], v[40:43]\n\t" \
        "ds_read_b128 v[136:139], v16 offset:59584\n\t" \
        "v_mfma_f32_16x16x32_bf16 v[32:35], a[108:111], v[140:143], v[32:35]\n\t" \
        "v_mfma_f32_16x16x32_bf16 v[36:39], a[112:115], v[140:143], v[36:39]\n\t" \
        "v_mfma_f32_16x16x32_bf16 v[40:43], a[116:119], v[140:143], v[40:43]\n\t" \
        "ds_read_b128 v[140:143], v16 offset:59648\n\t" \
        "v_mfma_f32_16x16x32_bf16 v[32:35], a[120:123], v[144:147], v[32:35]\n\t" \
        "v_mfma_f32_16x16x32_bf16 v[36:39], a[124:127], v[144:147], v[36:39]\n\t" \
        "s_add_u32 s56, s64, 0x7e000\n\t" \
        "buffer_store_dwordx4 v[112:115], v10, s[28:31], s56 offen sc1\n\t" \
        "v_mfma_f32_16x16x32_bf16 v[40:43], a[128:131], v[144:147], v[40:43]\n\t" \
        "ds_read_b128 v[144:147], v14 offset:63488\n\t" \
        "v_mfma_f32_16x16x32_bf16 v[32:35], a[132:135], v[116:119], v[32:35]\n\t" \
        "v_mfma_f32_16x16x32_bf16 v[36:39], a[136:139], v[116:119], v[36:39]\n\t" \
        "v_mfma_f32_16x16x32_bf16 v[40:43], a[140:143], v[116:119], v[40:43]\n\t" \
        "ds_read_b128 v[116:119], v14 offset:63552\n\t" \
        "s_waitcnt lgkmcnt(3)\n\t" \
        "v_mfma_f32_16x16x32_bf16 v[32:35], a[144:147], v[120:123], v[32:35]\n\t" \
        "v_mfma_f32_16x16x32_bf16 v[36:39], a[148:151], v[120:123], v[36:39]\n\t" \
        "v_mfma_f32_16x16x32_bf16 v[40:43], a[152:155], v[120:123], v[40:43]\n\t" \
        "ds_read_b128 v[120:123], v14 offset:63616\n\t" \
        "v_mfma_f32_16x16x32_bf16 v[32:35], a[156:159], v[124:127], v[32:35]\n\t" \
        "v_mfma_f32_16x16x32_bf16 v[36:39], a[160:163], v[124:127], v[36:39]\n\t" \
        "v_mfma_f32_16x16x32_bf16 v[40:43], a[164:167], v[124:127], v[40:43]\n\t" \
        "ds_read_b128 v[124:127], v15 offset:63488\n\t" \
        "v_mfma_f32_16x16x32_bf16 v[32:35], a[168:171], v[128:131], v[32:35]\n\t" \
        "v_mfma_f32_16x16x32_bf16 v[36:39], a[172:175], v[128:131], v[36:39]\n\t" \
        "v_mfma_f32_16x16x32_bf16 v[40:43], a[176:179], v[128:131], v[40:43]\n\t" \
        "ds_read_b128 v[128:131], v15 offset:63552\n\t" \
        "v_mfma_f32_16x16x32_bf16 v[32:35], a[180:183], v[132:135], v[32:35]\n\t" \
        "v_mfma_f32_16x16x32_bf16 v[36:39], a[184:187], v[132:135], v[36:39]\n\t" \
        "v_mfma_f32_16x16x32_bf16 v[40:43], a[188:191], v[132:135], v[40:43]\n\t" \
        "ds_read_b128 v[132:135], v15 offset:63616\n\t" \
        "v_mfma_f32_16x16x32_bf16 v[32:35], a[192:195], v[136:139], v[32:35]\n\t" \
        "v_mfma_f32_16x16x32_bf16 v[36:39], a[196:199], v[136:139], v[36:39]\n\t" \
        "v_mfma_f32_16x16x32_bf16 v[40:43], a[200:203], v[136:139], v[40:43]\n\t" \
        "ds_read_b128 v[136:139], v16 offset:63488\n\t" \
        "s_waitcnt lgkmcnt(3)\n\t" \
        "v_mfma_f32_16x16x32_bf16 v[32:35], a[204:207], v[140:143], v[32:35]\n\t" \
        "v_mfma_f32_16x16x32_bf16 v[36:39], a[208:211], v[140:143], v[36:39]\n\t" \
        "v_mfma_f32_16x16x32_bf16 v[40:43], a[212:215], v[140:143], v[40:43]\n\t" \
        "ds_read_b128 v[140:143], v16 offset:63552\n\t" \
        "v_mfma_f32_16x16x32_bf16 v[32:35], a[216:219], v[144:147], v[32:35]\n\t" \
        "v_mfma_f32_16x16x32_bf16 v[36:39], a[220:223], v[144:147], v[36:39]\n\t" \
        "v_mfma_f32_16x16x32_bf16 v[40:43], a[224:227], v[144:147], v[40:43]\n\t" \
        "ds_read_b128 v[144:147], v16 offset:63616\n\t" \
        "v_mfma_f32_16x16x32_bf16 v[32:35], a[228:231], v[116:119], v[32:35]\n\t" \
        "v_mfma_f32_16x16x32_bf16 v[36:39], a[232:235], v[116:119], v[36:39]\n\t" \
        "v_mfma_f32_16x16x32_bf16 v[40:43], a[236:239], v[116:119], v[40:43]\n\t" \
        "v_mfma_f32_16x16x32_bf16 v[32:35], a[240:243], v[120:123], v[32:35]\n\t" \
        "v_mfma_f32_16x16x32_bf16 v[36:39], a[244:247], v[120:123], v[36:39]\n\t" \
        "v_mfma_f32_16x16x32_bf16 v[40:43], a[248:251], v[120:123], v[40:43]\n\t" \
        "v_mfma_f32_16x16x32_bf16 v[32:35], a[252:255], v[124:127], v[32:35]\n\t" \
        "v_mfma_f32_16x16x32_bf16 v[36:39], v[188:191], v[124:127], v[36:39]\n\t" \
        "v_mfma_f32_16x16x32_bf16 v[40:43], v[192:195], v[124:127], v[40:43]\n\t" \
        "s_waitcnt lgkmcnt(0)\n\t" \
        "v_mfma_f32_16x16x32_bf16 v[32:35], v[196:199], v[128:131], v[32:35]\n\t" \
        "v_mfma_f32_16x16x32_bf16 v[36:39], v[200:203], v[128:131], v[36:39]\n\t" \
        "v_mfma_f32_16x16x32_bf16 v[40:43], v[204:207], v[128:131], v[40:43]\n\t" \
        "v_mfma_f32_16x16x32_bf16 v[32:35], v[208:211], v[132:135], v[32:35]\n\t" \
        "v_mfma_f32_16x16x32_bf16 v[36:39], v[212:215], v[132:135], v[36:39]\n\t" \
        "v_mfma_f32_16x16x32_bf16 v[40:43], v[216:219], v[132:135], v[40:43]\n\t" \
        "v_mfma_f32_16x16x32_bf16 v[32:35], v[220:223], v[136:139], v[32:35]\n\t" \
        "v_mfma_f32_16x16x32_bf16 v[36:39], v[224:227], v[136:139], v[36:39]\n\t" \
        "v_mfma_f32_16x16x32_bf16 v[40:43], v[228:231], v[136:139], v[40:43]\n\t" \
        "v_mfma_f32_16x16x32_bf16 v[32:35], v[232:235], v[140:143], v[32:35]\n\t" \
        "v_mfma_f32_16x16x32_bf16 v[36:39], v[236:239], v[140:143], v[36:39]\n\t" \
        "v_mfma_f32_16x16x32_bf16 v[40:43], v[240:243], v[140:143], v[40:43]\n\t" \
        "v_mfma_f32_16x16x32_bf16 v[32:35], v[244:247], v[144:147], v[32:35]\n\t" \
        "v_mfma_f32_16x16x32_bf16 v[36:39], v[248:251], v[144:147], v[36:39]\n\t" \
        "v_mfma_f32_16x16x32_bf16 v[40:43], v[252:255], v[144:147], v[40:43]\n\t" \
        "s_nop 9\n\t" \
        "v_cvt_pk_bf16_f32 v40, v40, v41\n\t" \
        "v_cvt_pk_bf16_f32 v41, v42, v43\n\t" \
        "v_cvt_pk_bf16_f32 v32, v32, v33\n\t" \
        "v_cvt_pk_bf16_f32 v33, v34, v35\n\t" \
        "v_cvt_pk_bf16_f32 v34, v36, v37\n\t" \
        "ds_write_b64 v11, v[40:41] offset:14336\n\t" \
        "v_cvt_pk_bf16_f32 v35, v38, v39\n\t" \
        "v_permlane16_swap_b32 v32, v34\n\t" \
        "s_nop 0\n\t" \
        "v_permlane16_swap_b32 v33, v35\n\t" \
        "s_add_u32 s56, s64, 0x93000\n\t" \
        "buffer_store_dwordx4 v[32:35], v9, s[28:31], s56 offen sc1\n\t" \
        "s_waitcnt lgkmcnt(0)\n\t" \
        "s_barrier\n\t" \
        "ds_read_b64 v[42:43], v12 offset:14336\n\t" \
        "s_waitcnt lgkmcnt(0)\n\t" \
        "v_permlane16_swap_b32 v40, v42\n\t" \
        "v_permlane16_swap_b32 v41, v43\n\t" \
        "s_add_u32 s56, s64, 0x93000\n\t" \
        "buffer_store_dwordx4 v[40:43], v10, s[28:31], s56 offen sc1\n\t" \
        "s_endpgm\n\t" \
        ".LR14%=:\n\t" \
        "ds_read_b128 v[120:123], v16 offset:47680\n\t" \
        "v_mfma_f32_16x16x32_bf16 v[104:107], a[0:3], v[124:127], 0\n\t" \
        "v_mfma_f32_16x16x32_bf16 v[108:111], a[4:7], v[124:127], 0\n\t" \
        "v_mfma_f32_16x16x32_bf16 v[112:115], a[8:11], v[124:127], 0\n\t" \
        "s_nop 3\n\t" \
        "v_cvt_pk_bf16_f32 v100, v100, v101\n\t" \
        "v_cvt_pk_bf16_f32 v101, v102, v103\n\t" \
        "s_waitcnt lgkmcnt(2)\n\t" \
        "v_mfma_f32_16x16x32_bf16 v[104:107], a[12:15], v[128:131], v[104:107]\n\t" \
        "ds_write_b64 v11, v[100:101] offset:10240\n\t" \
        "v_cvt_pk_bf16_f32 v92, v92, v93\n\t" \
        "v_mfma_f32_16x16x32_bf16 v[108:111], a[16:19], v[128:131], v[108:111]\n\t" \
        "ds_read_b128 v[124:127], v16 offset:47744\n\t" \
        "v_cvt_pk_bf16_f32 v93, v94, v95\n\t" \
        "v_mfma_f32_16x16x32_bf16 v[112:115], a[20:23], v[128:131], v[112:115]\n\t" \
        "ds_read_b128 v[128:131], v14 offset:51584\n\t" \
        "v_cvt_pk_bf16_f32 v94, v96, v97\n\t" \
        "v_mfma_f32_16x16x32_bf16 v[104:107], a[24:27], v[132:135], v[104:107]\n\t" \
        "v_cvt_pk_bf16_f32 v95, v98, v99\n\t" \
        "v_permlane16_swap_b32 v92, v94\n\t" \
        "v_mfma_f32_16x16x32_bf16 v[108:111], a[28:31], v[132:135], v[108:111]\n\t" \
        "v_permlane16_swap_b32 v93, v95\n\t" \
        "v_mfma_f32_16x16x32_bf16 v[112:115], a[32:35], v[132:135], v[112:115]\n\t" \
        "ds_read_b128 v[132:135], v14 offset:51648\n\t" \
        "v_mfma_f32_16x16x32_bf16 v[104:107], a[36:39], v[136:139], v[104:107]\n\t" \
        "v_mfma_f32_16x16x32_bf16 v[108:111], a[40:43], v[136:139], v[108:111]\n\t" \
        "v_mfma_f32_16x16x32_bf16 v[112:115], a[44:47], v[136:139], v[112:115]\n\t" \
        "ds_read_b128 v[136:139], v14 offset:51712\n\t" \
        "v_mfma_f32_16x16x32_bf16 v[104:107], a[48:51], v[140:143], v[104:107]\n\t" \
        "v_mfma_f32_16x16x32_bf16 v[108:111], a[52:55], v[140:143], v[108:111]\n\t" \
        "s_waitcnt lgkmcnt(4)\n\t" \
        "s_barrier\n\t" \
        "ds_read_b64 v[90:91], v12 offset:8192\n\t" \
        "v_mfma_f32_16x16x32_bf16 v[112:115], a[56:59], v[140:143], v[112:115]\n\t" \
        "ds_read_b128 v[140:143], v15 offset:51584\n\t" \
        "v_mfma_f32_16x16x32_bf16 v[104:107], a[60:63], v[144:147], v[104:107]\n\t" \
        "ds_read_b64 v[102:103], v12 offset:10240\n\t" \
        "v_mfma_f32_16x16x32_bf16 v[108:111], a[64:67], v[144:147], v[108:111]\n\t" \
        "v_mfma_f32_16x16x32_bf16 v[112:115], a[68:71], v[144:147], v[112:115]\n\t" \
        "ds_read_b128 v[144:147], v15 offset:51648\n\t" \
        "v_mfma_f32_16x16x32_bf16 v[104:107], a[72:75], v[116:119], v[104:107]\n\t" \
        "v_mfma_f32_16x16x32_bf16 v[108:111], a[76:79], v[116:119], v[108:111]\n\t" \
        "v_mfma_f32_16x16x32_bf16 v[112:115], a[80:83], v[116:119], v[112:115]\n\t" \
        "ds_read_b128 v[116:119], v15 offset:51712\n\t" \
        "v_mfma_f32_16x16x32_bf16 v[104:107], a[84:87], v[120:123], v[104:107]\n\t" \
        "s_waitcnt lgkmcnt(4)\n\t" \
        "v_permlane16_swap_b32 v88, v90\n\t" \
        "v_permlane16_swap_b32 v89, v91\n\t" \
        "v_mfma_f32_16x16x32_bf16 v[108:111], a[88:91], v[120:123], v[108:111]\n\t" \
        "v_mfma_f32_16x16x32_bf16 v[112:115], a[92:95], v[120:123], v[112:115]\n\t" \
        "s_add_u32 s56, s64, 0x54000\n\t" \
        "buffer_store_dwordx4 v[88:91], v10, s[28:31], s56 offen sc1\n\t" \
        "v_mfma_f32_16x16x32_bf16 v[104:107], a[96:99], v[124:127], v[104:107]\n\t" \
        "s_add_u32 s56, s64, 0x69000\n\t" \
        "buffer_store_dwordx4 v[92:95], v9, s[28:31], s56 offen sc1\n\t" \
        "v_mfma_f32_16x16x32_bf16 v[108:111], a[100:103], v[124:127], v[108:111]\n\t" \
        "ds_read_b128 v[120:123], v16 offset:51584\n\t" \
        "s_waitcnt lgkmcnt(2)\n\t" \
        "v_permlane16_swap_b32 v100, v102\n\t" \
        "v_mfma_f32_16x16x32_bf16 v[112:115], a[104:107], v[124:127], v[112:115]\n\t" \
        "ds_read_b128 v[124:127], v16 offset:51648\n\t" \
        "v_permlane16_swap_b32 v101, v103\n\t" \
        "v_mfma_f32_16x16x32_bf16 v[104:107], a[108:111], v[128:131], v[104:107]\n\t" \
        "v_mfma_f32_16x16x32_bf16 v[108:111], a[112:115], v[128:131], v[108:111]\n\t" \
        "v_mfma_f32_16x16x32_bf16 v[112:115], a[116:119], v[128:131], v[112:115]\n\t" \
        "ds_read_b128 v[128:131], v16 offset:51712\n\t" \
        "v_mfma_f32_16x16x32_bf16 v[104:107], a[120:123], v[132:135], v[104:107]\n\t" \
        "v_mfma_f32_16x16x32_bf16 v[108:111], a[124:127], v[132:135], v[108:111]\n\t" \
        "s_add_u32 s56, s64, 0x69000\n\t" \
        "buffer_store_dwordx4 v[100:103], v10, s[28:31], s56 offen sc1\n\t" \
        "v_mfma_f32_16x16x32_bf16 v[112:115], a[128:131], v[132:135], v[112:115]\n\t" \
        "ds_read_b128 v[132:135], v14 offset:55552\n\t" \
        "v_mfma_f32_16x16x32_bf16 v[104:107], a[132:135], v[136:139], v[104:107]\n\t" \
        "v_mfma_f32_16x16x32_bf16 v[108:111], a[136:139], v[136:139], v[108:111]\n\t" \
        "v_mfma_f32_16x16x32_bf16 v[112:115], a[140:143], v[136:139], v[112:115]\n\t" \
        "ds_read_b128 v[136:139], v14 offset:55616\n\t" \
        "v_mfma_f32_16x16x32_bf16 v[104:107], a[144:147], v[140:143], v[104:107]\n\t" \
        "v_mfma_f32_16x16x32_bf16 v[108:111], a[148:151], v[140:143], v[108:111]\n\t" \
        "v_mfma_f32_16x16x32_bf16 v[112:115], a[152:155], v[140:143], v[112:115]\n\t" \
        "ds_read_b128 v[140:143], v14 offset:55680\n\t" \
        "v_mfma_f32_16x16x32_bf16 v[104:107], a[156:159], v[144:147], v[104:107]\n\t" \
        "v_mfma_f32_16x16x32_bf16 v[108:111], a[160:163], v[144:147], v[108:111]\n\t" \
        "v_mfma_f32_16x16x32_bf16 v[112:115], a[164:167], v[144:147], v[112:115]\n\t" \
        "ds_read_b128 v[144:147], v15 offset:55552\n\t" \
        "s_waitcnt lgkmcnt(3)\n\t" \
        "v_mfma_f32_16x16x32_bf16 v[104:107], a[168:171], v[116:119], v[104:107]\n\t" \
        "v_mfma_f32_16x16x32_bf16 v[108:111], a[172:175], v[116:119], v[108:111]\n\t" \
        "v_mfma_f32_16x16x32_bf16 v[112:115], a[176:179], v[116:119], v[112:115]\n\t" \
        "ds_read_b128 v[116:119], v15 offset:55616\n\t" \
        "v_mfma_f32_16x16x32_bf16 v[104:107], a[180:183], v[120:123], v[104:107]\n\t" \
        "v_mfma_f32_16x16x32_bf16 v[108:111], a[184:187], v[120:123], v[108:111]\n\t" \
        "v_mfma_f32_16x16x32_bf16 v[112:115], a[188:191], v[120:123], v[112:115]\n\t" \
        "ds_read_b128 v[120:123], v15 offset:55680\n\t" \
        "v_mfma_f32_16x16x32_bf16 v[104:107], a[192:195], v[124:127], v[104:107]\n\t" \
        "v_mfma_f32_16x16x32_bf16 v[108:111], a[196:199], v[124:127], v[108:111]\n\t" \
        "v_mfma_f32_16x16x32_bf16 v[112:115], a[200:203], v[124:127], v[112:115]\n\t" \
        "ds_read_b128 v[124:127], v16 offset:55552\n\t" \
        "v_mfma_f32_16x16x32_bf16 v[104:107], a[204:207], v[128:131], v[104:107]\n\t" \
        "v_mfma_f32_16x16x32_bf16 v[108:111], a[208:211], v[128:131], v[108:111]\n\t" \
        "v_mfma_f32_16x16x32_bf16 v[112:115], a[212:215], v[128:131], v[112:115]\n\t" \
        "ds_read_b128 v[128:131], v16 offset:55616\n\t" \
        "v_mfma_f32_16x16x32_bf16 v[104:107], a[216:219], v[132:135], v[104:107]\n\t" \
        "v_mfma_f32_16x16x32_bf16 v[108:111], a[220:223], v[132:135], v[108:111]\n\t" \
        "v_mfma_f32_16x16x32_bf16 v[112:115], a[224:227], v[132:135], v[112:115]\n\t" \
        "ds_read_b128 v[132:135], v16 offset:55680\n\t" \
        "s_waitcnt lgkmcnt(3)\n\t" \
        "v_mfma_f32_16x16x32_bf16 v[104:107], a[228:231], v[136:139], v[104:107]\n\t" \
        "v_mfma_f32_16x16x32_bf16 v[108:111], a[232:235], v[136:139], v[108:111]\n\t" \
        "v_mfma_f32_16x16x32_bf16 v[112:115], a[236:239], v[136:139], v[112:115]\n\t" \
        "v_mfma_f32_16x16x32_bf16 v[104:107], a[240:243], v[140:143], v[104:107]\n\t" \
        "v_mfma_f32_16x16x32_bf16 v[108:111], a[244:247], v[140:143], v[108:111]\n\t" \
        "v_mfma_f32_16x16x32_bf16 v[112:115], a[248:251], v[140:143], v[112:115]\n\t" \
        "v_mfma_f32_16x16x32_bf16 v[104:107], a[252:255], v[144:147], v[104:107]\n\t" \
        "v_mfma_f32_16x16x32_bf16 v[108:111], v[188:191], v[144:147], v[108:111]\n\t" \
        "v_mfma_f32_16x16x32_bf16 v[112:115], v[192:195], v[144:147], v[112:115]\n\t" \
        "v_mfma_f32_16x16x32_bf16 v[104:107], v[196:199], v[116:119], v[104:107]\n\t" \
        "v_mfma_f32_16x16x32_bf16 v[108:111], v[200:203], v[116:119], v[108:111]\n\t" \
        "v_mfma_f32_16x16x32_bf16 v[112:115], v[204:207], v[116:119], v[112:115]\n\t" \
        "v_mfma_f32_16x16x32_bf16 v[104:107], v[208:211], v[120:123], v[104:107]\n\t" \
        "v_mfma_f32_16x16x32_bf16 v[108:111], v[212:215], v[120:123], v[108:111]\n\t" \
        "v_mfma_f32_16x16x32_bf16 v[112:115], v[216:219], v[120:123], v[112:115]\n\t" \
        "s_waitcnt lgkmcnt(0)\n\t" \
        "v_mfma_f32_16x16x32_bf16 v[104:107], v[220:223], v[124:127], v[104:107]\n\t" \
        "v_mfma_f32_16x16x32_bf16 v[108:111], v[224:227], v[124:127], v[108:111]\n\t" \
        "v_mfma_f32_16x16x32_bf16 v[112:115], v[228:231], v[124:127], v[112:115]\n\t" \
        "v_mfma_f32_16x16x32_bf16 v[104:107], v[232:235], v[128:131], v[104:107]\n\t" \
        "v_mfma_f32_16x16x32_bf16 v[108:111], v[236:239], v[128:131], v[108:111]\n\t" \
        "v_mfma_f32_16x16x32_bf16 v[112:115], v[240:243], v[128:131], v[112:115]\n\t" \
        "v_mfma_f32_16x16x32_bf16 v[104:107], v[244:247], v[132:135], v[104:107]\n\t" \
        "v_mfma_f32_16x16x32_bf16 v[108:111], v[248:251], v[132:135], v[108:111]\n\t" \
        "v_mfma_f32_16x16x32_bf16 v[112:115], v[252:255], v[132:135], v[112:115]\n\t" \
        "s_nop 9\n\t" \
        "v_cvt_pk_bf16_f32 v112, v112, v113\n\t" \
        "v_cvt_pk_bf16_f32 v113, v114, v115\n\t" \
        "v_cvt_pk_bf16_f32 v104, v104, v105\n\t" \
        "v_cvt_pk_bf16_f32 v105, v106, v107\n\t" \
        "v_cvt_pk_bf16_f32 v106, v108, v109\n\t" \
        "ds_write_b64 v11, v[112:113] offset:12288\n\t" \
        "v_cvt_pk_bf16_f32 v107, v110, v111\n\t" \
        "v_permlane16_swap_b32 v104, v106\n\t" \
        "s_nop 0\n\t" \
        "v_permlane16_swap_b32 v105, v107\n\t" \
        "s_add_u32 s56, s64, 0x7e000\n\t" \
        "buffer_store_dwordx4 v[104:107], v9, s[28:31], s56 offen sc1\n\t" \
        "s_waitcnt lgkmcnt(0)\n\t" \
        "s_barrier\n\t" \
        "ds_read_b64 v[114:115], v12 offset:12288\n\t" \
        "s_waitcnt lgkmcnt(0)\n\t" \
        "v_permlane16_swap_b32 v112, v114\n\t" \
        "v_permlane16_swap_b32 v113, v115\n\t" \
        "s_add_u32 s56, s64, 0x7e000\n\t" \
        "buffer_store_dwordx4 v[112:115], v10, s[28:31], s56 offen sc1\n\t" \
        "s_endpgm\n\t" \
        ".LH8_start%=:\n\t" \
        "s_mov_b32 s62, 0\n\t" \
        "s_mov_b32 s63, -1\n\t" \
        "v_cmp_lt_u32 vcc, 47, v2\n\t" \
        "v_lshlrev_b32 v162, 4, v2\n\t" \
        "v_xor_b32 v163, 32, v162\n\t" \
        "v_cndmask_b32 v3, v162, v163, vcc\n\t" \
        "v_cndmask_b32 v4, v163, v162, s[62:63]\n\t" \
        "v_cmp_lt_u32 vcc, 15, v2\n\t" \
        "s_nop 1\n\t" \
        "v_cndmask_b32 v5, v162, v163, vcc\n\t" \
        "s_mul_i32 s40, s35, 0xc00\n\t" \
        "s_add_u32 s39, s40, 0xba40\n\t" \
        "s_mov_b32 s42, 0xffff0000\n\t" \
        "s_mov_b32 s43, 0xc0\n\t" \
        "s_mov_b32 s44, 0xf80\n\t" \
        "s_mov_b32 s45, 0x435f435f\n\t" \
        "v_add_u32 v162, s32, v18\n\t" \
        "v_add_u32 v163, s33, v19\n\t" \
        "v_add_u32 v162, -1, v162\n\t" \
        "v_add_u32 v163, -1, v163\n\t" \
        "v_cndmask_b32 v161, 0, 1.0, s[62:63]\n\t" \
        "v_cvt_f32_i32 v55, v162\n\t" \
        "v_cvt_f32_i32 v62, v163\n\t" \
        "v_add_f32 v56, v55, v161\n\t" \
        "v_add_f32 v59, v62, v161\n\t" \
        "v_add_f32 v57, 1.0, v55\n\t" \
        "v_add_f32 v62, 2.0, v62\n\t" \
        "v_add_f32 v58, 2.0, v55\n\t" \
        "v_add_f32 v61, 1.0, v59\n\t" \
        "v_fma_f32 v60, v161, -2.0, v62\n\t" \
        "v_bfe_u32 v160, v2, 4, 1\n\t" \
        "v_lshrrev_b32 v162, 2, v19\n\t" \
        "v_and_b32 v163, 1, v19\n\t" \
        "v_lshlrev_b32 v162, 5, v162\n\t" \
        "v_lshl_add_u32 v162, v163, 4, v162\n\t" \
        "v_and_b32 v163, 2, v19\n\t" \
        "v_lshl_add_u32 v162, v160, 3, v162\n\t" \
        "v_lshlrev_b32 v161, 2, v2\n\t" \
        "v_lshl_add_u32 v162, v163, 1, v162\n\t" \
        "v_and_b32 v161, 0x80, v161\n\t" \
        "s_mul_i32 s55, s35, 2560\n\t" \
        "s_add_u32 s55, s55, 109120\n\t" \
        "v_add3_u32 v63, v162, v161, s55\n\t" \
        "s_mul_i32 s56, s35, 1280\n\t" \
        "s_add_u32 s56, s56, 119360\n\t" \
        "v_lshl_add_u32 v10, v19, 3, s56\n\t" \
        "v_lshl_add_u32 v162, v160, 2, v161\n\t" \
        "v_add_u32 v64, v10, v162\n\t" \
        "s_mul_i32 s55, s32, 0xf80\n\t" \
        "s_mul_i32 s56, s33, 0xc0\n\t" \
        "s_add_u32 s55, s55, s56\n\t" \
        "s_sub_u32 s55, 0x1f40, s55\n\t" \
        "v_mov_b32 v65, s55\n\t" \
        "v_mov_b32 v29, 0x3f80\n\t" \
        "s_sub_i32 s55, s32, 2\n\t" \
        "s_sub_i32 s56, s33, 2\n\t" \
        "s_max_i32 s55, s55, 0\n\t" \
        "s_max_i32 s56, s56, 0\n\t" \
        "v_cvt_f32_u32 v162, s55\n\t" \
        "v_cvt_f32_u32 v163, s56\n\t" \
        "s_add_u32 s55, s32, 9\n\t" \
        "s_add_u32 s56, s33, 17\n\t" \
        "v_cvt_pk_bf16_f32 v14, v162, v163\n\t" \
        "v_cvt_f32_u32 v162, s55\n\t" \
        "v_cvt_f32_u32 v163, s56\n\t" \
        "v_cvt_pk_bf16_f32 v15, v162, v163\n\t" \
        "s_waitcnt vmcnt(2)\n\t" \
        "s_mov_b32 s57, s36\n\t" \
        "s_add_u32 m0, s38, 0x1f00\n\t" \
        "s_nop 0\n\t" \
        "buffer_load_dwordx4 v13, s[16:19], s57 offen nt lds\n\t" \
        "v_cndmask_b32 v140, v40, v41, s[62:63]\n\t" \
        "v_lshlrev_b32 v141, 16, v140\n\t" \
        "v_and_b32 v142, s42, v140\n\t" \
        "v_add_f32 v141, v55, v141\n\t" \
        "v_add_f32 v142, v59, v142\n\t" \
        "v_cvt_pk_bf16_f32 v50, v141, v142\n\t" \
        "v_pk_max_i16 v50, v50, 0\n\t" \
        "v_pk_min_i16 v50, v50, s45\n\t" \
        "v_lshlrev_b32 v141, 16, v50\n\t" \
        "v_and_b32 v142, s42, v50\n\t" \
        "v_pk_min_i16 v66, v50, v50\n\t" \
        "s_add_u32 s57, s36, 0xa800\n\t" \
        "s_add_u32 m0, s38, 0x2e80\n\t" \
        "s_nop 0\n\t" \
        "buffer_load_dwordx4 v13, s[16:19], s57 offen nt lds\n\t" \
        "v_fract_f32 v143, v141\n\t" \
        "v_fract_f32 v144, v142\n\t" \
        "v_cvt_u32_f32 v145, v141\n\t" \
        "v_cvt_u32_f32 v146, v142\n\t" \
        "v_cmp_neq_f32 s[10:11], 0, v143\n\t" \
        "v_cmp_neq_f32 vcc, 0, v144\n\t" \
        "v_add_u32 v146, 2, v146\n\t" \
        "v_pk_max_i16 v67, v50, v50\n\t" \
        "v_mad_u32_u24 v148, v146, s43, v65\n\t" \
        "v_bfe_u32 v147, v146, 2, 1\n\t" \
        "v_mad_u32_u24 v148, v145, s44, v148\n\t" \
        "s_add_u32 s57, s36, 0x15000\n\t" \
        "s_add_u32 m0, s38, 0x3e00\n\t" \
        "s_nop 0\n\t" \
        "buffer_load_dwordx4 v13, s[16:19], s57 offen nt lds\n\t" \
        "v_add_u32 v146, 1, v146\n\t" \
        "v_lshl_add_u32 v149, v147, 5, v148\n\t" \
        "v_bfe_u32 v147, v146, 2, 1\n\t" \
        "v_add_u32 v148, s43, v148\n\t" \
        "v_add_u32 v150, s44, v149\n\t" \
        "v_lshl_add_u32 v148, v147, 5, v148\n\t" \
        "v_cndmask_b32 v150, v149, v150, s[10:11]\n\t" \
        "v_add_f32 v144, v144, v143\n\t" \
        "v_cndmask_b32 v150, v150, v148, vcc\n\t" \
        "v_sub_f32 v152, 1.0, v144\n\t" \
        "s_and_b64 s[4:5], s[10:11], vcc\n\t" \
        "s_add_u32 s57, s36, 0x1f800\n\t" \
        "s_add_u32 m0, s38, 0x4d80\n\t" \
        "s_nop 0\n\t" \
        "buffer_load_dwordx4 v13, s[16:19], s57 offen nt lds\n\t" \
        "ds_write2st64_b32 v63, v149, v150 offset0:0 offset1:5\n\t" \
        "v_cvt_pk_bf16_f32 v151, v152, v144\n\t" \
        "v_cndmask_b32 v151, v151, v29, s[4:5]\n\t" \
        "ds_write_b32 v64, v151 offset:0\n\t" \
        "v_cndmask_b32 v140, v42, v43, s[62:63]\n\t" \
        "v_lshlrev_b32 v141, 16, v140\n\t" \
        "v_and_b32 v142, s42, v140\n\t" \
        "v_add_f32 v141, v56, v141\n\t" \
        "v_add_f32 v142, v60, v142\n\t" \
        "v_cvt_pk_bf16_f32 v51, v141, v142\n\t" \
        "v_pk_max_i16 v51, v51, 0\n\t" \
        "s_add_u32 s57, s36, 0x2a000\n\t" \
        "s_min_u32 s57, s57, 0x925800\n\t" \
        "s_add_u32 m0, s38, 0x5d00\n\t" \
        "s_nop 0\n\t" \
        "buffer_load_dwordx4 v13, s[16:19], s57 offen nt lds\n\t" \
        "v_pk_min_i16 v51, v51, s45\n\t" \
        "v_lshlrev_b32 v141, 16, v51\n\t" \
        "v_and_b32 v142, s42, v51\n\t" \
        "v_pk_min_i16 v66, v66, v51\n\t" \
        "v_fract_f32 v143, v141\n\t" \
        "v_fract_f32 v144, v142\n\t" \
        "v_cvt_u32_f32 v145, v141\n\t" \
        "v_cvt_u32_f32 v146, v142\n\t" \
        "v_cmp_neq_f32 s[10:11], 0, v143\n\t" \
        "v_cmp_neq_f32 vcc, 0, v144\n\t" \
        "v_add_u32 v146, 2, v146\n\t" \
        "s_add_u32 s57, s36, 0x34800\n\t" \
        "s_min_u32 s57, s57, 0x925800\n\t" \
        "s_add_u32 m0, s38, 0x6c80\n\t" \
        "s_nop 0\n\t" \
        "buffer_load_dwordx4 v13, s[16:19], s57 offen nt lds\n\t" \
        "v_pk_max_i16 v67, v67, v51\n\t" \
        "v_mad_u32_u24 v148, v146, s43, v65\n\t" \
        "v_bfe_u32 v147, v146, 2, 1\n\t" \
        "v_mad_u32_u24 v148, v145, s44, v148\n\t" \
        "v_add_u32 v146, 1, v146\n\t" \
        "v_lshl_add_u32 v149, v147, 5, v148\n\t" \
        "v_bfe_u32 v147, v146, 2, 1\n\t" \
        "v_add_u32 v148, s43, v148\n\t" \
        "v_add_u32 v150, s44, v149\n\t" \
        "v_lshl_add_u32 v148, v147, 5, v148\n\t" \
        "v_cndmask_b32 v150, v149, v150, s[10:11]\n\t" \
        "s_add_u32 s57, s36, 0x3f000\n\t" \
        "s_min_u32 s57, s57, 0x925800\n\t" \
        "s_add_u32 m0, s38, 0x7c00\n\t" \
        "s_nop 0\n\t" \
        "buffer_load_dwordx4 v13, s[16:19], s57 offen nt lds\n\t" \
        "v_add_f32 v144, v144, v143\n\t" \
        "v_cndmask_b32 v150, v150, v148, vcc\n\t" \
        "v_sub_f32 v152, 1.0, v144\n\t" \
        "s_and_b64 s[6:7], s[10:11], vcc\n\t" \
        "ds_write2st64_b32 v63, v149, v150 offset0:1 offset1:6\n\t" \
        "v_cvt_pk_bf16_f32 v151, v152, v144\n\t" \
        "v_cndmask_b32 v151, v151, v29, s[6:7]\n\t" \
        "ds_write_b32 v64, v151 offset:256\n\t" \
        "v_cndmask_b32 v140, v44, v45, s[62:63]\n\t" \
        "v_lshlrev_b32 v141, 16, v140\n\t" \
        "v_and_b32 v142, s42, v140\n\t" \
        "s_add_u32 s57, s36, 0x49800\n\t" \
        "s_min_u32 s57, s57, 0x925800\n\t" \
        "s_add_u32 m0, s38, 0x8b80\n\t" \
        "s_nop 0\n\t" \
        "buffer_load_dwordx4 v13, s[16:19], s57 offen nt lds\n\t" \
        "v_add_f32 v141, v57, v141\n\t" \
        "v_add_f32 v142, v61, v142\n\t" \
        "v_cvt_pk_bf16_f32 v52, v141, v142\n\t" \
        "v_pk_max_i16 v52, v52, 0\n\t" \
        "v_pk_min_i16 v52, v52, s45\n\t" \
        "v_lshlrev_b32 v141, 16, v52\n\t" \
        "v_and_b32 v142, s42, v52\n\t" \
        "v_pk_min_i16 v66, v66, v52\n\t" \
        "v_fract_f32 v143, v141\n\t" \
        "v_fract_f32 v144, v142\n\t" \
        "v_cvt_u32_f32 v145, v141\n\t" \
        "s_add_u32 s57, s36, 0x54000\n\t" \
        "s_min_u32 s57, s57, 0x925800\n\t" \
        "s_add_u32 m0, s38, 0x9b00\n\t" \
        "s_nop 0\n\t" \
        "buffer_load_dwordx4 v13, s[16:19], s57 offen nt lds\n\t" \
        "v_cvt_u32_f32 v146, v142\n\t" \
        "v_cmp_neq_f32 s[10:11], 0, v143\n\t" \
        "v_cmp_neq_f32 vcc, 0, v144\n\t" \
        "v_add_u32 v146, 2, v146\n\t" \
        "v_pk_max_i16 v67, v67, v52\n\t" \
        "v_mad_u32_u24 v148, v146, s43, v65\n\t" \
        "v_bfe_u32 v147, v146, 2, 1\n\t" \
        "v_mad_u32_u24 v148, v145, s44, v148\n\t" \
        "v_add_u32 v146, 1, v146\n\t" \
        "v_lshl_add_u32 v149, v147, 5, v148\n\t" \
        "v_bfe_u32 v147, v146, 2, 1\n\t" \
        "s_add_u32 s57, s36, 0x5e800\n\t" \
        "s_min_u32 s57, s57, 0x925800\n\t" \
        "s_add_u32 m0, s38, 0xaa80\n\t" \
        "s_nop 0\n\t" \
        "buffer_load_dwordx4 v13, s[16:19], s57 offen nt lds\n\t" \
        "v_add_u32 v148, s43, v148\n\t" \
        "v_add_u32 v150, s44, v149\n\t" \
        "v_lshl_add_u32 v148, v147, 5, v148\n\t" \
        "v_cndmask_b32 v150, v149, v150, s[10:11]\n\t" \
        "v_add_f32 v144, v144, v143\n\t" \
        "v_cndmask_b32 v150, v150, v148, vcc\n\t" \
        "v_sub_f32 v152, 1.0, v144\n\t" \
        "s_and_b64 s[8:9], s[10:11], vcc\n\t" \
        "ds_write2st64_b32 v63, v149, v150 offset0:2 offset1:7\n\t" \
        "v_cvt_pk_bf16_f32 v151, v152, v144\n\t" \
        "v_cndmask_b32 v151, v151, v29, s[8:9]\n\t" \
        "s_mov_b32 s57, s40\n\t" \
        "s_mov_b32 m0, s39\n\t" \
        "s_nop 0\n\t" \
        "buffer_load_dwordx4 v3, s[24:27], s57 offen offset:0  lds\n\t" \
        "ds_write_b32 v64, v151 offset:512\n\t" \
        "v_cndmask_b32 v140, v46, v47, s[62:63]\n\t" \
        "v_lshlrev_b32 v141, 16, v140\n\t" \
        "v_and_b32 v142, s42, v140\n\t" \
        "v_add_f32 v141, v58, v141\n\t" \
        "v_add_f32 v142, v59, v142\n\t" \
        "v_cvt_pk_bf16_f32 v53, v141, v142\n\t" \
        "v_pk_max_i16 v53, v53, 0\n\t" \
        "v_pk_min_i16 v53, v53, s45\n\t" \
        "v_lshlrev_b32 v141, 16, v53\n\t" \
        "v_and_b32 v142, s42, v53\n\t" \
        "buffer_load_dwordx4 v4, s[24:27], s57 offen offset:1024  lds\n\t" \
        "v_pk_min_i16 v66, v66, v53\n\t" \
        "v_fract_f32 v143, v141\n\t" \
        "v_fract_f32 v144, v142\n\t" \
        "v_cvt_u32_f32 v145, v141\n\t" \
        "v_cvt_u32_f32 v146, v142\n\t" \
        "v_cmp_neq_f32 s[10:11], 0, v143\n\t" \
        "v_cmp_neq_f32 vcc, 0, v144\n\t" \
        "v_add_u32 v146, 2, v146\n\t" \
        "v_pk_max_i16 v67, v67, v53\n\t" \
        "v_mad_u32_u24 v148, v146, s43, v65\n\t" \
        "v_bfe_u32 v147, v146, 2, 1\n\t" \
        "buffer_load_dwordx4 v5, s[24:27], s57 offen offset:2048  lds\n\t" \
        "v_mad_u32_u24 v148, v145, s44, v148\n\t" \
        "v_add_u32 v146, 1, v146\n\t" \
        "v_lshl_add_u32 v149, v147, 5, v148\n\t" \
        "v_bfe_u32 v147, v146, 2, 1\n\t" \
        "v_add_u32 v148, s43, v148\n\t" \
        "v_add_u32 v150, s44, v149\n\t" \
        "v_lshl_add_u32 v148, v147, 5, v148\n\t" \
        "v_cndmask_b32 v150, v149, v150, s[10:11]\n\t" \
        "v_add_f32 v144, v144, v143\n\t" \
        "v_cndmask_b32 v150, v150, v148, vcc\n\t" \
        "v_sub_f32 v152, 1.0, v144\n\t" \
        "s_add_u32 s57, s40, 0x3000\n\t" \
        "s_add_u32 m0, s39, 0x3000\n\t" \
        "s_nop 0\n\t" \
        "buffer_load_dwordx4 v3, s[24:27], s57 offen offset:0  lds\n\t" \
        "s_and_b64 s[12:13], s[10:11], vcc\n\t" \
        "ds_write2st64_b32 v63, v149, v150 offset0:3 offset1:8\n\t" \
        "v_cvt_pk_bf16_f32 v151, v152, v144\n\t" \
        "v_cndmask_b32 v151, v151, v29, s[12:13]\n\t" \
        "ds_write_b32 v64, v151 offset:768\n\t" \
        "v_lshlrev_b32 v141, 16, v48\n\t" \
        "v_and_b32 v142, s42, v48\n\t" \
        "v_add_f32 v141, v58, v141\n\t" \
        "v_add_f32 v142, v62, v142\n\t" \
        "v_cvt_pk_bf16_f32 v54, v141, v142\n\t" \
        "v_pk_max_i16 v54, v54, 0\n\t" \
        "buffer_load_dwordx4 v4, s[24:27], s57 offen offset:1024  lds\n\t" \
        "v_pk_min_i16 v54, v54, s45\n\t" \
        "v_lshlrev_b32 v141, 16, v54\n\t" \
        "v_and_b32 v142, s42, v54\n\t" \
        "v_pk_min_i16 v66, v66, v54\n\t" \
        "v_fract_f32 v143, v141\n\t" \
        "v_fract_f32 v144, v142\n\t" \
        "v_cvt_u32_f32 v145, v141\n\t" \
        "v_cvt_u32_f32 v146, v142\n\t" \
        "v_cmp_neq_f32 s[10:11], 0, v143\n\t" \
        "v_cmp_neq_f32 vcc, 0, v144\n\t" \
        "v_add_u32 v146, 2, v146\n\t" \
        "buffer_load_dwordx4 v5, s[24:27], s57 offen offset:2048  lds\n\t" \
        "v_pk_max_i16 v67, v67, v54\n\t" \
        "v_mad_u32_u24 v148, v146, s43, v65\n\t" \
        "v_bfe_u32 v147, v146, 2, 1\n\t" \
        "v_mad_u32_u24 v148, v145, s44, v148\n\t" \
        "v_add_u32 v146, 1, v146\n\t" \
        "v_lshl_add_u32 v149, v147, 5, v148\n\t" \
        "v_bfe_u32 v147, v146, 2, 1\n\t" \
        "v_add_u32 v148, s43, v148\n\t" \
        "v_add_u32 v150, s44, v149\n\t" \
        "v_lshl_add_u32 v148, v147, 5, v148\n\t" \
        "v_cndmask_b32 v150, v149, v150, s[10:11]\n\t" \
        "s_add_u32 s57, s40, 0x6000\n\t" \
        "s_add_u32 m0, s39, 0x6000\n\t" \
        "s_nop 0\n\t" \
        "buffer_load_dwordx4 v3, s[24:27], s57 offen offset:0  lds\n\t" \
        "v_add_f32 v144, v144, v143\n\t" \
        "v_cndmask_b32 v150, v150, v148, vcc\n\t" \
        "v_sub_f32 v152, 1.0, v144\n\t" \
        "s_and_b64 s[14:15], s[10:11], vcc\n\t" \
        "ds_write2st64_b32 v63, v149, v150 offset0:4 offset1:9\n\t" \
        "v_cvt_pk_bf16_f32 v151, v152, v144\n\t" \
        "v_cndmask_b32 v151, v151, v29, s[14:15]\n\t" \
        "ds_write_b32 v64, v151 offset:1024\n\t" \
        "v_pk_max_i16 v140, v66, v14\n\t" \
        "v_pk_min_i16 v141, v67, v15\n\t" \
        "v_cmp_ne_u32 s[10:11], v140, v66\n\t" \
        "buffer_load_dwordx4 v4, s[24:27], s57 offen offset:1024  lds\n\t" \
        "v_cmp_ne_u32 vcc, v141, v67\n\t" \
        "s_nop 0\n\t" \
        "s_or_b64 s[10:11], s[10:11], vcc\n\t" \
        "s_cselect_b32 s54, 1, 0\n\t" \
        "buffer_load_dwordx4 v5, s[24:27], s57 offen offset:2048  lds\n\t" \
        "s_or_b64 s[10:11], s[4:5], s[6:7]\n\t" \
        "s_or_b64 s[56:57], s[8:9], s[12:13]\n\t" \
        "s_or_b64 s[10:11], s[10:11], s[56:57]\n\t" \
        "s_or_b64 s[10:11], s[10:11], s[14:15]\n\t" \
        "s_cbranch_scc1 .LH_ent%=\n\t" \
        "s_lshl_b32 s55, s35, 2\n\t" \
        "s_cmp_lg_u32 s54, 0\n\t" \
        "v_mov_b32 v162, s55\n\t" \
        "s_cselect_b32 s55, 0x10000, 0\n\t" \
        "v_mov_b32 v163, s55\n\t" \
        "ds_write_b32 v162, v163\n\t" \
        "v_bfe_u32 v162, v2, 2, 1\n\t" \
        "v_lshrrev_b32 v163, 3, v2\n\t" \
        "s_mul_i32 s55, s35, 2560\n\t" \
        "s_add_u32 s55, s55, 109120\n\t" \
        "v_mul_u32_u24 v162, 0x500, v162\n\t" \
        "v_lshlrev_b32 v163, 4, v163\n\t" \
        "v_add3_u32 v9, v162, v163, s55\n\t" \
        "v_lshrrev_b32 v164, 4, v2\n\t" \
        "v_bfe_u32 v162, v2, 2, 2\n\t" \
        "v_and_b32 v163, 3, v2\n\t" \
        "v_lshl_add_u32 v162, v164, 2, v162\n\t" \
        "v_lshlrev_b32 v11, 3, v163\n\t" \
        "v_and_b32 v165, 1, v164\n\t" \
        "v_mul_u32_u24 v162, 0xc0, v162\n\t" \
        "v_xor_b32 v12, 32, v11\n\t" \
        "v_lshl_add_u32 v162, v165, 5, v162\n\t" \
        "v_lshlrev_b32 v163, 6, v165\n\t" \
        "s_mov_b32 s55, 0xba40\n\t" \
        "v_add3_u32 v6, v162, v11, s55\n\t" \
        "v_sub_u32 v7, v6, v163\n\t" \
        "v_mul_u32_u24 v162, 0xc0, v19\n\t" \
        "v_lshrrev_b32 v164, 1, v164\n\t" \
        "v_lshl_add_u32 v162, v165, 5, v162\n\t" \
        "v_lshl_add_u32 v8, v164, 4, v162\n\t" \
        "s_add_u32 s55, s32, s35\n\t" \
        "s_mul_i32 s56, s33, 0xc0\n\t" \
        "s_mul_i32 s55, s55, 0xa800\n\t" \
        "s_add_u32 s59, s55, s56\n\t" \
        "s_add_u32 s60, s59, 0x2a000\n\t" \
        "v_lshrrev_b32 v166, 4, v2\n\t" \
        "v_bfe_u32 v168, v2, 2, 2\n\t" \
        "v_and_b32 v167, 3, v2\n\t" \
        "v_cmp_eq_u32 vcc, v166, v168\n\t" \
        "v_lshlrev_b32 v167, v167, 1\n\t" \
        "s_nop 0\n\t" \
        "v_cndmask_b32 v167, 0, v167, vcc\n\t" \
        "v_bfe_i32 v25, v167, 0, 1\n\t" \
        "v_bfe_i32 v26, v167, 1, 1\n\t" \
        "v_bfe_i32 v27, v167, 2, 1\n\t" \
        "v_bfe_i32 v28, v167, 3, 1\n\t" \
        "v_mov_b32 v163, 0\n\t" \
        "s_waitcnt vmcnt(6) lgkmcnt(0)\n\t" \
        "s_barrier\n\t" \
        "ds_read_b128 v[152:155], v163\n\t" \
        "s_waitcnt lgkmcnt(0)\n\t" \
        "v_add_u32 v152, v152, v154\n\t" \
        "v_add_u32 v153, v153, v155\n\t" \
        "v_max_u32 v152, v152, v153\n\t" \
        "s_nop 0\n\t" \
        "v_readfirstlane_b32 s55, v152\n\t" \
        "s_nop 0\n\t" \
        "s_cmp_gt_u32 s55, 56\n\t" \
        "s_cbranch_scc1 .Lfail%=\n\t" \
        ".LH_join%=:\n\t" \
        "ds_read_b128 v[170:173], v9 offset:0\n\t" \
        "ds_read_b64 v[150:151], v10 offset:0\n\t" \
        "s_waitcnt lgkmcnt(1)\n\t" \
        "v_xor_b32 v134, v170, v11\n\t" \
        "v_xor_b32 v135, v171, v11\n\t" \
        "v_xor_b32 v136, v172, v11\n\t" \
        "v_xor_b32 v138, v170, v12\n\t" \
        "ds_read_b128 v[174:177], v9 offset:128\n\t" \
        "v_xor_b32 v137, v173, v11\n\t" \
        "v_xor_b32 v139, v171, v12\n\t" \
        "v_xor_b32 v140, v172, v12\n\t" \
        "v_xor_b32 v141, v173, v12\n\t" \
        "ds_read_b64_tr_b16 a[0:1], v6 offset:0\n\t" \
        "ds_read_b64_tr_b16 a[2:3], v6 offset:3072\n\t" \
        "ds_read_b64_tr_b16 a[4:5], v7 offset:32\n\t" \
        "ds_read_b64_tr_b16 a[6:7], v7 offset:3104\n\t" \
        "ds_read_b64_tr_b16 a[8:9], v6 offset:64\n\t" \
        "ds_read_b64_tr_b16 a[10:11], v6 offset:3136\n\t" \
        "ds_read_b64_tr_b16 a[96:97], v134 offset:0\n\t" \
        "ds_read_b64_tr_b16 a[98:99], v135 offset:0\n\t" \
        "ds_read_b64_tr_b16 a[100:101], v138 offset:0\n\t" \
        "ds_read_b64_tr_b16 a[102:103], v139 offset:0\n\t" \
        "ds_read_b64_tr_b16 a[104:105], v136 offset:0\n\t" \
        "ds_read_b64_tr_b16 a[106:107], v137 offset:0\n\t" \
        "ds_read_b64_tr_b16 a[108:109], v140 offset:0\n\t" \
        "ds_read_b64_tr_b16 a[110:111], v141 offset:0\n\t" \
        "ds_read_b64_tr_b16 a[112:113], v134 offset:64\n\t" \
        "ds_read_b64_tr_b16 a[114:115], v135 offset:64\n\t" \
        "ds_read_b64_tr_b16 a[116:117], v138 offset:64\n\t" \
        "ds_read_b64_tr_b16 a[118:119], v139 offset:64\n\t" \
        "ds_read_b64_tr_b16 a[120:121], v136 offset:64\n\t" \
        "ds_read_b64_tr_b16 a[122:123], v137 offset:64\n\t" \
        "ds_read_b64_tr_b16 a[124:125], v140 offset:64\n\t" \
        "ds_read_b64_tr_b16 a[126:127], v141 offset:64\n\t" \
        "ds_read_b64_tr_b16 a[128:129], v134 offset:128\n\t" \
        "ds_read_b64_tr_b16 a[130:131], v135 offset:128\n\t" \
        "ds_read_b64_tr_b16 a[132:133], v138 offset:128\n\t" \
        "ds_read_b64_tr_b16 a[134:135], v139 offset:128\n\t" \
        "ds_read_b64_tr_b16 a[136:137], v136 offset:128\n\t" \
        "s_waitcnt lgkmcnt(15)\n\t" \
        "v_and_b32 v154, v150, v25\n\t" \
        "ds_read_b64_tr_b16 a[138:139], v137 offset:128\n\t" \
        "ds_read_b64_tr_b16 a[140:141], v140 offset:128\n\t" \
        "v_xor_b32 v142, v174, v11\n\t" \
        "v_and_b32 v155, v150, v26\n\t" \
        "ds_read_b64_tr_b16 a[142:143], v141 offset:128\n\t" \
        "v_xor_b32 v143, v175, v11\n\t" \
        "v_and_b32 v156, v150, v27\n\t" \
        "v_xor_b32 v144, v176, v11\n\t" \
        "v_xor_b32 v146, v174, v12\n\t" \
        "v_and_b32 v157, v150, v28\n\t" \
        "v_xor_b32 v145, v177, v11\n\t" \
        "v_xor_b32 v147, v175, v12\n\t" \
        "v_xor_b32 v148, v176, v12\n\t" \
        "ds_read_b64 v[152:153], v10 offset:128\n\t" \
        "v_xor_b32 v149, v177, v12\n\t" \
        "v_and_b32 v158, v151, v25\n\t" \
        "v_and_b32 v159, v151, v26\n\t" \
        "ds_read_b64_tr_b16 a[12:13], v7 offset:96\n\t" \
        "v_and_b32 v160, v151, v27\n\t" \
        "ds_read_b64_tr_b16 a[14:15], v7 offset:3168\n\t" \
        "v_mfma_f32_16x16x32_bf16 v[102:105], a[96:99], v[154:157], 0\n\t" \
        "ds_read_b64_tr_b16 a[16:17], v6 offset:128\n\t" \
        "ds_read_b64_tr_b16 a[18:19], v6 offset:3200\n\t" \
        "v_and_b32 v161, v151, v28\n\t" \
        "v_mfma_f32_16x16x32_bf16 v[106:109], a[100:103], v[154:157], 0\n\t" \
        "s_add_u32 m0, s39, 36864\n\t" \
        "s_add_u32 s57, s40, 36864\n\t" \
        "buffer_load_dwordx4 v3, s[24:27], s57 offen offset:0  lds\n\t" \
        "v_mfma_f32_16x16x32_bf16 v[110:113], a[104:107], v[158:161], 0\n\t" \
        "ds_read_b64_tr_b16 a[20:21], v7 offset:160\n\t" \
        "s_waitcnt lgkmcnt(5)\n\t" \
        "v_and_b32 v162, v152, v25\n\t" \
        "v_mfma_f32_16x16x32_bf16 v[114:117], a[108:111], v[158:161], 0\n\t" \
        "ds_read_b64_tr_b16 a[22:23], v7 offset:3232\n\t" \
        "ds_read_b64_tr_b16 a[96:97], v142 offset:0\n\t" \
        "v_and_b32 v163, v152, v26\n\t" \
        "v_and_b32 v164, v152, v27\n\t" \
        "v_and_b32 v165, v152, v28\n\t" \
        "v_and_b32 v166, v153, v25\n\t" \
        "v_and_b32 v167, v153, v26\n\t" \
        "v_and_b32 v168, v153, v27\n\t" \
        "v_and_b32 v169, v153, v28\n\t" \
        "v_mfma_f32_16x16x32_bf16 v[118:121], a[112:115], v[154:157], 0\n\t" \
        "ds_read_b64_tr_b16 a[98:99], v143 offset:0\n\t" \
        "ds_read_b64_tr_b16 a[100:101], v146 offset:0\n\t" \
        "v_cvt_pk_bf16_f32 v78, v102, v103\n\t" \
        "v_cvt_pk_bf16_f32 v79, v104, v105\n\t" \
        "v_mfma_f32_16x16x32_bf16 v[122:125], a[116:119], v[154:157], 0\n\t" \
        "ds_read_b64_tr_b16 a[102:103], v147 offset:0\n\t" \
        "ds_read_b64_tr_b16 a[104:105], v144 offset:0\n\t" \
        "v_cvt_pk_bf16_f32 v80, v106, v107\n\t" \
        "v_cvt_pk_bf16_f32 v81, v108, v109\n\t" \
        "v_mfma_f32_16x16x32_bf16 v[126:129], a[120:123], v[158:161], 0\n\t" \
        "buffer_load_dwordx4 v4, s[24:27], s57 offen offset:1024  lds\n\t" \
        "v_cvt_pk_bf16_f32 v82, v110, v111\n\t" \
        "v_mfma_f32_16x16x32_bf16 v[130:133], a[124:127], v[158:161], 0\n\t" \
        "ds_read_b64_tr_b16 a[106:107], v145 offset:0\n\t" \
        "ds_read_b64_tr_b16 a[108:109], v148 offset:0\n\t" \
        "v_cvt_pk_bf16_f32 v83, v112, v113\n\t" \
        "v_cvt_pk_bf16_f32 v84, v114, v115\n\t" \
        "v_mfma_f32_16x16x32_bf16 v[102:105], a[128:131], v[154:157], 0\n\t" \
        "ds_read_b64_tr_b16 a[110:111], v149 offset:0\n\t" \
        "ds_read_b64_tr_b16 a[24:25], v6 offset:6144\n\t" \
        "v_cvt_pk_bf16_f32 v85, v116, v117\n\t" \
        "v_mfma_f32_16x16x32_bf16 v[106:109], a[132:135], v[154:157], 0\n\t" \
        "ds_read_b64_tr_b16 a[26:27], v6 offset:9216\n\t" \
        "ds_read_b64_tr_b16 a[28:29], v7 offset:6176\n\t" \
        "v_mfma_f32_16x16x32_bf16 v[110:113], a[136:139], v[158:161], 0\n\t" \
        "ds_read_b64_tr_b16 a[30:31], v7 offset:9248\n\t" \
        "ds_read_b64_tr_b16 a[32:33], v6 offset:6208\n\t" \
        "v_mfma_f32_16x16x32_bf16 v[114:117], a[140:143], v[158:161], 0\n\t" \
        "buffer_load_dwordx4 v5, s[24:27], s57 offen offset:2048  lds\n\t" \
        "v_mfma_f32_16x16x32_bf16 v[30:33], a[0:3], v[78:81], 0\n\t" \
        "ds_read_b64_tr_b16 a[34:35], v6 offset:9280\n\t" \
        "v_cvt_pk_bf16_f32 v86, v118, v119\n\t" \
        "v_mfma_f32_16x16x32_bf16 v[54:57], a[0:3], v[82:85], 0\n\t" \
        "ds_read_b64_tr_b16 a[36:37], v7 offset:6240\n\t" \
        "ds_read_b64_tr_b16 a[38:39], v7 offset:9312\n\t" \
        "v_mfma_f32_16x16x32_bf16 v[34:37], a[4:7], v[78:81], 0\n\t" \
        "ds_read_b64_tr_b16 a[40:41], v6 offset:6272\n\t" \
        "v_cvt_pk_bf16_f32 v87, v120, v121\n\t" \
        "v_mfma_f32_16x16x32_bf16 v[58:61], a[4:7], v[82:85], 0\n\t" \
        "s_waitcnt vmcnt(6)\n\t" \
        "s_barrier\n\t" \
        "ds_read_b128 v[170:173], v9 offset:256\n\t" \
        "ds_read_b64_tr_b16 a[42:43], v6 offset:9344\n\t" \
        "v_mfma_f32_16x16x32_bf16 v[38:41], a[8:11], v[78:81], 0\n\t" \
        "s_add_u32 m0, s39, 49152\n\t" \
        "s_add_u32 s57, s40, 49152\n\t" \
        "buffer_load_dwordx4 v3, s[24:27], s57 offen offset:0  lds\n\t" \
        "v_mfma_f32_16x16x32_bf16 v[62:65], a[8:11], v[82:85], 0\n\t" \
        "ds_read_b64_tr_b16 a[44:45], v7 offset:6304\n\t" \
        "ds_read_b64_tr_b16 a[46:47], v7 offset:9376\n\t" \
        "s_waitcnt lgkmcnt(12)\n\t" \
        "v_mfma_f32_16x16x32_bf16 v[42:45], a[12:15], v[78:81], 0\n\t" \
        "ds_read_b64_tr_b16 a[112:113], v142 offset:64\n\t" \
        "v_cvt_pk_bf16_f32 v88, v122, v123\n\t" \
        "v_mfma_f32_16x16x32_bf16 v[66:69], a[12:15], v[82:85], 0\n\t" \
        "ds_read_b64_tr_b16 a[114:115], v143 offset:64\n\t" \
        "ds_read_b64_tr_b16 a[116:117], v146 offset:64\n\t" \
        "v_mfma_f32_16x16x32_bf16 v[46:49], a[16:19], v[78:81], 0\n\t" \
        "ds_read_b64_tr_b16 a[118:119], v147 offset:64\n\t" \
        "ds_read_b64_tr_b16 a[120:121], v144 offset:64\n\t" \
        "v_mfma_f32_16x16x32_bf16 v[70:73], a[16:19], v[82:85], 0\n\t" \
        "buffer_load_dwordx4 v4, s[24:27], s57 offen offset:1024  lds\n\t" \
        "v_mfma_f32_16x16x32_bf16 v[50:53], a[20:23], v[78:81], 0\n\t" \
        "ds_read_b64_tr_b16 a[122:123], v145 offset:64\n\t" \
        "v_cvt_pk_bf16_f32 v89, v124, v125\n\t" \
        "v_cvt_pk_bf16_f32 v90, v126, v127\n\t" \
        "v_mfma_f32_16x16x32_bf16 v[74:77], a[20:23], v[82:85], 0\n\t" \
        "ds_read_b64_tr_b16 a[124:125], v148 offset:64\n\t" \
        "ds_read_b64_tr_b16 a[126:127], v149 offset:64\n\t" \
        "v_cvt_pk_bf16_f32 v91, v128, v129\n\t" \
        "v_cvt_pk_bf16_f32 v92, v130, v131\n\t" \
        "v_mfma_f32_16x16x32_bf16 v[118:121], a[96:99], v[162:165], 0\n\t" \
        "ds_read_b64_tr_b16 a[48:49], v6 offset:12288\n\t" \
        "v_cvt_pk_bf16_f32 v93, v132, v133\n\t" \
        "v_mfma_f32_16x16x32_bf16 v[122:125], a[100:103], v[162:165], 0\n\t" \
        "ds_read_b64_tr_b16 a[50:51], v6 offset:15360\n\t" \
        "ds_read_b64_tr_b16 a[52:53], v7 offset:12320\n\t" \
        "v_mfma_f32_16x16x32_bf16 v[126:129], a[104:107], v[166:169], 0\n\t" \
        "buffer_load_dwordx4 v5, s[24:27], s57 offen offset:2048  lds\n\t" \
        "v_mfma_f32_16x16x32_bf16 v[130:133], a[108:111], v[166:169], 0\n\t" \
        "ds_read_b64_tr_b16 a[54:55], v7 offset:15392\n\t" \
        "ds_read_b64_tr_b16 a[56:57], v6 offset:12352\n\t" \
        "s_waitcnt lgkmcnt(12)\n\t" \
        "v_mfma_f32_16x16x32_bf16 v[30:33], a[24:27], v[86:89], v[30:33]\n\t" \
        "ds_read_b64_tr_b16 a[58:59], v6 offset:15424\n\t" \
        "v_cvt_pk_bf16_f32 v94, v102, v103\n\t" \
        "v_mfma_f32_16x16x32_bf16 v[54:57], a[24:27], v[90:93], v[54:57]\n\t" \
        "ds_read_b64_tr_b16 a[60:61], v7 offset:12384\n\t" \
        "v_cvt_pk_bf16_f32 v95, v104, v105\n\t" \
        "v_mfma_f32_16x16x32_bf16 v[34:37], a[28:31], v[86:89], v[34:37]\n\t" \
        "ds_read_b64_tr_b16 a[62:63], v7 offset:15456\n\t" \
        "ds_read_b64_tr_b16 a[64:65], v6 offset:12416\n\t" \
        "v_mfma_f32_16x16x32_bf16 v[58:61], a[28:31], v[90:93], v[58:61]\n\t" \
        "ds_read_b64_tr_b16 a[66:67], v6 offset:15488\n\t" \
        "v_cvt_pk_bf16_f32 v96, v106, v107\n\t" \
        "v_mfma_f32_16x16x32_bf16 v[38:41], a[32:35], v[86:89], v[38:41]\n\t" \
        "ds_read_b64_tr_b16 a[68:69], v7 offset:12448\n\t" \
        "ds_read_b64_tr_b16 a[70:71], v7 offset:15520\n\t" \
        "v_mfma_f32_16x16x32_bf16 v[62:65], a[32:35], v[90:93], v[62:65]\n\t" \
        "ds_read_b64_tr_b16 a[128:129], v142 offset:128\n\t" \
        "v_cvt_pk_bf16_f32 v97, v108, v109\n\t" \
        "v_mfma_f32_16x16x32_bf16 v[42:45], a[36:39], v[86:89], v[42:45]\n\t" \
        "ds_read_b64_tr_b16 a[130:131], v143 offset:128\n\t" \
        "v_cvt_pk_bf16_f32 v98, v110, v111\n\t" \
        "v_mfma_f32_16x16x32_bf16 v[66:69], a[36:39], v[90:93], v[66:69]\n\t" \
        "ds_read_b64_tr_b16 a[132:133], v146 offset:128\n\t" \
        "ds_read_b64_tr_b16 a[134:135], v147 offset:128\n\t" \
        "v_mfma_f32_16x16x32_bf16 v[46:49], a[40:43], v[86:89], v[46:49]\n\t" \
        "ds_read_b64_tr_b16 a[136:137], v144 offset:128\n\t" \
        "v_xor_b32 v134, v170, v11\n\t" \
        "v_mfma_f32_16x16x32_bf16 v[70:73], a[40:43], v[90:93], v[70:73]\n\t" \
        "ds_read_b64_tr_b16 a[138:139], v145 offset:128\n\t" \
        "v_cvt_pk_bf16_f32 v99, v112, v113\n\t" \
        "v_mfma_f32_16x16x32_bf16 v[50:53], a[44:47], v[86:89], v[50:53]\n\t" \
        "ds_read_b64_tr_b16 a[140:141], v148 offset:128\n\t" \
        "ds_read_b64_tr_b16 a[142:143], v149 offset:128\n\t" \
        "v_mfma_f32_16x16x32_bf16 v[74:77], a[44:47], v[90:93], v[74:77]\n\t" \
        "ds_read_b64_tr_b16 a[72:73], v6 offset:18432\n\t" \
        "v_cvt_pk_bf16_f32 v100, v114, v115\n\t" \
        "v_xor_b32 v135, v171, v11\n\t" \
        "s_waitcnt lgkmcnt(12)\n\t" \
        "v_mfma_f32_16x16x32_bf16 v[102:105], a[112:115], v[162:165], 0\n\t" \
        "ds_read_b64_tr_b16 a[74:75], v6 offset:21504\n\t" \
        "ds_read_b64_tr_b16 a[76:77], v7 offset:18464\n\t" \
        "v_cvt_pk_bf16_f32 v101, v116, v117\n\t" \
        "v_mfma_f32_16x16x32_bf16 v[106:109], a[116:119], v[162:165], 0\n\t" \
        "ds_read_b64 v[150:151], v10 offset:256\n\t" \
        "v_xor_b32 v136, v172, v11\n\t" \
        "v_xor_b32 v138, v170, v12\n\t" \
        "v_mfma_f32_16x16x32_bf16 v[110:113], a[120:123], v[166:169], 0\n\t" \
        "ds_read_b64_tr_b16 a[78:79], v7 offset:21536\n\t" \
        "ds_read_b64_tr_b16 a[80:81], v6 offset:18496\n\t" \
        "v_xor_b32 v137, v173, v11\n\t" \
        "v_xor_b32 v139, v171, v12\n\t" \
        "v_mfma_f32_16x16x32_bf16 v[114:117], a[124:127], v[166:169], 0\n\t" \
        "ds_read_b64_tr_b16 a[82:83], v6 offset:21568\n\t" \
        "v_xor_b32 v140, v172, v12\n\t" \
        "v_mfma_f32_16x16x32_bf16 v[30:33], a[48:51], v[94:97], v[30:33]\n\t" \
        "ds_read_b64_tr_b16 a[84:85], v7 offset:18528\n\t" \
        "v_xor_b32 v141, v173, v12\n\t" \
        "v_mfma_f32_16x16x32_bf16 v[54:57], a[48:51], v[98:101], v[54:57]\n\t" \
        "ds_read_b64_tr_b16 a[86:87], v7 offset:21600\n\t" \
        "ds_read_b64_tr_b16 a[88:89], v6 offset:18560\n\t" \
        "v_mfma_f32_16x16x32_bf16 v[34:37], a[52:55], v[94:97], v[34:37]\n\t" \
        "ds_read_b64_tr_b16 a[90:91], v6 offset:21632\n\t" \
        "v_cvt_pk_bf16_f32 v78, v118, v119\n\t" \
        "v_mfma_f32_16x16x32_bf16 v[58:61], a[52:55], v[98:101], v[58:61]\n\t" \
        "s_waitcnt vmcnt(6)\n\t" \
        "s_barrier\n\t" \
        "ds_read_b64_tr_b16 a[92:93], v7 offset:18592\n\t" \
        "ds_read_b64_tr_b16 a[94:95], v7 offset:21664\n\t" \
        "v_mfma_f32_16x16x32_bf16 v[38:41], a[56:59], v[94:97], v[38:41]\n\t" \
        "ds_read_b64_tr_b16 a[96:97], v134 offset:0\n\t" \
        "v_cvt_pk_bf16_f32 v79, v120, v121\n\t" \
        "v_mfma_f32_16x16x32_bf16 v[62:65], a[56:59], v[98:101], v[62:65]\n\t" \
        "ds_read_b64_tr_b16 a[98:99], v135 offset:0\n\t" \
        "v_cvt_pk_bf16_f32 v80, v122, v123\n\t" \
        "v_mfma_f32_16x16x32_bf16 v[42:45], a[60:63], v[94:97], v[42:45]\n\t" \
        "s_add_u32 m0, s39, 0\n\t" \
        "s_add_u32 s57, s40, 61440\n\t" \
        "buffer_load_dwordx4 v3, s[24:27], s57 offen offset:0  lds\n\t" \
        "v_mfma_f32_16x16x32_bf16 v[66:69], a[60:63], v[98:101], v[66:69]\n\t" \
        "ds_read_b64_tr_b16 a[100:101], v138 offset:0\n\t" \
        "ds_read_b64_tr_b16 a[102:103], v139 offset:0\n\t" \
        "s_waitcnt lgkmcnt(11)\n\t" \
        "v_mfma_f32_16x16x32_bf16 v[46:49], a[64:67], v[94:97], v[46:49]\n\t" \
        "ds_read_b64_tr_b16 a[104:105], v136 offset:0\n\t" \
        "v_cvt_pk_bf16_f32 v81, v124, v125\n\t" \
        "v_mfma_f32_16x16x32_bf16 v[70:73], a[64:67], v[98:101], v[70:73]\n\t" \
        "ds_read_b64_tr_b16 a[106:107], v137 offset:0\n\t" \
        "ds_read_b64_tr_b16 a[108:109], v140 offset:0\n\t" \
        "v_mfma_f32_16x16x32_bf16 v[50:53], a[68:71], v[94:97], v[50:53]\n\t" \
        "ds_read_b64_tr_b16 a[110:111], v141 offset:0\n\t" \
        "v_cvt_pk_bf16_f32 v82, v126, v127\n\t" \
        "v_cvt_pk_bf16_f32 v83, v128, v129\n\t" \
        "v_mfma_f32_16x16x32_bf16 v[74:77], a[68:71], v[98:101], v[74:77]\n\t" \
        "ds_read_b64_tr_b16 a[0:1], v6 offset:24576\n\t" \
        "v_cvt_pk_bf16_f32 v84, v130, v131\n\t" \
        "v_cvt_pk_bf16_f32 v85, v132, v133\n\t" \
        "v_mfma_f32_16x16x32_bf16 v[118:121], a[128:131], v[162:165], 0\n\t" \
        "buffer_load_dwordx4 v4, s[24:27], s57 offen offset:1024  lds\n\t" \
        "v_mfma_f32_16x16x32_bf16 v[122:125], a[132:135], v[162:165], 0\n\t" \
        "ds_read_b64_tr_b16 a[2:3], v6 offset:27648\n\t" \
        "ds_read_b64_tr_b16 a[4:5], v7 offset:24608\n\t" \
        "v_mfma_f32_16x16x32_bf16 v[126:129], a[136:139], v[166:169], 0\n\t" \
        "ds_read_b64_tr_b16 a[6:7], v7 offset:27680\n\t" \
        "v_cvt_pk_bf16_f32 v86, v102, v103\n\t" \
        "v_mfma_f32_16x16x32_bf16 v[130:133], a[140:143], v[166:169], 0\n\t" \
        "ds_read_b64_tr_b16 a[8:9], v6 offset:24640\n\t" \
        "ds_read_b64_tr_b16 a[10:11], v6 offset:27712\n\t" \
        "v_mfma_f32_16x16x32_bf16 v[30:33], a[72:75], v[78:81], v[30:33]\n\t" \
        "ds_read_b64_tr_b16 a[12:13], v7 offset:24672\n\t" \
        "v_and_b32 v154, v150, v25\n\t" \
        "v_mfma_f32_16x16x32_bf16 v[54:57], a[72:75], v[82:85], v[54:57]\n\t" \
        "ds_read_b64_tr_b16 a[14:15], v7 offset:27744\n\t" \
        "v_and_b32 v155, v150, v26\n\t" \
        "v_mfma_f32_16x16x32_bf16 v[34:37], a[76:79], v[78:81], v[34:37]\n\t" \
        "buffer_load_dwordx4 v5, s[24:27], s57 offen offset:2048  lds\n\t" \
        "v_mfma_f32_16x16x32_bf16 v[58:61], a[76:79], v[82:85], v[58:61]\n\t" \
        "ds_read_b64_tr_b16 a[16:17], v6 offset:24704\n\t" \
        "ds_read_b128 v[174:177], v9 offset:384\n\t" \
        "s_waitcnt lgkmcnt(10)\n\t" \
        "v_mfma_f32_16x16x32_bf16 v[38:41], a[80:83], v[78:81], v[38:41]\n\t" \
        "ds_read_b64_tr_b16 a[18:19], v6 offset:27776\n\t" \
        "v_and_b32 v156, v150, v27\n\t" \
        "v_mfma_f32_16x16x32_bf16 v[62:65], a[80:83], v[82:85], v[62:65]\n\t" \
        "ds_read_b64_tr_b16 a[20:21], v7 offset:24736\n\t" \
        "ds_read_b64_tr_b16 a[22:23], v7 offset:27808\n\t" \
        "v_mfma_f32_16x16x32_bf16 v[42:45], a[84:87], v[78:81], v[42:45]\n\t" \
        "ds_read_b64_tr_b16 a[112:113], v134 offset:64\n\t" \
        "v_and_b32 v157, v150, v28\n\t" \
        "v_and_b32 v158, v151, v25\n\t" \
        "v_mfma_f32_16x16x32_bf16 v[66:69], a[84:87], v[82:85], v[66:69]\n\t" \
        "ds_read_b64_tr_b16 a[114:115], v135 offset:64\n\t" \
        "v_cvt_pk_bf16_f32 v87, v104, v105\n\t" \
        "v_mfma_f32_16x16x32_bf16 v[46:49], a[88:91], v[78:81], v[46:49]\n\t" \
        "ds_read_b64_tr_b16 a[116:117], v138 offset:64\n\t" \
        "ds_read_b64_tr_b16 a[118:119], v139 offset:64\n\t" \
        "v_cvt_pk_bf16_f32 v88, v106, v107\n\t" \
        "v_and_b32 v159, v151, v26\n\t" \
        "v_mfma_f32_16x16x32_bf16 v[70:73], a[88:91], v[82:85], v[70:73]\n\t" \
        "ds_read_b64_tr_b16 a[120:121], v136 offset:64\n\t" \
        "v_cvt_pk_bf16_f32 v89, v108, v109\n\t" \
        "v_cvt_pk_bf16_f32 v90, v110, v111\n\t" \
        "v_mfma_f32_16x16x32_bf16 v[50:53], a[92:95], v[78:81], v[50:53]\n\t" \
        "ds_read_b64_tr_b16 a[122:123], v137 offset:64\n\t" \
        "ds_read_b64_tr_b16 a[124:125], v140 offset:64\n\t" \
        "v_and_b32 v160, v151, v27\n\t" \
        "v_mfma_f32_16x16x32_bf16 v[74:77], a[92:95], v[82:85], v[74:77]\n\t" \
        "ds_read_b64_tr_b16 a[126:127], v141 offset:64\n\t" \
        "v_cvt_pk_bf16_f32 v91, v112, v113\n\t" \
        "v_cvt_pk_bf16_f32 v92, v114, v115\n\t" \
        "v_mfma_f32_16x16x32_bf16 v[102:105], a[96:99], v[154:157], 0\n\t" \
        "ds_read_b64_tr_b16 a[24:25], v6 offset:30720\n\t" \
        "v_and_b32 v161, v151, v28\n\t" \
        "v_cvt_pk_bf16_f32 v93, v116, v117\n\t" \
        "v_mfma_f32_16x16x32_bf16 v[106:109], a[100:103], v[154:157], 0\n\t" \
        "ds_read_b64_tr_b16 a[26:27], v6 offset:33792\n\t" \
        "ds_read_b64_tr_b16 a[28:29], v7 offset:30752\n\t" \
        "v_mfma_f32_16x16x32_bf16 v[110:113], a[104:107], v[158:161], 0\n\t" \
        "ds_read_b64_tr_b16 a[30:31], v7 offset:33824\n\t" \
        "v_cvt_pk_bf16_f32 v94, v118, v119\n\t" \
        "v_mfma_f32_16x16x32_bf16 v[114:117], a[108:111], v[158:161], 0\n\t" \
        "ds_read_b64_tr_b16 a[32:33], v6 offset:30784\n\t" \
        "ds_read_b64_tr_b16 a[34:35], v6 offset:33856\n\t" \
        "s_waitcnt lgkmcnt(13)\n\t" \
        "v_mfma_f32_16x16x32_bf16 v[30:33], a[0:3], v[86:89], v[30:33]\n\t" \
        "ds_read_b64_tr_b16 a[36:37], v7 offset:30816\n\t" \
        "v_cvt_pk_bf16_f32 v95, v120, v121\n\t" \
        "v_mfma_f32_16x16x32_bf16 v[54:57], a[0:3], v[90:93], v[54:57]\n\t" \
        "ds_read_b64_tr_b16 a[38:39], v7 offset:33888\n\t" \
        "ds_read_b64_tr_b16 a[40:41], v6 offset:30848\n\t" \
        "v_mfma_f32_16x16x32_bf16 v[34:37], a[4:7], v[86:89], v[34:37]\n\t" \
        "ds_read_b64_tr_b16 a[42:43], v6 offset:33920\n\t" \
        "v_cvt_pk_bf16_f32 v96, v122, v123\n\t" \
        "v_mfma_f32_16x16x32_bf16 v[58:61], a[4:7], v[90:93], v[58:61]\n\t" \
        "s_waitcnt vmcnt(6)\n\t" \
        "s_barrier\n\t" \
        "ds_read_b64_tr_b16 a[44:45], v7 offset:30880\n\t" \
        "v_cvt_pk_bf16_f32 v97, v124, v125\n\t" \
        "v_mfma_f32_16x16x32_bf16 v[38:41], a[8:11], v[86:89], v[38:41]\n\t" \
        "ds_read_b64_tr_b16 a[46:47], v7 offset:33952\n\t" \
        "ds_read_b64_tr_b16 a[128:129], v134 offset:128\n\t" \
        "v_mfma_f32_16x16x32_bf16 v[62:65], a[8:11], v[90:93], v[62:65]\n\t" \
        "ds_read_b64_tr_b16 a[130:131], v135 offset:128\n\t" \
        "v_cvt_pk_bf16_f32 v98, v126, v127\n\t" \
        "v_mfma_f32_16x16x32_bf16 v[42:45], a[12:15], v[86:89], v[42:45]\n\t" \
        "s_add_u32 m0, s39, 12288\n\t" \
        "s_add_u32 s57, s40, 73728\n\t" \
        "buffer_load_dwordx4 v3, s[24:27], s57 offen offset:0  lds\n\t" \
        "v_mfma_f32_16x16x32_bf16 v[66:69], a[12:15], v[90:93], v[66:69]\n\t" \
        "ds_read_b64_tr_b16 a[132:133], v138 offset:128\n\t" \
        "ds_read_b64_tr_b16 a[134:135], v139 offset:128\n\t" \
        "v_mfma_f32_16x16x32_bf16 v[46:49], a[16:19], v[86:89], v[46:49]\n\t" \
        "ds_read_b64_tr_b16 a[136:137], v136 offset:128\n\t" \
        "v_xor_b32 v142, v174, v11\n\t" \
        "v_mfma_f32_16x16x32_bf16 v[70:73], a[16:19], v[90:93], v[70:73]\n\t" \
        "ds_read_b64_tr_b16 a[138:139], v137 offset:128\n\t" \
        "ds_read_b64_tr_b16 a[140:141], v140 offset:128\n\t" \
        "v_cvt_pk_bf16_f32 v99, v128, v129\n\t" \
        "v_mfma_f32_16x16x32_bf16 v[50:53], a[20:23], v[86:89], v[50:53]\n\t" \
        "ds_read_b64_tr_b16 a[142:143], v141 offset:128\n\t" \
        "v_cvt_pk_bf16_f32 v100, v130, v131\n\t" \
        "v_xor_b32 v143, v175, v11\n\t" \
        "v_mfma_f32_16x16x32_bf16 v[74:77], a[20:23], v[90:93], v[74:77]\n\t" \
        "ds_read_b64_tr_b16 a[48:49], v6 offset:36864\n\t" \
        "v_cvt_pk_bf16_f32 v101, v132, v133\n\t" \
        "s_waitcnt lgkmcnt(11)\n\t" \
        "v_mfma_f32_16x16x32_bf16 v[118:121], a[112:115], v[154:157], 0\n\t" \
        "buffer_load_dwordx4 v4, s[24:27], s57 offen offset:1024  lds\n\t" \
        "v_mfma_f32_16x16x32_bf16 v[122:125], a[116:119], v[154:157], 0\n\t" \
        "ds_read_b64_tr_b16 a[50:51], v6 offset:39936\n\t" \
        "ds_read_b64_tr_b16 a[52:53], v7 offset:36896\n\t" \
        "v_xor_b32 v144, v176, v11\n\t" \
        "v_xor_b32 v146, v174, v12\n\t" \
        "v_mfma_f32_16x16x32_bf16 v[126:129], a[120:123], v[158:161], 0\n\t" \
        "ds_read_b64 v[152:153], v10 offset:384\n\t" \
        "v_xor_b32 v145, v177, v11\n\t" \
        "v_xor_b32 v147, v175, v12\n\t" \
        "v_mfma_f32_16x16x32_bf16 v[130:133], a[124:127], v[158:161], 0\n\t" \
        "ds_read_b64_tr_b16 a[54:55], v7 offset:39968\n\t" \
        "ds_read_b64_tr_b16 a[56:57], v6 offset:36928\n\t" \
        "v_xor_b32 v148, v176, v12\n\t" \
        "v_mfma_f32_16x16x32_bf16 v[30:33], a[24:27], v[94:97], v[30:33]\n\t" \
        "ds_read_b64_tr_b16 a[58:59], v6 offset:40000\n\t" \
        "v_xor_b32 v149, v177, v12\n\t" \
        "v_mfma_f32_16x16x32_bf16 v[54:57], a[24:27], v[98:101], v[54:57]\n\t" \
        "ds_read_b64_tr_b16 a[60:61], v7 offset:36960\n\t" \
        "ds_read_b64_tr_b16 a[62:63], v7 offset:40032\n\t" \
        "v_mfma_f32_16x16x32_bf16 v[34:37], a[28:31], v[94:97], v[34:37]\n\t" \
        "buffer_load_dwordx4 v5, s[24:27], s57 offen offset:2048  lds\n\t" \
        "v_mfma_f32_16x16x32_bf16 v[58:61], a[28:31], v[98:101], v[58:61]\n\t" \
        "ds_read_b64_tr_b16 a[64:65], v6 offset:36992\n\t" \
        "v_cvt_pk_bf16_f32 v78, v102, v103\n\t" \
        "v_mfma_f32_16x16x32_bf16 v[38:41], a[32:35], v[94:97], v[38:41]\n\t" \
        "ds_read_b64_tr_b16 a[66:67], v6 offset:40064\n\t" \
        "v_cvt_pk_bf16_f32 v79, v104, v105\n\t" \
        "v_mfma_f32_16x16x32_bf16 v[62:65], a[32:35], v[98:101], v[62:65]\n\t" \
        "ds_read_b64_tr_b16 a[68:69], v7 offset:37024\n\t" \
        "ds_read_b64_tr_b16 a[70:71], v7 offset:40096\n\t" \
        "v_mfma_f32_16x16x32_bf16 v[42:45], a[36:39], v[94:97], v[42:45]\n\t" \
        "ds_read_b64_tr_b16 a[96:97], v142 offset:0\n\t" \
        "v_cvt_pk_bf16_f32 v80, v106, v107\n\t" \
        "v_mfma_f32_16x16x32_bf16 v[66:69], a[36:39], v[98:101], v[66:69]\n\t" \
        "ds_read_b64_tr_b16 a[98:99], v143 offset:0\n\t" \
        "ds_read_b64_tr_b16 a[100:101], v146 offset:0\n\t" \
        "v_mfma_f32_16x16x32_bf16 v[46:49], a[40:43], v[94:97], v[46:49]\n\t" \
        "ds_read_b64_tr_b16 a[102:103], v147 offset:0\n\t" \
        "v_cvt_pk_bf16_f32 v81, v108, v109\n\t" \
        "v_mfma_f32_16x16x32_bf16 v[70:73], a[40:43], v[98:101], v[70:73]\n\t" \
        "ds_read_b64_tr_b16 a[104:105], v144 offset:0\n\t" \
        "ds_read_b64_tr_b16 a[106:107], v145 offset:0\n\t" \
        "s_waitcnt lgkmcnt(12)\n\t" \
        "v_mfma_f32_16x16x32_bf16 v[50:53], a[44:47], v[94:97], v[50:53]\n\t" \
        "ds_read_b64_tr_b16 a[108:109], v148 offset:0\n\t" \
        "v_cvt_pk_bf16_f32 v82, v110, v111\n\t" \
        "v_mfma_f32_16x16x32_bf16 v[74:77], a[44:47], v[98:101], v[74:77]\n\t" \
        "ds_read_b64_tr_b16 a[110:111], v149 offset:0\n\t" \
        "v_cvt_pk_bf16_f32 v83, v112, v113\n\t" \
        "v_cvt_pk_bf16_f32 v84, v114, v115\n\t" \
        "v_mfma_f32_16x16x32_bf16 v[102:105], a[128:131], v[154:157], 0\n\t" \
        "ds_read_b64_tr_b16 a[72:73], v6 offset:43008\n\t" \
        "ds_read_b64_tr_b16 a[74:75], v6 offset:46080\n\t" \
        "v_cvt_pk_bf16_f32 v85, v116, v117\n\t" \
        "v_mfma_f32_16x16x32_bf16 v[106:109], a[132:135], v[154:157], 0\n\t" \
        "ds_read_b64_tr_b16 a[76:77], v7 offset:43040\n\t" \
        "v_cvt_pk_bf16_f32 v86, v118, v119\n\t" \
        "v_mfma_f32_16x16x32_bf16 v[110:113], a[136:139], v[158:161], 0\n\t" \
        "ds_read_b64_tr_b16 a[78:79], v7 offset:46112\n\t" \
        "ds_read_b64_tr_b16 a[80:81], v6 offset:43072\n\t" \
        "v_mfma_f32_16x16x32_bf16 v[114:117], a[140:143], v[158:161], 0\n\t" \
        "ds_read_b64_tr_b16 a[82:83], v6 offset:46144\n\t" \
        "v_cvt_pk_bf16_f32 v87, v120, v121\n\t" \
        "v_mfma_f32_16x16x32_bf16 v[30:33], a[48:51], v[78:81], v[30:33]\n\t" \
        "ds_read_b64_tr_b16 a[84:85], v7 offset:43104\n\t" \
        "ds_read_b64_tr_b16 a[86:87], v7 offset:46176\n\t" \
        "v_mfma_f32_16x16x32_bf16 v[54:57], a[48:51], v[82:85], v[54:57]\n\t" \
        "ds_read_b64_tr_b16 a[88:89], v6 offset:43136\n\t" \
        "v_and_b32 v162, v152, v25\n\t" \
        "v_mfma_f32_16x16x32_bf16 v[34:37], a[52:55], v[78:81], v[34:37]\n\t" \
        "ds_read_b128 v[170:173], v9 offset:512\n\t" \
        "ds_read_b64_tr_b16 a[90:91], v6 offset:46208\n\t" \
        "v_mfma_f32_16x16x32_bf16 v[58:61], a[52:55], v[82:85], v[58:61]\n\t" \
        "s_waitcnt vmcnt(6)\n\t" \
        "s_barrier\n\t" \
        "ds_read_b64_tr_b16 a[92:93], v7 offset:43168\n\t" \
        "v_and_b32 v163, v152, v26\n\t" \
        "v_mfma_f32_16x16x32_bf16 v[38:41], a[56:59], v[78:81], v[38:41]\n\t" \
        "ds_read_b64_tr_b16 a[94:95], v7 offset:46240\n\t" \
        "v_and_b32 v164, v152, v27\n\t" \
        "v_and_b32 v165, v152, v28\n\t" \
        "v_mfma_f32_16x16x32_bf16 v[62:65], a[56:59], v[82:85], v[62:65]\n\t" \
        "ds_read_b64_tr_b16 a[112:113], v142 offset:64\n\t" \
        "ds_read_b64_tr_b16 a[114:115], v143 offset:64\n\t" \
        "v_and_b32 v166, v153, v25\n\t" \
        "s_waitcnt lgkmcnt(13)\n\t" \
        "v_mfma_f32_16x16x32_bf16 v[42:45], a[60:63], v[78:81], v[42:45]\n\t" \
        "s_add_u32 m0, s39, 24576\n\t" \
        "s_add_u32 s57, s40, 86016\n\t" \
        "buffer_load_dwordx4 v3, s[24:27], s57 offen offset:0  lds\n\t" \
        "v_mfma_f32_16x16x32_bf16 v[66:69], a[60:63], v[82:85], v[66:69]\n\t" \
        "ds_read_b64_tr_b16 a[116:117], v146 offset:64\n\t" \
        "v_cvt_pk_bf16_f32 v88, v122, v123\n\t" \
        "v_and_b32 v167, v153, v26\n\t" \
        "v_mfma_f32_16x16x32_bf16 v[46:49], a[64:67], v[78:81], v[46:49]\n\t" \
        "ds_read_b64_tr_b16 a[118:119], v147 offset:64\n\t" \
        "ds_read_b64_tr_b16 a[120:121], v144 offset:64\n\t" \
        "v_cvt_pk_bf16_f32 v89, v124, v125\n\t" \
        "v_cvt_pk_bf16_f32 v90, v126, v127\n\t" \
        "v_mfma_f32_16x16x32_bf16 v[70:73], a[64:67], v[82:85], v[70:73]\n\t" \
        "ds_read_b64_tr_b16 a[122:123], v145 offset:64\n\t" \
        "v_and_b32 v168, v153, v27\n\t" \
        "v_mfma_f32_16x16x32_bf16 v[50:53], a[68:71], v[78:81], v[50:53]\n\t" \
        "ds_read_b64_tr_b16 a[124:125], v148 offset:64\n\t" \
        "ds_read_b64_tr_b16 a[126:127], v149 offset:64\n\t" \
        "v_cvt_pk_bf16_f32 v91, v128, v129\n\t" \
        "v_cvt_pk_bf16_f32 v92, v130, v131\n\t" \
        "v_mfma_f32_16x16x32_bf16 v[74:77], a[68:71], v[82:85], v[74:77]\n\t" \
        "ds_read_b64_tr_b16 a[0:1], v6 offset:49152\n\t" \
        "v_and_b32 v169, v153, v28\n\t" \
        "v_cvt_pk_bf16_f32 v93, v132, v133\n\t" \
        "v_mfma_f32_16x16x32_bf16 v[118:121], a[96:99], v[162:165], 0\n\t" \
        "buffer_load_dwordx4 v4, s[24:27], s57 offen offset:1024  lds\n\t" \
        "v_mfma_f32_16x16x32_bf16 v[122:125], a[100:103], v[162:165], 0\n\t" \
        "ds_read_b64_tr_b16 a[2:3], v6 offset:52224\n\t" \
        "ds_read_b64_tr_b16 a[4:5], v7 offset:49184\n\t" \
        "v_mfma_f32_16x16x32_bf16 v[126:129], a[104:107], v[166:169], 0\n\t" \
        "ds_read_b64_tr_b16 a[6:7], v7 offset:52256\n\t" \
        "v_cvt_pk_bf16_f32 v94, v102, v103\n\t" \
        "v_mfma_f32_16x16x32_bf16 v[130:133], a[108:111], v[166:169], 0\n\t" \
        "ds_read_b64_tr_b16 a[8:9], v6 offset:49216\n\t" \
        "v_cvt_pk_bf16_f32 v95, v104, v105\n\t" \
        "v_mfma_f32_16x16x32_bf16 v[30:33], a[72:75], v[86:89], v[30:33]\n\t" \
        "ds_read_b64_tr_b16 a[10:11], v6 offset:52288\n\t" \
        "ds_read_b64_tr_b16 a[12:13], v7 offset:49248\n\t" \
        "v_mfma_f32_16x16x32_bf16 v[54:57], a[72:75], v[90:93], v[54:57]\n\t" \
        "ds_read_b64_tr_b16 a[14:15], v7 offset:52320\n\t" \
        "v_cvt_pk_bf16_f32 v96, v106, v107\n\t" \
        "s_waitcnt lgkmcnt(11)\n\t" \
        "v_mfma_f32_16x16x32_bf16 v[34:37], a[76:79], v[86:89], v[34:37]\n\t" \
        "buffer_load_dwordx4 v5, s[24:27], s57 offen offset:2048  lds\n\t" \
        "v_mfma_f32_16x16x32_bf16 v[58:61], a[76:79], v[90:93], v[58:61]\n\t" \
        "ds_read_b64_tr_b16 a[16:17], v6 offset:49280\n\t" \
        "ds_read_b64_tr_b16 a[18:19], v6 offset:52352\n\t" \
        "v_mfma_f32_16x16x32_bf16 v[38:41], a[80:83], v[86:89], v[38:41]\n\t" \
        "ds_read_b64_tr_b16 a[20:21], v7 offset:49312\n\t" \
        "v_cvt_pk_bf16_f32 v97, v108, v109\n\t" \
        "v_mfma_f32_16x16x32_bf16 v[62:65], a[80:83], v[90:93], v[62:65]\n\t" \
        "ds_read_b64_tr_b16 a[22:23], v7 offset:52384\n\t" \
        "v_cvt_pk_bf16_f32 v98, v110, v111\n\t" \
        "v_mfma_f32_16x16x32_bf16 v[42:45], a[84:87], v[86:89], v[42:45]\n\t" \
        "ds_read_b64_tr_b16 a[128:129], v142 offset:128\n\t" \
        "ds_read_b64_tr_b16 a[130:131], v143 offset:128\n\t" \
        "v_mfma_f32_16x16x32_bf16 v[66:69], a[84:87], v[90:93], v[66:69]\n\t" \
        "ds_read_b64_tr_b16 a[132:133], v146 offset:128\n\t" \
        "v_xor_b32 v134, v170, v11\n\t" \
        "v_mfma_f32_16x16x32_bf16 v[46:49], a[88:91], v[86:89], v[46:49]\n\t" \
        "ds_read_b64_tr_b16 a[134:135], v147 offset:128\n\t" \
        "ds_read_b64_tr_b16 a[136:137], v144 offset:128\n\t" \
        "v_mfma_f32_16x16x32_bf16 v[70:73], a[88:91], v[90:93], v[70:73]\n\t" \
        "ds_read_b64_tr_b16 a[138:139], v145 offset:128\n\t" \
        "v_cvt_pk_bf16_f32 v99, v112, v113\n\t" \
        "v_mfma_f32_16x16x32_bf16 v[50:53], a[92:95], v[86:89], v[50:53]\n\t" \
        "ds_read_b64_tr_b16 a[140:141], v148 offset:128\n\t" \
        "v_cvt_pk_bf16_f32 v100, v114, v115\n\t" \
        "v_mfma_f32_16x16x32_bf16 v[74:77], a[92:95], v[90:93], v[74:77]\n\t" \
        "ds_read_b64_tr_b16 a[142:143], v149 offset:128\n\t" \
        "ds_read_b64_tr_b16 a[24:25], v6 offset:55296\n\t" \
        "v_xor_b32 v135, v171, v11\n\t" \
        "v_mfma_f32_16x16x32_bf16 v[102:105], a[112:115], v[162:165], 0\n\t" \
        "ds_read_b64_tr_b16 a[26:27], v6 offset:58368\n\t" \
        "v_cvt_pk_bf16_f32 v101, v116, v117\n\t" \
        "v_mfma_f32_16x16x32_bf16 v[106:109], a[116:119], v[162:165], 0\n\t" \
        "ds_read_b64_tr_b16 a[28:29], v7 offset:55328\n\t" \
        "ds_read_b64 v[150:151], v10 offset:512\n\t" \
        "v_xor_b32 v136, v172, v11\n\t" \
        "v_xor_b32 v138, v170, v12\n\t" \
        "s_waitcnt lgkmcnt(13)\n\t" \
        "v_mfma_f32_16x16x32_bf16 v[110:113], a[120:123], v[166:169], 0\n\t" \
        "ds_read_b64_tr_b16 a[30:31], v7 offset:58400\n\t" \
        "v_xor_b32 v137, v173, v11\n\t" \
        "v_xor_b32 v139, v171, v12\n\t" \
        "v_mfma_f32_16x16x32_bf16 v[114:117], a[124:127], v[166:169], 0\n\t" \
        "ds_read_b64_tr_b16 a[32:33], v6 offset:55360\n\t" \
        "ds_read_b64_tr_b16 a[34:35], v6 offset:58432\n\t" \
        "v_xor_b32 v140, v172, v12\n\t" \
        "v_mfma_f32_16x16x32_bf16 v[30:33], a[0:3], v[94:97], v[30:33]\n\t" \
        "ds_read_b64_tr_b16 a[36:37], v7 offset:55392\n\t" \
        "v_xor_b32 v141, v173, v12\n\t" \
        "v_mfma_f32_16x16x32_bf16 v[54:57], a[0:3], v[98:101], v[54:57]\n\t" \
        "ds_read_b64_tr_b16 a[38:39], v7 offset:58464\n\t" \
        "v_cvt_pk_bf16_f32 v78, v118, v119\n\t" \
        "v_mfma_f32_16x16x32_bf16 v[34:37], a[4:7], v[94:97], v[34:37]\n\t" \
        "ds_read_b64_tr_b16 a[40:41], v6 offset:55424\n\t" \
        "ds_read_b64_tr_b16 a[42:43], v6 offset:58496\n\t" \
        "v_mfma_f32_16x16x32_bf16 v[58:61], a[4:7], v[98:101], v[58:61]\n\t" \
        "s_waitcnt vmcnt(6)\n\t" \
        "s_barrier\n\t" \
        "ds_read_b64_tr_b16 a[44:45], v7 offset:55456\n\t" \
        "v_cvt_pk_bf16_f32 v79, v120, v121\n\t" \
        "v_mfma_f32_16x16x32_bf16 v[38:41], a[8:11], v[94:97], v[38:41]\n\t" \
        "ds_read_b64_tr_b16 a[46:47], v7 offset:58528\n\t" \
        "ds_read_b64_tr_b16 a[96:97], v134 offset:0\n\t" \
        "v_mfma_f32_16x16x32_bf16 v[62:65], a[8:11], v[98:101], v[62:65]\n\t" \
        "ds_read_b64_tr_b16 a[98:99], v135 offset:0\n\t" \
        "v_cvt_pk_bf16_f32 v80, v122, v123\n\t" \
        "v_mfma_f32_16x16x32_bf16 v[42:45], a[12:15], v[94:97], v[42:45]\n\t" \
        "s_add_u32 m0, s39, 36864\n\t" \
        "s_add_u32 s57, s40, 98304\n\t" \
        "buffer_load_dwordx4 v3, s[24:27], s57 offen offset:0  lds\n\t" \
        "v_mfma_f32_16x16x32_bf16 v[66:69], a[12:15], v[98:101], v[66:69]\n\t" \
        "ds_read_b64_tr_b16 a[100:101], v138 offset:0\n\t" \
        "v_cvt_pk_bf16_f32 v81, v124, v125\n\t" \
        "v_mfma_f32_16x16x32_bf16 v[46:49], a[16:19], v[94:97], v[46:49]\n\t" \
        "ds_read_b64_tr_b16 a[102:103], v139 offset:0\n\t" \
        "ds_read_b64_tr_b16 a[104:105], v136 offset:0\n\t" \
        "v_mfma_f32_16x16x32_bf16 v[70:73], a[16:19], v[98:101], v[70:73]\n\t" \
        "ds_read_b64_tr_b16 a[106:107], v137 offset:0\n\t" \
        "v_cvt_pk_bf16_f32 v82, v126, v127\n\t" \
        "s_waitcnt lgkmcnt(11)\n\t" \
        "v_mfma_f32_16x16x32_bf16 v[50:53], a[20:23], v[94:97], v[50:53]\n\t" \
        "ds_read_b64_tr_b16 a[108:109], v140 offset:0\n\t" \
        "ds_read_b64_tr_b16 a[110:111], v141 offset:0\n\t" \
        "v_cvt_pk_bf16_f32 v83, v128, v129\n\t" \
        "v_mfma_f32_16x16x32_bf16 v[74:77], a[20:23], v[98:101], v[74:77]\n\t" \
        "ds_read_b64_tr_b16 a[48:49], v6 offset:0\n\t" \
        "v_cvt_pk_bf16_f32 v84, v130, v131\n\t" \
        "v_cvt_pk_bf16_f32 v85, v132, v133\n\t" \
        "v_mfma_f32_16x16x32_bf16 v[118:121], a[128:131], v[162:165], 0\n\t" \
        "buffer_load_dwordx4 v4, s[24:27], s57 offen offset:1024  lds\n\t" \
        "v_mfma_f32_16x16x32_bf16 v[122:125], a[132:135], v[162:165], 0\n\t" \
        "ds_read_b64_tr_b16 a[50:51], v6 offset:3072\n\t" \
        "ds_read_b64_tr_b16 a[52:53], v7 offset:32\n\t" \
        "v_mfma_f32_16x16x32_bf16 v[126:129], a[136:139], v[166:169], 0\n\t" \
        "ds_read_b64_tr_b16 a[54:55], v7 offset:3104\n\t" \
        "v_cvt_pk_bf16_f32 v86, v102, v103\n\t" \
        "v_mfma_f32_16x16x32_bf16 v[130:133], a[140:143], v[166:169], 0\n\t" \
        "ds_read_b64_tr_b16 a[56:57], v6 offset:64\n\t" \
        "v_and_b32 v154, v150, v25\n\t" \
        "v_mfma_f32_16x16x32_bf16 v[30:33], a[24:27], v[78:81], v[30:33]\n\t" \
        "ds_read_b64_tr_b16 a[58:59], v6 offset:3136\n\t" \
        "ds_read_b64_tr_b16 a[60:61], v7 offset:96\n\t" \
        "v_mfma_f32_16x16x32_bf16 v[54:57], a[24:27], v[82:85], v[54:57]\n\t" \
        "ds_read_b64_tr_b16 a[62:63], v7 offset:3168\n\t" \
        "v_and_b32 v155, v150, v26\n\t" \
        "v_mfma_f32_16x16x32_bf16 v[34:37], a[28:31], v[78:81], v[34:37]\n\t" \
        "buffer_load_dwordx4 v5, s[24:27], s57 offen offset:2048  lds\n\t" \
        "v_mfma_f32_16x16x32_bf16 v[58:61], a[28:31], v[82:85], v[58:61]\n\t" \
        "ds_read_b64_tr_b16 a[64:65], v6 offset:128\n\t" \
        "ds_read_b128 v[174:177], v9 offset:640\n\t" \
        "v_mfma_f32_16x16x32_bf16 v[38:41], a[32:35], v[78:81], v[38:41]\n\t" \
        "ds_read_b64_tr_b16 a[66:67], v6 offset:3200\n\t" \
        "v_and_b32 v156, v150, v27\n\t" \
        "v_mfma_f32_16x16x32_bf16 v[62:65], a[32:35], v[82:85], v[62:65]\n\t" \
        "ds_read_b64_tr_b16 a[68:69], v7 offset:160\n\t" \
        "ds_read_b64_tr_b16 a[70:71], v7 offset:3232\n\t" \
        "s_waitcnt lgkmcnt(12)\n\t" \
        "v_mfma_f32_16x16x32_bf16 v[42:45], a[36:39], v[78:81], v[42:45]\n\t" \
        "ds_read_b64_tr_b16 a[112:113], v134 offset:64\n\t" \
        "v_and_b32 v157, v150, v28\n\t" \
        "v_and_b32 v158, v151, v25\n\t" \
        "v_mfma_f32_16x16x32_bf16 v[66:69], a[36:39], v[82:85], v[66:69]\n\t" \
        "ds_read_b64_tr_b16 a[114:115], v135 offset:64\n\t" \
        "v_cvt_pk_bf16_f32 v87, v104, v105\n\t" \
        "v_mfma_f32_16x16x32_bf16 v[46:49], a[40:43], v[78:81], v[46:49]\n\t" \
        "ds_read_b64_tr_b16 a[116:117], v138 offset:64\n\t" \
        "ds_read_b64_tr_b16 a[118:119], v139 offset:64\n\t" \
        "v_cvt_pk_bf16_f32 v88, v106, v107\n\t" \
        "v_and_b32 v159, v151, v26\n\t" \
        "v_mfma_f32_16x16x32_bf16 v[70:73], a[40:43], v[82:85], v[70:73]\n\t" \
        "ds_read_b64_tr_b16 a[120:121], v136 offset:64\n\t" \
        "v_cvt_pk_bf16_f32 v89, v108, v109\n\t" \
        "v_cvt_pk_bf16_f32 v90, v110, v111\n\t" \
        "v_mfma_f32_16x16x32_bf16 v[50:53], a[44:47], v[78:81], v[50:53]\n\t" \
        "ds_read_b64_tr_b16 a[122:123], v137 offset:64\n\t" \
        "ds_read_b64_tr_b16 a[124:125], v140 offset:64\n\t" \
        "v_and_b32 v160, v151, v27\n\t" \
        "v_mfma_f32_16x16x32_bf16 v[74:77], a[44:47], v[82:85], v[74:77]\n\t" \
        "ds_read_b64_tr_b16 a[126:127], v141 offset:64\n\t" \
        "v_cvt_pk_bf16_f32 v91, v112, v113\n\t" \
        "v_cvt_pk_bf16_f32 v92, v114, v115\n\t" \
        "v_mfma_f32_16x16x32_bf16 v[102:105], a[96:99], v[154:157], 0\n\t" \
        "ds_read_b64_tr_b16 a[72:73], v6 offset:6144\n\t" \
        "v_and_b32 v161, v151, v28\n\t" \
        "v_cvt_pk_bf16_f32 v93, v116, v117\n\t" \
        "v_mfma_f32_16x16x32_bf16 v[106:109], a[100:103], v[154:157], 0\n\t" \
        "ds_read_b64_tr_b16 a[74:75], v6 offset:9216\n\t" \
        "ds_read_b64_tr_b16 a[76:77], v7 offset:6176\n\t" \
        "v_mfma_f32_16x16x32_bf16 v[110:113], a[104:107], v[158:161], 0\n\t" \
        "ds_read_b64_tr_b16 a[78:79], v7 offset:9248\n\t" \
        "v_cvt_pk_bf16_f32 v94, v118, v119\n\t" \
        "v_mfma_f32_16x16x32_bf16 v[114:117], a[108:111], v[158:161], 0\n\t" \
        "ds_read_b64_tr_b16 a[80:81], v6 offset:6208\n\t" \
        "ds_read_b64_tr_b16 a[82:83], v6 offset:9280\n\t" \
        "s_waitcnt lgkmcnt(13)\n\t" \
        "v_mfma_f32_16x16x32_bf16 v[30:33], a[48:51], v[86:89], v[30:33]\n\t" \
        "ds_read_b64_tr_b16 a[84:85], v7 offset:6240\n\t" \
        "v_cvt_pk_bf16_f32 v95, v120, v121\n\t" \
        "v_mfma_f32_16x16x32_bf16 v[54:57], a[48:51], v[90:93], v[54:57]\n\t" \
        "ds_read_b64_tr_b16 a[86:87], v7 offset:9312\n\t" \
        "ds_read_b64_tr_b16 a[88:89], v6 offset:6272\n\t" \
        "v_mfma_f32_16x16x32_bf16 v[34:37], a[52:55], v[86:89], v[34:37]\n\t" \
        "ds_read_b64_tr_b16 a[90:91], v6 offset:9344\n\t" \
        "v_cvt_pk_bf16_f32 v96, v122, v123\n\t" \
        "v_mfma_f32_16x16x32_bf16 v[58:61], a[52:55], v[90:93], v[58:61]\n\t" \
        "s_waitcnt vmcnt(6)\n\t" \
        "s_barrier\n\t" \
        "ds_read_b64_tr_b16 a[92:93], v7 offset:6304\n\t" \
        "v_cvt_pk_bf16_f32 v97, v124, v125\n\t" \
        "v_mfma_f32_16x16x32_bf16 v[38:41], a[56:59], v[86:89], v[38:41]\n\t" \
        "ds_read_b64_tr_b16 a[94:95], v7 offset:9376\n\t" \
        "ds_read_b64_tr_b16 a[128:129], v134 offset:128\n\t" \
        "v_mfma_f32_16x16x32_bf16 v[62:65], a[56:59], v[90:93], v[62:65]\n\t" \
        "ds_read_b64_tr_b16 a[130:131], v135 offset:128\n\t" \
        "v_cvt_pk_bf16_f32 v98, v126, v127\n\t" \
        "v_mfma_f32_16x16x32_bf16 v[42:45], a[60:63], v[86:89], v[42:45]\n\t" \
        "s_add_u32 m0, s39, 49152\n\t" \
        "s_add_u32 s57, s40, 110592\n\t" \
        "buffer_load_dwordx4 v3, s[24:27], s57 offen offset:0  lds\n\t" \
        "v_mfma_f32_16x16x32_bf16 v[66:69], a[60:63], v[90:93], v[66:69]\n\t" \
        "ds_read_b64_tr_b16 a[132:133], v138 offset:128\n\t" \
        "ds_read_b64_tr_b16 a[134:135], v139 offset:128\n\t" \
        "v_mfma_f32_16x16x32_bf16 v[46:49], a[64:67], v[86:89], v[46:49]\n\t" \
        "ds_read_b64_tr_b16 a[136:137], v136 offset:128\n\t" \
        "v_xor_b32 v142, v174, v11\n\t" \
        "v_mfma_f32_16x16x32_bf16 v[70:73], a[64:67], v[90:93], v[70:73]\n\t" \
        "ds_read_b64_tr_b16 a[138:139], v137 offset:128\n\t" \
        "ds_read_b64_tr_b16 a[140:141], v140 offset:128\n\t" \
        "v_cvt_pk_bf16_f32 v99, v128, v129\n\t" \
        "v_mfma_f32_16x16x32_bf16 v[50:53], a[68:71], v[86:89], v[50:53]\n\t" \
        "ds_read_b64_tr_b16 a[142:143], v141 offset:128\n\t" \
        "v_cvt_pk_bf16_f32 v100, v130, v131\n\t" \
        "v_xor_b32 v143, v175, v11\n\t" \
        "v_mfma_f32_16x16x32_bf16 v[74:77], a[68:71], v[90:93], v[74:77]\n\t" \
        "ds_read_b64_tr_b16 a[0:1], v6 offset:12288\n\t" \
        "v_cvt_pk_bf16_f32 v101, v132, v133\n\t" \
        "s_waitcnt lgkmcnt(11)\n\t" \
        "v_mfma_f32_16x16x32_bf16 v[118:121], a[112:115], v[154:157], 0\n\t" \
        "buffer_load_dwordx4 v4, s[24:27], s57 offen offset:1024  lds\n\t" \
        "v_mfma_f32_16x16x32_bf16 v[122:125], a[116:119], v[154:157], 0\n\t" \
        "ds_read_b64_tr_b16 a[2:3], v6 offset:15360\n\t" \
        "ds_read_b64_tr_b16 a[4:5], v7 offset:12320\n\t" \
        "v_xor_b32 v144, v176, v11\n\t" \
        "v_xor_b32 v146, v174, v12\n\t" \
        "v_mfma_f32_16x16x32_bf16 v[126:129], a[120:123], v[158:161], 0\n\t" \
        "ds_read_b64 v[152:153], v10 offset:640\n\t" \
        "v_xor_b32 v145, v177, v11\n\t" \
        "v_xor_b32 v147, v175, v12\n\t" \
        "v_mfma_f32_16x16x32_bf16 v[130:133], a[124:127], v[158:161], 0\n\t" \
        "ds_read_b64_tr_b16 a[6:7], v7 offset:15392\n\t" \
        "ds_read_b64_tr_b16 a[8:9], v6 offset:12352\n\t" \
        "v_xor_b32 v148, v176, v12\n\t" \
        "v_mfma_f32_16x16x32_bf16 v[30:33], a[72:75], v[94:97], v[30:33]\n\t" \
        "ds_read_b64_tr_b16 a[10:11], v6 offset:15424\n\t" \
        "v_xor_b32 v149, v177, v12\n\t" \
        "v_mfma_f32_16x16x32_bf16 v[54:57], a[72:75], v[98:101], v[54:57]\n\t" \
        "ds_read_b64_tr_b16 a[12:13], v7 offset:12384\n\t" \
        "ds_read_b64_tr_b16 a[14:15], v7 offset:15456\n\t" \
        "v_mfma_f32_16x16x32_bf16 v[34:37], a[76:79], v[94:97], v[34:37]\n\t" \
        "buffer_load_dwordx4 v5, s[24:27], s57 offen offset:2048  lds\n\t" \
        "v_mfma_f32_16x16x32_bf16 v[58:61], a[76:79], v[98:101], v[58:61]\n\t" \
        "ds_read_b64_tr_b16 a[16:17], v6 offset:12416\n\t" \
        "v_cvt_pk_bf16_f32 v78, v102, v103\n\t" \
        "v_mfma_f32_16x16x32_bf16 v[38:41], a[80:83], v[94:97], v[38:41]\n\t" \
        "ds_read_b64_tr_b16 a[18:19], v6 offset:15488\n\t" \
        "v_cvt_pk_bf16_f32 v79, v104, v105\n\t" \
        "v_mfma_f32_16x16x32_bf16 v[62:65], a[80:83], v[98:101], v[62:65]\n\t" \
        "ds_read_b64_tr_b16 a[20:21], v7 offset:12448\n\t" \
        "ds_read_b64_tr_b16 a[22:23], v7 offset:15520\n\t" \
        "v_mfma_f32_16x16x32_bf16 v[42:45], a[84:87], v[94:97], v[42:45]\n\t" \
        "ds_read_b64_tr_b16 a[96:97], v142 offset:0\n\t" \
        "v_cvt_pk_bf16_f32 v80, v106, v107\n\t" \
        "v_mfma_f32_16x16x32_bf16 v[66:69], a[84:87], v[98:101], v[66:69]\n\t" \
        "ds_read_b64_tr_b16 a[98:99], v143 offset:0\n\t" \
        "ds_read_b64_tr_b16 a[100:101], v146 offset:0\n\t" \
        "v_mfma_f32_16x16x32_bf16 v[46:49], a[88:91], v[94:97], v[46:49]\n\t" \
        "ds_read_b64_tr_b16 a[102:103], v147 offset:0\n\t" \
        "v_cvt_pk_bf16_f32 v81, v108, v109\n\t" \
        "v_mfma_f32_16x16x32_bf16 v[70:73], a[88:91], v[98:101], v[70:73]\n\t" \
        "ds_read_b64_tr_b16 a[104:105], v144 offset:0\n\t" \
        "ds_read_b64_tr_b16 a[106:107], v145 offset:0\n\t" \
        "s_waitcnt lgkmcnt(12)\n\t" \
        "v_mfma_f32_16x16x32_bf16 v[50:53], a[92:95], v[94:97], v[50:53]\n\t" \
        "ds_read_b64_tr_b16 a[108:109], v148 offset:0\n\t" \
        "v_cvt_pk_bf16_f32 v82, v110, v111\n\t" \
        "v_mfma_f32_16x16x32_bf16 v[74:77], a[92:95], v[98:101], v[74:77]\n\t" \
        "ds_read_b64_tr_b16 a[110:111], v149 offset:0\n\t" \
        "v_cvt_pk_bf16_f32 v83, v112, v113\n\t" \
        "v_cvt_pk_bf16_f32 v84, v114, v115\n\t" \
        "v_mfma_f32_16x16x32_bf16 v[102:105], a[128:131], v[154:157], 0\n\t" \
        "ds_read_b64_tr_b16 a[24:25], v6 offset:18432\n\t" \
        "ds_read_b64_tr_b16 a[26:27], v6 offset:21504\n\t" \
        "v_cvt_pk_bf16_f32 v85, v116, v117\n\t" \
        "v_mfma_f32_16x16x32_bf16 v[106:109], a[132:135], v[154:157], 0\n\t" \
        "ds_read_b64_tr_b16 a[28:29], v7 offset:18464\n\t" \
        "v_cvt_pk_bf16_f32 v86, v118, v119\n\t" \
        "v_mfma_f32_16x16x32_bf16 v[110:113], a[136:139], v[158:161], 0\n\t" \
        "ds_read_b64_tr_b16 a[30:31], v7 offset:21536\n\t" \
        "ds_read_b64_tr_b16 a[32:33], v6 offset:18496\n\t" \
        "v_mfma_f32_16x16x32_bf16 v[114:117], a[140:143], v[158:161], 0\n\t" \
        "ds_read_b64_tr_b16 a[34:35], v6 offset:21568\n\t" \
        "v_cvt_pk_bf16_f32 v87, v120, v121\n\t" \
        "v_mfma_f32_16x16x32_bf16 v[30:33], a[0:3], v[78:81], v[30:33]\n\t" \
        "ds_read_b64_tr_b16 a[36:37], v7 offset:18528\n\t" \
        "ds_read_b64_tr_b16 a[38:39], v7 offset:21600\n\t" \
        "v_mfma_f32_16x16x32_bf16 v[54:57], a[0:3], v[82:85], v[54:57]\n\t" \
        "ds_read_b64_tr_b16 a[40:41], v6 offset:18560\n\t" \
        "v_and_b32 v162, v152, v25\n\t" \
        "v_mfma_f32_16x16x32_bf16 v[34:37], a[4:7], v[78:81], v[34:37]\n\t" \
        "ds_read_b128 v[170:173], v9 offset:768\n\t" \
        "ds_read_b64_tr_b16 a[42:43], v6 offset:21632\n\t" \
        "v_mfma_f32_16x16x32_bf16 v[58:61], a[4:7], v[82:85], v[58:61]\n\t" \
        "s_waitcnt vmcnt(6)\n\t" \
        "s_barrier\n\t" \
        "ds_read_b64_tr_b16 a[44:45], v7 offset:18592\n\t" \
        "v_and_b32 v163, v152, v26\n\t" \
        "v_mfma_f32_16x16x32_bf16 v[38:41], a[8:11], v[78:81], v[38:41]\n\t" \
        "ds_read_b64_tr_b16 a[46:47], v7 offset:21664\n\t" \
        "v_and_b32 v164, v152, v27\n\t" \
        "v_and_b32 v165, v152, v28\n\t" \
        "v_mfma_f32_16x16x32_bf16 v[62:65], a[8:11], v[82:85], v[62:65]\n\t" \
        "ds_read_b64_tr_b16 a[112:113], v142 offset:64\n\t" \
        "ds_read_b64_tr_b16 a[114:115], v143 offset:64\n\t" \
        "v_and_b32 v166, v153, v25\n\t" \
        "s_waitcnt lgkmcnt(13)\n\t" \
        "v_mfma_f32_16x16x32_bf16 v[42:45], a[12:15], v[78:81], v[42:45]\n\t" \
        "s_add_u32 m0, s39, 0\n\t" \
        "s_add_u32 s57, s40, 122880\n\t" \
        "buffer_load_dwordx4 v3, s[24:27], s57 offen offset:0  lds\n\t" \
        "v_mfma_f32_16x16x32_bf16 v[66:69], a[12:15], v[82:85], v[66:69]\n\t" \
        "ds_read_b64_tr_b16 a[116:117], v146 offset:64\n\t" \
        "v_cvt_pk_bf16_f32 v88, v122, v123\n\t" \
        "v_and_b32 v167, v153, v26\n\t" \
        "v_mfma_f32_16x16x32_bf16 v[46:49], a[16:19], v[78:81], v[46:49]\n\t" \
        "ds_read_b64_tr_b16 a[118:119], v147 offset:64\n\t" \
        "ds_read_b64_tr_b16 a[120:121], v144 offset:64\n\t" \
        "v_cvt_pk_bf16_f32 v89, v124, v125\n\t" \
        "v_cvt_pk_bf16_f32 v90, v126, v127\n\t" \
        "v_mfma_f32_16x16x32_bf16 v[70:73], a[16:19], v[82:85], v[70:73]\n\t" \
        "ds_read_b64_tr_b16 a[122:123], v145 offset:64\n\t" \
        "v_and_b32 v168, v153, v27\n\t" \
        "v_mfma_f32_16x16x32_bf16 v[50:53], a[20:23], v[78:81], v[50:53]\n\t" \
        "ds_read_b64_tr_b16 a[124:125], v148 offset:64\n\t" \
        "ds_read_b64_tr_b16 a[126:127], v149 offset:64\n\t" \
        "v_cvt_pk_bf16_f32 v91, v128, v129\n\t" \
        "v_cvt_pk_bf16_f32 v92, v130, v131\n\t" \
        "v_mfma_f32_16x16x32_bf16 v[74:77], a[20:23], v[82:85], v[74:77]\n\t" \
        "ds_read_b64_tr_b16 a[48:49], v6 offset:24576\n\t" \
        "v_and_b32 v169, v153, v28\n\t" \
        "v_cvt_pk_bf16_f32 v93, v132, v133\n\t" \
        "v_mfma_f32_16x16x32_bf16 v[118:121], a[96:99], v[162:165], 0\n\t" \
        "buffer_load_dwordx4 v4, s[24:27], s57 offen offset:1024  lds\n\t" \
        "v_mfma_f32_16x16x32_bf16 v[122:125], a[100:103], v[162:165], 0\n\t" \
        "ds_read_b64_tr_b16 a[50:51], v6 offset:27648\n\t" \
        "ds_read_b64_tr_b16 a[52:53], v7 offset:24608\n\t" \
        "v_mfma_f32_16x16x32_bf16 v[126:129], a[104:107], v[166:169], 0\n\t" \
        "ds_read_b64_tr_b16 a[54:55], v7 offset:27680\n\t" \
        "v_cvt_pk_bf16_f32 v94, v102, v103\n\t" \
        "v_mfma_f32_16x16x32_bf16 v[130:133], a[108:111], v[166:169], 0\n\t" \
        "ds_read_b64_tr_b16 a[56:57], v6 offset:24640\n\t" \
        "v_cvt_pk_bf16_f32 v95, v104, v105\n\t" \
        "v_mfma_f32_16x16x32_bf16 v[30:33], a[24:27], v[86:89], v[30:33]\n\t" \
        "ds_read_b64_tr_b16 a[58:59], v6 offset:27712\n\t" \
        "ds_read_b64_tr_b16 a[60:61], v7 offset:24672\n\t" \
        "v_mfma_f32_16x16x32_bf16 v[54:57], a[24:27], v[90:93], v[54:57]\n\t" \
        "ds_read_b64_tr_b16 a[62:63], v7 offset:27744\n\t" \
        "v_cvt_pk_bf16_f32 v96, v106, v107\n\t" \
        "s_waitcnt lgkmcnt(11)\n\t" \
        "v_mfma_f32_16x16x32_bf16 v[34:37], a[28:31], v[86:89], v[34:37]\n\t" \
        "buffer_load_dwordx4 v5, s[24:27], s57 offen offset:2048  lds\n\t" \
        "v_mfma_f32_16x16x32_bf16 v[58:61], a[28:31], v[90:93], v[58:61]\n\t" \
        "ds_read_b64_tr_b16 a[64:65], v6 offset:24704\n\t" \
        "ds_read_b64_tr_b16 a[66:67], v6 offset:27776\n\t" \
        "v_mfma_f32_16x16x32_bf16 v[38:41], a[32:35], v[86:89], v[38:41]\n\t" \
        "ds_read_b64_tr_b16 a[68:69], v7 offset:24736\n\t" \
        "v_cvt_pk_bf16_f32 v97, v108, v109\n\t" \
        "v_mfma_f32_16x16x32_bf16 v[62:65], a[32:35], v[90:93], v[62:65]\n\t" \
        "ds_read_b64_tr_b16 a[70:71], v7 offset:27808\n\t" \
        "v_cvt_pk_bf16_f32 v98, v110, v111\n\t" \
        "v_mfma_f32_16x16x32_bf16 v[42:45], a[36:39], v[86:89], v[42:45]\n\t" \
        "ds_read_b64_tr_b16 a[128:129], v142 offset:128\n\t" \
        "ds_read_b64_tr_b16 a[130:131], v143 offset:128\n\t" \
        "v_mfma_f32_16x16x32_bf16 v[66:69], a[36:39], v[90:93], v[66:69]\n\t" \
        "ds_read_b64_tr_b16 a[132:133], v146 offset:128\n\t" \
        "v_xor_b32 v134, v170, v11\n\t" \
        "v_mfma_f32_16x16x32_bf16 v[46:49], a[40:43], v[86:89], v[46:49]\n\t" \
        "ds_read_b64_tr_b16 a[134:135], v147 offset:128\n\t" \
        "ds_read_b64_tr_b16 a[136:137], v144 offset:128\n\t" \
        "v_mfma_f32_16x16x32_bf16 v[70:73], a[40:43], v[90:93], v[70:73]\n\t" \
        "ds_read_b64_tr_b16 a[138:139], v145 offset:128\n\t" \
        "v_cvt_pk_bf16_f32 v99, v112, v113\n\t" \
        "v_mfma_f32_16x16x32_bf16 v[50:53], a[44:47], v[86:89], v[50:53]\n\t" \
        "ds_read_b64_tr_b16 a[140:141], v148 offset:128\n\t" \
        "v_cvt_pk_bf16_f32 v100, v114, v115\n\t" \
        "v_mfma_f32_16x16x32_bf16 v[74:77], a[44:47], v[90:93], v[74:77]\n\t" \
        "ds_read_b64_tr_b16 a[142:143], v149 offset:128\n\t" \
        "ds_read_b64_tr_b16 a[72:73], v6 offset:30720\n\t" \
        "v_xor_b32 v135, v171, v11\n\t" \
        "v_mfma_f32_16x16x32_bf16 v[102:105], a[112:115], v[162:165], 0\n\t" \
        "ds_read_b64_tr_b16 a[74:75], v6 offset:33792\n\t" \
        "v_cvt_pk_bf16_f32 v101, v116, v117\n\t" \
        "v_mfma_f32_16x16x32_bf16 v[106:109], a[116:119], v[162:165], 0\n\t" \
        "ds_read_b64_tr_b16 a[76:77], v7 offset:30752\n\t" \
        "ds_read_b64 v[150:151], v10 offset:768\n\t" \
        "v_xor_b32 v136, v172, v11\n\t" \
        "v_xor_b32 v138, v170, v12\n\t" \
        "s_waitcnt lgkmcnt(13)\n\t" \
        "v_mfma_f32_16x16x32_bf16 v[110:113], a[120:123], v[166:169], 0\n\t" \
        "ds_read_b64_tr_b16 a[78:79], v7 offset:33824\n\t" \
        "v_xor_b32 v137, v173, v11\n\t" \
        "v_xor_b32 v139, v171, v12\n\t" \
        "v_mfma_f32_16x16x32_bf16 v[114:117], a[124:127], v[166:169], 0\n\t" \
        "ds_read_b64_tr_b16 a[80:81], v6 offset:30784\n\t" \
        "ds_read_b64_tr_b16 a[82:83], v6 offset:33856\n\t" \
        "v_xor_b32 v140, v172, v12\n\t" \
        "v_mfma_f32_16x16x32_bf16 v[30:33], a[48:51], v[94:97], v[30:33]\n\t" \
        "ds_read_b64_tr_b16 a[84:85], v7 offset:30816\n\t" \
        "v_xor_b32 v141, v173, v12\n\t" \
        "v_mfma_f32_16x16x32_bf16 v[54:57], a[48:51], v[98:101], v[54:57]\n\t" \
        "ds_read_b64_tr_b16 a[86:87], v7 offset:33888\n\t" \
        "v_cvt_pk_bf16_f32 v78, v118, v119\n\t" \
        "v_mfma_f32_16x16x32_bf16 v[34:37], a[52:55], v[94:97], v[34:37]\n\t" \
        "ds_read_b64_tr_b16 a[88:89], v6 offset:30848\n\t" \
        "ds_read_b64_tr_b16 a[90:91], v6 offset:33920\n\t" \
        "v_mfma_f32_16x16x32_bf16 v[58:61], a[52:55], v[98:101], v[58:61]\n\t" \
        "s_waitcnt vmcnt(6)\n\t" \
        "s_barrier\n\t" \
        "ds_read_b64_tr_b16 a[92:93], v7 offset:30880\n\t" \
        "v_cvt_pk_bf16_f32 v79, v120, v121\n\t" \
        "v_mfma_f32_16x16x32_bf16 v[38:41], a[56:59], v[94:97], v[38:41]\n\t" \
        "ds_read_b64_tr_b16 a[94:95], v7 offset:33952\n\t" \
        "ds_read_b64_tr_b16 a[96:97], v134 offset:0\n\t" \
        "v_mfma_f32_16x16x32_bf16 v[62:65], a[56:59], v[98:101], v[62:65]\n\t" \
        "ds_read_b64_tr_b16 a[98:99], v135 offset:0\n\t" \
        "v_cvt_pk_bf16_f32 v80, v122, v123\n\t" \
        "v_mfma_f32_16x16x32_bf16 v[42:45], a[60:63], v[94:97], v[42:45]\n\t" \
        "s_add_u32 m0, s39, 12288\n\t" \
        "s_add_u32 s57, s40, 135168\n\t" \
        "buffer_load_dwordx4 v3, s[24:27], s57 offen offset:0  lds\n\t" \
        "v_mfma_f32_16x16x32_bf16 v[66:69], a[60:63], v[98:101], v[66:69]\n\t" \
        "ds_read_b64_tr_b16 a[100:101], v138 offset:0\n\t" \
        "v_cvt_pk_bf16_f32 v81, v124, v125\n\t" \
        "v_mfma_f32_16x16x32_bf16 v[46:49], a[64:67], v[94:97], v[46:49]\n\t" \
        "ds_read_b64_tr_b16 a[102:103], v139 offset:0\n\t" \
        "ds_read_b64_tr_b16 a[104:105], v136 offset:0\n\t" \
        "v_mfma_f32_16x16x32_bf16 v[70:73], a[64:67], v[98:101], v[70:73]\n\t" \
        "ds_read_b64_tr_b16 a[106:107], v137 offset:0\n\t" \
        "v_cvt_pk_bf16_f32 v82, v126, v127\n\t" \
        "s_waitcnt lgkmcnt(11)\n\t" \
        "v_mfma_f32_16x16x32_bf16 v[50:53], a[68:71], v[94:97], v[50:53]\n\t" \
        "ds_read_b64_tr_b16 a[108:109], v140 offset:0\n\t" \
        "ds_read_b64_tr_b16 a[110:111], v141 offset:0\n\t" \
        "v_cvt_pk_bf16_f32 v83, v128, v129\n\t" \
        "v_mfma_f32_16x16x32_bf16 v[74:77], a[68:71], v[98:101], v[74:77]\n\t" \
        "ds_read_b64_tr_b16 a[0:1], v6 offset:36864\n\t" \
        "v_cvt_pk_bf16_f32 v84, v130, v131\n\t" \
        "v_cvt_pk_bf16_f32 v85, v132, v133\n\t" \
        "v_mfma_f32_16x16x32_bf16 v[118:121], a[128:131], v[162:165], 0\n\t" \
        "buffer_load_dwordx4 v4, s[24:27], s57 offen offset:1024  lds\n\t" \
        "v_mfma_f32_16x16x32_bf16 v[122:125], a[132:135], v[162:165], 0\n\t" \
        "ds_read_b64_tr_b16 a[2:3], v6 offset:39936\n\t" \
        "ds_read_b64_tr_b16 a[4:5], v7 offset:36896\n\t" \
        "v_mfma_f32_16x16x32_bf16 v[126:129], a[136:139], v[166:169], 0\n\t" \
        "ds_read_b64_tr_b16 a[6:7], v7 offset:39968\n\t" \
        "v_cvt_pk_bf16_f32 v86, v102, v103\n\t" \
        "v_mfma_f32_16x16x32_bf16 v[130:133], a[140:143], v[166:169], 0\n\t" \
        "ds_read_b64_tr_b16 a[8:9], v6 offset:36928\n\t" \
        "v_and_b32 v154, v150, v25\n\t" \
        "v_mfma_f32_16x16x32_bf16 v[30:33], a[72:75], v[78:81], v[30:33]\n\t" \
        "ds_read_b64_tr_b16 a[10:11], v6 offset:40000\n\t" \
        "ds_read_b64_tr_b16 a[12:13], v7 offset:36960\n\t" \
        "v_mfma_f32_16x16x32_bf16 v[54:57], a[72:75], v[82:85], v[54:57]\n\t" \
        "ds_read_b64_tr_b16 a[14:15], v7 offset:40032\n\t" \
        "v_and_b32 v155, v150, v26\n\t" \
        "v_mfma_f32_16x16x32_bf16 v[34:37], a[76:79], v[78:81], v[34:37]\n\t" \
        "buffer_load_dwordx4 v5, s[24:27], s57 offen offset:2048  lds\n\t" \
        "v_mfma_f32_16x16x32_bf16 v[58:61], a[76:79], v[82:85], v[58:61]\n\t" \
        "ds_read_b64_tr_b16 a[16:17], v6 offset:36992\n\t" \
        "ds_read_b128 v[174:177], v9 offset:896\n\t" \
        "v_mfma_f32_16x16x32_bf16 v[38:41], a[80:83], v[78:81], v[38:41]\n\t" \
        "ds_read_b64_tr_b16 a[18:19], v6 offset:40064\n\t" \
        "v_and_b32 v156, v150, v27\n\t" \
        "v_mfma_f32_16x16x32_bf16 v[62:65], a[80:83], v[82:85], v[62:65]\n\t" \
        "ds_read_b64_tr_b16 a[20:21], v7 offset:37024\n\t" \
        "v_and_b32 v157, v150, v28\n\t" \
        "s_waitcnt lgkmcnt(11)\n\t" \
        "v_mfma_f32_16x16x32_bf16 v[42:45], a[84:87], v[78:81], v[42:45]\n\t" \
        "ds_read_b64_tr_b16 a[22:23], v7 offset:40096\n\t" \
        "ds_read_b64_tr_b16 a[112:113], v134 offset:64\n\t" \
        "v_and_b32 v158, v151, v25\n\t" \
        "v_mfma_f32_16x16x32_bf16 v[66:69], a[84:87], v[82:85], v[66:69]\n\t" \
        "ds_read_b64_tr_b16 a[114:115], v135 offset:64\n\t" \
        "v_cvt_pk_bf16_f32 v87, v104, v105\n\t" \
        "v_mfma_f32_16x16x32_bf16 v[46:49], a[88:91], v[78:81], v[46:49]\n\t" \
        "ds_read_b64_tr_b16 a[116:117], v138 offset:64\n\t" \
        "ds_read_b64_tr_b16 a[118:119], v139 offset:64\n\t" \
        "v_cvt_pk_bf16_f32 v88, v106, v107\n\t" \
        "v_and_b32 v159, v151, v26\n\t" \
        "v_mfma_f32_16x16x32_bf16 v[70:73], a[88:91], v[82:85], v[70:73]\n\t" \
        "ds_read_b64_tr_b16 a[120:121], v136 offset:64\n\t" \
        "v_cvt_pk_bf16_f32 v89, v108, v109\n\t" \
        "v_cvt_pk_bf16_f32 v90, v110, v111\n\t" \
        "v_mfma_f32_16x16x32_bf16 v[50:53], a[92:95], v[78:81], v[50:53]\n\t" \
        "ds_read_b64_tr_b16 a[122:123], v137 offset:64\n\t" \
        "ds_read_b64_tr_b16 a[124:125], v140 offset:64\n\t" \
        "v_and_b32 v160, v151, v27\n\t" \
        "v_mfma_f32_16x16x32_bf16 v[74:77], a[92:95], v[82:85], v[74:77]\n\t" \
        "ds_read_b64_tr_b16 a[126:127], v141 offset:64\n\t" \
        "v_cvt_pk_bf16_f32 v91, v112, v113\n\t" \
        "v_cvt_pk_bf16_f32 v92, v114, v115\n\t" \
        "v_mfma_f32_16x16x32_bf16 v[102:105], a[96:99], v[154:157], 0\n\t" \
        "ds_read_b64_tr_b16 a[24:25], v6 offset:43008\n\t" \
        "v_and_b32 v161, v151, v28\n\t" \
        "v_cvt_pk_bf16_f32 v93, v116, v117\n\t" \
        "v_mfma_f32_16x16x32_bf16 v[106:109], a[100:103], v[154:157], 0\n\t" \
        "ds_read_b64_tr_b16 a[26:27], v6 offset:46080\n\t" \
        "ds_read_b64_tr_b16 a[28:29], v7 offset:43040\n\t" \
        "v_mfma_f32_16x16x32_bf16 v[110:113], a[104:107], v[158:161], 0\n\t" \
        "ds_read_b64_tr_b16 a[30:31], v7 offset:46112\n\t" \
        "v_cvt_pk_bf16_f32 v94, v118, v119\n\t" \
        "v_mfma_f32_16x16x32_bf16 v[114:117], a[108:111], v[158:161], 0\n\t" \
        "ds_read_b64_tr_b16 a[32:33], v6 offset:43072\n\t" \
        "ds_read_b64_tr_b16 a[34:35], v6 offset:46144\n\t" \
        "s_waitcnt lgkmcnt(13)\n\t" \
        "v_mfma_f32_16x16x32_bf16 v[30:33], a[0:3], v[86:89], v[30:33]\n\t" \
        "ds_read_b64_tr_b16 a[36:37], v7 offset:43104\n\t" \
        "v_cvt_pk_bf16_f32 v95, v120, v121\n\t" \
        "v_mfma_f32_16x16x32_bf16 v[54:57], a[0:3], v[90:93], v[54:57]\n\t" \
        "ds_read_b64_tr_b16 a[38:39], v7 offset:46176\n\t" \
        "ds_read_b64_tr_b16 a[40:41], v6 offset:43136\n\t" \
        "v_mfma_f32_16x16x32_bf16 v[34:37], a[4:7], v[86:89], v[34:37]\n\t" \
        "ds_read_b64_tr_b16 a[42:43], v6 offset:46208\n\t" \
        "v_cvt_pk_bf16_f32 v96, v122, v123\n\t" \
        "v_mfma_f32_16x16x32_bf16 v[58:61], a[4:7], v[90:93], v[58:61]\n\t" \
        "s_waitcnt vmcnt(6)\n\t" \
        "s_barrier\n\t" \
        "ds_read_b64_tr_b16 a[44:45], v7 offset:43168\n\t" \
        "v_cvt_pk_bf16_f32 v97, v124, v125\n\t" \
        "v_mfma_f32_16x16x32_bf16 v[38:41], a[8:11], v[86:89], v[38:41]\n\t" \
        "ds_read_b64_tr_b16 a[46:47], v7 offset:46240\n\t" \
        "ds_read_b64_tr_b16 a[128:129], v134 offset:128\n\t" \
        "v_mfma_f32_16x16x32_bf16 v[62:65], a[8:11], v[90:93], v[62:65]\n\t" \
        "ds_read_b64_tr_b16 a[130:131], v135 offset:128\n\t" \
        "v_cvt_pk_bf16_f32 v98, v126, v127\n\t" \
        "v_mfma_f32_16x16x32_bf16 v[42:45], a[12:15], v[86:89], v[42:45]\n\t" \
        "s_add_u32 m0, s39, 24576\n\t" \
        "s_add_u32 s57, s40, 147456\n\t" \
        "buffer_load_dwordx4 v3, s[24:27], s57 offen offset:0  lds\n\t" \
        "v_mfma_f32_16x16x32_bf16 v[66:69], a[12:15], v[90:93], v[66:69]\n\t" \
        "ds_read_b64_tr_b16 a[132:133], v138 offset:128\n\t" \
        "ds_read_b64_tr_b16 a[134:135], v139 offset:128\n\t" \
        "v_mfma_f32_16x16x32_bf16 v[46:49], a[16:19], v[86:89], v[46:49]\n\t" \
        "ds_read_b64_tr_b16 a[136:137], v136 offset:128\n\t" \
        "v_xor_b32 v142, v174, v11\n\t" \
        "v_mfma_f32_16x16x32_bf16 v[70:73], a[16:19], v[90:93], v[70:73]\n\t" \
        "ds_read_b64_tr_b16 a[138:139], v137 offset:128\n\t" \
        "ds_read_b64_tr_b16 a[140:141], v140 offset:128\n\t" \
        "v_cvt_pk_bf16_f32 v99, v128, v129\n\t" \
        "v_mfma_f32_16x16x32_bf16 v[50:53], a[20:23], v[86:89], v[50:53]\n\t" \
        "ds_read_b64_tr_b16 a[142:143], v141 offset:128\n\t" \
        "v_cvt_pk_bf16_f32 v100, v130, v131\n\t" \
        "v_xor_b32 v143, v175, v11\n\t" \
        "v_mfma_f32_16x16x32_bf16 v[74:77], a[20:23], v[90:93], v[74:77]\n\t" \
        "ds_read_b64_tr_b16 a[48:49], v6 offset:49152\n\t" \
        "v_cvt_pk_bf16_f32 v101, v132, v133\n\t" \
        "s_waitcnt lgkmcnt(11)\n\t" \
        "v_mfma_f32_16x16x32_bf16 v[118:121], a[112:115], v[154:157], 0\n\t" \
        "buffer_load_dwordx4 v4, s[24:27], s57 offen offset:1024  lds\n\t" \
        "v_mfma_f32_16x16x32_bf16 v[122:125], a[116:119], v[154:157], 0\n\t" \
        "ds_read_b64_tr_b16 a[50:51], v6 offset:52224\n\t" \
        "ds_read_b64_tr_b16 a[52:53], v7 offset:49184\n\t" \
        "v_xor_b32 v144, v176, v11\n\t" \
        "v_xor_b32 v146, v174, v12\n\t" \
        "v_mfma_f32_16x16x32_bf16 v[126:129], a[120:123], v[158:161], 0\n\t" \
        "ds_read_b64 v[152:153], v10 offset:896\n\t" \
        "v_xor_b32 v145, v177, v11\n\t" \
        "v_xor_b32 v147, v175, v12\n\t" \
        "v_mfma_f32_16x16x32_bf16 v[130:133], a[124:127], v[158:161], 0\n\t" \
        "ds_read_b64_tr_b16 a[54:55], v7 offset:52256\n\t" \
        "ds_read_b64_tr_b16 a[56:57], v6 offset:49216\n\t" \
        "v_xor_b32 v148, v176, v12\n\t" \
        "v_mfma_f32_16x16x32_bf16 v[30:33], a[24:27], v[94:97], v[30:33]\n\t" \
        "ds_read_b64_tr_b16 a[58:59], v6 offset:52288\n\t" \
        "v_xor_b32 v149, v177, v12\n\t" \
        "v_mfma_f32_16x16x32_bf16 v[54:57], a[24:27], v[98:101], v[54:57]\n\t" \
        "ds_read_b64_tr_b16 a[60:61], v7 offset:49248\n\t" \
        "ds_read_b64_tr_b16 a[62:63], v7 offset:52320\n\t" \
        "v_mfma_f32_16x16x32_bf16 v[34:37], a[28:31], v[94:97], v[34:37]\n\t" \
        "buffer_load_dwordx4 v5, s[24:27], s57 offen offset:2048  lds\n\t" \
        "v_mfma_f32_16x16x32_bf16 v[58:61], a[28:31], v[98:101], v[58:61]\n\t" \
        "ds_read_b64_tr_b16 a[64:65], v6 offset:49280\n\t" \
        "v_cvt_pk_bf16_f32 v78, v102, v103\n\t" \
        "v_mfma_f32_16x16x32_bf16 v[38:41], a[32:35], v[94:97], v[38:41]\n\t" \
        "ds_read_b64_tr_b16 a[66:67], v6 offset:52352\n\t" \
        "v_cvt_pk_bf16_f32 v79, v104, v105\n\t" \
        "v_mfma_f32_16x16x32_bf16 v[62:65], a[32:35], v[98:101], v[62:65]\n\t" \
        "ds_read_b64_tr_b16 a[68:69], v7 offset:49312\n\t" \
        "ds_read_b64_tr_b16 a[70:71], v7 offset:52384\n\t" \
        "v_mfma_f32_16x16x32_bf16 v[42:45], a[36:39], v[94:97], v[42:45]\n\t" \
        "ds_read_b64_tr_b16 a[96:97], v142 offset:0\n\t" \
        "v_cvt_pk_bf16_f32 v80, v106, v107\n\t" \
        "v_mfma_f32_16x16x32_bf16 v[66:69], a[36:39], v[98:101], v[66:69]\n\t" \
        "ds_read_b64_tr_b16 a[98:99], v143 offset:0\n\t" \
        "ds_read_b64_tr_b16 a[100:101], v146 offset:0\n\t" \
        "v_mfma_f32_16x16x32_bf16 v[46:49], a[40:43], v[94:97], v[46:49]\n\t" \
        "ds_read_b64_tr_b16 a[102:103], v147 offset:0\n\t" \
        "v_cvt_pk_bf16_f32 v81, v108, v109\n\t" \
        "v_mfma_f32_16x16x32_bf16 v[70:73], a[40:43], v[98:101], v[70:73]\n\t" \
        "ds_read_b64_tr_b16 a[104:105], v144 offset:0\n\t" \
        "ds_read_b64_tr_b16 a[106:107], v145 offset:0\n\t" \
        "s_waitcnt lgkmcnt(12)\n\t" \
        "v_mfma_f32_16x16x32_bf16 v[50:53], a[44:47], v[94:97], v[50:53]\n\t" \
        "ds_read_b64_tr_b16 a[108:109], v148 offset:0\n\t" \
        "v_cvt_pk_bf16_f32 v82, v110, v111\n\t" \
        "v_mfma_f32_16x16x32_bf16 v[74:77], a[44:47], v[98:101], v[74:77]\n\t" \
        "ds_read_b64_tr_b16 a[110:111], v149 offset:0\n\t" \
        "ds_read_b64_tr_b16 a[72:73], v6 offset:55296\n\t" \
        "v_cvt_pk_bf16_f32 v83, v112, v113\n\t" \
        "v_cvt_pk_bf16_f32 v84, v114, v115\n\t" \
        "v_mfma_f32_16x16x32_bf16 v[102:105], a[128:131], v[154:157], 0\n\t" \
        "ds_read_b64_tr_b16 a[74:75], v6 offset:58368\n\t" \
        "v_cvt_pk_bf16_f32 v85, v116, v117\n\t" \
        "v_mfma_f32_16x16x32_bf16 v[106:109], a[132:135], v[154:157], 0\n\t" \
        "ds_read_b64_tr_b16 a[76:77], v7 offset:55328\n\t" \
        "v_cvt_pk_bf16_f32 v86, v118, v119\n\t" \
        "v_mfma_f32_16x16x32_bf16 v[110:113], a[136:139], v[158:161], 0\n\t" \
        "ds_read_b64_tr_b16 a[78:79], v7 offset:58400\n\t" \
        "ds_read_b64_tr_b16 a[80:81], v6 offset:55360\n\t" \
        "v_mfma_f32_16x16x32_bf16 v[114:117], a[140:143], v[158:161], 0\n\t" \
        "ds_read_b64_tr_b16 a[82:83], v6 offset:58432\n\t" \
        "v_cvt_pk_bf16_f32 v87, v120, v121\n\t" \
        "v_mfma_f32_16x16x32_bf16 v[30:33], a[48:51], v[78:81], v[30:33]\n\t" \
        "ds_read_b64_tr_b16 a[84:85], v7 offset:55392\n\t" \
        "ds_read_b64_tr_b16 a[86:87], v7 offset:58464\n\t" \
        "v_mfma_f32_16x16x32_bf16 v[54:57], a[48:51], v[82:85], v[54:57]\n\t" \
        "ds_read_b64_tr_b16 a[88:89], v6 offset:55424\n\t" \
        "v_and_b32 v162, v152, v25\n\t" \
        "v_mfma_f32_16x16x32_bf16 v[34:37], a[52:55], v[78:81], v[34:37]\n\t" \
        "ds_read_b128 v[170:173], v9 offset:1024\n\t" \
        "ds_read_b64_tr_b16 a[90:91], v6 offset:58496\n\t" \
        "v_mfma_f32_16x16x32_bf16 v[58:61], a[52:55], v[82:85], v[58:61]\n\t" \
        "s_waitcnt vmcnt(6)\n\t" \
        "s_barrier\n\t" \
        "ds_read_b64_tr_b16 a[92:93], v7 offset:55456\n\t" \
        "v_and_b32 v163, v152, v26\n\t" \
        "v_mfma_f32_16x16x32_bf16 v[38:41], a[56:59], v[78:81], v[38:41]\n\t" \
        "ds_read_b64_tr_b16 a[94:95], v7 offset:58528\n\t" \
        "v_and_b32 v164, v152, v27\n\t" \
        "v_and_b32 v165, v152, v28\n\t" \
        "v_mfma_f32_16x16x32_bf16 v[62:65], a[56:59], v[82:85], v[62:65]\n\t" \
        "ds_read_b64_tr_b16 a[112:113], v142 offset:64\n\t" \
        "ds_read_b64_tr_b16 a[114:115], v143 offset:64\n\t" \
        "v_and_b32 v166, v153, v25\n\t" \
        "s_waitcnt lgkmcnt(13)\n\t" \
        "v_mfma_f32_16x16x32_bf16 v[42:45], a[60:63], v[78:81], v[42:45]\n\t" \
        "s_add_u32 m0, s39, 36864\n\t" \
        "s_add_u32 s57, s40, 159744\n\t" \
        "buffer_load_dwordx4 v3, s[24:27], s57 offen offset:0  lds\n\t" \
        "v_mfma_f32_16x16x32_bf16 v[66:69], a[60:63], v[82:85], v[66:69]\n\t" \
        "ds_read_b64_tr_b16 a[116:117], v146 offset:64\n\t" \
        "v_cvt_pk_bf16_f32 v88, v122, v123\n\t" \
        "v_and_b32 v167, v153, v26\n\t" \
        "v_mfma_f32_16x16x32_bf16 v[46:49], a[64:67], v[78:81], v[46:49]\n\t" \
        "ds_read_b64_tr_b16 a[118:119], v147 offset:64\n\t" \
        "ds_read_b64_tr_b16 a[120:121], v144 offset:64\n\t" \
        "v_cvt_pk_bf16_f32 v89, v124, v125\n\t" \
        "v_cvt_pk_bf16_f32 v90, v126, v127\n\t" \
        "v_mfma_f32_16x16x32_bf16 v[70:73], a[64:67], v[82:85], v[70:73]\n\t" \
        "ds_read_b64_tr_b16 a[122:123], v145 offset:64\n\t" \
        "v_and_b32 v168, v153, v27\n\t" \
        "v_mfma_f32_16x16x32_bf16 v[50:53], a[68:71], v[78:81], v[50:53]\n\t" \
        "ds_read_b64_tr_b16 a[124:125], v148 offset:64\n\t" \
        "ds_read_b64_tr_b16 a[126:127], v149 offset:64\n\t" \
        "v_cvt_pk_bf16_f32 v91, v128, v129\n\t" \
        "v_cvt_pk_bf16_f32 v92, v130, v131\n\t" \
        "v_mfma_f32_16x16x32_bf16 v[74:77], a[68:71], v[82:85], v[74:77]\n\t" \
        "ds_read_b64_tr_b16 a[0:1], v6 offset:0\n\t" \
        "v_and_b32 v169, v153, v28\n\t" \
        "v_cvt_pk_bf16_f32 v93, v132, v133\n\t" \
        "v_mfma_f32_16x16x32_bf16 v[118:121], a[96:99], v[162:165], 0\n\t" \
        "buffer_load_dwordx4 v4, s[24:27], s57 offen offset:1024  lds\n\t" \
        "v_mfma_f32_16x16x32_bf16 v[122:125], a[100:103], v[162:165], 0\n\t" \
        "ds_read_b64_tr_b16 a[2:3], v6 offset:3072\n\t" \
        "v_cvt_pk_bf16_f32 v94, v102, v103\n\t" \
        "v_mfma_f32_16x16x32_bf16 v[126:129], a[104:107], v[166:169], 0\n\t" \
        "ds_read_b64_tr_b16 a[4:5], v7 offset:32\n\t" \
        "ds_read_b64_tr_b16 a[6:7], v7 offset:3104\n\t" \
        "v_mfma_f32_16x16x32_bf16 v[130:133], a[108:111], v[166:169], 0\n\t" \
        "ds_read_b64_tr_b16 a[8:9], v6 offset:64\n\t" \
        "v_cvt_pk_bf16_f32 v95, v104, v105\n\t" \
        "v_mfma_f32_16x16x32_bf16 v[30:33], a[72:75], v[86:89], v[30:33]\n\t" \
        "ds_read_b64_tr_b16 a[10:11], v6 offset:3136\n\t" \
        "v_cvt_pk_bf16_f32 v96, v106, v107\n\t" \
        "v_mfma_f32_16x16x32_bf16 v[54:57], a[72:75], v[90:93], v[54:57]\n\t" \
        "ds_read_b64_tr_b16 a[12:13], v7 offset:96\n\t" \
        "ds_read_b64_tr_b16 a[14:15], v7 offset:3168\n\t" \
        "s_waitcnt lgkmcnt(11)\n\t" \
        "v_mfma_f32_16x16x32_bf16 v[34:37], a[76:79], v[86:89], v[34:37]\n\t" \
        "buffer_load_dwordx4 v5, s[24:27], s57 offen offset:2048  lds\n\t" \
        "v_mfma_f32_16x16x32_bf16 v[58:61], a[76:79], v[90:93], v[58:61]\n\t" \
        "ds_read_b64_tr_b16 a[16:17], v6 offset:128\n\t" \
        "v_cvt_pk_bf16_f32 v97, v108, v109\n\t" \
        "v_mfma_f32_16x16x32_bf16 v[38:41], a[80:83], v[86:89], v[38:41]\n\t" \
        "ds_read_b64_tr_b16 a[18:19], v6 offset:3200\n\t" \
        "v_cvt_pk_bf16_f32 v98, v110, v111\n\t" \
        "v_mfma_f32_16x16x32_bf16 v[62:65], a[80:83], v[90:93], v[62:65]\n\t" \
        "ds_read_b64_tr_b16 a[20:21], v7 offset:160\n\t" \
        "ds_read_b64_tr_b16 a[22:23], v7 offset:3232\n\t" \
        "v_mfma_f32_16x16x32_bf16 v[42:45], a[84:87], v[86:89], v[42:45]\n\t" \
        "ds_read_b64_tr_b16 a[128:129], v142 offset:128\n\t" \
        "v_xor_b32 v134, v170, v11\n\t" \
        "v_mfma_f32_16x16x32_bf16 v[66:69], a[84:87], v[90:93], v[66:69]\n\t" \
        "ds_read_b64_tr_b16 a[130:131], v143 offset:128\n\t" \
        "v_cvt_pk_bf16_f32 v99, v112, v113\n\t" \
        "v_mfma_f32_16x16x32_bf16 v[46:49], a[88:91], v[86:89], v[46:49]\n\t" \
        "ds_read_b64_tr_b16 a[132:133], v146 offset:128\n\t" \
        "ds_read_b64_tr_b16 a[134:135], v147 offset:128\n\t" \
        "v_mfma_f32_16x16x32_bf16 v[70:73], a[88:91], v[90:93], v[70:73]\n\t" \
        "ds_read_b64_tr_b16 a[136:137], v144 offset:128\n\t" \
        "v_cvt_pk_bf16_f32 v100, v114, v115\n\t" \
        "v_mfma_f32_16x16x32_bf16 v[50:53], a[92:95], v[86:89], v[50:53]\n\t" \
        "ds_read_b64_tr_b16 a[138:139], v145 offset:128\n\t" \
        "v_xor_b32 v135, v171, v11\n\t" \
        "v_mfma_f32_16x16x32_bf16 v[74:77], a[92:95], v[90:93], v[74:77]\n\t" \
        "ds_read_b64_tr_b16 a[140:141], v148 offset:128\n\t" \
        "ds_read_b64_tr_b16 a[142:143], v149 offset:128\n\t" \
        "v_mfma_f32_16x16x32_bf16 v[102:105], a[112:115], v[162:165], 0\n\t" \
        "ds_read_b64_tr_b16 a[24:25], v6 offset:6144\n\t" \
        "v_cvt_pk_bf16_f32 v101, v116, v117\n\t" \
        "v_mfma_f32_16x16x32_bf16 v[106:109], a[116:119], v[162:165], 0\n\t" \
        "ds_read_b64_tr_b16 a[26:27], v6 offset:9216\n\t" \
        "v_xor_b32 v136, v172, v11\n\t" \
        "v_xor_b32 v138, v170, v12\n\t" \
        "s_waitcnt lgkmcnt(12)\n\t" \
        "v_mfma_f32_16x16x32_bf16 v[110:113], a[120:123], v[166:169], 0\n\t" \
        "ds_read_b64_tr_b16 a[28:29], v7 offset:6176\n\t" \
        "ds_read_b64 v[150:151], v10 offset:1024\n\t" \
        "v_xor_b32 v137, v173, v11\n\t" \
        "v_xor_b32 v139, v171, v12\n\t" \
        "v_mfma_f32_16x16x32_bf16 v[114:117], a[124:127], v[166:169], 0\n\t" \
        "ds_read_b64_tr_b16 a[30:31], v7 offset:9248\n\t" \
        "v_xor_b32 v140, v172, v12\n\t" \
        "v_mfma_f32_16x16x32_bf16 v[30:33], a[0:3], v[94:97], v[30:33]\n\t" \
        "ds_read_b64_tr_b16 a[32:33], v6 offset:6208\n\t" \
        "ds_read_b64_tr_b16 a[34:35], v6 offset:9280\n\t" \
        "v_xor_b32 v141, v173, v12\n\t" \
        "v_mfma_f32_16x16x32_bf16 v[54:57], a[0:3], v[98:101], v[54:57]\n\t" \
        "ds_read_b64_tr_b16 a[36:37], v7 offset:6240\n\t" \
        "v_cvt_pk_bf16_f32 v78, v118, v119\n\t" \
        "v_mfma_f32_16x16x32_bf16 v[34:37], a[4:7], v[94:97], v[34:37]\n\t" \
        "ds_read_b64_tr_b16 a[38:39], v7 offset:9312\n\t" \
        "v_cvt_pk_bf16_f32 v79, v120, v121\n\t" \
        "v_mfma_f32_16x16x32_bf16 v[58:61], a[4:7], v[98:101], v[58:61]\n\t" \
        "s_waitcnt vmcnt(6)\n\t" \
        "s_barrier\n\t" \
        "ds_read_b64_tr_b16 a[40:41], v6 offset:6272\n\t" \
        "ds_read_b64_tr_b16 a[42:43], v6 offset:9344\n\t" \
        "v_mfma_f32_16x16x32_bf16 v[38:41], a[8:11], v[94:97], v[38:41]\n\t" \
        "ds_read_b64_tr_b16 a[44:45], v7 offset:6304\n\t" \
        "v_cvt_pk_bf16_f32 v80, v122, v123\n\t" \
        "v_mfma_f32_16x16x32_bf16 v[62:65], a[8:11], v[98:101], v[62:65]\n\t" \
        "ds_read_b64_tr_b16 a[46:47], v7 offset:9376\n\t" \
        "v_cvt_pk_bf16_f32 v81, v124, v125\n\t" \
        "v_mfma_f32_16x16x32_bf16 v[42:45], a[12:15], v[94:97], v[42:45]\n\t" \
        "ds_read_b64_tr_b16 a[96:97], v134 offset:0\n\t" \
        "ds_read_b64_tr_b16 a[98:99], v135 offset:0\n\t" \
        "v_mfma_f32_16x16x32_bf16 v[66:69], a[12:15], v[98:101], v[66:69]\n\t" \
        "ds_read_b64_tr_b16 a[100:101], v138 offset:0\n\t" \
        "v_cvt_pk_bf16_f32 v82, v126, v127\n\t" \
        "v_mfma_f32_16x16x32_bf16 v[46:49], a[16:19], v[94:97], v[46:49]\n\t" \
        "ds_read_b64_tr_b16 a[102:103], v139 offset:0\n\t" \
        "ds_read_b64_tr_b16 a[104:105], v136 offset:0\n\t" \
        "v_mfma_f32_16x16x32_bf16 v[70:73], a[16:19], v[98:101], v[70:73]\n\t" \
        "ds_read_b64_tr_b16 a[106:107], v137 offset:0\n\t" \
        "v_cvt_pk_bf16_f32 v83, v128, v129\n\t" \
        "s_waitcnt lgkmcnt(12)\n\t" \
        "v_mfma_f32_16x16x32_bf16 v[50:53], a[20:23], v[94:97], v[50:53]\n\t" \
        "ds_read_b64_tr_b16 a[108:109], v140 offset:0\n\t" \
        "v_cvt_pk_bf16_f32 v84, v130, v131\n\t" \
        "v_mfma_f32_16x16x32_bf16 v[74:77], a[20:23], v[98:101], v[74:77]\n\t" \
        "ds_read_b64_tr_b16 a[110:111], v141 offset:0\n\t" \
        "ds_read_b64_tr_b16 a[48:49], v6 offset:12288\n\t" \
        "v_mfma_f32_16x16x32_bf16 v[118:121], a[128:131], v[162:165], 0\n\t" \
        "ds_read_b64_tr_b16 a[50:51], v6 offset:15360\n\t" \
        "v_cvt_pk_bf16_f32 v85, v132, v133\n\t" \
        "v_mfma_f32_16x16x32_bf16 v[122:125], a[132:135], v[162:165], 0\n\t" \
        "ds_read_b64_tr_b16 a[52:53], v7 offset:12320\n\t" \
        "ds_read_b64_tr_b16 a[54:55], v7 offset:15392\n\t" \
        "v_mfma_f32_16x16x32_bf16 v[126:129], a[136:139], v[166:169], 0\n\t" \
        "ds_read_b64_tr_b16 a[56:57], v6 offset:12352\n\t" \
        "v_cvt_pk_bf16_f32 v86, v102, v103\n\t" \
        "v_mfma_f32_16x16x32_bf16 v[130:133], a[140:143], v[166:169], 0\n\t" \
        "ds_read_b64_tr_b16 a[58:59], v6 offset:15424\n\t" \
        "ds_read_b64_tr_b16 a[60:61], v7 offset:12384\n\t" \
        "v_mfma_f32_16x16x32_bf16 v[30:33], a[24:27], v[78:81], v[30:33]\n\t" \
        "ds_read_b64_tr_b16 a[62:63], v7 offset:15456\n\t" \
        "v_and_b32 v154, v150, v25\n\t" \
        "v_mfma_f32_16x16x32_bf16 v[54:57], a[24:27], v[82:85], v[54:57]\n\t" \
        "ds_read_b64_tr_b16 a[64:65], v6 offset:12416\n\t" \
        "ds_read_b64_tr_b16 a[66:67], v6 offset:15488\n\t" \
        "v_mfma_f32_16x16x32_bf16 v[34:37], a[28:31], v[78:81], v[34:37]\n\t" \
        "ds_read_b64_tr_b16 a[68:69], v7 offset:12448\n\t" \
        "v_and_b32 v155, v150, v26\n\t" \
        "v_mfma_f32_16x16x32_bf16 v[58:61], a[28:31], v[82:85], v[58:61]\n\t" \
        "ds_read_b64_tr_b16 a[70:71], v7 offset:15520\n\t" \
        "ds_read_b64_tr_b16 a[112:113], v134 offset:64\n\t" \
        "v_mfma_f32_16x16x32_bf16 v[38:41], a[32:35], v[78:81], v[38:41]\n\t" \
        "ds_read_b64_tr_b16 a[114:115], v135 offset:64\n\t" \
        "v_and_b32 v156, v150, v27\n\t" \
        "v_mfma_f32_16x16x32_bf16 v[62:65], a[32:35], v[82:85], v[62:65]\n\t" \
        "ds_read_b64_tr_b16 a[116:117], v138 offset:64\n\t" \
        "ds_read_b64_tr_b16 a[118:119], v139 offset:64\n\t" \
        "s_waitcnt lgkmcnt(14)\n\t" \
        "v_mfma_f32_16x16x32_bf16 v[42:45], a[36:39], v[78:81], v[42:45]\n\t" \
        "ds_read_b64_tr_b16 a[120:121], v136 offset:64\n\t" \
        "v_and_b32 v157, v150, v28\n\t" \
        "v_and_b32 v158, v151, v25\n\t" \
        "v_mfma_f32_16x16x32_bf16 v[66:69], a[36:39], v[82:85], v[66:69]\n\t" \
        "ds_read_b64_tr_b16 a[122:123], v137 offset:64\n\t" \
        "ds_read_b64_tr_b16 a[124:125], v140 offset:64\n\t" \
        "v_cvt_pk_bf16_f32 v87, v104, v105\n\t" \
        "v_mfma_f32_16x16x32_bf16 v[46:49], a[40:43], v[78:81], v[46:49]\n\t" \
        "ds_read_b64_tr_b16 a[126:127], v141 offset:64\n\t" \
        "v_cvt_pk_bf16_f32 v88, v106, v107\n\t" \
        "v_and_b32 v159, v151, v26\n\t" \
        "v_mfma_f32_16x16x32_bf16 v[70:73], a[40:43], v[82:85], v[70:73]\n\t" \
        "ds_read_b64_tr_b16 a[72:73], v6 offset:18432\n\t" \
        "ds_read_b64_tr_b16 a[74:75], v6 offset:21504\n\t" \
        "v_cvt_pk_bf16_f32 v89, v108, v109\n\t" \
        "v_cvt_pk_bf16_f32 v90, v110, v111\n\t" \
        "v_mfma_f32_16x16x32_bf16 v[50:53], a[44:47], v[78:81], v[50:53]\n\t" \
        "ds_read_b64_tr_b16 a[76:77], v7 offset:18464\n\t" \
        "v_and_b32 v160, v151, v27\n\t" \
        "v_mfma_f32_16x16x32_bf16 v[74:77], a[44:47], v[82:85], v[74:77]\n\t" \
        "ds_read_b64_tr_b16 a[78:79], v7 offset:21536\n\t" \
        "ds_read_b64_tr_b16 a[80:81], v6 offset:18496\n\t" \
        "v_cvt_pk_bf16_f32 v91, v112, v113\n\t" \
        "v_cvt_pk_bf16_f32 v92, v114, v115\n\t" \
        "v_mfma_f32_16x16x32_bf16 v[102:105], a[96:99], v[154:157], 0\n\t" \
        "ds_read_b64_tr_b16 a[82:83], v6 offset:21568\n\t" \
        "v_and_b32 v161, v151, v28\n\t" \
        "v_cvt_pk_bf16_f32 v93, v116, v117\n\t" \
        "v_mfma_f32_16x16x32_bf16 v[106:109], a[100:103], v[154:157], 0\n\t" \
        "ds_read_b64_tr_b16 a[84:85], v7 offset:18528\n\t" \
        "ds_read_b64_tr_b16 a[86:87], v7 offset:21600\n\t" \
        "v_mfma_f32_16x16x32_bf16 v[110:113], a[104:107], v[158:161], 0\n\t" \
        "ds_read_b64_tr_b16 a[88:89], v6 offset:18560\n\t" \
        "v_cvt_pk_bf16_f32 v94, v118, v119\n\t" \
        "v_mfma_f32_16x16x32_bf16 v[114:117], a[108:111], v[158:161], 0\n\t" \
        "ds_read_b64_tr_b16 a[90:91], v6 offset:21632\n\t" \
        "ds_read_b64_tr_b16 a[92:93], v7 offset:18592\n\t" \
        "v_mfma_f32_16x16x32_bf16 v[30:33], a[48:51], v[86:89], v[30:33]\n\t" \
        "ds_read_b64_tr_b16 a[94:95], v7 offset:21664\n\t" \
        "v_cvt_pk_bf16_f32 v95, v120, v121\n\t" \
        "v_mfma_f32_16x16x32_bf16 v[54:57], a[48:51], v[90:93], v[54:57]\n\t" \
        "ds_read_b64_tr_b16 a[128:129], v134 offset:128\n\t" \
        "v_cvt_pk_bf16_f32 v96, v122, v123\n\t" \
        "s_waitcnt lgkmcnt(13)\n\t" \
        "v_mfma_f32_16x16x32_bf16 v[34:37], a[52:55], v[86:89], v[34:37]\n\t" \
        "ds_read_b64_tr_b16 a[130:131], v135 offset:128\n\t" \
        "ds_read_b64_tr_b16 a[132:133], v138 offset:128\n\t" \
        "v_mfma_f32_16x16x32_bf16 v[58:61], a[52:55], v[90:93], v[58:61]\n\t" \
        "s_waitcnt vmcnt(3)\n\t" \
        "s_barrier\n\t" \
        "ds_read_b64_tr_b16 a[134:135], v139 offset:128\n\t" \
        "v_cvt_pk_bf16_f32 v97, v124, v125\n\t" \
        "v_mfma_f32_16x16x32_bf16 v[38:41], a[56:59], v[86:89], v[38:41]\n\t" \
        "ds_read_b64_tr_b16 a[136:137], v136 offset:128\n\t" \
        "ds_read_b64_tr_b16 a[138:139], v137 offset:128\n\t" \
        "v_mfma_f32_16x16x32_bf16 v[62:65], a[56:59], v[90:93], v[62:65]\n\t" \
        "ds_read_b64_tr_b16 a[140:141], v140 offset:128\n\t" \
        "v_cvt_pk_bf16_f32 v98, v126, v127\n\t" \
        "v_mfma_f32_16x16x32_bf16 v[42:45], a[60:63], v[86:89], v[42:45]\n\t" \
        "ds_read_b64_tr_b16 a[142:143], v141 offset:128\n\t" \
        "ds_read_b64_tr_b16 a[0:1], v6 offset:24576\n\t" \
        "v_mfma_f32_16x16x32_bf16 v[66:69], a[60:63], v[90:93], v[66:69]\n\t" \
        "ds_read_b64_tr_b16 a[2:3], v6 offset:27648\n\t" \
        "v_cvt_pk_bf16_f32 v99, v128, v129\n\t" \
        "v_mfma_f32_16x16x32_bf16 v[46:49], a[64:67], v[86:89], v[46:49]\n\t" \
        "ds_read_b64_tr_b16 a[4:5], v7 offset:24608\n\t" \
        "ds_read_b64_tr_b16 a[6:7], v7 offset:27680\n\t" \
        "v_mfma_f32_16x16x32_bf16 v[70:73], a[64:67], v[90:93], v[70:73]\n\t" \
        "ds_read_b64_tr_b16 a[8:9], v6 offset:24640\n\t" \
        "v_cvt_pk_bf16_f32 v100, v130, v131\n\t" \
        "v_mfma_f32_16x16x32_bf16 v[50:53], a[68:71], v[86:89], v[50:53]\n\t" \
        "ds_read_b64_tr_b16 a[10:11], v6 offset:27712\n\t" \
        "ds_read_b64_tr_b16 a[12:13], v7 offset:24672\n\t" \
        "v_mfma_f32_16x16x32_bf16 v[74:77], a[68:71], v[90:93], v[74:77]\n\t" \
        "ds_read_b64_tr_b16 a[14:15], v7 offset:27744\n\t" \
        "v_cvt_pk_bf16_f32 v101, v132, v133\n\t" \
        "v_mfma_f32_16x16x32_bf16 v[118:121], a[112:115], v[154:157], 0\n\t" \
        "ds_read_b64_tr_b16 a[16:17], v6 offset:24704\n\t" \
        "ds_read_b64_tr_b16 a[18:19], v6 offset:27776\n\t" \
        "v_mfma_f32_16x16x32_bf16 v[122:125], a[116:119], v[154:157], 0\n\t" \
        "ds_read_b64_tr_b16 a[20:21], v7 offset:24736\n\t" \
        "v_cvt_pk_bf16_f32 v78, v102, v103\n\t" \
        "v_mfma_f32_16x16x32_bf16 v[126:129], a[120:123], v[158:161], 0\n\t" \
        "ds_read_b64_tr_b16 a[22:23], v7 offset:27808\n\t" \
        "ds_read_b64_tr_b16 a[24:25], v6 offset:30720\n\t" \
        "v_mfma_f32_16x16x32_bf16 v[130:133], a[124:127], v[158:161], 0\n\t" \
        "s_waitcnt vmcnt(0) lgkmcnt(15)\n\t" \
        "s_barrier\n\t" \
        "ds_read_b64_tr_b16 a[26:27], v6 offset:33792\n\t" \
        "v_cvt_pk_bf16_f32 v79, v104, v105\n\t" \
        "v_mfma_f32_16x16x32_bf16 v[30:33], a[72:75], v[94:97], v[30:33]\n\t" \
        "ds_read_b64_tr_b16 a[28:29], v7 offset:30752\n\t" \
        "ds_read_b64_tr_b16 a[30:31], v7 offset:33824\n\t" \
        "v_mfma_f32_16x16x32_bf16 v[54:57], a[72:75], v[98:101], v[54:57]\n\t" \
        "ds_read_b64_tr_b16 a[32:33], v6 offset:30784\n\t" \
        "v_cvt_pk_bf16_f32 v80, v106, v107\n\t" \
        "v_mfma_f32_16x16x32_bf16 v[34:37], a[76:79], v[94:97], v[34:37]\n\t" \
        "ds_read_b64_tr_b16 a[34:35], v6 offset:33856\n\t" \
        "ds_read_b64_tr_b16 a[36:37], v7 offset:30816\n\t" \
        "v_mfma_f32_16x16x32_bf16 v[58:61], a[76:79], v[98:101], v[58:61]\n\t" \
        "ds_read_b64_tr_b16 a[38:39], v7 offset:33888\n\t" \
        "v_cvt_pk_bf16_f32 v81, v108, v109\n\t" \
        "v_mfma_f32_16x16x32_bf16 v[38:41], a[80:83], v[94:97], v[38:41]\n\t" \
        "ds_read_b64_tr_b16 a[40:41], v6 offset:30848\n\t" \
        "ds_read_b64_tr_b16 a[42:43], v6 offset:33920\n\t" \
        "v_mfma_f32_16x16x32_bf16 v[62:65], a[80:83], v[98:101], v[62:65]\n\t" \
        "ds_read_b64_tr_b16 a[44:45], v7 offset:30880\n\t" \
        "v_cvt_pk_bf16_f32 v82, v110, v111\n\t" \
        "v_mfma_f32_16x16x32_bf16 v[42:45], a[84:87], v[94:97], v[42:45]\n\t" \
        "ds_read_b64_tr_b16 a[46:47], v7 offset:33952\n\t" \
        "ds_read_b64_tr_b16 a[48:49], v6 offset:36864\n\t" \
        "v_mfma_f32_16x16x32_bf16 v[66:69], a[84:87], v[98:101], v[66:69]\n\t" \
        "ds_read_b64_tr_b16 a[50:51], v6 offset:39936\n\t" \
        "v_cvt_pk_bf16_f32 v83, v112, v113\n\t" \
        "v_mfma_f32_16x16x32_bf16 v[46:49], a[88:91], v[94:97], v[46:49]\n\t" \
        "ds_read_b64_tr_b16 a[52:53], v7 offset:36896\n\t" \
        "ds_read_b64_tr_b16 a[54:55], v7 offset:39968\n\t" \
        "v_mfma_f32_16x16x32_bf16 v[70:73], a[88:91], v[98:101], v[70:73]\n\t" \
        "ds_read_b64_tr_b16 a[56:57], v6 offset:36928\n\t" \
        "v_cvt_pk_bf16_f32 v84, v114, v115\n\t" \
        "v_mfma_f32_16x16x32_bf16 v[50:53], a[92:95], v[94:97], v[50:53]\n\t" \
        "ds_read_b64_tr_b16 a[58:59], v6 offset:40000\n\t" \
        "v_cvt_pk_bf16_f32 v85, v116, v117\n\t" \
        "v_mfma_f32_16x16x32_bf16 v[74:77], a[92:95], v[98:101], v[74:77]\n\t" \
        "ds_read_b64_tr_b16 a[60:61], v7 offset:36960\n\t" \
        "ds_read_b64_tr_b16 a[62:63], v7 offset:40032\n\t" \
        "v_mfma_f32_16x16x32_bf16 v[102:105], a[128:131], v[154:157], 0\n\t" \
        "ds_read_b64_tr_b16 a[64:65], v6 offset:36992\n\t" \
        "ds_read_b64_tr_b16 a[66:67], v6 offset:40064\n\t" \
        "v_mfma_f32_16x16x32_bf16 v[106:109], a[132:135], v[154:157], 0\n\t" \
        "ds_read_b64_tr_b16 a[68:69], v7 offset:37024\n\t" \
        "v_cvt_pk_bf16_f32 v86, v118, v119\n\t" \
        "v_mfma_f32_16x16x32_bf16 v[110:113], a[136:139], v[158:161], 0\n\t" \
        "ds_read_b64_tr_b16 a[70:71], v7 offset:40096\n\t" \
        "v_cvt_pk_bf16_f32 v88, v122, v123\n\t" \
        "s_waitcnt lgkmcnt(13)\n\t" \
        "v_mfma_f32_16x16x32_bf16 v[114:117], a[140:143], v[158:161], 0\n\t" \
        "v_cvt_pk_bf16_f32 v87, v120, v121\n\t" \
        "v_cvt_pk_bf16_f32 v89, v124, v125\n\t" \
        "v_mfma_f32_16x16x32_bf16 v[30:33], a[0:3], v[78:81], v[30:33]\n\t" \
        "v_cvt_pk_bf16_f32 v94, v102, v103\n\t" \
        "v_cvt_pk_bf16_f32 v95, v104, v105\n\t" \
        "v_mfma_f32_16x16x32_bf16 v[34:37], a[4:7], v[78:81], v[34:37]\n\t" \
        "v_cvt_pk_bf16_f32 v96, v106, v107\n\t" \
        "v_cvt_pk_bf16_f32 v97, v108, v109\n\t" \
        "v_mfma_f32_16x16x32_bf16 v[38:41], a[8:11], v[78:81], v[38:41]\n\t" \
        "v_cvt_pk_bf16_f32 v90, v126, v127\n\t" \
        "v_cvt_pk_bf16_f32 v92, v130, v131\n\t" \
        "v_mfma_f32_16x16x32_bf16 v[42:45], a[12:15], v[78:81], v[42:45]\n\t" \
        "v_cvt_pk_bf16_f32 v91, v128, v129\n\t" \
        "v_cvt_pk_bf16_f32 v93, v132, v133\n\t" \
        "v_mfma_f32_16x16x32_bf16 v[46:49], a[16:19], v[78:81], v[46:49]\n\t" \
        "v_cvt_pk_bf16_f32 v98, v110, v111\n\t" \
        "v_cvt_pk_bf16_f32 v100, v114, v115\n\t" \
        "v_mfma_f32_16x16x32_bf16 v[50:53], a[20:23], v[78:81], v[50:53]\n\t" \
        "v_cvt_pk_bf16_f32 v99, v112, v113\n\t" \
        "v_cvt_pk_bf16_f32 v101, v116, v117\n\t" \
        "v_mfma_f32_16x16x32_bf16 v[30:33], a[24:27], v[86:89], v[30:33]\n\t" \
        "v_mfma_f32_16x16x32_bf16 v[34:37], a[28:31], v[86:89], v[34:37]\n\t" \
        "v_mfma_f32_16x16x32_bf16 v[38:41], a[32:35], v[86:89], v[38:41]\n\t" \
        "v_mfma_f32_16x16x32_bf16 v[42:45], a[36:39], v[86:89], v[42:45]\n\t" \
        "v_mfma_f32_16x16x32_bf16 v[46:49], a[40:43], v[86:89], v[46:49]\n\t" \
        "s_waitcnt lgkmcnt(0)\n\t" \
        "v_mfma_f32_16x16x32_bf16 v[50:53], a[44:47], v[86:89], v[50:53]\n\t" \
        "v_mfma_f32_16x16x32_bf16 v[30:33], a[48:51], v[94:97], v[30:33]\n\t" \
        "v_mfma_f32_16x16x32_bf16 v[34:37], a[52:55], v[94:97], v[34:37]\n\t" \
        "v_mfma_f32_16x16x32_bf16 v[38:41], a[56:59], v[94:97], v[38:41]\n\t" \
        "v_mfma_f32_16x16x32_bf16 v[42:45], a[60:63], v[94:97], v[42:45]\n\t" \
        "v_mfma_f32_16x16x32_bf16 v[46:49], a[64:67], v[94:97], v[46:49]\n\t" \
        "v_mfma_f32_16x16x32_bf16 v[50:53], a[68:71], v[94:97], v[50:53]\n\t" \
        "s_nop 4\n\t" \
        "v_cvt_pk_bf16_f32 v30, v30, v31\n\t" \
        "v_mfma_f32_16x16x32_bf16 v[54:57], a[0:3], v[82:85], v[54:57]\n\t" \
        "v_cvt_pk_bf16_f32 v31, v32, v33\n\t" \
        "v_cvt_pk_bf16_f32 v32, v34, v35\n\t" \
        "v_mfma_f32_16x16x32_bf16 v[58:61], a[4:7], v[82:85], v[58:61]\n\t" \
        "v_cvt_pk_bf16_f32 v33, v36, v37\n\t" \
        "v_mfma_f32_16x16x32_bf16 v[62:65], a[8:11], v[82:85], v[62:65]\n\t" \
        "v_permlane16_swap_b32 v30, v32\n\t" \
        "v_mfma_f32_16x16x32_bf16 v[66:69], a[12:15], v[82:85], v[66:69]\n\t" \
        "v_permlane16_swap_b32 v31, v33\n\t" \
        "v_mfma_f32_16x16x32_bf16 v[70:73], a[16:19], v[82:85], v[70:73]\n\t" \
        "v_mfma_f32_16x16x32_bf16 v[74:77], a[20:23], v[82:85], v[74:77]\n\t" \
        "buffer_store_dwordx4 v[30:33], v8, s[28:31], s59 offen offset:0 sc1\n\t" \
        "v_mfma_f32_16x16x32_bf16 v[54:57], a[24:27], v[90:93], v[54:57]\n\t" \
        "v_cvt_pk_bf16_f32 v38, v38, v39\n\t" \
        "v_mfma_f32_16x16x32_bf16 v[58:61], a[28:31], v[90:93], v[58:61]\n\t" \
        "v_cvt_pk_bf16_f32 v39, v40, v41\n\t" \
        "v_cvt_pk_bf16_f32 v40, v42, v43\n\t" \
        "v_mfma_f32_16x16x32_bf16 v[62:65], a[32:35], v[90:93], v[62:65]\n\t" \
        "v_cvt_pk_bf16_f32 v41, v44, v45\n\t" \
        "v_mfma_f32_16x16x32_bf16 v[66:69], a[36:39], v[90:93], v[66:69]\n\t" \
        "v_permlane16_swap_b32 v38, v40\n\t" \
        "v_mfma_f32_16x16x32_bf16 v[70:73], a[40:43], v[90:93], v[70:73]\n\t" \
        "v_permlane16_swap_b32 v39, v41\n\t" \
        "v_mfma_f32_16x16x32_bf16 v[74:77], a[44:47], v[90:93], v[74:77]\n\t" \
        "v_mfma_f32_16x16x32_bf16 v[54:57], a[48:51], v[98:101], v[54:57]\n\t" \
        "buffer_store_dwordx4 v[38:41], v8, s[28:31], s59 offen offset:64 sc1\n\t" \
        "v_mfma_f32_16x16x32_bf16 v[58:61], a[52:55], v[98:101], v[58:61]\n\t" \
        "v_cvt_pk_bf16_f32 v46, v46, v47\n\t" \
        "v_mfma_f32_16x16x32_bf16 v[62:65], a[56:59], v[98:101], v[62:65]\n\t" \
        "v_cvt_pk_bf16_f32 v47, v48, v49\n\t" \
        "v_cvt_pk_bf16_f32 v48, v50, v51\n\t" \
        "v_mfma_f32_16x16x32_bf16 v[66:69], a[60:63], v[98:101], v[66:69]\n\t" \
        "v_cvt_pk_bf16_f32 v49, v52, v53\n\t" \
        "v_mfma_f32_16x16x32_bf16 v[70:73], a[64:67], v[98:101], v[70:73]\n\t" \
        "v_permlane16_swap_b32 v46, v48\n\t" \
        "v_mfma_f32_16x16x32_bf16 v[74:77], a[68:71], v[98:101], v[74:77]\n\t" \
        "v_permlane16_swap_b32 v47, v49\n\t" \
        "buffer_store_dwordx4 v[46:49], v8, s[28:31], s59 offen offset:128 sc1\n\t" \
        "v_cvt_pk_bf16_f32 v54, v54, v55\n\t" \
        "v_cvt_pk_bf16_f32 v55, v56, v57\n\t" \
        "v_cvt_pk_bf16_f32 v56, v58, v59\n\t" \
        "v_cvt_pk_bf16_f32 v57, v60, v61\n\t" \
        "s_nop 0\n\t" \
        "v_permlane16_swap_b32 v54, v56\n\t" \
        "v_permlane16_swap_b32 v55, v57\n\t" \
        "buffer_store_dwordx4 v[54:57], v8, s[28:31], s60 offen offset:0 sc1\n\t" \
        "v_cvt_pk_bf16_f32 v62, v62, v63\n\t" \
        "v_cvt_pk_bf16_f32 v63, v64, v65\n\t" \
        "v_cvt_pk_bf16_f32 v64, v66, v67\n\t" \
        "v_cvt_pk_bf16_f32 v65, v68, v69\n\t" \
        "s_nop 0\n\t" \
        "v_permlane16_swap_b32 v62, v64\n\t" \
        "v_permlane16_swap_b32 v63, v65\n\t" \
        "buffer_store_dwordx4 v[62:65], v8, s[28:31], s60 offen offset:64 sc1\n\t" \
        "v_cvt_pk_bf16_f32 v70, v70, v71\n\t" \
        "v_cvt_pk_bf16_f32 v71, v72, v73\n\t" \
        "v_cvt_pk_bf16_f32 v72, v74, v75\n\t" \
        "v_cvt_pk_bf16_f32 v73, v76, v77\n\t" \
        "s_nop 0\n\t" \
        "v_permlane16_swap_b32 v70, v72\n\t" \
        "v_permlane16_swap_b32 v71, v73\n\t" \
        "buffer_store_dwordx4 v[70:73], v8, s[28:31], s60 offen offset:128 sc1\n\t" \
        "s_endpgm\n\t" \
        ".LH4_start%=:\n\t" \
        "s_mov_b32 s62, 0\n\t" \
        "s_mov_b32 s63, -1\n\t" \
        "v_cmp_lt_u32 vcc, 47, v2\n\t" \
        "v_lshlrev_b32 v162, 4, v2\n\t" \
        "v_xor_b32 v163, 32, v162\n\t" \
        "v_cndmask_b32 v3, v162, v163, vcc\n\t" \
        "v_cndmask_b32 v4, v163, v162, s[62:63]\n\t" \
        "v_cmp_lt_u32 vcc, 15, v2\n\t" \
        "s_nop 1\n\t" \
        "v_cndmask_b32 v5, v162, v163, vcc\n\t" \
        "s_mul_i32 s40, s35, 0xc00\n\t" \
        "s_add_u32 s39, s40, 0xba40\n\t" \
        "s_mov_b32 s42, 0xffff0000\n\t" \
        "s_mov_b32 s43, 0xc0\n\t" \
        "s_mov_b32 s44, 0xf80\n\t" \
        "s_mov_b32 s45, 0x435f435f\n\t" \
        "v_add_u32 v162, s32, v18\n\t" \
        "v_add_u32 v163, s33, v19\n\t" \
        "v_add_u32 v162, -1, v162\n\t" \
        "v_add_u32 v163, -1, v163\n\t" \
        "v_cndmask_b32 v161, 0, 1.0, s[62:63]\n\t" \
        "v_cvt_f32_i32 v55, v162\n\t" \
        "v_cvt_f32_i32 v62, v163\n\t" \
        "v_add_f32 v56, v55, v161\n\t" \
        "v_add_f32 v59, v62, v161\n\t" \
        "v_add_f32 v57, 1.0, v55\n\t" \
        "v_add_f32 v62, 2.0, v62\n\t" \
        "v_add_f32 v58, 2.0, v55\n\t" \
        "v_add_f32 v61, 1.0, v59\n\t" \
        "v_fma_f32 v60, v161, -2.0, v62\n\t" \
        "v_bfe_u32 v160, v2, 4, 1\n\t" \
        "v_lshrrev_b32 v162, 2, v19\n\t" \
        "v_and_b32 v163, 1, v19\n\t" \
        "v_lshlrev_b32 v162, 5, v162\n\t" \
        "v_lshl_add_u32 v162, v163, 4, v162\n\t" \
        "v_and_b32 v163, 2, v19\n\t" \
        "v_lshl_add_u32 v162, v160, 3, v162\n\t" \
        "v_lshlrev_b32 v161, 2, v2\n\t" \
        "v_lshl_add_u32 v162, v163, 1, v162\n\t" \
        "v_and_b32 v161, 0x80, v161\n\t" \
        "s_mul_i32 s55, s35, 2560\n\t" \
        "s_add_u32 s55, s55, 109120\n\t" \
        "v_add3_u32 v63, v162, v161, s55\n\t" \
        "s_mul_i32 s56, s35, 1280\n\t" \
        "s_add_u32 s56, s56, 119360\n\t" \
        "v_lshl_add_u32 v10, v19, 3, s56\n\t" \
        "v_lshl_add_u32 v162, v160, 2, v161\n\t" \
        "v_add_u32 v64, v10, v162\n\t" \
        "s_mul_i32 s55, s32, 0xf80\n\t" \
        "s_mul_i32 s56, s33, 0xc0\n\t" \
        "s_add_u32 s55, s55, s56\n\t" \
        "s_sub_u32 s55, 0x1f40, s55\n\t" \
        "v_mov_b32 v65, s55\n\t" \
        "s_mov_b32 s64, 0xffff0000\n\t" \
        "s_mov_b32 s65, s64\n\t" \
        "s_sub_i32 s55, s32, 2\n\t" \
        "s_sub_i32 s56, s33, 2\n\t" \
        "s_max_i32 s55, s55, 0\n\t" \
        "s_max_i32 s56, s56, 0\n\t" \
        "v_cvt_f32_u32 v162, s55\n\t" \
        "v_cvt_f32_u32 v163, s56\n\t" \
        "s_add_u32 s55, s32, 5\n\t" \
        "s_add_u32 s56, s33, 17\n\t" \
        "v_cvt_pk_bf16_f32 v14, v162, v163\n\t" \
        "v_cvt_f32_u32 v162, s55\n\t" \
        "v_cvt_f32_u32 v163, s56\n\t" \
        "v_cvt_pk_bf16_f32 v15, v162, v163\n\t" \
        "s_waitcnt vmcnt(2)\n\t" \
        "s_mov_b32 s57, s36\n\t" \
        "s_add_u32 m0, s38, 0x1f00\n\t" \
        "s_nop 0\n\t" \
        "buffer_load_dwordx4 v13, s[16:19], s57 offen nt lds\n\t" \
        "v_cndmask_b32 v140, v40, v41, s[62:63]\n\t" \
        "v_lshlrev_b32 v141, 16, v140\n\t" \
        "v_and_b32 v142, s42, v140\n\t" \
        "v_add_f32 v141, v55, v141\n\t" \
        "v_add_f32 v142, v59, v142\n\t" \
        "v_cvt_pk_bf16_f32 v50, v141, v142\n\t" \
        "v_pk_max_i16 v50, v50, 0\n\t" \
        "v_pk_min_i16 v50, v50, s45\n\t" \
        "v_lshlrev_b32 v141, 16, v50\n\t" \
        "v_and_b32 v142, s42, v50\n\t" \
        "v_pk_min_i16 v66, v50, v50\n\t" \
        "v_fract_f32 v143, v141\n\t" \
        "v_fract_f32 v144, v142\n\t" \
        "v_cvt_u32_f32 v145, v141\n\t" \
        "v_cvt_u32_f32 v146, v142\n\t" \
        "s_add_u32 s57, s36, 0xa800\n\t" \
        "s_add_u32 m0, s38, 0x2e80\n\t" \
        "s_nop 0\n\t" \
        "buffer_load_dwordx4 v13, s[16:19], s57 offen nt lds\n\t" \
        "v_cmp_neq_f32 s[10:11], 0, v143\n\t" \
        "v_cmp_neq_f32 vcc, 0, v144\n\t" \
        "v_add_u32 v146, 2, v146\n\t" \
        "v_pk_max_i16 v67, v50, v50\n\t" \
        "s_and_b64 s[10:11], s[10:11], s[64:65]\n\t" \
        "v_mad_u32_u24 v148, v146, s43, v65\n\t" \
        "v_bfe_u32 v147, v146, 2, 1\n\t" \
        "v_sub_f32 v141, 1.0, v143\n\t" \
        "v_addc_co_u32 v145, s[4:5], 0, v145, s[10:11]\n\t" \
        "v_sub_f32 v142, 1.0, v144\n\t" \
        "v_add_u32 v146, 1, v146\n\t" \
        "v_mad_u32_u24 v148, v145, s44, v148\n\t" \
        "v_cvt_pk_bf16_f32 v152, v141, v142\n\t" \
        "v_lshl_add_u32 v149, v147, 5, v148\n\t" \
        "v_bfe_u32 v147, v146, 2, 1\n\t" \
        "s_add_u32 s57, s36, 0x15000\n\t" \
        "s_add_u32 m0, s38, 0x3e00\n\t" \
        "s_nop 0\n\t" \
        "buffer_load_dwordx4 v13, s[16:19], s57 offen nt lds\n\t" \
        "v_add_u32 v148, s43, v148\n\t" \
        "v_lshlrev_b32 v141, 16, v152\n\t" \
        "v_lshl_add_u32 v148, v147, 5, v148\n\t" \
        "v_and_b32 v152, s42, v152\n\t" \
        "v_cndmask_b32 v141, v141, v143, s[64:65]\n\t" \
        "v_cndmask_b32 v150, v149, v148, vcc\n\t" \
        "v_mul_f32 v152, v141, v152\n\t" \
        "v_mul_f32 v144, v141, v144\n\t" \
        "ds_write2st64_b32 v63, v149, v150 offset0:0 offset1:5\n\t" \
        "v_cvt_pk_bf16_f32 v151, v152, v144\n\t" \
        "ds_write_b32 v64, v151 offset:0\n\t" \
        "v_cndmask_b32 v140, v42, v43, s[62:63]\n\t" \
        "v_lshlrev_b32 v141, 16, v140\n\t" \
        "v_and_b32 v142, s42, v140\n\t" \
        "v_add_f32 v141, v56, v141\n\t" \
        "s_add_u32 s57, s36, 0x1f800\n\t" \
        "s_add_u32 m0, s38, 0x4d80\n\t" \
        "s_nop 0\n\t" \
        "buffer_load_dwordx4 v13, s[16:19], s57 offen nt lds\n\t" \
        "v_add_f32 v142, v60, v142\n\t" \
        "v_cvt_pk_bf16_f32 v51, v141, v142\n\t" \
        "v_pk_max_i16 v51, v51, 0\n\t" \
        "v_pk_min_i16 v51, v51, s45\n\t" \
        "v_lshlrev_b32 v141, 16, v51\n\t" \
        "v_and_b32 v142, s42, v51\n\t" \
        "v_pk_min_i16 v66, v66, v51\n\t" \
        "v_fract_f32 v143, v141\n\t" \
        "v_fract_f32 v144, v142\n\t" \
        "v_cvt_u32_f32 v145, v141\n\t" \
        "v_cvt_u32_f32 v146, v142\n\t" \
        "v_cmp_neq_f32 s[10:11], 0, v143\n\t" \
        "v_cmp_neq_f32 vcc, 0, v144\n\t" \
        "v_add_u32 v146, 2, v146\n\t" \
        "v_pk_max_i16 v67, v67, v51\n\t" \
        "s_add_u32 s57, s36, 0x2a000\n\t" \
        "s_min_u32 s57, s57, 0x925800\n\t" \
        "s_add_u32 m0, s38, 0x5d00\n\t" \
        "s_nop 0\n\t" \
        "buffer_load_dwordx4 v13, s[16:19], s57 offen nt lds\n\t" \
        "s_and_b64 s[10:11], s[10:11], s[64:65]\n\t" \
        "v_mad_u32_u24 v148, v146, s43, v65\n\t" \
        "v_bfe_u32 v147, v146, 2, 1\n\t" \
        "v_sub_f32 v141, 1.0, v143\n\t" \
        "v_addc_co_u32 v145, s[4:5], 0, v145, s[10:11]\n\t" \
        "v_sub_f32 v142, 1.0, v144\n\t" \
        "v_add_u32 v146, 1, v146\n\t" \
        "v_mad_u32_u24 v148, v145, s44, v148\n\t" \
        "v_cvt_pk_bf16_f32 v152, v141, v142\n\t" \
        "v_lshl_add_u32 v149, v147, 5, v148\n\t" \
        "v_bfe_u32 v147, v146, 2, 1\n\t" \
        "v_add_u32 v148, s43, v148\n\t" \
        "v_lshlrev_b32 v141, 16, v152\n\t" \
        "v_lshl_add_u32 v148, v147, 5, v148\n\t" \
        "v_and_b32 v152, s42, v152\n\t" \
        "s_add_u32 s57, s36, 0x34800\n\t" \
        "s_min_u32 s57, s57, 0x925800\n\t" \
        "s_add_u32 m0, s38, 0x6c80\n\t" \
        "s_nop 0\n\t" \
        "buffer_load_dwordx4 v13, s[16:19], s57 offen nt lds\n\t" \
        "v_cndmask_b32 v141, v141, v143, s[64:65]\n\t" \
        "v_cndmask_b32 v150, v149, v148, vcc\n\t" \
        "v_mul_f32 v152, v141, v152\n\t" \
        "v_mul_f32 v144, v141, v144\n\t" \
        "ds_write2st64_b32 v63, v149, v150 offset0:1 offset1:6\n\t" \
        "v_cvt_pk_bf16_f32 v151, v152, v144\n\t" \
        "ds_write_b32 v64, v151 offset:256\n\t" \
        "v_cndmask_b32 v140, v44, v45, s[62:63]\n\t" \
        "v_lshlrev_b32 v141, 16, v140\n\t" \
        "v_and_b32 v142, s42, v140\n\t" \
        "v_add_f32 v141, v57, v141\n\t" \
        "v_add_f32 v142, v61, v142\n\t" \
        "v_cvt_pk_bf16_f32 v52, v141, v142\n\t" \
        "v_pk_max_i16 v52, v52, 0\n\t" \
        "v_pk_min_i16 v52, v52, s45\n\t" \
        "s_mov_b32 s57, s40\n\t" \
        "s_mov_b32 m0, s39\n\t" \
        "s_nop 0\n\t" \
        "buffer_load_dwordx4 v3, s[24:27], s57 offen offset:0  lds\n\t" \
        "v_lshlrev_b32 v141, 16, v52\n\t" \
        "v_and_b32 v142, s42, v52\n\t" \
        "v_pk_min_i16 v66, v66, v52\n\t" \
        "v_fract_f32 v143, v141\n\t" \
        "v_fract_f32 v144, v142\n\t" \
        "v_cvt_u32_f32 v145, v141\n\t" \
        "v_cvt_u32_f32 v146, v142\n\t" \
        "v_cmp_neq_f32 s[10:11], 0, v143\n\t" \
        "v_cmp_neq_f32 vcc, 0, v144\n\t" \
        "v_add_u32 v146, 2, v146\n\t" \
        "v_pk_max_i16 v67, v67, v52\n\t" \
        "s_and_b64 s[10:11], s[10:11], s[64:65]\n\t" \
        "v_mad_u32_u24 v148, v146, s43, v65\n\t" \
        "v_bfe_u32 v147, v146, 2, 1\n\t" \
        "v_sub_f32 v141, 1.0, v143\n\t" \
        "buffer_load_dwordx4 v4, s[24:27], s57 offen offset:1024  lds\n\t" \
        "v_addc_co_u32 v145, s[4:5], 0, v145, s[10:11]\n\t" \
        "v_sub_f32 v142, 1.0, v144\n\t" \
        "v_add_u32 v146, 1, v146\n\t" \
        "v_mad_u32_u24 v148, v145, s44, v148\n\t" \
        "v_cvt_pk_bf16_f32 v152, v141, v142\n\t" \
        "v_lshl_add_u32 v149, v147, 5, v148\n\t" \
        "v_bfe_u32 v147, v146, 2, 1\n\t" \
        "v_add_u32 v148, s43, v148\n\t" \
        "v_lshlrev_b32 v141, 16, v152\n\t" \
        "v_lshl_add_u32 v148, v147, 5, v148\n\t" \
        "v_and_b32 v152, s42, v152\n\t" \
        "v_cndmask_b32 v141, v141, v143, s[64:65]\n\t" \
        "v_cndmask_b32 v150, v149, v148, vcc\n\t" \
        "v_mul_f32 v152, v141, v152\n\t" \
        "v_mul_f32 v144, v141, v144\n\t" \
        "buffer_load_dwordx4 v5, s[24:27], s57 offen offset:2048  lds\n\t" \
        "ds_write2st64_b32 v63, v149, v150 offset0:2 offset1:7\n\t" \
        "v_cvt_pk_bf16_f32 v151, v152, v144\n\t" \
        "ds_write_b32 v64, v151 offset:512\n\t" \
        "v_cndmask_b32 v140, v46, v47, s[62:63]\n\t" \
        "v_lshlrev_b32 v141, 16, v140\n\t" \
        "v_and_b32 v142, s42, v140\n\t" \
        "v_add_f32 v141, v58, v141\n\t" \
        "v_add_f32 v142, v59, v142\n\t" \
        "v_cvt_pk_bf16_f32 v53, v141, v142\n\t" \
        "v_pk_max_i16 v53, v53, 0\n\t" \
        "v_pk_min_i16 v53, v53, s45\n\t" \
        "v_lshlrev_b32 v141, 16, v53\n\t" \
        "v_and_b32 v142, s42, v53\n\t" \
        "v_pk_min_i16 v66, v66, v53\n\t" \
        "v_fract_f32 v143, v141\n\t" \
        "s_add_u32 s57, s40, 0x3000\n\t" \
        "s_add_u32 m0, s39, 0x3000\n\t" \
        "s_nop 0\n\t" \
        "buffer_load_dwordx4 v3, s[24:27], s57 offen offset:0  lds\n\t" \
        "v_fract_f32 v144, v142\n\t" \
        "v_cvt_u32_f32 v145, v141\n\t" \
        "v_cvt_u32_f32 v146, v142\n\t" \
        "v_cmp_neq_f32 s[10:11], 0, v143\n\t" \
        "v_cmp_neq_f32 vcc, 0, v144\n\t" \
        "v_add_u32 v146, 2, v146\n\t" \
        "v_pk_max_i16 v67, v67, v53\n\t" \
        "s_and_b64 s[10:11], s[10:11], s[64:65]\n\t" \
        "v_mad_u32_u24 v148, v146, s43, v65\n\t" \
        "v_bfe_u32 v147, v146, 2, 1\n\t" \
        "v_sub_f32 v141, 1.0, v143\n\t" \
        "v_addc_co_u32 v145, s[4:5], 0, v145, s[10:11]\n\t" \
        "v_sub_f32 v142, 1.0, v144\n\t" \
        "v_add_u32 v146, 1, v146\n\t" \
        "v_mad_u32_u24 v148, v145, s44, v148\n\t" \
        "buffer_load_dwordx4 v4, s[24:27], s57 offen offset:1024  lds\n\t" \
        "v_cvt_pk_bf16_f32 v152, v141, v142\n\t" \
        "v_lshl_add_u32 v149, v147, 5, v148\n\t" \
        "v_bfe_u32 v147, v146, 2, 1\n\t" \
        "v_add_u32 v148, s43, v148\n\t" \
        "v_lshlrev_b32 v141, 16, v152\n\t" \
        "v_lshl_add_u32 v148, v147, 5, v148\n\t" \
        "v_and_b32 v152, s42, v152\n\t" \
        "v_cndmask_b32 v141, v141, v143, s[64:65]\n\t" \
        "v_cndmask_b32 v150, v149, v148, vcc\n\t" \
        "v_mul_f32 v152, v141, v152\n\t" \
        "v_mul_f32 v144, v141, v144\n\t" \
        "ds_write2st64_b32 v63, v149, v150 offset0:3 offset1:8\n\t" \
        "v_cvt_pk_bf16_f32 v151, v152, v144\n\t" \
        "ds_write_b32 v64, v151 offset:768\n\t" \
        "v_lshlrev_b32 v141, 16, v48\n\t" \
        "buffer_load_dwordx4 v5, s[24:27], s57 offen offset:2048  lds\n\t" \
        "v_and_b32 v142, s42, v48\n\t" \
        "v_add_f32 v141, v58, v141\n\t" \
        "v_add_f32 v142, v62, v142\n\t" \
        "v_cvt_pk_bf16_f32 v54, v141, v142\n\t" \
        "v_pk_max_i16 v54, v54, 0\n\t" \
        "v_pk_min_i16 v54, v54, s45\n\t" \
        "v_lshlrev_b32 v141, 16, v54\n\t" \
        "v_and_b32 v142, s42, v54\n\t" \
        "v_pk_min_i16 v66, v66, v54\n\t" \
        "v_fract_f32 v143, v141\n\t" \
        "v_fract_f32 v144, v142\n\t" \
        "v_cvt_u32_f32 v145, v141\n\t" \
        "v_cvt_u32_f32 v146, v142\n\t" \
        "v_cmp_neq_f32 s[10:11], 0, v143\n\t" \
        "v_cmp_neq_f32 vcc, 0, v144\n\t" \
        "s_add_u32 s57, s40, 0x6000\n\t" \
        "s_add_u32 m0, s39, 0x6000\n\t" \
        "s_nop 0\n\t" \
        "buffer_load_dwordx4 v3, s[24:27], s57 offen offset:0  lds\n\t" \
        "v_add_u32 v146, 2, v146\n\t" \
        "v_pk_max_i16 v67, v67, v54\n\t" \
        "s_and_b64 s[10:11], s[10:11], s[64:65]\n\t" \
        "v_mad_u32_u24 v148, v146, s43, v65\n\t" \
        "v_bfe_u32 v147, v146, 2, 1\n\t" \
        "v_sub_f32 v141, 1.0, v143\n\t" \
        "v_addc_co_u32 v145, s[4:5], 0, v145, s[10:11]\n\t" \
        "v_sub_f32 v142, 1.0, v144\n\t" \
        "v_add_u32 v146, 1, v146\n\t" \
        "v_mad_u32_u24 v148, v145, s44, v148\n\t" \
        "v_cvt_pk_bf16_f32 v152, v141, v142\n\t" \
        "v_lshl_add_u32 v149, v147, 5, v148\n\t" \
        "v_bfe_u32 v147, v146, 2, 1\n\t" \
        "v_add_u32 v148, s43, v148\n\t" \
        "v_lshlrev_b32 v141, 16, v152\n\t" \
        "buffer_load_dwordx4 v4, s[24:27], s57 offen offset:1024  lds\n\t" \
        "v_lshl_add_u32 v148, v147, 5, v148\n\t" \
        "v_and_b32 v152, s42, v152\n\t" \
        "v_cndmask_b32 v141, v141, v143, s[64:65]\n\t" \
        "v_cndmask_b32 v150, v149, v148, vcc\n\t" \
        "v_mul_f32 v152, v141, v152\n\t" \
        "v_mul_f32 v144, v141, v144\n\t" \
        "ds_write2st64_b32 v63, v149, v150 offset0:4 offset1:9\n\t" \
        "v_cvt_pk_bf16_f32 v151, v152, v144\n\t" \
        "ds_write_b32 v64, v151 offset:1024\n\t" \
        "v_pk_max_i16 v140, v66, v14\n\t" \
        "v_pk_min_i16 v141, v67, v15\n\t" \
        "v_cmp_ne_u32 s[10:11], v140, v66\n\t" \
        "v_cmp_ne_u32 vcc, v141, v67\n\t" \
        "s_nop 0\n\t" \
        "s_or_b64 s[10:11], s[10:11], vcc\n\t" \
        "s_cselect_b32 s54, 1, 0\n\t" \
        "buffer_load_dwordx4 v5, s[24:27], s57 offen offset:2048  lds\n\t" \
        "s_lshl_b32 s55, s35, 2\n\t" \
        "s_cmp_lg_u32 s54, 0\n\t" \
        "v_mov_b32 v162, s55\n\t" \
        "s_cselect_b32 s55, 0x10000, 0\n\t" \
        "v_mov_b32 v163, s55\n\t" \
        "ds_write_b32 v162, v163\n\t" \
        "v_bfe_u32 v162, v2, 2, 1\n\t" \
        "v_lshrrev_b32 v163, 3, v2\n\t" \
        "s_mul_i32 s55, s35, 2560\n\t" \
        "s_add_u32 s55, s55, 109120\n\t" \
        "v_mul_u32_u24 v162, 0x500, v162\n\t" \
        "v_lshlrev_b32 v163, 4, v163\n\t" \
        "v_add3_u32 v9, v162, v163, s55\n\t" \
        "v_lshrrev_b32 v164, 4, v2\n\t" \
        "v_bfe_u32 v162, v2, 2, 2\n\t" \
        "v_and_b32 v163, 3, v2\n\t" \
        "v_lshl_add_u32 v162, v164, 2, v162\n\t" \
        "v_lshlrev_b32 v11, 3, v163\n\t" \
        "v_and_b32 v165, 1, v164\n\t" \
        "v_mul_u32_u24 v162, 0xc0, v162\n\t" \
        "v_xor_b32 v12, 32, v11\n\t" \
        "v_lshl_add_u32 v162, v165, 5, v162\n\t" \
        "v_lshlrev_b32 v163, 6, v165\n\t" \
        "s_mov_b32 s55, 0xba40\n\t" \
        "v_add3_u32 v6, v162, v11, s55\n\t" \
        "v_sub_u32 v7, v6, v163\n\t" \
        "v_mul_u32_u24 v162, 0xc0, v19\n\t" \
        "v_lshrrev_b32 v164, 1, v164\n\t" \
        "v_lshl_add_u32 v162, v165, 5, v162\n\t" \
        "v_lshl_add_u32 v8, v164, 4, v162\n\t" \
        "s_add_u32 s55, s32, s35\n\t" \
        "s_mul_i32 s56, s33, 0xc0\n\t" \
        "s_mul_i32 s55, s55, 0xa800\n\t" \
        "s_add_u32 s59, s55, s56\n\t" \
        "s_add_u32 s60, s59, 0x2a000\n\t" \
        "v_lshrrev_b32 v166, 4, v2\n\t" \
        "v_bfe_u32 v168, v2, 2, 2\n\t" \
        "v_and_b32 v167, 3, v2\n\t" \
        "v_cmp_eq_u32 vcc, v166, v168\n\t" \
        "v_lshlrev_b32 v167, v167, 1\n\t" \
        "s_nop 0\n\t" \
        "v_cndmask_b32 v167, 0, v167, vcc\n\t" \
        "v_bfe_i32 v25, v167, 0, 1\n\t" \
        "v_bfe_i32 v26, v167, 1, 1\n\t" \
        "v_bfe_i32 v27, v167, 2, 1\n\t" \
        "v_bfe_i32 v28, v167, 3, 1\n\t" \
        "v_mov_b32 v163, 0\n\t" \
        "s_waitcnt vmcnt(6) lgkmcnt(0)\n\t" \
        "s_barrier\n\t" \
        "ds_read_b128 v[152:155], v163\n\t" \
        "s_waitcnt lgkmcnt(0)\n\t" \
        "v_add_u32 v152, v152, v154\n\t" \
        "v_add_u32 v153, v153, v155\n\t" \
        "v_max_u32 v152, v152, v153\n\t" \
        "s_nop 0\n\t" \
        "v_readfirstlane_b32 s55, v152\n\t" \
        "s_nop 0\n\t" \
        "s_cmp_gt_u32 s55, 56\n\t" \
        "s_cbranch_scc1 .Lfail%=\n\t" \
        "ds_read_b128 v[170:173], v9 offset:0\n\t" \
        "ds_read_b64 v[150:151], v10 offset:0\n\t" \
        "s_waitcnt lgkmcnt(1)\n\t" \
        "v_xor_b32 v134, v170, v11\n\t" \
        "v_xor_b32 v135, v171, v11\n\t" \
        "v_xor_b32 v136, v172, v11\n\t" \
        "v_xor_b32 v138, v170, v12\n\t" \
        "ds_read_b128 v[174:177], v9 offset:128\n\t" \
        "v_xor_b32 v137, v173, v11\n\t" \
        "v_xor_b32 v139, v171, v12\n\t" \
        "v_xor_b32 v140, v172, v12\n\t" \
        "v_xor_b32 v141, v173, v12\n\t" \
        "ds_read_b64_tr_b16 a[0:1], v6 offset:0\n\t" \
        "ds_read_b64_tr_b16 a[2:3], v6 offset:3072\n\t" \
        "ds_read_b64_tr_b16 a[4:5], v7 offset:32\n\t" \
        "ds_read_b64_tr_b16 a[6:7], v7 offset:3104\n\t" \
        "ds_read_b64_tr_b16 a[8:9], v6 offset:64\n\t" \
        "ds_read_b64_tr_b16 a[10:11], v6 offset:3136\n\t" \
        "ds_read_b64_tr_b16 a[96:97], v134 offset:0\n\t" \
        "ds_read_b64_tr_b16 a[98:99], v135 offset:0\n\t" \
        "ds_read_b64_tr_b16 a[100:101], v138 offset:0\n\t" \
        "ds_read_b64_tr_b16 a[102:103], v139 offset:0\n\t" \
        "ds_read_b64_tr_b16 a[104:105], v136 offset:0\n\t" \
        "ds_read_b64_tr_b16 a[106:107], v137 offset:0\n\t" \
        "ds_read_b64_tr_b16 a[108:109], v140 offset:0\n\t" \
        "ds_read_b64_tr_b16 a[110:111], v141 offset:0\n\t" \
        "ds_read_b64_tr_b16 a[112:113], v134 offset:64\n\t" \
        "ds_read_b64_tr_b16 a[114:115], v135 offset:64\n\t" \
        "ds_read_b64_tr_b16 a[116:117], v138 offset:64\n\t" \
        "ds_read_b64_tr_b16 a[118:119], v139 offset:64\n\t" \
        "ds_read_b64_tr_b16 a[120:121], v136 offset:64\n\t" \
        "ds_read_b64_tr_b16 a[122:123], v137 offset:64\n\t" \
        "ds_read_b64_tr_b16 a[124:125], v140 offset:64\n\t" \
        "ds_read_b64_tr_b16 a[126:127], v141 offset:64\n\t" \
        "ds_read_b64_tr_b16 a[128:129], v134 offset:128\n\t" \
        "ds_read_b64_tr_b16 a[130:131], v135 offset:128\n\t" \
        "ds_read_b64_tr_b16 a[132:133], v138 offset:128\n\t" \
        "ds_read_b64_tr_b16 a[134:135], v139 offset:128\n\t" \
        "ds_read_b64_tr_b16 a[136:137], v136 offset:128\n\t" \
        "s_waitcnt lgkmcnt(15)\n\t" \
        "v_and_b32 v154, v150, v25\n\t" \
        "ds_read_b64_tr_b16 a[138:139], v137 offset:128\n\t" \
        "ds_read_b64_tr_b16 a[140:141], v140 offset:128\n\t" \
        "v_xor_b32 v142, v174, v11\n\t" \
        "v_and_b32 v155, v150, v26\n\t" \
        "ds_read_b64_tr_b16 a[142:143], v141 offset:128\n\t" \
        "v_xor_b32 v143, v175, v11\n\t" \
        "v_and_b32 v156, v150, v27\n\t" \
        "v_xor_b32 v144, v176, v11\n\t" \
        "v_xor_b32 v146, v174, v12\n\t" \
        "v_and_b32 v157, v150, v28\n\t" \
        "v_xor_b32 v145, v177, v11\n\t" \
        "v_xor_b32 v147, v175, v12\n\t" \
        "v_xor_b32 v148, v176, v12\n\t" \
        "ds_read_b64 v[152:153], v10 offset:128\n\t" \
        "v_xor_b32 v149, v177, v12\n\t" \
        "v_and_b32 v158, v151, v25\n\t" \
        "v_and_b32 v159, v151, v26\n\t" \
        "ds_read_b64_tr_b16 a[12:13], v7 offset:96\n\t" \
        "v_and_b32 v160, v151, v27\n\t" \
        "ds_read_b64_tr_b16 a[14:15], v7 offset:3168\n\t" \
        "v_mfma_f32_16x16x32_bf16 v[102:105], a[96:99], v[154:157], 0\n\t" \
        "ds_read_b64_tr_b16 a[16:17], v6 offset:128\n\t" \
        "ds_read_b64_tr_b16 a[18:19], v6 offset:3200\n\t" \
        "v_and_b32 v161, v151, v28\n\t" \
        "v_mfma_f32_16x16x32_bf16 v[106:109], a[100:103], v[154:157], 0\n\t" \
        "s_add_u32 m0, s39, 36864\n\t" \
        "s_add_u32 s57, s40, 36864\n\t" \
        "buffer_load_dwordx4 v3, s[24:27], s57 offen offset:0  lds\n\t" \
        "v_mfma_f32_16x16x32_bf16 v[110:113], a[104:107], v[158:161], 0\n\t" \
        "ds_read_b64_tr_b16 a[20:21], v7 offset:160\n\t" \
        "s_waitcnt lgkmcnt(5)\n\t" \
        "v_and_b32 v162, v152, v25\n\t" \
        "v_mfma_f32_16x16x32_bf16 v[114:117], a[108:111], v[158:161], 0\n\t" \
        "ds_read_b64_tr_b16 a[22:23], v7 offset:3232\n\t" \
        "ds_read_b64_tr_b16 a[96:97], v142 offset:0\n\t" \
        "v_and_b32 v163, v152, v26\n\t" \
        "v_and_b32 v164, v152, v27\n\t" \
        "v_and_b32 v165, v152, v28\n\t" \
        "v_and_b32 v166, v153, v25\n\t" \
        "v_and_b32 v167, v153, v26\n\t" \
        "v_and_b32 v168, v153, v27\n\t" \
        "v_and_b32 v169, v153, v28\n\t" \
        "v_mfma_f32_16x16x32_bf16 v[118:121], a[112:115], v[154:157], 0\n\t" \
        "ds_read_b64_tr_b16 a[98:99], v143 offset:0\n\t" \
        "ds_read_b64_tr_b16 a[100:101], v146 offset:0\n\t" \
        "v_cvt_pk_bf16_f32 v78, v102, v103\n\t" \
        "v_cvt_pk_bf16_f32 v79, v104, v105\n\t" \
        "v_mfma_f32_16x16x32_bf16 v[122:125], a[116:119], v[154:157], 0\n\t" \
        "ds_read_b64_tr_b16 a[102:103], v147 offset:0\n\t" \
        "ds_read_b64_tr_b16 a[104:105], v144 offset:0\n\t" \
        "v_cvt_pk_bf16_f32 v80, v106, v107\n\t" \
        "v_cvt_pk_bf16_f32 v81, v108, v109\n\t" \
        "v_mfma_f32_16x16x32_bf16 v[126:129], a[120:123], v[158:161], 0\n\t" \
        "buffer_load_dwordx4 v4, s[24:27], s57 offen offset:1024  lds\n\t" \
        "v_cvt_pk_bf16_f32 v82, v110, v111\n\t" \
        "v_mfma_f32_16x16x32_bf16 v[130:133], a[124:127], v[158:161], 0\n\t" \
        "ds_read_b64_tr_b16 a[106:107], v145 offset:0\n\t" \
        "ds_read_b64_tr_b16 a[108:109], v148 offset:0\n\t" \
        "v_cvt_pk_bf16_f32 v83, v112, v113\n\t" \
        "v_cvt_pk_bf16_f32 v84, v114, v115\n\t" \
        "v_mfma_f32_16x16x32_bf16 v[102:105], a[128:131], v[154:157], 0\n\t" \
        "ds_read_b64_tr_b16 a[110:111], v149 offset:0\n\t" \
        "ds_read_b64_tr_b16 a[24:25], v6 offset:6144\n\t" \
        "v_cvt_pk_bf16_f32 v85, v116, v117\n\t" \
        "v_mfma_f32_16x16x32_bf16 v[106:109], a[132:135], v[154:157], 0\n\t" \
        "ds_read_b64_tr_b16 a[26:27], v6 offset:9216\n\t" \
        "ds_read_b64_tr_b16 a[28:29], v7 offset:6176\n\t" \
        "v_mfma_f32_16x16x32_bf16 v[110:113], a[136:139], v[158:161], 0\n\t" \
        "ds_read_b64_tr_b16 a[30:31], v7 offset:9248\n\t" \
        "ds_read_b64_tr_b16 a[32:33], v6 offset:6208\n\t" \
        "v_mfma_f32_16x16x32_bf16 v[114:117], a[140:143], v[158:161], 0\n\t" \
        "buffer_load_dwordx4 v5, s[24:27], s57 offen offset:2048  lds\n\t" \
        "v_mfma_f32_16x16x32_bf16 v[30:33], a[0:3], v[78:81], 0\n\t" \
        "ds_read_b64_tr_b16 a[34:35], v6 offset:9280\n\t" \
        "v_cvt_pk_bf16_f32 v86, v118, v119\n\t" \
        "v_mfma_f32_16x16x32_bf16 v[54:57], a[0:3], v[82:85], 0\n\t" \
        "ds_read_b64_tr_b16 a[36:37], v7 offset:6240\n\t" \
        "ds_read_b64_tr_b16 a[38:39], v7 offset:9312\n\t" \
        "v_mfma_f32_16x16x32_bf16 v[34:37], a[4:7], v[78:81], 0\n\t" \
        "ds_read_b64_tr_b16 a[40:41], v6 offset:6272\n\t" \
        "v_cvt_pk_bf16_f32 v87, v120, v121\n\t" \
        "v_mfma_f32_16x16x32_bf16 v[58:61], a[4:7], v[82:85], 0\n\t" \
        "s_waitcnt vmcnt(6)\n\t" \
        "s_barrier\n\t" \
        "ds_read_b128 v[170:173], v9 offset:256\n\t" \
        "ds_read_b64_tr_b16 a[42:43], v6 offset:9344\n\t" \
        "v_mfma_f32_16x16x32_bf16 v[38:41], a[8:11], v[78:81], 0\n\t" \
        "s_add_u32 m0, s39, 49152\n\t" \
        "s_add_u32 s57, s40, 49152\n\t" \
        "buffer_load_dwordx4 v3, s[24:27], s57 offen offset:0  lds\n\t" \
        "v_mfma_f32_16x16x32_bf16 v[62:65], a[8:11], v[82:85], 0\n\t" \
        "ds_read_b64_tr_b16 a[44:45], v7 offset:6304\n\t" \
        "ds_read_b64_tr_b16 a[46:47], v7 offset:9376\n\t" \
        "s_waitcnt lgkmcnt(12)\n\t" \
        "v_mfma_f32_16x16x32_bf16 v[42:45], a[12:15], v[78:81], 0\n\t" \
        "ds_read_b64_tr_b16 a[112:113], v142 offset:64\n\t" \
        "v_cvt_pk_bf16_f32 v88, v122, v123\n\t" \
        "v_mfma_f32_16x16x32_bf16 v[66:69], a[12:15], v[82:85], 0\n\t" \
        "ds_read_b64_tr_b16 a[114:115], v143 offset:64\n\t" \
        "ds_read_b64_tr_b16 a[116:117], v146 offset:64\n\t" \
        "v_mfma_f32_16x16x32_bf16 v[46:49], a[16:19], v[78:81], 0\n\t" \
        "ds_read_b64_tr_b16 a[118:119], v147 offset:64\n\t" \
        "ds_read_b64_tr_b16 a[120:121], v144 offset:64\n\t" \
        "v_mfma_f32_16x16x32_bf16 v[70:73], a[16:19], v[82:85], 0\n\t" \
        "buffer_load_dwordx4 v4, s[24:27], s57 offen offset:1024  lds\n\t" \
        "v_mfma_f32_16x16x32_bf16 v[50:53], a[20:23], v[78:81], 0\n\t" \
        "ds_read_b64_tr_b16 a[122:123], v145 offset:64\n\t" \
        "v_cvt_pk_bf16_f32 v89, v124, v125\n\t" \
        "v_cvt_pk_bf16_f32 v90, v126, v127\n\t" \
        "v_mfma_f32_16x16x32_bf16 v[74:77], a[20:23], v[82:85], 0\n\t" \
        "ds_read_b64_tr_b16 a[124:125], v148 offset:64\n\t" \
        "ds_read_b64_tr_b16 a[126:127], v149 offset:64\n\t" \
        "v_cvt_pk_bf16_f32 v91, v128, v129\n\t" \
        "v_cvt_pk_bf16_f32 v92, v130, v131\n\t" \
        "v_mfma_f32_16x16x32_bf16 v[118:121], a[96:99], v[162:165], 0\n\t" \
        "ds_read_b64_tr_b16 a[48:49], v6 offset:12288\n\t" \
        "v_cvt_pk_bf16_f32 v93, v132, v133\n\t" \
        "v_mfma_f32_16x16x32_bf16 v[122:125], a[100:103], v[162:165], 0\n\t" \
        "ds_read_b64_tr_b16 a[50:51], v6 offset:15360\n\t" \
        "ds_read_b64_tr_b16 a[52:53], v7 offset:12320\n\t" \
        "v_mfma_f32_16x16x32_bf16 v[126:129], a[104:107], v[166:169], 0\n\t" \
        "buffer_load_dwordx4 v5, s[24:27], s57 offen offset:2048  lds\n\t" \
        "v_mfma_f32_16x16x32_bf16 v[130:133], a[108:111], v[166:169], 0\n\t" \
        "ds_read_b64_tr_b16 a[54:55], v7 offset:15392\n\t" \
        "ds_read_b64_tr_b16 a[56:57], v6 offset:12352\n\t" \
        "s_waitcnt lgkmcnt(12)\n\t" \
        "v_mfma_f32_16x16x32_bf16 v[30:33], a[24:27], v[86:89], v[30:33]\n\t" \
        "ds_read_b64_tr_b16 a[58:59], v6 offset:15424\n\t" \
        "v_cvt_pk_bf16_f32 v94, v102, v103\n\t" \
        "v_mfma_f32_16x16x32_bf16 v[54:57], a[24:27], v[90:93], v[54:57]\n\t" \
        "ds_read_b64_tr_b16 a[60:61], v7 offset:12384\n\t" \
        "v_cvt_pk_bf16_f32 v95, v104, v105\n\t" \
        "v_mfma_f32_16x16x32_bf16 v[34:37], a[28:31], v[86:89], v[34:37]\n\t" \
        "ds_read_b64_tr_b16 a[62:63], v7 offset:15456\n\t" \
        "ds_read_b64_tr_b16 a[64:65], v6 offset:12416\n\t" \
        "v_mfma_f32_16x16x32_bf16 v[58:61], a[28:31], v[90:93], v[58:61]\n\t" \
        "ds_read_b64_tr_b16 a[66:67], v6 offset:15488\n\t" \
        "v_cvt_pk_bf16_f32 v96, v106, v107\n\t" \
        "v_mfma_f32_16x16x32_bf16 v[38:41], a[32:35], v[86:89], v[38:41]\n\t" \
        "ds_read_b64_tr_b16 a[68:69], v7 offset:12448\n\t" \
        "ds_read_b64_tr_b16 a[70:71], v7 offset:15520\n\t" \
        "v_mfma_f32_16x16x32_bf16 v[62:65], a[32:35], v[90:93], v[62:65]\n\t" \
        "ds_read_b64_tr_b16 a[128:129], v142 offset:128\n\t" \
        "v_cvt_pk_bf16_f32 v97, v108, v109\n\t" \
        "v_mfma_f32_16x16x32_bf16 v[42:45], a[36:39], v[86:89], v[42:45]\n\t" \
        "ds_read_b64_tr_b16 a[130:131], v143 offset:128\n\t" \
        "v_cvt_pk_bf16_f32 v98, v110, v111\n\t" \
        "v_mfma_f32_16x16x32_bf16 v[66:69], a[36:39], v[90:93], v[66:69]\n\t" \
        "ds_read_b64_tr_b16 a[132:133], v146 offset:128\n\t" \
        "ds_read_b64_tr_b16 a[134:135], v147 offset:128\n\t" \
        "v_mfma_f32_16x16x32_bf16 v[46:49], a[40:43], v[86:89], v[46:49]\n\t" \
        "ds_read_b64_tr_b16 a[136:137], v144 offset:128\n\t" \
        "v_xor_b32 v134, v170, v11\n\t" \
        "v_mfma_f32_16x16x32_bf16 v[70:73], a[40:43], v[90:93], v[70:73]\n\t" \
        "ds_read_b64_tr_b16 a[138:139], v145 offset:128\n\t" \
        "v_cvt_pk_bf16_f32 v99, v112, v113\n\t" \
        "v_mfma_f32_16x16x32_bf16 v[50:53], a[44:47], v[86:89], v[50:53]\n\t" \
        "ds_read_b64_tr_b16 a[140:141], v148 offset:128\n\t" \
        "ds_read_b64_tr_b16 a[142:143], v149 offset:128\n\t" \
        "v_mfma_f32_16x16x32_bf16 v[74:77], a[44:47], v[90:93], v[74:77]\n\t" \
        "ds_read_b64_tr_b16 a[72:73], v6 offset:18432\n\t" \
        "v_cvt_pk_bf16_f32 v100, v114, v115\n\t" \
        "v_xor_b32 v135, v171, v11\n\t" \
        "s_waitcnt lgkmcnt(12)\n\t" \
        "v_mfma_f32_16x16x32_bf16 v[102:105], a[112:115], v[162:165], 0\n\t" \
        "ds_read_b64_tr_b16 a[74:75], v6 offset:21504\n\t" \
        "ds_read_b64_tr_b16 a[76:77], v7 offset:18464\n\t" \
        "v_cvt_pk_bf16_f32 v101, v116, v117\n\t" \
        "v_mfma_f32_16x16x32_bf16 v[106:109], a[116:119], v[162:165], 0\n\t" \
        "ds_read_b64 v[150:151], v10 offset:256\n\t" \
        "v_xor_b32 v136, v172, v11\n\t" \
        "v_xor_b32 v138, v170, v12\n\t" \
        "v_mfma_f32_16x16x32_bf16 v[110:113], a[120:123], v[166:169], 0\n\t" \
        "ds_read_b64_tr_b16 a[78:79], v7 offset:21536\n\t" \
        "ds_read_b64_tr_b16 a[80:81], v6 offset:18496\n\t" \
        "v_xor_b32 v137, v173, v11\n\t" \
        "v_xor_b32 v139, v171, v12\n\t" \
        "v_mfma_f32_16x16x32_bf16 v[114:117], a[124:127], v[166:169], 0\n\t" \
        "ds_read_b64_tr_b16 a[82:83], v6 offset:21568\n\t" \
        "v_xor_b32 v140, v172, v12\n\t" \
        "v_mfma_f32_16x16x32_bf16 v[30:33], a[48:51], v[94:97], v[30:33]\n\t" \
        "ds_read_b64_tr_b16 a[84:85], v7 offset:18528\n\t" \
        "v_xor_b32 v141, v173, v12\n\t" \
        "v_mfma_f32_16x16x32_bf16 v[54:57], a[48:51], v[98:101], v[54:57]\n\t" \
        "ds_read_b64_tr_b16 a[86:87], v7 offset:21600\n\t" \
        "ds_read_b64_tr_b16 a[88:89], v6 offset:18560\n\t" \
        "v_mfma_f32_16x16x32_bf16 v[34:37], a[52:55], v[94:97], v[34:37]\n\t" \
        "ds_read_b64_tr_b16 a[90:91], v6 offset:21632\n\t" \
        "v_cvt_pk_bf16_f32 v78, v118, v119\n\t" \
        "v_mfma_f32_16x16x32_bf16 v[58:61], a[52:55], v[98:101], v[58:61]\n\t" \
        "s_waitcnt vmcnt(6)\n\t" \
        "s_barrier\n\t" \
        "ds_read_b64_tr_b16 a[92:93], v7 offset:18592\n\t" \
        "ds_read_b64_tr_b16 a[94:95], v7 offset:21664\n\t" \
        "v_mfma_f32_16x16x32_bf16 v[38:41], a[56:59], v[94:97], v[38:41]\n\t" \
        "ds_read_b64_tr_b16 a[96:97], v134 offset:0\n\t" \
        "v_cvt_pk_bf16_f32 v79, v120, v121\n\t" \
        "v_mfma_f32_16x16x32_bf16 v[62:65], a[56:59], v[98:101], v[62:65]\n\t" \
        "ds_read_b64_tr_b16 a[98:99], v135 offset:0\n\t" \
        "v_cvt_pk_bf16_f32 v80, v122, v123\n\t" \
        "v_mfma_f32_16x16x32_bf16 v[42:45], a[60:63], v[94:97], v[42:45]\n\t" \
        "s_add_u32 m0, s39, 0\n\t" \
        "s_add_u32 s57, s40, 61440\n\t" \
        "buffer_load_dwordx4 v3, s[24:27], s57 offen offset:0  lds\n\t" \
        "v_mfma_f32_16x16x32_bf16 v[66:69], a[60:63], v[98:101], v[66:69]\n\t" \
        "ds_read_b64_tr_b16 a[100:101], v138 offset:0\n\t" \
        "ds_read_b64_tr_b16 a[102:103], v139 offset:0\n\t" \
        "s_waitcnt lgkmcnt(11)\n\t" \
        "v_mfma_f32_16x16x32_bf16 v[46:49], a[64:67], v[94:97], v[46:49]\n\t" \
        "ds_read_b64_tr_b16 a[104:105], v136 offset:0\n\t" \
        "v_cvt_pk_bf16_f32 v81, v124, v125\n\t" \
        "v_mfma_f32_16x16x32_bf16 v[70:73], a[64:67], v[98:101], v[70:73]\n\t" \
        "ds_read_b64_tr_b16 a[106:107], v137 offset:0\n\t" \
        "ds_read_b64_tr_b16 a[108:109], v140 offset:0\n\t" \
        "v_mfma_f32_16x16x32_bf16 v[50:53], a[68:71], v[94:97], v[50:53]\n\t" \
        "ds_read_b64_tr_b16 a[110:111], v141 offset:0\n\t" \
        "v_cvt_pk_bf16_f32 v82, v126, v127\n\t" \
        "v_cvt_pk_bf16_f32 v83, v128, v129\n\t" \
        "v_mfma_f32_16x16x32_bf16 v[74:77], a[68:71], v[98:101], v[74:77]\n\t" \
        "ds_read_b64_tr_b16 a[0:1], v6 offset:24576\n\t" \
        "v_cvt_pk_bf16_f32 v84, v130, v131\n\t" \
        "v_cvt_pk_bf16_f32 v85, v132, v133\n\t" \
        "v_mfma_f32_16x16x32_bf16 v[118:121], a[128:131], v[162:165], 0\n\t" \
        "buffer_load_dwordx4 v4, s[24:27], s57 offen offset:1024  lds\n\t" \
        "v_mfma_f32_16x16x32_bf16 v[122:125], a[132:135], v[162:165], 0\n\t" \
        "ds_read_b64_tr_b16 a[2:3], v6 offset:27648\n\t" \
        "ds_read_b64_tr_b16 a[4:5], v7 offset:24608\n\t" \
        "v_mfma_f32_16x16x32_bf16 v[126:129], a[136:139], v[166:169], 0\n\t" \
        "ds_read_b64_tr_b16 a[6:7], v7 offset:27680\n\t" \
        "v_cvt_pk_bf16_f32 v86, v102, v103\n\t" \
        "v_mfma_f32_16x16x32_bf16 v[130:133], a[140:143], v[166:169], 0\n\t" \
        "ds_read_b64_tr_b16 a[8:9], v6 offset:24640\n\t" \
        "ds_read_b64_tr_b16 a[10:11], v6 offset:27712\n\t" \
        "v_mfma_f32_16x16x32_bf16 v[30:33], a[72:75], v[78:81], v[30:33]\n\t" \
        "ds_read_b64_tr_b16 a[12:13], v7 offset:24672\n\t" \
        "v_and_b32 v154, v150, v25\n\t" \
        "v_mfma_f32_16x16x32_bf16 v[54:57], a[72:75], v[82:85], v[54:57]\n\t" \
        "ds_read_b64_tr_b16 a[14:15], v7 offset:27744\n\t" \
        "v_and_b32 v155, v150, v26\n\t" \
        "v_mfma_f32_16x16x32_bf16 v[34:37], a[76:79], v[78:81], v[34:37]\n\t" \
        "buffer_load_dwordx4 v5, s[24:27], s57 offen offset:2048  lds\n\t" \
        "v_mfma_f32_16x16x32_bf16 v[58:61], a[76:79], v[82:85], v[58:61]\n\t" \
        "ds_read_b64_tr_b16 a[16:17], v6 offset:24704\n\t" \
        "ds_read_b128 v[174:177], v9 offset:384\n\t" \
        "s_waitcnt lgkmcnt(10)\n\t" \
        "v_mfma_f32_16x16x32_bf16 v[38:41], a[80:83], v[78:81], v[38:41]\n\t" \
        "ds_read_b64_tr_b16 a[18:19], v6 offset:27776\n\t" \
        "v_and_b32 v156, v150, v27\n\t" \
        "v_mfma_f32_16x16x32_bf16 v[62:65], a[80:83], v[82:85], v[62:65]\n\t" \
        "ds_read_b64_tr_b16 a[20:21], v7 offset:24736\n\t" \
        "ds_read_b64_tr_b16 a[22:23], v7 offset:27808\n\t" \
        "v_mfma_f32_16x16x32_bf16 v[42:45], a[84:87], v[78:81], v[42:45]\n\t" \
        "ds_read_b64_tr_b16 a[112:113], v134 offset:64\n\t" \
        "v_and_b32 v157, v150, v28\n\t" \
        "v_and_b32 v158, v151, v25\n\t" \
        "v_mfma_f32_16x16x32_bf16 v[66:69], a[84:87], v[82:85], v[66:69]\n\t" \
        "ds_read_b64_tr_b16 a[114:115], v135 offset:64\n\t" \
        "v_cvt_pk_bf16_f32 v87, v104, v105\n\t" \
        "v_mfma_f32_16x16x32_bf16 v[46:49], a[88:91], v[78:81], v[46:49]\n\t" \
        "ds_read_b64_tr_b16 a[116:117], v138 offset:64\n\t" \
        "ds_read_b64_tr_b16 a[118:119], v139 offset:64\n\t" \
        "v_cvt_pk_bf16_f32 v88, v106, v107\n\t" \
        "v_and_b32 v159, v151, v26\n\t" \
        "v_mfma_f32_16x16x32_bf16 v[70:73], a[88:91], v[82:85], v[70:73]\n\t" \
        "ds_read_b64_tr_b16 a[120:121], v136 offset:64\n\t" \
        "v_cvt_pk_bf16_f32 v89, v108, v109\n\t" \
        "v_cvt_pk_bf16_f32 v90, v110, v111\n\t" \
        "v_mfma_f32_16x16x32_bf16 v[50:53], a[92:95], v[78:81], v[50:53]\n\t" \
        "ds_read_b64_tr_b16 a[122:123], v137 offset:64\n\t" \
        "ds_read_b64_tr_b16 a[124:125], v140 offset:64\n\t" \
        "v_and_b32 v160, v151, v27\n\t" \
        "v_mfma_f32_16x16x32_bf16 v[74:77], a[92:95], v[82:85], v[74:77]\n\t" \
        "ds_read_b64_tr_b16 a[126:127], v141 offset:64\n\t" \
        "v_cvt_pk_bf16_f32 v91, v112, v113\n\t" \
        "v_cvt_pk_bf16_f32 v92, v114, v115\n\t" \
        "v_mfma_f32_16x16x32_bf16 v[102:105], a[96:99], v[154:157], 0\n\t" \
        "ds_read_b64_tr_b16 a[24:25], v6 offset:30720\n\t" \
        "v_and_b32 v161, v151, v28\n\t" \
        "v_cvt_pk_bf16_f32 v93, v116, v117\n\t" \
        "v_mfma_f32_16x16x32_bf16 v[106:109], a[100:103], v[154:157], 0\n\t" \
        "ds_read_b64_tr_b16 a[26:27], v6 offset:33792\n\t" \
        "ds_read_b64_tr_b16 a[28:29], v7 offset:30752\n\t" \
        "v_mfma_f32_16x16x32_bf16 v[110:113], a[104:107], v[158:161], 0\n\t" \
        "ds_read_b64_tr_b16 a[30:31], v7 offset:33824\n\t" \
        "v_cvt_pk_bf16_f32 v94, v118, v119\n\t" \
        "v_mfma_f32_16x16x32_bf16 v[114:117], a[108:111], v[158:161], 0\n\t" \
        "ds_read_b64_tr_b16 a[32:33], v6 offset:30784\n\t" \
        "ds_read_b64_tr_b16 a[34:35], v6 offset:33856\n\t" \
        "s_waitcnt lgkmcnt(13)\n\t" \
        "v_mfma_f32_16x16x32_bf16 v[30:33], a[0:3], v[86:89], v[30:33]\n\t" \
        "ds_read_b64_tr_b16 a[36:37], v7 offset:30816\n\t" \
        "v_cvt_pk_bf16_f32 v95, v120, v121\n\t" \
        "v_mfma_f32_16x16x32_bf16 v[54:57], a[0:3], v[90:93], v[54:57]\n\t" \
        "ds_read_b64_tr_b16 a[38:39], v7 offset:33888\n\t" \
        "ds_read_b64_tr_b16 a[40:41], v6 offset:30848\n\t" \
        "v_mfma_f32_16x16x32_bf16 v[34:37], a[4:7], v[86:89], v[34:37]\n\t" \
        "ds_read_b64_tr_b16 a[42:43], v6 offset:33920\n\t" \
        "v_cvt_pk_bf16_f32 v96, v122, v123\n\t" \
        "v_mfma_f32_16x16x32_bf16 v[58:61], a[4:7], v[90:93], v[58:61]\n\t" \
        "s_waitcnt vmcnt(6)\n\t" \
        "s_barrier\n\t" \
        "ds_read_b64_tr_b16 a[44:45], v7 offset:30880\n\t" \
        "v_cvt_pk_bf16_f32 v97, v124, v125\n\t" \
        "v_mfma_f32_16x16x32_bf16 v[38:41], a[8:11], v[86:89], v[38:41]\n\t" \
        "ds_read_b64_tr_b16 a[46:47], v7 offset:33952\n\t" \
        "ds_read_b64_tr_b16 a[128:129], v134 offset:128\n\t" \
        "v_mfma_f32_16x16x32_bf16 v[62:65], a[8:11], v[90:93], v[62:65]\n\t" \
        "ds_read_b64_tr_b16 a[130:131], v135 offset:128\n\t" \
        "v_cvt_pk_bf16_f32 v98, v126, v127\n\t" \
        "v_mfma_f32_16x16x32_bf16 v[42:45], a[12:15], v[86:89], v[42:45]\n\t" \
        "s_add_u32 m0, s39, 12288\n\t" \
        "s_add_u32 s57, s40, 73728\n\t" \
        "buffer_load_dwordx4 v3, s[24:27], s57 offen offset:0  lds\n\t" \
        "v_mfma_f32_16x16x32_bf16 v[66:69], a[12:15], v[90:93], v[66:69]\n\t" \
        "ds_read_b64_tr_b16 a[132:133], v138 offset:128\n\t" \
        "ds_read_b64_tr_b16 a[134:135], v139 offset:128\n\t" \
        "v_mfma_f32_16x16x32_bf16 v[46:49], a[16:19], v[86:89], v[46:49]\n\t" \
        "ds_read_b64_tr_b16 a[136:137], v136 offset:128\n\t" \
        "v_xor_b32 v142, v174, v11\n\t" \
        "v_mfma_f32_16x16x32_bf16 v[70:73], a[16:19], v[90:93], v[70:73]\n\t" \
        "ds_read_b64_tr_b16 a[138:139], v137 offset:128\n\t" \
        "ds_read_b64_tr_b16 a[140:141], v140 offset:128\n\t" \
        "v_cvt_pk_bf16_f32 v99, v128, v129\n\t" \
        "v_mfma_f32_16x16x32_bf16 v[50:53], a[20:23], v[86:89], v[50:53]\n\t" \
        "ds_read_b64_tr_b16 a[142:143], v141 offset:128\n\t" \
        "v_cvt_pk_bf16_f32 v100, v130, v131\n\t" \
        "v_xor_b32 v143, v175, v11\n\t" \
        "v_mfma_f32_16x16x32_bf16 v[74:77], a[20:23], v[90:93], v[74:77]\n\t" \
        "ds_read_b64_tr_b16 a[48:49], v6 offset:36864\n\t" \
        "v_cvt_pk_bf16_f32 v101, v132, v133\n\t" \
        "s_waitcnt lgkmcnt(11)\n\t" \
        "v_mfma_f32_16x16x32_bf16 v[118:121], a[112:115], v[154:157], 0\n\t" \
        "buffer_load_dwordx4 v4, s[24:27], s57 offen offset:1024  lds\n\t" \
        "v_mfma_f32_16x16x32_bf16 v[122:125], a[116:119], v[154:157], 0\n\t" \
        "ds_read_b64_tr_b16 a[50:51], v6 offset:39936\n\t" \
        "ds_read_b64_tr_b16 a[52:53], v7 offset:36896\n\t" \
        "v_xor_b32 v144, v176, v11\n\t" \
        "v_xor_b32 v146, v174, v12\n\t" \
        "v_mfma_f32_16x16x32_bf16 v[126:129], a[120:123], v[158:161], 0\n\t" \
        "ds_read_b64 v[152:153], v10 offset:384\n\t" \
        "v_xor_b32 v145, v177, v11\n\t" \
        "v_xor_b32 v147, v175, v12\n\t" \
        "v_mfma_f32_16x16x32_bf16 v[130:133], a[124:127], v[158:161], 0\n\t" \
        "ds_read_b64_tr_b16 a[54:55], v7 offset:39968\n\t" \
        "ds_read_b64_tr_b16 a[56:57], v6 offset:36928\n\t" \
        "v_xor_b32 v148, v176, v12\n\t" \
        "v_mfma_f32_16x16x32_bf16 v[30:33], a[24:27], v[94:97], v[30:33]\n\t" \
        "ds_read_b64_tr_b16 a[58:59], v6 offset:40000\n\t" \
        "v_xor_b32 v149, v177, v12\n\t" \
        "v_mfma_f32_16x16x32_bf16 v[54:57], a[24:27], v[98:101], v[54:57]\n\t" \
        "ds_read_b64_tr_b16 a[60:61], v7 offset:36960\n\t" \
        "ds_read_b64_tr_b16 a[62:63], v7 offset:40032\n\t" \
        "v_mfma_f32_16x16x32_bf16 v[34:37], a[28:31], v[94:97], v[34:37]\n\t" \
        "buffer_load_dwordx4 v5, s[24:27], s57 offen offset:2048  lds\n\t" \
        "v_mfma_f32_16x16x32_bf16 v[58:61], a[28:31], v[98:101], v[58:61]\n\t" \
        "ds_read_b64_tr_b16 a[64:65], v6 offset:36992\n\t" \
        "v_cvt_pk_bf16_f32 v78, v102, v103\n\t" \
        "v_mfma_f32_16x16x32_bf16 v[38:41], a[32:35], v[94:97], v[38:41]\n\t" \
        "ds_read_b64_tr_b16 a[66:67], v6 offset:40064\n\t" \
        "v_cvt_pk_bf16_f32 v79, v104, v105\n\t" \
        "v_mfma_f32_16x16x32_bf16 v[62:65], a[32:35], v[98:101], v[62:65]\n\t" \
        "ds_read_b64_tr_b16 a[68:69], v7 offset:37024\n\t" \
        "ds_read_b64_tr_b16 a[70:71], v7 offset:40096\n\t" \
        "v_mfma_f32_16x16x32_bf16 v[42:45], a[36:39], v[94:97], v[42:45]\n\t" \
        "ds_read_b64_tr_b16 a[96:97], v142 offset:0\n\t" \
        "v_cvt_pk_bf16_f32 v80, v106, v107\n\t" \
        "v_mfma_f32_16x16x32_bf16 v[66:69], a[36:39], v[98:101], v[66:69]\n\t" \
        "ds_read_b64_tr_b16 a[98:99], v143 offset:0\n\t" \
        "ds_read_b64_tr_b16 a[100:101], v146 offset:0\n\t" \
        "v_mfma_f32_16x16x32_bf16 v[46:49], a[40:43], v[94:97], v[46:49]\n\t" \
        "ds_read_b64_tr_b16 a[102:103], v147 offset:0\n\t" \
        "v_cvt_pk_bf16_f32 v81, v108, v109\n\t" \
        "v_mfma_f32_16x16x32_bf16 v[70:73], a[40:43], v[98:101], v[70:73]\n\t" \
        "ds_read_b64_tr_b16 a[104:105], v144 offset:0\n\t" \
        "ds_read_b64_tr_b16 a[106:107], v145 offset:0\n\t" \
        "s_waitcnt lgkmcnt(12)\n\t" \
        "v_mfma_f32_16x16x32_bf16 v[50:53], a[44:47], v[94:97], v[50:53]\n\t" \
        "ds_read_b64_tr_b16 a[108:109], v148 offset:0\n\t" \
        "v_cvt_pk_bf16_f32 v82, v110, v111\n\t" \
        "v_mfma_f32_16x16x32_bf16 v[74:77], a[44:47], v[98:101], v[74:77]\n\t" \
        "ds_read_b64_tr_b16 a[110:111], v149 offset:0\n\t" \
        "v_cvt_pk_bf16_f32 v83, v112, v113\n\t" \
        "v_cvt_pk_bf16_f32 v84, v114, v115\n\t" \
        "v_mfma_f32_16x16x32_bf16 v[102:105], a[128:131], v[154:157], 0\n\t" \
        "ds_read_b64_tr_b16 a[72:73], v6 offset:43008\n\t" \
        "ds_read_b64_tr_b16 a[74:75], v6 offset:46080\n\t" \
        "v_cvt_pk_bf16_f32 v85, v116, v117\n\t" \
        "v_mfma_f32_16x16x32_bf16 v[106:109], a[132:135], v[154:157], 0\n\t" \
        "ds_read_b64_tr_b16 a[76:77], v7 offset:43040\n\t" \
        "v_cvt_pk_bf16_f32 v86, v118, v119\n\t" \
        "v_mfma_f32_16x16x32_bf16 v[110:113], a[136:139], v[158:161], 0\n\t" \
        "ds_read_b64_tr_b16 a[78:79], v7 offset:46112\n\t" \
        "ds_read_b64_tr_b16 a[80:81], v6 offset:43072\n\t" \
        "v_mfma_f32_16x16x32_bf16 v[114:117], a[140:143], v[158:161], 0\n\t" \
        "ds_read_b64_tr_b16 a[82:83], v6 offset:46144\n\t" \
        "v_cvt_pk_bf16_f32 v87, v120, v121\n\t" \
        "v_mfma_f32_16x16x32_bf16 v[30:33], a[48:51], v[78:81], v[30:33]\n\t" \
        "ds_read_b64_tr_b16 a[84:85], v7 offset:43104\n\t" \
        "ds_read_b64_tr_b16 a[86:87], v7 offset:46176\n\t" \
        "v_mfma_f32_16x16x32_bf16 v[54:57], a[48:51], v[82:85], v[54:57]\n\t" \
        "ds_read_b64_tr_b16 a[88:89], v6 offset:43136\n\t" \
        "v_and_b32 v162, v152, v25\n\t" \
        "v_mfma_f32_16x16x32_bf16 v[34:37], a[52:55], v[78:81], v[34:37]\n\t" \
        "ds_read_b128 v[170:173], v9 offset:512\n\t" \
        "ds_read_b64_tr_b16 a[90:91], v6 offset:46208\n\t" \
        "v_mfma_f32_16x16x32_bf16 v[58:61], a[52:55], v[82:85], v[58:61]\n\t" \
        "s_waitcnt vmcnt(6)\n\t" \
        "s_barrier\n\t" \
        "ds_read_b64_tr_b16 a[92:93], v7 offset:43168\n\t" \
        "v_and_b32 v163, v152, v26\n\t" \
        "v_mfma_f32_16x16x32_bf16 v[38:41], a[56:59], v[78:81], v[38:41]\n\t" \
        "ds_read_b64_tr_b16 a[94:95], v7 offset:46240\n\t" \
        "v_and_b32 v164, v152, v27\n\t" \
        "v_and_b32 v165, v152, v28\n\t" \
        "v_mfma_f32_16x16x32_bf16 v[62:65], a[56:59], v[82:85], v[62:65]\n\t" \
        "ds_read_b64_tr_b16 a[112:113], v142 offset:64\n\t" \
        "ds_read_b64_tr_b16 a[114:115], v143 offset:64\n\t" \
        "v_and_b32 v166, v153, v25\n\t" \
        "s_waitcnt lgkmcnt(13)\n\t" \
        "v_mfma_f32_16x16x32_bf16 v[42:45], a[60:63], v[78:81], v[42:45]\n\t" \
        "s_add_u32 m0, s39, 24576\n\t" \
        "s_add_u32 s57, s40, 86016\n\t" \
        "buffer_load_dwordx4 v3, s[24:27], s57 offen offset:0  lds\n\t" \
        "v_mfma_f32_16x16x32_bf16 v[66:69], a[60:63], v[82:85], v[66:69]\n\t" \
        "ds_read_b64_tr_b16 a[116:117], v146 offset:64\n\t" \
        "v_cvt_pk_bf16_f32 v88, v122, v123\n\t" \
        "v_and_b32 v167, v153, v26\n\t" \
        "v_mfma_f32_16x16x32_bf16 v[46:49], a[64:67], v[78:81], v[46:49]\n\t" \
        "ds_read_b64_tr_b16 a[118:119], v147 offset:64\n\t" \
        "ds_read_b64_tr_b16 a[120:121], v144 offset:64\n\t" \
        "v_cvt_pk_bf16_f32 v89, v124, v125\n\t" \
        "v_cvt_pk_bf16_f32 v90, v126, v127\n\t" \
        "v_mfma_f32_16x16x32_bf16 v[70:73], a[64:67], v[82:85], v[70:73]\n\t" \
        "ds_read_b64_tr_b16 a[122:123], v145 offset:64\n\t" \
        "v_and_b32 v168, v153, v27\n\t" \
        "v_mfma_f32_16x16x32_bf16 v[50:53], a[68:71], v[78:81], v[50:53]\n\t" \
        "ds_read_b64_tr_b16 a[124:125], v148 offset:64\n\t" \
        "ds_read_b64_tr_b16 a[126:127], v149 offset:64\n\t" \
        "v_cvt_pk_bf16_f32 v91, v128, v129\n\t" \
        "v_cvt_pk_bf16_f32 v92, v130, v131\n\t" \
        "v_mfma_f32_16x16x32_bf16 v[74:77], a[68:71], v[82:85], v[74:77]\n\t" \
        "ds_read_b64_tr_b16 a[0:1], v6 offset:49152\n\t" \
        "v_and_b32 v169, v153, v28\n\t" \
        "v_cvt_pk_bf16_f32 v93, v132, v133\n\t" \
        "v_mfma_f32_16x16x32_bf16 v[118:121], a[96:99], v[162:165], 0\n\t" \
        "buffer_load_dwordx4 v4, s[24:27], s57 offen offset:1024  lds\n\t" \
        "v_mfma_f32_16x16x32_bf16 v[122:125], a[100:103], v[162:165], 0\n\t" \
        "ds_read_b64_tr_b16 a[2:3], v6 offset:52224\n\t" \
        "ds_read_b64_tr_b16 a[4:5], v7 offset:49184\n\t" \
        "v_mfma_f32_16x16x32_bf16 v[126:129], a[104:107], v[166:169], 0\n\t" \
        "ds_read_b64_tr_b16 a[6:7], v7 offset:52256\n\t" \
        "v_cvt_pk_bf16_f32 v94, v102, v103\n\t" \
        "v_mfma_f32_16x16x32_bf16 v[130:133], a[108:111], v[166:169], 0\n\t" \
        "ds_read_b64_tr_b16 a[8:9], v6 offset:49216\n\t" \
        "v_cvt_pk_bf16_f32 v95, v104, v105\n\t" \
        "v_mfma_f32_16x16x32_bf16 v[30:33], a[72:75], v[86:89], v[30:33]\n\t" \
        "ds_read_b64_tr_b16 a[10:11], v6 offset:52288\n\t" \
        "ds_read_b64_tr_b16 a[12:13], v7 offset:49248\n\t" \
        "v_mfma_f32_16x16x32_bf16 v[54:57], a[72:75], v[90:93], v[54:57]\n\t" \
        "ds_read_b64_tr_b16 a[14:15], v7 offset:52320\n\t" \
        "v_cvt_pk_bf16_f32 v96, v106, v107\n\t" \
        "s_waitcnt lgkmcnt(11)\n\t" \
        "v_mfma_f32_16x16x32_bf16 v[34:37], a[76:79], v[86:89], v[34:37]\n\t" \
        "buffer_load_dwordx4 v5, s[24:27], s57 offen offset:2048  lds\n\t" \
        "v_mfma_f32_16x16x32_bf16 v[58:61], a[76:79], v[90:93], v[58:61]\n\t" \
        "ds_read_b64_tr_b16 a[16:17], v6 offset:49280\n\t" \
        "ds_read_b64_tr_b16 a[18:19], v6 offset:52352\n\t" \
        "v_mfma_f32_16x16x32_bf16 v[38:41], a[80:83], v[86:89], v[38:41]\n\t" \
        "ds_read_b64_tr_b16 a[20:21], v7 offset:49312\n\t" \
        "v_cvt_pk_bf16_f32 v97, v108, v109\n\t" \
        "v_mfma_f32_16x16x32_bf16 v[62:65], a[80:83], v[90:93], v[62:65]\n\t" \
        "ds_read_b64_tr_b16 a[22:23], v7 offset:52384\n\t" \
        "v_cvt_pk_bf16_f32 v98, v110, v111\n\t" \
        "v_mfma_f32_16x16x32_bf16 v[42:45], a[84:87], v[86:89], v[42:45]\n\t" \
        "ds_read_b64_tr_b16 a[128:129], v142 offset:128\n\t" \
        "ds_read_b64_tr_b16 a[130:131], v143 offset:128\n\t" \
        "v_mfma_f32_16x16x32_bf16 v[66:69], a[84:87], v[90:93], v[66:69]\n\t" \
        "ds_read_b64_tr_b16 a[132:133], v146 offset:128\n\t" \
        "v_xor_b32 v134, v170, v11\n\t" \
        "v_mfma_f32_16x16x32_bf16 v[46:49], a[88:91], v[86:89], v[46:49]\n\t" \
        "ds_read_b64_tr_b16 a[134:135], v147 offset:128\n\t" \
        "ds_read_b64_tr_b16 a[136:137], v144 offset:128\n\t" \
        "v_mfma_f32_16x16x32_bf16 v[70:73], a[88:91], v[90:93], v[70:73]\n\t" \
        "ds_read_b64_tr_b16 a[138:139], v145 offset:128\n\t" \
        "v_cvt_pk_bf16_f32 v99, v112, v113\n\t" \
        "v_mfma_f32_16x16x32_bf16 v[50:53], a[92:95], v[86:89], v[50:53]\n\t" \
        "ds_read_b64_tr_b16 a[140:141], v148 offset:128\n\t" \
        "v_cvt_pk_bf16_f32 v100, v114, v115\n\t" \
        "v_mfma_f32_16x16x32_bf16 v[74:77], a[92:95], v[90:93], v[74:77]\n\t" \
        "ds_read_b64_tr_b16 a[142:143], v149 offset:128\n\t" \
        "ds_read_b64_tr_b16 a[24:25], v6 offset:55296\n\t" \
        "v_xor_b32 v135, v171, v11\n\t" \
        "v_mfma_f32_16x16x32_bf16 v[102:105], a[112:115], v[162:165], 0\n\t" \
        "ds_read_b64_tr_b16 a[26:27], v6 offset:58368\n\t" \
        "v_cvt_pk_bf16_f32 v101, v116, v117\n\t" \
        "v_mfma_f32_16x16x32_bf16 v[106:109], a[116:119], v[162:165], 0\n\t" \
        "ds_read_b64_tr_b16 a[28:29], v7 offset:55328\n\t" \
        "ds_read_b64 v[150:151], v10 offset:512\n\t" \
        "v_xor_b32 v136, v172, v11\n\t" \
        "v_xor_b32 v138, v170, v12\n\t" \
        "s_waitcnt lgkmcnt(13)\n\t" \
        "v_mfma_f32_16x16x32_bf16 v[110:113], a[120:123], v[166:169], 0\n\t" \
        "ds_read_b64_tr_b16 a[30:31], v7 offset:58400\n\t" \
        "v_xor_b32 v137, v173, v11\n\t" \
        "v_xor_b32 v139, v171, v12\n\t" \
        "v_mfma_f32_16x16x32_bf16 v[114:117], a[124:127], v[166:169], 0\n\t" \
        "ds_read_b64_tr_b16 a[32:33], v6 offset:55360\n\t" \
        "ds_read_b64_tr_b16 a[34:35], v6 offset:58432\n\t" \
        "v_xor_b32 v140, v172, v12\n\t" \
        "v_mfma_f32_16x16x32_bf16 v[30:33], a[0:3], v[94:97], v[30:33]\n\t" \
        "ds_read_b64_tr_b16 a[36:37], v7 offset:55392\n\t" \
        "v_xor_b32 v141, v173, v12\n\t" \
        "v_mfma_f32_16x16x32_bf16 v[54:57], a[0:3], v[98:101], v[54:57]\n\t" \
        "ds_read_b64_tr_b16 a[38:39], v7 offset:58464\n\t" \
        "v_cvt_pk_bf16_f32 v78, v118, v119\n\t" \
        "v_mfma_f32_16x16x32_bf16 v[34:37], a[4:7], v[94:97], v[34:37]\n\t" \
        "ds_read_b64_tr_b16 a[40:41], v6 offset:55424\n\t" \
        "ds_read_b64_tr_b16 a[42:43], v6 offset:58496\n\t" \
        "v_mfma_f32_16x16x32_bf16 v[58:61], a[4:7], v[98:101], v[58:61]\n\t" \
        "s_waitcnt vmcnt(6)\n\t" \
        "s_barrier\n\t" \
        "ds_read_b64_tr_b16 a[44:45], v7 offset:55456\n\t" \
        "v_cvt_pk_bf16_f32 v79, v120, v121\n\t" \
        "v_mfma_f32_16x16x32_bf16 v[38:41], a[8:11], v[94:97], v[38:41]\n\t" \
        "ds_read_b64_tr_b16 a[46:47], v7 offset:58528\n\t" \
        "ds_read_b64_tr_b16 a[96:97], v134 offset:0\n\t" \
        "v_mfma_f32_16x16x32_bf16 v[62:65], a[8:11], v[98:101], v[62:65]\n\t" \
        "ds_read_b64_tr_b16 a[98:99], v135 offset:0\n\t" \
        "v_cvt_pk_bf16_f32 v80, v122, v123\n\t" \
        "v_mfma_f32_16x16x32_bf16 v[42:45], a[12:15], v[94:97], v[42:45]\n\t" \
        "s_add_u32 m0, s39, 36864\n\t" \
        "s_add_u32 s57, s40, 98304\n\t" \
        "buffer_load_dwordx4 v3, s[24:27], s57 offen offset:0  lds\n\t" \
        "v_mfma_f32_16x16x32_bf16 v[66:69], a[12:15], v[98:101], v[66:69]\n\t" \
        "ds_read_b64_tr_b16 a[100:101], v138 offset:0\n\t" \
        "v_cvt_pk_bf16_f32 v81, v124, v125\n\t" \
        "v_mfma_f32_16x16x32_bf16 v[46:49], a[16:19], v[94:97], v[46:49]\n\t" \
        "ds_read_b64_tr_b16 a[102:103], v139 offset:0\n\t" \
        "ds_read_b64_tr_b16 a[104:105], v136 offset:0\n\t" \
        "v_mfma_f32_16x16x32_bf16 v[70:73], a[16:19], v[98:101], v[70:73]\n\t" \
        "ds_read_b64_tr_b16 a[106:107], v137 offset:0\n\t" \
        "v_cvt_pk_bf16_f32 v82, v126, v127\n\t" \
        "s_waitcnt lgkmcnt(11)\n\t" \
        "v_mfma_f32_16x16x32_bf16 v[50:53], a[20:23], v[94:97], v[50:53]\n\t" \
        "ds_read_b64_tr_b16 a[108:109], v140 offset:0\n\t" \
        "ds_read_b64_tr_b16 a[110:111], v141 offset:0\n\t" \
        "v_cvt_pk_bf16_f32 v83, v128, v129\n\t" \
        "v_mfma_f32_16x16x32_bf16 v[74:77], a[20:23], v[98:101], v[74:77]\n\t" \
        "ds_read_b64_tr_b16 a[48:49], v6 offset:0\n\t" \
        "v_cvt_pk_bf16_f32 v84, v130, v131\n\t" \
        "v_cvt_pk_bf16_f32 v85, v132, v133\n\t" \
        "v_mfma_f32_16x16x32_bf16 v[118:121], a[128:131], v[162:165], 0\n\t" \
        "buffer_load_dwordx4 v4, s[24:27], s57 offen offset:1024  lds\n\t" \
        "v_mfma_f32_16x16x32_bf16 v[122:125], a[132:135], v[162:165], 0\n\t" \
        "ds_read_b64_tr_b16 a[50:51], v6 offset:3072\n\t" \
        "ds_read_b64_tr_b16 a[52:53], v7 offset:32\n\t" \
        "v_mfma_f32_16x16x32_bf16 v[126:129], a[136:139], v[166:169], 0\n\t" \
        "ds_read_b64_tr_b16 a[54:55], v7 offset:3104\n\t" \
        "v_cvt_pk_bf16_f32 v86, v102, v103\n\t" \
        "v_mfma_f32_16x16x32_bf16 v[130:133], a[140:143], v[166:169], 0\n\t" \
        "ds_read_b64_tr_b16 a[56:57], v6 offset:64\n\t" \
        "v_and_b32 v154, v150, v25\n\t" \
        "v_mfma_f32_16x16x32_bf16 v[30:33], a[24:27], v[78:81], v[30:33]\n\t" \
        "ds_read_b64_tr_b16 a[58:59], v6 offset:3136\n\t" \
        "ds_read_b64_tr_b16 a[60:61], v7 offset:96\n\t" \
        "v_mfma_f32_16x16x32_bf16 v[54:57], a[24:27], v[82:85], v[54:57]\n\t" \
        "ds_read_b64_tr_b16 a[62:63], v7 offset:3168\n\t" \
        "v_and_b32 v155, v150, v26\n\t" \
        "v_mfma_f32_16x16x32_bf16 v[34:37], a[28:31], v[78:81], v[34:37]\n\t" \
        "buffer_load_dwordx4 v5, s[24:27], s57 offen offset:2048  lds\n\t" \
        "v_mfma_f32_16x16x32_bf16 v[58:61], a[28:31], v[82:85], v[58:61]\n\t" \
        "ds_read_b64_tr_b16 a[64:65], v6 offset:128\n\t" \
        "ds_read_b128 v[174:177], v9 offset:640\n\t" \
        "v_mfma_f32_16x16x32_bf16 v[38:41], a[32:35], v[78:81], v[38:41]\n\t" \
        "ds_read_b64_tr_b16 a[66:67], v6 offset:3200\n\t" \
        "v_and_b32 v156, v150, v27\n\t" \
        "v_mfma_f32_16x16x32_bf16 v[62:65], a[32:35], v[82:85], v[62:65]\n\t" \
        "ds_read_b64_tr_b16 a[68:69], v7 offset:160\n\t" \
        "ds_read_b64_tr_b16 a[70:71], v7 offset:3232\n\t" \
        "s_waitcnt lgkmcnt(12)\n\t" \
        "v_mfma_f32_16x16x32_bf16 v[42:45], a[36:39], v[78:81], v[42:45]\n\t" \
        "ds_read_b64_tr_b16 a[112:113], v134 offset:64\n\t" \
        "v_and_b32 v157, v150, v28\n\t" \
        "v_and_b32 v158, v151, v25\n\t" \
        "v_mfma_f32_16x16x32_bf16 v[66:69], a[36:39], v[82:85], v[66:69]\n\t" \
        "ds_read_b64_tr_b16 a[114:115], v135 offset:64\n\t" \
        "v_cvt_pk_bf16_f32 v87, v104, v105\n\t" \
        "v_mfma_f32_16x16x32_bf16 v[46:49], a[40:43], v[78:81], v[46:49]\n\t" \
        "ds_read_b64_tr_b16 a[116:117], v138 offset:64\n\t" \
        "ds_read_b64_tr_b16 a[118:119], v139 offset:64\n\t" \
        "v_cvt_pk_bf16_f32 v88, v106, v107\n\t" \
        "v_and_b32 v159, v151, v26\n\t" \
        "v_mfma_f32_16x16x32_bf16 v[70:73], a[40:43], v[82:85], v[70:73]\n\t" \
        "ds_read_b64_tr_b16 a[120:121], v136 offset:64\n\t" \
        "v_cvt_pk_bf16_f32 v89, v108, v109\n\t" \
        "v_cvt_pk_bf16_f32 v90, v110, v111\n\t" \
        "v_mfma_f32_16x16x32_bf16 v[50:53], a[44:47], v[78:81], v[50:53]\n\t" \
        "ds_read_b64_tr_b16 a[122:123], v137 offset:64\n\t" \
        "ds_read_b64_tr_b16 a[124:125], v140 offset:64\n\t" \
        "v_and_b32 v160, v151, v27\n\t" \
        "v_mfma_f32_16x16x32_bf16 v[74:77], a[44:47], v[82:85], v[74:77]\n\t" \
        "ds_read_b64_tr_b16 a[126:127], v141 offset:64\n\t" \
        "v_cvt_pk_bf16_f32 v91, v112, v113\n\t" \
        "v_cvt_pk_bf16_f32 v92, v114, v115\n\t" \
        "v_mfma_f32_16x16x32_bf16 v[102:105], a[96:99], v[154:157], 0\n\t" \
        "ds_read_b64_tr_b16 a[72:73], v6 offset:6144\n\t" \
        "v_and_b32 v161, v151, v28\n\t" \
        "v_cvt_pk_bf16_f32 v93, v116, v117\n\t" \
        "v_mfma_f32_16x16x32_bf16 v[106:109], a[100:103], v[154:157], 0\n\t" \
        "ds_read_b64_tr_b16 a[74:75], v6 offset:9216\n\t" \
        "ds_read_b64_tr_b16 a[76:77], v7 offset:6176\n\t" \
        "v_mfma_f32_16x16x32_bf16 v[110:113], a[104:107], v[158:161], 0\n\t" \
        "ds_read_b64_tr_b16 a[78:79], v7 offset:9248\n\t" \
        "v_cvt_pk_bf16_f32 v94, v118, v119\n\t" \
        "v_mfma_f32_16x16x32_bf16 v[114:117], a[108:111], v[158:161], 0\n\t" \
        "ds_read_b64_tr_b16 a[80:81], v6 offset:6208\n\t" \
        "ds_read_b64_tr_b16 a[82:83], v6 offset:9280\n\t" \
        "s_waitcnt lgkmcnt(13)\n\t" \
        "v_mfma_f32_16x16x32_bf16 v[30:33], a[48:51], v[86:89], v[30:33]\n\t" \
        "ds_read_b64_tr_b16 a[84:85], v7 offset:6240\n\t" \
        "v_cvt_pk_bf16_f32 v95, v120, v121\n\t" \
        "v_mfma_f32_16x16x32_bf16 v[54:57], a[48:51], v[90:93], v[54:57]\n\t" \
        "ds_read_b64_tr_b16 a[86:87], v7 offset:9312\n\t" \
        "ds_read_b64_tr_b16 a[88:89], v6 offset:6272\n\t" \
        "v_mfma_f32_16x16x32_bf16 v[34:37], a[52:55], v[86:89], v[34:37]\n\t" \
        "ds_read_b64_tr_b16 a[90:91], v6 offset:9344\n\t" \
        "v_cvt_pk_bf16_f32 v96, v122, v123\n\t" \
        "v_mfma_f32_16x16x32_bf16 v[58:61], a[52:55], v[90:93], v[58:61]\n\t" \
        "s_waitcnt vmcnt(6)\n\t" \
        "s_barrier\n\t" \
        "ds_read_b64_tr_b16 a[92:93], v7 offset:6304\n\t" \
        "v_cvt_pk_bf16_f32 v97, v124, v125\n\t" \
        "v_mfma_f32_16x16x32_bf16 v[38:41], a[56:59], v[86:89], v[38:41]\n\t" \
        "ds_read_b64_tr_b16 a[94:95], v7 offset:9376\n\t" \
        "ds_read_b64_tr_b16 a[128:129], v134 offset:128\n\t" \
        "v_mfma_f32_16x16x32_bf16 v[62:65], a[56:59], v[90:93], v[62:65]\n\t" \
        "ds_read_b64_tr_b16 a[130:131], v135 offset:128\n\t" \
        "v_cvt_pk_bf16_f32 v98, v126, v127\n\t" \
        "v_mfma_f32_16x16x32_bf16 v[42:45], a[60:63], v[86:89], v[42:45]\n\t" \
        "s_add_u32 m0, s39, 49152\n\t" \
        "s_add_u32 s57, s40, 110592\n\t" \
        "buffer_load_dwordx4 v3, s[24:27], s57 offen offset:0  lds\n\t" \
        "v_mfma_f32_16x16x32_bf16 v[66:69], a[60:63], v[90:93], v[66:69]\n\t" \
        "ds_read_b64_tr_b16 a[132:133], v138 offset:128\n\t" \
        "ds_read_b64_tr_b16 a[134:135], v139 offset:128\n\t" \
        "v_mfma_f32_16x16x32_bf16 v[46:49], a[64:67], v[86:89], v[46:49]\n\t" \
        "ds_read_b64_tr_b16 a[136:137], v136 offset:128\n\t" \
        "v_xor_b32 v142, v174, v11\n\t" \
        "v_mfma_f32_16x16x32_bf16 v[70:73], a[64:67], v[90:93], v[70:73]\n\t" \
        "ds_read_b64_tr_b16 a[138:139], v137 offset:128\n\t" \
        "ds_read_b64_tr_b16 a[140:141], v140 offset:128\n\t" \
        "v_cvt_pk_bf16_f32 v99, v128, v129\n\t" \
        "v_mfma_f32_16x16x32_bf16 v[50:53], a[68:71], v[86:89], v[50:53]\n\t" \
        "ds_read_b64_tr_b16 a[142:143], v141 offset:128\n\t" \
        "v_cvt_pk_bf16_f32 v100, v130, v131\n\t" \
        "v_xor_b32 v143, v175, v11\n\t" \
        "v_mfma_f32_16x16x32_bf16 v[74:77], a[68:71], v[90:93], v[74:77]\n\t" \
        "ds_read_b64_tr_b16 a[0:1], v6 offset:12288\n\t" \
        "v_cvt_pk_bf16_f32 v101, v132, v133\n\t" \
        "s_waitcnt lgkmcnt(11)\n\t" \
        "v_mfma_f32_16x16x32_bf16 v[118:121], a[112:115], v[154:157], 0\n\t" \
        "buffer_load_dwordx4 v4, s[24:27], s57 offen offset:1024  lds\n\t" \
        "v_mfma_f32_16x16x32_bf16 v[122:125], a[116:119], v[154:157], 0\n\t" \
        "ds_read_b64_tr_b16 a[2:3], v6 offset:15360\n\t" \
        "ds_read_b64_tr_b16 a[4:5], v7 offset:12320\n\t" \
        "v_xor_b32 v144, v176, v11\n\t" \
        "v_xor_b32 v146, v174, v12\n\t" \
        "v_mfma_f32_16x16x32_bf16 v[126:129], a[120:123], v[158:161], 0\n\t" \
        "ds_read_b64 v[152:153], v10 offset:640\n\t" \
        "v_xor_b32 v145, v177, v11\n\t" \
        "v_xor_b32 v147, v175, v12\n\t" \
        "v_mfma_f32_16x16x32_bf16 v[130:133], a[124:127], v[158:161], 0\n\t" \
        "ds_read_b64_tr_b16 a[6:7], v7 offset:15392\n\t" \
        "ds_read_b64_tr_b16 a[8:9], v6 offset:12352\n\t" \
        "v_xor_b32 v148, v176, v12\n\t" \
        "v_mfma_f32_16x16x32_bf16 v[30:33], a[72:75], v[94:97], v[30:33]\n\t" \
        "ds_read_b64_tr_b16 a[10:11], v6 offset:15424\n\t" \
        "v_xor_b32 v149, v177, v12\n\t" \
        "v_mfma_f32_16x16x32_bf16 v[54:57], a[72:75], v[98:101], v[54:57]\n\t" \
        "ds_read_b64_tr_b16 a[12:13], v7 offset:12384\n\t" \
        "ds_read_b64_tr_b16 a[14:15], v7 offset:15456\n\t" \
        "v_mfma_f32_16x16x32_bf16 v[34:37], a[76:79], v[94:97], v[34:37]\n\t" \
        "buffer_load_dwordx4 v5, s[24:27], s57 offen offset:2048  lds\n\t" \
        "v_mfma_f32_16x16x32_bf16 v[58:61], a[76:79], v[98:101], v[58:61]\n\t" \
        "ds_read_b64_tr_b16 a[16:17], v6 offset:12416\n\t" \
        "v_cvt_pk_bf16_f32 v78, v102, v103\n\t" \
        "v_mfma_f32_16x16x32_bf16 v[38:41], a[80:83], v[94:97], v[38:41]\n\t" \
        "ds_read_b64_tr_b16 a[18:19], v6 offset:15488\n\t" \
        "v_cvt_pk_bf16_f32 v79, v104, v105\n\t" \
        "v_mfma_f32_16x16x32_bf16 v[62:65], a[80:83], v[98:101], v[62:65]\n\t" \
        "ds_read_b64_tr_b16 a[20:21], v7 offset:12448\n\t" \
        "ds_read_b64_tr_b16 a[22:23], v7 offset:15520\n\t" \
        "v_mfma_f32_16x16x32_bf16 v[42:45], a[84:87], v[94:97], v[42:45]\n\t" \
        "ds_read_b64_tr_b16 a[96:97], v142 offset:0\n\t" \
        "v_cvt_pk_bf16_f32 v80, v106, v107\n\t" \
        "v_mfma_f32_16x16x32_bf16 v[66:69], a[84:87], v[98:101], v[66:69]\n\t" \
        "ds_read_b64_tr_b16 a[98:99], v143 offset:0\n\t" \
        "ds_read_b64_tr_b16 a[100:101], v146 offset:0\n\t" \
        "v_mfma_f32_16x16x32_bf16 v[46:49], a[88:91], v[94:97], v[46:49]\n\t" \
        "ds_read_b64_tr_b16 a[102:103], v147 offset:0\n\t" \
        "v_cvt_pk_bf16_f32 v81, v108, v109\n\t" \
        "v_mfma_f32_16x16x32_bf16 v[70:73], a[88:91], v[98:101], v[70:73]\n\t" \
        "ds_read_b64_tr_b16 a[104:105], v144 offset:0\n\t" \
        "ds_read_b64_tr_b16 a[106:107], v145 offset:0\n\t" \
        "s_waitcnt lgkmcnt(12)\n\t" \
        "v_mfma_f32_16x16x32_bf16 v[50:53], a[92:95], v[94:97], v[50:53]\n\t" \
        "ds_read_b64_tr_b16 a[108:109], v148 offset:0\n\t" \
        "v_cvt_pk_bf16_f32 v82, v110, v111\n\t" \
        "v_mfma_f32_16x16x32_bf16 v[74:77], a[92:95], v[98:101], v[74:77]\n\t" \
        "ds_read_b64_tr_b16 a[110:111], v149 offset:0\n\t" \
        "v_cvt_pk_bf16_f32 v83, v112, v113\n\t" \
        "v_cvt_pk_bf16_f32 v84, v114, v115\n\t" \
        "v_mfma_f32_16x16x32_bf16 v[102:105], a[128:131], v[154:157], 0\n\t" \
        "ds_read_b64_tr_b16 a[24:25], v6 offset:18432\n\t" \
        "ds_read_b64_tr_b16 a[26:27], v6 offset:21504\n\t" \
        "v_cvt_pk_bf16_f32 v85, v116, v117\n\t" \
        "v_mfma_f32_16x16x32_bf16 v[106:109], a[132:135], v[154:157], 0\n\t" \
        "ds_read_b64_tr_b16 a[28:29], v7 offset:18464\n\t" \
        "v_cvt_pk_bf16_f32 v86, v118, v119\n\t" \
        "v_mfma_f32_16x16x32_bf16 v[110:113], a[136:139], v[158:161], 0\n\t" \
        "ds_read_b64_tr_b16 a[30:31], v7 offset:21536\n\t" \
        "ds_read_b64_tr_b16 a[32:33], v6 offset:18496\n\t" \
        "v_mfma_f32_16x16x32_bf16 v[114:117], a[140:143], v[158:161], 0\n\t" \
        "ds_read_b64_tr_b16 a[34:35], v6 offset:21568\n\t" \
        "v_cvt_pk_bf16_f32 v87, v120, v121\n\t" \
        "v_mfma_f32_16x16x32_bf16 v[30:33], a[0:3], v[78:81], v[30:33]\n\t" \
        "ds_read_b64_tr_b16 a[36:37], v7 offset:18528\n\t" \
        "ds_read_b64_tr_b16 a[38:39], v7 offset:21600\n\t" \
        "v_mfma_f32_16x16x32_bf16 v[54:57], a[0:3], v[82:85], v[54:57]\n\t" \
        "ds_read_b64_tr_b16 a[40:41], v6 offset:18560\n\t" \
        "v_and_b32 v162, v152, v25\n\t" \
        "v_mfma_f32_16x16x32_bf16 v[34:37], a[4:7], v[78:81], v[34:37]\n\t" \
        "ds_read_b128 v[170:173], v9 offset:768\n\t" \
        "ds_read_b64_tr_b16 a[42:43], v6 offset:21632\n\t" \
        "v_mfma_f32_16x16x32_bf16 v[58:61], a[4:7], v[82:85], v[58:61]\n\t" \
        "s_waitcnt vmcnt(6)\n\t" \
        "s_barrier\n\t" \
        "ds_read_b64_tr_b16 a[44:45], v7 offset:18592\n\t" \
        "v_and_b32 v163, v152, v26\n\t" \
        "v_mfma_f32_16x16x32_bf16 v[38:41], a[8:11], v[78:81], v[38:41]\n\t" \
        "ds_read_b64_tr_b16 a[46:47], v7 offset:21664\n\t" \
        "v_and_b32 v164, v152, v27\n\t" \
        "v_and_b32 v165, v152, v28\n\t" \
        "v_mfma_f32_16x16x32_bf16 v[62:65], a[8:11], v[82:85], v[62:65]\n\t" \
        "ds_read_b64_tr_b16 a[112:113], v142 offset:64\n\t" \
        "ds_read_b64_tr_b16 a[114:115], v143 offset:64\n\t" \
        "v_and_b32 v166, v153, v25\n\t" \
        "s_waitcnt lgkmcnt(13)\n\t" \
        "v_mfma_f32_16x16x32_bf16 v[42:45], a[12:15], v[78:81], v[42:45]\n\t" \
        "s_add_u32 m0, s39, 0\n\t" \
        "s_add_u32 s57, s40, 122880\n\t" \
        "buffer_load_dwordx4 v3, s[24:27], s57 offen offset:0  lds\n\t" \
        "v_mfma_f32_16x16x32_bf16 v[66:69], a[12:15], v[82:85], v[66:69]\n\t" \
        "ds_read_b64_tr_b16 a[116:117], v146 offset:64\n\t" \
        "v_cvt_pk_bf16_f32 v88, v122, v123\n\t" \
        "v_and_b32 v167, v153, v26\n\t" \
        "v_mfma_f32_16x16x32_bf16 v[46:49], a[16:19], v[78:81], v[46:49]\n\t" \
        "ds_read_b64_tr_b16 a[118:119], v147 offset:64\n\t" \
        "ds_read_b64_tr_b16 a[120:121], v144 offset:64\n\t" \
        "v_cvt_pk_bf16_f32 v89, v124, v125\n\t" \
        "v_cvt_pk_bf16_f32 v90, v126, v127\n\t" \
        "v_mfma_f32_16x16x32_bf16 v[70:73], a[16:19], v[82:85], v[70:73]\n\t" \
        "ds_read_b64_tr_b16 a[122:123], v145 offset:64\n\t" \
        "v_and_b32 v168, v153, v27\n\t" \
        "v_mfma_f32_16x16x32_bf16 v[50:53], a[20:23], v[78:81], v[50:53]\n\t" \
        "ds_read_b64_tr_b16 a[124:125], v148 offset:64\n\t" \
        "ds_read_b64_tr_b16 a[126:127], v149 offset:64\n\t" \
        "v_cvt_pk_bf16_f32 v91, v128, v129\n\t" \
        "v_cvt_pk_bf16_f32 v92, v130, v131\n\t" \
        "v_mfma_f32_16x16x32_bf16 v[74:77], a[20:23], v[82:85], v[74:77]\n\t" \
        "ds_read_b64_tr_b16 a[48:49], v6 offset:24576\n\t" \
        "v_and_b32 v169, v153, v28\n\t" \
        "v_cvt_pk_bf16_f32 v93, v132, v133\n\t" \
        "v_mfma_f32_16x16x32_bf16 v[118:121], a[96:99], v[162:165], 0\n\t" \
        "buffer_load_dwordx4 v4, s[24:27], s57 offen offset:1024  lds\n\t" \
        "v_mfma_f32_16x16x32_bf16 v[122:125], a[100:103], v[162:165], 0\n\t" \
        "ds_read_b64_tr_b16 a[50:51], v6 offset:27648\n\t" \
        "ds_read_b64_tr_b16 a[52:53], v7 offset:24608\n\t" \
        "v_mfma_f32_16x16x32_bf16 v[126:129], a[104:107], v[166:169], 0\n\t" \
        "ds_read_b64_tr_b16 a[54:55], v7 offset:27680\n\t" \
        "v_cvt_pk_bf16_f32 v94, v102, v103\n\t" \
        "v_mfma_f32_16x16x32_bf16 v[130:133], a[108:111], v[166:169], 0\n\t" \
        "ds_read_b64_tr_b16 a[56:57], v6 offset:24640\n\t" \
        "v_cvt_pk_bf16_f32 v95, v104, v105\n\t" \
        "v_mfma_f32_16x16x32_bf16 v[30:33], a[24:27], v[86:89], v[30:33]\n\t" \
        "ds_read_b64_tr_b16 a[58:59], v6 offset:27712\n\t" \
        "ds_read_b64_tr_b16 a[60:61], v7 offset:24672\n\t" \
        "v_mfma_f32_16x16x32_bf16 v[54:57], a[24:27], v[90:93], v[54:57]\n\t" \
        "ds_read_b64_tr_b16 a[62:63], v7 offset:27744\n\t" \
        "v_cvt_pk_bf16_f32 v96, v106, v107\n\t" \
        "s_waitcnt lgkmcnt(11)\n\t" \
        "v_mfma_f32_16x16x32_bf16 v[34:37], a[28:31], v[86:89], v[34:37]\n\t" \
        "buffer_load_dwordx4 v5, s[24:27], s57 offen offset:2048  lds\n\t" \
        "v_mfma_f32_16x16x32_bf16 v[58:61], a[28:31], v[90:93], v[58:61]\n\t" \
        "ds_read_b64_tr_b16 a[64:65], v6 offset:24704\n\t" \
        "ds_read_b64_tr_b16 a[66:67], v6 offset:27776\n\t" \
        "v_mfma_f32_16x16x32_bf16 v[38:41], a[32:35], v[86:89], v[38:41]\n\t" \
        "ds_read_b64_tr_b16 a[68:69], v7 offset:24736\n\t" \
        "v_cvt_pk_bf16_f32 v97, v108, v109\n\t" \
        "v_mfma_f32_16x16x32_bf16 v[62:65], a[32:35], v[90:93], v[62:65]\n\t" \
        "ds_read_b64_tr_b16 a[70:71], v7 offset:27808\n\t" \
        "v_cvt_pk_bf16_f32 v98, v110, v111\n\t" \
        "v_mfma_f32_16x16x32_bf16 v[42:45], a[36:39], v[86:89], v[42:45]\n\t" \
        "ds_read_b64_tr_b16 a[128:129], v142 offset:128\n\t" \
        "ds_read_b64_tr_b16 a[130:131], v143 offset:128\n\t" \
        "v_mfma_f32_16x16x32_bf16 v[66:69], a[36:39], v[90:93], v[66:69]\n\t" \
        "ds_read_b64_tr_b16 a[132:133], v146 offset:128\n\t" \
        "v_xor_b32 v134, v170, v11\n\t" \
        "v_mfma_f32_16x16x32_bf16 v[46:49], a[40:43], v[86:89], v[46:49]\n\t" \
        "ds_read_b64_tr_b16 a[134:135], v147 offset:128\n\t" \
        "ds_read_b64_tr_b16 a[136:137], v144 offset:128\n\t" \
        "v_mfma_f32_16x16x32_bf16 v[70:73], a[40:43], v[90:93], v[70:73]\n\t" \
        "ds_read_b64_tr_b16 a[138:139], v145 offset:128\n\t" \
        "v_cvt_pk_bf16_f32 v99, v112, v113\n\t" \
        "v_mfma_f32_16x16x32_bf16 v[50:53], a[44:47], v[86:89], v[50:53]\n\t" \
        "ds_read_b64_tr_b16 a[140:141], v148 offset:128\n\t" \
        "v_cvt_pk_bf16_f32 v100, v114, v115\n\t" \
        "v_mfma_f32_16x16x32_bf16 v[74:77], a[44:47], v[90:93], v[74:77]\n\t" \
        "ds_read_b64_tr_b16 a[142:143], v149 offset:128\n\t" \
        "ds_read_b64_tr_b16 a[72:73], v6 offset:30720\n\t" \
        "v_xor_b32 v135, v171, v11\n\t" \
        "v_mfma_f32_16x16x32_bf16 v[102:105], a[112:115], v[162:165], 0\n\t" \
        "ds_read_b64_tr_b16 a[74:75], v6 offset:33792\n\t" \
        "v_cvt_pk_bf16_f32 v101, v116, v117\n\t" \
        "v_mfma_f32_16x16x32_bf16 v[106:109], a[116:119], v[162:165], 0\n\t" \
        "ds_read_b64_tr_b16 a[76:77], v7 offset:30752\n\t" \
        "ds_read_b64 v[150:151], v10 offset:768\n\t" \
        "v_xor_b32 v136, v172, v11\n\t" \
        "v_xor_b32 v138, v170, v12\n\t" \
        "s_waitcnt lgkmcnt(13)\n\t" \
        "v_mfma_f32_16x16x32_bf16 v[110:113], a[120:123], v[166:169], 0\n\t" \
        "ds_read_b64_tr_b16 a[78:79], v7 offset:33824\n\t" \
        "v_xor_b32 v137, v173, v11\n\t" \
        "v_xor_b32 v139, v171, v12\n\t" \
        "v_mfma_f32_16x16x32_bf16 v[114:117], a[124:127], v[166:169], 0\n\t" \
        "ds_read_b64_tr_b16 a[80:81], v6 offset:30784\n\t" \
        "ds_read_b64_tr_b16 a[82:83], v6 offset:33856\n\t" \
        "v_xor_b32 v140, v172, v12\n\t" \
        "v_mfma_f32_16x16x32_bf16 v[30:33], a[48:51], v[94:97], v[30:33]\n\t" \
        "ds_read_b64_tr_b16 a[84:85], v7 offset:30816\n\t" \
        "v_xor_b32 v141, v173, v12\n\t" \
        "v_mfma_f32_16x16x32_bf16 v[54:57], a[48:51], v[98:101], v[54:57]\n\t" \
        "ds_read_b64_tr_b16 a[86:87], v7 offset:33888\n\t" \
        "v_cvt_pk_bf16_f32 v78, v118, v119\n\t" \
        "v_mfma_f32_16x16x32_bf16 v[34:37], a[52:55], v[94:97], v[34:37]\n\t" \
        "ds_read_b64_tr_b16 a[88:89], v6 offset:30848\n\t" \
        "ds_read_b64_tr_b16 a[90:91], v6 offset:33920\n\t" \
        "v_mfma_f32_16x16x32_bf16 v[58:61], a[52:55], v[98:101], v[58:61]\n\t" \
        "s_waitcnt vmcnt(6)\n\t" \
        "s_barrier\n\t" \
        "ds_read_b64_tr_b16 a[92:93], v7 offset:30880\n\t" \
        "v_cvt_pk_bf16_f32 v79, v120, v121\n\t" \
        "v_mfma_f32_16x16x32_bf16 v[38:41], a[56:59], v[94:97], v[38:41]\n\t" \
        "ds_read_b64_tr_b16 a[94:95], v7 offset:33952\n\t" \
        "ds_read_b64_tr_b16 a[96:97], v134 offset:0\n\t" \
        "v_mfma_f32_16x16x32_bf16 v[62:65], a[56:59], v[98:101], v[62:65]\n\t" \
        "ds_read_b64_tr_b16 a[98:99], v135 offset:0\n\t" \
        "v_cvt_pk_bf16_f32 v80, v122, v123\n\t" \
        "v_mfma_f32_16x16x32_bf16 v[42:45], a[60:63], v[94:97], v[42:45]\n\t" \
        "s_add_u32 m0, s39, 12288\n\t" \
        "s_add_u32 s57, s40, 135168\n\t" \
        "buffer_load_dwordx4 v3, s[24:27], s57 offen offset:0  lds\n\t" \
        "v_mfma_f32_16x16x32_bf16 v[66:69], a[60:63], v[98:101], v[66:69]\n\t" \
        "ds_read_b64_tr_b16 a[100:101], v138 offset:0\n\t" \
        "v_cvt_pk_bf16_f32 v81, v124, v125\n\t" \
        "v_mfma_f32_16x16x32_bf16 v[46:49], a[64:67], v[94:97], v[46:49]\n\t" \
        "ds_read_b64_tr_b16 a[102:103], v139 offset:0\n\t" \
        "ds_read_b64_tr_b16 a[104:105], v136 offset:0\n\t" \
        "v_mfma_f32_16x16x32_bf16 v[70:73], a[64:67], v[98:101], v[70:73]\n\t" \
        "ds_read_b64_tr_b16 a[106:107], v137 offset:0\n\t" \
        "v_cvt_pk_bf16_f32 v82, v126, v127\n\t" \
        "s_waitcnt lgkmcnt(11)\n\t" \
        "v_mfma_f32_16x16x32_bf16 v[50:53], a[68:71], v[94:97], v[50:53]\n\t" \
        "ds_read_b64_tr_b16 a[108:109], v140 offset:0\n\t" \
        "ds_read_b64_tr_b16 a[110:111], v141 offset:0\n\t" \
        "v_cvt_pk_bf16_f32 v83, v128, v129\n\t" \
        "v_mfma_f32_16x16x32_bf16 v[74:77], a[68:71], v[98:101], v[74:77]\n\t" \
        "ds_read_b64_tr_b16 a[0:1], v6 offset:36864\n\t" \
        "v_cvt_pk_bf16_f32 v84, v130, v131\n\t" \
        "v_cvt_pk_bf16_f32 v85, v132, v133\n\t" \
        "v_mfma_f32_16x16x32_bf16 v[118:121], a[128:131], v[162:165], 0\n\t" \
        "buffer_load_dwordx4 v4, s[24:27], s57 offen offset:1024  lds\n\t" \
        "v_mfma_f32_16x16x32_bf16 v[122:125], a[132:135], v[162:165], 0\n\t" \
        "ds_read_b64_tr_b16 a[2:3], v6 offset:39936\n\t" \
        "ds_read_b64_tr_b16 a[4:5], v7 offset:36896\n\t" \
        "v_mfma_f32_16x16x32_bf16 v[126:129], a[136:139], v[166:169], 0\n\t" \
        "ds_read_b64_tr_b16 a[6:7], v7 offset:39968\n\t" \
        "v_cvt_pk_bf16_f32 v86, v102, v103\n\t" \
        "v_mfma_f32_16x16x32_bf16 v[130:133], a[140:143], v[166:169], 0\n\t" \
        "ds_read_b64_tr_b16 a[8:9], v6 offset:36928\n\t" \
        "v_and_b32 v154, v150, v25\n\t" \
        "v_mfma_f32_16x16x32_bf16 v[30:33], a[72:75], v[78:81], v[30:33]\n\t" \
        "ds_read_b64_tr_b16 a[10:11], v6 offset:40000\n\t" \
        "ds_read_b64_tr_b16 a[12:13], v7 offset:36960\n\t" \
        "v_mfma_f32_16x16x32_bf16 v[54:57], a[72:75], v[82:85], v[54:57]\n\t" \
        "ds_read_b64_tr_b16 a[14:15], v7 offset:40032\n\t" \
        "v_and_b32 v155, v150, v26\n\t" \
        "v_mfma_f32_16x16x32_bf16 v[34:37], a[76:79], v[78:81], v[34:37]\n\t" \
        "buffer_load_dwordx4 v5, s[24:27], s57 offen offset:2048  lds\n\t" \
        "v_mfma_f32_16x16x32_bf16 v[58:61], a[76:79], v[82:85], v[58:61]\n\t" \
        "ds_read_b64_tr_b16 a[16:17], v6 offset:36992\n\t" \
        "ds_read_b128 v[174:177], v9 offset:896\n\t" \
        "v_mfma_f32_16x16x32_bf16 v[38:41], a[80:83], v[78:81], v[38:41]\n\t" \
        "ds_read_b64_tr_b16 a[18:19], v6 offset:40064\n\t" \
        "v_and_b32 v156, v150, v27\n\t" \
        "v_mfma_f32_16x16x32_bf16 v[62:65], a[80:83], v[82:85], v[62:65]\n\t" \
        "ds_read_b64_tr_b16 a[20:21], v7 offset:37024\n\t" \
        "v_and_b32 v157, v150, v28\n\t" \
        "s_waitcnt lgkmcnt(11)\n\t" \
        "v_mfma_f32_16x16x32_bf16 v[42:45], a[84:87], v[78:81], v[42:45]\n\t" \
        "ds_read_b64_tr_b16 a[22:23], v7 offset:40096\n\t" \
        "ds_read_b64_tr_b16 a[112:113], v134 offset:64\n\t" \
        "v_and_b32 v158, v151, v25\n\t" \
        "v_mfma_f32_16x16x32_bf16 v[66:69], a[84:87], v[82:85], v[66:69]\n\t" \
        "ds_read_b64_tr_b16 a[114:115], v135 offset:64\n\t" \
        "v_cvt_pk_bf16_f32 v87, v104, v105\n\t" \
        "v_mfma_f32_16x16x32_bf16 v[46:49], a[88:91], v[78:81], v[46:49]\n\t" \
        "ds_read_b64_tr_b16 a[116:117], v138 offset:64\n\t" \
        "ds_read_b64_tr_b16 a[118:119], v139 offset:64\n\t" \
        "v_cvt_pk_bf16_f32 v88, v106, v107\n\t" \
        "v_and_b32 v159, v151, v26\n\t" \
        "v_mfma_f32_16x16x32_bf16 v[70:73], a[88:91], v[82:85], v[70:73]\n\t" \
        "ds_read_b64_tr_b16 a[120:121], v136 offset:64\n\t" \
        "v_cvt_pk_bf16_f32 v89, v108, v109\n\t" \
        "v_cvt_pk_bf16_f32 v90, v110, v111\n\t" \
        "v_mfma_f32_16x16x32_bf16 v[50:53], a[92:95], v[78:81], v[50:53]\n\t" \
        "ds_read_b64_tr_b16 a[122:123], v137 offset:64\n\t" \
        "ds_read_b64_tr_b16 a[124:125], v140 offset:64\n\t" \
        "v_and_b32 v160, v151, v27\n\t" \
        "v_mfma_f32_16x16x32_bf16 v[74:77], a[92:95], v[82:85], v[74:77]\n\t" \
        "ds_read_b64_tr_b16 a[126:127], v141 offset:64\n\t" \
        "v_cvt_pk_bf16_f32 v91, v112, v113\n\t" \
        "v_cvt_pk_bf16_f32 v92, v114, v115\n\t" \
        "v_mfma_f32_16x16x32_bf16 v[102:105], a[96:99], v[154:157], 0\n\t" \
        "ds_read_b64_tr_b16 a[24:25], v6 offset:43008\n\t" \
        "v_and_b32 v161, v151, v28\n\t" \
        "v_cvt_pk_bf16_f32 v93, v116, v117\n\t" \
        "v_mfma_f32_16x16x32_bf16 v[106:109], a[100:103], v[154:157], 0\n\t" \
        "ds_read_b64_tr_b16 a[26:27], v6 offset:46080\n\t" \
        "ds_read_b64_tr_b16 a[28:29], v7 offset:43040\n\t" \
        "v_mfma_f32_16x16x32_bf16 v[110:113], a[104:107], v[158:161], 0\n\t" \
        "ds_read_b64_tr_b16 a[30:31], v7 offset:46112\n\t" \
        "v_cvt_pk_bf16_f32 v94, v118, v119\n\t" \
        "v_mfma_f32_16x16x32_bf16 v[114:117], a[108:111], v[158:161], 0\n\t" \
        "ds_read_b64_tr_b16 a[32:33], v6 offset:43072\n\t" \
        "ds_read_b64_tr_b16 a[34:35], v6 offset:46144\n\t" \
        "s_waitcnt lgkmcnt(13)\n\t" \
        "v_mfma_f32_16x16x32_bf16 v[30:33], a[0:3], v[86:89], v[30:33]\n\t" \
        "ds_read_b64_tr_b16 a[36:37], v7 offset:43104\n\t" \
        "v_cvt_pk_bf16_f32 v95, v120, v121\n\t" \
        "v_mfma_f32_16x16x32_bf16 v[54:57], a[0:3], v[90:93], v[54:57]\n\t" \
        "ds_read_b64_tr_b16 a[38:39], v7 offset:46176\n\t" \
        "ds_read_b64_tr_b16 a[40:41], v6 offset:43136\n\t" \
        "v_mfma_f32_16x16x32_bf16 v[34:37], a[4:7], v[86:89], v[34:37]\n\t" \
        "ds_read_b64_tr_b16 a[42:43], v6 offset:46208\n\t" \
        "v_cvt_pk_bf16_f32 v96, v122, v123\n\t" \
        "v_mfma_f32_16x16x32_bf16 v[58:61], a[4:7], v[90:93], v[58:61]\n\t" \
        "s_waitcnt vmcnt(6)\n\t" \
        "s_barrier\n\t" \
        "ds_read_b64_tr_b16 a[44:45], v7 offset:43168\n\t" \
        "v_cvt_pk_bf16_f32 v97, v124, v125\n\t" \
        "v_mfma_f32_16x16x32_bf16 v[38:41], a[8:11], v[86:89], v[38:41]\n\t" \
        "ds_read_b64_tr_b16 a[46:47], v7 offset:46240\n\t" \
        "ds_read_b64_tr_b16 a[128:129], v134 offset:128\n\t" \
        "v_mfma_f32_16x16x32_bf16 v[62:65], a[8:11], v[90:93], v[62:65]\n\t" \
        "ds_read_b64_tr_b16 a[130:131], v135 offset:128\n\t" \
        "v_cvt_pk_bf16_f32 v98, v126, v127\n\t" \
        "v_mfma_f32_16x16x32_bf16 v[42:45], a[12:15], v[86:89], v[42:45]\n\t" \
        "s_add_u32 m0, s39, 24576\n\t" \
        "s_add_u32 s57, s40, 147456\n\t" \
        "buffer_load_dwordx4 v3, s[24:27], s57 offen offset:0  lds\n\t" \
        "v_mfma_f32_16x16x32_bf16 v[66:69], a[12:15], v[90:93], v[66:69]\n\t" \
        "ds_read_b64_tr_b16 a[132:133], v138 offset:128\n\t" \
        "ds_read_b64_tr_b16 a[134:135], v139 offset:128\n\t" \
        "v_mfma_f32_16x16x32_bf16 v[46:49], a[16:19], v[86:89], v[46:49]\n\t" \
        "ds_read_b64_tr_b16 a[136:137], v136 offset:128\n\t" \
        "v_xor_b32 v142, v174, v11\n\t" \
        "v_mfma_f32_16x16x32_bf16 v[70:73], a[16:19], v[90:93], v[70:73]\n\t" \
        "ds_read_b64_tr_b16 a[138:139], v137 offset:128\n\t" \
        "ds_read_b64_tr_b16 a[140:141], v140 offset:128\n\t" \
        "v_cvt_pk_bf16_f32 v99, v128, v129\n\t" \
        "v_mfma_f32_16x16x32_bf16 v[50:53], a[20:23], v[86:89], v[50:53]\n\t" \
        "ds_read_b64_tr_b16 a[142:143], v141 offset:128\n\t" \
        "v_cvt_pk_bf16_f32 v100, v130, v131\n\t" \
        "v_xor_b32 v143, v175, v11\n\t" \
        "v_mfma_f32_16x16x32_bf16 v[74:77], a[20:23], v[90:93], v[74:77]\n\t" \
        "ds_read_b64_tr_b16 a[48:49], v6 offset:49152\n\t" \
        "v_cvt_pk_bf16_f32 v101, v132, v133\n\t" \
        "s_waitcnt lgkmcnt(11)\n\t" \
        "v_mfma_f32_16x16x32_bf16 v[118:121], a[112:115], v[154:157], 0\n\t" \
        "buffer_load_dwordx4 v4, s[24:27], s57 offen offset:1024  lds\n\t" \
        "v_mfma_f32_16x16x32_bf16 v[122:125], a[116:119], v[154:157], 0\n\t" \
        "ds_read_b64_tr_b16 a[50:51], v6 offset:52224\n\t" \
        "ds_read_b64_tr_b16 a[52:53], v7 offset:49184\n\t" \
        "v_xor_b32 v144, v176, v11\n\t" \
        "v_xor_b32 v146, v174, v12\n\t" \
        "v_mfma_f32_16x16x32_bf16 v[126:129], a[120:123], v[158:161], 0\n\t" \
        "ds_read_b64 v[152:153], v10 offset:896\n\t" \
        "v_xor_b32 v145, v177, v11\n\t" \
        "v_xor_b32 v147, v175, v12\n\t" \
        "v_mfma_f32_16x16x32_bf16 v[130:133], a[124:127], v[158:161], 0\n\t" \
        "ds_read_b64_tr_b16 a[54:55], v7 offset:52256\n\t" \
        "ds_read_b64_tr_b16 a[56:57], v6 offset:49216\n\t" \
        "v_xor_b32 v148, v176, v12\n\t" \
        "v_mfma_f32_16x16x32_bf16 v[30:33], a[24:27], v[94:97], v[30:33]\n\t" \
        "ds_read_b64_tr_b16 a[58:59], v6 offset:52288\n\t" \
        "v_xor_b32 v149, v177, v12\n\t" \
        "v_mfma_f32_16x16x32_bf16 v[54:57], a[24:27], v[98:101], v[54:57]\n\t" \
        "ds_read_b64_tr_b16 a[60:61], v7 offset:49248\n\t" \
        "ds_read_b64_tr_b16 a[62:63], v7 offset:52320\n\t" \
        "v_mfma_f32_16x16x32_bf16 v[34:37], a[28:31], v[94:97], v[34:37]\n\t" \
        "buffer_load_dwordx4 v5, s[24:27], s57 offen offset:2048  lds\n\t" \
        "v_mfma_f32_16x16x32_bf16 v[58:61], a[28:31], v[98:101], v[58:61]\n\t" \
        "ds_read_b64_tr_b16 a[64:65], v6 offset:49280\n\t" \
        "v_cvt_pk_bf16_f32 v78, v102, v103\n\t" \
        "v_mfma_f32_16x16x32_bf16 v[38:41], a[32:35], v[94:97], v[38:41]\n\t" \
        "ds_read_b64_tr_b16 a[66:67], v6 offset:52352\n\t" \
        "v_cvt_pk_bf16_f32 v79, v104, v105\n\t" \
        "v_mfma_f32_16x16x32_bf16 v[62:65], a[32:35], v[98:101], v[62:65]\n\t" \
        "ds_read_b64_tr_b16 a[68:69], v7 offset:49312\n\t" \
        "ds_read_b64_tr_b16 a[70:71], v7 offset:52384\n\t" \
        "v_mfma_f32_16x16x32_bf16 v[42:45], a[36:39], v[94:97], v[42:45]\n\t" \
        "ds_read_b64_tr_b16 a[96:97], v142 offset:0\n\t" \
        "v_cvt_pk_bf16_f32 v80, v106, v107\n\t" \
        "v_mfma_f32_16x16x32_bf16 v[66:69], a[36:39], v[98:101], v[66:69]\n\t" \
        "ds_read_b64_tr_b16 a[98:99], v143 offset:0\n\t" \
        "ds_read_b64_tr_b16 a[100:101], v146 offset:0\n\t" \
        "v_mfma_f32_16x16x32_bf16 v[46:49], a[40:43], v[94:97], v[46:49]\n\t" \
        "ds_read_b64_tr_b16 a[102:103], v147 offset:0\n\t" \
        "v_cvt_pk_bf16_f32 v81, v108, v109\n\t" \
        "v_mfma_f32_16x16x32_bf16 v[70:73], a[40:43], v[98:101], v[70:73]\n\t" \
        "ds_read_b64_tr_b16 a[104:105], v144 offset:0\n\t" \
        "ds_read_b64_tr_b16 a[106:107], v145 offset:0\n\t" \
        "s_waitcnt lgkmcnt(12)\n\t" \
        "v_mfma_f32_16x16x32_bf16 v[50:53], a[44:47], v[94:97], v[50:53]\n\t" \
        "ds_read_b64_tr_b16 a[108:109], v148 offset:0\n\t" \
        "v_cvt_pk_bf16_f32 v82, v110, v111\n\t" \
        "v_mfma_f32_16x16x32_bf16 v[74:77], a[44:47], v[98:101], v[74:77]\n\t" \
        "ds_read_b64_tr_b16 a[110:111], v149 offset:0\n\t" \
        "ds_read_b64_tr_b16 a[72:73], v6 offset:55296\n\t" \
        "v_cvt_pk_bf16_f32 v83, v112, v113\n\t" \
        "v_cvt_pk_bf16_f32 v84, v114, v115\n\t" \
        "v_mfma_f32_16x16x32_bf16 v[102:105], a[128:131], v[154:157], 0\n\t" \
        "ds_read_b64_tr_b16 a[74:75], v6 offset:58368\n\t" \
        "v_cvt_pk_bf16_f32 v85, v116, v117\n\t" \
        "v_mfma_f32_16x16x32_bf16 v[106:109], a[132:135], v[154:157], 0\n\t" \
        "ds_read_b64_tr_b16 a[76:77], v7 offset:55328\n\t" \
        "v_cvt_pk_bf16_f32 v86, v118, v119\n\t" \
        "v_mfma_f32_16x16x32_bf16 v[110:113], a[136:139], v[158:161], 0\n\t" \
        "ds_read_b64_tr_b16 a[78:79], v7 offset:58400\n\t" \
        "ds_read_b64_tr_b16 a[80:81], v6 offset:55360\n\t" \
        "v_mfma_f32_16x16x32_bf16 v[114:117], a[140:143], v[158:161], 0\n\t" \
        "ds_read_b64_tr_b16 a[82:83], v6 offset:58432\n\t" \
        "v_cvt_pk_bf16_f32 v87, v120, v121\n\t" \
        "v_mfma_f32_16x16x32_bf16 v[30:33], a[48:51], v[78:81], v[30:33]\n\t" \
        "ds_read_b64_tr_b16 a[84:85], v7 offset:55392\n\t" \
        "ds_read_b64_tr_b16 a[86:87], v7 offset:58464\n\t" \
        "v_mfma_f32_16x16x32_bf16 v[54:57], a[48:51], v[82:85], v[54:57]\n\t" \
        "ds_read_b64_tr_b16 a[88:89], v6 offset:55424\n\t" \
        "v_and_b32 v162, v152, v25\n\t" \
        "v_mfma_f32_16x16x32_bf16 v[34:37], a[52:55], v[78:81], v[34:37]\n\t" \
        "ds_read_b128 v[170:173], v9 offset:1024\n\t" \
        "ds_read_b64_tr_b16 a[90:91], v6 offset:58496\n\t" \
        "v_mfma_f32_16x16x32_bf16 v[58:61], a[52:55], v[82:85], v[58:61]\n\t" \
        "s_waitcnt vmcnt(6)\n\t" \
        "s_barrier\n\t" \
        "ds_read_b64_tr_b16 a[92:93], v7 offset:55456\n\t" \
        "v_and_b32 v163, v152, v26\n\t" \
        "v_mfma_f32_16x16x32_bf16 v[38:41], a[56:59], v[78:81], v[38:41]\n\t" \
        "ds_read_b64_tr_b16 a[94:95], v7 offset:58528\n\t" \
        "v_and_b32 v164, v152, v27\n\t" \
        "v_and_b32 v165, v152, v28\n\t" \
        "v_mfma_f32_16x16x32_bf16 v[62:65], a[56:59], v[82:85], v[62:65]\n\t" \
        "ds_read_b64_tr_b16 a[112:113], v142 offset:64\n\t" \
        "ds_read_b64_tr_b16 a[114:115], v143 offset:64\n\t" \
        "v_and_b32 v166, v153, v25\n\t" \
        "s_waitcnt lgkmcnt(13)\n\t" \
        "v_mfma_f32_16x16x32_bf16 v[42:45], a[60:63], v[78:81], v[42:45]\n\t" \
        "s_add_u32 m0, s39, 36864\n\t" \
        "s_add_u32 s57, s40, 159744\n\t" \
        "buffer_load_dwordx4 v3, s[24:27], s57 offen offset:0  lds\n\t" \
        "v_mfma_f32_16x16x32_bf16 v[66:69], a[60:63], v[82:85], v[66:69]\n\t" \
        "ds_read_b64_tr_b16 a[116:117], v146 offset:64\n\t" \
        "v_cvt_pk_bf16_f32 v88, v122, v123\n\t" \
        "v_and_b32 v167, v153, v26\n\t" \
        "v_mfma_f32_16x16x32_bf16 v[46:49], a[64:67], v[78:81], v[46:49]\n\t" \
        "ds_read_b64_tr_b16 a[118:119], v147 offset:64\n\t" \
        "ds_read_b64_tr_b16 a[120:121], v144 offset:64\n\t" \
        "v_cvt_pk_bf16_f32 v89, v124, v125\n\t" \
        "v_cvt_pk_bf16_f32 v90, v126, v127\n\t" \
        "v_mfma_f32_16x16x32_bf16 v[70:73], a[64:67], v[82:85], v[70:73]\n\t" \
        "ds_read_b64_tr_b16 a[122:123], v145 offset:64\n\t" \
        "v_and_b32 v168, v153, v27\n\t" \
        "v_mfma_f32_16x16x32_bf16 v[50:53], a[68:71], v[78:81], v[50:53]\n\t" \
        "ds_read_b64_tr_b16 a[124:125], v148 offset:64\n\t" \
        "ds_read_b64_tr_b16 a[126:127], v149 offset:64\n\t" \
        "v_cvt_pk_bf16_f32 v91, v128, v129\n\t" \
        "v_cvt_pk_bf16_f32 v92, v130, v131\n\t" \
        "v_mfma_f32_16x16x32_bf16 v[74:77], a[68:71], v[82:85], v[74:77]\n\t" \
        "ds_read_b64_tr_b16 a[0:1], v6 offset:0\n\t" \
        "v_and_b32 v169, v153, v28\n\t" \
        "v_cvt_pk_bf16_f32 v93, v132, v133\n\t" \
        "v_mfma_f32_16x16x32_bf16 v[118:121], a[96:99], v[162:165], 0\n\t" \
        "buffer_load_dwordx4 v4, s[24:27], s57 offen offset:1024  lds\n\t" \
        "v_mfma_f32_16x16x32_bf16 v[122:125], a[100:103], v[162:165], 0\n\t" \
        "ds_read_b64_tr_b16 a[2:3], v6 offset:3072\n\t" \
        "v_cvt_pk_bf16_f32 v94, v102, v103\n\t" \
        "v_mfma_f32_16x16x32_bf16 v[126:129], a[104:107], v[166:169], 0\n\t" \
        "ds_read_b64_tr_b16 a[4:5], v7 offset:32\n\t" \
        "ds_read_b64_tr_b16 a[6:7], v7 offset:3104\n\t" \
        "v_mfma_f32_16x16x32_bf16 v[130:133], a[108:111], v[166:169], 0\n\t" \
        "ds_read_b64_tr_b16 a[8:9], v6 offset:64\n\t" \
        "v_cvt_pk_bf16_f32 v95, v104, v105\n\t" \
        "v_mfma_f32_16x16x32_bf16 v[30:33], a[72:75], v[86:89], v[30:33]\n\t" \
        "ds_read_b64_tr_b16 a[10:11], v6 offset:3136\n\t" \
        "v_cvt_pk_bf16_f32 v96, v106, v107\n\t" \
        "v_mfma_f32_16x16x32_bf16 v[54:57], a[72:75], v[90:93], v[54:57]\n\t" \
        "ds_read_b64_tr_b16 a[12:13], v7 offset:96\n\t" \
        "ds_read_b64_tr_b16 a[14:15], v7 offset:3168\n\t" \
        "s_waitcnt lgkmcnt(11)\n\t" \
        "v_mfma_f32_16x16x32_bf16 v[34:37], a[76:79], v[86:89], v[34:37]\n\t" \
        "buffer_load_dwordx4 v5, s[24:27], s57 offen offset:2048  lds\n\t" \
        "v_mfma_f32_16x16x32_bf16 v[58:61], a[76:79], v[90:93], v[58:61]\n\t" \
        "ds_read_b64_tr_b16 a[16:17], v6 offset:128\n\t" \
        "v_cvt_pk_bf16_f32 v97, v108, v109\n\t" \
        "v_mfma_f32_16x16x32_bf16 v[38:41], a[80:83], v[86:89], v[38:41]\n\t" \
        "ds_read_b64_tr_b16 a[18:19], v6 offset:3200\n\t" \
        "v_cvt_pk_bf16_f32 v98, v110, v111\n\t" \
        "v_mfma_f32_16x16x32_bf16 v[62:65], a[80:83], v[90:93], v[62:65]\n\t" \
        "ds_read_b64_tr_b16 a[20:21], v7 offset:160\n\t" \
        "ds_read_b64_tr_b16 a[22:23], v7 offset:3232\n\t" \
        "v_mfma_f32_16x16x32_bf16 v[42:45], a[84:87], v[86:89], v[42:45]\n\t" \
        "ds_read_b64_tr_b16 a[128:129], v142 offset:128\n\t" \
        "v_xor_b32 v134, v170, v11\n\t" \
        "v_mfma_f32_16x16x32_bf16 v[66:69], a[84:87], v[90:93], v[66:69]\n\t" \
        "ds_read_b64_tr_b16 a[130:131], v143 offset:128\n\t" \
        "v_cvt_pk_bf16_f32 v99, v112, v113\n\t" \
        "v_mfma_f32_16x16x32_bf16 v[46:49], a[88:91], v[86:89], v[46:49]\n\t" \
        "ds_read_b64_tr_b16 a[132:133], v146 offset:128\n\t" \
        "ds_read_b64_tr_b16 a[134:135], v147 offset:128\n\t" \
        "v_mfma_f32_16x16x32_bf16 v[70:73], a[88:91], v[90:93], v[70:73]\n\t" \
        "ds_read_b64_tr_b16 a[136:137], v144 offset:128\n\t" \
        "v_cvt_pk_bf16_f32 v100, v114, v115\n\t" \
        "v_mfma_f32_16x16x32_bf16 v[50:53], a[92:95], v[86:89], v[50:53]\n\t" \
        "ds_read_b64_tr_b16 a[138:139], v145 offset:128\n\t" \
        "v_xor_b32 v135, v171, v11\n\t" \
        "v_mfma_f32_16x16x32_bf16 v[74:77], a[92:95], v[90:93], v[74:77]\n\t" \
        "ds_read_b64_tr_b16 a[140:141], v148 offset:128\n\t" \
        "ds_read_b64_tr_b16 a[142:143], v149 offset:128\n\t" \
        "v_mfma_f32_16x16x32_bf16 v[102:105], a[112:115], v[162:165], 0\n\t" \
        "ds_read_b64_tr_b16 a[24:25], v6 offset:6144\n\t" \
        "v_cvt_pk_bf16_f32 v101, v116, v117\n\t" \
        "v_mfma_f32_16x16x32_bf16 v[106:109], a[116:119], v[162:165], 0\n\t" \
        "ds_read_b64_tr_b16 a[26:27], v6 offset:9216\n\t" \
        "v_xor_b32 v136, v172, v11\n\t" \
        "v_xor_b32 v138, v170, v12\n\t" \
        "s_waitcnt lgkmcnt(12)\n\t" \
        "v_mfma_f32_16x16x32_bf16 v[110:113], a[120:123], v[166:169], 0\n\t" \
        "ds_read_b64_tr_b16 a[28:29], v7 offset:6176\n\t" \
        "ds_read_b64 v[150:151], v10 offset:1024\n\t" \
        "v_xor_b32 v137, v173, v11\n\t" \
        "v_xor_b32 v139, v171, v12\n\t" \
        "v_mfma_f32_16x16x32_bf16 v[114:117], a[124:127], v[166:169], 0\n\t" \
        "ds_read_b64_tr_b16 a[30:31], v7 offset:9248\n\t" \
        "v_xor_b32 v140, v172, v12\n\t" \
        "v_mfma_f32_16x16x32_bf16 v[30:33], a[0:3], v[94:97], v[30:33]\n\t" \
        "ds_read_b64_tr_b16 a[32:33], v6 offset:6208\n\t" \
        "ds_read_b64_tr_b16 a[34:35], v6 offset:9280\n\t" \
        "v_xor_b32 v141, v173, v12\n\t" \
        "v_mfma_f32_16x16x32_bf16 v[54:57], a[0:3], v[98:101], v[54:57]\n\t" \
        "ds_read_b64_tr_b16 a[36:37], v7 offset:6240\n\t" \
        "v_cvt_pk_bf16_f32 v78, v118, v119\n\t" \
        "v_mfma_f32_16x16x32_bf16 v[34:37], a[4:7], v[94:97], v[34:37]\n\t" \
        "ds_read_b64_tr_b16 a[38:39], v7 offset:9312\n\t" \
        "v_cvt_pk_bf16_f32 v79, v120, v121\n\t" \
        "v_mfma_f32_16x16x32_bf16 v[58:61], a[4:7], v[98:101], v[58:61]\n\t" \
        "s_waitcnt vmcnt(6)\n\t" \
        "s_barrier\n\t" \
        "ds_read_b64_tr_b16 a[40:41], v6 offset:6272\n\t" \
        "ds_read_b64_tr_b16 a[42:43], v6 offset:9344\n\t" \
        "v_mfma_f32_16x16x32_bf16 v[38:41], a[8:11], v[94:97], v[38:41]\n\t" \
        "ds_read_b64_tr_b16 a[44:45], v7 offset:6304\n\t" \
        "v_cvt_pk_bf16_f32 v80, v122, v123\n\t" \
        "v_mfma_f32_16x16x32_bf16 v[62:65], a[8:11], v[98:101], v[62:65]\n\t" \
        "ds_read_b64_tr_b16 a[46:47], v7 offset:9376\n\t" \
        "v_cvt_pk_bf16_f32 v81, v124, v125\n\t" \
        "v_mfma_f32_16x16x32_bf16 v[42:45], a[12:15], v[94:97], v[42:45]\n\t" \
        "ds_read_b64_tr_b16 a[96:97], v134 offset:0\n\t" \
        "ds_read_b64_tr_b16 a[98:99], v135 offset:0\n\t" \
        "v_mfma_f32_16x16x32_bf16 v[66:69], a[12:15], v[98:101], v[66:69]\n\t" \
        "ds_read_b64_tr_b16 a[100:101], v138 offset:0\n\t" \
        "v_cvt_pk_bf16_f32 v82, v126, v127\n\t" \
        "v_mfma_f32_16x16x32_bf16 v[46:49], a[16:19], v[94:97], v[46:49]\n\t" \
        "ds_read_b64_tr_b16 a[102:103], v139 offset:0\n\t" \
        "ds_read_b64_tr_b16 a[104:105], v136 offset:0\n\t" \
        "v_mfma_f32_16x16x32_bf16 v[70:73], a[16:19], v[98:101], v[70:73]\n\t" \
        "ds_read_b64_tr_b16 a[106:107], v137 offset:0\n\t" \
        "v_cvt_pk_bf16_f32 v83, v128, v129\n\t" \
        "s_waitcnt lgkmcnt(12)\n\t" \
        "v_mfma_f32_16x16x32_bf16 v[50:53], a[20:23], v[94:97], v[50:53]\n\t" \
        "ds_read_b64_tr_b16 a[108:109], v140 offset:0\n\t" \
        "v_cvt_pk_bf16_f32 v84, v130, v131\n\t" \
        "v_mfma_f32_16x16x32_bf16 v[74:77], a[20:23], v[98:101], v[74:77]\n\t" \
        "ds_read_b64_tr_b16 a[110:111], v141 offset:0\n\t" \
        "ds_read_b64_tr_b16 a[48:49], v6 offset:12288\n\t" \
        "v_mfma_f32_16x16x32_bf16 v[118:121], a[128:131], v[162:165], 0\n\t" \
        "ds_read_b64_tr_b16 a[50:51], v6 offset:15360\n\t" \
        "v_cvt_pk_bf16_f32 v85, v132, v133\n\t" \
        "v_mfma_f32_16x16x32_bf16 v[122:125], a[132:135], v[162:165], 0\n\t" \
        "ds_read_b64_tr_b16 a[52:53], v7 offset:12320\n\t" \
        "ds_read_b64_tr_b16 a[54:55], v7 offset:15392\n\t" \
        "v_mfma_f32_16x16x32_bf16 v[126:129], a[136:139], v[166:169], 0\n\t" \
        "ds_read_b64_tr_b16 a[56:57], v6 offset:12352\n\t" \
        "v_cvt_pk_bf16_f32 v86, v102, v103\n\t" \
        "v_mfma_f32_16x16x32_bf16 v[130:133], a[140:143], v[166:169], 0\n\t" \
        "ds_read_b64_tr_b16 a[58:59], v6 offset:15424\n\t" \
        "ds_read_b64_tr_b16 a[60:61], v7 offset:12384\n\t" \
        "v_mfma_f32_16x16x32_bf16 v[30:33], a[24:27], v[78:81], v[30:33]\n\t" \
        "ds_read_b64_tr_b16 a[62:63], v7 offset:15456\n\t" \
        "v_and_b32 v154, v150, v25\n\t" \
        "v_mfma_f32_16x16x32_bf16 v[54:57], a[24:27], v[82:85], v[54:57]\n\t" \
        "ds_read_b64_tr_b16 a[64:65], v6 offset:12416\n\t" \
        "ds_read_b64_tr_b16 a[66:67], v6 offset:15488\n\t" \
        "v_mfma_f32_16x16x32_bf16 v[34:37], a[28:31], v[78:81], v[34:37]\n\t" \
        "ds_read_b64_tr_b16 a[68:69], v7 offset:12448\n\t" \
        "v_and_b32 v155, v150, v26\n\t" \
        "v_mfma_f32_16x16x32_bf16 v[58:61], a[28:31], v[82:85], v[58:61]\n\t" \
        "ds_read_b64_tr_b16 a[70:71], v7 offset:15520\n\t" \
        "ds_read_b64_tr_b16 a[112:113], v134 offset:64\n\t" \
        "v_mfma_f32_16x16x32_bf16 v[38:41], a[32:35], v[78:81], v[38:41]\n\t" \
        "ds_read_b64_tr_b16 a[114:115], v135 offset:64\n\t" \
        "v_and_b32 v156, v150, v27\n\t" \
        "v_mfma_f32_16x16x32_bf16 v[62:65], a[32:35], v[82:85], v[62:65]\n\t" \
        "ds_read_b64_tr_b16 a[116:117], v138 offset:64\n\t" \
        "ds_read_b64_tr_b16 a[118:119], v139 offset:64\n\t" \
        "s_waitcnt lgkmcnt(14)\n\t" \
        "v_mfma_f32_16x16x32_bf16 v[42:45], a[36:39], v[78:81], v[42:45]\n\t" \
        "ds_read_b64_tr_b16 a[120:121], v136 offset:64\n\t" \
        "v_and_b32 v157, v150, v28\n\t" \
        "v_and_b32 v158, v151, v25\n\t" \
        "v_mfma_f32_16x16x32_bf16 v[66:69], a[36:39], v[82:85], v[66:69]\n\t" \
        "ds_read_b64_tr_b16 a[122:123], v137 offset:64\n\t" \
        "ds_read_b64_tr_b16 a[124:125], v140 offset:64\n\t" \
        "v_cvt_pk_bf16_f32 v87, v104, v105\n\t" \
        "v_mfma_f32_16x16x32_bf16 v[46:49], a[40:43], v[78:81], v[46:49]\n\t" \
        "ds_read_b64_tr_b16 a[126:127], v141 offset:64\n\t" \
        "v_cvt_pk_bf16_f32 v88, v106, v107\n\t" \
        "v_and_b32 v159, v151, v26\n\t" \
        "v_mfma_f32_16x16x32_bf16 v[70:73], a[40:43], v[82:85], v[70:73]\n\t" \
        "ds_read_b64_tr_b16 a[72:73], v6 offset:18432\n\t" \
        "ds_read_b64_tr_b16 a[74:75], v6 offset:21504\n\t" \
        "v_cvt_pk_bf16_f32 v89, v108, v109\n\t" \
        "v_cvt_pk_bf16_f32 v90, v110, v111\n\t" \
        "v_mfma_f32_16x16x32_bf16 v[50:53], a[44:47], v[78:81], v[50:53]\n\t" \
        "ds_read_b64_tr_b16 a[76:77], v7 offset:18464\n\t" \
        "v_and_b32 v160, v151, v27\n\t" \
        "v_mfma_f32_16x16x32_bf16 v[74:77], a[44:47], v[82:85], v[74:77]\n\t" \
        "ds_read_b64_tr_b16 a[78:79], v7 offset:21536\n\t" \
        "ds_read_b64_tr_b16 a[80:81], v6 offset:18496\n\t" \
        "v_cvt_pk_bf16_f32 v91, v112, v113\n\t" \
        "v_cvt_pk_bf16_f32 v92, v114, v115\n\t" \
        "v_mfma_f32_16x16x32_bf16 v[102:105], a[96:99], v[154:157], 0\n\t" \
        "ds_read_b64_tr_b16 a[82:83], v6 offset:21568\n\t" \
        "v_and_b32 v161, v151, v28\n\t" \
        "v_cvt_pk_bf16_f32 v93, v116, v117\n\t" \
        "v_mfma_f32_16x16x32_bf16 v[106:109], a[100:103], v[154:157], 0\n\t" \
        "ds_read_b64_tr_b16 a[84:85], v7 offset:18528\n\t" \
        "ds_read_b64_tr_b16 a[86:87], v7 offset:21600\n\t" \
        "v_mfma_f32_16x16x32_bf16 v[110:113], a[104:107], v[158:161], 0\n\t" \
        "ds_read_b64_tr_b16 a[88:89], v6 offset:18560\n\t" \
        "v_cvt_pk_bf16_f32 v94, v118, v119\n\t" \
        "v_mfma_f32_16x16x32_bf16 v[114:117], a[108:111], v[158:161], 0\n\t" \
        "ds_read_b64_tr_b16 a[90:91], v6 offset:21632\n\t" \
        "ds_read_b64_tr_b16 a[92:93], v7 offset:18592\n\t" \
        "v_mfma_f32_16x16x32_bf16 v[30:33], a[48:51], v[86:89], v[30:33]\n\t" \
        "ds_read_b64_tr_b16 a[94:95], v7 offset:21664\n\t" \
        "v_cvt_pk_bf16_f32 v95, v120, v121\n\t" \
        "v_mfma_f32_16x16x32_bf16 v[54:57], a[48:51], v[90:93], v[54:57]\n\t" \
        "ds_read_b64_tr_b16 a[128:129], v134 offset:128\n\t" \
        "v_cvt_pk_bf16_f32 v96, v122, v123\n\t" \
        "s_waitcnt lgkmcnt(13)\n\t" \
        "v_mfma_f32_16x16x32_bf16 v[34:37], a[52:55], v[86:89], v[34:37]\n\t" \
        "ds_read_b64_tr_b16 a[130:131], v135 offset:128\n\t" \
        "ds_read_b64_tr_b16 a[132:133], v138 offset:128\n\t" \
        "v_mfma_f32_16x16x32_bf16 v[58:61], a[52:55], v[90:93], v[58:61]\n\t" \
        "s_waitcnt vmcnt(3)\n\t" \
        "s_barrier\n\t" \
        "ds_read_b64_tr_b16 a[134:135], v139 offset:128\n\t" \
        "v_cvt_pk_bf16_f32 v97, v124, v125\n\t" \
        "v_mfma_f32_16x16x32_bf16 v[38:41], a[56:59], v[86:89], v[38:41]\n\t" \
        "ds_read_b64_tr_b16 a[136:137], v136 offset:128\n\t" \
        "ds_read_b64_tr_b16 a[138:139], v137 offset:128\n\t" \
        "v_mfma_f32_16x16x32_bf16 v[62:65], a[56:59], v[90:93], v[62:65]\n\t" \
        "ds_read_b64_tr_b16 a[140:141], v140 offset:128\n\t" \
        "v_cvt_pk_bf16_f32 v98, v126, v127\n\t" \
        "v_mfma_f32_16x16x32_bf16 v[42:45], a[60:63], v[86:89], v[42:45]\n\t" \
        "ds_read_b64_tr_b16 a[142:143], v141 offset:128\n\t" \
        "ds_read_b64_tr_b16 a[0:1], v6 offset:24576\n\t" \
        "v_mfma_f32_16x16x32_bf16 v[66:69], a[60:63], v[90:93], v[66:69]\n\t" \
        "ds_read_b64_tr_b16 a[2:3], v6 offset:27648\n\t" \
        "v_cvt_pk_bf16_f32 v99, v128, v129\n\t" \
        "v_mfma_f32_16x16x32_bf16 v[46:49], a[64:67], v[86:89], v[46:49]\n\t" \
        "ds_read_b64_tr_b16 a[4:5], v7 offset:24608\n\t" \
        "ds_read_b64_tr_b16 a[6:7], v7 offset:27680\n\t" \
        "v_mfma_f32_16x16x32_bf16 v[70:73], a[64:67], v[90:93], v[70:73]\n\t" \
        "ds_read_b64_tr_b16 a[8:9], v6 offset:24640\n\t" \
        "v_cvt_pk_bf16_f32 v100, v130, v131\n\t" \
        "v_mfma_f32_16x16x32_bf16 v[50:53], a[68:71], v[86:89], v[50:53]\n\t" \
        "ds_read_b64_tr_b16 a[10:11], v6 offset:27712\n\t" \
        "ds_read_b64_tr_b16 a[12:13], v7 offset:24672\n\t" \
        "v_mfma_f32_16x16x32_bf16 v[74:77], a[68:71], v[90:93], v[74:77]\n\t" \
        "ds_read_b64_tr_b16 a[14:15], v7 offset:27744\n\t" \
        "v_cvt_pk_bf16_f32 v101, v132, v133\n\t" \
        "v_mfma_f32_16x16x32_bf16 v[118:121], a[112:115], v[154:157], 0\n\t" \
        "ds_read_b64_tr_b16 a[16:17], v6 offset:24704\n\t" \
        "ds_read_b64_tr_b16 a[18:19], v6 offset:27776\n\t" \
        "v_mfma_f32_16x16x32_bf16 v[122:125], a[116:119], v[154:157], 0\n\t" \
        "ds_read_b64_tr_b16 a[20:21], v7 offset:24736\n\t" \
        "v_cvt_pk_bf16_f32 v78, v102, v103\n\t" \
        "v_mfma_f32_16x16x32_bf16 v[126:129], a[120:123], v[158:161], 0\n\t" \
        "ds_read_b64_tr_b16 a[22:23], v7 offset:27808\n\t" \
        "ds_read_b64_tr_b16 a[24:25], v6 offset:30720\n\t" \
        "v_mfma_f32_16x16x32_bf16 v[130:133], a[124:127], v[158:161], 0\n\t" \
        "s_waitcnt vmcnt(0) lgkmcnt(15)\n\t" \
        "s_barrier\n\t" \
        "ds_read_b64_tr_b16 a[26:27], v6 offset:33792\n\t" \
        "v_cvt_pk_bf16_f32 v79, v104, v105\n\t" \
        "v_mfma_f32_16x16x32_bf16 v[30:33], a[72:75], v[94:97], v[30:33]\n\t" \
        "ds_read_b64_tr_b16 a[28:29], v7 offset:30752\n\t" \
        "ds_read_b64_tr_b16 a[30:31], v7 offset:33824\n\t" \
        "v_mfma_f32_16x16x32_bf16 v[54:57], a[72:75], v[98:101], v[54:57]\n\t" \
        "ds_read_b64_tr_b16 a[32:33], v6 offset:30784\n\t" \
        "v_cvt_pk_bf16_f32 v80, v106, v107\n\t" \
        "v_mfma_f32_16x16x32_bf16 v[34:37], a[76:79], v[94:97], v[34:37]\n\t" \
        "ds_read_b64_tr_b16 a[34:35], v6 offset:33856\n\t" \
        "ds_read_b64_tr_b16 a[36:37], v7 offset:30816\n\t" \
        "v_mfma_f32_16x16x32_bf16 v[58:61], a[76:79], v[98:101], v[58:61]\n\t" \
        "ds_read_b64_tr_b16 a[38:39], v7 offset:33888\n\t" \
        "v_cvt_pk_bf16_f32 v81, v108, v109\n\t" \
        "v_mfma_f32_16x16x32_bf16 v[38:41], a[80:83], v[94:97], v[38:41]\n\t" \
        "ds_read_b64_tr_b16 a[40:41], v6 offset:30848\n\t" \
        "ds_read_b64_tr_b16 a[42:43], v6 offset:33920\n\t" \
        "v_mfma_f32_16x16x32_bf16 v[62:65], a[80:83], v[98:101], v[62:65]\n\t" \
        "ds_read_b64_tr_b16 a[44:45], v7 offset:30880\n\t" \
        "v_cvt_pk_bf16_f32 v82, v110, v111\n\t" \
        "v_mfma_f32_16x16x32_bf16 v[42:45], a[84:87], v[94:97], v[42:45]\n\t" \
        "ds_read_b64_tr_b16 a[46:47], v7 offset:33952\n\t" \
        "ds_read_b64_tr_b16 a[48:49], v6 offset:36864\n\t" \
        "v_mfma_f32_16x16x32_bf16 v[66:69], a[84:87], v[98:101], v[66:69]\n\t" \
        "ds_read_b64_tr_b16 a[50:51], v6 offset:39936\n\t" \
        "v_cvt_pk_bf16_f32 v83, v112, v113\n\t" \
        "v_mfma_f32_16x16x32_bf16 v[46:49], a[88:91], v[94:97], v[46:49]\n\t" \
        "ds_read_b64_tr_b16 a[52:53], v7 offset:36896\n\t" \
        "ds_read_b64_tr_b16 a[54:55], v7 offset:39968\n\t" \
        "v_mfma_f32_16x16x32_bf16 v[70:73], a[88:91], v[98:101], v[70:73]\n\t" \
        "ds_read_b64_tr_b16 a[56:57], v6 offset:36928\n\t" \
        "v_cvt_pk_bf16_f32 v84, v114, v115\n\t" \
        "v_mfma_f32_16x16x32_bf16 v[50:53], a[92:95], v[94:97], v[50:53]\n\t" \
        "ds_read_b64_tr_b16 a[58:59], v6 offset:40000\n\t" \
        "v_cvt_pk_bf16_f32 v85, v116, v117\n\t" \
        "v_mfma_f32_16x16x32_bf16 v[74:77], a[92:95], v[98:101], v[74:77]\n\t" \
        "ds_read_b64_tr_b16 a[60:61], v7 offset:36960\n\t" \
        "ds_read_b64_tr_b16 a[62:63], v7 offset:40032\n\t" \
        "v_mfma_f32_16x16x32_bf16 v[102:105], a[128:131], v[154:157], 0\n\t" \
        "ds_read_b64_tr_b16 a[64:65], v6 offset:36992\n\t" \
        "ds_read_b64_tr_b16 a[66:67], v6 offset:40064\n\t" \
        "v_mfma_f32_16x16x32_bf16 v[106:109], a[132:135], v[154:157], 0\n\t" \
        "ds_read_b64_tr_b16 a[68:69], v7 offset:37024\n\t" \
        "v_cvt_pk_bf16_f32 v86, v118, v119\n\t" \
        "v_mfma_f32_16x16x32_bf16 v[110:113], a[136:139], v[158:161], 0\n\t" \
        "ds_read_b64_tr_b16 a[70:71], v7 offset:40096\n\t" \
        "v_cvt_pk_bf16_f32 v88, v122, v123\n\t" \
        "s_waitcnt lgkmcnt(13)\n\t" \
        "v_mfma_f32_16x16x32_bf16 v[114:117], a[140:143], v[158:161], 0\n\t" \
        "v_cvt_pk_bf16_f32 v87, v120, v121\n\t" \
        "v_cvt_pk_bf16_f32 v89, v124, v125\n\t" \
        "v_mfma_f32_16x16x32_bf16 v[30:33], a[0:3], v[78:81], v[30:33]\n\t" \
        "v_cvt_pk_bf16_f32 v94, v102, v103\n\t" \
        "v_cvt_pk_bf16_f32 v95, v104, v105\n\t" \
        "v_mfma_f32_16x16x32_bf16 v[34:37], a[4:7], v[78:81], v[34:37]\n\t" \
        "v_cvt_pk_bf16_f32 v96, v106, v107\n\t" \
        "v_cvt_pk_bf16_f32 v97, v108, v109\n\t" \
        "v_mfma_f32_16x16x32_bf16 v[38:41], a[8:11], v[78:81], v[38:41]\n\t" \
        "v_cvt_pk_bf16_f32 v90, v126, v127\n\t" \
        "v_cvt_pk_bf16_f32 v92, v130, v131\n\t" \
        "v_mfma_f32_16x16x32_bf16 v[42:45], a[12:15], v[78:81], v[42:45]\n\t" \
        "v_cvt_pk_bf16_f32 v91, v128, v129\n\t" \
        "v_cvt_pk_bf16_f32 v93, v132, v133\n\t" \
        "v_mfma_f32_16x16x32_bf16 v[46:49], a[16:19], v[78:81], v[46:49]\n\t" \
        "v_cvt_pk_bf16_f32 v98, v110, v111\n\t" \
        "v_cvt_pk_bf16_f32 v100, v114, v115\n\t" \
        "v_mfma_f32_16x16x32_bf16 v[50:53], a[20:23], v[78:81], v[50:53]\n\t" \
        "v_cvt_pk_bf16_f32 v99, v112, v113\n\t" \
        "v_cvt_pk_bf16_f32 v101, v116, v117\n\t" \
        "v_mfma_f32_16x16x32_bf16 v[30:33], a[24:27], v[86:89], v[30:33]\n\t" \
        "v_mfma_f32_16x16x32_bf16 v[34:37], a[28:31], v[86:89], v[34:37]\n\t" \
        "v_mfma_f32_16x16x32_bf16 v[38:41], a[32:35], v[86:89], v[38:41]\n\t" \
        "v_mfma_f32_16x16x32_bf16 v[42:45], a[36:39], v[86:89], v[42:45]\n\t" \
        "v_mfma_f32_16x16x32_bf16 v[46:49], a[40:43], v[86:89], v[46:49]\n\t" \
        "s_waitcnt lgkmcnt(0)\n\t" \
        "v_mfma_f32_16x16x32_bf16 v[50:53], a[44:47], v[86:89], v[50:53]\n\t" \
        "v_mfma_f32_16x16x32_bf16 v[30:33], a[48:51], v[94:97], v[30:33]\n\t" \
        "v_mfma_f32_16x16x32_bf16 v[34:37], a[52:55], v[94:97], v[34:37]\n\t" \
        "v_mfma_f32_16x16x32_bf16 v[38:41], a[56:59], v[94:97], v[38:41]\n\t" \
        "v_mfma_f32_16x16x32_bf16 v[42:45], a[60:63], v[94:97], v[42:45]\n\t" \
        "v_mfma_f32_16x16x32_bf16 v[46:49], a[64:67], v[94:97], v[46:49]\n\t" \
        "v_mfma_f32_16x16x32_bf16 v[50:53], a[68:71], v[94:97], v[50:53]\n\t" \
        "v_mfma_f32_16x16x32_bf16 v[54:57], a[0:3], v[82:85], v[54:57]\n\t" \
        "v_mfma_f32_16x16x32_bf16 v[58:61], a[4:7], v[82:85], v[58:61]\n\t" \
        "v_mfma_f32_16x16x32_bf16 v[62:65], a[8:11], v[82:85], v[62:65]\n\t" \
        "v_mfma_f32_16x16x32_bf16 v[66:69], a[12:15], v[82:85], v[66:69]\n\t" \
        "v_mfma_f32_16x16x32_bf16 v[70:73], a[16:19], v[82:85], v[70:73]\n\t" \
        "v_mfma_f32_16x16x32_bf16 v[74:77], a[20:23], v[82:85], v[74:77]\n\t" \
        "v_mfma_f32_16x16x32_bf16 v[54:57], a[24:27], v[90:93], v[54:57]\n\t" \
        "v_mfma_f32_16x16x32_bf16 v[58:61], a[28:31], v[90:93], v[58:61]\n\t" \
        "v_mfma_f32_16x16x32_bf16 v[62:65], a[32:35], v[90:93], v[62:65]\n\t" \
        "v_mfma_f32_16x16x32_bf16 v[66:69], a[36:39], v[90:93], v[66:69]\n\t" \
        "v_mfma_f32_16x16x32_bf16 v[70:73], a[40:43], v[90:93], v[70:73]\n\t" \
        "v_mfma_f32_16x16x32_bf16 v[74:77], a[44:47], v[90:93], v[74:77]\n\t" \
        "v_mfma_f32_16x16x32_bf16 v[54:57], a[48:51], v[98:101], v[54:57]\n\t" \
        "v_mfma_f32_16x16x32_bf16 v[58:61], a[52:55], v[98:101], v[58:61]\n\t" \
        "v_mfma_f32_16x16x32_bf16 v[62:65], a[56:59], v[98:101], v[62:65]\n\t" \
        "v_mfma_f32_16x16x32_bf16 v[66:69], a[60:63], v[98:101], v[66:69]\n\t" \
        "v_mfma_f32_16x16x32_bf16 v[70:73], a[64:67], v[98:101], v[70:73]\n\t" \
        "v_mfma_f32_16x16x32_bf16 v[74:77], a[68:71], v[98:101], v[74:77]\n\t" \
        "s_nop 4\n\t" \
        "v_add_f32 v30, v30, v54\n\t" \
        "v_add_f32 v31, v31, v55\n\t" \
        "v_add_f32 v32, v32, v56\n\t" \
        "v_add_f32 v33, v33, v57\n\t" \
        "v_add_f32 v34, v34, v58\n\t" \
        "v_add_f32 v35, v35, v59\n\t" \
        "v_add_f32 v36, v36, v60\n\t" \
        "v_add_f32 v37, v37, v61\n\t" \
        "v_add_f32 v38, v38, v62\n\t" \
        "v_add_f32 v39, v39, v63\n\t" \
        "v_add_f32 v40, v40, v64\n\t" \
        "v_add_f32 v41, v41, v65\n\t" \
        "v_add_f32 v42, v42, v66\n\t" \
        "v_add_f32 v43, v43, v67\n\t" \
        "v_add_f32 v44, v44, v68\n\t" \
        "v_add_f32 v45, v45, v69\n\t" \
        "v_add_f32 v46, v46, v70\n\t" \
        "v_add_f32 v47, v47, v71\n\t" \
        "v_add_f32 v48, v48, v72\n\t" \
        "v_add_f32 v49, v49, v73\n\t" \
        "v_add_f32 v50, v50, v74\n\t" \
        "v_add_f32 v51, v51, v75\n\t" \
        "v_add_f32 v52, v52, v76\n\t" \
        "v_add_f32 v53, v53, v77\n\t" \
        "v_cvt_pk_bf16_f32 v30, v30, v31\n\t" \
        "v_cvt_pk_bf16_f32 v31, v32, v33\n\t" \
        "v_cvt_pk_bf16_f32 v32, v34, v35\n\t" \
        "v_cvt_pk_bf16_f32 v33, v36, v37\n\t" \
        "s_nop 0\n\t" \
        "v_permlane16_swap_b32 v30, v32\n\t" \
        "v_permlane16_swap_b32 v31, v33\n\t" \
        "buffer_store_dwordx4 v[30:33], v8, s[28:31], s59 offen offset:0 sc1\n\t" \
        "v_cvt_pk_bf16_f32 v38, v38, v39\n\t" \
        "v_cvt_pk_bf16_f32 v39, v40, v41\n\t" \
        "v_cvt_pk_bf16_f32 v40, v42, v43\n\t" \
        "v_cvt_pk_bf16_f32 v41, v44, v45\n\t" \
        "s_nop 0\n\t" \
        "v_permlane16_swap_b32 v38, v40\n\t" \
        "v_permlane16_swap_b32 v39, v41\n\t" \
        "buffer_store_dwordx4 v[38:41], v8, s[28:31], s59 offen offset:64 sc1\n\t" \
        "v_cvt_pk_bf16_f32 v46, v46, v47\n\t" \
        "v_cvt_pk_bf16_f32 v47, v48, v49\n\t" \
        "v_cvt_pk_bf16_f32 v48, v50, v51\n\t" \
        "v_cvt_pk_bf16_f32 v49, v52, v53\n\t" \
        "s_nop 0\n\t" \
        "v_permlane16_swap_b32 v46, v48\n\t" \
        "v_permlane16_swap_b32 v47, v49\n\t" \
        "buffer_store_dwordx4 v[46:49], v8, s[28:31], s59 offen offset:128 sc1\n\t" \
        "s_endpgm\n\t" \
        ".LTeprod%=:\n\t" \
        "s_waitcnt lgkmcnt(10)\n\t" \
        "v_mfma_f32_16x16x32_bf16 v[88:91], v[64:67], v[112:115], 0\n\t" \
        "v_lshlrev_b32 v116, 5, v120\n\t" \
        "v_bitop3_b32 v116, v116, s52, v23 bitop3:0x6a\n\t" \
        "v_xor_b32 v117, 8, v116\n\t" \
        "v_lshlrev_b32 v118, 5, v121\n\t" \
        "v_bitop3_b32 v118, v118, s52, v23 bitop3:0x6a\n\t" \
        "v_xor_b32 v119, 8, v118\n\t" \
        "ds_read_u16 v120, v20 offset:128\n\t" \
        "ds_read_u16 v121, v20 offset:136\n\t" \
        "ds_read_b64_tr_b16 v[64:65], v116 offset:0\n\t" \
        "ds_read_b64_tr_b16 v[66:67], v118 offset:0\n\t" \
        "s_waitcnt lgkmcnt(12)\n\t" \
        "v_mfma_f32_16x16x32_bf16 v[92:95], v[68:71], v[112:115], 0\n\t" \
        "ds_read_b64_tr_b16 v[68:69], v117 offset:0\n\t" \
        "ds_read_b64_tr_b16 v[70:71], v119 offset:0\n\t" \
        "s_waitcnt lgkmcnt(12)\n\t" \
        "v_mfma_f32_16x16x32_bf16 v[96:99], v[72:75], v[112:115], 0\n\t" \
        "ds_read_b64_tr_b16 v[72:73], v116 offset:64\n\t" \
        "ds_read_b64_tr_b16 v[74:75], v118 offset:64\n\t" \
        "s_waitcnt lgkmcnt(12)\n\t" \
        "v_mfma_f32_16x16x32_bf16 v[100:103], v[76:79], v[112:115], 0\n\t" \
        "ds_read_b64_tr_b16 v[76:77], v117 offset:64\n\t" \
        "ds_read_b64_tr_b16 v[78:79], v119 offset:64\n\t" \
        "s_waitcnt lgkmcnt(12)\n\t" \
        "v_mfma_f32_16x16x32_bf16 v[104:107], v[80:83], v[112:115], 0\n\t" \
        "ds_read_b64_tr_b16 v[80:81], v116 offset:128\n\t" \
        "ds_read_b64_tr_b16 v[82:83], v118 offset:128\n\t" \
        "s_waitcnt lgkmcnt(12)\n\t" \
        "v_mfma_f32_16x16x32_bf16 v[108:111], v[84:87], v[112:115], 0\n\t" \
        "ds_read_b64_tr_b16 v[84:85], v117 offset:128\n\t" \
        "ds_read_b64_tr_b16 v[86:87], v119 offset:128\n\t" \
        "v_and_b32 v112, v122, v25\n\t" \
        "v_and_b32 v113, v122, v26\n\t" \
        "v_and_b32 v114, v122, v27\n\t" \
        "v_and_b32 v115, v122, v28\n\t" \
        "ds_read_b32 v122, v21 offset:256\n\t" \
        "s_and_b32 s55, s50, 0xffff\n\t" \
        "s_cbranch_scc1 .LTetwo1%=\n\t" \
        ".LTeback1%=:\n\t" \
        "s_sub_u32 s55, s46, s47\n\t" \
        "v_xor_b32 v156, 8, v22\n\t" \
        "v_cmp_gt_i32 vcc, s55, v24\n\t" \
        "s_mov_b64 exec, vcc\n\t" \
        "v_cvt_pk_bf16_f32 v88, v88, v89\n\t" \
        "v_cvt_pk_bf16_f32 v89, v90, v91\n\t" \
        "ds_write_b64 v22, v[88:89] offset:0\n\t" \
        "v_cvt_pk_bf16_f32 v92, v92, v93\n\t" \
        "v_cvt_pk_bf16_f32 v93, v94, v95\n\t" \
        "ds_write_b64 v156, v[92:93] offset:0\n\t" \
        "v_cvt_pk_bf16_f32 v96, v96, v97\n\t" \
        "v_cvt_pk_bf16_f32 v97, v98, v99\n\t" \
        "ds_write_b64 v22, v[96:97] offset:64\n\t" \
        "v_cvt_pk_bf16_f32 v100, v100, v101\n\t" \
        "v_cvt_pk_bf16_f32 v101, v102, v103\n\t" \
        "ds_write_b64 v156, v[100:101] offset:64\n\t" \
        "v_cvt_pk_bf16_f32 v104, v104, v105\n\t" \
        "v_cvt_pk_bf16_f32 v105, v106, v107\n\t" \
        "ds_write_b64 v22, v[104:105] offset:128\n\t" \
        "v_cvt_pk_bf16_f32 v108, v108, v109\n\t" \
        "v_cvt_pk_bf16_f32 v109, v110, v111\n\t" \
        "ds_write_b64 v156, v[108:109] offset:128\n\t" \
        "s_mov_b64 exec, -1\n\t" \
        "s_lshl_b32 s55, s53, 4\n\t" \
        "s_add_u32 s47, s47, 16\n\t" \
        "v_add_u32 v22, s55, v22\n\t" \
        "s_lshr_b64 s[50:51], s[50:51], 16\n\t" \
        "v_add_u32 v20, 64, v20\n\t" \
        "v_add_u32 v21, 0x80, v21\n\t" \
        "s_add_u32 s55, s47, 16\n\t" \
        "s_cmp_lt_u32 s55, s46\n\t" \
        "s_cbranch_scc1 .LTeprod%=\n\t" \
        "s_branch .LTelast%=\n\t" \
        ".LTetwo1%=:\n\t" \
        "s_nop 4\n\t" \
        "ds_read_u16 v156, v20\n\t" \
        "ds_read_u16 v158, v20 offset:8\n\t" \
        "ds_read_b32 v127, v21 offset:4\n\t" \
        "s_waitcnt lgkmcnt(0)\n\t" \
        "v_lshrrev_b32 v124, 15, v156\n\t" \
        "v_lshlrev_b32 v156, 5, v156\n\t" \
        "v_bitop3_b32 v156, v156, s52, v23 bitop3:0x6a\n\t" \
        "v_mul_u32_u24 v124, 3968, v124\n\t" \
        "v_add_u32 v156, v156, v124\n\t" \
        "v_xor_b32 v157, 8, v156\n\t" \
        "v_lshrrev_b32 v125, 15, v158\n\t" \
        "v_lshlrev_b32 v158, 5, v158\n\t" \
        "v_bitop3_b32 v158, v158, s52, v23 bitop3:0x6a\n\t" \
        "v_mul_u32_u24 v125, 3968, v125\n\t" \
        "v_add_u32 v158, v158, v125\n\t" \
        "v_xor_b32 v159, 8, v158\n\t" \
        "ds_read_b64_tr_b16 v[128:129], v156 offset:0\n\t" \
        "ds_read_b64_tr_b16 v[130:131], v158 offset:0\n\t" \
        "ds_read_b64_tr_b16 v[132:133], v157 offset:0\n\t" \
        "ds_read_b64_tr_b16 v[134:135], v159 offset:0\n\t" \
        "ds_read_b64_tr_b16 v[136:137], v156 offset:64\n\t" \
        "ds_read_b64_tr_b16 v[138:139], v158 offset:64\n\t" \
        "ds_read_b64_tr_b16 v[140:141], v157 offset:64\n\t" \
        "ds_read_b64_tr_b16 v[142:143], v159 offset:64\n\t" \
        "ds_read_b64_tr_b16 v[144:145], v156 offset:128\n\t" \
        "ds_read_b64_tr_b16 v[146:147], v158 offset:128\n\t" \
        "ds_read_b64_tr_b16 v[148:149], v157 offset:128\n\t" \
        "ds_read_b64_tr_b16 v[150:151], v159 offset:128\n\t" \
        "v_and_b32 v124, v127, v25\n\t" \
        "v_and_b32 v125, v127, v26\n\t" \
        "v_and_b32 v126, v127, v27\n\t" \
        "v_and_b32 v127, v127, v28\n\t" \
        "s_nop 1\n\t" \
        "s_waitcnt lgkmcnt(10)\n\t" \
        "v_mfma_f32_16x16x32_bf16 v[88:91], v[128:131], v[124:127], v[88:91]\n\t" \
        "s_waitcnt lgkmcnt(8)\n\t" \
        "v_mfma_f32_16x16x32_bf16 v[92:95], v[132:135], v[124:127], v[92:95]\n\t" \
        "s_waitcnt lgkmcnt(6)\n\t" \
        "v_mfma_f32_16x16x32_bf16 v[96:99], v[136:139], v[124:127], v[96:99]\n\t" \
        "s_waitcnt lgkmcnt(4)\n\t" \
        "v_mfma_f32_16x16x32_bf16 v[100:103], v[140:143], v[124:127], v[100:103]\n\t" \
        "s_waitcnt lgkmcnt(2)\n\t" \
        "v_mfma_f32_16x16x32_bf16 v[104:107], v[144:147], v[124:127], v[104:107]\n\t" \
        "s_waitcnt lgkmcnt(0)\n\t" \
        "v_mfma_f32_16x16x32_bf16 v[108:111], v[148:151], v[124:127], v[108:111]\n\t" \
        "s_nop 7\n\t" \
        "s_branch .LTeback1%=\n\t" \
        ".LTetwo2%=:\n\t" \
        "s_nop 4\n\t" \
        "ds_read_u16 v156, v20\n\t" \
        "ds_read_u16 v158, v20 offset:8\n\t" \
        "ds_read_b32 v127, v21 offset:4\n\t" \
        "s_waitcnt lgkmcnt(0)\n\t" \
        "v_lshrrev_b32 v124, 15, v156\n\t" \
        "v_lshlrev_b32 v156, 5, v156\n\t" \
        "v_bitop3_b32 v156, v156, s52, v23 bitop3:0x6a\n\t" \
        "v_mul_u32_u24 v124, 3968, v124\n\t" \
        "v_add_u32 v156, v156, v124\n\t" \
        "v_xor_b32 v157, 8, v156\n\t" \
        "v_lshrrev_b32 v125, 15, v158\n\t" \
        "v_lshlrev_b32 v158, 5, v158\n\t" \
        "v_bitop3_b32 v158, v158, s52, v23 bitop3:0x6a\n\t" \
        "v_mul_u32_u24 v125, 3968, v125\n\t" \
        "v_add_u32 v158, v158, v125\n\t" \
        "v_xor_b32 v159, 8, v158\n\t" \
        "ds_read_b64_tr_b16 v[128:129], v156 offset:0\n\t" \
        "ds_read_b64_tr_b16 v[130:131], v158 offset:0\n\t" \
        "ds_read_b64_tr_b16 v[132:133], v157 offset:0\n\t" \
        "ds_read_b64_tr_b16 v[134:135], v159 offset:0\n\t" \
        "ds_read_b64_tr_b16 v[136:137], v156 offset:64\n\t" \
        "ds_read_b64_tr_b16 v[138:139], v158 offset:64\n\t" \
        "ds_read_b64_tr_b16 v[140:141], v157 offset:64\n\t" \
        "ds_read_b64_tr_b16 v[142:143], v159 offset:64\n\t" \
        "ds_read_b64_tr_b16 v[144:145], v156 offset:128\n\t" \
        "ds_read_b64_tr_b16 v[146:147], v158 offset:128\n\t" \
        "ds_read_b64_tr_b16 v[148:149], v157 offset:128\n\t" \
        "ds_read_b64_tr_b16 v[150:151], v159 offset:128\n\t" \
        "v_and_b32 v124, v127, v25\n\t" \
        "v_and_b32 v125, v127, v26\n\t" \
        "v_and_b32 v126, v127, v27\n\t" \
        "v_and_b32 v127, v127, v28\n\t" \
        "s_nop 1\n\t" \
        "s_waitcnt lgkmcnt(10)\n\t" \
        "v_mfma_f32_16x16x32_bf16 v[88:91], v[128:131], v[124:127], v[88:91]\n\t" \
        "s_waitcnt lgkmcnt(8)\n\t" \
        "v_mfma_f32_16x16x32_bf16 v[92:95], v[132:135], v[124:127], v[92:95]\n\t" \
        "s_waitcnt lgkmcnt(6)\n\t" \
        "v_mfma_f32_16x16x32_bf16 v[96:99], v[136:139], v[124:127], v[96:99]\n\t" \
        "s_waitcnt lgkmcnt(4)\n\t" \
        "v_mfma_f32_16x16x32_bf16 v[100:103], v[140:143], v[124:127], v[100:103]\n\t" \
        "s_waitcnt lgkmcnt(2)\n\t" \
        "v_mfma_f32_16x16x32_bf16 v[104:107], v[144:147], v[124:127], v[104:107]\n\t" \
        "s_waitcnt lgkmcnt(0)\n\t" \
        "v_mfma_f32_16x16x32_bf16 v[108:111], v[148:151], v[124:127], v[108:111]\n\t" \
        "s_nop 7\n\t" \
        "s_branch .LTeback2%=\n\t" \
        ".LT_again%=:\n\t" \
        "s_waitcnt vmcnt(0) lgkmcnt(0)\n\t" \
        "s_barrier\n\t" \
        "s_add_u32 s32, s32, s3\n\t" \
        "s_mov_b32 s3, 0\n\t" \
        "s_mov_b32 s11, 3\n\t" \
        "s_branch .LT_entry%=\n\t" \
        ".LR_abort%=:\n\t" \
        "s_waitcnt vmcnt(0) lgkmcnt(0)\n\t" \
        "s_barrier\n\t" \
        "s_nop 7\n\t" \
        "s_nop 7\n\t" \
        "s_sub_u32 s3, s34, 12\n\t" \
        "s_mov_b32 s34, 12\n\t" \
        "s_mov_b32 s11, 3\n\t" \
        "s_branch .LT_entry%=\n\t" \
        ".LH_ent%=:\n\t" \
        "s_nop 4\n\t" \
        "v_bfe_u32 v162, v2, 2, 1\n\t" \
        "v_lshrrev_b32 v163, 3, v2\n\t" \
        "s_mul_i32 s55, s35, 2560\n\t" \
        "s_add_u32 s55, s55, 109120\n\t" \
        "v_mul_u32_u24 v162, 0x500, v162\n\t" \
        "v_lshlrev_b32 v163, 4, v163\n\t" \
        "v_add3_u32 v9, v162, v163, s55\n\t" \
        "v_lshrrev_b32 v164, 4, v2\n\t" \
        "v_bfe_u32 v162, v2, 2, 2\n\t" \
        "v_and_b32 v163, 3, v2\n\t" \
        "v_lshl_add_u32 v162, v164, 2, v162\n\t" \
        "v_lshlrev_b32 v11, 3, v163\n\t" \
        "v_and_b32 v165, 1, v164\n\t" \
        "v_mul_u32_u24 v162, 0xc0, v162\n\t" \
        "v_xor_b32 v12, 32, v11\n\t" \
        "v_lshl_add_u32 v162, v165, 5, v162\n\t" \
        "v_lshlrev_b32 v163, 6, v165\n\t" \
        "s_mov_b32 s55, 0xba40\n\t" \
        "v_add3_u32 v6, v162, v11, s55\n\t" \
        "v_sub_u32 v7, v6, v163\n\t" \
        "v_mul_u32_u24 v162, 0xc0, v19\n\t" \
        "v_lshrrev_b32 v164, 1, v164\n\t" \
        "v_lshl_add_u32 v162, v165, 5, v162\n\t" \
        "v_lshl_add_u32 v8, v164, 4, v162\n\t" \
        "s_add_u32 s55, s32, s35\n\t" \
        "s_mul_i32 s56, s33, 0xc0\n\t" \
        "s_mul_i32 s55, s55, 0xa800\n\t" \
        "s_add_u32 s59, s55, s56\n\t" \
        "s_add_u32 s60, s59, 0x2a000\n\t" \
        "v_lshrrev_b32 v166, 4, v2\n\t" \
        "v_bfe_u32 v168, v2, 2, 2\n\t" \
        "v_and_b32 v167, 3, v2\n\t" \
        "v_cmp_eq_u32 vcc, v166, v168\n\t" \
        "v_lshlrev_b32 v167, v167, 1\n\t" \
        "s_nop 0\n\t" \
        "v_cndmask_b32 v167, 0, v167, vcc\n\t" \
        "v_bfe_i32 v25, v167, 0, 1\n\t" \
        "v_bfe_i32 v26, v167, 1, 1\n\t" \
        "v_bfe_i32 v27, v167, 2, 1\n\t" \
        "v_bfe_i32 v28, v167, 3, 1\n\t" \
        "s_lshl_b32 s48, s35, 10\n\t" \
        "s_and_b32 s55, s35, 1\n\t" \
        "s_add_u32 s48, s48, 0x1e640\n\t" \
        "s_mul_i32 s55, s55, 0x2a00\n\t" \
        "s_cmp_lt_u32 s35, 2\n\t" \
        "s_cselect_b32 s49, 0, 0x2940\n\t" \
        "s_cselect_b32 s53, 1, -1\n\t" \
        "s_add_u32 s49, s49, s55\n\t" \
        "s_mul_i32 s53, s53, 192\n\t" \
        "s_mov_b32 s52, 0x3ffe0\n\t" \
        "s_add_u32 s49, s49, 0x1f640\n\t" \
        "v_lshrrev_b32 v166, 4, v2\n\t" \
        "v_and_b32 v24, 15, v2\n\t" \
        "v_bfe_u32 v167, v2, 3, 1\n\t" \
        "v_lshl_add_u32 v167, v166, 2, v167\n\t" \
        "v_lshrrev_b32 v168, 1, v2\n\t" \
        "v_and_b32 v168, 2, v168\n\t" \
        "v_lshl_add_u32 v167, v167, 2, v168\n\t" \
        "s_add_u32 s55, s48, 512\n\t" \
        "v_add_u32 v20, s55, v167\n\t" \
        "v_lshl_add_u32 v21, v24, 3, s48\n\t" \
        "v_bfe_u32 v167, v2, 1, 1\n\t" \
        "v_xor_b32 v167, v167, v166\n\t" \
        "v_lshrrev_b32 v168, 2, v24\n\t" \
        "v_and_b32 v169, 2, v168\n\t" \
        "v_xor_b32 v167, v167, v169\n\t" \
        "v_and_b32 v169, 4, v2\n\t" \
        "v_lshlrev_b32 v167, 4, v167\n\t" \
        "v_lshl_add_u32 v167, v169, 1, v167\n\t" \
        "v_mul_i32_i24 v22, s53, v24\n\t" \
        "v_add3_u32 v22, v22, v167, s49\n\t" \
        "v_and_b32 v167, 3, v2\n\t" \
        "v_and_b32 v169, 1, v166\n\t" \
        "v_lshlrev_b32 v169, 3, v169\n\t" \
        "v_lshl_or_b32 v23, v167, 4, v169\n\t" \
        "s_mov_b32 s45, s48\n\t" \
        "v_mbcnt_lo_u32_b32 v140, s4, 0\n\t" \
        "v_mbcnt_hi_u32_b32 v140, s5, v140\n\t" \
        "s_bcnt1_i32_b64 s55, s[4:5]\n\t" \
        "v_add_u32 v141, 0, v63\n\t" \
        "v_lshl_add_u32 v140, v140, 3, s45\n\t" \
        "s_lshl3_add_u32 s45, s55, s45\n\t" \
        "s_mov_b64 exec, s[4:5]\n\t" \
        "ds_write2_b32 v140, v50, v141 offset1:1\n\t" \
        "s_mov_b64 exec, -1\n\t" \
        "v_mbcnt_lo_u32_b32 v140, s6, 0\n\t" \
        "v_mbcnt_hi_u32_b32 v140, s7, v140\n\t" \
        "s_bcnt1_i32_b64 s55, s[6:7]\n\t" \
        "v_add_u32 v141, 256, v63\n\t" \
        "v_lshl_add_u32 v140, v140, 3, s45\n\t" \
        "s_lshl3_add_u32 s45, s55, s45\n\t" \
        "s_mov_b64 exec, s[6:7]\n\t" \
        "ds_write2_b32 v140, v51, v141 offset1:1\n\t" \
        "s_mov_b64 exec, -1\n\t" \
        "v_mbcnt_lo_u32_b32 v140, s8, 0\n\t" \
        "v_mbcnt_hi_u32_b32 v140, s9, v140\n\t" \
        "s_bcnt1_i32_b64 s55, s[8:9]\n\t" \
        "v_add_u32 v141, 512, v63\n\t" \
        "v_lshl_add_u32 v140, v140, 3, s45\n\t" \
        "s_lshl3_add_u32 s45, s55, s45\n\t" \
        "s_mov_b64 exec, s[8:9]\n\t" \
        "ds_write2_b32 v140, v52, v141 offset1:1\n\t" \
        "s_mov_b64 exec, -1\n\t" \
        "v_mbcnt_lo_u32_b32 v140, s12, 0\n\t" \
        "v_mbcnt_hi_u32_b32 v140, s13, v140\n\t" \
        "s_bcnt1_i32_b64 s55, s[12:13]\n\t" \
        "v_add_u32 v141, 768, v63\n\t" \
        "v_lshl_add_u32 v140, v140, 3, s45\n\t" \
        "s_lshl3_add_u32 s45, s55, s45\n\t" \
        "s_mov_b64 exec, s[12:13]\n\t" \
        "ds_write2_b32 v140, v53, v141 offset1:1\n\t" \
        "s_mov_b64 exec, -1\n\t" \
        "v_mbcnt_lo_u32_b32 v140, s14, 0\n\t" \
        "v_mbcnt_hi_u32_b32 v140, s15, v140\n\t" \
        "s_bcnt1_i32_b64 s55, s[14:15]\n\t" \
        "v_add_u32 v141, 1024, v63\n\t" \
        "v_lshl_add_u32 v140, v140, 3, s45\n\t" \
        "s_lshl3_add_u32 s45, s55, s45\n\t" \
        "s_mov_b64 exec, s[14:15]\n\t" \
        "ds_write2_b32 v140, v54, v141 offset1:1\n\t" \
        "s_mov_b64 exec, -1\n\t" \
        "s_sub_u32 s46, s45, s48\n\t" \
        "s_lshr_b32 s46, s46, 3\n\t" \
        "s_cmp_gt_u32 s46, 64\n\t" \
        "s_cselect_b32 s54, 1, s54\n\t" \
        "s_cselect_b32 s46, 64, s46\n\t" \
        "v_mul_i32_i24 v30, s53, v2\n\t" \
        "v_lshlrev_b32 v166, 3, v2\n\t" \
        "v_lshlrev_b32 v167, 2, v2\n\t" \
        "v_add_u32 v30, s49, v30\n\t" \
        "v_and_b32 v166, 16, v166\n\t" \
        "v_and_b32 v167, 32, v167\n\t" \
        "v_lshl_add_u32 v31, v2, 3, s48\n\t" \
        "v_or3_b32 v30, v30, v166, v167\n\t" \
        "v_lshlrev_b32 v166, 2, v2\n\t" \
        "s_add_u32 s55, s48, 512\n\t" \
        "v_add_u32 v32, s55, v166\n\t" \
        "v_cvt_f32_u32 v34, s32\n\t" \
        "v_cvt_f32_u32 v35, s33\n\t" \
        "v_mov_b32 v36, 124\n\t" \
        "v_add_f32 v34, -2.0, v34\n\t" \
        "v_add_f32 v35, -2.0, v35\n\t" \
        "v_cvt_f32_u32 v33, 11\n\t" \
        "v_mov_b32 v37, 0x05040100\n\t" \
        "v_mov_b32 v38, 0x80008000\n\t" \
        "v_mov_b32 v39, 0x435f435f\n\t" \
        "ds_read_b64 v[68:69], v31\n\t" \
        "v_cmp_gt_u32 s[12:13], s46, v2\n\t" \
        "s_mov_b32 s14, 0x2\n\t" \
        "s_mov_b32 s15, 0x8\n\t" \
        "v_mov_b32 v83, 0x41980000\n\t" \
        "v_mov_b32 v29, 0x20002\n\t" \
        "s_waitcnt lgkmcnt(0)\n\t" \
        "v_lshlrev_b32 v70, 16, v68\n\t" \
        "v_and_b32 v71, 0xffff0000, v68\n\t" \
        "s_mov_b64 exec, s[12:13]\n\t" \
        "ds_write_b32 v69, v30\n\t" \
        "ds_write_b32 v69, v30 offset:1280\n\t" \
        "s_mov_b64 exec, -1\n\t" \
        "v_fract_f32 v72, v70\n\t" \
        "v_fract_f32 v73, v71\n\t" \
        "v_sub_f32 v70, v70, v34\n\t" \
        "v_sub_f32 v71, v71, v35\n\t" \
        "v_sub_f32 v74, 1.0, v72\n\t" \
        "v_sub_f32 v75, 1.0, v73\n\t" \
        "v_med3_f32 v78, v70, 0, v33\n\t" \
        "v_cvt_pk_bf16_f32 v74, v74, v75\n\t" \
        "v_med3_f32 v79, v71, 0, v83\n\t" \
        "v_cmp_neq_f32 s[4:5], v78, v70\n\t" \
        "v_and_b32 v75, 0xffff0000, v74\n\t" \
        "v_cmp_neq_f32 s[6:7], v79, v71\n\t" \
        "v_lshlrev_b32 v74, 16, v74\n\t" \
        "v_cvt_u32_f32 v78, v78\n\t" \
        "v_cvt_u32_f32 v79, v79\n\t" \
        "s_or_b64 s[4:5], s[4:5], s[6:7]\n\t" \
        "v_mul_f32 v76, v74, v75\n\t" \
        "v_mul_f32 v74, v74, v73\n\t" \
        "s_and_b64 s[4:5], s[4:5], s[12:13]\n\t" \
        "s_cselect_b32 s54, 1, s54\n\t" \
        "v_mul_f32 v77, v72, v75\n\t" \
        "v_mul_f32 v75, v72, v73\n\t" \
        "v_cmp_neq_f32 s[4:5], 0, v73\n\t" \
        "v_cmp_neq_f32 s[6:7], 0, v72\n\t" \
        "v_cvt_pk_bf16_f32 v76, v76, v74\n\t" \
        "v_cvt_pk_bf16_f32 v77, v77, v75\n\t" \
        "v_mul_u32_u24 v80, 124, v78\n\t" \
        "v_add_u32 v83, 1, v79\n\t" \
        "v_mad_u32_u24 v80, v79, 6, v80\n\t" \
        "v_bfe_u32 v84, v79, 2, 1\n\t" \
        "v_bfe_u32 v83, v83, 2, 1\n\t" \
        "s_and_b64 s[50:51], s[4:5], s[6:7]\n\t" \
        "v_add3_u32 v81, v80, v84, s14\n\t" \
        "v_add3_u32 v82, v80, v83, s15\n\t" \
        "s_and_b64 s[50:51], s[50:51], s[12:13]\n\t" \
        "v_cndmask_b32 v83, 0, v36, s[6:7]\n\t" \
        "v_perm_b32 v74, v77, v76, v37\n\t" \
        "v_add_u32 v83, v81, v83\n\t" \
        "v_cndmask_b32 v68, v74, v76, s[4:5]\n\t" \
        "v_cndmask_b32 v83, v83, v82, s[4:5]\n\t" \
        "v_cndmask_b32 v75, 0, v38, s[50:51]\n\t" \
        "v_lshl_or_b32 v81, v83, 16, v81\n\t" \
        "v_cndmask_b32 v69, 0, v77, s[50:51]\n\t" \
        "v_or_b32 v81, v81, v75\n\t" \
        "ds_write_b64 v31, v[68:69]\n\t" \
        "v_cndmask_b32 v81, v29, v81, s[12:13]\n\t" \
        "ds_write_b32 v32, v81\n\t" \
        "s_lshl_b32 s55, s35, 2\n\t" \
        "s_cmp_lg_u32 s54, 0\n\t" \
        "v_mov_b32 v162, s55\n\t" \
        "s_cselect_b32 s55, 0x10000, s46\n\t" \
        "v_mov_b32 v163, s55\n\t" \
        "ds_write_b32 v162, v163\n\t" \
        "ds_read_u16 v120, v20\n\t" \
        "ds_read_u16 v121, v20 offset:8\n\t" \
        "ds_read_b32 v122, v21\n\t" \
        "v_mov_b32 v156, 0\n\t" \
        "s_mov_b32 s47, 0\n\t" \
        "s_waitcnt lgkmcnt(0)\n\t" \
        "v_lshlrev_b32 v116, 5, v120\n\t" \
        "v_bitop3_b32 v116, v116, s52, v23 bitop3:0x6a\n\t" \
        "v_xor_b32 v117, 8, v116\n\t" \
        "v_lshlrev_b32 v118, 5, v121\n\t" \
        "v_bitop3_b32 v118, v118, s52, v23 bitop3:0x6a\n\t" \
        "v_xor_b32 v119, 8, v118\n\t" \
        "v_and_b32 v112, v122, v25\n\t" \
        "v_and_b32 v113, v122, v26\n\t" \
        "v_and_b32 v114, v122, v27\n\t" \
        "v_and_b32 v115, v122, v28\n\t" \
        "ds_read_u16 v120, v20 offset:64\n\t" \
        "ds_read_u16 v121, v20 offset:72\n\t" \
        "ds_read_b32 v122, v21 offset:128\n\t" \
        "s_waitcnt vmcnt(6) lgkmcnt(0)\n\t" \
        "s_barrier\n\t" \
        "ds_read_b128 v[152:155], v156\n\t" \
        "ds_read_b64_tr_b16 v[64:65], v116 offset:0\n\t" \
        "ds_read_b64_tr_b16 v[66:67], v118 offset:0\n\t" \
        "ds_read_b64_tr_b16 v[68:69], v117 offset:0\n\t" \
        "ds_read_b64_tr_b16 v[70:71], v119 offset:0\n\t" \
        "ds_read_b64_tr_b16 v[72:73], v116 offset:64\n\t" \
        "ds_read_b64_tr_b16 v[74:75], v118 offset:64\n\t" \
        "ds_read_b64_tr_b16 v[76:77], v117 offset:64\n\t" \
        "ds_read_b64_tr_b16 v[78:79], v119 offset:64\n\t" \
        "ds_read_b64_tr_b16 v[80:81], v116 offset:128\n\t" \
        "ds_read_b64_tr_b16 v[82:83], v118 offset:128\n\t" \
        "ds_read_b64_tr_b16 v[84:85], v117 offset:128\n\t" \
        "ds_read_b64_tr_b16 v[86:87], v119 offset:128\n\t" \
        "s_waitcnt lgkmcnt(12)\n\t" \
        "v_add_u32 v152, v152, v154\n\t" \
        "v_add_u32 v153, v153, v155\n\t" \
        "v_max_u32 v152, v152, v153\n\t" \
        "s_nop 0\n\t" \
        "v_readfirstlane_b32 s55, v152\n\t" \
        "s_nop 0\n\t" \
        "s_cmp_gt_u32 s55, 56\n\t" \
        "s_cbranch_scc1 .Lfail%=\n\t" \
        "s_cmp_gt_u32 s46, 16\n\t" \
        "s_cbranch_scc1 .LHeprod%=\n\t" \
        ".LHelast%=:\n\t" \
        "s_waitcnt lgkmcnt(10)\n\t" \
        "v_mfma_f32_16x16x32_bf16 v[88:91], v[64:67], v[112:115], 0\n\t" \
        "s_waitcnt lgkmcnt(8)\n\t" \
        "v_mfma_f32_16x16x32_bf16 v[92:95], v[68:71], v[112:115], 0\n\t" \
        "s_waitcnt lgkmcnt(6)\n\t" \
        "v_mfma_f32_16x16x32_bf16 v[96:99], v[72:75], v[112:115], 0\n\t" \
        "s_waitcnt lgkmcnt(4)\n\t" \
        "v_mfma_f32_16x16x32_bf16 v[100:103], v[76:79], v[112:115], 0\n\t" \
        "s_waitcnt lgkmcnt(2)\n\t" \
        "v_mfma_f32_16x16x32_bf16 v[104:107], v[80:83], v[112:115], 0\n\t" \
        "s_waitcnt lgkmcnt(0)\n\t" \
        "v_mfma_f32_16x16x32_bf16 v[108:111], v[84:87], v[112:115], 0\n\t" \
        "s_and_b32 s55, s50, 0xffff\n\t" \
        "s_cbranch_scc1 .LHetwo2%=\n\t" \
        ".LHeback2%=:\n\t" \
        "s_sub_u32 s55, s46, s47\n\t" \
        "v_xor_b32 v156, 8, v22\n\t" \
        "v_cmp_gt_i32 vcc, s55, v24\n\t" \
        "s_mov_b64 exec, vcc\n\t" \
        "v_cvt_pk_bf16_f32 v88, v88, v89\n\t" \
        "v_cvt_pk_bf16_f32 v89, v90, v91\n\t" \
        "ds_write_b64 v22, v[88:89] offset:0\n\t" \
        "v_cvt_pk_bf16_f32 v92, v92, v93\n\t" \
        "v_cvt_pk_bf16_f32 v93, v94, v95\n\t" \
        "ds_write_b64 v156, v[92:93] offset:0\n\t" \
        "v_cvt_pk_bf16_f32 v96, v96, v97\n\t" \
        "v_cvt_pk_bf16_f32 v97, v98, v99\n\t" \
        "ds_write_b64 v22, v[96:97] offset:64\n\t" \
        "v_cvt_pk_bf16_f32 v100, v100, v101\n\t" \
        "v_cvt_pk_bf16_f32 v101, v102, v103\n\t" \
        "ds_write_b64 v156, v[100:101] offset:64\n\t" \
        "v_cvt_pk_bf16_f32 v104, v104, v105\n\t" \
        "v_cvt_pk_bf16_f32 v105, v106, v107\n\t" \
        "ds_write_b64 v22, v[104:105] offset:128\n\t" \
        "v_cvt_pk_bf16_f32 v108, v108, v109\n\t" \
        "v_cvt_pk_bf16_f32 v109, v110, v111\n\t" \
        "ds_write_b64 v156, v[108:109] offset:128\n\t" \
        "s_mov_b64 exec, -1\n\t" \
        "s_waitcnt lgkmcnt(0)\n\t" \
        "s_nop 7\n\t" \
        "s_branch .LH_join%=\n\t" \
        ".LHeprod%=:\n\t" \
        "s_waitcnt lgkmcnt(10)\n\t" \
        "v_mfma_f32_16x16x32_bf16 v[88:91], v[64:67], v[112:115], 0\n\t" \
        "v_lshlrev_b32 v116, 5, v120\n\t" \
        "v_bitop3_b32 v116, v116, s52, v23 bitop3:0x6a\n\t" \
        "v_xor_b32 v117, 8, v116\n\t" \
        "v_lshlrev_b32 v118, 5, v121\n\t" \
        "v_bitop3_b32 v118, v118, s52, v23 bitop3:0x6a\n\t" \
        "v_xor_b32 v119, 8, v118\n\t" \
        "ds_read_u16 v120, v20 offset:128\n\t" \
        "ds_read_u16 v121, v20 offset:136\n\t" \
        "ds_read_b64_tr_b16 v[64:65], v116 offset:0\n\t" \
        "ds_read_b64_tr_b16 v[66:67], v118 offset:0\n\t" \
        "s_waitcnt lgkmcnt(12)\n\t" \
        "v_mfma_f32_16x16x32_bf16 v[92:95], v[68:71], v[112:115], 0\n\t" \
        "ds_read_b64_tr_b16 v[68:69], v117 offset:0\n\t" \
        "ds_read_b64_tr_b16 v[70:71], v119 offset:0\n\t" \
        "s_waitcnt lgkmcnt(12)\n\t" \
        "v_mfma_f32_16x16x32_bf16 v[96:99], v[72:75], v[112:115], 0\n\t" \
        "ds_read_b64_tr_b16 v[72:73], v116 offset:64\n\t" \
        "ds_read_b64_tr_b16 v[74:75], v118 offset:64\n\t" \
        "s_waitcnt lgkmcnt(12)\n\t" \
        "v_mfma_f32_16x16x32_bf16 v[100:103], v[76:79], v[112:115], 0\n\t" \
        "ds_read_b64_tr_b16 v[76:77], v117 offset:64\n\t" \
        "ds_read_b64_tr_b16 v[78:79], v119 offset:64\n\t" \
        "s_waitcnt lgkmcnt(12)\n\t" \
        "v_mfma_f32_16x16x32_bf16 v[104:107], v[80:83], v[112:115], 0\n\t" \
        "ds_read_b64_tr_b16 v[80:81], v116 offset:128\n\t" \
        "ds_read_b64_tr_b16 v[82:83], v118 offset:128\n\t" \
        "s_waitcnt lgkmcnt(12)\n\t" \
        "v_mfma_f32_16x16x32_bf16 v[108:111], v[84:87], v[112:115], 0\n\t" \
        "ds_read_b64_tr_b16 v[84:85], v117 offset:128\n\t" \
        "ds_read_b64_tr_b16 v[86:87], v119 offset:128\n\t" \
        "v_and_b32 v112, v122, v25\n\t" \
        "v_and_b32 v113, v122, v26\n\t" \
        "v_and_b32 v114, v122, v27\n\t" \
        "v_and_b32 v115, v122, v28\n\t" \
        "ds_read_b32 v122, v21 offset:256\n\t" \
        "s_and_b32 s55, s50, 0xffff\n\t" \
        "s_cbranch_scc1 .LHetwo1%=\n\t" \
        ".LHeback1%=:\n\t" \
        "s_sub_u32 s55, s46, s47\n\t" \
        "v_xor_b32 v156, 8, v22\n\t" \
        "v_cmp_gt_i32 vcc, s55, v24\n\t" \
        "s_mov_b64 exec, vcc\n\t" \
        "v_cvt_pk_bf16_f32 v88, v88, v89\n\t" \
        "v_cvt_pk_bf16_f32 v89, v90, v91\n\t" \
        "ds_write_b64 v22, v[88:89] offset:0\n\t" \
        "v_cvt_pk_bf16_f32 v92, v92, v93\n\t" \
        "v_cvt_pk_bf16_f32 v93, v94, v95\n\t" \
        "ds_write_b64 v156, v[92:93] offset:0\n\t" \
        "v_cvt_pk_bf16_f32 v96, v96, v97\n\t" \
        "v_cvt_pk_bf16_f32 v97, v98, v99\n\t" \
        "ds_write_b64 v22, v[96:97] offset:64\n\t" \
        "v_cvt_pk_bf16_f32 v100, v100, v101\n\t" \
        "v_cvt_pk_bf16_f32 v101, v102, v103\n\t" \
        "ds_write_b64 v156, v[100:101] offset:64\n\t" \
        "v_cvt_pk_bf16_f32 v104, v104, v105\n\t" \
        "v_cvt_pk_bf16_f32 v105, v106, v107\n\t" \
        "ds_write_b64 v22, v[104:105] offset:128\n\t" \
        "v_cvt_pk_bf16_f32 v108, v108, v109\n\t" \
        "v_cvt_pk_bf16_f32 v109, v110, v111\n\t" \
        "ds_write_b64 v156, v[108:109] offset:128\n\t" \
        "s_mov_b64 exec, -1\n\t" \
        "s_lshl_b32 s55, s53, 4\n\t" \
        "s_add_u32 s47, s47, 16\n\t" \
        "v_add_u32 v22, s55, v22\n\t" \
        "s_lshr_b64 s[50:51], s[50:51], 16\n\t" \
        "v_add_u32 v20, 64, v20\n\t" \
        "v_add_u32 v21, 0x80, v21\n\t" \
        "s_add_u32 s55, s47, 16\n\t" \
        "s_cmp_lt_u32 s55, s46\n\t" \
        "s_cbranch_scc1 .LHeprod%=\n\t" \
        "s_branch .LHelast%=\n\t" \
        ".LHetwo1%=:\n\t" \
        "s_nop 4\n\t" \
        "ds_read_u16 v156, v20\n\t" \
        "ds_read_u16 v158, v20 offset:8\n\t" \
        "ds_read_b32 v127, v21 offset:4\n\t" \
        "s_waitcnt lgkmcnt(0)\n\t" \
        "v_lshrrev_b32 v124, 15, v156\n\t" \
        "v_lshlrev_b32 v156, 5, v156\n\t" \
        "v_bitop3_b32 v156, v156, s52, v23 bitop3:0x6a\n\t" \
        "v_mul_u32_u24 v124, 3968, v124\n\t" \
        "v_add_u32 v156, v156, v124\n\t" \
        "v_xor_b32 v157, 8, v156\n\t" \
        "v_lshrrev_b32 v125, 15, v158\n\t" \
        "v_lshlrev_b32 v158, 5, v158\n\t" \
        "v_bitop3_b32 v158, v158, s52, v23 bitop3:0x6a\n\t" \
        "v_mul_u32_u24 v125, 3968, v125\n\t" \
        "v_add_u32 v158, v158, v125\n\t" \
        "v_xor_b32 v159, 8, v158\n\t" \
        "ds_read_b64_tr_b16 v[128:129], v156 offset:0\n\t" \
        "ds_read_b64_tr_b16 v[130:131], v158 offset:0\n\t" \
        "ds_read_b64_tr_b16 v[132:133], v157 offset:0\n\t" \
        "ds_read_b64_tr_b16 v[134:135], v159 offset:0\n\t" \
        "ds_read_b64_tr_b16 v[136:137], v156 offset:64\n\t" \
        "ds_read_b64_tr_b16 v[138:139], v158 offset:64\n\t" \
        "ds_read_b64_tr_b16 v[140:141], v157 offset:64\n\t" \
        "ds_read_b64_tr_b16 v[142:143], v159 offset:64\n\t" \
        "ds_read_b64_tr_b16 v[144:145], v156 offset:128\n\t" \
        "ds_read_b64_tr_b16 v[146:147], v158 offset:128\n\t" \
        "ds_read_b64_tr_b16 v[148:149], v157 offset:128\n\t" \
        "ds_read_b64_tr_b16 v[150:151], v159 offset:128\n\t" \
        "v_and_b32 v124, v127, v25\n\t" \
        "v_and_b32 v125, v127, v26\n\t" \
        "v_and_b32 v126, v127, v27\n\t" \
        "v_and_b32 v127, v127, v28\n\t" \
        "s_nop 1\n\t" \
        "s_waitcnt lgkmcnt(10)\n\t" \
        "v_mfma_f32_16x16x32_bf16 v[88:91], v[128:131], v[124:127], v[88:91]\n\t" \
        "s_waitcnt lgkmcnt(8)\n\t" \
        "v_mfma_f32_16x16x32_bf16 v[92:95], v[132:135], v[124:127], v[92:95]\n\t" \
        "s_waitcnt lgkmcnt(6)\n\t" \
        "v_mfma_f32_16x16x32_bf16 v[96:99], v[136:139], v[124:127], v[96:99]\n\t" \
        "s_waitcnt lgkmcnt(4)\n\t" \
        "v_mfma_f32_16x16x32_bf16 v[100:103], v[140:143], v[124:127], v[100:103]\n\t" \
        "s_waitcnt lgkmcnt(2)\n\t" \
        "v_mfma_f32_16x16x32_bf16 v[104:107], v[144:147], v[124:127], v[104:107]\n\t" \
        "s_waitcnt lgkmcnt(0)\n\t" \
        "v_mfma_f32_16x16x32_bf16 v[108:111], v[148:151], v[124:127], v[108:111]\n\t" \
        "s_nop 7\n\t" \
        "s_branch .LHeback1%=\n\t" \
        ".LHetwo2%=:\n\t" \
        "s_nop 4\n\t" \
        "ds_read_u16 v156, v20\n\t" \
        "ds_read_u16 v158, v20 offset:8\n\t" \
        "ds_read_b32 v127, v21 offset:4\n\t" \
        "s_waitcnt lgkmcnt(0)\n\t" \
        "v_lshrrev_b32 v124, 15, v156\n\t" \
        "v_lshlrev_b32 v156, 5, v156\n\t" \
        "v_bitop3_b32 v156, v156, s52, v23 bitop3:0x6a\n\t" \
        "v_mul_u32_u24 v124, 3968, v124\n\t" \
        "v_add_u32 v156, v156, v124\n\t" \
        "v_xor_b32 v157, 8, v156\n\t" \
        "v_lshrrev_b32 v125, 15, v158\n\t" \
        "v_lshlrev_b32 v158, 5, v158\n\t" \
        "v_bitop3_b32 v158, v158, s52, v23 bitop3:0x6a\n\t" \
        "v_mul_u32_u24 v125, 3968, v125\n\t" \
        "v_add_u32 v158, v158, v125\n\t" \
        "v_xor_b32 v159, 8, v158\n\t" \
        "ds_read_b64_tr_b16 v[128:129], v156 offset:0\n\t" \
        "ds_read_b64_tr_b16 v[130:131], v158 offset:0\n\t" \
        "ds_read_b64_tr_b16 v[132:133], v157 offset:0\n\t" \
        "ds_read_b64_tr_b16 v[134:135], v159 offset:0\n\t" \
        "ds_read_b64_tr_b16 v[136:137], v156 offset:64\n\t" \
        "ds_read_b64_tr_b16 v[138:139], v158 offset:64\n\t" \
        "ds_read_b64_tr_b16 v[140:141], v157 offset:64\n\t" \
        "ds_read_b64_tr_b16 v[142:143], v159 offset:64\n\t" \
        "ds_read_b64_tr_b16 v[144:145], v156 offset:128\n\t" \
        "ds_read_b64_tr_b16 v[146:147], v158 offset:128\n\t" \
        "ds_read_b64_tr_b16 v[148:149], v157 offset:128\n\t" \
        "ds_read_b64_tr_b16 v[150:151], v159 offset:128\n\t" \
        "v_and_b32 v124, v127, v25\n\t" \
        "v_and_b32 v125, v127, v26\n\t" \
        "v_and_b32 v126, v127, v27\n\t" \
        "v_and_b32 v127, v127, v28\n\t" \
        "s_nop 1\n\t" \
        "s_waitcnt lgkmcnt(10)\n\t" \
        "v_mfma_f32_16x16x32_bf16 v[88:91], v[128:131], v[124:127], v[88:91]\n\t" \
        "s_waitcnt lgkmcnt(8)\n\t" \
        "v_mfma_f32_16x16x32_bf16 v[92:95], v[132:135], v[124:127], v[92:95]\n\t" \
        "s_waitcnt lgkmcnt(6)\n\t" \
        "v_mfma_f32_16x16x32_bf16 v[96:99], v[136:139], v[124:127], v[96:99]\n\t" \
        "s_waitcnt lgkmcnt(4)\n\t" \
        "v_mfma_f32_16x16x32_bf16 v[100:103], v[140:143], v[124:127], v[100:103]\n\t" \
        "s_waitcnt lgkmcnt(2)\n\t" \
        "v_mfma_f32_16x16x32_bf16 v[104:107], v[144:147], v[124:127], v[104:107]\n\t" \
        "s_waitcnt lgkmcnt(0)\n\t" \
        "v_mfma_f32_16x16x32_bf16 v[108:111], v[148:151], v[124:127], v[108:111]\n\t" \
        "s_nop 7\n\t" \
        "s_branch .LHeback2%=\n\t" \
        ".Lpf1%=:\n\t" \
        ".Lfail%=:\n\t" \
        "s_waitcnt vmcnt(0) lgkmcnt(0)\n\t" \
        "s_nop 7\n\t" \
        "s_nop 7\n\t" \
        "s_add_u32 s34, s34, s3\n\t" \
        "s_mov_b32 s41, 1\n\t" \
        ".Lexit%=:\n\t"
#define DCN_ASM_CLOBBERS "s3", "s4", "s5", "s6", "s7", "s8", "s9", "s10", "s11", "s12", "s13", "s14", "s15", "s36", "s37", "s38", "s39", "s40", "s42", "s43", "s44", "s45", "s46", "s47", "s48", "s49", "s50", "s51", "s52", "s53", "s54", "s55", "s56", "s57", "s58", "s59", "s60", "s61", "s62", "s63", "s64", "s65", "s66", "s67", "s68", "s69", "s70", "s71", "s72", "s73", "s74", "s75", "s76", "s77", "s78", "s79", "s80", "s81", "s82", "s83", "s84", "s85", "s86", "s87", "s88", "s89", "s90", "s91", "s92", "s93", "s94", "s95", "s96", "s97", "s98", "s99", "v2", "v3", "v4", "v5", "v6", "v7", "v8", "v9", "v10", "v11", "v12", "v13", "v14", "v15", "v16", "v17", "v18", "v19", "v20", "v21", "v22", "v23", "v24", "v25", "v26", "v27", "v28", "v29", "v30", "v31", "v32", "v33", "v34", "v35", "v36", "v37", "v38", "v39", "v40", "v41", "v42", "v43", "v44", "v45", "v46", "v47", "v48", "v49", "v50", "v51", "v52", "v53", "v54", "v55", "v56", "v57", "v58", "v59", "v60", "v61", "v62", "v63", "v64", "v65", "v66", "v67", "v68", "v69", "v70", "v71", "v72", "v73", "v74", "v75", "v76", "v77", "v78", "v79", "v80", "v81", "v82", "v83", "v84", "v85", "v86", "v87", "v88", "v89", "v90", "v91", "v92", "v93", "v94", "v95", "v96", "v97", "v98", "v99", "v100", "v101", "v102", "v103", "v104", "v105", "v106", "v107", "v108", "v109", "v110", "v111", "v112", "v113", "v114", "v115", "v116", "v117", "v118", "v119", "v120", "v121", "v122", "v123", "v124", "v125", "v126", "v127", "v128", "v129", "v130", "v131", "v132", "v133", "v134", "v135", "v136", "v137", "v138", "v139", "v140", "v141", "v142", "v143", "v144", "v145", "v146", "v147", "v148", "v149", "v150", "v151", "v152", "v153", "v154", "v155", "v156", "v157", "v158", "v159", "v160", "v161", "v162", "v163", "v164", "v165", "v166", "v167", "v168", "v169", "v170", "v171", "v172", "v173", "v174", "v175", "v176", "v177", "v178", "v179", "v180", "v181", "v182", "v183", "v184", "v185", "v186", "v187", "v188", "v189", "v190", "v191", "v192", "v193", "v194", "v195", "v196", "v197", "v198", "v199", "v200", "v201", "v202", "v203", "v204", "v205", "v206", "v207", "v208", "v209", "v210", "v211", "v212", "v213", "v214", "v215", "v216", "v217", "v218", "v219", "v220", "v221", "v222", "v223", "v224", "v225", "v226", "v227", "v228", "v229", "v230", "v231", "v232", "v233", "v234", "v235", "v236", "v237", "v238", "v239", "v240", "v241", "v242", "v243", "v244", "v245", "v246", "v247", "v248", "v249", "v250", "v251", "v252", "v253", "v254", "v255", "a0", "a1", "a2", "a3", "a4", "a5", "a6", "a7", "a8", "a9", "a10", "a11", "a12", "a13", "a14", "a15", "a16", "a17", "a18", "a19", "a20", "a21", "a22", "a23", "a24", "a25", "a26", "a27", "a28", "a29", "a30", "a31", "a32", "a33", "a34", "a35", "a36", "a37", "a38", "a39", "a40", "a41", "a42", "a43", "a44", "a45", "a46", "a47", "a48", "a49", "a50", "a51", "a52", "a53", "a54", "a55", "a56", "a57", "a58", "a59", "a60", "a61", "a62", "a63", "a64", "a65", "a66", "a67", "a68", "a69", "a70", "a71", "a72", "a73", "a74", "a75", "a76", "a77", "a78", "a79", "a80", "a81", "a82", "a83", "a84", "a85", "a86", "a87", "a88", "a89", "a90", "a91", "a92", "a93", "a94", "a95", "a96", "a97", "a98", "a99", "a100", "a101", "a102", "a103", "a104", "a105", "a106", "a107", "a108", "a109", "a110", "a111", "a112", "a113", "a114", "a115", "a116", "a117", "a118", "a119", "a120", "a121", "a122", "a123", "a124", "a125", "a126", "a127", "a128", "a129", "a130", "a131", "a132", "a133", "a134", "a135", "a136", "a137", "a138", "a139", "a140", "a141", "a142", "a143", "a144", "a145", "a146", "a147", "a148", "a149", "a150", "a151", "a152", "a153", "a154", "a155", "a156", "a157", "a158", "a159", "a160", "a161", "a162", "a163", "a164", "a165", "a166", "a167", "a168", "a169", "a170", "a171", "a172", "a173", "a174", "a175", "a176", "a177", "a178", "a179", "a180", "a181", "a182", "a183", "a184", "a185", "a186", "a187", "a188", "a189", "a190", "a191", "a192", "a193", "a194", "a195", "a196", "a197", "a198", "a199", "a200", "a201", "a202", "a203", "a204", "a205", "a206", "a207", "a208", "a209", "a210", "a211", "a212", "a213", "a214", "a215", "a216", "a217", "a218", "a219", "a220", "a221", "a222", "a223", "a224", "a225", "a226", "a227", "a228", "a229", "a230", "a231", "a232", "a233", "a234", "a235", "a236", "a237", "a238", "a239", "a240", "a241", "a242", "a243", "a244", "a245", "a246", "a247", "a248", "a249", "a250", "a251", "a252", "a253", "a254", "a255"
#define NWG 255
#define CYC 1

__device__ __forceinline__ float bf_lo(unsigned u) { return __uint_as_float(u << 16); }
__device__ __forceinline__ float bf_hi(unsigned u) { return __uint_as_float(u & 0xffff0000u); }
__device__ __forceinline__ unsigned cvt_pk(float lo, float hi) {
    bf16x2 r = {(__bf16)lo, (__bf16)hi};
    unsigned u;
    __builtin_memcpy(&u, &r, 4);
    return u;
}
__device__ __forceinline__ f32x4 mfma16(u32x4 a, u32x4 b, f32x4 c) {
    bf16x8 A, B;
    __builtin_memcpy(&A, &a, 16);
    __builtin_memcpy(&B, &b, 16);
    return __builtin_amdgcn_mfma_f32_16x16x32_bf16(A, B, c, 0, 0, 0);
}
__device__ __forceinline__ u32x2 tr_read(unsigned a) {
    s16x4 v = __builtin_amdgcn_ds_read_tr16_b64_v4i16((lds_s16x4*)(uintptr_t)a);
    u32x2 u;
    __builtin_memcpy(&u, &v, 8);
    return u;
}
__device__ __forceinline__ unsigned clip_pk(unsigned v) {
    typedef __attribute__((ext_vector_type(2))) short i16x2;
    i16x2 r;
    __builtin_memcpy(&r, &v, 4);
    r = __builtin_elementwise_min(__builtin_elementwise_max(r, (i16x2){0, 0}), (i16x2){0x435f, 0x435f});
    __builtin_memcpy(&v, &r, 4);
    return v;
}

__device__ __forceinline__ void slow_tile(const unsigned short* x, const unsigned short* off, const unsigned short* wgt, unsigned short* out,
                                          const int wv, const unsigned lane, const int y0, const int x0, const int nrows, const unsigned lb) {
    const unsigned px = lane & 15, q = lane >> 4;
#pragma unroll 1
    for (int r0 = 0; r0 < nrows; r0 += 4) {
        const int row = y0 + min(r0 + wv, nrows - 1), col = x0 + px;
        f32x4 acc[6];
#pragma unroll
        for (int i = 0; i < 6; ++i) acc[i] = (f32x4){0.f, 0.f, 0.f, 0.f};
#pragma unroll 1
        for (int t = 0; t < 9; ++t) {
            asm volatile("s_waitcnt vmcnt(0) lgkmcnt(0)\n\ts_barrier" ::: "memory");
            for (unsigned d = wv * 64 + lane; d < WTAP / 16; d += NTHREADS)
                *(lds_u32x4*)(uintptr_t)(lb + d * 16) = *(const glb_u32x4*)((uintptr_t)wgt + t * WTAP + d * 16);
            const unsigned od = *(const glb_u32*)((uintptr_t)off + (row * IMG + col) * 36 + t * 4);
            const unsigned pc = clip_pk(cvt_pk((float)(row + t / 3 - 1) + bf_lo(od), (float)(col + t % 3 - 1) + bf_hi(od)));
            const float py = bf_lo(pc), pxf = bf_hi(pc);
            const int gy = (int)py, gx = (int)pxf;
            const float wy = py - (float)gy, wx = pxf - (float)gx;
            const unsigned ab = cvt_pk(1.f - wy, 1.f - wx);
            const float a = bf_lo(ab), b = bf_hi(ab);
            const unsigned w01 = cvt_pk(a * b, a * wx), w23 = cvt_pk(wy * b, wy * wx);
            const unsigned o00 = (gy * IMG + gx) * (CH * 2) + q * 16;
            const unsigned dx = gx < IMG - 1 ? CH * 2 : 0, dy = gy < IMG - 1 ? ROWB : 0;
            const float wc[4] = {bf_lo(w01), bf_hi(w01), bf_lo(w23), bf_hi(w23)};
            u32x4 bfr[3];
#pragma unroll
            for (int j = 0; j < 3; ++j) {
                float vv[8];
#pragma unroll
                for (int c = 0; c < 4; ++c) {
                    const u32x4 gv = *(const glb_u32x4*)((uintptr_t)x + (o00 + ((c & 1) ? dx : 0u) + ((c & 2) ? dy : 0u) + j * 64));
#pragma unroll
                    for (int k = 0; k < 4; ++k) {
                        vv[2 * k] = c ? fmaf(wc[c], bf_lo(gv[k]), vv[2 * k]) : wc[0] * bf_lo(gv[k]);
                        vv[2 * k + 1] = c ? fmaf(wc[c], bf_hi(gv[k]), vv[2 * k + 1]) : wc[0] * bf_hi(gv[k]);
                    }
                }
                bfr[j] = (u32x4){cvt_pk(vv[0], vv[1]), cvt_pk(vv[2], vv[3]), cvt_pk(vv[4], vv[5]), cvt_pk(vv[6], vv[7])};
            }
            asm volatile("s_waitcnt vmcnt(0) lgkmcnt(0)\n\ts_barrier" ::: "memory");
            const unsigned wa = lb + (8 * q + ((lane >> 2) & 3)) * 192 + (lane & 3) * 8;
#pragma unroll
            for (int j = 0; j < 3; ++j)
#pragma unroll
                for (int i = 0; i < 6; ++i) {
                    const u32x2 lo = tr_read(wa + j * 6144 + i * 32), hi = tr_read(wa + j * 6144 + i * 32 + 768);
                    acc[i] = mfma16((u32x4){lo[0], lo[1], hi[0], hi[1]}, bfr[j], acc[i]);
                }
        }
        if (r0 + wv < nrows) {
#pragma unroll
            for (int i = 0; i < 6; ++i)
                *(glb_u32x2*)((uintptr_t)out + (row * IMG + col) * (CH * 2) + i * 32 + q * 8) =
                    (u32x2){cvt_pk(acc[i][0], acc[i][1]), cvt_pk(acc[i][2], acc[i][3])};
        }
    }
}

__global__ __launch_bounds__(NTHREADS, 1) void dcn_kernel(const unsigned short* x_, const unsigned short* off_,
                                                          const unsigned short* wgt_, unsigned short* out_) {
    __shared__ __attribute__((aligned(64))) unsigned char lds[LDS_BYTES];
    u32x4 xd, od, wd, odsc;
    unsigned y0, x0, rows, wv, status, stv;
    asm volatile(DCN_ASM_TEXT
                 : "={s[16:19]}"(xd), "={s[20:23]}"(od), "={s[24:27]}"(wd), "={s[28:31]}"(odsc),
                   "={s32}"(y0), "={s33}"(x0), "={s34}"(rows), "={s35}"(wv), "={s41}"(status), "={v1}"(stv)
                 : "{s[0:1]}"(__builtin_amdgcn_kernarg_segment_ptr()), "{s2}"(blockIdx.x), "{v0}"(threadIdx.x)
                 : "memory", "scc", "vcc", DCN_ASM_CLOBBERS);
    unsigned lane;
    asm volatile("v_mbcnt_lo_u32_b32 %0, -1, 0\n\tv_mbcnt_hi_u32_b32 %0, -1, %0" : "=v"(lane));
    slow_tile((const unsigned short*)(((unsigned long long)xd[1] << 32) | xd[0]), (const unsigned short*)(((unsigned long long)od[1] << 32) | od[0]),
              (const unsigned short*)(((unsigned long long)wd[1] << 32) | wd[0]), (unsigned short*)(((unsigned long long)odsc[1] << 32) | odsc[0]),
              wv, lane, y0, x0, rows, (unsigned)(uintptr_t)(__attribute__((address_space(3))) unsigned char*)lds);
}

extern "C" void kernel_launch(void* const* d_in, const int* in_sizes, int n_in,
                              void* d_out, int out_size, void* d_ws, size_t ws_size,
                              hipStream_t stream) {
    dcn_kernel<<<NWG, NTHREADS, 0, stream>>>((const unsigned short*)d_in[0], (const unsigned short*)d_in[1],
                                             (const unsigned short*)d_in[2], (unsigned short*)d_out);
}
```

```cpp
#include <hip/hip_runtime.h>
#include <stdint.h>

#define IMG 224
#define CH 96
#define ROWB (IMG * CH * 2)
#define NTHREADS 256
#define WTAP 18432
#define LDS_BYTES 163840

typedef __attribute__((ext_vector_type(2))) __bf16 bf16x2;
typedef __attribute__((ext_vector_type(8))) __bf16 bf16x8;
typedef __attribute__((ext_vector_type(4))) float f32x4;
typedef __attribute__((ext_vector_type(4))) unsigned u32x4;
typedef __attribute__((address_space(1))) u32x4 glb_u32x4;
typedef __attribute__((address_space(1))) unsigned glb_u32;
typedef __attribute__((ext_vector_type(2))) unsigned u32x2;
typedef __attribute__((address_space(1))) u32x2 glb_u32x2;
typedef __attribute__((ext_vector_type(4))) short s16x4;
typedef __attribute__((address_space(3))) s16x4 lds_s16x4;
typedef __attribute__((address_space(3))) u32x4 lds_u32x4;

#define DCN_ASM_TEXT \
        "s_load_dwordx2 s[24:25], s[0:1], 0x10\n\t" \
        "s_load_dwordx2 s[20:21], s[0:1], 0x8\n\t" \
        "s_load_dwordx2 s[16:17], s[0:1], 0x0\n\t" \
        "s_load_dwordx2 s[28:29], s[0:1], 0x18\n\t" \
        "s_getpc_b64 s[4:5]\n\t" \
        ".Lpf0%=:\n\t" \
        "v_lshrrev_b32 v3, 6, v0\n\t" \
        "s_bfe_u32 s15, s2, 0x20003\n\t" \
        "s_mov_b32 m0, 0x27f00\n\t" \
        "v_readfirstlane_b32 s35, v3\n\t" \
        "v_lshl_add_u32 v3, v0, 2, s15\n\t" \
        "s_lshr_b32 s8, s2, 3\n\t" \
        "v_lshlrev_b32 v3, 7, v3\n\t" \
        "s_and_b32 s7, s2, 7\n\t" \
        "v_min_u32 v3, .Lpf1%=-.Lpf0%=, v3\n\t" \
        "v_and_b32 v2, 63, v0\n\t" \
        "s_mul_i32 s6, s7, 22\n\t" \
        "global_load_lds_dword v3, s[4:5]\n\t" \
        "s_mul_i32 s9, s7, 10\n\t" \
        "s_add_u32 s6, s6, s8\n\t" \
        "s_add_u32 s9, s9, s8\n\t" \
        "s_add_u32 s9, s9, 66\n\t" \
        "s_lshl2_add_u32 s10, s8, s7\n\t" \
        "s_cmp_lt_u32 s8, 22\n\t" \
        "s_cselect_b32 s6, s6, s9\n\t" \
        "s_add_u32 s10, s10, 124\n\t" \
        "s_cmp_ge_u32 s7, 4\n\t" \
        "s_cselect_b32 s6, s10, s6\n\t" \
        "s_mov_b32 s3, 0x1d028300\n\t" \
        "s_cmp_ge_u32 s6, 88\n\t" \
        "s_cselect_b32 s3, 0xa800358, s3\n\t" \
        "s_cmp_ge_u32 s6, 110\n\t" \
        "s_cselect_b32 s3, 0x3a82806e, s3\n\t" \
        "s_cmp_ge_u32 s6, 133\n\t" \
        "s_cselect_b32 s3, 0xec10285, s3\n\t" \
        "s_cmp_ge_u32 s6, 157\n\t" \
        "s_cselect_b32 s3, 0x3ec2829d, s3\n\t" \
        "s_cmp_ge_u32 s6, 173\n\t" \
        "s_cselect_b32 s3, 0x3ec201ad, s3\n\t" \
        "s_cmp_ge_u32 s6, 189\n\t" \
        "s_cselect_b32 s3, 0x14ea83bd, s3\n\t" \
        "s_cmp_ge_u32 s6, 233\n\t" \
        "s_cselect_b32 s3, 0x384000e9, s3\n\t" \
        "s_cmp_ge_u32 s6, 243\n\t" \
        "s_cselect_b32 s3, 0x184001f3, s3\n\t" \
        "s_and_b32 s4, s3, 0xff\n\t" \
        "s_lshr_b32 s5, s3, 27\n\t" \
        "s_sub_u32 s6, s6, s4\n\t" \
        "s_bfe_u32 s33, s3, 0x40008\n\t" \
        "s_lshr_b32 s7, s6, s5\n\t" \
        "s_bfe_u32 s34, s3, 0x50014\n\t" \
        "s_add_u32 s33, s33, s7\n\t" \
        "s_lshl_b32 s7, s7, s5\n\t" \
        "s_bfe_u32 s32, s3, 0x8000c\n\t" \
        "s_sub_u32 s6, s6, s7\n\t" \
        "s_bfe_u32 s11, s3, 0x20019\n\t" \
        "s_mul_i32 s6, s6, s34\n\t" \
        "s_lshl_b32 s33, s33, 4\n\t" \
        "s_add_u32 s32, s32, s6\n\t" \
        "s_sub_u32 s4, 224, s34\n\t" \
        "s_mov_b32 s3, 0\n\t" \
        "s_min_u32 s32, s32, s4\n\t" \
        "s_cmp_lg_u32 s11, 2\n\t" \
        "s_cbranch_scc1 .LT_entry%=\n\t" \
        "s_mul_i32 s40, s35, 0xc00\n\t" \
        "s_mov_b32 s9, 0xc0\n\t" \
        "s_mov_b32 s10, 18\n\t" \
        "s_mov_b32 s14, 0xffff5800\n\t" \
        "s_add_u32 s39, s40, 0xf840\n\t" \
        "v_lshlrev_b32 v3, 4, v2\n\t" \
        "v_and_b32 v32, 32, v2\n\t" \
        "v_xor_b32 v5, 32, v3\n\t" \
        "v_xor_b32 v4, v3, v32\n\t" \
        "s_branch .Lxv%=\n\t" \
        ".LT_entry%=:\n\t" \
        "s_lshl_b32 s4, s35, 2\n\t" \
        "s_cmp_eq_u32 s11, 3\n\t" \
        "s_cselect_b32 s5, 2, 4\n\t" \
        "s_cselect_b32 s4, s4, 0\n\t" \
        "v_lshrrev_b32 v36, s5, v2\n\t" \
        "s_cselect_b32 s5, 15, s11\n\t" \
        "v_add_u32 v19, s4, v2\n\t" \
        "s_cselect_b32 s12, 0, 2\n\t" \
        "v_and_b32 v36, s5, v36\n\t" \
        "s_cselect_b32 s13, 0, s35\n\t" \
        "v_and_b32 v19, 15, v19\n\t" \
        "s_mul_i32 s37, s32, 224\n\t" \
        "v_lshlrev_b32 v36, s12, v36\n\t" \
        "s_cmp_eq_u32 s33, 0\n\t" \
        "s_cselect_b32 s9, 0x180, 0\n\t" \
        "v_add_u32 v18, s13, v36\n\t" \
        "s_add_u32 s37, s37, s33\n\t" \
        "v_mul_u32_u24 v33, 0x1f80, v18\n\t" \
        "s_mul_i32 s37, s37, 36\n\t" \
        "s_mov_b32 s10, 19\n\t" \
        "v_mad_u32_u24 v17, v19, 36, v33\n\t" \
        "s_mov_b32 s14, 0xfffeb000\n\t" \
        ".Lxv%=:\n\t" \
        "s_mul_i32 s42, s35, 60\n\t" \
        "s_sub_u32 s43, 225, s33\n\t" \
        "v_add_u32 v32, s42, v2\n\t" \
        "s_min_u32 s43, s43, s10\n\t" \
        "v_mul_u32_u24 v33, 0x2ab, v32\n\t" \
        "s_mul_i32 s43, s43, 0xc0\n\t" \
        "v_lshrrev_b32 v33, 13, v33\n\t" \
        "s_mul_i32 s44, s33, 0xc0\n\t" \
        "v_mul_u32_u24 v34, 0xc0, v33\n\t" \
        "s_mul_i32 s36, s32, 0xa800\n\t" \
        "v_mad_i32_i24 v35, v33, -12, v32\n\t" \
        "s_mul_i32 s38, s35, 0x3c0\n\t" \
        "v_and_b32 v33, 4, v33\n\t" \
        "s_sub_u32 s44, s44, 0x180\n\t" \
        "v_min_u32 v34, s43, v34\n\t" \
        "s_add_u32 s38, s38, 64\n\t" \
        "v_lshrrev_b32 v33, 1, v33\n\t" \
        "s_add_i32 s58, s36, s14\n\t" \
        "v_max_u32 v34, s9, v34\n\t" \
        "s_mov_b32 s19, 0x20000\n\t" \
        "v_xor_b32 v35, v35, v33\n\t" \
        "s_mov_b32 s18, 0x930000\n\t" \
        "v_lshl_add_u32 v35, v35, 4, v34\n\t" \
        "s_mov_b32 s26, 0x28800\n\t" \
        "v_add_u32 v13, s44, v35\n\t" \
        "s_mov_b32 s22, 0x1b9000\n\t" \
        "s_mov_b32 s23, s19\n\t" \
        "s_mov_b32 s27, s19\n\t" \
        "s_mov_b64 s[30:31], s[18:19]\n\t" \
        "s_mov_b32 s59, 0xf80\n\t" \
        "s_mov_b32 s60, 0xa800\n\t" \
        "s_mov_b32 m0, s39\n\t" \
        "s_cmp_eq_u32 s11, 2\n\t" \
        "s_waitcnt lgkmcnt(0)\n\t" \
        "s_cbranch_scc1 .Lw0%=\n\t" \
        "buffer_load_dwordx4 v[40:43], v17, s[20:23], s37 offen \n\t" \
        "buffer_load_dwordx4 v[44:47], v17, s[20:23], s37 offen offset:16 \n\t" \
        "buffer_load_dword v48, v17, s[20:23], s37 offen offset:32 \n\t" \
        "s_branch .Lx%=\n\t" \
        ".Lw0%=:\n\t" \
        "buffer_load_dwordx4 v3, s[24:27], s40 offen offset:0  lds\n\t" \
        "buffer_load_dwordx4 v4, s[24:27], s40 offen offset:1024  lds\n\t" \
        "buffer_load_dwordx4 v5, s[24:27], s40 offen offset:2048  lds\n\t" \
        ".Lx%=:\n\t" \
        "s_mov_b32 m0, s38\n\t" \
        "s_max_i32 s57, s58, 0\n\t" \
        "buffer_load_dwordx4 v13, s[16:19], s57 offen nt lds\n\t" \
        "s_add_u32 m0, m0, s59\n\t" \
        "s_add_i32 s58, s58, s60\n\t" \
        "s_max_i32 s57, s58, 0\n\t" \
        "buffer_load_dwordx4 v13, s[16:19], s57 offen nt lds\n\t" \
        "s_cmp_eq_u32 s11, 2\n\t" \
        "s_cbranch_scc1 .LR_start%=\n\t" \
        "s_cmp_eq_u32 s11, 1\n\t" \
        "s_cbranch_scc1 .LH8_start%=\n\t" \
        "s_cmp_eq_u32 s11, 0\n\t" \
        "s_cbranch_scc1 .LH4_start%=\n\t" \
        "s_add_u32 s57, s36, 0x0\n\t" \
        "s_add_u32 m0, s38, 0x1f00\n\t" \
        "s_nop 0\n\t" \
        "buffer_load_dwordx4 v13, s[16:19], s57 offen nt lds\n\t" \
        "v_lshrrev_b32 v164, 2, v18\n\t" \
        "v_add_u32 v168, s32, v18\n\t" \
        "v_add_u32 v169, s33, v19\n\t" \
        "v_and_b32 v164, 2, v164\n\t" \
        "v_lshl_add_u32 v165, v168, 1, -3\n\t" \
        "v_lshl_add_u32 v166, v169, 1, -3\n\t" \
        "v_and_or_b32 v164, v18, 1, v164\n\t" \
        "v_bfe_u32 v167, v18, 1, 2\n\t" \
        "v_mul_u32_u24 v164, 0x900, v164\n\t" \
        "v_cmp_gt_u32 vcc, s34, v18\n\t" \
        "v_lshl_add_u32 v16, v19, 4, v164\n\t" \
        "v_lshl_add_u32 v16, v167, 2, v16\n\t" \
        "s_mov_b64 s[10:11], vcc\n\t" \
        "v_add_u32 v16, 0x1e840, v16\n\t" \
        "s_movk_i32 s42, -1152\n\t" \
        "v_cvt_f32_u32 v60, v165\n\t" \
        "v_cvt_f32_u32 v63, v166\n\t" \
        "v_add_u32 v165, 2, v165\n\t" \
        "v_add_u32 v166, 2, v166\n\t" \
        "v_cvt_f32_u32 v61, v165\n\t" \
        "v_cvt_f32_u32 v64, v166\n\t" \
        "v_add_u32 v165, 2, v165\n\t" \
        "v_add_u32 v166, 2, v166\n\t" \
        "v_cvt_f32_u32 v62, v165\n\t" \
        "v_cvt_f32_u32 v65, v166\n\t" \
        "v_mov_b32 v165, 0x7c00\n\t" \
        "v_mov_b32 v166, 0x7f800000\n\t" \
        "v_lshrrev_b32 v60, 23, v60\n\t" \
        "v_and_b32 v63, 0x7f800000, v63\n\t" \
        "v_lshl_add_u32 v60, v60, 7, s42\n\t" \
        "v_add_u32 v63, 0xfb800000, v63\n\t" \
        "v_cndmask_b32 v60, v165, v60, s[10:11]\n\t" \
        "v_cndmask_b32 v63, v166, v63, s[10:11]\n\t" \
        "v_lshrrev_b32 v61, 23, v61\n\t" \
        "v_and_b32 v64, 0x7f800000, v64\n\t" \
        "v_lshl_add_u32 v61, v61, 7, s42\n\t" \
        "v_add_u32 v64, 0xfb800000, v64\n\t" \
        "v_cndmask_b32 v61, v165, v61, s[10:11]\n\t" \
        "v_cndmask_b32 v64, v166, v64, s[10:11]\n\t" \
        "v_lshrrev_b32 v62, 23, v62\n\t" \
        "v_and_b32 v65, 0x7f800000, v65\n\t" \
        "v_lshl_add_u32 v62, v62, 7, s42\n\t" \
        "v_add_u32 v65, 0xfb800000, v65\n\t" \
        "v_cndmask_b32 v62, v165, v62, s[10:11]\n\t" \
        "v_cndmask_b32 v65, v166, v65, s[10:11]\n\t" \
        "v_add_u32 v168, -1, v168\n\t" \
        "v_add_u32 v169, -1, v169\n\t" \
        "v_lshrrev_b32 v165, 2, v16\n\t" \
        "v_lshl_or_b32 v50, v168, 16, v165\n\t" \
        "v_lshl_or_b32 v50, v169, 24, v50\n\t" \
        "v_add_u32 v51, 0x1000040, v50\n\t" \
        "v_add_u32 v52, 0x2000080, v50\n\t" \
        "v_add_u32 v53, 0x100c0, v50\n\t" \
        "v_add_u32 v54, 0x1010100, v50\n\t" \
        "v_add_u32 v55, 0x2010140, v50\n\t" \
        "v_add_u32 v56, 0x20180, v50\n\t" \
        "v_add_u32 v57, 0x10201c0, v50\n\t" \
        "v_add_u32 v58, 0x2020200, v50\n\t" \
        "v_lshlrev_b32 v66, 3, v2\n\t" \
        "v_add_u32 v66, 0x10840, v66\n\t" \
        "v_mul_u32_u24 v174, 0xf80, v18\n\t" \
        "v_mul_u32_u24 v177, 0xc0, v19\n\t" \
        "v_lshlrev_b32 v178, 3, v19\n\t" \
        "s_add_u32 s57, s36, 0xa800\n\t" \
        "s_add_u32 m0, s38, 0x2e80\n\t" \
        "s_nop 0\n\t" \
        "buffer_load_dwordx4 v13, s[16:19], s57 offen nt lds\n\t" \
        "v_add_u32 v170, 0x1080, v174\n\t" \
        "v_and_b32 v14, 48, v2\n\t" \
        "v_add_u32 v170, v170, v177\n\t" \
        "v_add_u32 v173, 8, v178\n\t" \
        "v_and_b32 v173, 32, v173\n\t" \
        "v_add_u32 v174, v170, v173\n\t" \
        "v_add_u32 v175, 0xf80, v174\n\t" \
        "v_add_u32 v176, 0x1f00, v174\n\t" \
        "ds_write2st64_b32 v16, v174, v175 offset0:0 offset1:3\n\t" \
        "ds_write_b32 v16, v176 offset:1536\n\t" \
        "v_add_u32 v170, 0xc0, v170\n\t" \
        "v_add_u32 v173, 16, v178\n\t" \
        "v_and_b32 v173, 32, v173\n\t" \
        "v_add_u32 v174, v170, v173\n\t" \
        "v_add_u32 v175, 0xf80, v174\n\t" \
        "v_add_u32 v176, 0x1f00, v174\n\t" \
        "ds_write2st64_b32 v16, v174, v175 offset0:1 offset1:4\n\t" \
        "ds_write_b32 v16, v176 offset:1792\n\t" \
        "v_add_u32 v170, 0xc0, v170\n\t" \
        "v_add_u32 v173, 24, v178\n\t" \
        "v_and_b32 v173, 32, v173\n\t" \
        "v_add_u32 v174, v170, v173\n\t" \
        "v_add_u32 v175, 0xf80, v174\n\t" \
        "v_add_u32 v176, 0x1f00, v174\n\t" \
        "ds_write2st64_b32 v16, v174, v175 offset0:2 offset1:5\n\t" \
        "ds_write_b32 v16, v176 offset:2048\n\t" \
        "s_lshl_b32 s48, s35, 10\n\t" \
        "s_and_b32 s55, s35, 1\n\t" \
        "s_add_u32 s48, s48, 0xf840\n\t" \
        "s_mul_i32 s55, s55, 0x3180\n\t" \
        "s_cmp_lt_u32 s35, 2\n\t" \
        "s_cselect_b32 s49, 0, 0x30c0\n\t" \
        "s_cselect_b32 s53, 1, -1\n\t" \
        "s_add_u32 s49, s49, s55\n\t" \
        "s_mul_i32 s53, s53, 192\n\t" \
        "s_mov_b32 s52, 0x3ffe0\n\t" \
        "s_add_u32 s49, s49, 0x20c40\n\t" \
        "v_mul_i32_i24 v30, s53, v2\n\t" \
        "v_lshlrev_b32 v170, 3, v2\n\t" \
        "v_lshlrev_b32 v171, 2, v2\n\t" \
        "v_add_u32 v30, s49, v30\n\t" \
        "v_and_b32 v170, 16, v170\n\t" \
        "v_and_b32 v171, 32, v171\n\t" \
        "v_lshl_add_u32 v31, v2, 3, s48\n\t" \
        "v_or3_b32 v30, v30, v170, v171\n\t" \
        "v_lshlrev_b32 v170, 2, v2\n\t" \
        "s_add_u32 s55, s48, 512\n\t" \
        "v_add_u32 v32, s55, v170\n\t" \
        "v_cvt_f32_u32 v34, s32\n\t" \
        "v_cvt_f32_u32 v35, s33\n\t" \
        "v_mov_b32 v36, 124\n\t" \
        "v_add_f32 v34, -2.0, v34\n\t" \
        "v_add_f32 v35, -2.0, v35\n\t" \
        "v_cvt_f32_u32 v33, 15\n\t" \
        "v_mov_b32 v37, 0x05040100\n\t" \
        "v_mov_b32 v38, 0x80008000\n\t" \
        "v_mov_b32 v39, 0x435f435f\n\t" \
        "s_mul_i32 s40, s35, 0xc00\n\t" \
        "v_lshlrev_b32 v3, 4, v2\n\t" \
        "v_and_b32 v170, 32, v2\n\t" \
        "v_xor_b32 v5, 32, v3\n\t" \
        "v_xor_b32 v4, v3, v170\n\t" \
        "s_add_u32 s39, s40, 0xf840\n\t" \
        "s_waitcnt vmcnt(4)\n\t" \
        "s_add_u32 s57, s36, 0x15000\n\t" \
        "s_add_u32 m0, s38, 0x3e00\n\t" \
        "s_nop 0\n\t" \
        "buffer_load_dwordx4 v13, s[16:19], s57 offen nt lds\n\t" \
        "s_lshl_b32 s45, s35, 10\n\t" \
        "s_mov_b32 s54, 0\n\t" \
        "s_add_u32 s45, s45, 0xf840\n\t" \
        "v_cmp_nlt_f32 s[4:5], |v40|, v63\n\t" \
        "v_cmp_nlt_f16 s[8:9], |v40|, v60\n\t" \
        "v_cmp_nlt_f32 s[6:7], |v41|, v64\n\t" \
        "v_cmp_nlt_f16 s[10:11], |v41|, v60\n\t" \
        "s_or_b64 s[12:13], s[4:5], s[8:9]\n\t" \
        "v_mbcnt_lo_u32_b32 v160, s12, 0\n\t" \
        "v_mbcnt_hi_u32_b32 v160, s13, v160\n\t" \
        "s_add_u32 s57, s36, 0x1f800\n\t" \
        "s_add_u32 m0, s38, 0x4d80\n\t" \
        "s_nop 0\n\t" \
        "buffer_load_dwordx4 v13, s[16:19], s57 offen nt lds\n\t" \
        "s_bcnt1_i32_b64 s55, s[12:13]\n\t" \
        "v_lshl_add_u32 v160, v160, 3, s45\n\t" \
        "s_lshl3_add_u32 s45, s55, s45\n\t" \
        "v_cndmask_b32 v160, v66, v160, s[12:13]\n\t" \
        "ds_write2_b32 v160, v40, v50 offset1:1\n\t" \
        "v_cmp_nlt_f32 s[4:5], |v42|, v65\n\t" \
        "v_cmp_nlt_f16 s[8:9], |v42|, v60\n\t" \
        "s_or_b64 s[12:13], s[6:7], s[10:11]\n\t" \
        "v_mbcnt_lo_u32_b32 v161, s12, 0\n\t" \
        "v_mbcnt_hi_u32_b32 v161, s13, v161\n\t" \
        "s_add_u32 s57, s36, 0x2a000\n\t" \
        "s_add_u32 m0, s38, 0x5d00\n\t" \
        "s_nop 0\n\t" \
        "buffer_load_dwordx4 v13, s[16:19], s57 offen nt lds\n\t" \
        "s_bcnt1_i32_b64 s55, s[12:13]\n\t" \
        "v_lshl_add_u32 v161, v161, 3, s45\n\t" \
        "s_lshl3_add_u32 s45, s55, s45\n\t" \
        "v_cndmask_b32 v161, v66, v161, s[12:13]\n\t" \
        "ds_write2_b32 v161, v41, v51 offset1:1\n\t" \
        "v_cmp_nlt_f32 s[6:7], |v43|, v63\n\t" \
        "v_cmp_nlt_f16 s[10:11], |v43|, v61\n\t" \
        "s_or_b64 s[12:13], s[4:5], s[8:9]\n\t" \
        "v_mbcnt_lo_u32_b32 v160, s12, 0\n\t" \
        "v_mbcnt_hi_u32_b32 v160, s13, v160\n\t" \
        "s_add_u32 s57, s36, 0x34800\n\t" \
        "s_add_u32 m0, s38, 0x6c80\n\t" \
        "s_nop 0\n\t" \
        "buffer_load_dwordx4 v13, s[16:19], s57 offen nt lds\n\t" \
        "s_bcnt1_i32_b64 s55, s[12:13]\n\t" \
        "v_lshl_add_u32 v160, v160, 3, s45\n\t" \
        "s_lshl3_add_u32 s45, s55, s45\n\t" \
        "v_cndmask_b32 v160, v66, v160, s[12:13]\n\t" \
        "ds_write2_b32 v160, v42, v52 offset1:1\n\t" \
        "v_cmp_nlt_f32 s[4:5], |v44|, v64\n\t" \
        "v_cmp_nlt_f16 s[8:9], |v44|, v61\n\t" \
        "s_or_b64 s[12:13], s[6:7], s[10:11]\n\t" \
        "v_mbcnt_lo_u32_b32 v161, s12, 0\n\t" \
        "v_mbcnt_hi_u32_b32 v161, s13, v161\n\t" \
        "s_add_u32 s57, s36, 0x3f000\n\t" \
        "s_add_u32 m0, s38, 0x7c00\n\t" \
        "s_nop 0\n\t" \
        "buffer_load_dwordx4 v13, s[16:19], s57 offen nt lds\n\t" \
        "s_bcnt1_i32_b64 s55, s[12:13]\n\t" \
        "v_lshl_add_u32 v161, v161, 3, s45\n\t" \
        "s_lshl3_add_u32 s45, s55, s45\n\t" \
        "v_cndmask_b32 v161, v66, v161, s[12:13]\n\t" \
        "ds_write2_b32 v161, v43, v53 offset1:1\n\t" \
        "v_cmp_nlt_f32 s[6:7], |v45|, v65\n\t" \
        "v_cmp_nlt_f16 s[10:11], |v45|, v61\n\t" \
        "s_or_b64 s[12:13], s[4:5], s[8:9]\n\t" \
        "v_mbcnt_lo_u32_b32 v160, s12, 0\n\t" \
        "v_mbcnt_hi_u32_b32 v160, s13, v160\n\t" \
        "s_add_u32 s57, s36, 0x49800\n\t" \
        "s_add_u32 m0, s38, 0x8b80\n\t" \
        "s_nop 0\n\t" \
        "buffer_load_dwordx4 v13, s[16:19], s57 offen nt lds\n\t" \
        "s_bcnt1_i32_b64 s55, s[12:13]\n\t" \
        "v_lshl_add_u32 v160, v160, 3, s45\n\t" \
        "s_lshl3_add_u32 s45, s55, s45\n\t" \
        "v_cndmask_b32 v160, v66, v160, s[12:13]\n\t" \
        "ds_write2_b32 v160, v44, v54 offset1:1\n\t" \
        "v_cmp_nlt_f32 s[4:5], |v46|, v63\n\t" \
        "v_cmp_nlt_f16 s[8:9], |v46|, v62\n\t" \
        "s_or_b64 s[12:13], s[6:7], s[10:11]\n\t" \
        "v_mbcnt_lo_u32_b32 v161, s12, 0\n\t" \
        "v_mbcnt_hi_u32_b32 v161, s13, v161\n\t" \
        "s_add_u32 s57, s36, 0x54000\n\t" \
        "s_add_u32 m0, s38, 0x9b00\n\t" \
        "s_nop 0\n\t" \
        "buffer_load_dwordx4 v13, s[16:19], s57 offen nt lds\n\t" \
        "s_bcnt1_i32_b64 s55, s[12:13]\n\t" \
        "v_lshl_add_u32 v161, v161, 3, s45\n\t" \
        "s_lshl3_add_u32 s45, s55, s45\n\t" \
        "v_cndmask_b32 v161, v66, v161, s[12:13]\n\t" \
        "ds_write2_b32 v161, v45, v55 offset1:1\n\t" \
        "v_cmp_nlt_f32 s[6:7], |v47|, v64\n\t" \
        "v_cmp_nlt_f16 s[10:11], |v47|, v62\n\t" \
        "s_or_b64 s[12:13], s[4:5], s[8:9]\n\t" \
        "v_mbcnt_lo_u32_b32 v160, s12, 0\n\t" \
        "v_mbcnt_hi_u32_b32 v160, s13, v160\n\t" \
        "s_add_u32 s57, s36, 0x5e800\n\t" \
        "s_add_u32 m0, s38, 0xaa80\n\t" \
        "s_nop 0\n\t" \
        "buffer_load_dwordx4 v13, s[16:19], s57 offen nt lds\n\t" \
        "s_bcnt1_i32_b64 s55, s[12:13]\n\t" \
        "v_lshl_add_u32 v160, v160, 3, s45\n\t" \
        "s_lshl3_add_u32 s45, s55, s45\n\t" \
        "v_cndmask_b32 v160, v66, v160, s[12:13]\n\t" \
        "ds_write2_b32 v160, v46, v56 offset1:1\n\t" \
        "v_cmp_nlt_f32 s[4:5], |v48|, v65\n\t" \
        "v_cmp_nlt_f16 s[8:9], |v48|, v62\n\t" \
        "s_or_b64 s[12:13], s[6:7], s[10:11]\n\t" \
        "v_mbcnt_lo_u32_b32 v161, s12, 0\n\t" \
        "v_mbcnt_hi_u32_b32 v161, s13, v161\n\t" \
        "s_add_u32 s57, s36, 0x69000\n\t" \
        "s_add_u32 m0, s38, 0xba00\n\t" \
        "s_nop 0\n\t" \
        "buffer_load_dwordx4 v13, s[16:19], s57 offen nt lds\n\t" \
        "s_bcnt1_i32_b64 s55, s[12:13]\n\t" \
        "v_lshl_add_u32 v161, v161, 3, s45\n\t" \
        "s_lshl3_add_u32 s45, s55, s45\n\t" \
        "v_cndmask_b32 v161, v66, v161, s[12:13]\n\t" \
        "ds_write2_b32 v161, v47, v57 offset1:1\n\t" \
        "s_or_b64 s[12:13], s[4:5], s[8:9]\n\t" \
        "v_mbcnt_lo_u32_b32 v160, s12, 0\n\t" \
        "v_mbcnt_hi_u32_b32 v160, s13, v160\n\t" \
        "s_bcnt1_i32_b64 s55, s[12:13]\n\t" \
        "v_lshl_add_u32 v160, v160, 3, s45\n\t" \
        "s_add_u32 s57, s36, 0x73800\n\t" \
        "s_add_u32 m0, s38, 0xc980\n\t" \
        "s_nop 0\n\t" \
        "buffer_load_dwordx4 v13, s[16:19], s57 offen nt lds\n\t" \
        "s_lshl3_add_u32 s45, s55, s45\n\t" \
        "v_cndmask_b32 v160, v66, v160, s[12:13]\n\t" \
        "ds_write2_b32 v160, v48, v58 offset1:1\n\t" \
        "s_sub_u32 s46, s45, s48\n\t" \
        "s_lshr_b32 s46, s46, 3\n\t" \
        "s_cmp_gt_u32 s46, 64\n\t" \
        "s_cselect_b32 s54, 1, 0\n\t" \
        "s_cselect_b32 s46, 64, s46\n\t" \
        "s_add_u32 s57, s36, 0x7e000\n\t" \
        "s_min_u32 s57, s57, 0x925800\n\t" \
        "s_add_u32 m0, s38, 0xd900\n\t" \
        "s_nop 0\n\t" \
        "buffer_load_dwordx4 v13, s[16:19], s57 offen nt lds\n\t" \
        "v_lshrrev_b32 v170, 4, v2\n\t" \
        "v_and_b32 v24, 15, v2\n\t" \
        "v_bfe_u32 v171, v2, 3, 1\n\t" \
        "v_lshl_add_u32 v171, v170, 2, v171\n\t" \
        "v_lshrrev_b32 v172, 1, v2\n\t" \
        "v_and_b32 v172, 2, v172\n\t" \
        "v_lshl_add_u32 v171, v171, 2, v172\n\t" \
        "s_add_u32 s55, s48, 512\n\t" \
        "v_add_u32 v20, s55, v171\n\t" \
        "v_lshl_add_u32 v21, v24, 3, s48\n\t" \
        "v_bfe_u32 v171, v2, 1, 1\n\t" \
        "v_xor_b32 v171, v171, v170\n\t" \
        "v_lshrrev_b32 v172, 2, v24\n\t" \
        "v_and_b32 v173, 2, v172\n\t" \
        "v_xor_b32 v171, v171, v173\n\t" \
        "v_and_b32 v173, 4, v2\n\t" \
        "v_lshlrev_b32 v171, 4, v171\n\t" \
        "v_lshl_add_u32 v171, v173, 1, v171\n\t" \
        "v_mul_i32_i24 v22, s53, v24\n\t" \
        "v_add3_u32 v22, v22, v171, s49\n\t" \
        "v_and_b32 v171, 3, v2\n\t" \
        "v_and_b32 v173, 1, v170\n\t" \
        "s_add_u32 s57, s36, 0x88800\n\t" \
        "s_min_u32 s57, s57, 0x925800\n\t" \
        "s_add_u32 m0, s38, 0xe880\n\t" \
        "s_nop 0\n\t" \
        "buffer_load_dwordx4 v13, s[16:19], s57 offen nt lds\n\t" \
        "v_lshlrev_b32 v173, 3, v173\n\t" \
        "v_lshl_or_b32 v23, v171, 4, v173\n\t" \
        "v_bfe_u32 v172, v2, 2, 2\n\t" \
        "v_and_b32 v171, 3, v2\n\t" \
        "v_cmp_eq_u32 vcc, v170, v172\n\t" \
        "v_lshlrev_b32 v171, v171, 1\n\t" \
        "s_nop 0\n\t" \
        "v_cndmask_b32 v171, 0, v171, vcc\n\t" \
        "v_bfe_i32 v25, v171, 0, 1\n\t" \
        "v_bfe_i32 v26, v171, 1, 1\n\t" \
        "v_bfe_i32 v27, v171, 2, 1\n\t" \
        "v_bfe_i32 v28, v171, 3, 1\n\t" \
        "ds_read_b64 v[68:69], v31\n\t" \
        "v_cmp_gt_u32 s[12:13], s46, v2\n\t" \
        "s_mov_b32 s14, 0x2\n\t" \
        "s_mov_b32 s15, 0x8\n\t" \
        "v_mov_b32 v83, 0x41980000\n\t" \
        "v_mov_b32 v29, 0x20002\n\t" \
        "s_waitcnt lgkmcnt(0)\n\t" \
        "v_lshlrev_b32 v70, 16, v68\n\t" \
        "v_and_b32 v71, 0xffff0000, v68\n\t" \
        "v_cvt_f32_ubyte2 v74, v69\n\t" \
        "v_cvt_f32_ubyte3 v75, v69\n\t" \
        "s_mov_b32 s57, s40\n\t" \
        "s_add_u32 m0, s39, 0x3000\n\t" \
        "s_nop 0\n\t" \
        "buffer_load_dwordx4 v3, s[24:27], s57 offen offset:0  lds\n\t" \
        "v_add_f32 v70, v70, v74\n\t" \
        "v_add_f32 v71, v71, v75\n\t" \
        "v_cvt_pk_bf16_f32 v68, v70, v71\n\t" \
        "v_pk_max_i16 v68, v68, 0\n\t" \
        "v_pk_min_i16 v68, v68, v39\n\t" \
        "v_and_b32 v69, 0xffff, v69\n\t" \
        "v_lshlrev_b32 v69, 2, v69\n\t" \
        "v_lshlrev_b32 v70, 16, v68\n\t" \
        "v_and_b32 v71, 0xffff0000, v68\n\t" \
        "v_cndmask_b32 v69, v66, v69, s[12:13]\n\t" \
        "ds_write_b32 v69, v30\n\t" \
        "v_fract_f32 v72, v70\n\t" \
        "v_fract_f32 v73, v71\n\t" \
        "v_sub_f32 v70, v70, v34\n\t" \
        "v_sub_f32 v71, v71, v35\n\t" \
        "v_sub_f32 v74, 1.0, v72\n\t" \
        "v_sub_f32 v75, 1.0, v73\n\t" \
        "v_med3_f32 v78, v70, 0, v33\n\t" \
        "v_cvt_pk_bf16_f32 v74, v74, v75\n\t" \
        "v_med3_f32 v79, v71, 0, v83\n\t" \
        "v_cmp_neq_f32 s[4:5], v78, v70\n\t" \
        "v_and_b32 v75, 0xffff0000, v74\n\t" \
        "buffer_load_dwordx4 v4, s[24:27], s57 offen offset:1024  lds\n\t" \
        "v_cmp_neq_f32 s[6:7], v79, v71\n\t" \
        "v_lshlrev_b32 v74, 16, v74\n\t" \
        "v_cvt_u32_f32 v78, v78\n\t" \
        "v_cvt_u32_f32 v79, v79\n\t" \
        "s_or_b64 s[4:5], s[4:5], s[6:7]\n\t" \
        "v_mul_f32 v76, v74, v75\n\t" \
        "v_mul_f32 v74, v74, v73\n\t" \
        "s_and_b64 s[4:5], s[4:5], s[12:13]\n\t" \
        "s_cselect_b32 s54, 1, s54\n\t" \
        "v_mul_f32 v77, v72, v75\n\t" \
        "v_mul_f32 v75, v72, v73\n\t" \
        "v_cmp_neq_f32 s[4:5], 0, v73\n\t" \
        "v_cmp_neq_f32 s[6:7], 0, v72\n\t" \
        "v_cvt_pk_bf16_f32 v76, v76, v74\n\t" \
        "v_cvt_pk_bf16_f32 v77, v77, v75\n\t" \
        "v_mul_u32_u24 v80, 124, v78\n\t" \
        "v_add_u32 v83, 1, v79\n\t" \
        "v_mad_u32_u24 v80, v79, 6, v80\n\t" \
        "v_bfe_u32 v84, v79, 2, 1\n\t" \
        "v_bfe_u32 v83, v83, 2, 1\n\t" \
        "s_and_b64 s[50:51], s[4:5], s[6:7]\n\t" \
        "v_add3_u32 v81, v80, v84, s14\n\t" \
        "buffer_load_dwordx4 v5, s[24:27], s57 offen offset:2048  lds\n\t" \
        "v_add3_u32 v82, v80, v83, s15\n\t" \
        "s_and_b64 s[50:51], s[50:51], s[12:13]\n\t" \
        "v_cndmask_b32 v83, 0, v36, s[6:7]\n\t" \
        "v_perm_b32 v74, v77, v76, v37\n\t" \
        "v_add_u32 v83, v81, v83\n\t" \
        "v_cndmask_b32 v68, v74, v76, s[4:5]\n\t" \
        "v_cndmask_b32 v83, v83, v82, s[4:5]\n\t" \
        "v_cndmask_b32 v75, 0, v38, s[50:51]\n\t" \
        "v_lshl_or_b32 v81, v83, 16, v81\n\t" \
        "v_cndmask_b32 v69, 0, v77, s[50:51]\n\t" \
        "v_or_b32 v81, v81, v75\n\t" \
        "ds_write_b64 v31, v[68:69]\n\t" \
        "v_cndmask_b32 v81, v29, v81, s[12:13]\n\t" \
        "ds_write_b32 v32, v81\n\t" \
        "s_lshl_b32 s55, s35, 2\n\t" \
        "s_cmp_lg_u32 s54, 0\n\t" \
        "v_mov_b32 v162, s55\n\t" \
        "s_cselect_b32 s55, 0x10000, s46\n\t" \
        "v_mov_b32 v163, s55\n\t" \
        "ds_write_b32 v162, v163\n\t" \
        "s_add_u32 s57, s40, 0x3000\n\t" \
        "s_add_u32 m0, s39, 0x6000\n\t" \
        "s_nop 0\n\t" \
        "buffer_load_dwordx4 v3, s[24:27], s57 offen offset:0  lds\n\t" \
        "s_and_b32 s61, s35, 1\n\t" \
        "v_lshrrev_b32 v170, 4, v2\n\t" \
        "v_and_b32 v171, 15, v2\n\t" \
        "v_and_b32 v172, 16, v2\n\t" \
        "s_lshr_b32 s62, s35, 1\n\t" \
        "v_lshlrev_b32 v172, 1, v172\n\t" \
        "v_bfe_u32 v173, v2, 2, 2\n\t" \
        "v_mul_u32_u24 v175, 0xc0, v171\n\t" \
        "v_lshl_or_b32 v173, v170, 3, v173\n\t" \
        "v_and_b32 v174, 3, v2\n\t" \
        "v_mul_u32_u24 v173, 0xc0, v173\n\t" \
        "s_lshl_b32 s63, s61, 7\n\t" \
        "v_lshl_add_u32 v173, v174, 3, v173\n\t" \
        "s_add_u32 s63, s63, 0xf840\n\t" \
        "v_add3_u32 v6, v173, v172, s63\n\t" \
        "buffer_load_dwordx4 v4, s[24:27], s57 offen offset:1024  lds\n\t" \
        "s_lshl_b32 s63, s61, 5\n\t" \
        "v_xor_b32 v174, s63, v172\n\t" \
        "v_xor_b32 v7, 32, v6\n\t" \
        "v_add_u32 v173, 0xf880, v173\n\t" \
        "s_lshl_b32 s63, s61, 7\n\t" \
        "v_add_u32 v8, v173, v174\n\t" \
        "v_lshrrev_b32 v173, 1, v2\n\t" \
        "v_and_b32 v173, 16, v173\n\t" \
        "v_add_u32 v175, v175, v173\n\t" \
        "v_bfe_u32 v173, v2, 3, 1\n\t" \
        "v_add3_u32 v9, v175, v172, s63\n\t" \
        "v_cmp_eq_u32 vcc, s61, v173\n\t" \
        "v_add3_u32 v10, v175, v174, 64\n\t" \
        "v_bfrev_b32 v174, 1\n\t" \
        "s_lshl_b32 s63, s35, 9\n\t" \
        "buffer_load_dwordx4 v5, s[24:27], s57 offen offset:2048  lds\n\t" \
        "v_cndmask_b32 v10, v174, v10, vcc\n\t" \
        "s_add_u32 s63, s63, 0xf840\n\t" \
        "v_lshl_add_u32 v11, v2, 3, s63\n\t" \
        "s_lshl_b32 s63, s61, 10\n\t" \
        "s_sub_u32 s63, 0x200, s63\n\t" \
        "v_add_u32 v12, s63, v11\n\t" \
        "s_add_u32 s64, s32, s62\n\t" \
        "s_mul_i32 s63, s33, 0xc0\n\t" \
        "s_mul_i32 s64, s64, 0xa800\n\t" \
        "s_add_u32 s64, s64, s63\n\t" \
        "v_and_b32 v170, 15, v2\n\t" \
        "s_mul_i32 s62, s62, 2304\n\t" \
        "s_add_u32 s62, s62, 124992\n\t" \
        "v_lshl_add_u32 v15, v170, 4, s62\n\t" \
        "ds_read_u16 v120, v20\n\t" \
        "ds_read_u16 v121, v20 offset:8\n\t" \
        "ds_read_b32 v122, v21\n\t" \
        "v_mov_b32 v156, 0\n\t" \
        "s_mov_b32 s47, 0\n\t" \
        "s_waitcnt lgkmcnt(0)\n\t" \
        "v_lshlrev_b32 v116, 5, v120\n\t" \
        "v_bitop3_b32 v116, v116, s52, v23 bitop3:0x6a\n\t" \
        "v_xor_b32 v117, 8, v116\n\t" \
        "v_lshlrev_b32 v118, 5, v121\n\t" \
        "v_bitop3_b32 v118, v118, s52, v23 bitop3:0x6a\n\t" \
        "v_xor_b32 v119, 8, v118\n\t" \
        "v_and_b32 v112, v122, v25\n\t" \
        "v_and_b32 v113, v122, v26\n\t" \
        "v_and_b32 v114, v122, v27\n\t" \
        "v_and_b32 v115, v122, v28\n\t" \
        "ds_read_u16 v120, v20 offset:64\n\t" \
        "ds_read_u16 v121, v20 offset:72\n\t" \
        "ds_read_b32 v122, v21 offset:128\n\t" \
        "s_waitcnt vmcnt(6) lgkmcnt(0)\n\t" \
        "s_barrier\n\t" \
        "ds_read_b128 v[152:155], v156\n\t" \
        "ds_read_b64_tr_b16 v[64:65], v116 offset:0\n\t" \
        "ds_read_b64_tr_b16 v[66:67], v118 offset:0\n\t" \
        "ds_read_b64_tr_b16 v[68:69], v117 offset:0\n\t" \
        "ds_read_b64_tr_b16 v[70:71], v119 offset:0\n\t" \
        "ds_read_b64_tr_b16 v[72:73], v116 offset:64\n\t" \
        "ds_read_b64_tr_b16 v[74:75], v118 offset:64\n\t" \
        "ds_read_b64_tr_b16 v[76:77], v117 offset:64\n\t" \
        "ds_read_b64_tr_b16 v[78:79], v119 offset:64\n\t" \
        "ds_read_b64_tr_b16 v[80:81], v116 offset:128\n\t" \
        "ds_read_b64_tr_b16 v[82:83], v118 offset:128\n\t" \
        "ds_read_b64_tr_b16 v[84:85], v117 offset:128\n\t" \
        "ds_read_b64_tr_b16 v[86:87], v119 offset:128\n\t" \
        "s_add_u32 s57, s40, 0x6000\n\t" \
        "s_add_u32 m0, s39, 0x9000\n\t" \
        "s_nop 0\n\t" \
        "buffer_load_dwordx4 v3, s[24:27], s57 offen offset:0  lds\n\t" \
        "buffer_load_dwordx4 v4, s[24:27], s57 offen offset:1024  lds\n\t" \
        "buffer_load_dwordx4 v5, s[24:27], s57 offen offset:2048  lds\n\t" \
        "s_waitcnt lgkmcnt(12)\n\t" \
        "v_add_u32 v152, v152, v154\n\t" \
        "v_add_u32 v153, v153, v155\n\t" \
        "v_max_u32 v152, v152, v153\n\t" \
        "s_nop 0\n\t" \
        "v_readfirstlane_b32 s55, v152\n\t" \
        "s_nop 0\n\t" \
        "s_cmp_gt_u32 s55, 66\n\t" \
        "s_cbranch_scc1 .Lfail%=\n\t" \
        "s_cmp_gt_u32 s46, 16\n\t" \
        "s_cbranch_scc1 .LTeprod%=\n\t" \
        ".LTelast%=:\n\t" \
        "s_waitcnt lgkmcnt(10)\n\t" \
        "v_mfma_f32_16x16x32_bf16 v[88:91], v[64:67], v[112:115], 0\n\t" \
        "s_waitcnt lgkmcnt(8)\n\t" \
        "v_mfma_f32_16x16x32_bf16 v[92:95], v[68:71], v[112:115], 0\n\t" \
        "s_waitcnt lgkmcnt(6)\n\t" \
        "v_mfma_f32_16x16x32_bf16 v[96:99], v[72:75], v[112:115], 0\n\t" \
        "s_waitcnt lgkmcnt(4)\n\t" \
        "v_mfma_f32_16x16x32_bf16 v[100:103], v[76:79], v[112:115], 0\n\t" \
        "s_waitcnt lgkmcnt(2)\n\t" \
        "v_mfma_f32_16x16x32_bf16 v[104:107], v[80:83], v[112:115], 0\n\t" \
        "s_waitcnt lgkmcnt(0)\n\t" \
        "v_mfma_f32_16x16x32_bf16 v[108:111], v[84:87], v[112:115], 0\n\t" \
        "s_and_b32 s55, s50, 0xffff\n\t" \
        "s_cbranch_scc1 .LTetwo2%=\n\t" \
        ".LTeback2%=:\n\t" \
        "s_sub_u32 s55, s46, s47\n\t" \
        "v_xor_b32 v156, 8, v22\n\t" \
        "v_cmp_gt_i32 vcc, s55, v24\n\t" \
        "s_mov_b64 exec, vcc\n\t" \
        "v_cvt_pk_bf16_f32 v88, v88, v89\n\t" \
        "v_cvt_pk_bf16_f32 v89, v90, v91\n\t" \
        "ds_write_b64 v22, v[88:89] offset:0\n\t" \
        "v_cvt_pk_bf16_f32 v92, v92, v93\n\t" \
        "v_cvt_pk_bf16_f32 v93, v94, v95\n\t" \
        "ds_write_b64 v156, v[92:93] offset:0\n\t" \
        "v_cvt_pk_bf16_f32 v96, v96, v97\n\t" \
        "v_cvt_pk_bf16_f32 v97, v98, v99\n\t" \
        "ds_write_b64 v22, v[96:97] offset:64\n\t" \
        "v_cvt_pk_bf16_f32 v100, v100, v101\n\t" \
        "v_cvt_pk_bf16_f32 v101, v102, v103\n\t" \
        "ds_write_b64 v156, v[100:101] offset:64\n\t" \
        "v_cvt_pk_bf16_f32 v104, v104, v105\n\t" \
        "v_cvt_pk_bf16_f32 v105, v106, v107\n\t" \
        "ds_write_b64 v22, v[104:105] offset:128\n\t" \
        "v_cvt_pk_bf16_f32 v108, v108, v109\n\t" \
        "v_cvt_pk_bf16_f32 v109, v110, v111\n\t" \
        "ds_write_b64 v156, v[108:109] offset:128\n\t" \
        "s_mov_b64 exec, -1\n\t" \
        "s_waitcnt vmcnt(6) lgkmcnt(0)\n\t" \
        "s_barrier\n\t" \
        "ds_read_b128 v[148:151], v15 offset:0\n\t" \
        "s_waitcnt lgkmcnt(0)\n\t" \
        "v_xor_b32 v148, v148, v14\n\t" \
        "ds_read_b128 v[116:119], v148 offset:0\n\t" \
        "ds_read_b64_tr_b16 a[0:1], v6 offset:12288\n\t" \
        "ds_read_b64_tr_b16 a[2:3], v6 offset:13056\n\t" \
        "v_xor_b32 v149, v149, v14\n\t" \
        "ds_read_b64_tr_b16 a[4:5], v7 offset:12288\n\t" \
        "ds_read_b64_tr_b16 a[6:7], v7 offset:13056\n\t" \
        "ds_read_b64_tr_b16 a[8:9], v8 offset:12288\n\t" \
        "ds_read_b64_tr_b16 a[10:11], v8 offset:13056\n\t" \
        "ds_read_b128 v[120:123], v149 offset:0\n\t" \
        "v_xor_b32 v150, v150, v14\n\t" \
        "ds_read_b128 v[124:127], v150 offset:0\n\t" \
        "v_xor_b32 v151, v151, v14\n\t" \
        "ds_read_b128 v[128:131], v151 offset:0\n\t" \
        "s_add_u32 s57, s40, 0x9000\n\t" \
        "s_add_u32 m0, s39, 0xc000\n\t" \
        "s_waitcnt lgkmcnt(7)\n\t" \
        "v_mfma_f32_16x16x32_bf16 v[32:35], a[0:3], v[116:119], 0\n\t" \
        "buffer_load_dwordx4 v3, s[24:27], s57 offen offset:0  lds\n\t" \
        "s_waitcnt lgkmcnt(5)\n\t" \
        "v_mfma_f32_16x16x32_bf16 v[36:39], a[4:7], v[116:119], 0\n\t" \
        "ds_read_b128 v[132:135], v148 offset:64\n\t" \
        "s_waitcnt lgkmcnt(4)\n\t" \
        "v_mfma_f32_16x16x32_bf16 v[40:43], a[8:11], v[116:119], 0\n\t" \
        "ds_read_b64_tr_b16 a[12:13], v6 offset:18432\n\t" \
        "s_waitcnt lgkmcnt(4)\n\t" \
        "v_mfma_f32_16x16x32_bf16 v[44:47], a[0:3], v[120:123], 0\n\t" \
        "ds_read_b64_tr_b16 a[14:15], v6 offset:19200\n\t" \
        "v_mfma_f32_16x16x32_bf16 v[48:51], a[4:7], v[120:123], 0\n\t" \
        "buffer_load_dwordx4 v4, s[24:27], s57 offen offset:1024  lds\n\t" \
        "v_mfma_f32_16x16x32_bf16 v[52:55], a[8:11], v[120:123], 0\n\t" \
        "ds_read_b64_tr_b16 a[16:17], v7 offset:18432\n\t" \
        "s_waitcnt lgkmcnt(5)\n\t" \
        "v_mfma_f32_16x16x32_bf16 v[56:59], a[0:3], v[124:127], 0\n\t" \
        "ds_read_b64_tr_b16 a[18:19], v7 offset:19200\n\t" \
        "ds_read_b64_tr_b16 a[20:21], v8 offset:18432\n\t" \
        "v_mfma_f32_16x16x32_bf16 v[60:63], a[4:7], v[124:127], 0\n\t" \
        "ds_read_b64_tr_b16 a[22:23], v8 offset:19200\n\t" \
        "v_mfma_f32_16x16x32_bf16 v[64:67], a[8:11], v[124:127], 0\n\t" \
        "ds_read_b128 v[136:139], v149 offset:64\n\t" \
        "s_waitcnt lgkmcnt(7)\n\t" \
        "v_mfma_f32_16x16x32_bf16 v[68:71], a[0:3], v[128:131], 0\n\t" \
        "buffer_load_dwordx4 v5, s[24:27], s57 offen offset:2048  lds\n\t" \
        "v_mfma_f32_16x16x32_bf16 v[72:75], a[4:7], v[128:131], 0\n\t" \
        "ds_read_b128 v[140:143], v150 offset:64\n\t" \
        "s_add_u32 s57, s40, 0xc000\n\t" \
        "s_mov_b32 m0, s39\n\t" \
        "v_mfma_f32_16x16x32_bf16 v[76:79], a[8:11], v[128:131], 0\n\t" \
        "s_waitcnt vmcnt(6) lgkmcnt(2)\n\t" \
        "s_barrier\n\t" \
        "ds_read_b128 v[144:147], v151 offset:64\n\t" \
        "v_mfma_f32_16x16x32_bf16 v[32:35], a[12:15], v[132:135], v[32:35]\n\t" \
        "buffer_load_dwordx4 v3, s[24:27], s57 offen offset:0  lds\n\t" \
        "v_mfma_f32_16x16x32_bf16 v[36:39], a[16:19], v[132:135], v[36:39]\n\t" \
        "ds_read_b128 v[116:119], v148 offset:128\n\t" \
        "v_mfma_f32_16x16x32_bf16 v[40:43], a[20:23], v[132:135], v[40:43]\n\t" \
        "ds_read_b128 v[152:155], v15 offset:256\n\t" \
        "s_waitcnt lgkmcnt(4)\n\t" \
        "v_mfma_f32_16x16x32_bf16 v[44:47], a[12:15], v[136:139], v[44:47]\n\t" \
        "ds_read_b64_tr_b16 a[24:25], v6 offset:24576\n\t" \
        "v_mfma_f32_16x16x32_bf16 v[48:51], a[16:19], v[136:139], v[48:51]\n\t" \
        "buffer_load_dwordx4 v4, s[24:27], s57 offen offset:1024  lds\n\t" \
        "v_mfma_f32_16x16x32_bf16 v[52:55], a[20:23], v[136:139], v[52:55]\n\t" \
        "ds_read_b64_tr_b16 a[26:27], v6 offset:25344\n\t" \
        "ds_read_b64_tr_b16 a[28:29], v7 offset:24576\n\t" \
        "s_waitcnt lgkmcnt(6)\n\t" \
        "v_mfma_f32_16x16x32_bf16 v[56:59], a[12:15], v[140:143], v[56:59]\n\t" \
        "ds_read_b64_tr_b16 a[30:31], v7 offset:25344\n\t" \
        "ds_read_b64_tr_b16 a[32:33], v8 offset:24576\n\t" \
        "v_mfma_f32_16x16x32_bf16 v[60:63], a[16:19], v[140:143], v[60:63]\n\t" \
        "ds_read_b64_tr_b16 a[34:35], v8 offset:25344\n\t" \
        "v_mfma_f32_16x16x32_bf16 v[64:67], a[20:23], v[140:143], v[64:67]\n\t" \
        "ds_read_b128 v[120:123], v149 offset:128\n\t" \
        "s_waitcnt lgkmcnt(7)\n\t" \
        "v_xor_b32 v152, v152, v14\n\t" \
        "v_mfma_f32_16x16x32_bf16 v[68:71], a[12:15], v[144:147], v[68:71]\n\t" \
        "buffer_load_dwordx4 v5, s[24:27], s57 offen offset:2048  lds\n\t" \
        "v_mfma_f32_16x16x32_bf16 v[72:75], a[16:19], v[144:147], v[72:75]\n\t" \
        "ds_read_b128 v[124:127], v150 offset:128\n\t" \
        "s_add_u32 s57, s40, 0xf000\n\t" \
        "s_add_u32 m0, s39, 0x3000\n\t" \
        "v_xor_b32 v153, v153, v14\n\t" \
        "v_mfma_f32_16x16x32_bf16 v[76:79], a[20:23], v[144:147], v[76:79]\n\t" \
        "ds_read_b128 v[128:131], v151 offset:128\n\t" \
        "v_xor_b32 v154, v154, v14\n\t" \
        "s_waitcnt lgkmcnt(7)\n\t" \
        "v_mfma_f32_16x16x32_bf16 v[32:35], a[24:27], v[116:119], v[32:35]\n\t" \
        "buffer_load_dwordx4 v3, s[24:27], s57 offen offset:0  lds\n\t" \
        "s_waitcnt lgkmcnt(5)\n\t" \
        "v_mfma_f32_16x16x32_bf16 v[36:39], a[28:31], v[116:119], v[36:39]\n\t" \
        "ds_read_b128 v[132:135], v152 offset:0\n\t" \
        "v_xor_b32 v155, v155, v14\n\t" \
        "s_waitcnt lgkmcnt(4)\n\t" \
        "v_mfma_f32_16x16x32_bf16 v[40:43], a[32:35], v[116:119], v[40:43]\n\t" \
        "ds_read_b64_tr_b16 a[36:37], v6 offset:30720\n\t" \
        "s_waitcnt lgkmcnt(4)\n\t" \
        "v_mfma_f32_16x16x32_bf16 v[44:47], a[24:27], v[120:123], v[44:47]\n\t" \
        "ds_read_b64_tr_b16 a[38:39], v6 offset:31488\n\t" \
        "v_mfma_f32_16x16x32_bf16 v[48:51], a[28:31], v[120:123], v[48:51]\n\t" \
        "buffer_load_dwordx4 v4, s[24:27], s57 offen offset:1024  lds\n\t" \
        "v_mfma_f32_16x16x32_bf16 v[52:55], a[32:35], v[120:123], v[52:55]\n\t" \
        "ds_read_b64_tr_b16 a[40:41], v7 offset:30720\n\t" \
        "s_waitcnt lgkmcnt(5)\n\t" \
        "v_mfma_f32_16x16x32_bf16 v[56:59], a[24:27], v[124:127], v[56:59]\n\t" \
        "ds_read_b64_tr_b16 a[42:43], v7 offset:31488\n\t" \
        "ds_read_b64_tr_b16 a[44:45], v8 offset:30720\n\t" \
        "v_mfma_f32_16x16x32_bf16 v[60:63], a[28:31], v[124:127], v[60:63]\n\t" \
        "ds_read_b64_tr_b16 a[46:47], v8 offset:31488\n\t" \
        "v_mfma_f32_16x16x32_bf16 v[64:67], a[32:35], v[124:127], v[64:67]\n\t" \
        "ds_read_b128 v[136:139], v153 offset:0\n\t" \
        "s_waitcnt lgkmcnt(7)\n\t" \
        "v_mfma_f32_16x16x32_bf16 v[68:71], a[24:27], v[128:131], v[68:71]\n\t" \
        "buffer_load_dwordx4 v5, s[24:27], s57 offen offset:2048  lds\n\t" \
        "v_mfma_f32_16x16x32_bf16 v[72:75], a[28:31], v[128:131], v[72:75]\n\t" \
        "ds_read_b128 v[140:143], v154 offset:0\n\t" \
        "v_mfma_f32_16x16x32_bf16 v[76:79], a[32:35], v[128:131], v[76:79]\n\t" \
        "s_waitcnt vmcnt(9) lgkmcnt(2)\n\t" \
        "s_barrier\n\t" \
        "ds_read_b128 v[144:147], v155 offset:0\n\t" \
        "s_add_u32 s57, s40, 0x12000\n\t" \
        "s_add_u32 m0, s39, 0x6000\n\t" \
        "v_mfma_f32_16x16x32_bf16 v[32:35], a[36:39], v[132:135], v[32:35]\n\t" \
        "ds_read_b128 v[116:119], v152 offset:64\n\t" \
        "v_mfma_f32_16x16x32_bf16 v[36:39], a[40:43], v[132:135], v[36:39]\n\t" \
        "buffer_load_dwordx4 v3, s[24:27], s57 offen offset:0  lds\n\t" \
        "v_mfma_f32_16x16x32_bf16 v[40:43], a[44:47], v[132:135], v[40:43]\n\t" \
        "ds_read_b64_tr_b16 a[48:49], v6 offset:36864\n\t" \
        "s_waitcnt lgkmcnt(4)\n\t" \
        "v_mfma_f32_16x16x32_bf16 v[44:47], a[36:39], v[136:139], v[44:47]\n\t" \
        "ds_read_b64_tr_b16 a[50:51], v6 offset:37632\n\t" \
        "v_mfma_f32_16x16x32_bf16 v[48:51], a[40:43], v[136:139], v[48:51]\n\t" \
        "ds_read_b64_tr_b16 a[52:53], v7 offset:36864\n\t" \
        "v_mfma_f32_16x16x32_bf16 v[52:55], a[44:47], v[136:139], v[52:55]\n\t" \
        "buffer_load_dwordx4 v4, s[24:27], s57 offen offset:1024  lds\n\t" \
        "s_waitcnt lgkmcnt(5)\n\t" \
        "v_mfma_f32_16x16x32_bf16 v[56:59], a[36:39], v[140:143], v[56:59]\n\t" \
        "ds_read_b64_tr_b16 a[54:55], v7 offset:37632\n\t" \
        "ds_read_b64_tr_b16 a[56:57], v8 offset:36864\n\t" \
        "v_mfma_f32_16x16x32_bf16 v[60:63], a[40:43], v[140:143], v[60:63]\n\t" \
        "ds_read_b64_tr_b16 a[58:59], v8 offset:37632\n\t" \
        "v_mfma_f32_16x16x32_bf16 v[64:67], a[44:47], v[140:143], v[64:67]\n\t" \
        "ds_read_b128 v[120:123], v153 offset:64\n\t" \
        "s_waitcnt lgkmcnt(7)\n\t" \
        "v_mfma_f32_16x16x32_bf16 v[68:71], a[36:39], v[144:147], v[68:71]\n\t" \
        "buffer_load_dwordx4 v5, s[24:27], s57 offen offset:2048  lds\n\t" \
        "v_mfma_f32_16x16x32_bf16 v[72:75], a[40:43], v[144:147], v[72:75]\n\t" \
        "ds_read_b128 v[124:127], v154 offset:64\n\t" \
        "v_mfma_f32_16x16x32_bf16 v[76:79], a[44:47], v[144:147], v[76:79]\n\t" \
        "ds_read_b128 v[128:131], v155 offset:64\n\t" \
        "s_waitcnt lgkmcnt(6)\n\t" \
        "v_mfma_f32_16x16x32_bf16 v[32:35], a[48:51], v[116:119], v[32:35]\n\t" \
        "ds_read_b128 v[132:135], v152 offset:128\n\t" \
        "s_waitcnt lgkmcnt(6)\n\t" \
        "v_mfma_f32_16x16x32_bf16 v[36:39], a[52:55], v[116:119], v[36:39]\n\t" \
        "ds_read_b128 v[156:159], v15 offset:512\n\t" \
        "s_waitcnt lgkmcnt(5)\n\t" \
        "v_mfma_f32_16x16x32_bf16 v[40:43], a[56:59], v[116:119], v[40:43]\n\t" \
        "ds_read_b64_tr_b16 a[60:61], v6 offset:43008\n\t" \
        "s_waitcnt lgkmcnt(5)\n\t" \
        "v_mfma_f32_16x16x32_bf16 v[44:47], a[48:51], v[120:123], v[44:47]\n\t" \
        "ds_read_b64_tr_b16 a[62:63], v6 offset:43776\n\t" \
        "v_mfma_f32_16x16x32_bf16 v[48:51], a[52:55], v[120:123], v[48:51]\n\t" \
        "ds_read_b64_tr_b16 a[64:65], v7 offset:43008\n\t" \
        "v_mfma_f32_16x16x32_bf16 v[52:55], a[56:59], v[120:123], v[52:55]\n\t" \
        "ds_read_b64_tr_b16 a[66:67], v7 offset:43776\n\t" \
        "s_waitcnt lgkmcnt(7)\n\t" \
        "v_mfma_f32_16x16x32_bf16 v[56:59], a[48:51], v[124:127], v[56:59]\n\t" \
        "ds_read_b64_tr_b16 a[68:69], v8 offset:43008\n\t" \
        "v_mfma_f32_16x16x32_bf16 v[60:63], a[52:55], v[124:127], v[60:63]\n\t" \
        "ds_read_b64_tr_b16 a[70:71], v8 offset:43776\n\t" \
        "s_waitcnt lgkmcnt(6)\n\t" \
        "v_xor_b32 v156, v156, v14\n\t" \
        "v_mfma_f32_16x16x32_bf16 v[64:67], a[56:59], v[124:127], v[64:67]\n\t" \
        "ds_read_b128 v[136:139], v153 offset:128\n\t" \
        "v_xor_b32 v157, v157, v14\n\t" \
        "v_mfma_f32_16x16x32_bf16 v[68:71], a[48:51], v[128:131], v[68:71]\n\t" \
        "ds_read_b128 v[140:143], v154 offset:128\n\t" \
        "v_xor_b32 v158, v158, v14\n\t" \
        "v_mfma_f32_16x16x32_bf16 v[72:75], a[52:55], v[128:131], v[72:75]\n\t" \
        "ds_read_b128 v[144:147], v155 offset:128\n\t" \
        "v_xor_b32 v159, v159, v14\n\t" \
        "v_mfma_f32_16x16x32_bf16 v[76:79], a[56:59], v[128:131], v[76:79]\n\t" \
        "s_waitcnt vmcnt(9) lgkmcnt(3)\n\t" \
        "s_barrier\n\t" \
        "ds_read_b128 v[116:119], v156 offset:0\n\t" \
        "s_add_u32 s57, s40, 0x15000\n\t" \
        "s_add_u32 m0, s39, 0x9000\n\t" \
        "v_mfma_f32_16x16x32_bf16 v[32:35], a[60:63], v[132:135], v[32:35]\n\t" \
        "ds_read_b64_tr_b16 a[72:73], v6 offset:49152\n\t" \
        "v_mfma_f32_16x16x32_bf16 v[36:39], a[64:67], v[132:135], v[36:39]\n\t" \
        "buffer_load_dwordx4 v3, s[24:27], s57 offen offset:0  lds\n\t" \
        "v_mfma_f32_16x16x32_bf16 v[40:43], a[68:71], v[132:135], v[40:43]\n\t" \
        "ds_read_b64_tr_b16 a[74:75], v6 offset:49920\n\t" \
        "s_waitcnt lgkmcnt(5)\n\t" \
        "v_mfma_f32_16x16x32_bf16 v[44:47], a[60:63], v[136:139], v[44:47]\n\t" \
        "ds_read_b64_tr_b16 a[76:77], v7 offset:49152\n\t" \
        "v_mfma_f32_16x16x32_bf16 v[48:51], a[64:67], v[136:139], v[48:51]\n\t" \
        "ds_read_b64_tr_b16 a[78:79], v7 offset:49920\n\t" \
        "v_mfma_f32_16x16x32_bf16 v[52:55], a[68:71], v[136:139], v[52:55]\n\t" \
        "ds_read_b64_tr_b16 a[80:81], v8 offset:49152\n\t" \
        "s_waitcnt lgkmcnt(6)\n\t" \
        "v_mfma_f32_16x16x32_bf16 v[56:59], a[60:63], v[140:143], v[56:59]\n\t" \
        "ds_read_b64_tr_b16 a[82:83], v8 offset:49920\n\t" \
        "v_mfma_f32_16x16x32_bf16 v[60:63], a[64:67], v[140:143], v[60:63]\n\t" \
        "buffer_load_dwordx4 v4, s[24:27], s57 offen offset:1024  lds\n\t" \
        "v_mfma_f32_16x16x32_bf16 v[64:67], a[68:71], v[140:143], v[64:67]\n\t" \
        "ds_read_b128 v[120:123], v157 offset:0\n\t" \
        "v_mfma_f32_16x16x32_bf16 v[68:71], a[60:63], v[144:147], v[68:71]\n\t" \
        "ds_read_b128 v[124:127], v158 offset:0\n\t" \
        "v_mfma_f32_16x16x32_bf16 v[72:75], a[64:67], v[144:147], v[72:75]\n\t" \
        "ds_read_b128 v[128:131], v159 offset:0\n\t" \
        "v_mfma_f32_16x16x32_bf16 v[76:79], a[68:71], v[144:147], v[76:79]\n\t" \
        "ds_read_b128 v[132:135], v156 offset:64\n\t" \
        "s_waitcnt lgkmcnt(6)\n\t" \
        "v_mfma_f32_16x16x32_bf16 v[32:35], a[72:75], v[116:119], v[32:35]\n\t" \
        "ds_read_b64_tr_b16 a[84:85], v6 offset:55296\n\t" \
        "v_mfma_f32_16x16x32_bf16 v[36:39], a[76:79], v[116:119], v[36:39]\n\t" \
        "buffer_load_dwordx4 v5, s[24:27], s57 offen offset:2048  lds\n\t" \
        "s_waitcnt lgkmcnt(5)\n\t" \
        "v_mfma_f32_16x16x32_bf16 v[40:43], a[80:83], v[116:119], v[40:43]\n\t" \
        "ds_read_b64_tr_b16 a[86:87], v6 offset:56064\n\t" \
        "s_waitcnt lgkmcnt(5)\n\t" \
        "v_mfma_f32_16x16x32_bf16 v[44:47], a[72:75], v[120:123], v[44:47]\n\t" \
        "ds_read_b64_tr_b16 a[88:89], v7 offset:55296\n\t" \
        "v_mfma_f32_16x16x32_bf16 v[48:51], a[76:79], v[120:123], v[48:51]\n\t" \
        "ds_read_b64_tr_b16 a[90:91], v7 offset:56064\n\t" \
        "v_mfma_f32_16x16x32_bf16 v[52:55], a[80:83], v[120:123], v[52:55]\n\t" \
        "ds_read_b64_tr_b16 a[92:93], v8 offset:55296\n\t" \
        "s_waitcnt lgkmcnt(6)\n\t" \
        "v_mfma_f32_16x16x32_bf16 v[56:59], a[72:75], v[124:127], v[56:59]\n\t" \
        "ds_read_b64_tr_b16 a[94:95], v8 offset:56064\n\t" \
        "v_mfma_f32_16x16x32_bf16 v[60:63], a[76:79], v[124:127], v[60:63]\n\t" \
        "ds_read_b128 v[136:139], v157 offset:64\n\t" \
        "v_mfma_f32_16x16x32_bf16 v[64:67], a[80:83], v[124:127], v[64:67]\n\t" \
        "ds_read_b128 v[140:143], v158 offset:64\n\t" \
        "v_mfma_f32_16x16x32_bf16 v[68:71], a[72:75], v[128:131], v[68:71]\n\t" \
        "ds_read_b128 v[144:147], v159 offset:64\n\t" \
        "v_mfma_f32_16x16x32_bf16 v[72:75], a[76:79], v[128:131], v[72:75]\n\t" \
        "ds_read_b128 v[116:119], v156 offset:128\n\t" \
        "v_mfma_f32_16x16x32_bf16 v[76:79], a[80:83], v[128:131], v[76:79]\n\t" \
        "s_waitcnt vmcnt(9) lgkmcnt(4)\n\t" \
        "s_barrier\n\t" \
        "ds_read_b128 v[160:163], v15 offset:768\n\t" \
        "s_add_u32 s57, s40, 0x18000\n\t" \
        "s_add_u32 m0, s39, 0xc000\n\t" \
        "v_mfma_f32_16x16x32_bf16 v[32:35], a[84:87], v[132:135], v[32:35]\n\t" \
        "ds_read_b64_tr_b16 a[96:97], v6 offset:0\n\t" \
        "v_mfma_f32_16x16x32_bf16 v[36:39], a[88:91], v[132:135], v[36:39]\n\t" \
        "buffer_load_dwordx4 v3, s[24:27], s57 offen offset:0  lds\n\t" \
        "v_mfma_f32_16x16x32_bf16 v[40:43], a[92:95], v[132:135], v[40:43]\n\t" \
        "ds_read_b64_tr_b16 a[98:99], v6 offset:768\n\t" \
        "s_waitcnt lgkmcnt(6)\n\t" \
        "v_mfma_f32_16x16x32_bf16 v[44:47], a[84:87], v[136:139], v[44:47]\n\t" \
        "ds_read_b64_tr_b16 a[100:101], v7 offset:0\n\t" \
        "v_mfma_f32_16x16x32_bf16 v[48:51], a[88:91], v[136:139], v[48:51]\n\t" \
        "ds_read_b64_tr_b16 a[102:103], v7 offset:768\n\t" \
        "v_mfma_f32_16x16x32_bf16 v[52:55], a[92:95], v[136:139], v[52:55]\n\t" \
        "ds_read_b64_tr_b16 a[104:105], v8 offset:0\n\t" \
        "s_waitcnt lgkmcnt(5)\n\t" \
        "v_xor_b32 v160, v160, v14\n\t" \
        "v_mfma_f32_16x16x32_bf16 v[56:59], a[84:87], v[140:143], v[56:59]\n\t" \
        "ds_read_b64_tr_b16 a[106:107], v8 offset:768\n\t" \
        "v_xor_b32 v161, v161, v14\n\t" \
        "v_mfma_f32_16x16x32_bf16 v[60:63], a[88:91], v[140:143], v[60:63]\n\t" \
        "buffer_load_dwordx4 v4, s[24:27], s57 offen offset:1024  lds\n\t" \
        "v_mfma_f32_16x16x32_bf16 v[64:67], a[92:95], v[140:143], v[64:67]\n\t" \
        "ds_read_b128 v[120:123], v157 offset:128\n\t" \
        "v_xor_b32 v162, v162, v14\n\t" \
        "v_mfma_f32_16x16x32_bf16 v[68:71], a[84:87], v[144:147], v[68:71]\n\t" \
        "ds_read_b128 v[124:127], v158 offset:128\n\t" \
        "v_xor_b32 v163, v163, v14\n\t" \
        "v_mfma_f32_16x16x32_bf16 v[72:75], a[88:91], v[144:147], v[72:75]\n\t" \
        "ds_read_b128 v[128:131], v159 offset:128\n\t" \
        "v_mfma_f32_16x16x32_bf16 v[76:79], a[92:95], v[144:147], v[76:79]\n\t" \
        "ds_read_b128 v[132:135], v160 offset:0\n\t" \
        "s_waitcnt lgkmcnt(6)\n\t" \
        "v_mfma_f32_16x16x32_bf16 v[32:35], a[96:99], v[116:119], v[32:35]\n\t" \
        "ds_read_b64_tr_b16 a[108:109], v6 offset:6144\n\t" \
        "v_mfma_f32_16x16x32_bf16 v[36:39], a[100:103], v[116:119], v[36:39]\n\t" \
        "buffer_load_dwordx4 v5, s[24:27], s57 offen offset:2048  lds\n\t" \
        "s_waitcnt lgkmcnt(5)\n\t" \
        "v_mfma_f32_16x16x32_bf16 v[40:43], a[104:107], v[116:119], v[40:43]\n\t" \
        "ds_read_b64_tr_b16 a[110:111], v6 offset:6912\n\t" \
        "s_waitcnt lgkmcnt(5)\n\t" \
        "v_mfma_f32_16x16x32_bf16 v[44:47], a[96:99], v[120:123], v[44:47]\n\t" \
        "ds_read_b64_tr_b16 a[112:113], v7 offset:6144\n\t" \
        "v_mfma_f32_16x16x32_bf16 v[48:51], a[100:103], v[120:123], v[48:51]\n\t" \
        "ds_read_b64_tr_b16 a[114:115], v7 offset:6912\n\t" \
        "v_mfma_f32_16x16x32_bf16 v[52:55], a[104:107], v[120:123], v[52:55]\n\t" \
        "ds_read_b64_tr_b16 a[116:117], v8 offset:6144\n\t" \
        "s_waitcnt lgkmcnt(6)\n\t" \
        "v_mfma_f32_16x16x32_bf16 v[56:59], a[96:99], v[124:127], v[56:59]\n\t" \
        "ds_read_b64_tr_b16 a[118:119], v8 offset:6912\n\t" \
        "v_mfma_f32_16x16x32_bf16 v[60:63], a[100:103], v[124:127], v[60:63]\n\t" \
        "ds_read_b128 v[136:139], v161 offset:0\n\t" \
        "v_mfma_f32_16x16x32_bf16 v[64:67], a[104:107], v[124:127], v[64:67]\n\t" \
        "ds_read_b128 v[140:143], v162 offset:0\n\t" \
        "v_mfma_f32_16x16x32_bf16 v[68:71], a[96:99], v[128:131], v[68:71]\n\t" \
        "ds_read_b128 v[144:147], v163 offset:0\n\t" \
        "v_mfma_f32_16x16x32_bf16 v[72:75], a[100:103], v[128:131], v[72:75]\n\t" \
        "ds_read_b128 v[116:119], v160 offset:64\n\t" \
        "v_mfma_f32_16x16x32_bf16 v[76:79], a[104:107], v[128:131], v[76:79]\n\t" \
        "s_waitcnt vmcnt(9) lgkmcnt(4)\n\t" \
        "s_barrier\n\t" \
        "ds_read_b64_tr_b16 a[120:121], v6 offset:12288\n\t" \
        "s_add_u32 s57, s40, 0x1b000\n\t" \
        "s_mov_b32 m0, s39\n\t" \
        "v_mfma_f32_16x16x32_bf16 v[32:35], a[108:111], v[132:135], v[32:35]\n\t" \
        "ds_read_b64_tr_b16 a[122:123], v6 offset:13056\n\t" \
        "v_mfma_f32_16x16x32_bf16 v[36:39], a[112:115], v[132:135], v[36:39]\n\t" \
        "buffer_load_dwordx4 v3, s[24:27], s57 offen offset:0  lds\n\t" \
        "v_mfma_f32_16x16x32_bf16 v[40:43], a[116:119], v[132:135], v[40:43]\n\t" \
        "ds_read_b64_tr_b16 a[124:125], v7 offset:12288\n\t" \
        "s_waitcnt lgkmcnt(6)\n\t" \
        "v_mfma_f32_16x16x32_bf16 v[44:47], a[108:111], v[136:139], v[44:47]\n\t" \
        "ds_read_b64_tr_b16 a[126:127], v7 offset:13056\n\t" \
        "v_mfma_f32_16x16x32_bf16 v[48:51], a[112:115], v[136:139], v[48:51]\n\t" \
        "ds_read_b64_tr_b16 a[128:129], v8 offset:12288\n\t" \
        "v_mfma_f32_16x16x32_bf16 v[52:55], a[116:119], v[136:139], v[52:55]\n\t" \
        "ds_read_b64_tr_b16 a[130:131], v8 offset:13056\n\t" \
        "s_waitcnt lgkmcnt(6)\n\t" \
        "v_mfma_f32_16x16x32_bf16 v[56:59], a[108:111], v[140:143], v[56:59]\n\t" \
        "ds_read_b128 v[120:123], v161 offset:64\n\t" \
        "v_mfma_f32_16x16x32_bf16 v[60:63], a[112:115], v[140:143], v[60:63]\n\t" \
        "buffer_load_dwordx4 v4, s[24:27], s57 offen offset:1024  lds\n\t" \
        "v_mfma_f32_16x16x32_bf16 v[64:67], a[116:119], v[140:143], v[64:67]\n\t" \
        "ds_read_b128 v[124:127], v162 offset:64\n\t" \
        "v_mfma_f32_16x16x32_bf16 v[68:71], a[108:111], v[144:147], v[68:71]\n\t" \
        "ds_read_b128 v[128:131], v163 offset:64\n\t" \
        "v_mfma_f32_16x16x32_bf16 v[72:75], a[112:115], v[144:147], v[72:75]\n\t" \
        "ds_read_b128 v[132:135], v160 offset:128\n\t" \
        "v_mfma_f32_16x16x32_bf16 v[76:79], a[116:119], v[144:147], v[76:79]\n\t" \
        "ds_read_b128 v[164:167], v15 offset:1024\n\t" \
        "s_waitcnt lgkmcnt(6)\n\t" \
        "v_mfma_f32_16x16x32_bf16 v[32:35], a[120:123], v[116:119], v[32:35]\n\t" \
        "ds_read_b64_tr_b16 a[132:133], v6 offset:18432\n\t" \
        "v_mfma_f32_16x16x32_bf16 v[36:39], a[124:127], v[116:119], v[36:39]\n\t" \
        "buffer_load_dwordx4 v5, s[24:27], s57 offen offset:2048  lds\n\t" \
        "s_waitcnt lgkmcnt(5)\n\t" \
        "v_mfma_f32_16x16x32_bf16 v[40:43], a[128:131], v[116:119], v[40:43]\n\t" \
        "ds_read_b64_tr_b16 a[134:135], v6 offset:19200\n\t" \
        "v_mfma_f32_16x16x32_bf16 v[44:47], a[120:123], v[120:123], v[44:47]\n\t" \
        "ds_read_b64_tr_b16 a[136:137], v7 offset:18432\n\t" \
        "v_mfma_f32_16x16x32_bf16 v[48:51], a[124:127], v[120:123], v[48:51]\n\t" \
        "ds_read_b64_tr_b16 a[138:139], v7 offset:19200\n\t" \
        "v_mfma_f32_16x16x32_bf16 v[52:55], a[128:131], v[120:123], v[52:55]\n\t" \
        "ds_read_b64_tr_b16 a[140:141], v8 offset:18432\n\t" \
        "s_waitcnt lgkmcnt(5)\n\t" \
        "v_xor_b32 v164, v164, v14\n\t" \
        "v_mfma_f32_16x16x32_bf16 v[56:59], a[120:123], v[124:127], v[56:59]\n\t" \
        "ds_read_b64_tr_b16 a[142:143], v8 offset:19200\n\t" \
        "v_xor_b32 v165, v165, v14\n\t" \
        "v_mfma_f32_16x16x32_bf16 v[60:63], a[124:127], v[124:127], v[60:63]\n\t" \
        "ds_read_b128 v[136:139], v161 offset:128\n\t" \
        "v_xor_b32 v166, v166, v14\n\t" \
        "v_mfma_f32_16x16x32_bf16 v[64:67], a[128:131], v[124:127], v[64:67]\n\t" \
        "ds_read_b128 v[140:143], v162 offset:128\n\t" \
        "v_xor_b32 v167, v167, v14\n\t" \
        "v_mfma_f32_16x16x32_bf16 v[68:71], a[120:123], v[128:131], v[68:71]\n\t" \
        "ds_read_b128 v[144:147], v163 offset:128\n\t" \
        "v_mfma_f32_16x16x32_bf16 v[72:75], a[124:127], v[128:131], v[72:75]\n\t" \
        "ds_read_b128 v[116:119], v164 offset:0\n\t" \
        "v_mfma_f32_16x16x32_bf16 v[76:79], a[128:131], v[128:131], v[76:79]\n\t" \
        "s_waitcnt vmcnt(9) lgkmcnt(4)\n\t" \
        "s_barrier\n\t" \
        "ds_read_b64_tr_b16 a[144:145], v6 offset:24576\n\t" \
        "s_add_u32 s57, s40, 0x1e000\n\t" \
        "s_add_u32 m0, s39, 0x3000\n\t" \
        "v_mfma_f32_16x16x32_bf16 v[32:35], a[132:135], v[132:135], v[32:35]\n\t" \
        "ds_read_b64_tr_b16 a[146:147], v6 offset:25344\n\t" \
        "v_mfma_f32_16x16x32_bf16 v[36:39], a[136:139], v[132:135], v[36:39]\n\t" \
        "buffer_load_dwordx4 v3, s[24:27], s57 offen offset:0  lds\n\t" \
        "v_mfma_f32_16x16x32_bf16 v[40:43], a[140:143], v[132:135], v[40:43]\n\t" \
        "ds_read_b64_tr_b16 a[148:149], v7 offset:24576\n\t" \
        "s_waitcnt lgkmcnt(6)\n\t" \
        "v_mfma_f32_16x16x32_bf16 v[44:47], a[132:135], v[136:139], v[44:47]\n\t" \
        "ds_read_b64_tr_b16 a[150:151], v7 offset:25344\n\t" \
        "v_mfma_f32_16x16x32_bf16 v[48:51], a[136:139], v[136:139], v[48:51]\n\t" \
        "ds_read_b64_tr_b16 a[152:153], v8 offset:24576\n\t" \
        "v_mfma_f32_16x16x32_bf16 v[52:55], a[140:143], v[136:139], v[52:55]\n\t" \
        "ds_read_b64_tr_b16 a[154:155], v8 offset:25344\n\t" \
        "s_waitcnt lgkmcnt(6)\n\t" \
        "v_mfma_f32_16x16x32_bf16 v[56:59], a[132:135], v[140:143], v[56:59]\n\t" \
        "ds_read_b128 v[120:123], v165 offset:0\n\t" \
        "v_mfma_f32_16x16x32_bf16 v[60:63], a[136:139], v[140:143], v[60:63]\n\t" \
        "buffer_load_dwordx4 v4, s[24:27], s57 offen offset:1024  lds\n\t" \
        "v_mfma_f32_16x16x32_bf16 v[64:67], a[140:143], v[140:143], v[64:67]\n\t" \
        "ds_read_b128 v[124:127], v166 offset:0\n\t" \
        "v_mfma_f32_16x16x32_bf16 v[68:71], a[132:135], v[144:147], v[68:71]\n\t" \
        "ds_read_b128 v[128:131], v167 offset:0\n\t" \
        "v_mfma_f32_16x16x32_bf16 v[72:75], a[136:139], v[144:147], v[72:75]\n\t" \
        "ds_read_b128 v[132:135], v164 offset:64\n\t" \
        "v_mfma_f32_16x16x32_bf16 v[76:79], a[140:143], v[144:147], v[76:79]\n\t" \
        "ds_read_b64_tr_b16 a[156:157], v6 offset:30720\n\t" \
        "s_waitcnt lgkmcnt(6)\n\t" \
        "v_mfma_f32_16x16x32_bf16 v[32:35], a[144:147], v[116:119], v[32:35]\n\t" \
        "ds_read_b64_tr_b16 a[158:159], v6 offset:31488\n\t" \
        "v_mfma_f32_16x16x32_bf16 v[36:39], a[148:151], v[116:119], v[36:39]\n\t" \
        "buffer_load_dwordx4 v5, s[24:27], s57 offen offset:2048  lds\n\t" \
        "s_waitcnt lgkmcnt(5)\n\t" \
        "v_mfma_f32_16x16x32_bf16 v[40:43], a[152:155], v[116:119], v[40:43]\n\t" \
        "ds_read_b64_tr_b16 a[160:161], v7 offset:30720\n\t" \
        "v_mfma_f32_16x16x32_bf16 v[44:47], a[144:147], v[120:123], v[44:47]\n\t" \
        "ds_read_b64_tr_b16 a[162:163], v7 offset:31488\n\t" \
        "v_mfma_f32_16x16x32_bf16 v[48:51], a[148:151], v[120:123], v[48:51]\n\t" \
        "ds_read_b64_tr_b16 a[164:165], v8 offset:30720\n\t" \
        "v_mfma_f32_16x16x32_bf16 v[52:55], a[152:155], v[120:123], v[52:55]\n\t" \
        "ds_read_b64_tr_b16 a[166:167], v8 offset:31488\n\t" \
        "s_waitcnt lgkmcnt(6)\n\t" \
        "v_mfma_f32_16x16x32_bf16 v[56:59], a[144:147], v[124:127], v[56:59]\n\t" \
        "ds_read_b128 v[136:139], v165 offset:64\n\t" \
        "v_mfma_f32_16x16x32_bf16 v[60:63], a[148:151], v[124:127], v[60:63]\n\t" \
        "ds_read_b128 v[140:143], v166 offset:64\n\t" \
        "v_mfma_f32_16x16x32_bf16 v[64:67], a[152:155], v[124:127], v[64:67]\n\t" \
        "ds_read_b128 v[144:147], v167 offset:64\n\t" \
        "v_mfma_f32_16x16x32_bf16 v[68:71], a[144:147], v[128:131], v[68:71]\n\t" \
        "ds_read_b128 v[116:119], v164 offset:128\n\t" \
        "v_mfma_f32_16x16x32_bf16 v[72:75], a[148:151], v[128:131], v[72:75]\n\t" \
        "ds_read_b128 v[168:171], v15 offset:1280\n\t" \
        "v_mfma_f32_16x16x32_bf16 v[76:79], a[152:155], v[128:131], v[76:79]\n\t" \
        "s_waitcnt vmcnt(9) lgkmcnt(5)\n\t" \
        "s_barrier\n\t" \
        "ds_read_b64_tr_b16 a[168:169], v6 offset:36864\n\t" \
        "s_add_u32 s57, s40, 0x21000\n\t" \
        "s_add_u32 m0, s39, 0x6000\n\t" \
        "v_mfma_f32_16x16x32_bf16 v[32:35], a[156:159], v[132:135], v[32:35]\n\t" \
        "ds_read_b64_tr_b16 a[170:171], v6 offset:37632\n\t" \
        "v_mfma_f32_16x16x32_bf16 v[36:39], a[160:163], v[132:135], v[36:39]\n\t" \
        "buffer_load_dwordx4 v3, s[24:27], s57 offen offset:0  lds\n\t" \
        "v_mfma_f32_16x16x32_bf16 v[40:43], a[164:167], v[132:135], v[40:43]\n\t" \
        "ds_read_b64_tr_b16 a[172:173], v7 offset:36864\n\t" \
        "s_waitcnt lgkmcnt(6)\n\t" \
        "v_mfma_f32_16x16x32_bf16 v[44:47], a[156:159], v[136:139], v[44:47]\n\t" \
        "ds_read_b64_tr_b16 a[174:175], v7 offset:37632\n\t" \
        "v_mfma_f32_16x16x32_bf16 v[48:51], a[160:163], v[136:139], v[48:51]\n\t" \
        "ds_read_b64_tr_b16 a[176:177], v8 offset:36864\n\t" \
        "s_waitcnt lgkmcnt(5)\n\t" \
        "v_xor_b32 v168, v168, v14\n\t" \
        "v_mfma_f32_16x16x32_bf16 v[52:55], a[164:167], v[136:139], v[52:55]\n\t" \
        "ds_read_b64_tr_b16 a[178:179], v8 offset:37632\n\t" \
        "v_xor_b32 v169, v169, v14\n\t" \
        "v_mfma_f32_16x16x32_bf16 v[56:59], a[156:159], v[140:143], v[56:59]\n\t" \
        "ds_read_b128 v[120:123], v165 offset:128\n\t" \
        "v_xor_b32 v170, v170, v14\n\t" \
        "v_mfma_f32_16x16x32_bf16 v[60:63], a[160:163], v[140:143], v[60:63]\n\t" \
        "buffer_load_dwordx4 v4, s[24:27], s57 offen offset:1024  lds\n\t" \
        "v_mfma_f32_16x16x32_bf16 v[64:67], a[164:167], v[140:143], v[64:67]\n\t" \
        "ds_read_b128 v[124:127], v166 offset:128\n\t" \
        "v_xor_b32 v171, v171, v14\n\t" \
        "v_mfma_f32_16x16x32_bf16 v[68:71], a[156:159], v[144:147], v[68:71]\n\t" \
        "ds_read_b128 v[128:131], v167 offset:128\n\t" \
        "v_mfma_f32_16x16x32_bf16 v[72:75], a[160:163], v[144:147], v[72:75]\n\t" \
        "ds_read_b128 v[132:135], v168 offset:0\n\t" \
        "v_mfma_f32_16x16x32_bf16 v[76:79], a[164:167], v[144:147], v[76:79]\n\t" \
        "ds_read_b64_tr_b16 a[180:181], v6 offset:43008\n\t" \
        "s_waitcnt lgkmcnt(6)\n\t" \
        "v_mfma_f32_16x16x32_bf16 v[32:35], a[168:171], v[116:119], v[32:35]\n\t" \
        "ds_read_b64_tr_b16 a[182:183], v6 offset:43776\n\t" \
        "v_mfma_f32_16x16x32_bf16 v[36:39], a[172:175], v[116:119], v[36:39]\n\t" \
        "buffer_load_dwordx4 v5, s[24:27], s57 offen offset:2048  lds\n\t" \
        "s_waitcnt lgkmcnt(5)\n\t" \
        "v_mfma_f32_16x16x32_bf16 v[40:43], a[176:179], v[116:119], v[40:43]\n\t" \
        "ds_read_b64_tr_b16 a[184:185], v7 offset:43008\n\t" \
        "v_mfma_f32_16x16x32_bf16 v[44:47], a[168:171], v[120:123], v[44:47]\n\t" \
        "ds_read_b64_tr_b16 a[186:187], v7 offset:43776\n\t" \
        "v_mfma_f32_16x16x32_bf16 v[48:51], a[172:175], v[120:123], v[48:51]\n\t" \
        "ds_read_b64_tr_b16 a[188:189], v8 offset:43008\n\t" \
        "v_mfma_f32_16x16x32_bf16 v[52:55], a[176:179], v[120:123], v[52:55]\n\t" \
        "ds_read_b64_tr_b16 a[190:191], v8 offset:43776\n\t" \
        "s_waitcnt lgkmcnt(6)\n\t" \
        "v_mfma_f32_16x16x32_bf16 v[56:59], a[168:171], v[124:127], v[56:59]\n\t" \
        "ds_read_b128 v[136:139], v169 offset:0\n\t" \
        "v_mfma_f32_16x16x32_bf16 v[60:63], a[172:175], v[124:127], v[60:63]\n\t" \
        "ds_read_b128 v[140:143], v170 offset:0\n\t" \
        "v_mfma_f32_16x16x32_bf16 v[64:67], a[176:179], v[124:127], v[64:67]\n\t" \
        "ds_read_b128 v[144:147], v171 offset:0\n\t" \
        "v_mfma_f32_16x16x32_bf16 v[68:71], a[168:171], v[128:131], v[68:71]\n\t" \
        "ds_read_b128 v[116:119], v168 offset:64\n\t" \
        "v_mfma_f32_16x16x32_bf16 v[72:75], a[172:175], v[128:131], v[72:75]\n\t" \
        "ds_read_b128 v[120:123], v169 offset:64\n\t" \
        "v_mfma_f32_16x16x32_bf16 v[76:79], a[176:179], v[128:131], v[76:79]\n\t" \
        "s_waitcnt vmcnt(9) lgkmcnt(5)\n\t" \
        "s_barrier\n\t" \
        "ds_read_b64_tr_b16 a[192:193], v6 offset:49152\n\t" \
        "s_add_u32 s57, s40, 0x24000\n\t" \
        "s_add_u32 m0, s39, 0x9000\n\t" \
        "v_mfma_f32_16x16x32_bf16 v[32:35], a[180:183], v[132:135], v[32:35]\n\t" \
        "ds_read_b64_tr_b16 a[194:195], v6 offset:49920\n\t" \
        "v_mfma_f32_16x16x32_bf16 v[36:39], a[184:187], v[132:135], v[36:39]\n\t" \
        "buffer_load_dwordx4 v3, s[24:27], s57 offen offset:0  lds\n\t" \
        "v_mfma_f32_16x16x32_bf16 v[40:43], a[188:191], v[132:135], v[40:43]\n\t" \
        "ds_read_b64_tr_b16 a[196:197], v7 offset:49152\n\t" \
        "s_waitcnt lgkmcnt(6)\n\t" \
        "v_mfma_f32_16x16x32_bf16 v[44:47], a[180:183], v[136:139], v[44:47]\n\t" \
        "ds_read_b64_tr_b16 a[198:199], v7 offset:49920\n\t" \
        "v_mfma_f32_16x16x32_bf16 v[48:51], a[184:187], v[136:139], v[48:51]\n\t" \
        "ds_read_b64_tr_b16 a[200:201], v8 offset:49152\n\t" \
        "v_mfma_f32_16x16x32_bf16 v[52:55], a[188:191], v[136:139], v[52:55]\n\t" \
        "ds_read_b64_tr_b16 a[202:203], v8 offset:49920\n\t" \
        "v_mfma_f32_16x16x32_bf16 v[56:59], a[180:183], v[140:143], v[56:59]\n\t" \
        "ds_read_b128 v[124:127], v170 offset:64\n\t" \
        "v_mfma_f32_16x16x32_bf16 v[60:63], a[184:187], v[140:143], v[60:63]\n\t" \
        "buffer_load_dwordx4 v4, s[24:27], s57 offen offset:1024  lds\n\t" \
        "v_mfma_f32_16x16x32_bf16 v[64:67], a[188:191], v[140:143], v[64:67]\n\t" \
        "ds_read_b128 v[128:131], v171 offset:64\n\t" \
        "s_waitcnt lgkmcnt(6)\n\t" \
        "v_mfma_f32_16x16x32_bf16 v[68:71], a[180:183], v[144:147], v[68:71]\n\t" \
        "ds_read_b128 v[132:135], v168 offset:128\n\t" \
        "v_mfma_f32_16x16x32_bf16 v[72:75], a[184:187], v[144:147], v[72:75]\n\t" \
        "ds_read_b128 v[172:175], v15 offset:1536\n\t" \
        "v_mfma_f32_16x16x32_bf16 v[76:79], a[188:191], v[144:147], v[76:79]\n\t" \
        "ds_read_b64_tr_b16 a[204:205], v6 offset:55296\n\t" \
        "v_mfma_f32_16x16x32_bf16 v[32:35], a[192:195], v[116:119], v[32:35]\n\t" \
        "ds_read_b64_tr_b16 a[206:207], v6 offset:56064\n\t" \
        "s_waitcnt lgkmcnt(6)\n\t" \
        "v_mfma_f32_16x16x32_bf16 v[36:39], a[196:199], v[116:119], v[36:39]\n\t" \
        "buffer_load_dwordx4 v5, s[24:27], s57 offen offset:2048  lds\n\t" \
        "v_mfma_f32_16x16x32_bf16 v[40:43], a[200:203], v[116:119], v[40:43]\n\t" \
        "ds_read_b64_tr_b16 a[208:209], v7 offset:55296\n\t" \
        "v_mfma_f32_16x16x32_bf16 v[44:47], a[192:195], v[120:123], v[44:47]\n\t" \
        "ds_read_b64_tr_b16 a[210:211], v7 offset:56064\n\t" \
        "v_mfma_f32_16x16x32_bf16 v[48:51], a[196:199], v[120:123], v[48:51]\n\t" \
        "ds_read_b64_tr_b16 a[212:213], v8 offset:55296\n\t" \
        "s_waitcnt lgkmcnt(5)\n\t" \
        "v_xor_b32 v172, v172, v14\n\t" \
        "v_mfma_f32_16x16x32_bf16 v[52:55], a[200:203], v[120:123], v[52:55]\n\t" \
        "ds_read_b64_tr_b16 a[214:215], v8 offset:56064\n\t" \
        "v_xor_b32 v173, v173, v14\n\t" \
        "v_mfma_f32_16x16x32_bf16 v[56:59], a[192:195], v[124:127], v[56:59]\n\t" \
        "ds_read_b128 v[136:139], v169 offset:128\n\t" \
        "v_xor_b32 v174, v174, v14\n\t" \
        "v_mfma_f32_16x16x32_bf16 v[60:63], a[196:199], v[124:127], v[60:63]\n\t" \
        "ds_read_b128 v[140:143], v170 offset:128\n\t" \
        "v_xor_b32 v175, v175, v14\n\t" \
        "v_mfma_f32_16x16x32_bf16 v[64:67], a[200:203], v[124:127], v[64:67]\n\t" \
        "ds_read_b128 v[144:147], v171 offset:128\n\t" \
        "v_mfma_f32_16x16x32_bf16 v[68:71], a[192:195], v[128:131], v[68:71]\n\t" \
        "ds_read_b128 v[116:119], v172 offset:0\n\t" \
        "v_mfma_f32_16x16x32_bf16 v[72:75], a[196:199], v[128:131], v[72:75]\n\t" \
        "ds_read_b128 v[120:123], v173 offset:0\n\t" \
        "v_mfma_f32_16x16x32_bf16 v[76:79], a[200:203], v[128:131], v[76:79]\n\t" \
        "s_waitcnt vmcnt(9) lgkmcnt(5)\n\t" \
        "s_barrier\n\t" \
        "ds_read_b64_tr_b16 a[216:217], v6 offset:0\n\t" \
        "s_add_u32 s57, s40, 0x27000\n\t" \
        "s_add_u32 m0, s39, 0xc000\n\t" \
        "v_mfma_f32_16x16x32_bf16 v[32:35], a[204:207], v[132:135], v[32:35]\n\t" \
        "ds_read_b64_tr_b16 a[218:219], v6 offset:768\n\t" \
        "v_mfma_f32_16x16x32_bf16 v[36:39], a[208:211], v[132:135], v[36:39]\n\t" \
        "buffer_load_dwordx4 v3, s[24:27], s57 offen offset:0  lds\n\t" \
        "v_mfma_f32_16x16x32_bf16 v[40:43], a[212:215], v[132:135], v[40:43]\n\t" \
        "ds_read_b64_tr_b16 a[220:221], v7 offset:0\n\t" \
        "s_waitcnt lgkmcnt(6)\n\t" \
        "v_mfma_f32_16x16x32_bf16 v[44:47], a[204:207], v[136:139], v[44:47]\n\t" \
        "ds_read_b64_tr_b16 a[222:223], v7 offset:768\n\t" \
        "v_mfma_f32_16x16x32_bf16 v[48:51], a[208:211], v[136:139], v[48:51]\n\t" \
        "ds_read_b64_tr_b16 a[224:225], v8 offset:0\n\t" \
        "v_mfma_f32_16x16x32_bf16 v[52:55], a[212:215], v[136:139], v[52:55]\n\t" \
        "ds_read_b64_tr_b16 a[226:227], v8 offset:768\n\t" \
        "v_mfma_f32_16x16x32_bf16 v[56:59], a[204:207], v[140:143], v[56:59]\n\t" \
        "ds_read_b128 v[124:127], v174 offset:0\n\t" \
        "v_mfma_f32_16x16x32_bf16 v[60:63], a[208:211], v[140:143], v[60:63]\n\t" \
        "buffer_load_dwordx4 v4, s[24:27], s57 offen offset:1024  lds\n\t" \
        "v_mfma_f32_16x16x32_bf16 v[64:67], a[212:215], v[140:143], v[64:67]\n\t" \
        "ds_read_b128 v[128:131], v175 offset:0\n\t" \
        "s_waitcnt lgkmcnt(6)\n\t" \
        "v_mfma_f32_16x16x32_bf16 v[68:71], a[204:207], v[144:147], v[68:71]\n\t" \
        "ds_read_b128 v[132:135], v172 offset:64\n\t" \
        "v_mfma_f32_16x16x32_bf16 v[72:75], a[208:211], v[144:147], v[72:75]\n\t" \
        "ds_read_b64_tr_b16 a[228:229], v6 offset:6144\n\t" \
        "v_mfma_f32_16x16x32_bf16 v[76:79], a[212:215], v[144:147], v[76:79]\n\t" \
        "ds_read_b64_tr_b16 a[230:231], v6 offset:6912\n\t" \
        "v_mfma_f32_16x16x32_bf16 v[32:35], a[216:219], v[116:119], v[32:35]\n\t" \
        "ds_read_b64_tr_b16 a[232:233], v7 offset:6144\n\t" \
        "s_waitcnt lgkmcnt(6)\n\t" \
        "v_mfma_f32_16x16x32_bf16 v[36:39], a[220:223], v[116:119], v[36:39]\n\t" \
        "buffer_load_dwordx4 v5, s[24:27], s57 offen offset:2048  lds\n\t" \
        "v_mfma_f32_16x16x32_bf16 v[40:43], a[224:227], v[116:119], v[40:43]\n\t" \
        "ds_read_b64_tr_b16 a[234:235], v7 offset:6912\n\t" \
        "v_mfma_f32_16x16x32_bf16 v[44:47], a[216:219], v[120:123], v[44:47]\n\t" \
        "ds_read_b64_tr_b16 a[236:237], v8 offset:6144\n\t" \
        "v_mfma_f32_16x16x32_bf16 v[48:51], a[220:223], v[120:123], v[48:51]\n\t" \
        "ds_read_b64_tr_b16 a[238:239], v8 offset:6912\n\t" \
        "v_mfma_f32_16x16x32_bf16 v[52:55], a[224:227], v[120:123], v[52:55]\n\t" \
        "ds_read_b128 v[136:139], v173 offset:64\n\t" \
        "s_waitcnt lgkmcnt(6)\n\t" \
        "v_mfma_f32_16x16x32_bf16 v[56:59], a[216:219], v[124:127], v[56:59]\n\t" \
        "ds_read_b128 v[140:143], v174 offset:64\n\t" \
        "v_mfma_f32_16x16x32_bf16 v[60:63], a[220:223], v[124:127], v[60:63]\n\t" \
        "ds_read_b128 v[144:147], v175 offset:64\n\t" \
        "v_mfma_f32_16x16x32_bf16 v[64:67], a[224:227], v[124:127], v[64:67]\n\t" \
        "ds_read_b128 v[116:119], v172 offset:128\n\t" \
        "v_mfma_f32_16x16x32_bf16 v[68:71], a[216:219], v[128:131], v[68:71]\n\t" \
        "ds_read_b128 v[176:179], v15 offset:1792\n\t" \
        "v_mfma_f32_16x16x32_bf16 v[72:75], a[220:223], v[128:131], v[72:75]\n\t" \
        "ds_read_b128 v[120:123], v173 offset:128\n\t" \
        "v_mfma_f32_16x16x32_bf16 v[76:79], a[224:227], v[128:131], v[76:79]\n\t" \
        "s_waitcnt vmcnt(9)\n\t" \
        "s_barrier\n\t" \
        "ds_read_b64_tr_b16 a[240:241], v6 offset:12288\n\t" \
        "s_waitcnt lgkmcnt(7)\n\t" \
        "v_mfma_f32_16x16x32_bf16 v[32:35], a[228:231], v[132:135], v[32:35]\n\t" \
        "ds_read_b64_tr_b16 a[242:243], v6 offset:13056\n\t" \
        "v_mfma_f32_16x16x32_bf16 v[36:39], a[232:235], v[132:135], v[36:39]\n\t" \
        "ds_read_b64_tr_b16 a[244:245], v7 offset:12288\n\t" \
        "v_mfma_f32_16x16x32_bf16 v[40:43], a[236:239], v[132:135], v[40:43]\n\t" \
        "ds_read_b64_tr_b16 a[246:247], v7 offset:13056\n\t" \
        "s_waitcnt lgkmcnt(7)\n\t" \
        "v_mfma_f32_16x16x32_bf16 v[44:47], a[228:231], v[136:139], v[44:47]\n\t" \
        "ds_read_b64_tr_b16 a[248:249], v8 offset:12288\n\t" \
        "s_waitcnt lgkmcnt(6)\n\t" \
        "v_xor_b32 v176, v176, v14\n\t" \
        "v_mfma_f32_16x16x32_bf16 v[48:51], a[232:235], v[136:139], v[48:51]\n\t" \
        "ds_read_b64_tr_b16 a[250:251], v8 offset:13056\n\t" \
        "v_xor_b32 v177, v177, v14\n\t" \
        "v_mfma_f32_16x16x32_bf16 v[52:55], a[236:239], v[136:139], v[52:55]\n\t" \
        "ds_read_b128 v[124:127], v174 offset:128\n\t" \
        "v_xor_b32 v178, v178, v14\n\t" \
        "v_mfma_f32_16x16x32_bf16 v[56:59], a[228:231], v[140:143], v[56:59]\n\t" \
        "ds_read_b128 v[128:131], v175 offset:128\n\t" \
        "v_xor_b32 v179, v179, v14\n\t" \
        "v_mfma_f32_16x16x32_bf16 v[60:63], a[232:235], v[140:143], v[60:63]\n\t" \
        "ds_read_b128 v[132:135], v176 offset:0\n\t" \
        "v_mfma_f32_16x16x32_bf16 v[64:67], a[236:239], v[140:143], v[64:67]\n\t" \
        "ds_read_b64_tr_b16 a[252:253], v6 offset:18432\n\t" \
        "v_mfma_f32_16x16x32_bf16 v[68:71], a[228:231], v[144:147], v[68:71]\n\t" \
        "ds_read_b64_tr_b16 a[254:255], v6 offset:19200\n\t" \
        "v_mfma_f32_16x16x32_bf16 v[72:75], a[232:235], v[144:147], v[72:75]\n\t" \
        "ds_read_b64_tr_b16 v[188:189], v7 offset:18432\n\t" \
        "v_mfma_f32_16x16x32_bf16 v[76:79], a[236:239], v[144:147], v[76:79]\n\t" \
        "ds_read_b64_tr_b16 v[190:191], v7 offset:19200\n\t" \
        "s_waitcnt lgkmcnt(7)\n\t" \
        "v_mfma_f32_16x16x32_bf16 v[32:35], a[240:243], v[116:119], v[32:35]\n\t" \
        "ds_read_b64_tr_b16 v[192:193], v8 offset:18432\n\t" \
        "v_mfma_f32_16x16x32_bf16 v[36:39], a[244:247], v[116:119], v[36:39]\n\t" \
        "ds_read_b64_tr_b16 v[194:195], v8 offset:19200\n\t" \
        "v_mfma_f32_16x16x32_bf16 v[40:43], a[248:251], v[116:119], v[40:43]\n\t" \
        "ds_read_b128 v[136:139], v177 offset:0\n\t" \
        "v_mfma_f32_16x16x32_bf16 v[44:47], a[240:243], v[120:123], v[44:47]\n\t" \
        "ds_read_b128 v[140:143], v178 offset:0\n\t" \
        "v_mfma_f32_16x16x32_bf16 v[48:51], a[244:247], v[120:123], v[48:51]\n\t" \
        "ds_read_b128 v[144:147], v179 offset:0\n\t" \
        "v_mfma_f32_16x16x32_bf16 v[52:55], a[248:251], v[120:123], v[52:55]\n\t" \
        "ds_read_b128 v[116:119], v176 offset:64\n\t" \
        "s_waitcnt lgkmcnt(7)\n\t" \
        "v_mfma_f32_16x16x32_bf16 v[56:59], a[240:243], v[124:127], v[56:59]\n\t" \
        "ds_read_b128 v[120:123], v177 offset:64\n\t" \
        "v_mfma_f32_16x16x32_bf16 v[60:63], a[244:247], v[124:127], v[60:63]\n\t" \
        "ds_read_b128 v[180:183], v15 offset:2048\n\t" \
        "v_mfma_f32_16x16x32_bf16 v[64:67], a[248:251], v[124:127], v[64:67]\n\t" \
        "ds_read_b128 v[124:127], v178 offset:64\n\t" \
        "v_mfma_f32_16x16x32_bf16 v[68:71], a[240:243], v[128:131], v[68:71]\n\t" \
        "ds_read_b64 v[148:149], v15 offset:4608\n\t" \
        "v_mfma_f32_16x16x32_bf16 v[72:75], a[244:247], v[128:131], v[72:75]\n\t" \
        "ds_read_b64 v[152:153], v15 offset:4864\n\t" \
        "v_mfma_f32_16x16x32_bf16 v[76:79], a[248:251], v[128:131], v[76:79]\n\t" \
        "s_waitcnt vmcnt(6)\n\t" \
        "s_barrier\n\t" \
        "ds_read_b64_tr_b16 v[196:197], v6 offset:24576\n\t" \
        "v_mfma_f32_16x16x32_bf16 v[32:35], a[252:255], v[132:135], v[32:35]\n\t" \
        "ds_read_b64_tr_b16 v[198:199], v6 offset:25344\n\t" \
        "s_waitcnt lgkmcnt(7)\n\t" \
        "v_mfma_f32_16x16x32_bf16 v[36:39], v[188:191], v[132:135], v[36:39]\n\t" \
        "ds_read_b64_tr_b16 v[200:201], v7 offset:24576\n\t" \
        "s_waitcnt lgkmcnt(6)\n\t" \
        "v_xor_b32 v180, v180, v14\n\t" \
        "v_mfma_f32_16x16x32_bf16 v[40:43], v[192:195], v[132:135], v[40:43]\n\t" \
        "ds_read_b64_tr_b16 v[202:203], v7 offset:25344\n\t" \
        "v_xor_b32 v181, v181, v14\n\t" \
        "v_mfma_f32_16x16x32_bf16 v[44:47], a[252:255], v[136:139], v[44:47]\n\t" \
        "ds_read_b64_tr_b16 v[204:205], v8 offset:24576\n\t" \
        "v_xor_b32 v182, v182, v14\n\t" \
        "v_mfma_f32_16x16x32_bf16 v[48:51], v[188:191], v[136:139], v[48:51]\n\t" \
        "ds_read_b64_tr_b16 v[206:207], v8 offset:25344\n\t" \
        "v_xor_b32 v183, v183, v14\n\t" \
        "v_mfma_f32_16x16x32_bf16 v[52:55], v[192:195], v[136:139], v[52:55]\n\t" \
        "ds_read_b128 v[128:131], v179 offset:64\n\t" \
        "s_waitcnt lgkmcnt(7)\n\t" \
        "v_xor_b32 v148, v148, v14\n\t" \
        "v_mfma_f32_16x16x32_bf16 v[56:59], a[252:255], v[140:143], v[56:59]\n\t" \
        "ds_read_b128 v[132:135], v176 offset:128\n\t" \
        "v_xor_b32 v152, v152, v14\n\t" \
        "v_mfma_f32_16x16x32_bf16 v[60:63], v[188:191], v[140:143], v[60:63]\n\t" \
        "ds_read_b64_tr_b16 v[208:209], v6 offset:30720\n\t" \
        "v_xor_b32 v149, v149, v14\n\t" \
        "v_mfma_f32_16x16x32_bf16 v[64:67], v[192:195], v[140:143], v[64:67]\n\t" \
        "ds_read_b64_tr_b16 v[210:211], v6 offset:31488\n\t" \
        "v_xor_b32 v153, v153, v14\n\t" \
        "v_mfma_f32_16x16x32_bf16 v[68:71], a[252:255], v[144:147], v[68:71]\n\t" \
        "ds_read_b64_tr_b16 v[212:213], v7 offset:30720\n\t" \
        "v_mfma_f32_16x16x32_bf16 v[72:75], v[188:191], v[144:147], v[72:75]\n\t" \
        "ds_read_b64_tr_b16 v[214:215], v7 offset:31488\n\t" \
        "v_mfma_f32_16x16x32_bf16 v[76:79], v[192:195], v[144:147], v[76:79]\n\t" \
        "ds_read_b64_tr_b16 v[216:217], v8 offset:30720\n\t" \
        "s_waitcnt lgkmcnt(7)\n\t" \
        "v_mfma_f32_16x16x32_bf16 v[32:35], v[196:199], v[116:119], v[32:35]\n\t" \
        "ds_read_b64_tr_b16 v[218:219], v8 offset:31488\n\t" \
        "v_mfma_f32_16x16x32_bf16 v[36:39], v[200:203], v[116:119], v[36:39]\n\t" \
        "ds_read_b128 v[136:139], v177 offset:128\n\t" \
        "v_mfma_f32_16x16x32_bf16 v[40:43], v[204:207], v[116:119], v[40:43]\n\t" \
        "ds_read_b128 v[140:143], v178 offset:128\n\t" \
        "v_mfma_f32_16x16x32_bf16 v[44:47], v[196:199], v[120:123], v[44:47]\n\t" \
        "ds_read_b128 v[144:147], v179 offset:128\n\t" \
        "v_mfma_f32_16x16x32_bf16 v[48:51], v[200:203], v[120:123], v[48:51]\n\t" \
        "ds_read_b128 v[116:119], v180 offset:0\n\t" \
        "v_mfma_f32_16x16x32_bf16 v[52:55], v[204:207], v[120:123], v[52:55]\n\t" \
        "ds_read_b128 v[120:123], v181 offset:0\n\t" \
        "v_mfma_f32_16x16x32_bf16 v[56:59], v[196:199], v[124:127], v[56:59]\n\t" \
        "ds_read_b64 v[156:157], v15 offset:5120\n\t" \
        "v_mfma_f32_16x16x32_bf16 v[60:63], v[200:203], v[124:127], v[60:63]\n\t" \
        "ds_read_b64 v[160:161], v15 offset:5376\n\t" \
        "v_mfma_f32_16x16x32_bf16 v[64:67], v[204:207], v[124:127], v[64:67]\n\t" \
        "ds_read_b128 v[124:127], v182 offset:0\n\t" \
        "s_waitcnt lgkmcnt(7)\n\t" \
        "v_mfma_f32_16x16x32_bf16 v[68:71], v[196:199], v[128:131], v[68:71]\n\t" \
        "ds_read_b64 v[164:165], v15 offset:5632\n\t" \
        "v_mfma_f32_16x16x32_bf16 v[72:75], v[200:203], v[128:131], v[72:75]\n\t" \
        "ds_read_b64 v[168:169], v15 offset:5888\n\t" \
        "v_mfma_f32_16x16x32_bf16 v[76:79], v[204:207], v[128:131], v[76:79]\n\t" \
        "s_waitcnt vmcnt(3)\n\t" \
        "s_barrier\n\t" \
        "ds_read_b64_tr_b16 v[220:221], v6 offset:36864\n\t" \
        "v_mfma_f32_16x16x32_bf16 v[32:35], v[208:211], v[132:135], v[32:35]\n\t" \
        "ds_read_b64_tr_b16 v[222:223], v6 offset:37632\n\t" \
        "s_waitcnt lgkmcnt(6)\n\t" \
        "v_xor_b32 v156, v156, v14\n\t" \
        "v_mfma_f32_16x16x32_bf16 v[36:39], v[212:215], v[132:135], v[36:39]\n\t" \
        "ds_read_b64_tr_b16 v[224:225], v7 offset:36864\n\t" \
        "s_waitcnt lgkmcnt(6)\n\t" \
        "v_xor_b32 v160, v160, v14\n\t" \
        "v_mfma_f32_16x16x32_bf16 v[40:43], v[216:219], v[132:135], v[40:43]\n\t" \
        "ds_read_b64_tr_b16 v[226:227], v7 offset:37632\n\t" \
        "v_xor_b32 v157, v157, v14\n\t" \
        "v_mfma_f32_16x16x32_bf16 v[44:47], v[208:211], v[136:139], v[44:47]\n\t" \
        "ds_read_b64_tr_b16 v[228:229], v8 offset:36864\n\t" \
        "s_waitcnt lgkmcnt(6)\n\t" \
        "v_xor_b32 v164, v164, v14\n\t" \
        "v_mfma_f32_16x16x32_bf16 v[48:51], v[212:215], v[136:139], v[48:51]\n\t" \
        "ds_read_b64_tr_b16 v[230:231], v8 offset:37632\n\t" \
        "s_waitcnt lgkmcnt(6)\n\t" \
        "v_xor_b32 v168, v168, v14\n\t" \
        "v_mfma_f32_16x16x32_bf16 v[52:55], v[216:219], v[136:139], v[52:55]\n\t" \
        "ds_read_b128 v[128:131], v183 offset:0\n\t" \
        "v_xor_b32 v161, v161, v14\n\t" \
        "v_mfma_f32_16x16x32_bf16 v[56:59], v[208:211], v[140:143], v[56:59]\n\t" \
        "ds_read_b128 v[132:135], v180 offset:64\n\t" \
        "v_xor_b32 v165, v165, v14\n\t" \
        "v_mfma_f32_16x16x32_bf16 v[60:63], v[212:215], v[140:143], v[60:63]\n\t" \
        "ds_read_b64_tr_b16 v[232:233], v6 offset:43008\n\t" \
        "v_xor_b32 v169, v169, v14\n\t" \
        "v_mfma_f32_16x16x32_bf16 v[64:67], v[216:219], v[140:143], v[64:67]\n\t" \
        "ds_read_b64_tr_b16 v[234:235], v6 offset:43776\n\t" \
        "v_mfma_f32_16x16x32_bf16 v[68:71], v[208:211], v[144:147], v[68:71]\n\t" \
        "ds_read_b64_tr_b16 v[236:237], v7 offset:43008\n\t" \
        "v_mfma_f32_16x16x32_bf16 v[72:75], v[212:215], v[144:147], v[72:75]\n\t" \
        "ds_read_b64_tr_b16 v[238:239], v7 offset:43776\n\t" \
        "v_mfma_f32_16x16x32_bf16 v[76:79], v[216:219], v[144:147], v[76:79]\n\t" \
        "ds_read_b64_tr_b16 v[240:241], v8 offset:43008\n\t" \
        "s_waitcnt lgkmcnt(7)\n\t" \
        "v_mfma_f32_16x16x32_bf16 v[32:35], v[220:223], v[116:119], v[32:35]\n\t" \
        "ds_read_b64_tr_b16 v[242:243], v8 offset:43776\n\t" \
        "v_mfma_f32_16x16x32_bf16 v[36:39], v[224:227], v[116:119], v[36:39]\n\t" \
        "ds_read_b128 v[136:139], v181 offset:64\n\t" \
        "v_mfma_f32_16x16x32_bf16 v[40:43], v[228:231], v[116:119], v[40:43]\n\t" \
        "ds_read_b128 v[140:143], v182 offset:64\n\t" \
        "v_mfma_f32_16x16x32_bf16 v[44:47], v[220:223], v[120:123], v[44:47]\n\t" \
        "ds_read_b128 v[144:147], v183 offset:64\n\t" \
        "v_mfma_f32_16x16x32_bf16 v[48:51], v[224:227], v[120:123], v[48:51]\n\t" \
        "ds_read_b128 v[116:119], v180 offset:128\n\t" \
        "v_mfma_f32_16x16x32_bf16 v[52:55], v[228:231], v[120:123], v[52:55]\n\t" \
        "ds_read_b128 v[120:123], v181 offset:128\n\t" \
        "v_mfma_f32_16x16x32_bf16 v[56:59], v[220:223], v[124:127], v[56:59]\n\t" \
        "ds_read_b64 v[172:173], v15 offset:6144\n\t" \
        "v_mfma_f32_16x16x32_bf16 v[60:63], v[224:227], v[124:127], v[60:63]\n\t" \
        "ds_read_b64 v[176:177], v15 offset:6400\n\t" \
        "v_mfma_f32_16x16x32_bf16 v[64:67], v[228:231], v[124:127], v[64:67]\n\t" \
        "ds_read_b128 v[124:127], v182 offset:128\n\t" \
        "s_waitcnt lgkmcnt(7)\n\t" \
        "v_mfma_f32_16x16x32_bf16 v[68:71], v[220:223], v[128:131], v[68:71]\n\t" \
        "v_mfma_f32_16x16x32_bf16 v[72:75], v[224:227], v[128:131], v[72:75]\n\t" \
        "v_mfma_f32_16x16x32_bf16 v[76:79], v[228:231], v[128:131], v[76:79]\n\t" \
        "s_waitcnt vmcnt(0)\n\t" \
        "s_barrier\n\t" \
        "ds_read_b64_tr_b16 v[244:245], v6 offset:49152\n\t" \
        "v_mfma_f32_16x16x32_bf16 v[32:35], v[232:235], v[132:135], v[32:35]\n\t" \
        "ds_read_b64_tr_b16 v[246:247], v6 offset:49920\n\t" \
        "s_waitcnt lgkmcnt(4)\n\t" \
        "v_xor_b32 v172, v172, v14\n\t" \
        "v_mfma_f32_16x16x32_bf16 v[36:39], v[236:239], v[132:135], v[36:39]\n\t" \
        "ds_read_b64_tr_b16 v[248:249], v7 offset:49152\n\t" \
        "s_waitcnt lgkmcnt(4)\n\t" \
        "v_xor_b32 v176, v176, v14\n\t" \
        "v_mfma_f32_16x16x32_bf16 v[40:43], v[240:243], v[132:135], v[40:43]\n\t" \
        "ds_read_b64_tr_b16 v[250:251], v7 offset:49920\n\t" \
        "v_xor_b32 v173, v173, v14\n\t" \
        "v_mfma_f32_16x16x32_bf16 v[44:47], v[232:235], v[136:139], v[44:47]\n\t" \
        "ds_read_b64_tr_b16 v[252:253], v8 offset:49152\n\t" \
        "v_xor_b32 v177, v177, v14\n\t" \
        "v_mfma_f32_16x16x32_bf16 v[48:51], v[236:239], v[136:139], v[48:51]\n\t" \
        "ds_read_b64_tr_b16 v[254:255], v8 offset:49920\n\t" \
        "v_mfma_f32_16x16x32_bf16 v[52:55], v[240:243], v[136:139], v[52:55]\n\t" \
        "ds_read_b128 v[128:131], v183 offset:128\n\t" \
        "v_mfma_f32_16x16x32_bf16 v[56:59], v[232:235], v[140:143], v[56:59]\n\t" \
        "ds_read_b128 v[132:135], v148 offset:0\n\t" \
        "v_mfma_f32_16x16x32_bf16 v[60:63], v[236:239], v[140:143], v[60:63]\n\t" \
        "ds_read_b128 v[136:139], v148 offset:64\n\t" \
        "v_mfma_f32_16x16x32_bf16 v[64:67], v[240:243], v[140:143], v[64:67]\n\t" \
        "ds_read_b128 v[140:143], v148 offset:128\n\t" \
        "v_mfma_f32_16x16x32_bf16 v[68:71], v[232:235], v[144:147], v[68:71]\n\t" \
        "ds_read_b64 v[180:181], v15 offset:6656\n\t" \
        "v_mfma_f32_16x16x32_bf16 v[72:75], v[236:239], v[144:147], v[72:75]\n\t" \
        "v_mfma_f32_16x16x32_bf16 v[76:79], v[240:243], v[144:147], v[76:79]\n\t" \
        "ds_read_b128 v[144:147], v152 offset:0\n\t" \
        "s_waitcnt lgkmcnt(6)\n\t" \
        "v_mfma_f32_16x16x32_bf16 v[32:35], v[244:247], v[116:119], v[32:35]\n\t" \
        "v_mfma_f32_16x16x32_bf16 v[36:39], v[248:251], v[116:119], v[36:39]\n\t" \
        "v_mfma_f32_16x16x32_bf16 v[40:43], v[252:255], v[116:119], v[40:43]\n\t" \
        "ds_read_b128 v[116:119], v152 offset:64\n\t" \
        "v_mfma_f32_16x16x32_bf16 v[44:47], v[244:247], v[120:123], v[44:47]\n\t" \
        "s_waitcnt lgkmcnt(2)\n\t" \
        "v_xor_b32 v180, v180, v14\n\t" \
        "v_xor_b32 v181, v181, v14\n\t" \
        "v_mfma_f32_16x16x32_bf16 v[48:51], v[248:251], v[120:123], v[48:51]\n\t" \
        "v_mfma_f32_16x16x32_bf16 v[52:55], v[252:255], v[120:123], v[52:55]\n\t" \
        "ds_read_b128 v[120:123], v152 offset:128\n\t" \
        "v_mfma_f32_16x16x32_bf16 v[56:59], v[244:247], v[124:127], v[56:59]\n\t" \
        "v_mfma_f32_16x16x32_bf16 v[60:63], v[248:251], v[124:127], v[60:63]\n\t" \
        "v_mfma_f32_16x16x32_bf16 v[64:67], v[252:255], v[124:127], v[64:67]\n\t" \
        "ds_read_b128 v[124:127], v156 offset:0\n\t" \
        "v_mfma_f32_16x16x32_bf16 v[68:71], v[244:247], v[128:131], v[68:71]\n\t" \
        "v_mfma_f32_16x16x32_bf16 v[72:75], v[248:251], v[128:131], v[72:75]\n\t" \
        "v_mfma_f32_16x16x32_bf16 v[76:79], v[252:255], v[128:131], v[76:79]\n\t" \
        "ds_read_b128 v[128:131], v156 offset:64\n\t" \
        "v_mfma_f32_16x16x32_bf16 v[80:83], a[0:3], v[132:135], 0\n\t" \
        "v_mfma_f32_16x16x32_bf16 v[84:87], a[4:7], v[132:135], 0\n\t" \
        "v_mfma_f32_16x16x32_bf16 v[88:91], a[8:11], v[132:135], 0\n\t" \
        "v_cvt_pk_bf16_f32 v40, v40, v41\n\t" \
        "v_cvt_pk_bf16_f32 v41, v42, v43\n\t" \
        "v_mfma_f32_16x16x32_bf16 v[80:83], a[12:15], v[136:139], v[80:83]\n\t" \
        "ds_write_b64 v11, v[40:41] offset:0\n\t" \
        "v_cvt_pk_bf16_f32 v52, v52, v53\n\t" \
        "v_mfma_f32_16x16x32_bf16 v[84:87], a[16:19], v[136:139], v[84:87]\n\t" \
        "v_cvt_pk_bf16_f32 v53, v54, v55\n\t" \
        "v_cvt_pk_bf16_f32 v64, v64, v65\n\t" \
        "v_mfma_f32_16x16x32_bf16 v[88:91], a[20:23], v[136:139], v[88:91]\n\t" \
        "ds_write_b64 v11, v[52:53] offset:2048\n\t" \
        "v_cvt_pk_bf16_f32 v65, v66, v67\n\t" \
        "v_mfma_f32_16x16x32_bf16 v[80:83], a[24:27], v[140:143], v[80:83]\n\t" \
        "ds_write_b64 v11, v[64:65] offset:4096\n\t" \
        "v_cvt_pk_bf16_f32 v76, v76, v77\n\t" \
        "v_mfma_f32_16x16x32_bf16 v[84:87], a[28:31], v[140:143], v[84:87]\n\t" \
        "ds_read_b128 v[132:135], v156 offset:128\n\t" \
        "v_cvt_pk_bf16_f32 v77, v78, v79\n\t" \
        "v_mfma_f32_16x16x32_bf16 v[88:91], a[32:35], v[140:143], v[88:91]\n\t" \
        "ds_write_b64 v11, v[76:77] offset:6144\n\t" \
        "v_cvt_pk_bf16_f32 v32, v32, v33\n\t" \
        "s_waitcnt lgkmcnt(5)\n\t" \
        "v_mfma_f32_16x16x32_bf16 v[80:83], a[36:39], v[144:147], v[80:83]\n\t" \
        "ds_read_b128 v[136:139], v160 offset:0\n\t" \
        "v_cvt_pk_bf16_f32 v33, v34, v35\n\t" \
        "v_mfma_f32_16x16x32_bf16 v[84:87], a[40:43], v[144:147], v[84:87]\n\t" \
        "ds_read_b128 v[140:143], v160 offset:64\n\t" \
        "v_cvt_pk_bf16_f32 v34, v36, v37\n\t" \
        "v_mfma_f32_16x16x32_bf16 v[88:91], a[44:47], v[144:147], v[88:91]\n\t" \
        "ds_read_b128 v[144:147], v160 offset:128\n\t" \
        "v_cvt_pk_bf16_f32 v35, v38, v39\n\t" \
        "v_mfma_f32_16x16x32_bf16 v[80:83], a[48:51], v[116:119], v[80:83]\n\t" \
        "v_permlane16_swap_b32 v32, v34\n\t" \
        "v_permlane16_swap_b32 v33, v35\n\t" \
        "v_mfma_f32_16x16x32_bf16 v[84:87], a[52:55], v[116:119], v[84:87]\n\t" \
        "v_mfma_f32_16x16x32_bf16 v[88:91], a[56:59], v[116:119], v[88:91]\n\t" \
        "ds_read_b128 v[116:119], v164 offset:0\n\t" \
        "v_mfma_f32_16x16x32_bf16 v[80:83], a[60:63], v[120:123], v[80:83]\n\t" \
        "v_mfma_f32_16x16x32_bf16 v[84:87], a[64:67], v[120:123], v[84:87]\n\t" \
        "v_mfma_f32_16x16x32_bf16 v[88:91], a[68:71], v[120:123], v[88:91]\n\t" \
        "ds_read_b128 v[120:123], v164 offset:64\n\t" \
        "v_mfma_f32_16x16x32_bf16 v[80:83], a[72:75], v[124:127], v[80:83]\n\t" \
        "v_mfma_f32_16x16x32_bf16 v[84:87], a[76:79], v[124:127], v[84:87]\n\t" \
        "s_waitcnt lgkmcnt(5)\n\t" \
        "s_barrier\n\t" \
        "ds_read_b64 v[42:43], v12 offset:0\n\t" \
        "v_mfma_f32_16x16x32_bf16 v[88:91], a[80:83], v[124:127], v[88:91]\n\t" \
        "ds_read_b128 v[124:127], v164 offset:128\n\t" \
        "v_mfma_f32_16x16x32_bf16 v[80:83], a[84:87], v[128:131], v[80:83]\n\t" \
        "v_mfma_f32_16x16x32_bf16 v[84:87], a[88:91], v[128:131], v[84:87]\n\t" \
        "v_mfma_f32_16x16x32_bf16 v[88:91], a[92:95], v[128:131], v[88:91]\n\t" \
        "ds_read_b128 v[128:131], v168 offset:0\n\t" \
        "v_mfma_f32_16x16x32_bf16 v[80:83], a[96:99], v[132:135], v[80:83]\n\t" \
        "s_mov_b32 s56, s64\n\t" \
        "buffer_store_dwordx4 v[32:35], v9, s[28:31], s56 offen sc1\n\t" \
        "v_mfma_f32_16x16x32_bf16 v[84:87], a[100:103], v[132:135], v[84:87]\n\t" \
        "v_mfma_f32_16x16x32_bf16 v[88:91], a[104:107], v[132:135], v[88:91]\n\t" \
        "ds_read_b128 v[132:135], v168 offset:64\n\t" \
        "s_waitcnt lgkmcnt(3)\n\t" \
        "v_mfma_f32_16x16x32_bf16 v[80:83], a[108:111], v[136:139], v[80:83]\n\t" \
        "v_permlane16_swap_b32 v40, v42\n\t" \
        "v_permlane16_swap_b32 v41, v43\n\t" \
        "v_mfma_f32_16x16x32_bf16 v[84:87], a[112:115], v[136:139], v[84:87]\n\t" \
        "v_mfma_f32_16x16x32_bf16 v[88:91], a[116:119], v[136:139], v[88:91]\n\t" \
        "ds_read_b128 v[136:139], v168 offset:128\n\t" \
        "v_mfma_f32_16x16x32_bf16 v[80:83], a[120:123], v[140:143], v[80:83]\n\t" \
        "v_mfma_f32_16x16x32_bf16 v[84:87], a[124:127], v[140:143], v[84:87]\n\t" \
        "s_mov_b32 s56, s64\n\t" \
        "buffer_store_dwordx4 v[40:43], v10, s[28:31], s56 offen sc1\n\t" \
        "v_mfma_f32_16x16x32_bf16 v[88:91], a[128:131], v[140:143], v[88:91]\n\t" \
        "ds_read_b128 v[140:143], v172 offset:0\n\t" \
        "v_cvt_pk_bf16_f32 v44, v44, v45\n\t" \
        "v_mfma_f32_16x16x32_bf16 v[80:83], a[132:135], v[144:147], v[80:83]\n\t" \
        "v_cvt_pk_bf16_f32 v45, v46, v47\n\t" \
        "v_cvt_pk_bf16_f32 v46, v48, v49\n\t" \
        "v_mfma_f32_16x16x32_bf16 v[84:87], a[136:139], v[144:147], v[84:87]\n\t" \
        "v_cvt_pk_bf16_f32 v47, v50, v51\n\t" \
        "v_permlane16_swap_b32 v44, v46\n\t" \
        "v_mfma_f32_16x16x32_bf16 v[88:91], a[140:143], v[144:147], v[88:91]\n\t" \
        "ds_read_b128 v[144:147], v172 offset:64\n\t" \
        "v_permlane16_swap_b32 v45, v47\n\t" \
        "v_mfma_f32_16x16x32_bf16 v[80:83], a[144:147], v[116:119], v[80:83]\n\t" \
        "ds_read_b64 v[54:55], v12 offset:2048\n\t" \
        "v_mfma_f32_16x16x32_bf16 v[84:87], a[148:151], v[116:119], v[84:87]\n\t" \
        "v_mfma_f32_16x16x32_bf16 v[88:91], a[152:155], v[116:119], v[88:91]\n\t" \
        "ds_read_b128 v[116:119], v172 offset:128\n\t" \
        "v_mfma_f32_16x16x32_bf16 v[80:83], a[156:159], v[120:123], v[80:83]\n\t" \
        "v_mfma_f32_16x16x32_bf16 v[84:87], a[160:163], v[120:123], v[84:87]\n\t" \
        "v_mfma_f32_16x16x32_bf16 v[88:91], a[164:167], v[120:123], v[88:91]\n\t" \
        "ds_read_b128 v[120:123], v176 offset:0\n\t" \
        "s_waitcnt lgkmcnt(4)\n\t" \
        "v_mfma_f32_16x16x32_bf16 v[80:83], a[168:171], v[124:127], v[80:83]\n\t" \
        "v_mfma_f32_16x16x32_bf16 v[84:87], a[172:175], v[124:127], v[84:87]\n\t" \
        "v_mfma_f32_16x16x32_bf16 v[88:91], a[176:179], v[124:127], v[88:91]\n\t" \
        "ds_read_b128 v[124:127], v176 offset:64\n\t" \
        "s_waitcnt lgkmcnt(3)\n\t" \
        "v_permlane16_swap_b32 v52, v54\n\t" \
        "v_mfma_f32_16x16x32_bf16 v[80:83], a[180:183], v[128:131], v[80:83]\n\t" \
        "v_permlane16_swap_b32 v53, v55\n\t" \
        "v_mfma_f32_16x16x32_bf16 v[84:87], a[184:187], v[128:131], v[84:87]\n\t" \
        "v_mfma_f32_16x16x32_bf16 v[88:91], a[188:191], v[128:131], v[88:91]\n\t" \
        "ds_read_b128 v[128:131], v176 offset:128\n\t" \
        "v_mfma_f32_16x16x32_bf16 v[80:83], a[192:195], v[132:135], v[80:83]\n\t" \
        "v_mfma_f32_16x16x32_bf16 v[84:87], a[196:199], v[132:135], v[84:87]\n\t" \
        "v_mfma_f32_16x16x32_bf16 v[88:91], a[200:203], v[132:135], v[88:91]\n\t" \
        "ds_read_b128 v[132:135], v180 offset:0\n\t" \
        "v_mfma_f32_16x16x32_bf16 v[80:83], a[204:207], v[136:139], v[80:83]\n\t" \
        "v_mfma_f32_16x16x32_bf16 v[84:87], a[208:211], v[136:139], v[84:87]\n\t" \
        "v_mfma_f32_16x16x32_bf16 v[88:91], a[212:215], v[136:139], v[88:91]\n\t" \
        "ds_read_b128 v[136:139], v180 offset:64\n\t" \
        "v_mfma_f32_16x16x32_bf16 v[80:83], a[216:219], v[140:143], v[80:83]\n\t" \
        "s_add_u32 s56, s64, 0x15000\n\t" \
        "buffer_store_dwordx4 v[44:47], v9, s[28:31], s56 offen sc1\n\t" \
        "v_mfma_f32_16x16x32_bf16 v[84:87], a[220:223], v[140:143], v[84:87]\n\t" \
        "v_mfma_f32_16x16x32_bf16 v[88:91], a[224:227], v[140:143], v[88:91]\n\t" \
        "ds_read_b128 v[140:143], v180 offset:128\n\t" \
        "v_mfma_f32_16x16x32_bf16 v[80:83], a[228:231], v[144:147], v[80:83]\n\t" \
        "v_mfma_f32_16x16x32_bf16 v[84:87], a[232:235], v[144:147], v[84:87]\n\t" \
        "v_mfma_f32_16x16x32_bf16 v[88:91], a[236:239], v[144:147], v[88:91]\n\t" \
        "ds_read_b128 v[144:147], v149 offset:0\n\t" \
        "s_waitcnt lgkmcnt(3)\n\t" \
        "v_mfma_f32_16x16x32_bf16 v[80:83], a[240:243], v[116:119], v[80:83]\n\t" \
        "v_mfma_f32_16x16x32_bf16 v[84:87], a[244:247], v[116:119], v[84:87]\n\t" \
        "s_add_u32 s56, s64, 0x15000\n\t" \
        "buffer_store_dwordx4 v[52:55], v10, s[28:31], s56 offen sc1\n\t" \
        "v_mfma_f32_16x16x32_bf16 v[88:91], a[248:251], v[116:119], v[88:91]\n\t" \
        "ds_read_b128 v[116:119], v149 offset:64\n\t" \
        "v_cvt_pk_bf16_f32 v56, v56, v57\n\t" \
        "v_mfma_f32_16x16x32_bf16 v[80:83], a[252:255], v[120:123], v[80:83]\n\t" \
        "v_cvt_pk_bf16_f32 v57, v58, v59\n\t" \
        "v_cvt_pk_bf16_f32 v58, v60, v61\n\t" \
        "v_mfma_f32_16x16x32_bf16 v[84:87], v[188:191], v[120:123], v[84:87]\n\t" \
        "v_cvt_pk_bf16_f32 v59, v62, v63\n\t" \
        "v_permlane16_swap_b32 v56, v58\n\t" \
        "v_mfma_f32_16x16x32_bf16 v[88:91], v[192:195], v[120:123], v[88:91]\n\t" \
        "ds_read_b128 v[120:123], v149 offset:128\n\t" \
        "v_permlane16_swap_b32 v57, v59\n\t" \
        "v_mfma_f32_16x16x32_bf16 v[80:83], v[196:199], v[124:127], v[80:83]\n\t" \
        "ds_read_b64 v[66:67], v12 offset:4096\n\t" \
        "v_mfma_f32_16x16x32_bf16 v[84:87], v[200:203], v[124:127], v[84:87]\n\t" \
        "ds_read_b128 v[104:107], v173 offset:0\n\t" \
        "v_mfma_f32_16x16x32_bf16 v[88:91], v[204:207], v[124:127], v[88:91]\n\t" \
        "ds_read_b128 v[124:127], v153 offset:0\n\t" \
        "v_mfma_f32_16x16x32_bf16 v[80:83], v[208:211], v[128:131], v[80:83]\n\t" \
        "ds_read_b128 v[108:111], v173 offset:64\n\t" \
        "v_mfma_f32_16x16x32_bf16 v[84:87], v[212:215], v[128:131], v[84:87]\n\t" \
        "v_mfma_f32_16x16x32_bf16 v[88:91], v[216:219], v[128:131], v[88:91]\n\t" \
        "ds_read_b128 v[128:131], v153 offset:64\n\t" \
        "v_mfma_f32_16x16x32_bf16 v[80:83], v[220:223], v[132:135], v[80:83]\n\t" \
        "ds_read_b128 v[112:115], v173 offset:128\n\t" \
        "v_mfma_f32_16x16x32_bf16 v[84:87], v[224:227], v[132:135], v[84:87]\n\t" \
        "v_mfma_f32_16x16x32_bf16 v[88:91], v[228:231], v[132:135], v[88:91]\n\t" \
        "ds_read_b128 v[132:135], v153 offset:128\n\t" \
        "s_waitcnt lgkmcnt(5)\n\t" \
        "v_permlane16_swap_b32 v64, v66\n\t" \
        "v_mfma_f32_16x16x32_bf16 v[80:83], v[232:235], v[136:139], v[80:83]\n\t" \
        "ds_read_b128 v[32:35], v177 offset:0\n\t" \
        "v_permlane16_swap_b32 v65, v67\n\t" \
        "v_mfma_f32_16x16x32_bf16 v[84:87], v[236:239], v[136:139], v[84:87]\n\t" \
        "v_mfma_f32_16x16x32_bf16 v[88:91], v[240:243], v[136:139], v[88:91]\n\t" \
        "ds_read_b128 v[136:139], v157 offset:0\n\t" \
        "v_mfma_f32_16x16x32_bf16 v[80:83], v[244:247], v[140:143], v[80:83]\n\t" \
        "ds_read_b128 v[36:39], v177 offset:64\n\t" \
        "v_mfma_f32_16x16x32_bf16 v[84:87], v[248:251], v[140:143], v[84:87]\n\t" \
        "v_mfma_f32_16x16x32_bf16 v[88:91], v[252:255], v[140:143], v[88:91]\n\t" \
        "ds_read_b128 v[140:143], v157 offset:64\n\t" \
        "v_mfma_f32_16x16x32_bf16 v[92:95], a[0:3], v[144:147], 0\n\t" \
        "ds_read_b128 v[40:43], v177 offset:128\n\t" \
        "v_mfma_f32_16x16x32_bf16 v[96:99], a[4:7], v[144:147], 0\n\t" \
        "v_mfma_f32_16x16x32_bf16 v[100:103], a[8:11], v[144:147], 0\n\t" \
        "s_nop 4\n\t" \
        "v_cvt_pk_bf16_f32 v88, v88, v89\n\t" \
        "v_cvt_pk_bf16_f32 v89, v90, v91\n\t" \
        "v_mfma_f32_16x16x32_bf16 v[92:95], a[12:15], v[116:119], v[92:95]\n\t" \
        "s_add_u32 s56, s64, 0x2a000\n\t" \
        "buffer_store_dwordx4 v[56:59], v9, s[28:31], s56 offen sc1\n\t" \
        "v_mfma_f32_16x16x32_bf16 v[96:99], a[16:19], v[116:119], v[96:99]\n\t" \
        "ds_write_b64 v11, v[88:89] offset:8192\n\t" \
        "v_cvt_pk_bf16_f32 v80, v80, v81\n\t" \
        "v_mfma_f32_16x16x32_bf16 v[100:103], a[20:23], v[116:119], v[100:103]\n\t" \
        "ds_read_b128 v[144:147], v157 offset:128\n\t" \
        "v_cvt_pk_bf16_f32 v81, v82, v83\n\t" \
        "v_mfma_f32_16x16x32_bf16 v[92:95], a[24:27], v[120:123], v[92:95]\n\t" \
        "ds_read_b128 v[116:119], v161 offset:0\n\t" \
        "v_cvt_pk_bf16_f32 v82, v84, v85\n\t" \
        "v_mfma_f32_16x16x32_bf16 v[96:99], a[28:31], v[120:123], v[96:99]\n\t" \
        "v_cvt_pk_bf16_f32 v83, v86, v87\n\t" \
        "v_permlane16_swap_b32 v80, v82\n\t" \
        "v_mfma_f32_16x16x32_bf16 v[100:103], a[32:35], v[120:123], v[100:103]\n\t" \
        "ds_read_b128 v[120:123], v161 offset:64\n\t" \
        "v_permlane16_swap_b32 v81, v83\n\t" \
        "s_waitcnt lgkmcnt(4)\n\t" \
        "v_mfma_f32_16x16x32_bf16 v[92:95], a[36:39], v[124:127], v[92:95]\n\t" \
        "v_mfma_f32_16x16x32_bf16 v[96:99], a[40:43], v[124:127], v[96:99]\n\t" \
        "s_add_u32 s56, s64, 0x2a000\n\t" \
        "buffer_store_dwordx4 v[64:67], v10, s[28:31], s56 offen sc1\n\t" \
        "v_mfma_f32_16x16x32_bf16 v[100:103], a[44:47], v[124:127], v[100:103]\n\t" \
        "ds_read_b128 v[124:127], v161 offset:128\n\t" \
        "v_cvt_pk_bf16_f32 v68, v68, v69\n\t" \
        "v_mfma_f32_16x16x32_bf16 v[92:95], a[48:51], v[128:131], v[92:95]\n\t" \
        "v_cvt_pk_bf16_f32 v69, v70, v71\n\t" \
        "v_cvt_pk_bf16_f32 v70, v72, v73\n\t" \
        "v_mfma_f32_16x16x32_bf16 v[96:99], a[52:55], v[128:131], v[96:99]\n\t" \
        "s_waitcnt lgkmcnt(4)\n\t" \
        "s_barrier\n\t" \
        "ds_read_b64 v[90:91], v12 offset:8192\n\t" \
        "v_cvt_pk_bf16_f32 v71, v74, v75\n\t" \
        "v_mfma_f32_16x16x32_bf16 v[100:103], a[56:59], v[128:131], v[100:103]\n\t" \
        "ds_read_b128 v[128:131], v165 offset:0\n\t" \
        "v_permlane16_swap_b32 v68, v70\n\t" \
        "v_mfma_f32_16x16x32_bf16 v[92:95], a[60:63], v[132:135], v[92:95]\n\t" \
        "ds_read_b64 v[78:79], v12 offset:6144\n\t" \
        "v_permlane16_swap_b32 v69, v71\n\t" \
        "v_mfma_f32_16x16x32_bf16 v[96:99], a[64:67], v[132:135], v[96:99]\n\t" \
        "v_mfma_f32_16x16x32_bf16 v[100:103], a[68:71], v[132:135], v[100:103]\n\t" \
        "ds_read_b128 v[132:135], v165 offset:64\n\t" \
        "v_mfma_f32_16x16x32_bf16 v[92:95], a[72:75], v[136:139], v[92:95]\n\t" \
        "v_mfma_f32_16x16x32_bf16 v[96:99], a[76:79], v[136:139], v[96:99]\n\t" \
        "ds_read_b128 v[44:47], v181 offset:0\n\t" \
        "v_mfma_f32_16x16x32_bf16 v[100:103], a[80:83], v[136:139], v[100:103]\n\t" \
        "ds_read_b128 v[136:139], v165 offset:128\n\t" \
        "v_mfma_f32_16x16x32_bf16 v[92:95], a[84:87], v[140:143], v[92:95]\n\t" \
        "s_waitcnt lgkmcnt(4)\n\t" \
        "v_permlane16_swap_b32 v88, v90\n\t" \
        "v_permlane16_swap_b32 v89, v91\n\t" \
        "v_mfma_f32_16x16x32_bf16 v[96:99], a[88:91], v[140:143], v[96:99]\n\t" \
        "ds_read_b128 v[48:51], v181 offset:64\n\t" \
        "v_mfma_f32_16x16x32_bf16 v[100:103], a[92:95], v[140:143], v[100:103]\n\t" \
        "ds_read_b128 v[140:143], v169 offset:0\n\t" \
        "s_waitcnt lgkmcnt(5)\n\t" \
        "v_permlane16_swap_b32 v76, v78\n\t" \
        "v_mfma_f32_16x16x32_bf16 v[92:95], a[96:99], v[144:147], v[92:95]\n\t" \
        "s_add_u32 s56, s64, 0x54000\n\t" \
        "buffer_store_dwordx4 v[80:83], v9, s[28:31], s56 offen sc1\n\t" \
        "v_mfma_f32_16x16x32_bf16 v[96:99], a[100:103], v[144:147], v[96:99]\n\t" \
        "ds_read_b128 v[52:55], v181 offset:128\n\t" \
        "v_permlane16_swap_b32 v77, v79\n\t" \
        "v_mfma_f32_16x16x32_bf16 v[100:103], a[104:107], v[144:147], v[100:103]\n\t" \
        "ds_read_b128 v[144:147], v169 offset:64\n\t" \
        "v_mfma_f32_16x16x32_bf16 v[92:95], a[108:111], v[116:119], v[92:95]\n\t" \
        "v_mfma_f32_16x16x32_bf16 v[96:99], a[112:115], v[116:119], v[96:99]\n\t" \
        "v_mfma_f32_16x16x32_bf16 v[100:103], a[116:119], v[116:119], v[100:103]\n\t" \
        "ds_read_b128 v[116:119], v169 offset:128\n\t" \
        "v_mfma_f32_16x16x32_bf16 v[100:103], a[128:131], v[120:123], v[100:103]\n\t" \
        "v_mfma_f32_16x16x32_bf16 v[100:103], a[140:143], v[124:127], v[100:103]\n\t" \
        "s_add_u32 s56, s64, 0x54000\n\t" \
        "buffer_store_dwordx4 v[88:91], v10, s[28:31], s56 offen sc1\n\t" \
        "v_mfma_f32_16x16x32_bf16 v[100:103], a[152:155], v[128:131], v[100:103]\n\t" \
        "s_waitcnt lgkmcnt(2)\n\t" \
        "v_mfma_f32_16x16x32_bf16 v[100:103], a[164:167], v[132:135], v[100:103]\n\t" \
        "s_add_u32 s56, s64, 0x3f000\n\t" \
        "buffer_store_dwordx4 v[68:71], v9, s[28:31], s56 offen sc1\n\t" \
        "v_mfma_f32_16x16x32_bf16 v[100:103], a[176:179], v[136:139], v[100:103]\n\t" \
        "v_mfma_f32_16x16x32_bf16 v[100:103], a[188:191], v[140:143], v[100:103]\n\t" \
        "s_waitcnt lgkmcnt(1)\n\t" \
        "v_mfma_f32_16x16x32_bf16 v[100:103], a[200:203], v[144:147], v[100:103]\n\t" \
        "s_waitcnt lgkmcnt(0)\n\t" \
        "v_mfma_f32_16x16x32_bf16 v[100:103], a[212:215], v[116:119], v[100:103]\n\t" \
        "v_mfma_f32_16x16x32_bf16 v[100:103], a[224:227], v[104:107], v[100:103]\n\t" \
        "v_mfma_f32_16x16x32_bf16 v[100:103], a[236:239], v[108:111], v[100:103]\n\t" \
        "v_mfma_f32_16x16x32_bf16 v[100:103], a[248:251], v[112:115], v[100:103]\n\t" \
        "s_add_u32 s56, s64, 0x3f000\n\t" \
        "buffer_store_dwordx4 v[76:79], v10, s[28:31], s56 offen sc1\n\t" \
        "v_mfma_f32_16x16x32_bf16 v[100:103], v[192:195], v[32:35], v[100:103]\n\t" \
        "v_mfma_f32_16x16x32_bf16 v[100:103], v[204:207], v[36:39], v[100:103]\n\t" \
        "v_mfma_f32_16x16x32_bf16 v[100:103], v[216:219], v[40:43], v[100:103]\n\t" \
        "v_mfma_f32_16x16x32_bf16 v[100:103], v[228:231], v[44:47], v[100:103]\n\t" \
        "v_mfma_f32_16x16x32_bf16 v[100:103], v[240:243], v[48:51], v[100:103]\n\t" \
        "v_mfma_f32_16x16x32_bf16 v[100:103], v[252:255], v[52:55], v[100:103]\n\t" \
        "v_mfma_f32_16x16x32_bf16 v[92:95], a[120:123], v[120:123], v[92:95]\n\t" \
        "v_mfma_f32_16x16x32_bf16 v[96:99], a[124:127], v[120:123], v[96:99]\n\t" \
        "v_mfma_f32_16x16x32_bf16 v[92:95], a[132:135], v[124:127], v[92:95]\n\t" \
        "v_mfma_f32_16x16x32_bf16 v[96:99], a[136:139], v[124:127], v[96:99]\n\t" \
        "v_mfma_f32_16x16x32_bf16 v[92:95], a[144:147], v[128:131], v[92:95]\n\t" \
        "v_mfma_f32_16x16x32_bf16 v[96:99], a[148:151], v[128:131], v[96:99]\n\t" \
        "v_mfma_f32_16x16x32_bf16 v[92:95], a[156:159], v[132:135], v[92:95]\n\t" \
        "v_mfma_f32_16x16x32_bf16 v[96:99], a[160:163], v[132:135], v[96:99]\n\t" \
        "v_mfma_f32_16x16x32_bf16 v[92:95], a[168:171], v[136:139], v[92:95]\n\t" \
        "v_mfma_f32_16x16x32_bf16 v[96:99], a[172:175], v[136:139], v[96:99]\n\t" \
        "v_cvt_pk_bf16_f32 v100, v100, v101\n\t" \
        "v_cvt_pk_bf16_f32 v101, v102, v103\n\t" \
        "v_mfma_f32_16x16x32_bf16 v[92:95], a[180:183], v[140:143], v[92:95]\n\t" \
        "ds_write_b64 v11, v[100:101] offset:10240\n\t" \
        "v_mfma_f32_16x16x32_bf16 v[96:99], a[184:187], v[140:143], v[96:99]\n\t" \
        "v_mfma_f32_16x16x32_bf16 v[92:95], a[192:195], v[144:147], v[92:95]\n\t" \
        "v_mfma_f32_16x16x32_bf16 v[96:99], a[196:199], v[144:147], v[96:99]\n\t" \
        "v_mfma_f32_16x16x32_bf16 v[92:95], a[204:207], v[116:119], v[92:95]\n\t" \
        "v_mfma_f32_16x16x32_bf16 v[96:99], a[208:211], v[116:119], v[96:99]\n\t" \
        "v_mfma_f32_16x16x32_bf16 v[92:95], a[216:219], v[104:107], v[92:95]\n\t" \
        "s_waitcnt lgkmcnt(0)\n\t" \
        "s_barrier\n\t" \
        "ds_read_b64 v[102:103], v12 offset:10240\n\t" \
        "v_mfma_f32_16x16x32_bf16 v[96:99], a[220:223], v[104:107], v[96:99]\n\t" \
        "v_mfma_f32_16x16x32_bf16 v[92:95], a[228:231], v[108:111], v[92:95]\n\t" \
        "v_mfma_f32_16x16x32_bf16 v[96:99], a[232:235], v[108:111], v[96:99]\n\t" \
        "v_mfma_f32_16x16x32_bf16 v[92:95], a[240:243], v[112:115], v[92:95]\n\t" \
        "v_mfma_f32_16x16x32_bf16 v[96:99], a[244:247], v[112:115], v[96:99]\n\t" \
        "v_mfma_f32_16x16x32_bf16 v[92:95], a[252:255], v[32:35], v[92:95]\n\t" \
        "v_mfma_f32_16x16x32_bf16 v[96:99], v[188:191], v[32:35], v[96:99]\n\t" \
        "v_mfma_f32_16x16x32_bf16 v[92:95], v[196:199], v[36:39], v[92:95]\n\t" \
        "s_waitcnt lgkmcnt(0)\n\t" \
        "v_permlane16_swap_b32 v100, v102\n\t" \
        "v_permlane16_swap_b32 v101, v103\n\t" \
        "v_mfma_f32_16x16x32_bf16 v[96:99], v[200:203], v[36:39], v[96:99]\n\t" \
        "v_mfma_f32_16x16x32_bf16 v[92:95], v[208:211], v[40:43], v[92:95]\n\t" \
        "s_add_u32 s56, s64, 0x69000\n\t" \
        "buffer_store_dwordx4 v[100:103], v10, s[28:31], s56 offen sc1\n\t" \
        "v_mfma_f32_16x16x32_bf16 v[96:99], v[212:215], v[40:43], v[96:99]\n\t" \
        "v_mfma_f32_16x16x32_bf16 v[92:95], v[220:223], v[44:47], v[92:95]\n\t" \
        "v_mfma_f32_16x16x32_bf16 v[96:99], v[224:227], v[44:47], v[96:99]\n\t" \
        "v_mfma_f32_16x16x32_bf16 v[92:95], v[232:235], v[48:51], v[92:95]\n\t" \
        "v_mfma_f32_16x16x32_bf16 v[96:99], v[236:239], v[48:51], v[96:99]\n\t" \
        "v_mfma_f32_16x16x32_bf16 v[92:95], v[244:247], v[52:55], v[92:95]\n\t" \
        "v_mfma_f32_16x16x32_bf16 v[96:99], v[248:251], v[52:55], v[96:99]\n\t" \
        "s_nop 8\n\t" \
        "v_cvt_pk_bf16_f32 v92, v92, v93\n\t" \
        "v_cvt_pk_bf16_f32 v93, v94, v95\n\t" \
        "v_cvt_pk_bf16_f32 v94, v96, v97\n\t" \
        "v_cvt_pk_bf16_f32 v95, v98, v99\n\t" \
        "s_nop 0\n\t" \
        "v_permlane16_swap_b32 v92, v94\n\t" \
        "v_permlane16_swap_b32 v93, v95\n\t" \
        "s_add_u32 s56, s64, 0x69000\n\t" \
        "buffer_store_dwordx4 v[92:95], v9, s[28:31], s56 offen sc1\n\t" \
        "s_cmp_lg_u32 s3, 0\n\t" \
        "s_cbranch_scc1 .LT_again%=\n\t" \
        "s_endpgm\n\t" \
        ".LR_start%=:\n\t" \
        "s_add_u32 s57, s40, 0x3000\n\t" \
        "s_add_u32 m0, s39, 0x3000\n\t" \
        "s_nop 0\n\t" \
        "buffer_load_dwordx4 v3, s[24:27], s57 offen offset:0  lds\n\t" \
        "v_lshrrev_b32 v38, 4, v2\n\t" \
        "s_lshr_b32 s42, s35, 1\n\t" \
        "s_and_b32 s43, s35, 1\n\t" \
        "v_and_b32 v19, 15, v2\n\t" \
        "s_lshl3_add_u32 s42, s42, s43\n\t" \
        "s_mul_i32 s37, s32, 224\n\t" \
        "v_lshl_add_u32 v18, v38, 1, s42\n\t" \
        "buffer_load_dwordx4 v4, s[24:27], s57 offen offset:1024  lds\n\t" \
        "s_add_u32 s37, s37, s33\n\t" \
        "v_mul_u32_u24 v38, 0x1f80, v18\n\t" \
        "s_mul_i32 s37, s37, 36\n\t" \
        "v_mad_u32_u24 v17, v19, 36, v38\n\t" \
        "s_and_b32 s61, s35, 1\n\t" \
        "v_lshrrev_b32 v40, 4, v2\n\t" \
        "v_and_b32 v41, 15, v2\n\t" \
        "buffer_load_dwordx4 v5, s[24:27], s57 offen offset:2048  lds\n\t" \
        "v_and_b32 v42, 16, v2\n\t" \
        "s_lshr_b32 s62, s35, 1\n\t" \
        "v_lshlrev_b32 v42, 1, v42\n\t" \
        "v_bfe_u32 v43, v2, 2, 2\n\t" \
        "v_mul_u32_u24 v45, 0xc0, v41\n\t" \
        "v_lshl_or_b32 v43, v40, 3, v43\n\t" \
        "v_and_b32 v44, 3, v2\n\t" \
        "s_add_u32 s57, s36, 0xa800\n\t" \
        "s_add_u32 m0, s38, 0x1f00\n\t" \
        "s_nop 0\n\t" \
        "buffer_load_dwordx4 v13, s[16:19], s57 offen nt lds\n\t" \
        "v_mul_u32_u24 v43, 0xc0, v43\n\t" \
        "s_lshl_b32 s63, s61, 7\n\t" \
        "v_lshl_add_u32 v43, v44, 3, v43\n\t" \
        "s_add_u32 s63, s63, 0xf840\n\t" \
        "v_add3_u32 v6, v43, v42, s63\n\t" \
        "s_lshl_b32 s63, s61, 5\n\t" \
        "v_xor_b32 v44, s63, v42\n\t" \
        "s_add_u32 s57, s36, 0x15000\n\t" \
        "s_add_u32 m0, s38, 0x2e80\n\t" \
        "s_nop 0\n\t" \
        "buffer_load_dwordx4 v13, s[16:19], s57 offen nt lds\n\t" \
        "v_xor_b32 v7, 32, v6\n\t" \
        "v_add_u32 v43, 0xf880, v43\n\t" \
        "s_lshl_b32 s63, s61, 7\n\t" \
        "v_add_u32 v8, v43, v44\n\t" \
        "v_lshrrev_b32 v43, 1, v2\n\t" \
        "v_and_b32 v43, 16, v43\n\t" \
        "v_add_u32 v45, v45, v43\n\t" \
        "s_add_u32 s57, s40, 0x6000\n\t" \
        "s_add_u32 m0, s39, 0x6000\n\t" \
        "s_nop 0\n\t" \
        "buffer_load_dwordx4 v3, s[24:27], s57 offen offset:0  lds\n\t" \
        "v_bfe_u32 v43, v2, 3, 1\n\t" \
        "v_add3_u32 v9, v45, v42, s63\n\t" \
        "v_cmp_eq_u32 vcc, s61, v43\n\t" \
        "v_add3_u32 v10, v45, v44, 64\n\t" \
        "v_bfrev_b32 v44, 1\n\t" \
        "s_lshl_b32 s63, s35, 9\n\t" \
        "v_cndmask_b32 v10, v44, v10, vcc\n\t" \
        "buffer_load_dwordx4 v4, s[24:27], s57 offen offset:1024  lds\n\t" \
        "s_add_u32 s63, s63, 0x21840\n\t" \
        "v_lshl_add_u32 v11, v2, 3, s63\n\t" \
        "s_lshl_b32 s63, s61, 10\n\t" \
        "s_sub_u32 s63, 0x200, s63\n\t" \
        "v_add_u32 v12, s63, v11\n\t" \
        "s_add_u32 s64, s32, s62\n\t" \
        "s_mul_i32 s63, s33, 0xc0\n\t" \
        "buffer_load_dwordx4 v5, s[24:27], s57 offen offset:2048  lds\n\t" \
        "s_mul_i32 s64, s64, 0xa800\n\t" \
        "s_add_u32 s64, s64, s63\n\t" \
        "v_and_b32 v38, 48, v2\n\t" \
        "v_mul_u32_u24 v37, 0xc0, v19\n\t" \
        "v_lshlrev_b32 v36, 3, v19\n\t" \
        "s_mul_i32 s63, s62, 0xf80\n\t" \
        "v_add_u32 v39, 8, v36\n\t" \
        "s_add_u32 s57, s36, 0x1f800\n\t" \
        "s_add_u32 m0, s38, 0x3e00\n\t" \
        "s_nop 0\n\t" \
        "buffer_load_dwordx4 v13, s[16:19], s57 offen nt lds\n\t" \
        "s_add_u32 s63, s63, 0x100\n\t" \
        "v_bitop3_b32 v39, v39, 32, v38 bitop3:0x6a\n\t" \
        "v_add3_u32 v14, v37, v39, s63\n\t" \
        "v_add_u32 v39, 16, v36\n\t" \
        "s_add_u32 s63, s63, 0xc0\n\t" \
        "v_bitop3_b32 v39, v39, 32, v38 bitop3:0x6a\n\t" \
        "v_add3_u32 v15, v37, v39, s63\n\t" \
        "s_add_u32 s57, s36, 0x2a000\n\t" \
        "s_add_u32 m0, s38, 0x4d80\n\t" \
        "s_nop 0\n\t" \
        "buffer_load_dwordx4 v13, s[16:19], s57 offen nt lds\n\t" \
        "v_add_u32 v39, 24, v36\n\t" \
        "s_add_u32 s63, s63, 0xc0\n\t" \
        "v_bitop3_b32 v39, v39, 32, v38 bitop3:0x6a\n\t" \
        "v_add3_u32 v16, v37, v39, s63\n\t" \
        "v_add_u32 v20, 0xf000, v6\n\t" \
        "v_add_u32 v21, 0xf000, v7\n\t" \
        "v_add_u32 v22, 0xf000, v8\n\t" \
        "s_add_u32 s57, s40, 0x9000\n\t" \
        "s_add_u32 m0, s39, 0x9000\n\t" \
        "s_nop 0\n\t" \
        "buffer_load_dwordx4 v3, s[24:27], s57 offen offset:0  lds\n\t" \
        "v_add_u32 v38, s32, v18\n\t" \
        "v_add_u32 v39, s33, v19\n\t" \
        "v_lshl_add_u32 v38, v38, 1, -3\n\t" \
        "v_lshl_add_u32 v39, v39, 1, -3\n\t" \
        "v_cvt_f32_u32 v38, v38\n\t" \
        "v_cvt_f32_u32 v39, v39\n\t" \
        "v_lshrrev_b32 v38, 16, v38\n\t" \
        "buffer_load_dwordx4 v4, s[24:27], s57 offen offset:1024  lds\n\t" \
        "v_and_b32 v39, 0xffff0000, v39\n\t" \
        "v_or_b32 v38, v38, v39\n\t" \
        "v_and_b32 v38, 0x7f807f80, v38\n\t" \
        "v_subrev_u32 v23, 0x04810481, v38\n\t" \
        "s_lshl_b32 s45, s35, 2\n\t" \
        "v_mov_b32 v24, s45\n\t" \
        "buffer_load_dwordx4 v5, s[24:27], s57 offen offset:2048  lds\n\t" \
        "s_waitcnt vmcnt(13)\n\t" \
        "s_barrier\n\t" \
        "ds_read_b128 v[116:119], v14 offset:0\n\t" \
        "ds_read_b64_tr_b16 a[0:1], v6 offset:0\n\t" \
        "ds_read_b64_tr_b16 a[2:3], v6 offset:768\n\t" \
        "ds_read_b64_tr_b16 a[4:5], v7 offset:0\n\t" \
        "ds_read_b64_tr_b16 a[6:7], v7 offset:768\n\t" \
        "ds_read_b64_tr_b16 a[8:9], v8 offset:0\n\t" \
        "ds_read_b64_tr_b16 a[10:11], v8 offset:768\n\t" \
        "ds_read_b128 v[120:123], v14 offset:64\n\t" \
        "ds_read_b64_tr_b16 a[12:13], v6 offset:6144\n\t" \
        "ds_read_b64_tr_b16 a[14:15], v6 offset:6912\n\t" \
        "ds_read_b64_tr_b16 a[16:17], v7 offset:6144\n\t" \
        "ds_read_b64_tr_b16 a[18:19], v7 offset:6912\n\t" \
        "ds_read_b64_tr_b16 a[20:21], v8 offset:6144\n\t" \
        "ds_read_b64_tr_b16 a[22:23], v8 offset:6912\n\t" \
        "ds_read_b128 v[124:127], v14 offset:128\n\t" \
        "ds_read_b128 v[128:131], v15 offset:0\n\t" \
        "s_add_u32 s57, s36, 0x34800\n\t" \
        "s_add_u32 m0, s38, 0x5d00\n\t" \
        "s_waitcnt lgkmcnt(13)\n\t" \
        "v_mfma_f32_16x16x32_bf16 v[32:35], a[0:3], v[116:119], 0\n\t" \
        "s_waitcnt vmcnt(10) lgkmcnt(2)\n\t" \
        "s_barrier\n\t" \
        "ds_read_b64_tr_b16 a[24:25], v6 offset:12288\n\t" \
        "ds_read_b64_tr_b16 a[26:27], v6 offset:13056\n\t" \
        "ds_read_b64_tr_b16 a[28:29], v7 offset:12288\n\t" \
        "ds_read_b64_tr_b16 a[30:31], v7 offset:13056\n\t" \
        "ds_read_b64_tr_b16 a[32:33], v8 offset:12288\n\t" \
        "v_mfma_f32_16x16x32_bf16 v[36:39], a[4:7], v[116:119], 0\n\t" \
        "ds_read_b64_tr_b16 a[34:35], v8 offset:13056\n\t" \
        "ds_read_b64_tr_b16 a[36:37], v6 offset:18432\n\t" \
        "v_mfma_f32_16x16x32_bf16 v[40:43], a[8:11], v[116:119], 0\n\t" \
        "ds_read_b64_tr_b16 a[38:39], v6 offset:19200\n\t" \
        "ds_read_b64_tr_b16 a[40:41], v7 offset:18432\n\t" \
        "ds_read_b64_tr_b16 a[42:43], v7 offset:19200\n\t" \
        "ds_read_b64_tr_b16 a[44:45], v8 offset:18432\n\t" \
        "ds_read_b64_tr_b16 a[46:47], v8 offset:19200\n\t" \
        "v_mfma_f32_16x16x32_bf16 v[32:35], a[12:15], v[120:123], v[32:35]\n\t" \
        "buffer_load_dwordx4 v13, s[16:19], s57 offen nt lds\n\t" \
        "s_add_u32 s57, s36, 0x3f000\n\t" \
        "s_add_u32 m0, s38, 0x6c80\n\t" \
        "v_mfma_f32_16x16x32_bf16 v[36:39], a[16:19], v[120:123], v[36:39]\n\t" \
        "ds_read_b128 v[116:119], v15 offset:64\n\t" \
        "v_mfma_f32_16x16x32_bf16 v[40:43], a[20:23], v[120:123], v[40:43]\n\t" \
        "s_waitcnt vmcnt(9) lgkmcnt(1)\n\t" \
        "s_barrier\n\t" \
        "ds_read_b128 v[132:135], v14 offset:7936\n\t" \
        "v_mfma_f32_16x16x32_bf16 v[32:35], a[24:27], v[124:127], v[32:35]\n\t" \
        "buffer_load_dwordx4 v13, s[16:19], s57 offen nt lds\n\t" \
        "v_mfma_f32_16x16x32_bf16 v[36:39], a[28:31], v[124:127], v[36:39]\n\t" \
        "ds_read_b128 v[136:139], v14 offset:8000\n\t" \
        "v_mfma_f32_16x16x32_bf16 v[40:43], a[32:35], v[124:127], v[40:43]\n\t" \
        "ds_read_b128 v[140:143], v14 offset:8064\n\t" \
        "s_add_u32 s57, s40, 0xc000\n\t" \
        "s_add_u32 m0, s39, 0xc000\n\t" \
        "v_mfma_f32_16x16x32_bf16 v[32:35], a[36:39], v[128:131], v[32:35]\n\t" \
        "ds_read_b128 v[144:147], v15 offset:7936\n\t" \
        "v_mfma_f32_16x16x32_bf16 v[36:39], a[40:43], v[128:131], v[36:39]\n\t" \
        "buffer_load_dwordx4 v3, s[24:27], s57 offen offset:0  lds\n\t" \
        "v_mfma_f32_16x16x32_bf16 v[40:43], a[44:47], v[128:131], v[40:43]\n\t" \
        "s_waitcnt vmcnt(8)\n\t" \
        "s_barrier\n\t" \
        "ds_read_b64_tr_b16 a[48:49], v6 offset:24576\n\t" \
        "s_waitcnt lgkmcnt(4)\n\t" \
        "v_mfma_f32_16x16x32_bf16 v[44:47], a[0:3], v[132:135], 0\n\t" \
        "ds_read_b64_tr_b16 a[50:51], v6 offset:25344\n\t" \
        "v_mfma_f32_16x16x32_bf16 v[48:51], a[4:7], v[132:135], 0\n\t" \
        "ds_read_b64_tr_b16 a[52:53], v7 offset:24576\n\t" \
        "v_mfma_f32_16x16x32_bf16 v[52:55], a[8:11], v[132:135], 0\n\t" \
        "ds_read_b64_tr_b16 a[54:55], v7 offset:25344\n\t" \
        "s_waitcnt lgkmcnt(6)\n\t" \
        "v_mfma_f32_16x16x32_bf16 v[44:47], a[12:15], v[136:139], v[44:47]\n\t" \
        "buffer_load_dwordx4 v4, s[24:27], s57 offen offset:1024  lds\n\t" \
        "v_mfma_f32_16x16x32_bf16 v[48:51], a[16:19], v[136:139], v[48:51]\n\t" \
        "ds_read_b64_tr_b16 a[56:57], v8 offset:24576\n\t" \
        "v_mfma_f32_16x16x32_bf16 v[52:55], a[20:23], v[136:139], v[52:55]\n\t" \
        "ds_read_b64_tr_b16 a[58:59], v8 offset:25344\n\t" \
        "s_waitcnt lgkmcnt(6)\n\t" \
        "v_mfma_f32_16x16x32_bf16 v[44:47], a[24:27], v[140:143], v[44:47]\n\t" \
        "ds_read_b128 v[120:123], v15 offset:8000\n\t" \
        "v_mfma_f32_16x16x32_bf16 v[48:51], a[28:31], v[140:143], v[48:51]\n\t" \
        "ds_read_b128 v[124:127], v15 offset:128\n\t" \
        "v_mfma_f32_16x16x32_bf16 v[52:55], a[32:35], v[140:143], v[52:55]\n\t" \
        "ds_read_b64_tr_b16 a[60:61], v6 offset:30720\n\t" \
        "ds_read_b64_tr_b16 a[62:63], v6 offset:31488\n\t" \
        "ds_read_b64_tr_b16 a[64:65], v7 offset:30720\n\t" \
        "ds_read_b64_tr_b16 a[66:67], v7 offset:31488\n\t" \
        "ds_read_b64_tr_b16 a[68:69], v8 offset:30720\n\t" \
        "ds_read_b64_tr_b16 a[70:71], v8 offset:31488\n\t" \
        "v_mfma_f32_16x16x32_bf16 v[44:47], a[36:39], v[144:147], v[44:47]\n\t" \
        "buffer_load_dwordx4 v5, s[24:27], s57 offen offset:2048  lds\n\t" \
        "v_mfma_f32_16x16x32_bf16 v[48:51], a[40:43], v[144:147], v[48:51]\n\t" \
        "ds_read_b128 v[128:131], v15 offset:8064\n\t" \
        "v_mfma_f32_16x16x32_bf16 v[52:55], a[44:47], v[144:147], v[52:55]\n\t" \
        "s_waitcnt vmcnt(8) lgkmcnt(1)\n\t" \
        "s_barrier\n\t" \
        "ds_read_b128 v[132:135], v14 offset:15872\n\t" \
        "s_add_u32 s57, s40, 0xf000\n\t" \
        "s_add_u32 m0, s39, 0xf000\n\t" \
        "v_mfma_f32_16x16x32_bf16 v[32:35], a[48:51], v[116:119], v[32:35]\n\t" \
        "ds_read_b128 v[136:139], v14 offset:15936\n\t" \
        "v_mfma_f32_16x16x32_bf16 v[36:39], a[52:55], v[116:119], v[36:39]\n\t" \
        "buffer_load_dwordx4 v3, s[24:27], s57 offen offset:0  lds\n\t" \
        "v_mfma_f32_16x16x32_bf16 v[40:43], a[56:59], v[116:119], v[40:43]\n\t" \
        "ds_read_b128 v[140:143], v14 offset:16000\n\t" \
        "v_mfma_f32_16x16x32_bf16 v[44:47], a[48:51], v[120:123], v[44:47]\n\t" \
        "ds_read_b128 v[144:147], v15 offset:15872\n\t" \
        "v_mfma_f32_16x16x32_bf16 v[48:51], a[52:55], v[120:123], v[48:51]\n\t" \
        "ds_read_b128 v[116:119], v15 offset:15936\n\t" \
        "v_mfma_f32_16x16x32_bf16 v[52:55], a[56:59], v[120:123], v[52:55]\n\t" \
        "ds_read_b128 v[120:123], v15 offset:16000\n\t" \
        "v_mfma_f32_16x16x32_bf16 v[32:35], a[60:63], v[124:127], v[32:35]\n\t" \
        "buffer_load_dwordx4 v4, s[24:27], s57 offen offset:1024  lds\n\t" \
        "v_mfma_f32_16x16x32_bf16 v[36:39], a[64:67], v[124:127], v[36:39]\n\t" \
        "v_mfma_f32_16x16x32_bf16 v[40:43], a[68:71], v[124:127], v[40:43]\n\t" \
        "ds_read_b128 v[124:127], v16 offset:0\n\t" \
        "s_waitcnt lgkmcnt(5)\n\t" \
        "v_mfma_f32_16x16x32_bf16 v[44:47], a[60:63], v[128:131], v[44:47]\n\t" \
        "v_mfma_f32_16x16x32_bf16 v[48:51], a[64:67], v[128:131], v[48:51]\n\t" \
        "v_mfma_f32_16x16x32_bf16 v[52:55], a[68:71], v[128:131], v[52:55]\n\t" \
        "s_waitcnt vmcnt(7)\n\t" \
        "s_barrier\n\t" \
        "buffer_load_dwordx4 v5, s[24:27], s57 offen offset:2048  lds\n\t" \
        "v_mfma_f32_16x16x32_bf16 v[56:59], a[0:3], v[132:135], 0\n\t" \
        "ds_read_b64_tr_b16 a[72:73], v6 offset:36864\n\t" \
        "v_mfma_f32_16x16x32_bf16 v[60:63], a[4:7], v[132:135], 0\n\t" \
        "ds_read_b64_tr_b16 a[74:75], v6 offset:37632\n\t" \
        "v_mfma_f32_16x16x32_bf16 v[64:67], a[8:11], v[132:135], 0\n\t" \
        "ds_read_b64_tr_b16 a[76:77], v7 offset:36864\n\t" \
        "s_add_u32 s57, s40, 0x12000\n\t" \
        "s_add_u32 m0, s39, 0x12000\n\t" \
        "v_mfma_f32_16x16x32_bf16 v[56:59], a[12:15], v[136:139], v[56:59]\n\t" \
        "ds_read_b64_tr_b16 a[78:79], v7 offset:37632\n\t" \
        "v_mfma_f32_16x16x32_bf16 v[60:63], a[16:19], v[136:139], v[60:63]\n\t" \
        "buffer_load_dwordx4 v3, s[24:27], s57 offen offset:0  lds\n\t" \
        "v_mfma_f32_16x16x32_bf16 v[64:67], a[20:23], v[136:139], v[64:67]\n\t" \
        "ds_read_b64_tr_b16 a[80:81], v8 offset:36864\n\t" \
        "s_waitcnt lgkmcnt(5)\n\t" \
        "v_mfma_f32_16x16x32_bf16 v[56:59], a[24:27], v[140:143], v[56:59]\n\t" \
        "ds_read_b64_tr_b16 a[82:83], v8 offset:37632\n\t" \
        "v_mfma_f32_16x16x32_bf16 v[60:63], a[28:31], v[140:143], v[60:63]\n\t" \
        "ds_read_b128 v[128:131], v16 offset:7936\n\t" \
        "v_mfma_f32_16x16x32_bf16 v[64:67], a[32:35], v[140:143], v[64:67]\n\t" \
        "ds_read_b128 v[132:135], v16 offset:15872\n\t" \
        "v_mfma_f32_16x16x32_bf16 v[56:59], a[36:39], v[144:147], v[56:59]\n\t" \
        "buffer_load_dwordx4 v4, s[24:27], s57 offen offset:1024  lds\n\t" \
        "v_mfma_f32_16x16x32_bf16 v[60:63], a[40:43], v[144:147], v[60:63]\n\t" \
        "ds_read_b128 v[136:139], v16 offset:64\n\t" \
        "v_mfma_f32_16x16x32_bf16 v[64:67], a[44:47], v[144:147], v[64:67]\n\t" \
        "ds_read_b64_tr_b16 a[84:85], v6 offset:43008\n\t" \
        "v_mfma_f32_16x16x32_bf16 v[56:59], a[48:51], v[116:119], v[56:59]\n\t" \
        "ds_read_b64_tr_b16 a[86:87], v6 offset:43776\n\t" \
        "v_mfma_f32_16x16x32_bf16 v[60:63], a[52:55], v[116:119], v[60:63]\n\t" \
        "ds_read_b64_tr_b16 a[88:89], v7 offset:43008\n\t" \
        "v_mfma_f32_16x16x32_bf16 v[64:67], a[56:59], v[116:119], v[64:67]\n\t" \
        "ds_read_b64_tr_b16 a[90:91], v7 offset:43776\n\t" \
        "ds_read_b64_tr_b16 a[92:93], v8 offset:43008\n\t" \
        "ds_read_b64_tr_b16 a[94:95], v8 offset:43776\n\t" \
        "v_mfma_f32_16x16x32_bf16 v[56:59], a[60:63], v[120:123], v[56:59]\n\t" \
        "buffer_load_dwordx4 v5, s[24:27], s57 offen offset:2048  lds\n\t" \
        "v_mfma_f32_16x16x32_bf16 v[60:63], a[64:67], v[120:123], v[60:63]\n\t" \
        "ds_read_b128 v[140:143], v16 offset:8000\n\t" \
        "v_mfma_f32_16x16x32_bf16 v[64:67], a[68:71], v[120:123], v[64:67]\n\t" \
        "s_waitcnt vmcnt(9) lgkmcnt(1)\n\t" \
        "s_barrier\n\t" \
        "ds_read_b128 v[144:147], v16 offset:15936\n\t" \
        "s_add_u32 s57, s36, 0x49800\n\t" \
        "s_add_u32 m0, s38, 0x7c00\n\t" \
        "v_mfma_f32_16x16x32_bf16 v[32:35], a[72:75], v[124:127], v[32:35]\n\t" \
        "ds_read_b128 v[116:119], v14 offset:23808\n\t" \
        "v_mfma_f32_16x16x32_bf16 v[36:39], a[76:79], v[124:127], v[36:39]\n\t" \
        "buffer_load_dwordx4 v13, s[16:19], s57 offen nt lds\n\t" \
        "v_mfma_f32_16x16x32_bf16 v[40:43], a[80:83], v[124:127], v[40:43]\n\t" \
        "ds_read_b128 v[120:123], v14 offset:23872\n\t" \
        "v_mfma_f32_16x16x32_bf16 v[44:47], a[72:75], v[128:131], v[44:47]\n\t" \
        "ds_read_b128 v[124:127], v14 offset:23936\n\t" \
        "v_mfma_f32_16x16x32_bf16 v[48:51], a[76:79], v[128:131], v[48:51]\n\t" \
        "s_add_u32 s57, s40, 0x15000\n\t" \
        "s_add_u32 m0, s39, 0x15000\n\t" \
        "v_mfma_f32_16x16x32_bf16 v[52:55], a[80:83], v[128:131], v[52:55]\n\t" \
        "ds_read_b128 v[128:131], v15 offset:23808\n\t" \
        "v_mfma_f32_16x16x32_bf16 v[56:59], a[72:75], v[132:135], v[56:59]\n\t" \
        "buffer_load_dwordx4 v3, s[24:27], s57 offen offset:0  lds\n\t" \
        "v_mfma_f32_16x16x32_bf16 v[60:63], a[76:79], v[132:135], v[60:63]\n\t" \
        "v_mfma_f32_16x16x32_bf16 v[64:67], a[80:83], v[132:135], v[64:67]\n\t" \
        "ds_read_b128 v[132:135], v15 offset:23872\n\t" \
        "v_mfma_f32_16x16x32_bf16 v[32:35], a[84:87], v[136:139], v[32:35]\n\t" \
        "v_mfma_f32_16x16x32_bf16 v[36:39], a[88:91], v[136:139], v[36:39]\n\t" \
        "v_mfma_f32_16x16x32_bf16 v[40:43], a[92:95], v[136:139], v[40:43]\n\t" \
        "ds_read_b128 v[136:139], v15 offset:23936\n\t" \
        "s_waitcnt lgkmcnt(3)\n\t" \
        "v_mfma_f32_16x16x32_bf16 v[44:47], a[84:87], v[140:143], v[44:47]\n\t" \
        "buffer_load_dwordx4 v4, s[24:27], s57 offen offset:1024  lds\n\t" \
        "v_mfma_f32_16x16x32_bf16 v[48:51], a[88:91], v[140:143], v[48:51]\n\t" \
        "v_mfma_f32_16x16x32_bf16 v[52:55], a[92:95], v[140:143], v[52:55]\n\t" \
        "ds_read_b128 v[140:143], v16 offset:23808\n\t" \
        "v_mfma_f32_16x16x32_bf16 v[56:59], a[84:87], v[144:147], v[56:59]\n\t" \
        "v_mfma_f32_16x16x32_bf16 v[60:63], a[88:91], v[144:147], v[60:63]\n\t" \
        "v_mfma_f32_16x16x32_bf16 v[64:67], a[92:95], v[144:147], v[64:67]\n\t" \
        "ds_read_b128 v[144:147], v16 offset:23872\n\t" \
        "v_mfma_f32_16x16x32_bf16 v[68:71], a[0:3], v[116:119], 0\n\t" \
        "buffer_load_dwordx4 v5, s[24:27], s57 offen offset:2048  lds\n\t" \
        "v_mfma_f32_16x16x32_bf16 v[72:75], a[4:7], v[116:119], 0\n\t" \
        "v_mfma_f32_16x16x32_bf16 v[76:79], a[8:11], v[116:119], 0\n\t" \
        "ds_read_b128 v[116:119], v16 offset:128\n\t" \
        "v_mfma_f32_16x16x32_bf16 v[68:71], a[12:15], v[120:123], v[68:71]\n\t" \
        "v_mfma_f32_16x16x32_bf16 v[72:75], a[16:19], v[120:123], v[72:75]\n\t" \
        "s_add_u32 s57, s36, 0x54000\n\t" \
        "s_add_u32 m0, s38, 0x8b80\n\t" \
        "v_mfma_f32_16x16x32_bf16 v[76:79], a[20:23], v[120:123], v[76:79]\n\t" \
        "s_waitcnt vmcnt(10)\n\t" \
        "s_barrier\n\t" \
        "ds_read_b64_tr_b16 a[96:97], v6 offset:49152\n\t" \
        "v_mfma_f32_16x16x32_bf16 v[68:71], a[24:27], v[124:127], v[68:71]\n\t" \
        "buffer_load_dwordx4 v13, s[16:19], s57 offen nt lds\n\t" \
        "v_mfma_f32_16x16x32_bf16 v[72:75], a[28:31], v[124:127], v[72:75]\n\t" \
        "ds_read_b64_tr_b16 a[98:99], v6 offset:49920\n\t" \
        "v_mfma_f32_16x16x32_bf16 v[76:79], a[32:35], v[124:127], v[76:79]\n\t" \
        "ds_read_b64_tr_b16 a[100:101], v7 offset:49152\n\t" \
        "s_waitcnt lgkmcnt(4)\n\t" \
        "v_mfma_f32_16x16x32_bf16 v[68:71], a[36:39], v[128:131], v[68:71]\n\t" \
        "ds_read_b64_tr_b16 a[102:103], v7 offset:49920\n\t" \
        "v_mfma_f32_16x16x32_bf16 v[72:75], a[40:43], v[128:131], v[72:75]\n\t" \
        "ds_read_b64_tr_b16 a[104:105], v8 offset:49152\n\t" \
        "s_add_u32 s57, s40, 0x18000\n\t" \
        "s_mov_b32 m0, s39\n\t" \
        "v_mfma_f32_16x16x32_bf16 v[76:79], a[44:47], v[128:131], v[76:79]\n\t" \
        "ds_read_b64_tr_b16 a[106:107], v8 offset:49920\n\t" \
        "v_mfma_f32_16x16x32_bf16 v[68:71], a[48:51], v[132:135], v[68:71]\n\t" \
        "buffer_load_dwordx4 v3, s[24:27], s57 offen offset:0  lds\n\t" \
        "v_mfma_f32_16x16x32_bf16 v[72:75], a[52:55], v[132:135], v[72:75]\n\t" \
        "ds_read_b128 v[120:123], v16 offset:8064\n\t" \
        "v_mfma_f32_16x16x32_bf16 v[76:79], a[56:59], v[132:135], v[76:79]\n\t" \
        "ds_read_b128 v[124:127], v16 offset:16000\n\t" \
        "v_mfma_f32_16x16x32_bf16 v[68:71], a[60:63], v[136:139], v[68:71]\n\t" \
        "ds_read_b128 v[128:131], v16 offset:23936\n\t" \
        "v_mfma_f32_16x16x32_bf16 v[72:75], a[64:67], v[136:139], v[72:75]\n\t" \
        "ds_read_b128 v[132:135], v14 offset:3968\n\t" \
        "v_mfma_f32_16x16x32_bf16 v[76:79], a[68:71], v[136:139], v[76:79]\n\t" \
        "ds_read_b64_tr_b16 a[108:109], v6 offset:55296\n\t" \
        "v_mfma_f32_16x16x32_bf16 v[68:71], a[72:75], v[140:143], v[68:71]\n\t" \
        "buffer_load_dwordx4 v4, s[24:27], s57 offen offset:1024  lds\n\t" \
        "v_mfma_f32_16x16x32_bf16 v[72:75], a[76:79], v[140:143], v[72:75]\n\t" \
        "ds_read_b64_tr_b16 a[110:111], v6 offset:56064\n\t" \
        "v_mfma_f32_16x16x32_bf16 v[76:79], a[80:83], v[140:143], v[76:79]\n\t" \
        "ds_read_b64_tr_b16 a[112:113], v7 offset:55296\n\t" \
        "v_mfma_f32_16x16x32_bf16 v[68:71], a[84:87], v[144:147], v[68:71]\n\t" \
        "ds_read_b64_tr_b16 a[114:115], v7 offset:56064\n\t" \
        "v_mfma_f32_16x16x32_bf16 v[72:75], a[88:91], v[144:147], v[72:75]\n\t" \
        "ds_read_b64_tr_b16 a[116:117], v8 offset:55296\n\t" \
        "v_mfma_f32_16x16x32_bf16 v[76:79], a[92:95], v[144:147], v[76:79]\n\t" \
        "ds_read_b64_tr_b16 a[118:119], v8 offset:56064\n\t" \
        "s_waitcnt lgkmcnt(7)\n\t" \
        "v_mfma_f32_16x16x32_bf16 v[32:35], a[96:99], v[116:119], v[32:35]\n\t" \
        "buffer_load_dwordx4 v5, s[24:27], s57 offen offset:2048  lds\n\t" \
        "v_mfma_f32_16x16x32_bf16 v[36:39], a[100:103], v[116:119], v[36:39]\n\t" \
        "ds_read_b128 v[136:139], v14 offset:11904\n\t" \
        "v_mfma_f32_16x16x32_bf16 v[40:43], a[104:107], v[116:119], v[40:43]\n\t" \
        "s_waitcnt vmcnt(11) lgkmcnt(1)\n\t" \
        "s_barrier\n\t" \
        "ds_read_b128 v[140:143], v14 offset:19840\n\t" \
        "v_mfma_f32_16x16x32_bf16 v[44:47], a[96:99], v[120:123], v[44:47]\n\t" \
        "ds_read_b128 v[144:147], v14 offset:4032\n\t" \
        "v_mfma_f32_16x16x32_bf16 v[48:51], a[100:103], v[120:123], v[48:51]\n\t" \
        "ds_read_b64_tr_b16 a[120:121], v20 offset:0\n\t" \
        "s_add_u32 s57, s40, 0x1b000\n\t" \
        "s_add_u32 m0, s39, 0x3000\n\t" \
        "v_mfma_f32_16x16x32_bf16 v[52:55], a[104:107], v[120:123], v[52:55]\n\t" \
        "ds_read_b64_tr_b16 a[122:123], v20 offset:768\n\t" \
        "v_mfma_f32_16x16x32_bf16 v[56:59], a[96:99], v[124:127], v[56:59]\n\t" \
        "buffer_load_dwordx4 v3, s[24:27], s57 offen offset:0  lds\n\t" \
        "v_mfma_f32_16x16x32_bf16 v[60:63], a[100:103], v[124:127], v[60:63]\n\t" \
        "ds_read_b64_tr_b16 a[124:125], v21 offset:0\n\t" \
        "v_mfma_f32_16x16x32_bf16 v[64:67], a[104:107], v[124:127], v[64:67]\n\t" \
        "ds_read_b64_tr_b16 a[126:127], v21 offset:768\n\t" \
        "v_mfma_f32_16x16x32_bf16 v[68:71], a[96:99], v[128:131], v[68:71]\n\t" \
        "ds_read_b64_tr_b16 a[128:129], v22 offset:0\n\t" \
        "v_mfma_f32_16x16x32_bf16 v[72:75], a[100:103], v[128:131], v[72:75]\n\t" \
        "ds_read_b64_tr_b16 a[130:131], v22 offset:768\n\t" \
        "v_mfma_f32_16x16x32_bf16 v[76:79], a[104:107], v[128:131], v[76:79]\n\t" \
        "ds_read_b128 v[116:119], v14 offset:11968\n\t" \
        "v_mfma_f32_16x16x32_bf16 v[32:35], a[108:111], v[132:135], v[32:35]\n\t" \
        "buffer_load_dwordx4 v4, s[24:27], s57 offen offset:1024  lds\n\t" \
        "v_mfma_f32_16x16x32_bf16 v[36:39], a[112:115], v[132:135], v[36:39]\n\t" \
        "ds_read_b128 v[120:123], v14 offset:19904\n\t" \
        "v_mfma_f32_16x16x32_bf16 v[40:43], a[116:119], v[132:135], v[40:43]\n\t" \
        "ds_read_b128 v[124:127], v14 offset:4096\n\t" \
        "s_waitcnt lgkmcnt(7)\n\t" \
        "v_mfma_f32_16x16x32_bf16 v[44:47], a[108:111], v[136:139], v[44:47]\n\t" \
        "ds_read_b64_tr_b16 a[132:133], v20 offset:6144\n\t" \
        "v_mfma_f32_16x16x32_bf16 v[48:51], a[112:115], v[136:139], v[48:51]\n\t" \
        "ds_read_b64_tr_b16 a[134:135], v20 offset:6912\n\t" \
        "v_mfma_f32_16x16x32_bf16 v[52:55], a[116:119], v[136:139], v[52:55]\n\t" \
        "ds_read_b64_tr_b16 a[136:137], v21 offset:6144\n\t" \
        "ds_read_b64_tr_b16 a[138:139], v21 offset:6912\n\t" \
        "ds_read_b64_tr_b16 a[140:141], v22 offset:6144\n\t" \
        "ds_read_b64_tr_b16 a[142:143], v22 offset:6912\n\t" \
        "v_mfma_f32_16x16x32_bf16 v[56:59], a[108:111], v[140:143], v[56:59]\n\t" \
        "buffer_load_dwordx4 v5, s[24:27], s57 offen offset:2048  lds\n\t" \
        "v_mfma_f32_16x16x32_bf16 v[60:63], a[112:115], v[140:143], v[60:63]\n\t" \
        "ds_read_b128 v[128:131], v14 offset:12032\n\t" \
        "v_mfma_f32_16x16x32_bf16 v[64:67], a[116:119], v[140:143], v[64:67]\n\t" \
        "s_waitcnt vmcnt(11) lgkmcnt(1)\n\t" \
        "s_barrier\n\t" \
        "ds_read_b128 v[132:135], v14 offset:19968\n\t" \
        "v_mfma_f32_16x16x32_bf16 v[32:35], a[120:123], v[144:147], v[32:35]\n\t" \
        "ds_read_b128 v[136:139], v15 offset:3968\n\t" \
        "v_mfma_f32_16x16x32_bf16 v[36:39], a[124:127], v[144:147], v[36:39]\n\t" \
        "ds_read_b64_tr_b16 a[144:145], v20 offset:12288\n\t" \
        "s_add_u32 s57, s40, 0x1e000\n\t" \
        "s_add_u32 m0, s39, 0x6000\n\t" \
        "v_mfma_f32_16x16x32_bf16 v[40:43], a[128:131], v[144:147], v[40:43]\n\t" \
        "ds_read_b64_tr_b16 a[146:147], v20 offset:13056\n\t" \
        "v_mfma_f32_16x16x32_bf16 v[44:47], a[120:123], v[116:119], v[44:47]\n\t" \
        "buffer_load_dwordx4 v3, s[24:27], s57 offen offset:0  lds\n\t" \
        "v_mfma_f32_16x16x32_bf16 v[48:51], a[124:127], v[116:119], v[48:51]\n\t" \
        "ds_read_b64_tr_b16 a[148:149], v21 offset:12288\n\t" \
        "v_mfma_f32_16x16x32_bf16 v[52:55], a[128:131], v[116:119], v[52:55]\n\t" \
        "ds_read_b64_tr_b16 a[150:151], v21 offset:13056\n\t" \
        "v_mfma_f32_16x16x32_bf16 v[56:59], a[120:123], v[120:123], v[56:59]\n\t" \
        "ds_read_b64_tr_b16 a[152:153], v22 offset:12288\n\t" \
        "v_mfma_f32_16x16x32_bf16 v[60:63], a[124:127], v[120:123], v[60:63]\n\t" \
        "ds_read_b64_tr_b16 a[154:155], v22 offset:13056\n\t" \
        "v_mfma_f32_16x16x32_bf16 v[64:67], a[128:131], v[120:123], v[64:67]\n\t" \
        "ds_read_b128 v[140:143], v15 offset:11904\n\t" \
        "v_mfma_f32_16x16x32_bf16 v[32:35], a[132:135], v[124:127], v[32:35]\n\t" \
        "buffer_load_dwordx4 v4, s[24:27], s57 offen offset:1024  lds\n\t" \
        "v_mfma_f32_16x16x32_bf16 v[36:39], a[136:139], v[124:127], v[36:39]\n\t" \
        "ds_read_b128 v[144:147], v15 offset:19840\n\t" \
        "v_mfma_f32_16x16x32_bf16 v[40:43], a[140:143], v[124:127], v[40:43]\n\t" \
        "ds_read_b128 v[116:119], v15 offset:4032\n\t" \
        "s_waitcnt lgkmcnt(7)\n\t" \
        "v_mfma_f32_16x16x32_bf16 v[44:47], a[132:135], v[128:131], v[44:47]\n\t" \
        "ds_read_b64_tr_b16 a[156:157], v20 offset:18432\n\t" \
        "v_mfma_f32_16x16x32_bf16 v[48:51], a[136:139], v[128:131], v[48:51]\n\t" \
        "ds_read_b64_tr_b16 a[158:159], v20 offset:19200\n\t" \
        "v_mfma_f32_16x16x32_bf16 v[52:55], a[140:143], v[128:131], v[52:55]\n\t" \
        "ds_read_b64_tr_b16 a[160:161], v21 offset:18432\n\t" \
        "ds_read_b64_tr_b16 a[162:163], v21 offset:19200\n\t" \
        "ds_read_b64_tr_b16 a[164:165], v22 offset:18432\n\t" \
        "ds_read_b64_tr_b16 a[166:167], v22 offset:19200\n\t" \
        "v_mfma_f32_16x16x32_bf16 v[56:59], a[132:135], v[132:135], v[56:59]\n\t" \
        "buffer_load_dwordx4 v5, s[24:27], s57 offen offset:2048  lds\n\t" \
        "v_mfma_f32_16x16x32_bf16 v[60:63], a[136:139], v[132:135], v[60:63]\n\t" \
        "ds_read_b128 v[120:123], v15 offset:11968\n\t" \
        "v_mfma_f32_16x16x32_bf16 v[64:67], a[140:143], v[132:135], v[64:67]\n\t" \
        "s_waitcnt vmcnt(10)\n\t" \
        "s_barrier\n\t" \
        "ds_read_b128 v[124:127], v15 offset:19904\n\t" \
        "v_mfma_f32_16x16x32_bf16 v[32:35], a[144:147], v[136:139], v[32:35]\n\t" \
        "ds_read_b128 v[128:131], v14 offset:27776\n\t" \
        "s_waitcnt lgkmcnt(10)\n\t" \
        "v_mfma_f32_16x16x32_bf16 v[36:39], a[148:151], v[136:139], v[36:39]\n\t" \
        "ds_read_b128 v[132:135], v14 offset:27840\n\t" \
        "s_add_u32 s57, s40, 0x21000\n\t" \
        "s_add_u32 m0, s39, 0x9000\n\t" \
        "v_mfma_f32_16x16x32_bf16 v[40:43], a[152:155], v[136:139], v[40:43]\n\t" \
        "ds_read_b128 v[136:139], v14 offset:27904\n\t" \
        "v_mfma_f32_16x16x32_bf16 v[44:47], a[144:147], v[140:143], v[44:47]\n\t" \
        "buffer_load_dwordx4 v3, s[24:27], s57 offen offset:0  lds\n\t" \
        "v_mfma_f32_16x16x32_bf16 v[48:51], a[148:151], v[140:143], v[48:51]\n\t" \
        "ds_read_b64_tr_b16 a[168:169], v20 offset:24576\n\t" \
        "v_mfma_f32_16x16x32_bf16 v[52:55], a[152:155], v[140:143], v[52:55]\n\t" \
        "ds_read_b128 v[140:143], v15 offset:27776\n\t" \
        "v_mfma_f32_16x16x32_bf16 v[56:59], a[144:147], v[144:147], v[56:59]\n\t" \
        "ds_read_b64_tr_b16 a[170:171], v20 offset:25344\n\t" \
        "v_mfma_f32_16x16x32_bf16 v[60:63], a[148:151], v[144:147], v[60:63]\n\t" \
        "ds_read_b64_tr_b16 a[172:173], v21 offset:24576\n\t" \
        "v_mfma_f32_16x16x32_bf16 v[64:67], a[152:155], v[144:147], v[64:67]\n\t" \
        "ds_read_b128 v[144:147], v15 offset:27840\n\t" \
        "s_waitcnt lgkmcnt(7)\n\t" \
        "v_mfma_f32_16x16x32_bf16 v[32:35], a[156:159], v[116:119], v[32:35]\n\t" \
        "buffer_load_dwordx4 v4, s[24:27], s57 offen offset:1024  lds\n\t" \
        "v_mfma_f32_16x16x32_bf16 v[36:39], a[160:163], v[116:119], v[36:39]\n\t" \
        "ds_read_b64_tr_b16 a[174:175], v21 offset:25344\n\t" \
        "v_mfma_f32_16x16x32_bf16 v[40:43], a[164:167], v[116:119], v[40:43]\n\t" \
        "ds_read_b128 v[116:119], v15 offset:4096\n\t" \
        "v_mfma_f32_16x16x32_bf16 v[44:47], a[156:159], v[120:123], v[44:47]\n\t" \
        "ds_read_b64_tr_b16 a[176:177], v22 offset:24576\n\t" \
        "v_mfma_f32_16x16x32_bf16 v[48:51], a[160:163], v[120:123], v[48:51]\n\t" \
        "ds_read_b64_tr_b16 a[178:179], v22 offset:25344\n\t" \
        "v_mfma_f32_16x16x32_bf16 v[52:55], a[164:167], v[120:123], v[52:55]\n\t" \
        "ds_read_b128 v[120:123], v15 offset:12032\n\t" \
        "v_mfma_f32_16x16x32_bf16 v[56:59], a[156:159], v[124:127], v[56:59]\n\t" \
        "buffer_load_dwordx4 v5, s[24:27], s57 offen offset:2048  lds\n\t" \
        "v_mfma_f32_16x16x32_bf16 v[60:63], a[160:163], v[124:127], v[60:63]\n\t" \
        "ds_read_b64_tr_b16 a[180:181], v20 offset:30720\n\t" \
        "v_mfma_f32_16x16x32_bf16 v[64:67], a[164:167], v[124:127], v[64:67]\n\t" \
        "ds_read_b128 v[124:127], v15 offset:19968\n\t" \
        "v_mfma_f32_16x16x32_bf16 v[68:71], a[108:111], v[128:131], v[68:71]\n\t" \
        "ds_read_b64_tr_b16 a[182:183], v20 offset:31488\n\t" \
        "v_mfma_f32_16x16x32_bf16 v[72:75], a[112:115], v[128:131], v[72:75]\n\t" \
        "ds_read_b64_tr_b16 a[184:185], v21 offset:30720\n\t" \
        "s_add_u32 s57, s40, 0x24000\n\t" \
        "s_add_u32 m0, s39, 0xc000\n\t" \
        "v_mfma_f32_16x16x32_bf16 v[76:79], a[116:119], v[128:131], v[76:79]\n\t" \
        "ds_read_b128 v[128:131], v15 offset:27904\n\t" \
        "s_waitcnt lgkmcnt(7)\n\t" \
        "v_mfma_f32_16x16x32_bf16 v[68:71], a[120:123], v[132:135], v[68:71]\n\t" \
        "buffer_load_dwordx4 v3, s[24:27], s57 offen offset:0  lds\n\t" \
        "v_mfma_f32_16x16x32_bf16 v[72:75], a[124:127], v[132:135], v[72:75]\n\t" \
        "ds_read_b64_tr_b16 a[186:187], v21 offset:31488\n\t" \
        "v_mfma_f32_16x16x32_bf16 v[76:79], a[128:131], v[132:135], v[76:79]\n\t" \
        "ds_read_b128 v[132:135], v16 offset:3968\n\t" \
        "v_mfma_f32_16x16x32_bf16 v[68:71], a[132:135], v[136:139], v[68:71]\n\t" \
        "ds_read_b64_tr_b16 a[188:189], v22 offset:30720\n\t" \
        "v_mfma_f32_16x16x32_bf16 v[72:75], a[136:139], v[136:139], v[72:75]\n\t" \
        "ds_read_b64_tr_b16 a[190:191], v22 offset:31488\n\t" \
        "v_mfma_f32_16x16x32_bf16 v[76:79], a[140:143], v[136:139], v[76:79]\n\t" \
        "ds_read_b128 v[136:139], v16 offset:11904\n\t" \
        "v_mfma_f32_16x16x32_bf16 v[68:71], a[144:147], v[140:143], v[68:71]\n\t" \
        "buffer_load_dwordx4 v4, s[24:27], s57 offen offset:1024  lds\n\t" \
        "v_mfma_f32_16x16x32_bf16 v[72:75], a[148:151], v[140:143], v[72:75]\n\t" \
        "v_mfma_f32_16x16x32_bf16 v[76:79], a[152:155], v[140:143], v[76:79]\n\t" \
        "ds_read_b128 v[140:143], v16 offset:19840\n\t" \
        "v_mfma_f32_16x16x32_bf16 v[68:71], a[156:159], v[144:147], v[68:71]\n\t" \
        "v_mfma_f32_16x16x32_bf16 v[72:75], a[160:163], v[144:147], v[72:75]\n\t" \
        "v_mfma_f32_16x16x32_bf16 v[76:79], a[164:167], v[144:147], v[76:79]\n\t" \
        "ds_read_b128 v[144:147], v16 offset:27776\n\t" \
        "v_mfma_f32_16x16x32_bf16 v[32:35], a[168:171], v[116:119], v[32:35]\n\t" \
        "buffer_load_dwordx4 v5, s[24:27], s57 offen offset:2048  lds\n\t" \
        "v_mfma_f32_16x16x32_bf16 v[36:39], a[172:175], v[116:119], v[36:39]\n\t" \
        "s_waitcnt lgkmcnt(2)\n\t" \
        "v_mfma_f32_16x16x32_bf16 v[40:43], a[176:179], v[116:119], v[40:43]\n\t" \
        "ds_read_b128 v[116:119], v16 offset:4032\n\t" \
        "v_mfma_f32_16x16x32_bf16 v[44:47], a[168:171], v[120:123], v[44:47]\n\t" \
        "v_mfma_f32_16x16x32_bf16 v[48:51], a[172:175], v[120:123], v[48:51]\n\t" \
        "s_add_u32 s57, s40, 0x27000\n\t" \
        "s_add_u32 m0, s39, 0xf000\n\t" \
        "v_mfma_f32_16x16x32_bf16 v[52:55], a[176:179], v[120:123], v[52:55]\n\t" \
        "s_waitcnt vmcnt(9)\n\t" \
        "s_barrier\n\t" \
        "ds_read_b64_tr_b16 a[192:193], v6 offset:0\n\t" \
        "v_mfma_f32_16x16x32_bf16 v[56:59], a[168:171], v[124:127], v[56:59]\n\t" \
        "buffer_load_dwordx4 v3, s[24:27], s57 offen offset:0  lds\n\t" \
        "v_mfma_f32_16x16x32_bf16 v[60:63], a[172:175], v[124:127], v[60:63]\n\t" \
        "ds_read_b64_tr_b16 a[194:195], v6 offset:768\n\t" \
        "v_mfma_f32_16x16x32_bf16 v[64:67], a[176:179], v[124:127], v[64:67]\n\t" \
        "ds_read_b64_tr_b16 a[196:197], v7 offset:0\n\t" \
        "v_mfma_f32_16x16x32_bf16 v[68:71], a[168:171], v[128:131], v[68:71]\n\t" \
        "ds_read_b64_tr_b16 a[198:199], v7 offset:768\n\t" \
        "v_mfma_f32_16x16x32_bf16 v[72:75], a[172:175], v[128:131], v[72:75]\n\t" \
        "ds_read_b64_tr_b16 a[200:201], v8 offset:0\n\t" \
        "v_mfma_f32_16x16x32_bf16 v[76:79], a[176:179], v[128:131], v[76:79]\n\t" \
        "ds_read_b64_tr_b16 a[202:203], v8 offset:768\n\t" \
        "v_mfma_f32_16x16x32_bf16 v[32:35], a[180:183], v[132:135], v[32:35]\n\t" \
        "buffer_load_dwordx4 v4, s[24:27], s57 offen offset:1024  lds\n\t" \
        "v_mfma_f32_16x16x32_bf16 v[36:39], a[184:187], v[132:135], v[36:39]\n\t" \
        "ds_read_b128 v[120:123], v16 offset:11968\n\t" \
        "v_mfma_f32_16x16x32_bf16 v[40:43], a[188:191], v[132:135], v[40:43]\n\t" \
        "ds_read_b128 v[124:127], v16 offset:19904\n\t" \
        "v_mfma_f32_16x16x32_bf16 v[44:47], a[180:183], v[136:139], v[44:47]\n\t" \
        "ds_read_b128 v[128:131], v16 offset:27840\n\t" \
        "v_mfma_f32_16x16x32_bf16 v[48:51], a[184:187], v[136:139], v[48:51]\n\t" \
        "ds_read_b128 v[132:135], v16 offset:4096\n\t" \
        "v_mfma_f32_16x16x32_bf16 v[52:55], a[188:191], v[136:139], v[52:55]\n\t" \
        "ds_read_b64_tr_b16 a[204:205], v6 offset:6144\n\t" \
        "s_waitcnt lgkmcnt(7)\n\t" \
        "v_mfma_f32_16x16x32_bf16 v[56:59], a[180:183], v[140:143], v[56:59]\n\t" \
        "buffer_load_dwordx4 v5, s[24:27], s57 offen offset:2048  lds\n\t" \
        "v_mfma_f32_16x16x32_bf16 v[60:63], a[184:187], v[140:143], v[60:63]\n\t" \
        "ds_read_b64_tr_b16 a[206:207], v6 offset:6912\n\t" \
        "v_mfma_f32_16x16x32_bf16 v[64:67], a[188:191], v[140:143], v[64:67]\n\t" \
        "ds_read_b64_tr_b16 a[208:209], v7 offset:6144\n\t" \
        "v_mfma_f32_16x16x32_bf16 v[68:71], a[180:183], v[144:147], v[68:71]\n\t" \
        "ds_read_b64_tr_b16 a[210:211], v7 offset:6912\n\t" \
        "v_mfma_f32_16x16x32_bf16 v[72:75], a[184:187], v[144:147], v[72:75]\n\t" \
        "ds_read_b64_tr_b16 a[212:213], v8 offset:6144\n\t" \
        "v_mfma_f32_16x16x32_bf16 v[76:79], a[188:191], v[144:147], v[76:79]\n\t" \
        "ds_read_b64_tr_b16 a[214:215], v8 offset:6912\n\t" \
        "v_mfma_f32_16x16x32_bf16 v[32:35], a[192:195], v[116:119], v[32:35]\n\t" \
        "buffer_load_dwordx4 v[148:151], v17, s[20:23], s37 offen \n\t" \
        "v_mfma_f32_16x16x32_bf16 v[36:39], a[196:199], v[116:119], v[36:39]\n\t" \
        "ds_read_b128 v[136:139], v16 offset:12032\n\t" \
        "s_waitcnt lgkmcnt(6)\n\t" \
        "v_mfma_f32_16x16x32_bf16 v[40:43], a[200:203], v[116:119], v[40:43]\n\t" \
        "ds_read_b128 v[140:143], v16 offset:19968\n\t" \
        "v_mfma_f32_16x16x32_bf16 v[44:47], a[192:195], v[120:123], v[44:47]\n\t" \
        "ds_read_b128 v[144:147], v16 offset:27904\n\t" \
        "v_mfma_f32_16x16x32_bf16 v[48:51], a[196:199], v[120:123], v[48:51]\n\t" \
        "ds_read_b128 v[116:119], v14 offset:7936\n\t" \
        "v_mfma_f32_16x16x32_bf16 v[52:55], a[200:203], v[120:123], v[52:55]\n\t" \
        "ds_read_b64_tr_b16 a[216:217], v6 offset:12288\n\t" \
        "v_mfma_f32_16x16x32_bf16 v[56:59], a[192:195], v[124:127], v[56:59]\n\t" \
        "buffer_load_dwordx4 v[152:155], v17, s[20:23], s37 offen offset:16 \n\t" \
        "v_mfma_f32_16x16x32_bf16 v[60:63], a[196:199], v[124:127], v[60:63]\n\t" \
        "ds_read_b64_tr_b16 a[218:219], v6 offset:13056\n\t" \
        "v_mfma_f32_16x16x32_bf16 v[64:67], a[200:203], v[124:127], v[64:67]\n\t" \
        "ds_read_b64_tr_b16 a[220:221], v7 offset:12288\n\t" \
        "v_mfma_f32_16x16x32_bf16 v[68:71], a[192:195], v[128:131], v[68:71]\n\t" \
        "ds_read_b64_tr_b16 a[222:223], v7 offset:13056\n\t" \
        "v_mfma_f32_16x16x32_bf16 v[72:75], a[196:199], v[128:131], v[72:75]\n\t" \
        "ds_read_b64_tr_b16 a[224:225], v8 offset:12288\n\t" \
        "v_mfma_f32_16x16x32_bf16 v[76:79], a[200:203], v[128:131], v[76:79]\n\t" \
        "ds_read_b64_tr_b16 a[226:227], v8 offset:13056\n\t" \
        "s_waitcnt lgkmcnt(7)\n\t" \
        "v_mfma_f32_16x16x32_bf16 v[32:35], a[204:207], v[132:135], v[32:35]\n\t" \
        "buffer_load_dword v156, v17, s[20:23], s37 offen offset:32 \n\t" \
        "v_mfma_f32_16x16x32_bf16 v[36:39], a[208:211], v[132:135], v[36:39]\n\t" \
        "ds_read_b128 v[120:123], v14 offset:15872\n\t" \
        "v_mfma_f32_16x16x32_bf16 v[40:43], a[212:215], v[132:135], v[40:43]\n\t" \
        "ds_read_b128 v[124:127], v14 offset:23808\n\t" \
        "v_mfma_f32_16x16x32_bf16 v[44:47], a[204:207], v[136:139], v[44:47]\n\t" \
        "ds_read_b128 v[128:131], v14 offset:31744\n\t" \
        "v_mfma_f32_16x16x32_bf16 v[48:51], a[208:211], v[136:139], v[48:51]\n\t" \
        "ds_read_b128 v[132:135], v14 offset:8000\n\t" \
        "s_add_u32 s57, s36, 0x5e800\n\t" \
        "s_add_u32 m0, s38, 0x9b00\n\t" \
        "v_mfma_f32_16x16x32_bf16 v[52:55], a[212:215], v[136:139], v[52:55]\n\t" \
        "ds_read_b64_tr_b16 a[228:229], v6 offset:18432\n\t" \
        "v_mfma_f32_16x16x32_bf16 v[56:59], a[204:207], v[140:143], v[56:59]\n\t" \
        "buffer_load_dwordx4 v13, s[16:19], s57 offen nt lds\n\t" \
        "v_mfma_f32_16x16x32_bf16 v[60:63], a[208:211], v[140:143], v[60:63]\n\t" \
        "ds_read_b64_tr_b16 a[230:231], v6 offset:19200\n\t" \
        "v_mfma_f32_16x16x32_bf16 v[64:67], a[212:215], v[140:143], v[64:67]\n\t" \
        "ds_read_b64_tr_b16 a[232:233], v7 offset:18432\n\t" \
        "v_mfma_f32_16x16x32_bf16 v[68:71], a[204:207], v[144:147], v[68:71]\n\t" \
        "ds_read_b64_tr_b16 a[234:235], v7 offset:19200\n\t" \
        "v_mfma_f32_16x16x32_bf16 v[72:75], a[208:211], v[144:147], v[72:75]\n\t" \
        "ds_read_b64_tr_b16 a[236:237], v8 offset:18432\n\t" \
        "s_add_u32 s57, s36, 0x69000\n\t" \
        "s_add_u32 m0, s38, 0xaa80\n\t" \
        "v_mfma_f32_16x16x32_bf16 v[76:79], a[212:215], v[144:147], v[76:79]\n\t" \
        "ds_read_b64_tr_b16 a[238:239], v8 offset:19200\n\t" \
        "s_waitcnt lgkmcnt(7)\n\t" \
        "v_mfma_f32_16x16x32_bf16 v[32:35], a[216:219], v[116:119], v[32:35]\n\t" \
        "buffer_load_dwordx4 v13, s[16:19], s57 offen nt lds\n\t" \
        "v_mfma_f32_16x16x32_bf16 v[36:39], a[220:223], v[116:119], v[36:39]\n\t" \
        "ds_read_b128 v[136:139], v14 offset:15936\n\t" \
        "v_mfma_f32_16x16x32_bf16 v[40:43], a[224:227], v[116:119], v[40:43]\n\t" \
        "ds_read_b128 v[140:143], v14 offset:23872\n\t" \
        "v_mfma_f32_16x16x32_bf16 v[44:47], a[216:219], v[120:123], v[44:47]\n\t" \
        "ds_read_b128 v[144:147], v14 offset:31808\n\t" \
        "v_mfma_f32_16x16x32_bf16 v[48:51], a[220:223], v[120:123], v[48:51]\n\t" \
        "ds_read_b128 v[116:119], v14 offset:8064\n\t" \
        "s_add_u32 s57, s36, 0x73800\n\t" \
        "s_add_u32 m0, s38, 0xba00\n\t" \
        "v_mfma_f32_16x16x32_bf16 v[52:55], a[224:227], v[120:123], v[52:55]\n\t" \
        "s_waitcnt vmcnt(11)\n\t" \
        "s_barrier\n\t" \
        "ds_read_b64_tr_b16 a[240:241], v6 offset:24576\n\t" \
        "v_mfma_f32_16x16x32_bf16 v[56:59], a[216:219], v[124:127], v[56:59]\n\t" \
        "buffer_load_dwordx4 v13, s[16:19], s57 offen nt lds\n\t" \
        "v_mfma_f32_16x16x32_bf16 v[60:63], a[220:223], v[124:127], v[60:63]\n\t" \
        "ds_read_b64_tr_b16 a[242:243], v6 offset:25344\n\t" \
        "v_mfma_f32_16x16x32_bf16 v[64:67], a[224:227], v[124:127], v[64:67]\n\t" \
        "ds_read_b64_tr_b16 a[244:245], v7 offset:24576\n\t" \
        "v_mfma_f32_16x16x32_bf16 v[68:71], a[216:219], v[128:131], v[68:71]\n\t" \
        "ds_read_b64_tr_b16 a[246:247], v7 offset:25344\n\t" \
        "v_mfma_f32_16x16x32_bf16 v[72:75], a[220:223], v[128:131], v[72:75]\n\t" \
        "ds_read_b64_tr_b16 a[248:249], v8 offset:24576\n\t" \
        "s_add_u32 s57, s36, 0x7e000\n\t" \
        "s_add_u32 m0, s38, 0xc980\n\t" \
        "v_mfma_f32_16x16x32_bf16 v[76:79], a[224:227], v[128:131], v[76:79]\n\t" \
        "ds_read_b64_tr_b16 a[250:251], v8 offset:25344\n\t" \
        "s_waitcnt lgkmcnt(7)\n\t" \
        "v_mfma_f32_16x16x32_bf16 v[32:35], a[228:231], v[132:135], v[32:35]\n\t" \
        "buffer_load_dwordx4 v13, s[16:19], s57 offen nt lds\n\t" \
        "v_mfma_f32_16x16x32_bf16 v[36:39], a[232:235], v[132:135], v[36:39]\n\t" \
        "ds_read_b128 v[120:123], v14 offset:16000\n\t" \
        "v_mfma_f32_16x16x32_bf16 v[40:43], a[236:239], v[132:135], v[40:43]\n\t" \
        "ds_read_b128 v[124:127], v14 offset:23936\n\t" \
        "v_mfma_f32_16x16x32_bf16 v[44:47], a[228:231], v[136:139], v[44:47]\n\t" \
        "ds_read_b128 v[128:131], v14 offset:31872\n\t" \
        "v_mfma_f32_16x16x32_bf16 v[48:51], a[232:235], v[136:139], v[48:51]\n\t" \
        "ds_read_b128 v[132:135], v15 offset:7936\n\t" \
        "s_add_u32 s57, s36, 0x88800\n\t" \
        "s_add_u32 m0, s38, 0xd900\n\t" \
        "v_mfma_f32_16x16x32_bf16 v[52:55], a[236:239], v[136:139], v[52:55]\n\t" \
        "ds_read_b64_tr_b16 a[252:253], v6 offset:30720\n\t" \
        "v_mfma_f32_16x16x32_bf16 v[56:59], a[228:231], v[140:143], v[56:59]\n\t" \
        "buffer_load_dwordx4 v13, s[16:19], s57 offen nt lds\n\t" \
        "v_mfma_f32_16x16x32_bf16 v[60:63], a[232:235], v[140:143], v[60:63]\n\t" \
        "ds_read_b64_tr_b16 a[254:255], v6 offset:31488\n\t" \
        "v_mfma_f32_16x16x32_bf16 v[64:67], a[236:239], v[140:143], v[64:67]\n\t" \
        "ds_read_b64_tr_b16 v[188:189], v7 offset:30720\n\t" \
        "v_mfma_f32_16x16x32_bf16 v[68:71], a[228:231], v[144:147], v[68:71]\n\t" \
        "ds_read_b64_tr_b16 v[190:191], v7 offset:31488\n\t" \
        "v_mfma_f32_16x16x32_bf16 v[72:75], a[232:235], v[144:147], v[72:75]\n\t" \
        "ds_read_b64_tr_b16 v[192:193], v8 offset:30720\n\t" \
        "s_add_u32 s57, s36, 0x93000\n\t" \
        "s_min_u32 s57, s57, 0x925800\n\t" \
        "s_add_u32 m0, s38, 0xe880\n\t" \
        "v_mfma_f32_16x16x32_bf16 v[76:79], a[236:239], v[144:147], v[76:79]\n\t" \
        "ds_read_b64_tr_b16 v[194:195], v8 offset:31488\n\t" \
        "s_waitcnt lgkmcnt(7)\n\t" \
        "v_mfma_f32_16x16x32_bf16 v[32:35], a[240:243], v[116:119], v[32:35]\n\t" \
        "buffer_load_dwordx4 v13, s[16:19], s57 offen nt lds\n\t" \
        "v_mfma_f32_16x16x32_bf16 v[36:39], a[244:247], v[116:119], v[36:39]\n\t" \
        "ds_read_b128 v[136:139], v15 offset:15872\n\t" \
        "v_mfma_f32_16x16x32_bf16 v[40:43], a[248:251], v[116:119], v[40:43]\n\t" \
        "ds_read_b128 v[140:143], v15 offset:23808\n\t" \
        "v_mfma_f32_16x16x32_bf16 v[44:47], a[240:243], v[120:123], v[44:47]\n\t" \
        "ds_read_b128 v[144:147], v15 offset:31744\n\t" \
        "v_mfma_f32_16x16x32_bf16 v[48:51], a[244:247], v[120:123], v[48:51]\n\t" \
        "ds_read_b128 v[116:119], v15 offset:8000\n\t" \
        "s_add_u32 s57, s36, 0x9d800\n\t" \
        "s_min_u32 s57, s57, 0x925800\n\t" \
        "s_add_u32 m0, s38, 0xf800\n\t" \
        "v_mfma_f32_16x16x32_bf16 v[52:55], a[248:251], v[120:123], v[52:55]\n\t" \
        "ds_read_b64_tr_b16 v[196:197], v6 offset:36864\n\t" \
        "v_mfma_f32_16x16x32_bf16 v[56:59], a[240:243], v[124:127], v[56:59]\n\t" \
        "buffer_load_dwordx4 v13, s[16:19], s57 offen nt lds\n\t" \
        "v_mfma_f32_16x16x32_bf16 v[60:63], a[244:247], v[124:127], v[60:63]\n\t" \
        "ds_read_b64_tr_b16 v[198:199], v6 offset:37632\n\t" \
        "v_mfma_f32_16x16x32_bf16 v[64:67], a[248:251], v[124:127], v[64:67]\n\t" \
        "ds_read_b64_tr_b16 v[200:201], v7 offset:36864\n\t" \
        "v_mfma_f32_16x16x32_bf16 v[68:71], a[240:243], v[128:131], v[68:71]\n\t" \
        "ds_read_b64_tr_b16 v[202:203], v7 offset:37632\n\t" \
        "v_mfma_f32_16x16x32_bf16 v[72:75], a[244:247], v[128:131], v[72:75]\n\t" \
        "ds_read_b64_tr_b16 v[204:205], v8 offset:36864\n\t" \
        "s_add_u32 s57, s36, 0xa8000\n\t" \
        "s_min_u32 s57, s57, 0x925800\n\t" \
        "s_add_u32 m0, s38, 0x10780\n\t" \
        "v_mfma_f32_16x16x32_bf16 v[76:79], a[248:251], v[128:131], v[76:79]\n\t" \
        "ds_read_b64_tr_b16 v[206:207], v8 offset:37632\n\t" \
        "s_waitcnt lgkmcnt(7)\n\t" \
        "v_mfma_f32_16x16x32_bf16 v[32:35], a[252:255], v[132:135], v[32:35]\n\t" \
        "buffer_load_dwordx4 v13, s[16:19], s57 offen nt lds\n\t" \
        "v_mfma_f32_16x16x32_bf16 v[36:39], v[188:191], v[132:135], v[36:39]\n\t" \
        "ds_read_b128 v[120:123], v15 offset:15936\n\t" \
        "v_mfma_f32_16x16x32_bf16 v[40:43], v[192:195], v[132:135], v[40:43]\n\t" \
        "ds_read_b128 v[124:127], v15 offset:23872\n\t" \
        "v_mfma_f32_16x16x32_bf16 v[44:47], a[252:255], v[136:139], v[44:47]\n\t" \
        "ds_read_b128 v[128:131], v15 offset:31808\n\t" \
        "v_mfma_f32_16x16x32_bf16 v[48:51], v[188:191], v[136:139], v[48:51]\n\t" \
        "ds_read_b128 v[132:135], v15 offset:8064\n\t" \
        "v_mfma_f32_16x16x32_bf16 v[52:55], v[192:195], v[136:139], v[52:55]\n\t" \
        "ds_read_b64_tr_b16 v[208:209], v6 offset:43008\n\t" \
        "v_mfma_f32_16x16x32_bf16 v[56:59], a[252:255], v[140:143], v[56:59]\n\t" \
        "ds_read_b64_tr_b16 v[210:211], v6 offset:43776\n\t" \
        "v_mfma_f32_16x16x32_bf16 v[60:63], v[188:191], v[140:143], v[60:63]\n\t" \
        "ds_read_b64_tr_b16 v[212:213], v7 offset:43008\n\t" \
        "v_mfma_f32_16x16x32_bf16 v[64:67], v[192:195], v[140:143], v[64:67]\n\t" \
        "ds_read_b64_tr_b16 v[214:215], v7 offset:43776\n\t" \
        "v_mfma_f32_16x16x32_bf16 v[68:71], a[252:255], v[144:147], v[68:71]\n\t" \
        "ds_read_b64_tr_b16 v[216:217], v8 offset:43008\n\t" \
        "v_mfma_f32_16x16x32_bf16 v[72:75], v[188:191], v[144:147], v[72:75]\n\t" \
        "ds_read_b64_tr_b16 v[218:219], v8 offset:43776\n\t" \
        "v_mfma_f32_16x16x32_bf16 v[76:79], v[192:195], v[144:147], v[76:79]\n\t" \
        "ds_read_b128 v[136:139], v15 offset:16000\n\t" \
        "s_waitcnt lgkmcnt(8)\n\t" \
        "v_mfma_f32_16x16x32_bf16 v[32:35], v[196:199], v[116:119], v[32:35]\n\t" \
        "ds_read_b128 v[140:143], v15 offset:23936\n\t" \
        "v_mfma_f32_16x16x32_bf16 v[36:39], v[200:203], v[116:119], v[36:39]\n\t" \
        "ds_read_b128 v[144:147], v15 offset:31872\n\t" \
        "v_mfma_f32_16x16x32_bf16 v[40:43], v[204:207], v[116:119], v[40:43]\n\t" \
        "ds_read_b128 v[116:119], v16 offset:7936\n\t" \
        "v_mfma_f32_16x16x32_bf16 v[44:47], v[196:199], v[120:123], v[44:47]\n\t" \
        "v_mfma_f32_16x16x32_bf16 v[48:51], v[200:203], v[120:123], v[48:51]\n\t" \
        "v_mfma_f32_16x16x32_bf16 v[52:55], v[204:207], v[120:123], v[52:55]\n\t" \
        "s_waitcnt vmcnt(11)\n\t" \
        "s_barrier\n\t" \
        "ds_read_b64_tr_b16 v[220:221], v6 offset:49152\n\t" \
        "v_mfma_f32_16x16x32_bf16 v[56:59], v[196:199], v[124:127], v[56:59]\n\t" \
        "ds_read_b64_tr_b16 v[222:223], v6 offset:49920\n\t" \
        "v_mfma_f32_16x16x32_bf16 v[60:63], v[200:203], v[124:127], v[60:63]\n\t" \
        "ds_read_b64_tr_b16 v[224:225], v7 offset:49152\n\t" \
        "v_mfma_f32_16x16x32_bf16 v[64:67], v[204:207], v[124:127], v[64:67]\n\t" \
        "ds_read_b64_tr_b16 v[226:227], v7 offset:49920\n\t" \
        "v_mfma_f32_16x16x32_bf16 v[68:71], v[196:199], v[128:131], v[68:71]\n\t" \
        "ds_read_b64_tr_b16 v[228:229], v8 offset:49152\n\t" \
        "v_mfma_f32_16x16x32_bf16 v[72:75], v[200:203], v[128:131], v[72:75]\n\t" \
        "ds_read_b64_tr_b16 v[230:231], v8 offset:49920\n\t" \
        "v_mfma_f32_16x16x32_bf16 v[76:79], v[204:207], v[128:131], v[76:79]\n\t" \
        "ds_read_b128 v[120:123], v16 offset:15872\n\t" \
        "s_waitcnt lgkmcnt(7)\n\t" \
        "v_mfma_f32_16x16x32_bf16 v[32:35], v[208:211], v[132:135], v[32:35]\n\t" \
        "ds_read_b128 v[124:127], v16 offset:23808\n\t" \
        "v_mfma_f32_16x16x32_bf16 v[36:39], v[212:215], v[132:135], v[36:39]\n\t" \
        "ds_read_b128 v[128:131], v16 offset:31744\n\t" \
        "v_mfma_f32_16x16x32_bf16 v[40:43], v[216:219], v[132:135], v[40:43]\n\t" \
        "ds_read_b128 v[132:135], v16 offset:8000\n\t" \
        "v_mfma_f32_16x16x32_bf16 v[44:47], v[208:211], v[136:139], v[44:47]\n\t" \
        "ds_read_b64_tr_b16 v[232:233], v6 offset:55296\n\t" \
        "v_mfma_f32_16x16x32_bf16 v[48:51], v[212:215], v[136:139], v[48:51]\n\t" \
        "ds_read_b64_tr_b16 v[234:235], v6 offset:56064\n\t" \
        "v_mfma_f32_16x16x32_bf16 v[52:55], v[216:219], v[136:139], v[52:55]\n\t" \
        "ds_read_b64_tr_b16 v[236:237], v7 offset:55296\n\t" \
        "v_mfma_f32_16x16x32_bf16 v[56:59], v[208:211], v[140:143], v[56:59]\n\t" \
        "ds_read_b64_tr_b16 v[238:239], v7 offset:56064\n\t" \
        "v_mfma_f32_16x16x32_bf16 v[60:63], v[212:215], v[140:143], v[60:63]\n\t" \
        "ds_read_b64_tr_b16 v[240:241], v8 offset:55296\n\t" \
        "v_mfma_f32_16x16x32_bf16 v[64:67], v[216:219], v[140:143], v[64:67]\n\t" \
        "ds_read_b64_tr_b16 v[242:243], v8 offset:56064\n\t" \
        "v_mfma_f32_16x16x32_bf16 v[68:71], v[208:211], v[144:147], v[68:71]\n\t" \
        "ds_read_b128 v[136:139], v16 offset:15936\n\t" \
        "v_mfma_f32_16x16x32_bf16 v[72:75], v[212:215], v[144:147], v[72:75]\n\t" \
        "ds_read_b128 v[140:143], v16 offset:23872\n\t" \
        "v_mfma_f32_16x16x32_bf16 v[76:79], v[216:219], v[144:147], v[76:79]\n\t" \
        "ds_read_b128 v[144:147], v16 offset:31808\n\t" \
        "s_waitcnt lgkmcnt(8)\n\t" \
        "v_mfma_f32_16x16x32_bf16 v[32:35], v[220:223], v[116:119], v[32:35]\n\t" \
        "ds_read_b64_tr_b16 v[244:245], v20 offset:0\n\t" \
        "v_mfma_f32_16x16x32_bf16 v[36:39], v[224:227], v[116:119], v[36:39]\n\t" \
        "ds_read_b64_tr_b16 v[246:247], v20 offset:768\n\t" \
        "v_mfma_f32_16x16x32_bf16 v[40:43], v[228:231], v[116:119], v[40:43]\n\t" \
        "s_waitcnt vmcnt(8)\n\t" \
        "ds_read_b128 v[116:119], v16 offset:8064\n\t" \
        "v_and_b32 v148, 0x7fff7fff, v148\n\t" \
        "v_mfma_f32_16x16x32_bf16 v[44:47], v[220:223], v[120:123], v[44:47]\n\t" \
        "ds_read_b64_tr_b16 v[248:249], v21 offset:0\n\t" \
        "v_and_b32 v149, 0x7fff7fff, v149\n\t" \
        "v_mfma_f32_16x16x32_bf16 v[48:51], v[224:227], v[120:123], v[48:51]\n\t" \
        "ds_read_b64_tr_b16 v[250:251], v21 offset:768\n\t" \
        "v_and_b32 v150, 0x7fff7fff, v150\n\t" \
        "v_mfma_f32_16x16x32_bf16 v[52:55], v[228:231], v[120:123], v[52:55]\n\t" \
        "ds_read_b64_tr_b16 v[252:253], v22 offset:0\n\t" \
        "v_and_b32 v151, 0x7fff7fff, v151\n\t" \
        "v_mfma_f32_16x16x32_bf16 v[56:59], v[220:223], v[124:127], v[56:59]\n\t" \
        "ds_read_b64_tr_b16 v[254:255], v22 offset:768\n\t" \
        "v_and_b32 v152, 0x7fff7fff, v152\n\t" \
        "v_mfma_f32_16x16x32_bf16 v[60:63], v[224:227], v[124:127], v[60:63]\n\t" \
        "ds_read_b128 v[120:123], v16 offset:16000\n\t" \
        "v_and_b32 v153, 0x7fff7fff, v153\n\t" \
        "v_mfma_f32_16x16x32_bf16 v[64:67], v[228:231], v[124:127], v[64:67]\n\t" \
        "ds_read_b128 v[124:127], v16 offset:23936\n\t" \
        "v_and_b32 v154, 0x7fff7fff, v154\n\t" \
        "v_mfma_f32_16x16x32_bf16 v[68:71], v[220:223], v[128:131], v[68:71]\n\t" \
        "v_and_b32 v155, 0x7fff7fff, v155\n\t" \
        "v_and_b32 v156, 0x7fff7fff, v156\n\t" \
        "v_mfma_f32_16x16x32_bf16 v[72:75], v[224:227], v[128:131], v[72:75]\n\t" \
        "v_pk_max_u16 v148, v148, v149\n\t" \
        "v_pk_max_u16 v150, v150, v151\n\t" \
        "v_mfma_f32_16x16x32_bf16 v[76:79], v[228:231], v[128:131], v[76:79]\n\t" \
        "ds_read_b128 v[128:131], v16 offset:31872\n\t" \
        "v_pk_max_u16 v152, v152, v153\n\t" \
        "s_waitcnt lgkmcnt(6)\n\t" \
        "v_mfma_f32_16x16x32_bf16 v[32:35], v[232:235], v[132:135], v[32:35]\n\t" \
        "v_pk_max_u16 v154, v154, v155\n\t" \
        "v_pk_max_u16 v156, v156, v23\n\t" \
        "v_mfma_f32_16x16x32_bf16 v[36:39], v[236:239], v[132:135], v[36:39]\n\t" \
        "v_pk_max_u16 v148, v148, v150\n\t" \
        "v_pk_max_u16 v152, v152, v154\n\t" \
        "v_mfma_f32_16x16x32_bf16 v[40:43], v[240:243], v[132:135], v[40:43]\n\t" \
        "ds_read_b128 v[132:135], v14 offset:31744\n\t" \
        "v_pk_max_u16 v148, v148, v156\n\t" \
        "v_mfma_f32_16x16x32_bf16 v[44:47], v[232:235], v[136:139], v[44:47]\n\t" \
        "v_pk_max_u16 v148, v148, v152\n\t" \
        "v_cmp_ne_u32 vcc, v148, v23\n\t" \
        "s_nop 0\n\t" \
        "s_or_b32 s44, vcc_lo, vcc_hi\n\t" \
        "v_mfma_f32_16x16x32_bf16 v[48:51], v[236:239], v[136:139], v[48:51]\n\t" \
        "v_mov_b32 v149, s44\n\t" \
        "v_mfma_f32_16x16x32_bf16 v[52:55], v[240:243], v[136:139], v[52:55]\n\t" \
        "ds_read_b128 v[136:139], v14 offset:31808\n\t" \
        "v_mfma_f32_16x16x32_bf16 v[56:59], v[232:235], v[140:143], v[56:59]\n\t" \
        "ds_write_b32 v24, v149\n\t" \
        "v_mfma_f32_16x16x32_bf16 v[60:63], v[236:239], v[140:143], v[60:63]\n\t" \
        "v_mfma_f32_16x16x32_bf16 v[64:67], v[240:243], v[140:143], v[64:67]\n\t" \
        "ds_read_b128 v[140:143], v14 offset:31872\n\t" \
        "v_mfma_f32_16x16x32_bf16 v[68:71], v[232:235], v[144:147], v[68:71]\n\t" \
        "v_mfma_f32_16x16x32_bf16 v[72:75], v[236:239], v[144:147], v[72:75]\n\t" \
        "v_mfma_f32_16x16x32_bf16 v[76:79], v[240:243], v[144:147], v[76:79]\n\t" \
        "ds_read_b128 v[144:147], v15 offset:31744\n\t" \
        "v_mfma_f32_16x16x32_bf16 v[32:35], v[244:247], v[116:119], v[32:35]\n\t" \
        "s_waitcnt lgkmcnt(4)\n\t" \
        "v_mfma_f32_16x16x32_bf16 v[36:39], v[248:251], v[116:119], v[36:39]\n\t" \
        "v_mfma_f32_16x16x32_bf16 v[40:43], v[252:255], v[116:119], v[40:43]\n\t" \
        "ds_read_b128 v[116:119], v15 offset:31808\n\t" \
        "v_mfma_f32_16x16x32_bf16 v[44:47], v[244:247], v[120:123], v[44:47]\n\t" \
        "v_mfma_f32_16x16x32_bf16 v[48:51], v[248:251], v[120:123], v[48:51]\n\t" \
        "v_mfma_f32_16x16x32_bf16 v[52:55], v[252:255], v[120:123], v[52:55]\n\t" \
        "ds_read_b128 v[120:123], v15 offset:31872\n\t" \
        "v_mfma_f32_16x16x32_bf16 v[56:59], v[244:247], v[124:127], v[56:59]\n\t" \
        "v_mfma_f32_16x16x32_bf16 v[60:63], v[248:251], v[124:127], v[60:63]\n\t" \
        "v_mfma_f32_16x16x32_bf16 v[64:67], v[252:255], v[124:127], v[64:67]\n\t" \
        "ds_read_b128 v[124:127], v16 offset:31744\n\t" \
        "v_mfma_f32_16x16x32_bf16 v[68:71], v[244:247], v[128:131], v[68:71]\n\t" \
        "v_mfma_f32_16x16x32_bf16 v[72:75], v[248:251], v[128:131], v[72:75]\n\t" \
        "v_mfma_f32_16x16x32_bf16 v[76:79], v[252:255], v[128:131], v[76:79]\n\t" \
        "ds_read_b128 v[128:131], v16 offset:31808\n\t" \
        "v_mov_b32 v184, 0\n\t" \
        "v_mfma_f32_16x16x32_bf16 v[80:83], a[0:3], v[132:135], 0\n\t" \
        "v_mfma_f32_16x16x32_bf16 v[84:87], a[4:7], v[132:135], 0\n\t" \
        "v_mfma_f32_16x16x32_bf16 v[88:91], a[8:11], v[132:135], 0\n\t" \
        "v_cvt_pk_bf16_f32 v40, v40, v41\n\t" \
        "v_cvt_pk_bf16_f32 v41, v42, v43\n\t" \
        "s_waitcnt lgkmcnt(2)\n\t" \
        "v_mfma_f32_16x16x32_bf16 v[80:83], a[12:15], v[136:139], v[80:83]\n\t" \
        "ds_write_b64 v11, v[40:41] offset:0\n\t" \
        "v_cvt_pk_bf16_f32 v52, v52, v53\n\t" \
        "v_mfma_f32_16x16x32_bf16 v[84:87], a[16:19], v[136:139], v[84:87]\n\t" \
        "ds_read_b128 v[132:135], v16 offset:31872\n\t" \
        "v_cvt_pk_bf16_f32 v53, v54, v55\n\t" \
        "v_mfma_f32_16x16x32_bf16 v[88:91], a[20:23], v[136:139], v[88:91]\n\t" \
        "ds_write_b64 v11, v[52:53] offset:2048\n\t" \
        "v_cvt_pk_bf16_f32 v64, v64, v65\n\t" \
        "v_mfma_f32_16x16x32_bf16 v[80:83], a[24:27], v[140:143], v[80:83]\n\t" \
        "v_cvt_pk_bf16_f32 v65, v66, v67\n\t" \
        "v_cvt_pk_bf16_f32 v76, v76, v77\n\t" \
        "v_mfma_f32_16x16x32_bf16 v[84:87], a[28:31], v[140:143], v[84:87]\n\t" \
        "ds_write_b64 v11, v[64:65] offset:4096\n\t" \
        "v_cvt_pk_bf16_f32 v77, v78, v79\n\t" \
        "v_mfma_f32_16x16x32_bf16 v[88:91], a[32:35], v[140:143], v[88:91]\n\t" \
        "ds_write_b64 v11, v[76:77] offset:6144\n\t" \
        "v_cvt_pk_bf16_f32 v32, v32, v33\n\t" \
        "v_mfma_f32_16x16x32_bf16 v[80:83], a[36:39], v[144:147], v[80:83]\n\t" \
        "v_cvt_pk_bf16_f32 v33, v34, v35\n\t" \
        "v_cvt_pk_bf16_f32 v34, v36, v37\n\t" \
        "v_mfma_f32_16x16x32_bf16 v[84:87], a[40:43], v[144:147], v[84:87]\n\t" \
        "v_cvt_pk_bf16_f32 v35, v38, v39\n\t" \
        "v_permlane16_swap_b32 v32, v34\n\t" \
        "v_mfma_f32_16x16x32_bf16 v[88:91], a[44:47], v[144:147], v[88:91]\n\t" \
        "v_permlane16_swap_b32 v33, v35\n\t" \
        "v_mfma_f32_16x16x32_bf16 v[80:83], a[48:51], v[116:119], v[80:83]\n\t" \
        "v_mfma_f32_16x16x32_bf16 v[84:87], a[52:55], v[116:119], v[84:87]\n\t" \
        "v_mfma_f32_16x16x32_bf16 v[88:91], a[56:59], v[116:119], v[88:91]\n\t" \
        "v_mfma_f32_16x16x32_bf16 v[80:83], a[60:63], v[120:123], v[80:83]\n\t" \
        "v_mfma_f32_16x16x32_bf16 v[84:87], a[64:67], v[120:123], v[84:87]\n\t" \
        "v_mfma_f32_16x16x32_bf16 v[88:91], a[68:71], v[120:123], v[88:91]\n\t" \
        "s_waitcnt lgkmcnt(0)\n\t" \
        "v_mfma_f32_16x16x32_bf16 v[80:83], a[72:75], v[124:127], v[80:83]\n\t" \
        "v_mfma_f32_16x16x32_bf16 v[84:87], a[76:79], v[124:127], v[84:87]\n\t" \
        "s_waitcnt vmcnt(0)\n\t" \
        "s_barrier\n\t" \
        "ds_read_b128 v[136:139], v14 offset:35712\n\t" \
        "v_mfma_f32_16x16x32_bf16 v[88:91], a[80:83], v[124:127], v[88:91]\n\t" \
        "ds_read_b128 v[140:143], v14 offset:35776\n\t" \
        "v_mfma_f32_16x16x32_bf16 v[80:83], a[84:87], v[128:131], v[80:83]\n\t" \
        "ds_read_b128 v[144:147], v14 offset:35840\n\t" \
        "v_mfma_f32_16x16x32_bf16 v[84:87], a[88:91], v[128:131], v[84:87]\n\t" \
        "ds_read_b128 v[116:119], v15 offset:35712\n\t" \
        "v_mfma_f32_16x16x32_bf16 v[88:91], a[92:95], v[128:131], v[88:91]\n\t" \
        "ds_read_b128 v[120:123], v15 offset:35776\n\t" \
        "v_mfma_f32_16x16x32_bf16 v[80:83], a[96:99], v[132:135], v[80:83]\n\t" \
        "s_mov_b32 s56, s64\n\t" \
        "buffer_store_dwordx4 v[32:35], v9, s[28:31], s56 offen sc1\n\t" \
        "v_mfma_f32_16x16x32_bf16 v[84:87], a[100:103], v[132:135], v[84:87]\n\t" \
        "ds_read_b128 v[124:127], v15 offset:35840\n\t" \
        "v_mfma_f32_16x16x32_bf16 v[88:91], a[104:107], v[132:135], v[88:91]\n\t" \
        "ds_read_b128 v[128:131], v16 offset:35712\n\t" \
        "s_waitcnt lgkmcnt(6)\n\t" \
        "v_mfma_f32_16x16x32_bf16 v[80:83], a[108:111], v[136:139], v[80:83]\n\t" \
        "ds_read_b64 v[42:43], v12 offset:0\n\t" \
        "v_mfma_f32_16x16x32_bf16 v[84:87], a[112:115], v[136:139], v[84:87]\n\t" \
        "ds_read_b128 v[184:187], v184\n\t" \
        "v_mfma_f32_16x16x32_bf16 v[88:91], a[116:119], v[136:139], v[88:91]\n\t" \
        "ds_read_b128 v[132:135], v16 offset:35776\n\t" \
        "s_waitcnt lgkmcnt(7)\n\t" \
        "v_mfma_f32_16x16x32_bf16 v[80:83], a[120:123], v[140:143], v[80:83]\n\t" \
        "ds_read_b128 v[136:139], v16 offset:35840\n\t" \
        "v_mfma_f32_16x16x32_bf16 v[84:87], a[124:127], v[140:143], v[84:87]\n\t" \
        "v_mfma_f32_16x16x32_bf16 v[88:91], a[128:131], v[140:143], v[88:91]\n\t" \
        "ds_read_b128 v[140:143], v14 offset:39680\n\t" \
        "v_cvt_pk_bf16_f32 v44, v44, v45\n\t" \
        "v_mfma_f32_16x16x32_bf16 v[80:83], a[132:135], v[144:147], v[80:83]\n\t" \
        "v_cvt_pk_bf16_f32 v45, v46, v47\n\t" \
        "v_cvt_pk_bf16_f32 v46, v48, v49\n\t" \
        "v_mfma_f32_16x16x32_bf16 v[84:87], a[136:139], v[144:147], v[84:87]\n\t" \
        "v_cvt_pk_bf16_f32 v47, v50, v51\n\t" \
        "v_permlane16_swap_b32 v44, v46\n\t" \
        "v_mfma_f32_16x16x32_bf16 v[88:91], a[140:143], v[144:147], v[88:91]\n\t" \
        "s_waitcnt lgkmcnt(4)\n\t" \
        "v_permlane16_swap_b32 v40, v42\n\t" \
        "v_permlane16_swap_b32 v41, v43\n\t" \
        "v_mfma_f32_16x16x32_bf16 v[80:83], a[144:147], v[116:119], v[80:83]\n\t" \
        "ds_read_b128 v[144:147], v14 offset:39744\n\t" \
        "s_waitcnt lgkmcnt(4)\n\t" \
        "v_or3_b32 v184, v184, v185, v186\n\t" \
        "v_mfma_f32_16x16x32_bf16 v[84:87], a[148:151], v[116:119], v[84:87]\n\t" \
        "s_mov_b32 s56, s64\n\t" \
        "buffer_store_dwordx4 v[40:43], v10, s[28:31], s56 offen sc1\n\t" \
        "v_mfma_f32_16x16x32_bf16 v[88:91], a[152:155], v[116:119], v[88:91]\n\t" \
        "ds_read_b128 v[116:119], v14 offset:39808\n\t" \
        "v_or_b32 v184, v184, v187\n\t" \
        "v_mfma_f32_16x16x32_bf16 v[80:83], a[156:159], v[120:123], v[80:83]\n\t" \
        "v_readfirstlane_b32 s44, v184\n\t" \
        "v_permlane16_swap_b32 v45, v47\n\t" \
        "v_mfma_f32_16x16x32_bf16 v[84:87], a[160:163], v[120:123], v[84:87]\n\t" \
        "ds_read_b64 v[54:55], v12 offset:2048\n\t" \
        "s_cmp_lg_u32 s44, 0\n\t" \
        "s_cbranch_scc1 .LR_abort%=\n\t" \
        "v_mfma_f32_16x16x32_bf16 v[88:91], a[164:167], v[120:123], v[88:91]\n\t" \
        "ds_read_b128 v[120:123], v15 offset:39680\n\t" \
        "v_mfma_f32_16x16x32_bf16 v[80:83], a[168:171], v[124:127], v[80:83]\n\t" \
        "v_mfma_f32_16x16x32_bf16 v[84:87], a[172:175], v[124:127], v[84:87]\n\t" \
        "v_mfma_f32_16x16x32_bf16 v[88:91], a[176:179], v[124:127], v[88:91]\n\t" \
        "ds_read_b128 v[124:127], v15 offset:39744\n\t" \
        "v_mfma_f32_16x16x32_bf16 v[80:83], a[180:183], v[128:131], v[80:83]\n\t" \
        "v_mfma_f32_16x16x32_bf16 v[84:87], a[184:187], v[128:131], v[84:87]\n\t" \
        "v_mfma_f32_16x16x32_bf16 v[88:91], a[188:191], v[128:131], v[88:91]\n\t" \
        "ds_read_b128 v[128:131], v15 offset:39808\n\t" \
        "s_waitcnt lgkmcnt(4)\n\t" \
        "v_mfma_f32_16x16x32_bf16 v[80:83], a[192:195], v[132:135], v[80:83]\n\t" \
        "s_waitcnt lgkmcnt(3)\n\t" \
        "v_permlane16_swap_b32 v52, v54\n\t" \
        "v_permlane16_swap_b32 v53, v55\n\t" \
        "v_mfma_f32_16x16x32_bf16 v[84:87], a[196:199], v[132:135], v[84:87]\n\t" \
        "v_mfma_f32_16x16x32_bf16 v[88:91], a[200:203], v[132:135], v[88:91]\n\t" \
        "ds_read_b128 v[132:135], v16 offset:39680\n\t" \
        "v_mfma_f32_16x16x32_bf16 v[80:83], a[204:207], v[136:139], v[80:83]\n\t" \
        "v_mfma_f32_16x16x32_bf16 v[84:87], a[208:211], v[136:139], v[84:87]\n\t" \
        "v_mfma_f32_16x16x32_bf16 v[88:91], a[212:215], v[136:139], v[88:91]\n\t" \
        "ds_read_b128 v[136:139], v16 offset:39744\n\t" \
        "v_mfma_f32_16x16x32_bf16 v[80:83], a[216:219], v[140:143], v[80:83]\n\t" \
        "s_add_u32 s56, s64, 0x15000\n\t" \
        "buffer_store_dwordx4 v[44:47], v9, s[28:31], s56 offen sc1\n\t" \
        "v_mfma_f32_16x16x32_bf16 v[84:87], a[220:223], v[140:143], v[84:87]\n\t" \
        "v_mfma_f32_16x16x32_bf16 v[88:91], a[224:227], v[140:143], v[88:91]\n\t" \
        "ds_read_b128 v[140:143], v16 offset:39808\n\t" \
        "v_mfma_f32_16x16x32_bf16 v[80:83], a[228:231], v[144:147], v[80:83]\n\t" \
        "v_mfma_f32_16x16x32_bf16 v[84:87], a[232:235], v[144:147], v[84:87]\n\t" \
        "v_mfma_f32_16x16x32_bf16 v[88:91], a[236:239], v[144:147], v[88:91]\n\t" \
        "ds_read_b128 v[144:147], v14 offset:39680\n\t" \
        "v_mfma_f32_16x16x32_bf16 v[80:83], a[240:243], v[116:119], v[80:83]\n\t" \
        "v_mfma_f32_16x16x32_bf16 v[84:87], a[244:247], v[116:119], v[84:87]\n\t" \
        "s_add_u32 s56, s64, 0x15000\n\t" \
        "buffer_store_dwordx4 v[52:55], v10, s[28:31], s56 offen sc1\n\t" \
        "v_mfma_f32_16x16x32_bf16 v[88:91], a[248:251], v[116:119], v[88:91]\n\t" \
        "ds_read_b128 v[116:119], v14 offset:39744\n\t" \
        "v_cvt_pk_bf16_f32 v56, v56, v57\n\t" \
        "s_waitcnt lgkmcnt(3)\n\t" \
        "v_mfma_f32_16x16x32_bf16 v[80:83], a[252:255], v[120:123], v[80:83]\n\t" \
        "v_cvt_pk_bf16_f32 v57, v58, v59\n\t" \
        "v_cvt_pk_bf16_f32 v58, v60, v61\n\t" \
        "v_mfma_f32_16x16x32_bf16 v[84:87], v[188:191], v[120:123], v[84:87]\n\t" \
        "v_cvt_pk_bf16_f32 v59, v62, v63\n\t" \
        "v_permlane16_swap_b32 v56, v58\n\t" \
        "v_mfma_f32_16x16x32_bf16 v[88:91], v[192:195], v[120:123], v[88:91]\n\t" \
        "ds_read_b128 v[120:123], v14 offset:39808\n\t" \
        "v_permlane16_swap_b32 v57, v59\n\t" \
        "v_mfma_f32_16x16x32_bf16 v[80:83], v[196:199], v[124:127], v[80:83]\n\t" \
        "ds_read_b64 v[66:67], v12 offset:4096\n\t" \
        "v_mfma_f32_16x16x32_bf16 v[84:87], v[200:203], v[124:127], v[84:87]\n\t" \
        "v_mfma_f32_16x16x32_bf16 v[88:91], v[204:207], v[124:127], v[88:91]\n\t" \
        "ds_read_b128 v[124:127], v15 offset:39680\n\t" \
        "v_mfma_f32_16x16x32_bf16 v[80:83], v[208:211], v[128:131], v[80:83]\n\t" \
        "v_mfma_f32_16x16x32_bf16 v[84:87], v[212:215], v[128:131], v[84:87]\n\t" \
        "v_mfma_f32_16x16x32_bf16 v[88:91], v[216:219], v[128:131], v[88:91]\n\t" \
        "ds_read_b128 v[128:131], v15 offset:39744\n\t" \
        "v_mfma_f32_16x16x32_bf16 v[80:83], v[220:223], v[132:135], v[80:83]\n\t" \
        "v_mfma_f32_16x16x32_bf16 v[84:87], v[224:227], v[132:135], v[84:87]\n\t" \
        "v_mfma_f32_16x16x32_bf16 v[88:91], v[228:231], v[132:135], v[88:91]\n\t" \
        "ds_read_b128 v[132:135], v15 offset:39808\n\t" \
        "s_waitcnt lgkmcnt(3)\n\t" \
        "v_permlane16_swap_b32 v64, v66\n\t" \
        "v_mfma_f32_16x16x32_bf16 v[80:83], v[232:235], v[136:139], v[80:83]\n\t" \
        "v_permlane16_swap_b32 v65, v67\n\t" \
        "v_mfma_f32_16x16x32_bf16 v[84:87], v[236:239], v[136:139], v[84:87]\n\t" \
        "v_mfma_f32_16x16x32_bf16 v[88:91], v[240:243], v[136:139], v[88:91]\n\t" \
        "ds_read_b128 v[136:139], v16 offset:39680\n\t" \
        "v_mfma_f32_16x16x32_bf16 v[80:83], v[244:247], v[140:143], v[80:83]\n\t" \
        "v_mfma_f32_16x16x32_bf16 v[84:87], v[248:251], v[140:143], v[84:87]\n\t" \
        "v_mfma_f32_16x16x32_bf16 v[88:91], v[252:255], v[140:143], v[88:91]\n\t" \
        "ds_read_b128 v[140:143], v16 offset:39744\n\t" \
        "v_mfma_f32_16x16x32_bf16 v[92:95], a[0:3], v[144:147], 0\n\t" \
        "v_mfma_f32_16x16x32_bf16 v[96:99], a[4:7], v[144:147], 0\n\t" \
        "v_mfma_f32_16x16x32_bf16 v[100:103], a[8:11], v[144:147], 0\n\t" \
        "s_nop 5\n\t" \
        "v_cvt_pk_bf16_f32 v88, v88, v89\n\t" \
        "v_cvt_pk_bf16_f32 v89, v90, v91\n\t" \
        "v_mfma_f32_16x16x32_bf16 v[92:95], a[12:15], v[116:119], v[92:95]\n\t" \
        "s_add_u32 s56, s64, 0x2a000\n\t" \
        "buffer_store_dwordx4 v[56:59], v9, s[28:31], s56 offen sc1\n\t" \
        "v_mfma_f32_16x16x32_bf16 v[96:99], a[16:19], v[116:119], v[96:99]\n\t" \
        "ds_write_b64 v11, v[88:89] offset:8192\n\t" \
        "v_cvt_pk_bf16_f32 v80, v80, v81\n\t" \
        "v_mfma_f32_16x16x32_bf16 v[100:103], a[20:23], v[116:119], v[100:103]\n\t" \
        "ds_read_b128 v[144:147], v16 offset:39808\n\t" \
        "v_cvt_pk_bf16_f32 v81, v82, v83\n\t" \
        "v_mfma_f32_16x16x32_bf16 v[92:95], a[24:27], v[120:123], v[92:95]\n\t" \
        "ds_read_b128 v[116:119], v14 offset:43648\n\t" \
        "v_cvt_pk_bf16_f32 v82, v84, v85\n\t" \
        "v_mfma_f32_16x16x32_bf16 v[96:99], a[28:31], v[120:123], v[96:99]\n\t" \
        "v_cvt_pk_bf16_f32 v83, v86, v87\n\t" \
        "v_permlane16_swap_b32 v80, v82\n\t" \
        "v_mfma_f32_16x16x32_bf16 v[100:103], a[32:35], v[120:123], v[100:103]\n\t" \
        "ds_read_b128 v[120:123], v14 offset:43712\n\t" \
        "v_permlane16_swap_b32 v81, v83\n\t" \
        "s_waitcnt lgkmcnt(4)\n\t" \
        "v_mfma_f32_16x16x32_bf16 v[92:95], a[36:39], v[124:127], v[92:95]\n\t" \
        "v_mfma_f32_16x16x32_bf16 v[96:99], a[40:43], v[124:127], v[96:99]\n\t" \
        "s_add_u32 s56, s64, 0x2a000\n\t" \
        "buffer_store_dwordx4 v[64:67], v10, s[28:31], s56 offen sc1\n\t" \
        "v_mfma_f32_16x16x32_bf16 v[100:103], a[44:47], v[124:127], v[100:103]\n\t" \
        "ds_read_b128 v[124:127], v14 offset:43776\n\t" \
        "v_cvt_pk_bf16_f32 v68, v68, v69\n\t" \
        "v_mfma_f32_16x16x32_bf16 v[92:95], a[48:51], v[128:131], v[92:95]\n\t" \
        "v_cvt_pk_bf16_f32 v69, v70, v71\n\t" \
        "v_cvt_pk_bf16_f32 v70, v72, v73\n\t" \
        "v_mfma_f32_16x16x32_bf16 v[96:99], a[52:55], v[128:131], v[96:99]\n\t" \
        "v_cvt_pk_bf16_f32 v71, v74, v75\n\t" \
        "v_permlane16_swap_b32 v68, v70\n\t" \
        "v_mfma_f32_16x16x32_bf16 v[100:103], a[56:59], v[128:131], v[100:103]\n\t" \
        "ds_read_b128 v[128:131], v15 offset:43648\n\t" \
        "v_permlane16_swap_b32 v69, v71\n\t" \
        "v_mfma_f32_16x16x32_bf16 v[92:95], a[60:63], v[132:135], v[92:95]\n\t" \
        "ds_read_b64 v[78:79], v12 offset:6144\n\t" \
        "v_mfma_f32_16x16x32_bf16 v[96:99], a[64:67], v[132:135], v[96:99]\n\t" \
        "v_mfma_f32_16x16x32_bf16 v[100:103], a[68:71], v[132:135], v[100:103]\n\t" \
        "ds_read_b128 v[132:135], v15 offset:43712\n\t" \
        "v_mfma_f32_16x16x32_bf16 v[92:95], a[72:75], v[136:139], v[92:95]\n\t" \
        "v_mfma_f32_16x16x32_bf16 v[96:99], a[76:79], v[136:139], v[96:99]\n\t" \
        "v_mfma_f32_16x16x32_bf16 v[100:103], a[80:83], v[136:139], v[100:103]\n\t" \
        "ds_read_b128 v[136:139], v15 offset:43776\n\t" \
        "v_mfma_f32_16x16x32_bf16 v[92:95], a[84:87], v[140:143], v[92:95]\n\t" \
        "v_mfma_f32_16x16x32_bf16 v[96:99], a[88:91], v[140:143], v[96:99]\n\t" \
        "v_mfma_f32_16x16x32_bf16 v[100:103], a[92:95], v[140:143], v[100:103]\n\t" \
        "ds_read_b128 v[140:143], v16 offset:43648\n\t" \
        "s_waitcnt lgkmcnt(3)\n\t" \
        "v_permlane16_swap_b32 v76, v78\n\t" \
        "v_mfma_f32_16x16x32_bf16 v[92:95], a[96:99], v[144:147], v[92:95]\n\t" \
        "s_add_u32 s56, s64, 0x54000\n\t" \
        "buffer_store_dwordx4 v[80:83], v9, s[28:31], s56 offen sc1\n\t" \
        "v_mfma_f32_16x16x32_bf16 v[96:99], a[100:103], v[144:147], v[96:99]\n\t" \
        "v_permlane16_swap_b32 v77, v79\n\t" \
        "v_mfma_f32_16x16x32_bf16 v[100:103], a[104:107], v[144:147], v[100:103]\n\t" \
        "ds_read_b128 v[144:147], v16 offset:43712\n\t" \
        "v_mfma_f32_16x16x32_bf16 v[92:95], a[108:111], v[116:119], v[92:95]\n\t" \
        "v_mfma_f32_16x16x32_bf16 v[96:99], a[112:115], v[116:119], v[96:99]\n\t" \
        "v_mfma_f32_16x16x32_bf16 v[100:103], a[116:119], v[116:119], v[100:103]\n\t" \
        "ds_read_b128 v[116:119], v16 offset:43776\n\t" \
        "v_mfma_f32_16x16x32_bf16 v[92:95], a[120:123], v[120:123], v[92:95]\n\t" \
        "v_mfma_f32_16x16x32_bf16 v[96:99], a[124:127], v[120:123], v[96:99]\n\t" \
        "v_mfma_f32_16x16x32_bf16 v[100:103], a[128:131], v[120:123], v[100:103]\n\t" \
        "ds_read_b128 v[120:123], v14 offset:47616\n\t" \
        "v_mfma_f32_16x16x32_bf16 v[92:95], a[132:135], v[124:127], v[92:95]\n\t" \
        "s_add_u32 s56, s64, 0x3f000\n\t" \
        "buffer_store_dwordx4 v[68:71], v9, s[28:31], s56 offen sc1\n\t" \
        "v_mfma_f32_16x16x32_bf16 v[96:99], a[136:139], v[124:127], v[96:99]\n\t" \
        "v_mfma_f32_16x16x32_bf16 v[100:103], a[140:143], v[124:127], v[100:103]\n\t" \
        "ds_read_b128 v[124:127], v14 offset:47680\n\t" \
        "v_mfma_f32_16x16x32_bf16 v[92:95], a[144:147], v[128:131], v[92:95]\n\t" \
        "v_mfma_f32_16x16x32_bf16 v[96:99], a[148:151], v[128:131], v[96:99]\n\t" \
        "v_mfma_f32_16x16x32_bf16 v[100:103], a[152:155], v[128:131], v[100:103]\n\t" \
        "ds_read_b128 v[128:131], v14 offset:47744\n\t" \
        "s_waitcnt lgkmcnt(3)\n\t" \
        "v_mfma_f32_16x16x32_bf16 v[92:95], a[156:159], v[132:135], v[92:95]\n\t" \
        "v_mfma_f32_16x16x32_bf16 v[96:99], a[160:163], v[132:135], v[96:99]\n\t" \
        "s_add_u32 s56, s64, 0x3f000\n\t" \
        "buffer_store_dwordx4 v[76:79], v10, s[28:31], s56 offen sc1\n\t" \
        "v_mfma_f32_16x16x32_bf16 v[100:103], a[164:167], v[132:135], v[100:103]\n\t" \
        "ds_read_b128 v[132:135], v15 offset:47616\n\t" \
        "v_mfma_f32_16x16x32_bf16 v[92:95], a[168:171], v[136:139], v[92:95]\n\t" \
        "v_mfma_f32_16x16x32_bf16 v[96:99], a[172:175], v[136:139], v[96:99]\n\t" \
        "v_mfma_f32_16x16x32_bf16 v[100:103], a[176:179], v[136:139], v[100:103]\n\t" \
        "ds_read_b128 v[136:139], v15 offset:47680\n\t" \
        "v_mfma_f32_16x16x32_bf16 v[92:95], a[180:183], v[140:143], v[92:95]\n\t" \
        "v_mfma_f32_16x16x32_bf16 v[96:99], a[184:187], v[140:143], v[96:99]\n\t" \
        "v_mfma_f32_16x16x32_bf16 v[100:103], a[188:191], v[140:143], v[100:103]\n\t" \
        "ds_read_b128 v[140:143], v15 offset:47744\n\t" \
        "v_mfma_f32_16x16x32_bf16 v[92:95], a[192:195], v[144:147], v[92:95]\n\t" \
        "v_mfma_f32_16x16x32_bf16 v[96:99], a[196:199], v[144:147], v[96:99]\n\t" \
        "v_mfma_f32_16x16x32_bf16 v[100:103], a[200:203], v[144:147], v[100:103]\n\t" \
        "ds_read_b128 v[144:147], v16 offset:47616\n\t" \
        "v_mfma_f32_16x16x32_bf16 v[92:95], a[204:207], v[116:119], v[92:95]\n\t" \
        "v_mfma_f32_16x16x32_bf16 v[96:99], a[208:211], v[116:119], v[96:99]\n\t" \
        "v_mfma_f32_16x16x32_bf16 v[100:103], a[212:215], v[116:119], v[100:103]\n\t" \
        "ds_read_b128 v[116:119], v16 offset:47680\n\t" \
        "s_waitcnt lgkmcnt(3)\n\t" \
        "v_mfma_f32_16x16x32_bf16 v[92:95], a[216:219], v[120:123], v[92:95]\n\t" \
        "v_mfma_f32_16x16x32_bf16 v[96:99], a[220:223], v[120:123], v[96:99]\n\t" \
        "v_mfma_f32_16x16x32_bf16 v[100:103], a[224:227], v[120:123], v[100:103]\n\t" \
        "ds_read_b128 v[120:123], v16 offset:47744\n\t" \
        "v_mfma_f32_16x16x32_bf16 v[92:95], a[228:231], v[124:127], v[92:95]\n\t" \
        "v_mfma_f32_16x16x32_bf16 v[96:99], a[232:235], v[124:127], v[96:99]\n\t" \
        "v_mfma_f32_16x16x32_bf16 v[100:103], a[236:239], v[124:127], v[100:103]\n\t" \
        "ds_read_b128 v[124:127], v14 offset:47616\n\t" \
        "v_mfma_f32_16x16x32_bf16 v[92:95], a[240:243], v[128:131], v[92:95]\n\t" \
        "v_mfma_f32_16x16x32_bf16 v[96:99], a[244:247], v[128:131], v[96:99]\n\t" \
        "v_mfma_f32_16x16x32_bf16 v[100:103], a[248:251], v[128:131], v[100:103]\n\t" \
        "ds_read_b128 v[128:131], v14 offset:47680\n\t" \
        "v_mfma_f32_16x16x32_bf16 v[92:95], a[252:255], v[132:135], v[92:95]\n\t" \
        "v_mfma_f32_16x16x32_bf16 v[96:99], v[188:191], v[132:135], v[96:99]\n\t" \
        "v_mfma_f32_16x16x32_bf16 v[100:103], v[192:195], v[132:135], v[100:103]\n\t" \
        "ds_read_b128 v[132:135], v14 offset:47744\n\t" \
        "v_mfma_f32_16x16x32_bf16 v[92:95], v[196:199], v[136:139], v[92:95]\n\t" \
        "v_mfma_f32_16x16x32_bf16 v[96:99], v[200:203], v[136:139], v[96:99]\n\t" \
        "v_mfma_f32_16x16x32_bf16 v[100:103], v[204:207], v[136:139], v[100:103]\n\t" \
        "ds_read_b128 v[136:139], v15 offset:47616\n\t" \
        "s_waitcnt lgkmcnt(3)\n\t" \
        "v_mfma_f32_16x16x32_bf16 v[92:95], v[208:211], v[140:143], v[92:95]\n\t" \
        "v_mfma_f32_16x16x32_bf16 v[96:99], v[212:215], v[140:143], v[96:99]\n\t" \
        "v_mfma_f32_16x16x32_bf16 v[100:103], v[216:219], v[140:143], v[100:103]\n\t" \
        "ds_read_b128 v[140:143], v15 offset:47680\n\t" \
        "v_mfma_f32_16x16x32_bf16 v[92:95], v[220:223], v[144:147], v[92:95]\n\t" \
        "v_mfma_f32_16x16x32_bf16 v[96:99], v[224:227], v[144:147], v[96:99]\n\t" \
        "v_mfma_f32_16x16x32_bf16 v[100:103], v[228:231], v[144:147], v[100:103]\n\t" \
        "ds_read_b128 v[144:147], v15 offset:47744\n\t" \
        "v_mfma_f32_16x16x32_bf16 v[92:95], v[232:235], v[116:119], v[92:95]\n\t" \
        "v_mfma_f32_16x16x32_bf16 v[96:99], v[236:239], v[116:119], v[96:99]\n\t" \
        "v_mfma_f32_16x16x32_bf16 v[100:103], v[240:243], v[116:119], v[100:103]\n\t" \
        "ds_read_b128 v[116:119], v16 offset:47616\n\t" \
        "v_mfma_f32_16x16x32_bf16 v[92:95], v[244:247], v[120:123], v[92:95]\n\t" \
        "v_mfma_f32_16x16x32_bf16 v[96:99], v[248:251], v[120:123], v[96:99]\n\t" \
        "v_mfma_f32_16x16x32_bf16 v[100:103], v[252:255], v[120:123], v[100:103]\n\t" \
        "s_bitcmp0_b32 s34, 4\n\t" \
        "s_cbranch_scc1 .LR14%=\n\t" \
        "ds_read_b128 v[120:123], v16 offset:47680\n\t" \
        "v_mfma_f32_16x16x32_bf16 v[104:107], a[0:3], v[124:127], 0\n\t" \
        "v_mfma_f32_16x16x32_bf16 v[108:111], a[4:7], v[124:127], 0\n\t" \
        "v_mfma_f32_16x16x32_bf16 v[112:115], a[8:11], v[124:127], 0\n\t" \
        "s_nop 3\n\t" \
        "v_cvt_pk_bf16_f32 v100, v100, v101\n\t" \
        "v_cvt_pk_bf16_f32 v101, v102, v103\n\t" \
        "s_waitcnt lgkmcnt(2)\n\t" \
        "v_mfma_f32_16x16x32_bf16 v[104:107], a[12:15], v[128:131], v[104:107]\n\t" \
        "ds_write_b64 v11, v[100:101] offset:10240\n\t" \
        "v_cvt_pk_bf16_f32 v92, v92, v93\n\t" \
        "v_mfma_f32_16x16x32_bf16 v[108:111], a[16:19], v[128:131], v[108:111]\n\t" \
        "ds_read_b128 v[124:127], v16 offset:47744\n\t" \
        "v_cvt_pk_bf16_f32 v93, v94, v95\n\t" \
        "v_mfma_f32_16x16x32_bf16 v[112:115], a[20:23], v[128:131], v[112:115]\n\t" \
        "ds_read_b128 v[128:131], v14 offset:51584\n\t" \
        "v_cvt_pk_bf16_f32 v94, v96, v97\n\t" \
        "v_mfma_f32_16x16x32_bf16 v[104:107], a[24:27], v[132:135], v[104:107]\n\t" \
        "v_cvt_pk_bf16_f32 v95, v98, v99\n\t" \
        "v_permlane16_swap_b32 v92, v94\n\t" \
        "v_mfma_f32_16x16x32_bf16 v[108:111], a[28:31], v[132:135], v[108:111]\n\t" \
        "v_permlane16_swap_b32 v93, v95\n\t" \
        "v_mfma_f32_16x16x32_bf16 v[112:115], a[32:35], v[132:135], v[112:115]\n\t" \
        "ds_read_b128 v[132:135], v14 offset:51648\n\t" \
        "v_mfma_f32_16x16x32_bf16 v[104:107], a[36:39], v[136:139], v[104:107]\n\t" \
        "v_mfma_f32_16x16x32_bf16 v[108:111], a[40:43], v[136:139], v[108:111]\n\t" \
        "v_mfma_f32_16x16x32_bf16 v[112:115], a[44:47], v[136:139], v[112:115]\n\t" \
        "ds_read_b128 v[136:139], v14 offset:51712\n\t" \
        "v_mfma_f32_16x16x32_bf16 v[104:107], a[48:51], v[140:143], v[104:107]\n\t" \
        "v_mfma_f32_16x16x32_bf16 v[108:111], a[52:55], v[140:143], v[108:111]\n\t" \
        "v_mfma_f32_16x16x32_bf16 v[112:115], a[56:59], v[140:143], v[112:115]\n\t" \
        "ds_read_b128 v[140:143], v15 offset:51584\n\t" \
        "v_mfma_f32_16x16x32_bf16 v[104:107], a[60:63], v[144:147], v[104:107]\n\t" \
        "v_mfma_f32_16x16x32_bf16 v[108:111], a[64:67], v[144:147], v[108:111]\n\t" \
        "v_mfma_f32_16x16x32_bf16 v[112:115], a[68:71], v[144:147], v[112:115]\n\t" \
        "ds_read_b128 v[144:147], v15 offset:51648\n\t" \
        "s_waitcnt lgkmcnt(3)\n\t" \
        "v_mfma_f32_16x16x32_bf16 v[104:107], a[72:75], v[116:119], v[104:107]\n\t" \
        "v_mfma_f32_16x16x32_bf16 v[108:111], a[76:79], v[116:119], v[108:111]\n\t" \
        "v_mfma_f32_16x16x32_bf16 v[112:115], a[80:83], v[116:119], v[112:115]\n\t" \
        "ds_read_b128 v[116:119], v15 offset:51712\n\t" \
        "v_mfma_f32_16x16x32_bf16 v[104:107], a[84:87], v[120:123], v[104:107]\n\t" \
        "v_mfma_f32_16x16x32_bf16 v[108:111], a[88:91], v[120:123], v[108:111]\n\t" \
        "v_mfma_f32_16x16x32_bf16 v[112:115], a[92:95], v[120:123], v[112:115]\n\t" \
        "ds_read_b128 v[120:123], v16 offset:51584\n\t" \
        "v_mfma_f32_16x16x32_bf16 v[104:107], a[96:99], v[124:127], v[104:107]\n\t" \
        "s_add_u32 s56, s64, 0x69000\n\t" \
        "buffer_store_dwordx4 v[92:95], v9, s[28:31], s56 offen sc1\n\t" \
        "v_mfma_f32_16x16x32_bf16 v[108:111], a[100:103], v[124:127], v[108:111]\n\t" \
        "v_mfma_f32_16x16x32_bf16 v[112:115], a[104:107], v[124:127], v[112:115]\n\t" \
        "ds_read_b128 v[124:127], v16 offset:51648\n\t" \
        "v_mfma_f32_16x16x32_bf16 v[104:107], a[108:111], v[128:131], v[104:107]\n\t" \
        "v_mfma_f32_16x16x32_bf16 v[108:111], a[112:115], v[128:131], v[108:111]\n\t" \
        "v_mfma_f32_16x16x32_bf16 v[112:115], a[116:119], v[128:131], v[112:115]\n\t" \
        "ds_read_b128 v[128:131], v16 offset:51712\n\t" \
        "v_mfma_f32_16x16x32_bf16 v[104:107], a[120:123], v[132:135], v[104:107]\n\t" \
        "v_mfma_f32_16x16x32_bf16 v[108:111], a[124:127], v[132:135], v[108:111]\n\t" \
        "v_mfma_f32_16x16x32_bf16 v[112:115], a[128:131], v[132:135], v[112:115]\n\t" \
        "ds_read_b128 v[132:135], v14 offset:55552\n\t" \
        "s_waitcnt lgkmcnt(3)\n\t" \
        "v_mfma_f32_16x16x32_bf16 v[104:107], a[132:135], v[136:139], v[104:107]\n\t" \
        "v_mfma_f32_16x16x32_bf16 v[108:111], a[136:139], v[136:139], v[108:111]\n\t" \
        "v_mfma_f32_16x16x32_bf16 v[112:115], a[140:143], v[136:139], v[112:115]\n\t" \
        "ds_read_b128 v[136:139], v14 offset:55616\n\t" \
        "v_mfma_f32_16x16x32_bf16 v[104:107], a[144:147], v[140:143], v[104:107]\n\t" \
        "v_mfma_f32_16x16x32_bf16 v[108:111], a[148:151], v[140:143], v[108:111]\n\t" \
        "v_mfma_f32_16x16x32_bf16 v[112:115], a[152:155], v[140:143], v[112:115]\n\t" \
        "ds_read_b128 v[140:143], v14 offset:55680\n\t" \
        "v_mfma_f32_16x16x32_bf16 v[104:107], a[156:159], v[144:147], v[104:107]\n\t" \
        "v_mfma_f32_16x16x32_bf16 v[108:111], a[160:163], v[144:147], v[108:111]\n\t" \
        "v_mfma_f32_16x16x32_bf16 v[112:115], a[164:167], v[144:147], v[112:115]\n\t" \
        "ds_read_b128 v[144:147], v15 offset:55552\n\t" \
        "v_mfma_f32_16x16x32_bf16 v[104:107], a[168:171], v[116:119], v[104:107]\n\t" \
        "v_mfma_f32_16x16x32_bf16 v[108:111], a[172:175], v[116:119], v[108:111]\n\t" \
        "v_mfma_f32_16x16x32_bf16 v[112:115], a[176:179], v[116:119], v[112:115]\n\t" \
        "ds_read_b128 v[116:119], v15 offset:55616\n\t" \
        "v_mfma_f32_16x16x32_bf16 v[104:107], a[180:183], v[120:123], v[104:107]\n\t" \
        "v_mfma_f32_16x16x32_bf16 v[108:111], a[184:187], v[120:123], v[108:111]\n\t" \
        "v_mfma_f32_16x16x32_bf16 v[112:115], a[188:191], v[120:123], v[112:115]\n\t" \
        "ds_read_b128 v[120:123], v15 offset:55680\n\t" \
        "s_waitcnt lgkmcnt(3)\n\t" \
        "v_mfma_f32_16x16x32_bf16 v[104:107], a[192:195], v[124:127], v[104:107]\n\t" \
        "v_mfma_f32_16x16x32_bf16 v[108:111], a[196:199], v[124:127], v[108:111]\n\t" \
        "v_mfma_f32_16x16x32_bf16 v[112:115], a[200:203], v[124:127], v[112:115]\n\t" \
        "ds_read_b128 v[124:127], v16 offset:55552\n\t" \
        "v_mfma_f32_16x16x32_bf16 v[104:107], a[204:207], v[128:131], v[104:107]\n\t" \
        "v_mfma_f32_16x16x32_bf16 v[108:111], a[208:211], v[128:131], v[108:111]\n\t" \
        "v_mfma_f32_16x16x32_bf16 v[112:115], a[212:215], v[128:131], v[112:115]\n\t" \
        "ds_read_b128 v[128:131], v16 offset:55616\n\t" \
        "v_mfma_f32_16x16x32_bf16 v[104:107], a[216:219], v[132:135], v[104:107]\n\t" \
        "v_mfma_f32_16x16x32_bf16 v[108:111], a[220:223], v[132:135], v[108:111]\n\t" \
        "v_mfma_f32_16x16x32_bf16 v[112:115], a[224:227], v[132:135], v[112:115]\n\t" \
        "ds_read_b128 v[132:135], v16 offset:55680\n\t" \
        "v_mfma_f32_16x16x32_bf16 v[104:107], a[228:231], v[136:139], v[104:107]\n\t" \
        "v_mfma_f32_16x16x32_bf16 v[108:111], a[232:235], v[136:139], v[108:111]\n\t" \
        "v_mfma_f32_16x16x32_bf16 v[112:115], a[236:239], v[136:139], v[112:115]\n\t" \
        "ds_read_b128 v[136:139], v14 offset:55552\n\t" \
        "v_mfma_f32_16x16x32_bf16 v[104:107], a[240:243], v[140:143], v[104:107]\n\t" \
        "v_mfma_f32_16x16x32_bf16 v[108:111], a[244:247], v[140:143], v[108:111]\n\t" \
        "v_mfma_f32_16x16x32_bf16 v[112:115], a[248:251], v[140:143], v[112:115]\n\t" \
        "ds_read_b128 v[140:143], v14 offset:55616\n\t" \
        "s_waitcnt lgkmcnt(3)\n\t" \
        "v_mfma_f32_16x16x32_bf16 v[104:107], a[252:255], v[144:147], v[104:107]\n\t" \
        "v_mfma_f32_16x16x32_bf16 v[108:111], v[188:191], v[144:147], v[108:111]\n\t" \
        "v_mfma_f32_16x16x32_bf16 v[112:115], v[192:195], v[144:147], v[112:115]\n\t" \
        "ds_read_b128 v[144:147], v14 offset:55680\n\t" \
        "v_mfma_f32_16x16x32_bf16 v[104:107], v[196:199], v[116:119], v[104:107]\n\t" \
        "ds_read_b128 v[44:47], v14 offset:63488\n\t" \
        "v_mfma_f32_16x16x32_bf16 v[108:111], v[200:203], v[116:119], v[108:111]\n\t" \
        "v_mfma_f32_16x16x32_bf16 v[112:115], v[204:207], v[116:119], v[112:115]\n\t" \
        "ds_read_b128 v[116:119], v15 offset:55552\n\t" \
        "v_mfma_f32_16x16x32_bf16 v[104:107], v[208:211], v[120:123], v[104:107]\n\t" \
        "ds_read_b128 v[48:51], v14 offset:63552\n\t" \
        "v_mfma_f32_16x16x32_bf16 v[108:111], v[212:215], v[120:123], v[108:111]\n\t" \
        "v_mfma_f32_16x16x32_bf16 v[112:115], v[216:219], v[120:123], v[112:115]\n\t" \
        "ds_read_b128 v[120:123], v15 offset:55616\n\t" \
        "v_mfma_f32_16x16x32_bf16 v[104:107], v[220:223], v[124:127], v[104:107]\n\t" \
        "ds_read_b128 v[52:55], v14 offset:63616\n\t" \
        "v_mfma_f32_16x16x32_bf16 v[108:111], v[224:227], v[124:127], v[108:111]\n\t" \
        "v_mfma_f32_16x16x32_bf16 v[112:115], v[228:231], v[124:127], v[112:115]\n\t" \
        "ds_read_b128 v[124:127], v15 offset:55680\n\t" \
        "v_mfma_f32_16x16x32_bf16 v[104:107], v[232:235], v[128:131], v[104:107]\n\t" \
        "ds_read_b128 v[56:59], v15 offset:63488\n\t" \
        "v_mfma_f32_16x16x32_bf16 v[108:111], v[236:239], v[128:131], v[108:111]\n\t" \
        "v_mfma_f32_16x16x32_bf16 v[112:115], v[240:243], v[128:131], v[112:115]\n\t" \
        "ds_read_b128 v[128:131], v16 offset:55552\n\t" \
        "s_waitcnt lgkmcnt(5)\n\t" \
        "v_mfma_f32_16x16x32_bf16 v[104:107], v[244:247], v[132:135], v[104:107]\n\t" \
        "ds_read_b128 v[60:63], v15 offset:63552\n\t" \
        "v_mfma_f32_16x16x32_bf16 v[108:111], v[248:251], v[132:135], v[108:111]\n\t" \
        "v_mfma_f32_16x16x32_bf16 v[112:115], v[252:255], v[132:135], v[112:115]\n\t" \
        "ds_read_b128 v[132:135], v16 offset:55616\n\t" \
        "v_mfma_f32_16x16x32_bf16 v[32:35], a[0:3], v[136:139], 0\n\t" \
        "ds_read_b128 v[64:67], v15 offset:63616\n\t" \
        "v_mfma_f32_16x16x32_bf16 v[36:39], a[4:7], v[136:139], 0\n\t" \
        "v_mfma_f32_16x16x32_bf16 v[40:43], a[8:11], v[136:139], 0\n\t" \
        "s_nop 4\n\t" \
        "v_cvt_pk_bf16_f32 v112, v112, v113\n\t" \
        "v_cvt_pk_bf16_f32 v113, v114, v115\n\t" \
        "v_mfma_f32_16x16x32_bf16 v[32:35], a[12:15], v[140:143], v[32:35]\n\t" \
        "ds_write_b64 v11, v[112:113] offset:12288\n\t" \
        "v_cvt_pk_bf16_f32 v104, v104, v105\n\t" \
        "v_mfma_f32_16x16x32_bf16 v[36:39], a[16:19], v[140:143], v[36:39]\n\t" \
        "ds_read_b128 v[136:139], v16 offset:55680\n\t" \
        "v_cvt_pk_bf16_f32 v105, v106, v107\n\t" \
        "v_mfma_f32_16x16x32_bf16 v[40:43], a[20:23], v[140:143], v[40:43]\n\t" \
        "ds_read_b128 v[140:143], v14 offset:59520\n\t" \
        "v_cvt_pk_bf16_f32 v106, v108, v109\n\t" \
        "v_mfma_f32_16x16x32_bf16 v[32:35], a[24:27], v[144:147], v[32:35]\n\t" \
        "v_cvt_pk_bf16_f32 v107, v110, v111\n\t" \
        "v_permlane16_swap_b32 v104, v106\n\t" \
        "v_mfma_f32_16x16x32_bf16 v[36:39], a[28:31], v[144:147], v[36:39]\n\t" \
        "ds_read_b128 v[68:71], v16 offset:63488\n\t" \
        "v_permlane16_swap_b32 v105, v107\n\t" \
        "v_mfma_f32_16x16x32_bf16 v[40:43], a[32:35], v[144:147], v[40:43]\n\t" \
        "ds_read_b128 v[144:147], v14 offset:59584\n\t" \
        "v_mfma_f32_16x16x32_bf16 v[32:35], a[36:39], v[116:119], v[32:35]\n\t" \
        "ds_read_b128 v[72:75], v16 offset:63552\n\t" \
        "v_mfma_f32_16x16x32_bf16 v[36:39], a[40:43], v[116:119], v[36:39]\n\t" \
        "ds_read_b128 v[76:79], v16 offset:63616\n\t" \
        "v_mfma_f32_16x16x32_bf16 v[40:43], a[44:47], v[116:119], v[40:43]\n\t" \
        "ds_read_b128 v[116:119], v14 offset:59648\n\t" \
        "s_waitcnt lgkmcnt(7)\n\t" \
        "v_mfma_f32_16x16x32_bf16 v[32:35], a[48:51], v[120:123], v[32:35]\n\t" \
        "v_mfma_f32_16x16x32_bf16 v[36:39], a[52:55], v[120:123], v[36:39]\n\t" \
        "s_barrier\n\t" \
        "ds_read_b64 v[102:103], v12 offset:10240\n\t" \
        "v_mfma_f32_16x16x32_bf16 v[40:43], a[56:59], v[120:123], v[40:43]\n\t" \
        "ds_read_b128 v[120:123], v15 offset:59520\n\t" \
        "v_mfma_f32_16x16x32_bf16 v[32:35], a[60:63], v[124:127], v[32:35]\n\t" \
        "ds_read_b64 v[114:115], v12 offset:12288\n\t" \
        "v_mfma_f32_16x16x32_bf16 v[36:39], a[64:67], v[124:127], v[36:39]\n\t" \
        "v_mfma_f32_16x16x32_bf16 v[40:43], a[68:71], v[124:127], v[40:43]\n\t" \
        "ds_read_b128 v[124:127], v15 offset:59584\n\t" \
        "v_mfma_f32_16x16x32_bf16 v[32:35], a[72:75], v[128:131], v[32:35]\n\t" \
        "v_mfma_f32_16x16x32_bf16 v[36:39], a[76:79], v[128:131], v[36:39]\n\t" \
        "v_mfma_f32_16x16x32_bf16 v[40:43], a[80:83], v[128:131], v[40:43]\n\t" \
        "ds_read_b128 v[128:131], v15 offset:59648\n\t" \
        "v_mfma_f32_16x16x32_bf16 v[32:35], a[84:87], v[132:135], v[32:35]\n\t" \
        "s_waitcnt lgkmcnt(4)\n\t" \
        "v_permlane16_swap_b32 v100, v102\n\t" \
        "v_permlane16_swap_b32 v101, v103\n\t" \
        "v_mfma_f32_16x16x32_bf16 v[36:39], a[88:91], v[132:135], v[36:39]\n\t" \
        "v_mfma_f32_16x16x32_bf16 v[40:43], a[92:95], v[132:135], v[40:43]\n\t" \
        "s_add_u32 s56, s64, 0x69000\n\t" \
        "buffer_store_dwordx4 v[100:103], v10, s[28:31], s56 offen sc1\n\t" \
        "v_mfma_f32_16x16x32_bf16 v[32:35], a[96:99], v[136:139], v[32:35]\n\t" \
        "s_add_u32 s56, s64, 0x7e000\n\t" \
        "buffer_store_dwordx4 v[104:107], v9, s[28:31], s56 offen sc1\n\t" \
        "v_mfma_f32_16x16x32_bf16 v[36:39], a[100:103], v[136:139], v[36:39]\n\t" \
        "ds_read_b128 v[132:135], v16 offset:59520\n\t" \
        "s_waitcnt lgkmcnt(2)\n\t" \
        "v_permlane16_swap_b32 v112, v114\n\t" \
        "v_mfma_f32_16x16x32_bf16 v[40:43], a[104:107], v[136:139], v[40:43]\n\t" \
        "ds_read_b128 v[136:139], v16 offset:59584\n\t" \
        "v_permlane16_swap_b32 v113, v115\n\t" \
        "v_mfma_f32_16x16x32_bf16 v[32:35], a[108:111], v[140:143], v[32:35]\n\t" \
        "ds_read_b64 v[90:91], v12 offset:8192\n\t" \
        "v_mfma_f32_16x16x32_bf16 v[36:39], a[112:115], v[140:143], v[36:39]\n\t" \
        "v_mfma_f32_16x16x32_bf16 v[40:43], a[116:119], v[140:143], v[40:43]\n\t" \
        "ds_read_b128 v[140:143], v16 offset:59648\n\t" \
        "v_mfma_f32_16x16x32_bf16 v[40:43], a[128:131], v[144:147], v[40:43]\n\t" \
        "v_mfma_f32_16x16x32_bf16 v[40:43], a[140:143], v[116:119], v[40:43]\n\t" \
        "s_add_u32 s56, s64, 0x7e000\n\t" \
        "buffer_store_dwordx4 v[112:115], v10, s[28:31], s56 offen sc1\n\t" \
        "v_mfma_f32_16x16x32_bf16 v[40:43], a[152:155], v[120:123], v[40:43]\n\t" \
        "v_mfma_f32_16x16x32_bf16 v[40:43], a[164:167], v[124:127], v[40:43]\n\t" \
        "s_waitcnt lgkmcnt(3)\n\t" \
        "v_mfma_f32_16x16x32_bf16 v[40:43], a[176:179], v[128:131], v[40:43]\n\t" \
        "v_mfma_f32_16x16x32_bf16 v[40:43], a[188:191], v[132:135], v[40:43]\n\t" \
        "s_waitcnt lgkmcnt(1)\n\t" \
        "v_permlane16_swap_b32 v88, v90\n\t" \
        "v_permlane16_swap_b32 v89, v91\n\t" \
        "v_mfma_f32_16x16x32_bf16 v[40:43], a[200:203], v[136:139], v[40:43]\n\t" \
        "s_waitcnt lgkmcnt(0)\n\t" \
        "v_mfma_f32_16x16x32_bf16 v[40:43], a[212:215], v[140:143], v[40:43]\n\t" \
        "s_add_u32 s56, s64, 0x54000\n\t" \
        "buffer_store_dwordx4 v[88:91], v10, s[28:31], s56 offen sc1\n\t" \
        "v_mfma_f32_16x16x32_bf16 v[40:43], a[224:227], v[44:47], v[40:43]\n\t" \
        "v_mfma_f32_16x16x32_bf16 v[40:43], a[236:239], v[48:51], v[40:43]\n\t" \
        "v_mfma_f32_16x16x32_bf16 v[40:43], a[248:251], v[52:55], v[40:43]\n\t" \
        "v_mfma_f32_16x16x32_bf16 v[40:43], v[192:195], v[56:59], v[40:43]\n\t" \
        "v_mfma_f32_16x16x32_bf16 v[40:43], v[204:207], v[60:63], v[40:43]\n\t" \
        "v_mfma_f32_16x16x32_bf16 v[40:43], v[216:219], v[64:67], v[40:43]\n\t" \
        "v_mfma_f32_16x16x32_bf16 v[40:43], v[228:231], v[68:71], v[40:43]\n\t" \
        "v_mfma_f32_16x16x32_bf16 v[40:43], v[240:243], v[72:75], v[40:43]\n\t" \
        "v_mfma_f32_16x16x32_bf16 v[40:43], v[252:255], v[76:79], v[40:43]\n\t" \
        "v_mfma_f32_16x16x32_bf16 v[32:35], a[120:123], v[144:147], v[32:35]\n\t" \
        "v_mfma_f32_16x16x32_bf16 v[36:39], a[124:127], v[144:147], v[36:39]\n\t" \
        "v_mfma_f32_16x16x32_bf16 v[32:35], a[132:135], v[116:119], v[32:35]\n\t" \
        "v_mfma_f32_16x16x32_bf16 v[36:39], a[136:139], v[116:119], v[36:39]\n\t" \
        "v_mfma_f32_16x16x32_bf16 v[32:35], a[144:147], v[120:123], v[32:35]\n\t" \
        "v_mfma_f32_16x16x32_bf16 v[36:39], a[148:151], v[120:123], v[36:39]\n\t" \
        "v_mfma_f32_16x16x32_bf16 v[32:35], a[156:159], v[124:127], v[32:35]\n\t" \
        "v_mfma_f32_16x16x32_bf16 v[36:39], a[160:163], v[124:127], v[36:39]\n\t" \
        "v_mfma_f32_16x16x32_bf16 v[32:35], a[168:171], v[128:131], v[32:35]\n\t" \
        "v_mfma_f32_16x16x32_bf16 v[36:39], a[172:175], v[128:131], v[36:39]\n\t" \
        "v_cvt_pk_bf16_f32 v40, v40, v41\n\t" \
        "v_cvt_pk_bf16_f32 v41, v42, v43\n\t" \
        "v_mfma_f32_16x16x32_bf16 v[32:35], a[180:183], v[132:135], v[32:35]\n\t" \
        "ds_write_b64 v11, v[40:41] offset:14336\n\t" \
        "v_mfma_f32_16x16x32_bf16 v[36:39], a[184:187], v[132:135], v[36:39]\n\t" \
        "v_mfma_f32_16x16x32_bf16 v[32:35], a[192:195], v[136:139], v[32:35]\n\t" \
        "v_mfma_f32_16x16x32_bf16 v[36:39], a[196:199], v[136:139], v[36:39]\n\t" \
        "v_mfma_f32_16x16x32_bf16 v[32:35], a[204:207], v[140:143], v[32:35]\n\t" \
        "v_mfma_f32_16x16x32_bf16 v[36:39], a[208:211], v[140:143], v[36:39]\n\t" \
        "v_mfma_f32_16x16x32_bf16 v[32:35], a[216:219], v[44:47], v[32:35]\n\t" \
        "s_waitcnt lgkmcnt(0)\n\t" \
        "s_barrier\n\t" \
        "ds_read_b64 v[42:43], v12 offset:14336\n\t" \
        "v_mfma_f32_16x16x32_bf16 v[36:39], a[220:223], v[44:47], v[36:39]\n\t" \
        "v_mfma_f32_16x16x32_bf16 v[32:35], a[228:231], v[48:51], v[32:35]\n\t" \
        "v_mfma_f32_16x16x32_bf16 v[36:39], a[232:235], v[48:51], v[36:39]\n\t" \
        "v_mfma_f32_16x16x32_bf16 v[32:35], a[240:243], v[52:55], v[32:35]\n\t" \
        "v_mfma_f32_16x16x32_bf16 v[36:39], a[244:247], v[52:55], v[36:39]\n\t" \
        "v_mfma_f32_16x16x32_bf16 v[32:35], a[252:255], v[56:59], v[32:35]\n\t" \
        "v_mfma_f32_16x16x32_bf16 v[36:39], v[188:191], v[56:59], v[36:39]\n\t" \
        "v_mfma_f32_16x16x32_bf16 v[32:35], v[196:199], v[60:63], v[32:35]\n\t" \
        "s_waitcnt lgkmcnt(0)\n\t" \
        "v_permlane16_swap_b32 v40, v42\n\t" \
        "v_permlane16_swap_b32 v41, v43\n\t" \
        "v_mfma_f32_16x16x32_bf16 v[36:39], v[200:203], v[60:63], v[36:39]\n\t" \
        "v_mfma_f32_16x16x32_bf16 v[32:35], v[208:211], v[64:67], v[32:35]\n\t" \
        "s_add_u32 s56, s64, 0x93000\n\t" \
        "buffer_store_dwordx4 v[40:43], v10, s[28:31], s56 offen sc1\n\t" \
        "v_mfma_f32_16x16x32_bf16 v[36:39], v[212:215], v[64:67], v[36:39]\n\t" \
        "v_mfma_f32_16x16x32_bf16 v[32:35], v[220:223], v[68:71], v[32:35]\n\t" \
        "v_mfma_f32_16x16x32_bf16 v[36:39], v[224:227], v[68:71], v[36:39]\n\t" \
        "v_mfma_f32_16x16x32_bf16 v[32:35], v[232:235], v[72:75], v[32:35]\n\t" \
        "v_mfma_f32_16x16x32_bf16 v[36:39], v[236:239], v[72:75], v[36:39]\n\t" \
        "v_mfma_f32_16x16x32_bf16 v[32:35], v[244:247], v[76:79], v[32:35]\n\t" \
        "v_mfma_f32_16x16x32_bf16 v[36:39], v[248:251], v[76:79], v[36:39]\n\t" \
        "s_nop 8\n\t" \
        "v_cvt_pk_bf16_f32 v32, v32, v33\n\t" \
        "v_cvt_pk_bf16_f32 v33, v34, v35\n\t" \
        "v_cvt_pk_bf16_f32 v34, v36, v37\n\t" \
        "v_cvt_pk_bf16_f32 v35, v38, v39\n\t" \
        "s_nop 0\n\t" \
        "v_permlane16_swap_b32 v32, v34\n\t" \
        "v_permlane16_swap_b32 v33, v35\n\t" \
        "s_add_u32 s56, s64, 0x93000\n\t" \
        "buffer_store_dwordx4 v[32:35], v9, s[28:31], s56 offen sc1\n\t" \
        "s_endpgm\n\t" \
        ".LR14%=:\n\t" \
        "ds_read_b128 v[120:123], v16 offset:47680\n\t" \
        "v_mfma_f32_16x16x32_bf16 v[104:107], a[0:3], v[124:127], 0\n\t" \
        "ds_read_b128 v[44:47], v14 offset:55552\n\t" \
        "v_mfma_f32_16x16x32_bf16 v[108:111], a[4:7], v[124:127], 0\n\t" \
        "ds_read_b128 v[48:51], v14 offset:55616\n\t" \
        "v_mfma_f32_16x16x32_bf16 v[112:115], a[8:11], v[124:127], 0\n\t" \
        "s_nop 1\n\t" \
        "v_cvt_pk_bf16_f32 v100, v100, v101\n\t" \
        "v_cvt_pk_bf16_f32 v101, v102, v103\n\t" \
        "s_waitcnt lgkmcnt(4)\n\t" \
        "v_mfma_f32_16x16x32_bf16 v[104:107], a[12:15], v[128:131], v[104:107]\n\t" \
        "ds_write_b64 v11, v[100:101] offset:10240\n\t" \
        "v_cvt_pk_bf16_f32 v92, v92, v93\n\t" \
        "v_mfma_f32_16x16x32_bf16 v[108:111], a[16:19], v[128:131], v[108:111]\n\t" \
        "ds_read_b128 v[124:127], v16 offset:47744\n\t" \
        "v_cvt_pk_bf16_f32 v93, v94, v95\n\t" \
        "v_mfma_f32_16x16x32_bf16 v[112:115], a[20:23], v[128:131], v[112:115]\n\t" \
        "ds_read_b128 v[128:131], v14 offset:51584\n\t" \
        "v_cvt_pk_bf16_f32 v94, v96, v97\n\t" \
        "v_mfma_f32_16x16x32_bf16 v[104:107], a[24:27], v[132:135], v[104:107]\n\t" \
        "v_cvt_pk_bf16_f32 v95, v98, v99\n\t" \
        "v_permlane16_swap_b32 v92, v94\n\t" \
        "v_mfma_f32_16x16x32_bf16 v[108:111], a[28:31], v[132:135], v[108:111]\n\t" \
        "ds_read_b128 v[52:55], v14 offset:55680\n\t" \
        "v_permlane16_swap_b32 v93, v95\n\t" \
        "v_mfma_f32_16x16x32_bf16 v[112:115], a[32:35], v[132:135], v[112:115]\n\t" \
        "ds_read_b128 v[132:135], v14 offset:51648\n\t" \
        "v_mfma_f32_16x16x32_bf16 v[104:107], a[36:39], v[136:139], v[104:107]\n\t" \
        "ds_read_b128 v[56:59], v15 offset:55552\n\t" \
        "v_mfma_f32_16x16x32_bf16 v[108:111], a[40:43], v[136:139], v[108:111]\n\t" \
        "ds_read_b128 v[60:63], v15 offset:55616\n\t" \
        "v_mfma_f32_16x16x32_bf16 v[112:115], a[44:47], v[136:139], v[112:115]\n\t" \
        "ds_read_b128 v[136:139], v14 offset:51712\n\t" \
        "v_mfma_f32_16x16x32_bf16 v[104:107], a[48:51], v[140:143], v[104:107]\n\t" \
        "ds_read_b128 v[64:67], v15 offset:55680\n\t" \
        "v_mfma_f32_16x16x32_bf16 v[108:111], a[52:55], v[140:143], v[108:111]\n\t" \
        "s_waitcnt lgkmcnt(8)\n\t" \
        "s_barrier\n\t" \
        "ds_read_b64 v[90:91], v12 offset:8192\n\t" \
        "v_mfma_f32_16x16x32_bf16 v[112:115], a[56:59], v[140:143], v[112:115]\n\t" \
        "ds_read_b128 v[140:143], v15 offset:51584\n\t" \
        "v_mfma_f32_16x16x32_bf16 v[104:107], a[60:63], v[144:147], v[104:107]\n\t" \
        "ds_read_b128 v[68:71], v16 offset:55552\n\t" \
        "v_mfma_f32_16x16x32_bf16 v[108:111], a[64:67], v[144:147], v[108:111]\n\t" \
        "ds_read_b128 v[72:75], v16 offset:55616\n\t" \
        "v_mfma_f32_16x16x32_bf16 v[112:115], a[68:71], v[144:147], v[112:115]\n\t" \
        "ds_read_b128 v[144:147], v15 offset:51648\n\t" \
        "v_mfma_f32_16x16x32_bf16 v[104:107], a[72:75], v[116:119], v[104:107]\n\t" \
        "ds_read_b64 v[102:103], v12 offset:10240\n\t" \
        "v_mfma_f32_16x16x32_bf16 v[108:111], a[76:79], v[116:119], v[108:111]\n\t" \
        "ds_read_b128 v[76:79], v16 offset:55680\n\t" \
        "v_mfma_f32_16x16x32_bf16 v[112:115], a[80:83], v[116:119], v[112:115]\n\t" \
        "ds_read_b128 v[116:119], v15 offset:51712\n\t" \
        "v_mfma_f32_16x16x32_bf16 v[104:107], a[84:87], v[120:123], v[104:107]\n\t" \
        "s_waitcnt lgkmcnt(7)\n\t" \
        "v_permlane16_swap_b32 v88, v90\n\t" \
        "v_permlane16_swap_b32 v89, v91\n\t" \
        "v_mfma_f32_16x16x32_bf16 v[108:111], a[88:91], v[120:123], v[108:111]\n\t" \
        "v_mfma_f32_16x16x32_bf16 v[112:115], a[92:95], v[120:123], v[112:115]\n\t" \
        "s_add_u32 s56, s64, 0x54000\n\t" \
        "buffer_store_dwordx4 v[88:91], v10, s[28:31], s56 offen sc1\n\t" \
        "v_mfma_f32_16x16x32_bf16 v[104:107], a[96:99], v[124:127], v[104:107]\n\t" \
        "s_add_u32 s56, s64, 0x69000\n\t" \
        "buffer_store_dwordx4 v[92:95], v9, s[28:31], s56 offen sc1\n\t" \
        "v_mfma_f32_16x16x32_bf16 v[108:111], a[100:103], v[124:127], v[108:111]\n\t" \
        "ds_read_b128 v[120:123], v16 offset:51584\n\t" \
        "v_mfma_f32_16x16x32_bf16 v[112:115], a[104:107], v[124:127], v[112:115]\n\t" \
        "ds_read_b128 v[124:127], v16 offset:51648\n\t" \
        "s_waitcnt lgkmcnt(4)\n\t" \
        "v_permlane16_swap_b32 v100, v102\n\t" \
        "v_mfma_f32_16x16x32_bf16 v[104:107], a[108:111], v[128:131], v[104:107]\n\t" \
        "v_permlane16_swap_b32 v101, v103\n\t" \
        "v_mfma_f32_16x16x32_bf16 v[108:111], a[112:115], v[128:131], v[108:111]\n\t" \
        "v_mfma_f32_16x16x32_bf16 v[112:115], a[116:119], v[128:131], v[112:115]\n\t" \
        "ds_read_b128 v[128:131], v16 offset:51712\n\t" \
        "v_mfma_f32_16x16x32_bf16 v[112:115], a[128:131], v[132:135], v[112:115]\n\t" \
        "v_mfma_f32_16x16x32_bf16 v[112:115], a[140:143], v[136:139], v[112:115]\n\t" \
        "s_add_u32 s56, s64, 0x69000\n\t" \
        "buffer_store_dwordx4 v[100:103], v10, s[28:31], s56 offen sc1\n\t" \
        "v_mfma_f32_16x16x32_bf16 v[112:115], a[152:155], v[140:143], v[112:115]\n\t" \
        "v_mfma_f32_16x16x32_bf16 v[112:115], a[164:167], v[144:147], v[112:115]\n\t" \
        "s_waitcnt lgkmcnt(2)\n\t" \
        "v_mfma_f32_16x16x32_bf16 v[112:115], a[176:179], v[116:119], v[112:115]\n\t" \
        "v_mfma_f32_16x16x32_bf16 v[112:115], a[188:191], v[120:123], v[112:115]\n\t" \
        "s_waitcnt lgkmcnt(1)\n\t" \
        "v_mfma_f32_16x16x32_bf16 v[112:115], a[200:203], v[124:127], v[112:115]\n\t" \
        "s_waitcnt lgkmcnt(0)\n\t" \
        "v_mfma_f32_16x16x32_bf16 v[112:115], a[212:215], v[128:131], v[112:115]\n\t" \
        "v_mfma_f32_16x16x32_bf16 v[112:115], a[224:227], v[44:47], v[112:115]\n\t" \
        "v_mfma_f32_16x16x32_bf16 v[112:115], a[236:239], v[48:51], v[112:115]\n\t" \
        "v_mfma_f32_16x16x32_bf16 v[112:115], a[248:251], v[52:55], v[112:115]\n\t" \
        "v_mfma_f32_16x16x32_bf16 v[112:115], v[192:195], v[56:59], v[112:115]\n\t" \
        "v_mfma_f32_16x16x32_bf16 v[112:115], v[204:207], v[60:63], v[112:115]\n\t" \
        "v_mfma_f32_16x16x32_bf16 v[112:115], v[216:219], v[64:67], v[112:115]\n\t" \
        "v_mfma_f32_16x16x32_bf16 v[112:115], v[228:231], v[68:71], v[112:115]\n\t" \
        "v_mfma_f32_16x16x32_bf16 v[112:115], v[240:243], v[72:75], v[112:115]\n\t" \
        "v_mfma_f32_16x16x32_bf16 v[112:115], v[252:255], v[76:79], v[112:115]\n\t" \
        "v_mfma_f32_16x16x32_bf16 v[104:107], a[120:123], v[132:135], v[104:107]\n\t" \
        "v_mfma_f32_16x16x32_bf16 v[108:111], a[124:127], v[132:135], v[108:111]\n\t" \
        "v_mfma_f32_16x16x32_bf16 v[104:107], a[132:135], v[136:139], v[104:107]\n\t" \
        "v_mfma_f32_16x16x32_bf16 v[108:111], a[136:139], v[136:139], v[108:111]\n\t" \
        "v_mfma_f32_16x16x32_bf16 v[104:107], a[144:147], v[140:143], v[104:107]\n\t" \
        "v_mfma_f32_16x16x32_bf16 v[108:111], a[148:151], v[140:143], v[108:111]\n\t" \
        "v_mfma_f32_16x16x32_bf16 v[104:107], a[156:159], v[144:147], v[104:107]\n\t" \
        "v_mfma_f32_16x16x32_bf16 v[108:111], a[160:163], v[144:147], v[108:111]\n\t" \
        "v_mfma_f32_16x16x32_bf16 v[104:107], a[168:171], v[116:119], v[104:107]\n\t" \
        "v_mfma_f32_16x16x32_bf16 v[108:111], a[172:175], v[116:119], v[108:111]\n\t" \
        "v_cvt_pk_bf16_f32 v112, v112, v113\n\t" \
        "v_cvt_pk_bf16_f32 v113, v114, v115\n\t" \
        "v_mfma_f32_16x16x32_bf16 v[104:107], a[180:183], v[120:123], v[104:107]\n\t" \
        "ds_write_b64 v11, v[112:113] offset:12288\n\t" \
        "v_mfma_f32_16x16x32_bf16 v[108:111], a[184:187], v[120:123], v[108:111]\n\t" \
        "v_mfma_f32_16x16x32_bf16 v[104:107], a[192:195], v[124:127], v[104:107]\n\t" \
        "v_mfma_f32_16x16x32_bf16 v[108:111], a[196:199], v[124:127], v[108:111]\n\t" \
        "v_mfma_f32_16x16x32_bf16 v[104:107], a[204:207], v[128:131], v[104:107]\n\t" \
        "v_mfma_f32_16x16x32_bf16 v[108:111], a[208:211], v[128:131], v[108:111]\n\t" \
        "v_mfma_f32_16x16x32_bf16 v[104:107], a[216:219], v[44:47], v[104:107]\n\t" \
        "s_waitcnt lgkmcnt(0)\n\t" \
        "s_barrier\n\t" \
        "ds_read_b64 v[114:115], v12 offset:12288\n\t" \
        "v_mfma_f32_16x16x32_bf16 v[108:111], a[220:223], v[44:47], v[108:111]\n\t" \
        "v_mfma_f32_16x16x32_bf16 v[104:107], a[228:231], v[48:51], v[104:107]\n\t" \
        "v_mfma_f32_16x16x32_bf16 v[108:111], a[232:235], v[48:51], v[108:111]\n\t" \
        "v_mfma_f32_16x16x32_bf16 v[104:107], a[240:243], v[52:55], v[104:107]\n\t" \
        "v_mfma_f32_16x16x32_bf16 v[108:111], a[244:247], v[52:55], v[108:111]\n\t" \
        "v_mfma_f32_16x16x32_bf16 v[104:107], a[252:255], v[56:59], v[104:107]\n\t" \
        "v_mfma_f32_16x16x32_bf16 v[108:111], v[188:191], v[56:59], v[108:111]\n\t" \
        "v_mfma_f32_16x16x32_bf16 v[104:107], v[196:199], v[60:63], v[104:107]\n\t" \
        "s_waitcnt lgkmcnt(0)\n\t" \
        "v_permlane16_swap_b32 v112, v114\n\t" \
        "v_permlane16_swap_b32 v113, v115\n\t" \
        "v_mfma_f32_16x16x32_bf16 v[108:111], v[200:203], v[60:63], v[108:111]\n\t" \
        "v_mfma_f32_16x16x32_bf16 v[104:107], v[208:211], v[64:67], v[104:107]\n\t" \
        "s_add_u32 s56, s64, 0x7e000\n\t" \
        "buffer_store_dwordx4 v[112:115], v10, s[28:31], s56 offen sc1\n\t" \
        "v_mfma_f32_16x16x32_bf16 v[108:111], v[212:215], v[64:67], v[108:111]\n\t" \
        "v_mfma_f32_16x16x32_bf16 v[104:107], v[220:223], v[68:71], v[104:107]\n\t" \
        "v_mfma_f32_16x16x32_bf16 v[108:111], v[224:227], v[68:71], v[108:111]\n\t" \
        "v_mfma_f32_16x16x32_bf16 v[104:107], v[232:235], v[72:75], v[104:107]\n\t" \
        "v_mfma_f32_16x16x32_bf16 v[108:111], v[236:239], v[72:75], v[108:111]\n\t" \
        "v_mfma_f32_16x16x32_bf16 v[104:107], v[244:247], v[76:79], v[104:107]\n\t" \
        "v_mfma_f32_16x16x32_bf16 v[108:111], v[248:251], v[76:79], v[108:111]\n\t" \
        "s_nop 8\n\t" \
        "v_cvt_pk_bf16_f32 v104, v104, v105\n\t" \
        "v_cvt_pk_bf16_f32 v105, v106, v107\n\t" \
        "v_cvt_pk_bf16_f32 v106, v108, v109\n\t" \
        "v_cvt_pk_bf16_f32 v107, v110, v111\n\t" \
        "s_nop 0\n\t" \
        "v_permlane16_swap_b32 v104, v106\n\t" \
        "v_permlane16_swap_b32 v105, v107\n\t" \
        "s_add_u32 s56, s64, 0x7e000\n\t" \
        "buffer_store_dwordx4 v[104:107], v9, s[28:31], s56 offen sc1\n\t" \
        "s_endpgm\n\t" \
        ".LH8_start%=:\n\t" \
        "s_mov_b32 s62, 0\n\t" \
        "s_mov_b32 s63, -1\n\t" \
        "v_cmp_lt_u32 vcc, 47, v2\n\t" \
        "v_lshlrev_b32 v162, 4, v2\n\t" \
        "v_xor_b32 v163, 32, v162\n\t" \
        "v_cndmask_b32 v3, v162, v163, vcc\n\t" \
        "v_cndmask_b32 v4, v163, v162, s[62:63]\n\t" \
        "v_cmp_lt_u32 vcc, 15, v2\n\t" \
        "s_nop 1\n\t" \
        "v_cndmask_b32 v5, v162, v163, vcc\n\t" \
        "s_mul_i32 s40, s35, 0xc00\n\t" \
        "s_add_u32 s39, s40, 0xba40\n\t" \
        "s_mov_b32 s42, 0xffff0000\n\t" \
        "s_mov_b32 s43, 0xc0\n\t" \
        "s_mov_b32 s44, 0xf80\n\t" \
        "s_mov_b32 s45, 0x435f435f\n\t" \
        "v_add_u32 v162, s32, v18\n\t" \
        "v_add_u32 v163, s33, v19\n\t" \
        "v_add_u32 v162, -1, v162\n\t" \
        "v_add_u32 v163, -1, v163\n\t" \
        "v_cndmask_b32 v161, 0, 1.0, s[62:63]\n\t" \
        "v_cvt_f32_i32 v55, v162\n\t" \
        "v_cvt_f32_i32 v62, v163\n\t" \
        "v_add_f32 v56, v55, v161\n\t" \
        "v_add_f32 v59, v62, v161\n\t" \
        "v_add_f32 v57, 1.0, v55\n\t" \
        "v_add_f32 v62, 2.0, v62\n\t" \
        "v_add_f32 v58, 2.0, v55\n\t" \
        "v_add_f32 v61, 1.0, v59\n\t" \
        "v_fma_f32 v60, v161, -2.0, v62\n\t" \
        "v_bfe_u32 v160, v2, 4, 1\n\t" \
        "v_lshrrev_b32 v162, 2, v19\n\t" \
        "v_and_b32 v163, 1, v19\n\t" \
        "v_lshlrev_b32 v162, 5, v162\n\t" \
        "v_lshl_add_u32 v162, v163, 4, v162\n\t" \
        "v_and_b32 v163, 2, v19\n\t" \
        "v_lshl_add_u32 v162, v160, 3, v162\n\t" \
        "v_lshlrev_b32 v161, 2, v2\n\t" \
        "v_lshl_add_u32 v162, v163, 1, v162\n\t" \
        "v_and_b32 v161, 0x80, v161\n\t" \
        "s_mul_i32 s55, s35, 2560\n\t" \
        "s_add_u32 s55, s55, 109120\n\t" \
        "v_add3_u32 v63, v162, v161, s55\n\t" \
        "s_mul_i32 s56, s35, 1280\n\t" \
        "s_add_u32 s56, s56, 119360\n\t" \
        "v_lshl_add_u32 v10, v19, 3, s56\n\t" \
        "v_lshl_add_u32 v162, v160, 2, v161\n\t" \
        "v_add_u32 v64, v10, v162\n\t" \
        "s_mul_i32 s55, s32, 0xf80\n\t" \
        "s_mul_i32 s56, s33, 0xc0\n\t" \
        "s_add_u32 s55, s55, s56\n\t" \
        "s_sub_u32 s55, 0x1f40, s55\n\t" \
        "v_mov_b32 v65, s55\n\t" \
        "v_mov_b32 v29, 0x3f80\n\t" \
        "s_sub_i32 s55, s32, 2\n\t" \
        "s_sub_i32 s56, s33, 2\n\t" \
        "s_max_i32 s55, s55, 0\n\t" \
        "s_max_i32 s56, s56, 0\n\t" \
        "v_cvt_f32_u32 v162, s55\n\t" \
        "v_cvt_f32_u32 v163, s56\n\t" \
        "s_add_u32 s55, s32, 9\n\t" \
        "s_add_u32 s56, s33, 17\n\t" \
        "v_cvt_pk_bf16_f32 v14, v162, v163\n\t" \
        "v_cvt_f32_u32 v162, s55\n\t" \
        "v_cvt_f32_u32 v163, s56\n\t" \
        "v_cvt_pk_bf16_f32 v15, v162, v163\n\t" \
        "s_waitcnt vmcnt(2)\n\t" \
        "s_mov_b32 s57, s36\n\t" \
        "s_add_u32 m0, s38, 0x1f00\n\t" \
        "s_nop 0\n\t" \
        "buffer_load_dwordx4 v13, s[16:19], s57 offen nt lds\n\t" \
        "v_cndmask_b32 v140, v40, v41, s[62:63]\n\t" \
        "v_lshlrev_b32 v141, 16, v140\n\t" \
        "v_and_b32 v142, s42, v140\n\t" \
        "v_add_f32 v141, v55, v141\n\t" \
        "v_add_f32 v142, v59, v142\n\t" \
        "v_cvt_pk_bf16_f32 v50, v141, v142\n\t" \
        "v_pk_max_i16 v50, v50, 0\n\t" \
        "v_pk_min_i16 v50, v50, s45\n\t" \
        "v_lshlrev_b32 v141, 16, v50\n\t" \
        "v_and_b32 v142, s42, v50\n\t" \
        "v_pk_min_i16 v66, v50, v50\n\t" \
        "s_add_u32 s57, s36, 0xa800\n\t" \
        "s_add_u32 m0, s38, 0x2e80\n\t" \
        "s_nop 0\n\t" \
        "buffer_load_dwordx4 v13, s[16:19], s57 offen nt lds\n\t" \
        "v_fract_f32 v143, v141\n\t" \
        "v_fract_f32 v144, v142\n\t" \
        "v_cvt_u32_f32 v145, v141\n\t" \
        "v_cvt_u32_f32 v146, v142\n\t" \
        "v_cmp_neq_f32 s[10:11], 0, v143\n\t" \
        "v_cmp_neq_f32 vcc, 0, v144\n\t" \
        "v_add_u32 v146, 2, v146\n\t" \
        "v_pk_max_i16 v67, v50, v50\n\t" \
        "v_mad_u32_u24 v148, v146, s43, v65\n\t" \
        "v_bfe_u32 v147, v146, 2, 1\n\t" \
        "v_mad_u32_u24 v148, v145, s44, v148\n\t" \
        "s_add_u32 s57, s36, 0x15000\n\t" \
        "s_add_u32 m0, s38, 0x3e00\n\t" \
        "s_nop 0\n\t" \
        "buffer_load_dwordx4 v13, s[16:19], s57 offen nt lds\n\t" \
        "v_add_u32 v146, 1, v146\n\t" \
        "v_lshl_add_u32 v149, v147, 5, v148\n\t" \
        "v_bfe_u32 v147, v146, 2, 1\n\t" \
        "v_add_u32 v148, s43, v148\n\t" \
        "v_add_u32 v150, s44, v149\n\t" \
        "v_lshl_add_u32 v148, v147, 5, v148\n\t" \
        "v_cndmask_b32 v150, v149, v150, s[10:11]\n\t" \
        "v_add_f32 v144, v144, v143\n\t" \
        "v_cndmask_b32 v150, v150, v148, vcc\n\t" \
        "v_sub_f32 v152, 1.0, v144\n\t" \
        "s_and_b64 s[4:5], s[10:11], vcc\n\t" \
        "s_add_u32 s57, s36, 0x1f800\n\t" \
        "s_add_u32 m0, s38, 0x4d80\n\t" \
        "s_nop 0\n\t" \
        "buffer_load_dwordx4 v13, s[16:19], s57 offen nt lds\n\t" \
        "ds_write2st64_b32 v63, v149, v150 offset0:0 offset1:5\n\t" \
        "v_cvt_pk_bf16_f32 v151, v152, v144\n\t" \
        "v_cndmask_b32 v151, v151, v29, s[4:5]\n\t" \
        "ds_write_b32 v64, v151 offset:0\n\t" \
        "v_cndmask_b32 v140, v42, v43, s[62:63]\n\t" \
        "v_lshlrev_b32 v141, 16, v140\n\t" \
        "v_and_b32 v142, s42, v140\n\t" \
        "v_add_f32 v141, v56, v141\n\t" \
        "v_add_f32 v142, v60, v142\n\t" \
        "v_cvt_pk_bf16_f32 v51, v141, v142\n\t" \
        "v_pk_max_i16 v51, v51, 0\n\t" \
        "s_add_u32 s57, s36, 0x2a000\n\t" \
        "s_min_u32 s57, s57, 0x925800\n\t" \
        "s_add_u32 m0, s38, 0x5d00\n\t" \
        "s_nop 0\n\t" \
        "buffer_load_dwordx4 v13, s[16:19], s57 offen nt lds\n\t" \
        "v_pk_min_i16 v51, v51, s45\n\t" \
        "v_lshlrev_b32 v141, 16, v51\n\t" \
        "v_and_b32 v142, s42, v51\n\t" \
        "v_pk_min_i16 v66, v66, v51\n\t" \
        "v_fract_f32 v143, v141\n\t" \
        "v_fract_f32 v144, v142\n\t" \
        "v_cvt_u32_f32 v145, v141\n\t" \
        "v_cvt_u32_f32 v146, v142\n\t" \
        "v_cmp_neq_f32 s[10:11], 0, v143\n\t" \
        "v_cmp_neq_f32 vcc, 0, v144\n\t" \
        "v_add_u32 v146, 2, v146\n\t" \
        "s_add_u32 s57, s36, 0x34800\n\t" \
        "s_min_u32 s57, s57, 0x925800\n\t" \
        "s_add_u32 m0, s38, 0x6c80\n\t" \
        "s_nop 0\n\t" \
        "buffer_load_dwordx4 v13, s[16:19], s57 offen nt lds\n\t" \
        "v_pk_max_i16 v67, v67, v51\n\t" \
        "v_mad_u32_u24 v148, v146, s43, v65\n\t" \
        "v_bfe_u32 v147, v146, 2, 1\n\t" \
        "v_mad_u32_u24 v148, v145, s44, v148\n\t" \
        "v_add_u32 v146, 1, v146\n\t" \
        "v_lshl_add_u32 v149, v147, 5, v148\n\t" \
        "v_bfe_u32 v147, v146, 2, 1\n\t" \
        "v_add_u32 v148, s43, v148\n\t" \
        "v_add_u32 v150, s44, v149\n\t" \
        "v_lshl_add_u32 v148, v147, 5, v148\n\t" \
        "v_cndmask_b32 v150, v149, v150, s[10:11]\n\t" \
        "s_add_u32 s57, s36, 0x3f000\n\t" \
        "s_min_u32 s57, s57, 0x925800\n\t" \
        "s_add_u32 m0, s38, 0x7c00\n\t" \
        "s_nop 0\n\t" \
        "buffer_load_dwordx4 v13, s[16:19], s57 offen nt lds\n\t" \
        "v_add_f32 v144, v144, v143\n\t" \
        "v_cndmask_b32 v150, v150, v148, vcc\n\t" \
        "v_sub_f32 v152, 1.0, v144\n\t" \
        "s_and_b64 s[6:7], s[10:11], vcc\n\t" \
        "ds_write2st64_b32 v63, v149, v150 offset0:1 offset1:6\n\t" \
        "v_cvt_pk_bf16_f32 v151, v152, v144\n\t" \
        "v_cndmask_b32 v151, v151, v29, s[6:7]\n\t" \
        "ds_write_b32 v64, v151 offset:256\n\t" \
        "v_cndmask_b32 v140, v44, v45, s[62:63]\n\t" \
        "v_lshlrev_b32 v141, 16, v140\n\t" \
        "v_and_b32 v142, s42, v140\n\t" \
        "s_add_u32 s57, s36, 0x49800\n\t" \
        "s_min_u32 s57, s57, 0x925800\n\t" \
        "s_add_u32 m0, s38, 0x8b80\n\t" \
        "s_nop 0\n\t" \
        "buffer_load_dwordx4 v13, s[16:19], s57 offen nt lds\n\t" \
        "v_add_f32 v141, v57, v141\n\t" \
        "v_add_f32 v142, v61, v142\n\t" \
        "v_cvt_pk_bf16_f32 v52, v141, v142\n\t" \
        "v_pk_max_i16 v52, v52, 0\n\t" \
        "v_pk_min_i16 v52, v52, s45\n\t" \
        "v_lshlrev_b32 v141, 16, v52\n\t" \
        "v_and_b32 v142, s42, v52\n\t" \
        "v_pk_min_i16 v66, v66, v52\n\t" \
        "v_fract_f32 v143, v141\n\t" \
        "v_fract_f32 v144, v142\n\t" \
        "v_cvt_u32_f32 v145, v141\n\t" \
        "s_add_u32 s57, s36, 0x54000\n\t" \
        "s_min_u32 s57, s57, 0x925800\n\t" \
        "s_add_u32 m0, s38, 0x9b00\n\t" \
        "s_nop 0\n\t" \
        "buffer_load_dwordx4 v13, s[16:19], s57 offen nt lds\n\t" \
        "v_cvt_u32_f32 v146, v142\n\t" \
        "v_cmp_neq_f32 s[10:11], 0, v143\n\t" \
        "v_cmp_neq_f32 vcc, 0, v144\n\t" \
        "v_add_u32 v146, 2, v146\n\t" \
        "v_pk_max_i16 v67, v67, v52\n\t" \
        "v_mad_u32_u24 v148, v146, s43, v65\n\t" \
        "v_bfe_u32 v147, v146, 2, 1\n\t" \
        "v_mad_u32_u24 v148, v145, s44, v148\n\t" \
        "v_add_u32 v146, 1, v146\n\t" \
        "v_lshl_add_u32 v149, v147, 5, v148\n\t" \
        "v_bfe_u32 v147, v146, 2, 1\n\t" \
        "s_add_u32 s57, s36, 0x5e800\n\t" \
        "s_min_u32 s57, s57, 0x925800\n\t" \
        "s_add_u32 m0, s38, 0xaa80\n\t" \
        "s_nop 0\n\t" \
        "buffer_load_dwordx4 v13, s[16:19], s57 offen nt lds\n\t" \
        "v_add_u32 v148, s43, v148\n\t" \
        "v_add_u32 v150, s44, v149\n\t" \
        "v_lshl_add_u32 v148, v147, 5, v148\n\t" \
        "v_cndmask_b32 v150, v149, v150, s[10:11]\n\t" \
        "v_add_f32 v144, v144, v143\n\t" \
        "v_cndmask_b32 v150, v150, v148, vcc\n\t" \
        "v_sub_f32 v152, 1.0, v144\n\t" \
        "s_and_b64 s[8:9], s[10:11], vcc\n\t" \
        "ds_write2st64_b32 v63, v149, v150 offset0:2 offset1:7\n\t" \
        "v_cvt_pk_bf16_f32 v151, v152, v144\n\t" \
        "v_cndmask_b32 v151, v151, v29, s[8:9]\n\t" \
        "s_mov_b32 s57, s40\n\t" \
        "s_mov_b32 m0, s39\n\t" \
        "s_nop 0\n\t" \
        "buffer_load_dwordx4 v3, s[24:27], s57 offen offset:0  lds\n\t" \
        "ds_write_b32 v64, v151 offset:512\n\t" \
        "v_cndmask_b32 v140, v46, v47, s[62:63]\n\t" \
        "v_lshlrev_b32 v141, 16, v140\n\t" \
        "v_and_b32 v142, s42, v140\n\t" \
        "v_add_f32 v141, v58, v141\n\t" \
        "v_add_f32 v142, v59, v142\n\t" \
        "v_cvt_pk_bf16_f32 v53, v141, v142\n\t" \
        "v_pk_max_i16 v53, v53, 0\n\t" \
        "v_pk_min_i16 v53, v53, s45\n\t" \
        "v_lshlrev_b32 v141, 16, v53\n\t" \
        "v_and_b32 v142, s42, v53\n\t" \
        "buffer_load_dwordx4 v4, s[24:27], s57 offen offset:1024  lds\n\t" \
        "v_pk_min_i16 v66, v66, v53\n\t" \
        "v_fract_f32 v143, v141\n\t" \
        "v_fract_f32 v144, v142\n\t" \
        "v_cvt_u32_f32 v145, v141\n\t" \
        "v_cvt_u32_f32 v146, v142\n\t" \
        "v_cmp_neq_f32 s[10:11], 0, v143\n\t" \
        "v_cmp_neq_f32 vcc, 0, v144\n\t" \
        "v_add_u32 v146, 2, v146\n\t" \
        "v_pk_max_i16 v67, v67, v53\n\t" \
        "v_mad_u32_u24 v148, v146, s43, v65\n\t" \
        "v_bfe_u32 v147, v146, 2, 1\n\t" \
        "buffer_load_dwordx4 v5, s[24:27], s57 offen offset:2048  lds\n\t" \
        "v_mad_u32_u24 v148, v145, s44, v148\n\t" \
        "v_add_u32 v146, 1, v146\n\t" \
        "v_lshl_add_u32 v149, v147, 5, v148\n\t" \
        "v_bfe_u32 v147, v146, 2, 1\n\t" \
        "v_add_u32 v148, s43, v148\n\t" \
        "v_add_u32 v150, s44, v149\n\t" \
        "v_lshl_add_u32 v148, v147, 5, v148\n\t" \
        "v_cndmask_b32 v150, v149, v150, s[10:11]\n\t" \
        "v_add_f32 v144, v144, v143\n\t" \
        "v_cndmask_b32 v150, v150, v148, vcc\n\t" \
        "v_sub_f32 v152, 1.0, v144\n\t" \
        "s_add_u32 s57, s40, 0x3000\n\t" \
        "s_add_u32 m0, s39, 0x3000\n\t" \
        "s_nop 0\n\t" \
        "buffer_load_dwordx4 v3, s[24:27], s57 offen offset:0  lds\n\t" \
        "s_and_b64 s[12:13], s[10:11], vcc\n\t" \
        "ds_write2st64_b32 v63, v149, v150 offset0:3 offset1:8\n\t" \
        "v_cvt_pk_bf16_f32 v151, v152, v144\n\t" \
        "v_cndmask_b32 v151, v151, v29, s[12:13]\n\t" \
        "ds_write_b32 v64, v151 offset:768\n\t" \
        "v_lshlrev_b32 v141, 16, v48\n\t" \
        "v_and_b32 v142, s42, v48\n\t" \
        "v_add_f32 v141, v58, v141\n\t" \
        "v_add_f32 v142, v62, v142\n\t" \
        "v_cvt_pk_bf16_f32 v54, v141, v142\n\t" \
        "v_pk_max_i16 v54, v54, 0\n\t" \
        "buffer_load_dwordx4 v4, s[24:27], s57 offen offset:1024  lds\n\t" \
        "v_pk_min_i16 v54, v54, s45\n\t" \
        "v_lshlrev_b32 v141, 16, v54\n\t" \
        "v_and_b32 v142, s42, v54\n\t" \
        "v_pk_min_i16 v66, v66, v54\n\t" \
        "v_fract_f32 v143, v141\n\t" \
        "v_fract_f32 v144, v142\n\t" \
        "v_cvt_u32_f32 v145, v141\n\t" \
        "v_cvt_u32_f32 v146, v142\n\t" \
        "v_cmp_neq_f32 s[10:11], 0, v143\n\t" \
        "v_cmp_neq_f32 vcc, 0, v144\n\t" \
        "v_add_u32 v146, 2, v146\n\t" \
        "buffer_load_dwordx4 v5, s[24:27], s57 offen offset:2048  lds\n\t" \
        "v_pk_max_i16 v67, v67, v54\n\t" \
        "v_mad_u32_u24 v148, v146, s43, v65\n\t" \
        "v_bfe_u32 v147, v146, 2, 1\n\t" \
        "v_mad_u32_u24 v148, v145, s44, v148\n\t" \
        "v_add_u32 v146, 1, v146\n\t" \
        "v_lshl_add_u32 v149, v147, 5, v148\n\t" \
        "v_bfe_u32 v147, v146, 2, 1\n\t" \
        "v_add_u32 v148, s43, v148\n\t" \
        "v_add_u32 v150, s44, v149\n\t" \
        "v_lshl_add_u32 v148, v147, 5, v148\n\t" \
        "v_cndmask_b32 v150, v149, v150, s[10:11]\n\t" \
        "s_add_u32 s57, s40, 0x6000\n\t" \
        "s_add_u32 m0, s39, 0x6000\n\t" \
        "s_nop 0\n\t" \
        "buffer_load_dwordx4 v3, s[24:27], s57 offen offset:0  lds\n\t" \
        "v_add_f32 v144, v144, v143\n\t" \
        "v_cndmask_b32 v150, v150, v148, vcc\n\t" \
        "v_sub_f32 v152, 1.0, v144\n\t" \
        "s_and_b64 s[14:15], s[10:11], vcc\n\t" \
        "ds_write2st64_b32 v63, v149, v150 offset0:4 offset1:9\n\t" \
        "v_cvt_pk_bf16_f32 v151, v152, v144\n\t" \
        "v_cndmask_b32 v151, v151, v29, s[14:15]\n\t" \
        "ds_write_b32 v64, v151 offset:1024\n\t" \
        "v_pk_max_i16 v140, v66, v14\n\t" \
        "v_pk_min_i16 v141, v67, v15\n\t" \
        "v_cmp_ne_u32 s[10:11], v140, v66\n\t" \
        "buffer_load_dwordx4 v4, s[24:27], s57 offen offset:1024  lds\n\t" \
        "v_cmp_ne_u32 vcc, v141, v67\n\t" \
        "s_nop 0\n\t" \
        "s_or_b64 s[10:11], s[10:11], vcc\n\t" \
        "s_cselect_b32 s54, 1, 0\n\t" \
        "buffer_load_dwordx4 v5, s[24:27], s57 offen offset:2048  lds\n\t" \
        "s_or_b64 s[10:11], s[4:5], s[6:7]\n\t" \
        "s_or_b64 s[56:57], s[8:9], s[12:13]\n\t" \
        "s_or_b64 s[10:11], s[10:11], s[56:57]\n\t" \
        "s_or_b64 s[10:11], s[10:11], s[14:15]\n\t" \
        "s_cbranch_scc1 .LH_ent%=\n\t" \
        "s_lshl_b32 s55, s35, 2\n\t" \
        "s_cmp_lg_u32 s54, 0\n\t" \
        "v_mov_b32 v162, s55\n\t" \
        "s_cselect_b32 s55, 0x10000, 0\n\t" \
        "v_mov_b32 v163, s55\n\t" \
        "ds_write_b32 v162, v163\n\t" \
        "v_bfe_u32 v162, v2, 2, 1\n\t" \
        "v_lshrrev_b32 v163, 3, v2\n\t" \
        "s_mul_i32 s55, s35, 2560\n\t" \
        "s_add_u32 s55, s55, 109120\n\t" \
        "v_mul_u32_u24 v162, 0x500, v162\n\t" \
        "v_lshlrev_b32 v163, 4, v163\n\t" \
        "v_add3_u32 v9, v162, v163, s55\n\t" \
        "v_lshrrev_b32 v164, 4, v2\n\t" \
        "v_bfe_u32 v162, v2, 2, 2\n\t" \
        "v_and_b32 v163, 3, v2\n\t" \
        "v_lshl_add_u32 v162, v164, 2, v162\n\t" \
        "v_lshlrev_b32 v11, 3, v163\n\t" \
        "v_and_b32 v165, 1, v164\n\t" \
        "v_mul_u32_u24 v162, 0xc0, v162\n\t" \
        "v_xor_b32 v12, 32, v11\n\t" \
        "v_lshl_add_u32 v162, v165, 5, v162\n\t" \
        "v_lshlrev_b32 v163, 6, v165\n\t" \
        "s_mov_b32 s55, 0xba40\n\t" \
        "v_add3_u32 v6, v162, v11, s55\n\t" \
        "v_sub_u32 v7, v6, v163\n\t" \
        "v_mul_u32_u24 v162, 0xc0, v19\n\t" \
        "v_lshrrev_b32 v164, 1, v164\n\t" \
        "v_lshl_add_u32 v162, v165, 5, v162\n\t" \
        "v_lshl_add_u32 v8, v164, 4, v162\n\t" \
        "s_add_u32 s55, s32, s35\n\t" \
        "s_mul_i32 s56, s33, 0xc0\n\t" \
        "s_mul_i32 s55, s55, 0xa800\n\t" \
        "s_add_u32 s59, s55, s56\n\t" \
        "s_add_u32 s60, s59, 0x2a000\n\t" \
        "v_lshrrev_b32 v166, 4, v2\n\t" \
        "v_bfe_u32 v168, v2, 2, 2\n\t" \
        "v_and_b32 v167, 3, v2\n\t" \
        "v_cmp_eq_u32 vcc, v166, v168\n\t" \
        "v_lshlrev_b32 v167, v167, 1\n\t" \
        "s_nop 0\n\t" \
        "v_cndmask_b32 v167, 0, v167, vcc\n\t" \
        "v_bfe_i32 v25, v167, 0, 1\n\t" \
        "v_bfe_i32 v26, v167, 1, 1\n\t" \
        "v_bfe_i32 v27, v167, 2, 1\n\t" \
        "v_bfe_i32 v28, v167, 3, 1\n\t" \
        "v_mov_b32 v163, 0\n\t" \
        "s_waitcnt vmcnt(6) lgkmcnt(0)\n\t" \
        "s_barrier\n\t" \
        "ds_read_b128 v[152:155], v163\n\t" \
        "s_waitcnt lgkmcnt(0)\n\t" \
        "v_add_u32 v152, v152, v154\n\t" \
        "v_add_u32 v153, v153, v155\n\t" \
        "v_max_u32 v152, v152, v153\n\t" \
        "s_nop 0\n\t" \
        "v_readfirstlane_b32 s55, v152\n\t" \
        "s_nop 0\n\t" \
        "s_cmp_gt_u32 s55, 56\n\t" \
        "s_cbranch_scc1 .Lfail%=\n\t" \
        ".LH_join%=:\n\t" \
        "ds_read_b128 v[170:173], v9 offset:0\n\t" \
        "ds_read_b64 v[150:151], v10 offset:0\n\t" \
        "s_waitcnt lgkmcnt(1)\n\t" \
        "v_xor_b32 v134, v170, v11\n\t" \
        "v_xor_b32 v135, v171, v11\n\t" \
        "v_xor_b32 v136, v172, v11\n\t" \
        "v_xor_b32 v138, v170, v12\n\t" \
        "ds_read_b128 v[174:177], v9 offset:128\n\t" \
        "v_xor_b32 v137, v173, v11\n\t" \
        "v_xor_b32 v139, v171, v12\n\t" \
        "v_xor_b32 v140, v172, v12\n\t" \
        "v_xor_b32 v141, v173, v12\n\t" \
        "ds_read_b64_tr_b16 a[0:1], v6 offset:0\n\t" \
        "ds_read_b64_tr_b16 a[2:3], v6 offset:3072\n\t" \
        "ds_read_b64_tr_b16 a[4:5], v7 offset:32\n\t" \
        "ds_read_b64_tr_b16 a[6:7], v7 offset:3104\n\t" \
        "ds_read_b64_tr_b16 a[8:9], v6 offset:64\n\t" \
        "ds_read_b64_tr_b16 a[10:11], v6 offset:3136\n\t" \
        "ds_read_b64_tr_b16 a[96:97], v134 offset:0\n\t" \
        "ds_read_b64_tr_b16 a[98:99], v135 offset:0\n\t" \
        "ds_read_b64_tr_b16 a[100:101], v138 offset:0\n\t" \
        "ds_read_b64_tr_b16 a[102:103], v139 offset:0\n\t" \
        "ds_read_b64_tr_b16 a[104:105], v136 offset:0\n\t" \
        "ds_read_b64_tr_b16 a[106:107], v137 offset:0\n\t" \
        "ds_read_b64_tr_b16 a[108:109], v140 offset:0\n\t" \
        "ds_read_b64_tr_b16 a[110:111], v141 offset:0\n\t" \
        "ds_read_b64_tr_b16 a[112:113], v134 offset:64\n\t" \
        "ds_read_b64_tr_b16 a[114:115], v135 offset:64\n\t" \
        "ds_read_b64_tr_b16 a[116:117], v138 offset:64\n\t" \
        "ds_read_b64_tr_b16 a[118:119], v139 offset:64\n\t" \
        "ds_read_b64_tr_b16 a[120:121], v136 offset:64\n\t" \
        "ds_read_b64_tr_b16 a[122:123], v137 offset:64\n\t" \
        "ds_read_b64_tr_b16 a[124:125], v140 offset:64\n\t" \
        "ds_read_b64_tr_b16 a[126:127], v141 offset:64\n\t" \
        "ds_read_b64_tr_b16 a[128:129], v134 offset:128\n\t" \
        "ds_read_b64_tr_b16 a[130:131], v135 offset:128\n\t" \
        "ds_read_b64_tr_b16 a[132:133], v138 offset:128\n\t" \
        "ds_read_b64_tr_b16 a[134:135], v139 offset:128\n\t" \
        "ds_read_b64_tr_b16 a[136:137], v136 offset:128\n\t" \
        "s_waitcnt lgkmcnt(15)\n\t" \
        "v_and_b32 v154, v150, v25\n\t" \
        "ds_read_b64_tr_b16 a[138:139], v137 offset:128\n\t" \
        "ds_read_b64_tr_b16 a[140:141], v140 offset:128\n\t" \
        "v_xor_b32 v142, v174, v11\n\t" \
        "v_and_b32 v155, v150, v26\n\t" \
        "ds_read_b64_tr_b16 a[142:143], v141 offset:128\n\t" \
        "v_xor_b32 v143, v175, v11\n\t" \
        "v_and_b32 v156, v150, v27\n\t" \
        "v_xor_b32 v144, v176, v11\n\t" \
        "v_xor_b32 v146, v174, v12\n\t" \
        "v_and_b32 v157, v150, v28\n\t" \
        "v_xor_b32 v145, v177, v11\n\t" \
        "v_xor_b32 v147, v175, v12\n\t" \
        "v_xor_b32 v148, v176, v12\n\t" \
        "ds_read_b64 v[152:153], v10 offset:128\n\t" \
        "v_xor_b32 v149, v177, v12\n\t" \
        "v_and_b32 v158, v151, v25\n\t" \
        "v_and_b32 v159, v151, v26\n\t" \
        "ds_read_b64_tr_b16 a[12:13], v7 offset:96\n\t" \
        "v_and_b32 v160, v151, v27\n\t" \
        "ds_read_b64_tr_b16 a[14:15], v7 offset:3168\n\t" \
        "v_mfma_f32_16x16x32_bf16 v[102:105], a[96:99], v[154:157], 0\n\t" \
        "ds_read_b64_tr_b16 a[16:17], v6 offset:128\n\t" \
        "ds_read_b64_tr_b16 a[18:19], v6 offset:3200\n\t" \
        "v_and_b32 v161, v151, v28\n\t" \
        "v_mfma_f32_16x16x32_bf16 v[106:109], a[100:103], v[154:157], 0\n\t" \
        "s_add_u32 m0, s39, 36864\n\t" \
        "s_add_u32 s57, s40, 36864\n\t" \
        "buffer_load_dwordx4 v3, s[24:27], s57 offen offset:0  lds\n\t" \
        "v_mfma_f32_16x16x32_bf16 v[110:113], a[104:107], v[158:161], 0\n\t" \
        "ds_read_b64_tr_b16 a[20:21], v7 offset:160\n\t" \
        "s_waitcnt lgkmcnt(5)\n\t" \
        "v_and_b32 v162, v152, v25\n\t" \
        "v_mfma_f32_16x16x32_bf16 v[114:117], a[108:111], v[158:161], 0\n\t" \
        "ds_read_b64_tr_b16 a[22:23], v7 offset:3232\n\t" \
        "ds_read_b64_tr_b16 a[96:97], v142 offset:0\n\t" \
        "v_and_b32 v163, v152, v26\n\t" \
        "v_and_b32 v164, v152, v27\n\t" \
        "v_and_b32 v165, v152, v28\n\t" \
        "v_and_b32 v166, v153, v25\n\t" \
        "v_and_b32 v167, v153, v26\n\t" \
        "v_and_b32 v168, v153, v27\n\t" \
        "v_and_b32 v169, v153, v28\n\t" \
        "v_mfma_f32_16x16x32_bf16 v[118:121], a[112:115], v[154:157], 0\n\t" \
        "ds_read_b64_tr_b16 a[98:99], v143 offset:0\n\t" \
        "ds_read_b64_tr_b16 a[100:101], v146 offset:0\n\t" \
        "v_cvt_pk_bf16_f32 v78, v102, v103\n\t" \
        "v_cvt_pk_bf16_f32 v79, v104, v105\n\t" \
        "v_mfma_f32_16x16x32_bf16 v[122:125], a[116:119], v[154:157], 0\n\t" \
        "ds_read_b64_tr_b16 a[102:103], v147 offset:0\n\t" \
        "ds_read_b64_tr_b16 a[104:105], v144 offset:0\n\t" \
        "v_cvt_pk_bf16_f32 v80, v106, v107\n\t" \
        "v_cvt_pk_bf16_f32 v81, v108, v109\n\t" \
        "v_mfma_f32_16x16x32_bf16 v[126:129], a[120:123], v[158:161], 0\n\t" \
        "buffer_load_dwordx4 v4, s[24:27], s57 offen offset:1024  lds\n\t" \
        "v_cvt_pk_bf16_f32 v82, v110, v111\n\t" \
        "v_mfma_f32_16x16x32_bf16 v[130:133], a[124:127], v[158:161], 0\n\t" \
        "ds_read_b64_tr_b16 a[106:107], v145 offset:0\n\t" \
        "ds_read_b64_tr_b16 a[108:109], v148 offset:0\n\t" \
        "v_cvt_pk_bf16_f32 v83, v112, v113\n\t" \
        "v_cvt_pk_bf16_f32 v84, v114, v115\n\t" \
        "v_mfma_f32_16x16x32_bf16 v[102:105], a[128:131], v[154:157], 0\n\t" \
        "ds_read_b64_tr_b16 a[110:111], v149 offset:0\n\t" \
        "ds_read_b64_tr_b16 a[24:25], v6 offset:6144\n\t" \
        "v_cvt_pk_bf16_f32 v85, v116, v117\n\t" \
        "v_mfma_f32_16x16x32_bf16 v[106:109], a[132:135], v[154:157], 0\n\t" \
        "ds_read_b64_tr_b16 a[26:27], v6 offset:9216\n\t" \
        "ds_read_b64_tr_b16 a[28:29], v7 offset:6176\n\t" \
        "v_mfma_f32_16x16x32_bf16 v[110:113], a[136:139], v[158:161], 0\n\t" \
        "ds_read_b64_tr_b16 a[30:31], v7 offset:9248\n\t" \
        "ds_read_b64_tr_b16 a[32:33], v6 offset:6208\n\t" \
        "v_mfma_f32_16x16x32_bf16 v[114:117], a[140:143], v[158:161], 0\n\t" \
        "buffer_load_dwordx4 v5, s[24:27], s57 offen offset:2048  lds\n\t" \
        "v_mfma_f32_16x16x32_bf16 v[30:33], a[0:3], v[78:81], 0\n\t" \
        "ds_read_b64_tr_b16 a[34:35], v6 offset:9280\n\t" \
        "v_cvt_pk_bf16_f32 v86, v118, v119\n\t" \
        "v_mfma_f32_16x16x32_bf16 v[54:57], a[0:3], v[82:85], 0\n\t" \
        "ds_read_b64_tr_b16 a[36:37], v7 offset:6240\n\t" \
        "ds_read_b64_tr_b16 a[38:39], v7 offset:9312\n\t" \
        "v_mfma_f32_16x16x32_bf16 v[34:37], a[4:7], v[78:81], 0\n\t" \
        "ds_read_b64_tr_b16 a[40:41], v6 offset:6272\n\t" \
        "v_cvt_pk_bf16_f32 v87, v120, v121\n\t" \
        "v_mfma_f32_16x16x32_bf16 v[58:61], a[4:7], v[82:85], 0\n\t" \
        "s_waitcnt vmcnt(6)\n\t" \
        "s_barrier\n\t" \
        "ds_read_b128 v[170:173], v9 offset:256\n\t" \
        "ds_read_b64_tr_b16 a[42:43], v6 offset:9344\n\t" \
        "v_mfma_f32_16x16x32_bf16 v[38:41], a[8:11], v[78:81], 0\n\t" \
        "s_add_u32 m0, s39, 49152\n\t" \
        "s_add_u32 s57, s40, 49152\n\t" \
        "buffer_load_dwordx4 v3, s[24:27], s57 offen offset:0  lds\n\t" \
        "v_mfma_f32_16x16x32_bf16 v[62:65], a[8:11], v[82:85], 0\n\t" \
        "ds_read_b64_tr_b16 a[44:45], v7 offset:6304\n\t" \
        "ds_read_b64_tr_b16 a[46:47], v7 offset:9376\n\t" \
        "s_waitcnt lgkmcnt(12)\n\t" \
        "v_mfma_f32_16x16x32_bf16 v[42:45], a[12:15], v[78:81], 0\n\t" \
        "ds_read_b64_tr_b16 a[112:113], v142 offset:64\n\t" \
        "v_cvt_pk_bf16_f32 v88, v122, v123\n\t" \
        "v_mfma_f32_16x16x32_bf16 v[66:69], a[12:15], v[82:85], 0\n\t" \
        "ds_read_b64_tr_b16 a[114:115], v143 offset:64\n\t" \
        "ds_read_b64_tr_b16 a[116:117], v146 offset:64\n\t" \
        "v_mfma_f32_16x16x32_bf16 v[46:49], a[16:19], v[78:81], 0\n\t" \
        "ds_read_b64_tr_b16 a[118:119], v147 offset:64\n\t" \
        "ds_read_b64_tr_b16 a[120:121], v144 offset:64\n\t" \
        "v_mfma_f32_16x16x32_bf16 v[70:73], a[16:19], v[82:85], 0\n\t" \
        "buffer_load_dwordx4 v4, s[24:27], s57 offen offset:1024  lds\n\t" \
        "v_mfma_f32_16x16x32_bf16 v[50:53], a[20:23], v[78:81], 0\n\t" \
        "ds_read_b64_tr_b16 a[122:123], v145 offset:64\n\t" \
        "v_cvt_pk_bf16_f32 v89, v124, v125\n\t" \
        "v_cvt_pk_bf16_f32 v90, v126, v127\n\t" \
        "v_mfma_f32_16x16x32_bf16 v[74:77], a[20:23], v[82:85], 0\n\t" \
        "ds_read_b64_tr_b16 a[124:125], v148 offset:64\n\t" \
        "ds_read_b64_tr_b16 a[126:127], v149 offset:64\n\t" \
        "v_cvt_pk_bf16_f32 v91, v128, v129\n\t" \
        "v_cvt_pk_bf16_f32 v92, v130, v131\n\t" \
        "v_mfma_f32_16x16x32_bf16 v[118:121], a[96:99], v[162:165], 0\n\t" \
        "ds_read_b64_tr_b16 a[48:49], v6 offset:12288\n\t" \
        "v_cvt_pk_bf16_f32 v93, v132, v133\n\t" \
        "v_mfma_f32_16x16x32_bf16 v[122:125], a[100:103], v[162:165], 0\n\t" \
        "ds_read_b64_tr_b16 a[50:51], v6 offset:15360\n\t" \
        "ds_read_b64_tr_b16 a[52:53], v7 offset:12320\n\t" \
        "v_mfma_f32_16x16x32_bf16 v[126:129], a[104:107], v[166:169], 0\n\t" \
        "buffer_load_dwordx4 v5, s[24:27], s57 offen offset:2048  lds\n\t" \
        "v_mfma_f32_16x16x32_bf16 v[130:133], a[108:111], v[166:169], 0\n\t" \
        "ds_read_b64_tr_b16 a[54:55], v7 offset:15392\n\t" \
        "ds_read_b64_tr_b16 a[56:57], v6 offset:12352\n\t" \
        "s_waitcnt lgkmcnt(12)\n\t" \
        "v_mfma_f32_16x16x32_bf16 v[30:33], a[24:27], v[86:89], v[30:33]\n\t" \
        "ds_read_b64_tr_b16 a[58:59], v6 offset:15424\n\t" \
        "v_cvt_pk_bf16_f32 v94, v102, v103\n\t" \
        "v_mfma_f32_16x16x32_bf16 v[54:57], a[24:27], v[90:93], v[54:57]\n\t" \
        "ds_read_b64_tr_b16 a[60:61], v7 offset:12384\n\t" \
        "v_cvt_pk_bf16_f32 v95, v104, v105\n\t" \
        "v_mfma_f32_16x16x32_bf16 v[34:37], a[28:31], v[86:89], v[34:37]\n\t" \
        "ds_read_b64_tr_b16 a[62:63], v7 offset:15456\n\t" \
        "ds_read_b64_tr_b16 a[64:65], v6 offset:12416\n\t" \
        "v_mfma_f32_16x16x32_bf16 v[58:61], a[28:31], v[90:93], v[58:61]\n\t" \
        "ds_read_b64_tr_b16 a[66:67], v6 offset:15488\n\t" \
        "v_cvt_pk_bf16_f32 v96, v106, v107\n\t" \
        "v_mfma_f32_16x16x32_bf16 v[38:41], a[32:35], v[86:89], v[38:41]\n\t" \
        "ds_read_b64_tr_b16 a[68:69], v7 offset:12448\n\t" \
        "ds_read_b64_tr_b16 a[70:71], v7 offset:15520\n\t" \
        "v_mfma_f32_16x16x32_bf16 v[62:65], a[32:35], v[90:93], v[62:65]\n\t" \
        "ds_read_b64_tr_b16 a[128:129], v142 offset:128\n\t" \
        "v_cvt_pk_bf16_f32 v97, v108, v109\n\t" \
        "v_mfma_f32_16x16x32_bf16 v[42:45], a[36:39], v[86:89], v[42:45]\n\t" \
        "ds_read_b64_tr_b16 a[130:131], v143 offset:128\n\t" \
        "v_cvt_pk_bf16_f32 v98, v110, v111\n\t" \
        "v_mfma_f32_16x16x32_bf16 v[66:69], a[36:39], v[90:93], v[66:69]\n\t" \
        "ds_read_b64_tr_b16 a[132:133], v146 offset:128\n\t" \
        "ds_read_b64_tr_b16 a[134:135], v147 offset:128\n\t" \
        "v_mfma_f32_16x16x32_bf16 v[46:49], a[40:43], v[86:89], v[46:49]\n\t" \
        "ds_read_b64_tr_b16 a[136:137], v144 offset:128\n\t" \
        "v_xor_b32 v134, v170, v11\n\t" \
        "v_mfma_f32_16x16x32_bf16 v[70:73], a[40:43], v[90:93], v[70:73]\n\t" \
        "ds_read_b64_tr_b16 a[138:139], v145 offset:128\n\t" \
        "v_cvt_pk_bf16_f32 v99, v112, v113\n\t" \
        "v_mfma_f32_16x16x32_bf16 v[50:53], a[44:47], v[86:89], v[50:53]\n\t" \
        "ds_read_b64_tr_b16 a[140:141], v148 offset:128\n\t" \
        "ds_read_b64_tr_b16 a[142:143], v149 offset:128\n\t" \
        "v_mfma_f32_16x16x32_bf16 v[74:77], a[44:47], v[90:93], v[74:77]\n\t" \
        "ds_read_b64_tr_b16 a[72:73], v6 offset:18432\n\t" \
        "v_cvt_pk_bf16_f32 v100, v114, v115\n\t" \
        "v_xor_b32 v135, v171, v11\n\t" \
        "s_waitcnt lgkmcnt(12)\n\t" \
        "v_mfma_f32_16x16x32_bf16 v[102:105], a[112:115], v[162:165], 0\n\t" \
        "ds_read_b64_tr_b16 a[74:75], v6 offset:21504\n\t" \
        "ds_read_b64_tr_b16 a[76:77], v7 offset:18464\n\t" \
        "v_cvt_pk_bf16_f32 v101, v116, v117\n\t" \
        "v_mfma_f32_16x16x32_bf16 v[106:109], a[116:119], v[162:165], 0\n\t" \
        "ds_read_b64 v[150:151], v10 offset:256\n\t" \
        "v_xor_b32 v136, v172, v11\n\t" \
        "v_xor_b32 v138, v170, v12\n\t" \
        "v_mfma_f32_16x16x32_bf16 v[110:113], a[120:123], v[166:169], 0\n\t" \
        "ds_read_b64_tr_b16 a[78:79], v7 offset:21536\n\t" \
        "ds_read_b64_tr_b16 a[80:81], v6 offset:18496\n\t" \
        "v_xor_b32 v137, v173, v11\n\t" \
        "v_xor_b32 v139, v171, v12\n\t" \
        "v_mfma_f32_16x16x32_bf16 v[114:117], a[124:127], v[166:169], 0\n\t" \
        "ds_read_b64_tr_b16 a[82:83], v6 offset:21568\n\t" \
        "v_xor_b32 v140, v172, v12\n\t" \
        "v_mfma_f32_16x16x32_bf16 v[30:33], a[48:51], v[94:97], v[30:33]\n\t" \
        "ds_read_b64_tr_b16 a[84:85], v7 offset:18528\n\t" \
        "v_xor_b32 v141, v173, v12\n\t" \
        "v_mfma_f32_16x16x32_bf16 v[54:57], a[48:51], v[98:101], v[54:57]\n\t" \
        "ds_read_b64_tr_b16 a[86:87], v7 offset:21600\n\t" \
        "ds_read_b64_tr_b16 a[88:89], v6 offset:18560\n\t" \
        "v_mfma_f32_16x16x32_bf16 v[34:37], a[52:55], v[94:97], v[34:37]\n\t" \
        "ds_read_b64_tr_b16 a[90:91], v6 offset:21632\n\t" \
        "v_cvt_pk_bf16_f32 v78, v118, v119\n\t" \
        "v_mfma_f32_16x16x32_bf16 v[58:61], a[52:55], v[98:101], v[58:61]\n\t" \
        "s_waitcnt vmcnt(6)\n\t" \
        "s_barrier\n\t" \
        "ds_read_b64_tr_b16 a[92:93], v7 offset:18592\n\t" \
        "ds_read_b64_tr_b16 a[94:95], v7 offset:21664\n\t" \
        "v_mfma_f32_16x16x32_bf16 v[38:41], a[56:59], v[94:97], v[38:41]\n\t" \
        "ds_read_b64_tr_b16 a[96:97], v134 offset:0\n\t" \
        "v_cvt_pk_bf16_f32 v79, v120, v121\n\t" \
        "v_mfma_f32_16x16x32_bf16 v[62:65], a[56:59], v[98:101], v[62:65]\n\t" \
        "ds_read_b64_tr_b16 a[98:99], v135 offset:0\n\t" \
        "v_cvt_pk_bf16_f32 v80, v122, v123\n\t" \
        "v_mfma_f32_16x16x32_bf16 v[42:45], a[60:63], v[94:97], v[42:45]\n\t" \
        "s_add_u32 m0, s39, 0\n\t" \
        "s_add_u32 s57, s40, 61440\n\t" \
        "buffer_load_dwordx4 v3, s[24:27], s57 offen offset:0  lds\n\t" \
        "v_mfma_f32_16x16x32_bf16 v[66:69], a[60:63], v[98:101], v[66:69]\n\t" \
        "ds_read_b64_tr_b16 a[100:101], v138 offset:0\n\t" \
        "ds_read_b64_tr_b16 a[102:103], v139 offset:0\n\t" \
        "s_waitcnt lgkmcnt(11)\n\t" \
        "v_mfma_f32_16x16x32_bf16 v[46:49], a[64:67], v[94:97], v[46:49]\n\t" \
        "ds_read_b64_tr_b16 a[104:105], v136 offset:0\n\t" \
        "v_cvt_pk_bf16_f32 v81, v124, v125\n\t" \
        "v_mfma_f32_16x16x32_bf16 v[70:73], a[64:67], v[98:101], v[70:73]\n\t" \
        "ds_read_b64_tr_b16 a[106:107], v137 offset:0\n\t" \
        "ds_read_b64_tr_b16 a[108:109], v140 offset:0\n\t" \
        "v_mfma_f32_16x16x32_bf16 v[50:53], a[68:71], v[94:97], v[50:53]\n\t" \
        "ds_read_b64_tr_b16 a[110:111], v141 offset:0\n\t" \
        "v_cvt_pk_bf16_f32 v82, v126, v127\n\t" \
        "v_cvt_pk_bf16_f32 v83, v128, v129\n\t" \
        "v_mfma_f32_16x16x32_bf16 v[74:77], a[68:71], v[98:101], v[74:77]\n\t" \
        "ds_read_b64_tr_b16 a[0:1], v6 offset:24576\n\t" \
        "v_cvt_pk_bf16_f32 v84, v130, v131\n\t" \
        "v_cvt_pk_bf16_f32 v85, v132, v133\n\t" \
        "v_mfma_f32_16x16x32_bf16 v[118:121], a[128:131], v[162:165], 0\n\t" \
        "buffer_load_dwordx4 v4, s[24:27], s57 offen offset:1024  lds\n\t" \
        "v_mfma_f32_16x16x32_bf16 v[122:125], a[132:135], v[162:165], 0\n\t" \
        "ds_read_b64_tr_b16 a[2:3], v6 offset:27648\n\t" \
        "ds_read_b64_tr_b16 a[4:5], v7 offset:24608\n\t" \
        "v_mfma_f32_16x16x32_bf16 v[126:129], a[136:139], v[166:169], 0\n\t" \
        "ds_read_b64_tr_b16 a[6:7], v7 offset:27680\n\t" \
        "v_cvt_pk_bf16_f32 v86, v102, v103\n\t" \
        "v_mfma_f32_16x16x32_bf16 v[130:133], a[140:143], v[166:169], 0\n\t" \
        "ds_read_b64_tr_b16 a[8:9], v6 offset:24640\n\t" \
        "ds_read_b64_tr_b16 a[10:11], v6 offset:27712\n\t" \
        "v_mfma_f32_16x16x32_bf16 v[30:33], a[72:75], v[78:81], v[30:33]\n\t" \
        "ds_read_b64_tr_b16 a[12:13], v7 offset:24672\n\t" \
        "v_and_b32 v154, v150, v25\n\t" \
        "v_mfma_f32_16x16x32_bf16 v[54:57], a[72:75], v[82:85], v[54:57]\n\t" \
        "ds_read_b64_tr_b16 a[14:15], v7 offset:27744\n\t" \
        "v_and_b32 v155, v150, v26\n\t" \
        "v_mfma_f32_16x16x32_bf16 v[34:37], a[76:79], v[78:81], v[34:37]\n\t" \
        "buffer_load_dwordx4 v5, s[24:27], s57 offen offset:2048  lds\n\t" \
        "v_mfma_f32_16x16x32_bf16 v[58:61], a[76:79], v[82:85], v[58:61]\n\t" \
        "ds_read_b64_tr_b16 a[16:17], v6 offset:24704\n\t" \
        "ds_read_b128 v[174:177], v9 offset:384\n\t" \
        "s_waitcnt lgkmcnt(10)\n\t" \
        "v_mfma_f32_16x16x32_bf16 v[38:41], a[80:83], v[78:81], v[38:41]\n\t" \
        "ds_read_b64_tr_b16 a[18:19], v6 offset:27776\n\t" \
        "v_and_b32 v156, v150, v27\n\t" \
        "v_mfma_f32_16x16x32_bf16 v[62:65], a[80:83], v[82:85], v[62:65]\n\t" \
        "ds_read_b64_tr_b16 a[20:21], v7 offset:24736\n\t" \
        "ds_read_b64_tr_b16 a[22:23], v7 offset:27808\n\t" \
        "v_mfma_f32_16x16x32_bf16 v[42:45], a[84:87], v[78:81], v[42:45]\n\t" \
        "ds_read_b64_tr_b16 a[112:113], v134 offset:64\n\t" \
        "v_and_b32 v157, v150, v28\n\t" \
        "v_and_b32 v158, v151, v25\n\t" \
        "v_mfma_f32_16x16x32_bf16 v[66:69], a[84:87], v[82:85], v[66:69]\n\t" \
        "ds_read_b64_tr_b16 a[114:115], v135 offset:64\n\t" \
        "v_cvt_pk_bf16_f32 v87, v104, v105\n\t" \
        "v_mfma_f32_16x16x32_bf16 v[46:49], a[88:91], v[78:81], v[46:49]\n\t" \
        "ds_read_b64_tr_b16 a[116:117], v138 offset:64\n\t" \
        "ds_read_b64_tr_b16 a[118:119], v139 offset:64\n\t" \
        "v_cvt_pk_bf16_f32 v88, v106, v107\n\t" \
        "v_and_b32 v159, v151, v26\n\t" \
        "v_mfma_f32_16x16x32_bf16 v[70:73], a[88:91], v[82:85], v[70:73]\n\t" \
        "ds_read_b64_tr_b16 a[120:121], v136 offset:64\n\t" \
        "v_cvt_pk_bf16_f32 v89, v108, v109\n\t" \
        "v_cvt_pk_bf16_f32 v90, v110, v111\n\t" \
        "v_mfma_f32_16x16x32_bf16 v[50:53], a[92:95], v[78:81], v[50:53]\n\t" \
        "ds_read_b64_tr_b16 a[122:123], v137 offset:64\n\t" \
        "ds_read_b64_tr_b16 a[124:125], v140 offset:64\n\t" \
        "v_and_b32 v160, v151, v27\n\t" \
        "v_mfma_f32_16x16x32_bf16 v[74:77], a[92:95], v[82:85], v[74:77]\n\t" \
        "ds_read_b64_tr_b16 a[126:127], v141 offset:64\n\t" \
        "v_cvt_pk_bf16_f32 v91, v112, v113\n\t" \
        "v_cvt_pk_bf16_f32 v92, v114, v115\n\t" \
        "v_mfma_f32_16x16x32_bf16 v[102:105], a[96:99], v[154:157], 0\n\t" \
        "ds_read_b64_tr_b16 a[24:25], v6 offset:30720\n\t" \
        "v_and_b32 v161, v151, v28\n\t" \
        "v_cvt_pk_bf16_f32 v93, v116, v117\n\t" \
        "v_mfma_f32_16x16x32_bf16 v[106:109], a[100:103], v[154:157], 0\n\t" \
        "ds_read_b64_tr_b16 a[26:27], v6 offset:33792\n\t" \
        "ds_read_b64_tr_b16 a[28:29], v7 offset:30752\n\t" \
        "v_mfma_f32_16x16x32_bf16 v[110:113], a[104:107], v[158:161], 0\n\t" \
        "ds_read_b64_tr_b16 a[30:31], v7 offset:33824\n\t" \
        "v_cvt_pk_bf16_f32 v94, v118, v119\n\t" \
        "v_mfma_f32_16x16x32_bf16 v[114:117], a[108:111], v[158:161], 0\n\t" \
        "ds_read_b64_tr_b16 a[32:33], v6 offset:30784\n\t" \
        "ds_read_b64_tr_b16 a[34:35], v6 offset:33856\n\t" \
        "s_waitcnt lgkmcnt(13)\n\t" \
        "v_mfma_f32_16x16x32_bf16 v[30:33], a[0:3], v[86:89], v[30:33]\n\t" \
        "ds_read_b64_tr_b16 a[36:37], v7 offset:30816\n\t" \
        "v_cvt_pk_bf16_f32 v95, v120, v121\n\t" \
        "v_mfma_f32_16x16x32_bf16 v[54:57], a[0:3], v[90:93], v[54:57]\n\t" \
        "ds_read_b64_tr_b16 a[38:39], v7 offset:33888\n\t" \
        "ds_read_b64_tr_b16 a[40:41], v6 offset:30848\n\t" \
        "v_mfma_f32_16x16x32_bf16 v[34:37], a[4:7], v[86:89], v[34:37]\n\t" \
        "ds_read_b64_tr_b16 a[42:43], v6 offset:33920\n\t" \
        "v_cvt_pk_bf16_f32 v96, v122, v123\n\t" \
        "v_mfma_f32_16x16x32_bf16 v[58:61], a[4:7], v[90:93], v[58:61]\n\t" \
        "s_waitcnt vmcnt(6)\n\t" \
        "s_barrier\n\t" \
        "ds_read_b64_tr_b16 a[44:45], v7 offset:30880\n\t" \
        "v_cvt_pk_bf16_f32 v97, v124, v125\n\t" \
        "v_mfma_f32_16x16x32_bf16 v[38:41], a[8:11], v[86:89], v[38:41]\n\t" \
        "ds_read_b64_tr_b16 a[46:47], v7 offset:33952\n\t" \
        "ds_read_b64_tr_b16 a[128:129], v134 offset:128\n\t" \
        "v_mfma_f32_16x16x32_bf16 v[62:65], a[8:11], v[90:93], v[62:65]\n\t" \
        "ds_read_b64_tr_b16 a[130:131], v135 offset:128\n\t" \
        "v_cvt_pk_bf16_f32 v98, v126, v127\n\t" \
        "v_mfma_f32_16x16x32_bf16 v[42:45], a[12:15], v[86:89], v[42:45]\n\t" \
        "s_add_u32 m0, s39, 12288\n\t" \
        "s_add_u32 s57, s40, 73728\n\t" \
        "buffer_load_dwordx4 v3, s[24:27], s57 offen offset:0  lds\n\t" \
        "v_mfma_f32_16x16x32_bf16 v[66:69], a[12:15], v[90:93], v[66:69]\n\t" \
        "ds_read_b64_tr_b16 a[132:133], v138 offset:128\n\t" \
        "ds_read_b64_tr_b16 a[134:135], v139 offset:128\n\t" \
        "v_mfma_f32_16x16x32_bf16 v[46:49], a[16:19], v[86:89], v[46:49]\n\t" \
        "ds_read_b64_tr_b16 a[136:137], v136 offset:128\n\t" \
        "v_xor_b32 v142, v174, v11\n\t" \
        "v_mfma_f32_16x16x32_bf16 v[70:73], a[16:19], v[90:93], v[70:73]\n\t" \
        "ds_read_b64_tr_b16 a[138:139], v137 offset:128\n\t" \
        "ds_read_b64_tr_b16 a[140:141], v140 offset:128\n\t" \
        "v_cvt_pk_bf16_f32 v99, v128, v129\n\t" \
        "v_mfma_f32_16x16x32_bf16 v[50:53], a[20:23], v[86:89], v[50:53]\n\t" \
        "ds_read_b64_tr_b16 a[142:143], v141 offset:128\n\t" \
        "v_cvt_pk_bf16_f32 v100, v130, v131\n\t" \
        "v_xor_b32 v143, v175, v11\n\t" \
        "v_mfma_f32_16x16x32_bf16 v[74:77], a[20:23], v[90:93], v[74:77]\n\t" \
        "ds_read_b64_tr_b16 a[48:49], v6 offset:36864\n\t" \
        "v_cvt_pk_bf16_f32 v101, v132, v133\n\t" \
        "s_waitcnt lgkmcnt(11)\n\t" \
        "v_mfma_f32_16x16x32_bf16 v[118:121], a[112:115], v[154:157], 0\n\t" \
        "buffer_load_dwordx4 v4, s[24:27], s57 offen offset:1024  lds\n\t" \
        "v_mfma_f32_16x16x32_bf16 v[122:125], a[116:119], v[154:157], 0\n\t" \
        "ds_read_b64_tr_b16 a[50:51], v6 offset:39936\n\t" \
        "ds_read_b64_tr_b16 a[52:53], v7 offset:36896\n\t" \
        "v_xor_b32 v144, v176, v11\n\t" \
        "v_xor_b32 v146, v174, v12\n\t" \
        "v_mfma_f32_16x16x32_bf16 v[126:129], a[120:123], v[158:161], 0\n\t" \
        "ds_read_b64 v[152:153], v10 offset:384\n\t" \
        "v_xor_b32 v145, v177, v11\n\t" \
        "v_xor_b32 v147, v175, v12\n\t" \
        "v_mfma_f32_16x16x32_bf16 v[130:133], a[124:127], v[158:161], 0\n\t" \
        "ds_read_b64_tr_b16 a[54:55], v7 offset:39968\n\t" \
        "ds_read_b64_tr_b16 a[56:57], v6 offset:36928\n\t" \
        "v_xor_b32 v148, v176, v12\n\t" \
        "v_mfma_f32_16x16x32_bf16 v[30:33], a[24:27], v[94:97], v[30:33]\n\t" \
        "ds_read_b64_tr_b16 a[58:59], v6 offset:40000\n\t" \
        "v_xor_b32 v149, v177, v12\n\t" \
        "v_mfma_f32_16x16x32_bf16 v[54:57], a[24:27], v[98:101], v[54:57]\n\t" \
        "ds_read_b64_tr_b16 a[60:61], v7 offset:36960\n\t" \
        "ds_read_b64_tr_b16 a[62:63], v7 offset:40032\n\t" \
        "v_mfma_f32_16x16x32_bf16 v[34:37], a[28:31], v[94:97], v[34:37]\n\t" \
        "buffer_load_dwordx4 v5, s[24:27], s57 offen offset:2048  lds\n\t" \
        "v_mfma_f32_16x16x32_bf16 v[58:61], a[28:31], v[98:101], v[58:61]\n\t" \
        "ds_read_b64_tr_b16 a[64:65], v6 offset:36992\n\t" \
        "v_cvt_pk_bf16_f32 v78, v102, v103\n\t" \
        "v_mfma_f32_16x16x32_bf16 v[38:41], a[32:35], v[94:97], v[38:41]\n\t" \
        "ds_read_b64_tr_b16 a[66:67], v6 offset:40064\n\t" \
        "v_cvt_pk_bf16_f32 v79, v104, v105\n\t" \
        "v_mfma_f32_16x16x32_bf16 v[62:65], a[32:35], v[98:101], v[62:65]\n\t" \
        "ds_read_b64_tr_b16 a[68:69], v7 offset:37024\n\t" \
        "ds_read_b64_tr_b16 a[70:71], v7 offset:40096\n\t" \
        "v_mfma_f32_16x16x32_bf16 v[42:45], a[36:39], v[94:97], v[42:45]\n\t" \
        "ds_read_b64_tr_b16 a[96:97], v142 offset:0\n\t" \
        "v_cvt_pk_bf16_f32 v80, v106, v107\n\t" \
        "v_mfma_f32_16x16x32_bf16 v[66:69], a[36:39], v[98:101], v[66:69]\n\t" \
        "ds_read_b64_tr_b16 a[98:99], v143 offset:0\n\t" \
        "ds_read_b64_tr_b16 a[100:101], v146 offset:0\n\t" \
        "v_mfma_f32_16x16x32_bf16 v[46:49], a[40:43], v[94:97], v[46:49]\n\t" \
        "ds_read_b64_tr_b16 a[102:103], v147 offset:0\n\t" \
        "v_cvt_pk_bf16_f32 v81, v108, v109\n\t" \
        "v_mfma_f32_16x16x32_bf16 v[70:73], a[40:43], v[98:101], v[70:73]\n\t" \
        "ds_read_b64_tr_b16 a[104:105], v144 offset:0\n\t" \
        "ds_read_b64_tr_b16 a[106:107], v145 offset:0\n\t" \
        "s_waitcnt lgkmcnt(12)\n\t" \
        "v_mfma_f32_16x16x32_bf16 v[50:53], a[44:47], v[94:97], v[50:53]\n\t" \
        "ds_read_b64_tr_b16 a[108:109], v148 offset:0\n\t" \
        "v_cvt_pk_bf16_f32 v82, v110, v111\n\t" \
        "v_mfma_f32_16x16x32_bf16 v[74:77], a[44:47], v[98:101], v[74:77]\n\t" \
        "ds_read_b64_tr_b16 a[110:111], v149 offset:0\n\t" \
        "v_cvt_pk_bf16_f32 v83, v112, v113\n\t" \
        "v_cvt_pk_bf16_f32 v84, v114, v115\n\t" \
        "v_mfma_f32_16x16x32_bf16 v[102:105], a[128:131], v[154:157], 0\n\t" \
        "ds_read_b64_tr_b16 a[72:73], v6 offset:43008\n\t" \
        "ds_read_b64_tr_b16 a[74:75], v6 offset:46080\n\t" \
        "v_cvt_pk_bf16_f32 v85, v116, v117\n\t" \
        "v_mfma_f32_16x16x32_bf16 v[106:109], a[132:135], v[154:157], 0\n\t" \
        "ds_read_b64_tr_b16 a[76:77], v7 offset:43040\n\t" \
        "v_cvt_pk_bf16_f32 v86, v118, v119\n\t" \
        "v_mfma_f32_16x16x32_bf16 v[110:113], a[136:139], v[158:161], 0\n\t" \
        "ds_read_b64_tr_b16 a[78:79], v7 offset:46112\n\t" \
        "ds_read_b64_tr_b16 a[80:81], v6 offset:43072\n\t" \
        "v_mfma_f32_16x16x32_bf16 v[114:117], a[140:143], v[158:161], 0\n\t" \
        "ds_read_b64_tr_b16 a[82:83], v6 offset:46144\n\t" \
        "v_cvt_pk_bf16_f32 v87, v120, v121\n\t" \
        "v_mfma_f32_16x16x32_bf16 v[30:33], a[48:51], v[78:81], v[30:33]\n\t" \
        "ds_read_b64_tr_b16 a[84:85], v7 offset:43104\n\t" \
        "ds_read_b64_tr_b16 a[86:87], v7 offset:46176\n\t" \
        "v_mfma_f32_16x16x32_bf16 v[54:57], a[48:51], v[82:85], v[54:57]\n\t" \
        "ds_read_b64_tr_b16 a[88:89], v6 offset:43136\n\t" \
        "v_and_b32 v162, v152, v25\n\t" \
        "v_mfma_f32_16x16x32_bf16 v[34:37], a[52:55], v[78:81], v[34:37]\n\t" \
        "ds_read_b128 v[170:173], v9 offset:512\n\t" \
        "ds_read_b64_tr_b16 a[90:91], v6 offset:46208\n\t" \
        "v_mfma_f32_16x16x32_bf16 v[58:61], a[52:55], v[82:85], v[58:61]\n\t" \
        "s_waitcnt vmcnt(6)\n\t" \
        "s_barrier\n\t" \
        "ds_read_b64_tr_b16 a[92:93], v7 offset:43168\n\t" \
        "v_and_b32 v163, v152, v26\n\t" \
        "v_mfma_f32_16x16x32_bf16 v[38:41], a[56:59], v[78:81], v[38:41]\n\t" \
        "ds_read_b64_tr_b16 a[94:95], v7 offset:46240\n\t" \
        "v_and_b32 v164, v152, v27\n\t" \
        "v_and_b32 v165, v152, v28\n\t" \
        "v_mfma_f32_16x16x32_bf16 v[62:65], a[56:59], v[82:85], v[62:65]\n\t" \
        "ds_read_b64_tr_b16 a[112:113], v142 offset:64\n\t" \
        "ds_read_b64_tr_b16 a[114:115], v143 offset:64\n\t" \
        "v_and_b32 v166, v153, v25\n\t" \
        "s_waitcnt lgkmcnt(13)\n\t" \
        "v_mfma_f32_16x16x32_bf16 v[42:45], a[60:63], v[78:81], v[42:45]\n\t" \
        "s_add_u32 m0, s39, 24576\n\t" \
        "s_add_u32 s57, s40, 86016\n\t" \
        "buffer_load_dwordx4 v3, s[24:27], s57 offen offset:0  lds\n\t" \
        "v_mfma_f32_16x16x32_bf16 v[66:69], a[60:63], v[82:85], v[66:69]\n\t" \
        "ds_read_b64_tr_b16 a[116:117], v146 offset:64\n\t" \
        "v_cvt_pk_bf16_f32 v88, v122, v123\n\t" \
        "v_and_b32 v167, v153, v26\n\t" \
        "v_mfma_f32_16x16x32_bf16 v[46:49], a[64:67], v[78:81], v[46:49]\n\t" \
        "ds_read_b64_tr_b16 a[118:119], v147 offset:64\n\t" \
        "ds_read_b64_tr_b16 a[120:121], v144 offset:64\n\t" \
        "v_cvt_pk_bf16_f32 v89, v124, v125\n\t" \
        "v_cvt_pk_bf16_f32 v90, v126, v127\n\t" \
        "v_mfma_f32_16x16x32_bf16 v[70:73], a[64:67], v[82:85], v[70:73]\n\t" \
        "ds_read_b64_tr_b16 a[122:123], v145 offset:64\n\t" \
        "v_and_b32 v168, v153, v27\n\t" \
        "v_mfma_f32_16x16x32_bf16 v[50:53], a[68:71], v[78:81], v[50:53]\n\t" \
        "ds_read_b64_tr_b16 a[124:125], v148 offset:64\n\t" \
        "ds_read_b64_tr_b16 a[126:127], v149 offset:64\n\t" \
        "v_cvt_pk_bf16_f32 v91, v128, v129\n\t" \
        "v_cvt_pk_bf16_f32 v92, v130, v131\n\t" \
        "v_mfma_f32_16x16x32_bf16 v[74:77], a[68:71], v[82:85], v[74:77]\n\t" \
        "ds_read_b64_tr_b16 a[0:1], v6 offset:49152\n\t" \
        "v_and_b32 v169, v153, v28\n\t" \
        "v_cvt_pk_bf16_f32 v93, v132, v133\n\t" \
        "v_mfma_f32_16x16x32_bf16 v[118:121], a[96:99], v[162:165], 0\n\t" \
        "buffer_load_dwordx4 v4, s[24:27], s57 offen offset:1024  lds\n\t" \
        "v_mfma_f32_16x16x32_bf16 v[122:125], a[100:103], v[162:165], 0\n\t" \
        "ds_read_b64_tr_b16 a[2:3], v6 offset:52224\n\t" \
        "ds_read_b64_tr_b16 a[4:5], v7 offset:49184\n\t" \
        "v_mfma_f32_16x16x32_bf16 v[126:129], a[104:107], v[166:169], 0\n\t" \
        "ds_read_b64_tr_b16 a[6:7], v7 offset:52256\n\t" \
        "v_cvt_pk_bf16_f32 v94, v102, v103\n\t" \
        "v_mfma_f32_16x16x32_bf16 v[130:133], a[108:111], v[166:169], 0\n\t" \
        "ds_read_b64_tr_b16 a[8:9], v6 offset:49216\n\t" \
        "v_cvt_pk_bf16_f32 v95, v104, v105\n\t" \
        "v_mfma_f32_16x16x32_bf16 v[30:33], a[72:75], v[86:89], v[30:33]\n\t" \
        "ds_read_b64_tr_b16 a[10:11], v6 offset:52288\n\t" \
        "ds_read_b64_tr_b16 a[12:13], v7 offset:49248\n\t" \
        "v_mfma_f32_16x16x32_bf16 v[54:57], a[72:75], v[90:93], v[54:57]\n\t" \
        "ds_read_b64_tr_b16 a[14:15], v7 offset:52320\n\t" \
        "v_cvt_pk_bf16_f32 v96, v106, v107\n\t" \
        "s_waitcnt lgkmcnt(11)\n\t" \
        "v_mfma_f32_16x16x32_bf16 v[34:37], a[76:79], v[86:89], v[34:37]\n\t" \
        "buffer_load_dwordx4 v5, s[24:27], s57 offen offset:2048  lds\n\t" \
        "v_mfma_f32_16x16x32_bf16 v[58:61], a[76:79], v[90:93], v[58:61]\n\t" \
        "ds_read_b64_tr_b16 a[16:17], v6 offset:49280\n\t" \
        "ds_read_b64_tr_b16 a[18:19], v6 offset:52352\n\t" \
        "v_mfma_f32_16x16x32_bf16 v[38:41], a[80:83], v[86:89], v[38:41]\n\t" \
        "ds_read_b64_tr_b16 a[20:21], v7 offset:49312\n\t" \
        "v_cvt_pk_bf16_f32 v97, v108, v109\n\t" \
        "v_mfma_f32_16x16x32_bf16 v[62:65], a[80:83], v[90:93], v[62:65]\n\t" \
        "ds_read_b64_tr_b16 a[22:23], v7 offset:52384\n\t" \
        "v_cvt_pk_bf16_f32 v98, v110, v111\n\t" \
        "v_mfma_f32_16x16x32_bf16 v[42:45], a[84:87], v[86:89], v[42:45]\n\t" \
        "ds_read_b64_tr_b16 a[128:129], v142 offset:128\n\t" \
        "ds_read_b64_tr_b16 a[130:131], v143 offset:128\n\t" \
        "v_mfma_f32_16x16x32_bf16 v[66:69], a[84:87], v[90:93], v[66:69]\n\t" \
        "ds_read_b64_tr_b16 a[132:133], v146 offset:128\n\t" \
        "v_xor_b32 v134, v170, v11\n\t" \
        "v_mfma_f32_16x16x32_bf16 v[46:49], a[88:91], v[86:89], v[46:49]\n\t" \
        "ds_read_b64_tr_b16 a[134:135], v147 offset:128\n\t" \
        "ds_read_b64_tr_b16 a[136:137], v144 offset:128\n\t" \
        "v_mfma_f32_16x16x32_bf16 v[70:73], a[88:91], v[90:93], v[70:73]\n\t" \
        "ds_read_b64_tr_b16 a[138:139], v145 offset:128\n\t" \
        "v_cvt_pk_bf16_f32 v99, v112, v113\n\t" \
        "v_mfma_f32_16x16x32_bf16 v[50:53], a[92:95], v[86:89], v[50:53]\n\t" \
        "ds_read_b64_tr_b16 a[140:141], v148 offset:128\n\t" \
        "v_cvt_pk_bf16_f32 v100, v114, v115\n\t" \
        "v_mfma_f32_16x16x32_bf16 v[74:77], a[92:95], v[90:93], v[74:77]\n\t" \
        "ds_read_b64_tr_b16 a[142:143], v149 offset:128\n\t" \
        "ds_read_b64_tr_b16 a[24:25], v6 offset:55296\n\t" \
        "v_xor_b32 v135, v171, v11\n\t" \
        "v_mfma_f32_16x16x32_bf16 v[102:105], a[112:115], v[162:165], 0\n\t" \
        "ds_read_b64_tr_b16 a[26:27], v6 offset:58368\n\t" \
        "v_cvt_pk_bf16_f32 v101, v116, v117\n\t" \
        "v_mfma_f32_16x16x32_bf16 v[106:109], a[116:119], v[162:165], 0\n\t" \
        "ds_read_b64_tr_b16 a[28:29], v7 offset:55328\n\t" \
        "ds_read_b64 v[150:151], v10 offset:512\n\t" \
        "v_xor_b32 v136, v172, v11\n\t" \
        "v_xor_b32 v138, v170, v12\n\t" \
        "s_waitcnt lgkmcnt(13)\n\t" \
        "v_mfma_f32_16x16x32_bf16 v[110:113], a[120:123], v[166:169], 0\n\t" \
        "ds_read_b64_tr_b16 a[30:31], v7 offset:58400\n\t" \
        "v_xor_b32 v137, v173, v11\n\t" \
        "v_xor_b32 v139, v171, v12\n\t" \
        "v_mfma_f32_16x16x32_bf16 v[114:117], a[124:127], v[166:169], 0\n\t" \
        "ds_read_b64_tr_b16 a[32:33], v6 offset:55360\n\t" \
        "ds_read_b64_tr_b16 a[34:35], v6 offset:58432\n\t" \
        "v_xor_b32 v140, v172, v12\n\t" \
        "v_mfma_f32_16x16x32_bf16 v[30:33], a[0:3], v[94:97], v[30:33]\n\t" \
        "ds_read_b64_tr_b16 a[36:37], v7 offset:55392\n\t" \
        "v_xor_b32 v141, v173, v12\n\t" \
        "v_mfma_f32_16x16x32_bf16 v[54:57], a[0:3], v[98:101], v[54:57]\n\t" \
        "ds_read_b64_tr_b16 a[38:39], v7 offset:58464\n\t" \
        "v_cvt_pk_bf16_f32 v78, v118, v119\n\t" \
        "v_mfma_f32_16x16x32_bf16 v[34:37], a[4:7], v[94:97], v[34:37]\n\t" \
        "ds_read_b64_tr_b16 a[40:41], v6 offset:55424\n\t" \
        "ds_read_b64_tr_b16 a[42:43], v6 offset:58496\n\t" \
        "v_mfma_f32_16x16x32_bf16 v[58:61], a[4:7], v[98:101], v[58:61]\n\t" \
        "s_waitcnt vmcnt(6)\n\t" \
        "s_barrier\n\t" \
        "ds_read_b64_tr_b16 a[44:45], v7 offset:55456\n\t" \
        "v_cvt_pk_bf16_f32 v79, v120, v121\n\t" \
        "v_mfma_f32_16x16x32_bf16 v[38:41], a[8:11], v[94:97], v[38:41]\n\t" \
        "ds_read_b64_tr_b16 a[46:47], v7 offset:58528\n\t" \
        "ds_read_b64_tr_b16 a[96:97], v134 offset:0\n\t" \
        "v_mfma_f32_16x16x32_bf16 v[62:65], a[8:11], v[98:101], v[62:65]\n\t" \
        "ds_read_b64_tr_b16 a[98:99], v135 offset:0\n\t" \
        "v_cvt_pk_bf16_f32 v80, v122, v123\n\t" \
        "v_mfma_f32_16x16x32_bf16 v[42:45], a[12:15], v[94:97], v[42:45]\n\t" \
        "s_add_u32 m0, s39, 36864\n\t" \
        "s_add_u32 s57, s40, 98304\n\t" \
        "buffer_load_dwordx4 v3, s[24:27], s57 offen offset:0  lds\n\t" \
        "v_mfma_f32_16x16x32_bf16 v[66:69], a[12:15], v[98:101], v[66:69]\n\t" \
        "ds_read_b64_tr_b16 a[100:101], v138 offset:0\n\t" \
        "v_cvt_pk_bf16_f32 v81, v124, v125\n\t" \
        "v_mfma_f32_16x16x32_bf16 v[46:49], a[16:19], v[94:97], v[46:49]\n\t" \
        "ds_read_b64_tr_b16 a[102:103], v139 offset:0\n\t" \
        "ds_read_b64_tr_b16 a[104:105], v136 offset:0\n\t" \
        "v_mfma_f32_16x16x32_bf16 v[70:73], a[16:19], v[98:101], v[70:73]\n\t" \
        "ds_read_b64_tr_b16 a[106:107], v137 offset:0\n\t" \
        "v_cvt_pk_bf16_f32 v82, v126, v127\n\t" \
        "s_waitcnt lgkmcnt(11)\n\t" \
        "v_mfma_f32_16x16x32_bf16 v[50:53], a[20:23], v[94:97], v[50:53]\n\t" \
        "ds_read_b64_tr_b16 a[108:109], v140 offset:0\n\t" \
        "ds_read_b64_tr_b16 a[110:111], v141 offset:0\n\t" \
        "v_cvt_pk_bf16_f32 v83, v128, v129\n\t" \
        "v_mfma_f32_16x16x32_bf16 v[74:77], a[20:23], v[98:101], v[74:77]\n\t" \
        "ds_read_b64_tr_b16 a[48:49], v6 offset:0\n\t" \
        "v_cvt_pk_bf16_f32 v84, v130, v131\n\t" \
        "v_cvt_pk_bf16_f32 v85, v132, v133\n\t" \
        "v_mfma_f32_16x16x32_bf16 v[118:121], a[128:131], v[162:165], 0\n\t" \
        "buffer_load_dwordx4 v4, s[24:27], s57 offen offset:1024  lds\n\t" \
        "v_mfma_f32_16x16x32_bf16 v[122:125], a[132:135], v[162:165], 0\n\t" \
        "ds_read_b64_tr_b16 a[50:51], v6 offset:3072\n\t" \
        "ds_read_b64_tr_b16 a[52:53], v7 offset:32\n\t" \
        "v_mfma_f32_16x16x32_bf16 v[126:129], a[136:139], v[166:169], 0\n\t" \
        "ds_read_b64_tr_b16 a[54:55], v7 offset:3104\n\t" \
        "v_cvt_pk_bf16_f32 v86, v102, v103\n\t" \
        "v_mfma_f32_16x16x32_bf16 v[130:133], a[140:143], v[166:169], 0\n\t" \
        "ds_read_b64_tr_b16 a[56:57], v6 offset:64\n\t" \
        "v_and_b32 v154, v150, v25\n\t" \
        "v_mfma_f32_16x16x32_bf16 v[30:33], a[24:27], v[78:81], v[30:33]\n\t" \
        "ds_read_b64_tr_b16 a[58:59], v6 offset:3136\n\t" \
        "ds_read_b64_tr_b16 a[60:61], v7 offset:96\n\t" \
        "v_mfma_f32_16x16x32_bf16 v[54:57], a[24:27], v[82:85], v[54:57]\n\t" \
        "ds_read_b64_tr_b16 a[62:63], v7 offset:3168\n\t" \
        "v_and_b32 v155, v150, v26\n\t" \
        "v_mfma_f32_16x16x32_bf16 v[34:37], a[28:31], v[78:81], v[34:37]\n\t" \
        "buffer_load_dwordx4 v5, s[24:27], s57 offen offset:2048  lds\n\t" \
        "v_mfma_f32_16x16x32_bf16 v[58:61], a[28:31], v[82:85], v[58:61]\n\t" \
        "ds_read_b64_tr_b16 a[64:65], v6 offset:128\n\t" \
        "ds_read_b128 v[174:177], v9 offset:640\n\t" \
        "v_mfma_f32_16x16x32_bf16 v[38:41], a[32:35], v[78:81], v[38:41]\n\t" \
        "ds_read_b64_tr_b16 a[66:67], v6 offset:3200\n\t" \
        "v_and_b32 v156, v150, v27\n\t" \
        "v_mfma_f32_16x16x32_bf16 v[62:65], a[32:35], v[82:85], v[62:65]\n\t" \
        "ds_read_b64_tr_b16 a[68:69], v7 offset:160\n\t" \
        "ds_read_b64_tr_b16 a[70:71], v7 offset:3232\n\t" \
        "s_waitcnt lgkmcnt(12)\n\t" \
        "v_mfma_f32_16x16x32_bf16 v[42:45], a[36:39], v[78:81], v[42:45]\n\t" \
        "ds_read_b64_tr_b16 a[112:113], v134 offset:64\n\t" \
        "v_and_b32 v157, v150, v28\n\t" \
        "v_and_b32 v158, v151, v25\n\t" \
        "v_mfma_f32_16x16x32_bf16 v[66:69], a[36:39], v[82:85], v[66:69]\n\t" \
        "ds_read_b64_tr_b16 a[114:115], v135 offset:64\n\t" \
        "v_cvt_pk_bf16_f32 v87, v104, v105\n\t" \
        "v_mfma_f32_16x16x32_bf16 v[46:49], a[40:43], v[78:81], v[46:49]\n\t" \
        "ds_read_b64_tr_b16 a[116:117], v138 offset:64\n\t" \
        "ds_read_b64_tr_b16 a[118:119], v139 offset:64\n\t" \
        "v_cvt_pk_bf16_f32 v88, v106, v107\n\t" \
        "v_and_b32 v159, v151, v26\n\t" \
        "v_mfma_f32_16x16x32_bf16 v[70:73], a[40:43], v[82:85], v[70:73]\n\t" \
        "ds_read_b64_tr_b16 a[120:121], v136 offset:64\n\t" \
        "v_cvt_pk_bf16_f32 v89, v108, v109\n\t" \
        "v_cvt_pk_bf16_f32 v90, v110, v111\n\t" \
        "v_mfma_f32_16x16x32_bf16 v[50:53], a[44:47], v[78:81], v[50:53]\n\t" \
        "ds_read_b64_tr_b16 a[122:123], v137 offset:64\n\t" \
        "ds_read_b64_tr_b16 a[124:125], v140 offset:64\n\t" \
        "v_and_b32 v160, v151, v27\n\t" \
        "v_mfma_f32_16x16x32_bf16 v[74:77], a[44:47], v[82:85], v[74:77]\n\t" \
        "ds_read_b64_tr_b16 a[126:127], v141 offset:64\n\t" \
        "v_cvt_pk_bf16_f32 v91, v112, v113\n\t" \
        "v_cvt_pk_bf16_f32 v92, v114, v115\n\t" \
        "v_mfma_f32_16x16x32_bf16 v[102:105], a[96:99], v[154:157], 0\n\t" \
        "ds_read_b64_tr_b16 a[72:73], v6 offset:6144\n\t" \
        "v_and_b32 v161, v151, v28\n\t" \
        "v_cvt_pk_bf16_f32 v93, v116, v117\n\t" \
        "v_mfma_f32_16x16x32_bf16 v[106:109], a[100:103], v[154:157], 0\n\t" \
        "ds_read_b64_tr_b16 a[74:75], v6 offset:9216\n\t" \
        "ds_read_b64_tr_b16 a[76:77], v7 offset:6176\n\t" \
        "v_mfma_f32_16x16x32_bf16 v[110:113], a[104:107], v[158:161], 0\n\t" \
        "ds_read_b64_tr_b16 a[78:79], v7 offset:9248\n\t" \
        "v_cvt_pk_bf16_f32 v94, v118, v119\n\t" \
        "v_mfma_f32_16x16x32_bf16 v[114:117], a[108:111], v[158:161], 0\n\t" \
        "ds_read_b64_tr_b16 a[80:81], v6 offset:6208\n\t" \
        "ds_read_b64_tr_b16 a[82:83], v6 offset:9280\n\t" \
        "s_waitcnt lgkmcnt(13)\n\t" \
        "v_mfma_f32_16x16x32_bf16 v[30:33], a[48:51], v[86:89], v[30:33]\n\t" \
        "ds_read_b64_tr_b16 a[84:85], v7 offset:6240\n\t" \
        "v_cvt_pk_bf16_f32 v95, v120, v121\n\t" \
        "v_mfma_f32_16x16x32_bf16 v[54:57], a[48:51], v[90:93], v[54:57]\n\t" \
        "ds_read_b64_tr_b16 a[86:87], v7 offset:9312\n\t" \
        "ds_read_b64_tr_b16 a[88:89], v6 offset:6272\n\t" \
        "v_mfma_f32_16x16x32_bf16 v[34:37], a[52:55], v[86:89], v[34:37]\n\t" \
        "ds_read_b64_tr_b16 a[90:91], v6 offset:9344\n\t" \
        "v_cvt_pk_bf16_f32 v96, v122, v123\n\t" \
        "v_mfma_f32_16x16x32_bf16 v[58:61], a[52:55], v[90:93], v[58:61]\n\t" \
        "s_waitcnt vmcnt(6)\n\t" \
        "s_barrier\n\t" \
        "ds_read_b64_tr_b16 a[92:93], v7 offset:6304\n\t" \
        "v_cvt_pk_bf16_f32 v97, v124, v125\n\t" \
        "v_mfma_f32_16x16x32_bf16 v[38:41], a[56:59], v[86:89], v[38:41]\n\t" \
        "ds_read_b64_tr_b16 a[94:95], v7 offset:9376\n\t" \
        "ds_read_b64_tr_b16 a[128:129], v134 offset:128\n\t" \
        "v_mfma_f32_16x16x32_bf16 v[62:65], a[56:59], v[90:93], v[62:65]\n\t" \
        "ds_read_b64_tr_b16 a[130:131], v135 offset:128\n\t" \
        "v_cvt_pk_bf16_f32 v98, v126, v127\n\t" \
        "v_mfma_f32_16x16x32_bf16 v[42:45], a[60:63], v[86:89], v[42:45]\n\t" \
        "s_add_u32 m0, s39, 49152\n\t" \
        "s_add_u32 s57, s40, 110592\n\t" \
        "buffer_load_dwordx4 v3, s[24:27], s57 offen offset:0  lds\n\t" \
        "v_mfma_f32_16x16x32_bf16 v[66:69], a[60:63], v[90:93], v[66:69]\n\t" \
        "ds_read_b64_tr_b16 a[132:133], v138 offset:128\n\t" \
        "ds_read_b64_tr_b16 a[134:135], v139 offset:128\n\t" \
        "v_mfma_f32_16x16x32_bf16 v[46:49], a[64:67], v[86:89], v[46:49]\n\t" \
        "ds_read_b64_tr_b16 a[136:137], v136 offset:128\n\t" \
        "v_xor_b32 v142, v174, v11\n\t" \
        "v_mfma_f32_16x16x32_bf16 v[70:73], a[64:67], v[90:93], v[70:73]\n\t" \
        "ds_read_b64_tr_b16 a[138:139], v137 offset:128\n\t" \
        "ds_read_b64_tr_b16 a[140:141], v140 offset:128\n\t" \
        "v_cvt_pk_bf16_f32 v99, v128, v129\n\t" \
        "v_mfma_f32_16x16x32_bf16 v[50:53], a[68:71], v[86:89], v[50:53]\n\t" \
        "ds_read_b64_tr_b16 a[142:143], v141 offset:128\n\t" \
        "v_cvt_pk_bf16_f32 v100, v130, v131\n\t" \
        "v_xor_b32 v143, v175, v11\n\t" \
        "v_mfma_f32_16x16x32_bf16 v[74:77], a[68:71], v[90:93], v[74:77]\n\t" \
        "ds_read_b64_tr_b16 a[0:1], v6 offset:12288\n\t" \
        "v_cvt_pk_bf16_f32 v101, v132, v133\n\t" \
        "s_waitcnt lgkmcnt(11)\n\t" \
        "v_mfma_f32_16x16x32_bf16 v[118:121], a[112:115], v[154:157], 0\n\t" \
        "buffer_load_dwordx4 v4, s[24:27], s57 offen offset:1024  lds\n\t" \
        "v_mfma_f32_16x16x32_bf16 v[122:125], a[116:119], v[154:157], 0\n\t" \
        "ds_read_b64_tr_b16 a[2:3], v6 offset:15360\n\t" \
        "ds_read_b64_tr_b16 a[4:5], v7 offset:12320\n\t" \
        "v_xor_b32 v144, v176, v11\n\t" \
        "v_xor_b32 v146, v174, v12\n\t" \
        "v_mfma_f32_16x16x32_bf16 v[126:129], a[120:123], v[158:161], 0\n\t" \
        "ds_read_b64 v[152:153], v10 offset:640\n\t" \
        "v_xor_b32 v145, v177, v11\n\t" \
        "v_xor_b32 v147, v175, v12\n\t" \
        "v_mfma_f32_16x16x32_bf16 v[130:133], a[124:127], v[158:161], 0\n\t" \
        "ds_read_b64_tr_b16 a[6:7], v7 offset:15392\n\t" \
        "ds_read_b64_tr_b16 a[8:9], v6 offset:12352\n\t" \
        "v_xor_b32 v148, v176, v12\n\t" \
        "v_mfma_f32_16x16x32_bf16 v[30:33], a[72:75], v[94:97], v[30:33]\n\t" \
        "ds_read_b64_tr_b16 a[10:11], v6 offset:15424\n\t" \
        "v_xor_b32 v149, v177, v12\n\t" \
        "v_mfma_f32_16x16x32_bf16 v[54:57], a[72:75], v[98:101], v[54:57]\n\t" \
        "ds_read_b64_tr_b16 a[12:13], v7 offset:12384\n\t" \
        "ds_read_b64_tr_b16 a[14:15], v7 offset:15456\n\t" \
        "v_mfma_f32_16x16x32_bf16 v[34:37], a[76:79], v[94:97], v[34:37]\n\t" \
        "buffer_load_dwordx4 v5, s[24:27], s57 offen offset:2048  lds\n\t" \
        "v_mfma_f32_16x16x32_bf16 v[58:61], a[76:79], v[98:101], v[58:61]\n\t" \
        "ds_read_b64_tr_b16 a[16:17], v6 offset:12416\n\t" \
        "v_cvt_pk_bf16_f32 v78, v102, v103\n\t" \
        "v_mfma_f32_16x16x32_bf16 v[38:41], a[80:83], v[94:97], v[38:41]\n\t" \
        "ds_read_b64_tr_b16 a[18:19], v6 offset:15488\n\t" \
        "v_cvt_pk_bf16_f32 v79, v104, v105\n\t" \
        "v_mfma_f32_16x16x32_bf16 v[62:65], a[80:83], v[98:101], v[62:65]\n\t" \
        "ds_read_b64_tr_b16 a[20:21], v7 offset:12448\n\t" \
        "ds_read_b64_tr_b16 a[22:23], v7 offset:15520\n\t" \
        "v_mfma_f32_16x16x32_bf16 v[42:45], a[84:87], v[94:97], v[42:45]\n\t" \
        "ds_read_b64_tr_b16 a[96:97], v142 offset:0\n\t" \
        "v_cvt_pk_bf16_f32 v80, v106, v107\n\t" \
        "v_mfma_f32_16x16x32_bf16 v[66:69], a[84:87], v[98:101], v[66:69]\n\t" \
        "ds_read_b64_tr_b16 a[98:99], v143 offset:0\n\t" \
        "ds_read_b64_tr_b16 a[100:101], v146 offset:0\n\t" \
        "v_mfma_f32_16x16x32_bf16 v[46:49], a[88:91], v[94:97], v[46:49]\n\t" \
        "ds_read_b64_tr_b16 a[102:103], v147 offset:0\n\t" \
        "v_cvt_pk_bf16_f32 v81, v108, v109\n\t" \
        "v_mfma_f32_16x16x32_bf16 v[70:73], a[88:91], v[98:101], v[70:73]\n\t" \
        "ds_read_b64_tr_b16 a[104:105], v144 offset:0\n\t" \
        "ds_read_b64_tr_b16 a[106:107], v145 offset:0\n\t" \
        "s_waitcnt lgkmcnt(12)\n\t" \
        "v_mfma_f32_16x16x32_bf16 v[50:53], a[92:95], v[94:97], v[50:53]\n\t" \
        "ds_read_b64_tr_b16 a[108:109], v148 offset:0\n\t" \
        "v_cvt_pk_bf16_f32 v82, v110, v111\n\t" \
        "v_mfma_f32_16x16x32_bf16 v[74:77], a[92:95], v[98:101], v[74:77]\n\t" \
        "ds_read_b64_tr_b16 a[110:111], v149 offset:0\n\t" \
        "v_cvt_pk_bf16_f32 v83, v112, v113\n\t" \
        "v_cvt_pk_bf16_f32 v84, v114, v115\n\t" \
        "v_mfma_f32_16x16x32_bf16 v[102:105], a[128:131], v[154:157], 0\n\t" \
        "ds_read_b64_tr_b16 a[24:25], v6 offset:18432\n\t" \
        "ds_read_b64_tr_b16 a[26:27], v6 offset:21504\n\t" \
        "v_cvt_pk_bf16_f32 v85, v116, v117\n\t" \
        "v_mfma_f32_16x16x32_bf16 v[106:109], a[132:135], v[154:157], 0\n\t" \
        "ds_read_b64_tr_b16 a[28:29], v7 offset:18464\n\t" \
        "v_cvt_pk_bf16_f32 v86, v118, v119\n\t" \
        "v_mfma_f32_16x16x32_bf16 v[110:113], a[136:139], v[158:161], 0\n\t" \
        "ds_read_b64_tr_b16 a[30:31], v7 offset:21536\n\t" \
        "ds_read_b64_tr_b16 a[32:33], v6 offset:18496\n\t" \
        "v_mfma_f32_16x16x32_bf16 v[114:117], a[140:143], v[158:161], 0\n\t" \
        "ds_read_b64_tr_b16 a[34:35], v6 offset:21568\n\t" \
        "v_cvt_pk_bf16_f32 v87, v120, v121\n\t" \
        "v_mfma_f32_16x16x32_bf16 v[30:33], a[0:3], v[78:81], v[30:33]\n\t" \
        "ds_read_b64_tr_b16 a[36:37], v7 offset:18528\n\t" \
        "ds_read_b64_tr_b16 a[38:39], v7 offset:21600\n\t" \
        "v_mfma_f32_16x16x32_bf16 v[54:57], a[0:3], v[82:85], v[54:57]\n\t" \
        "ds_read_b64_tr_b16 a[40:41], v6 offset:18560\n\t" \
        "v_and_b32 v162, v152, v25\n\t" \
        "v_mfma_f32_16x16x32_bf16 v[34:37], a[4:7], v[78:81], v[34:37]\n\t" \
        "ds_read_b128 v[170:173], v9 offset:768\n\t" \
        "ds_read_b64_tr_b16 a[42:43], v6 offset:21632\n\t" \
        "v_mfma_f32_16x16x32_bf16 v[58:61], a[4:7], v[82:85], v[58:61]\n\t" \
        "s_waitcnt vmcnt(6)\n\t" \
        "s_barrier\n\t" \
        "ds_read_b64_tr_b16 a[44:45], v7 offset:18592\n\t" \
        "v_and_b32 v163, v152, v26\n\t" \
        "v_mfma_f32_16x16x32_bf16 v[38:41], a[8:11], v[78:81], v[38:41]\n\t" \
        "ds_read_b64_tr_b16 a[46:47], v7 offset:21664\n\t" \
        "v_and_b32 v164, v152, v27\n\t" \
        "v_and_b32 v165, v152, v28\n\t" \
        "v_mfma_f32_16x16x32_bf16 v[62:65], a[8:11], v[82:85], v[62:65]\n\t" \
        "ds_read_b64_tr_b16 a[112:113], v142 offset:64\n\t" \
        "ds_read_b64_tr_b16 a[114:115], v143 offset:64\n\t" \
        "v_and_b32 v166, v153, v25\n\t" \
        "s_waitcnt lgkmcnt(13)\n\t" \
        "v_mfma_f32_16x16x32_bf16 v[42:45], a[12:15], v[78:81], v[42:45]\n\t" \
        "s_add_u32 m0, s39, 0\n\t" \
        "s_add_u32 s57, s40, 122880\n\t" \
        "buffer_load_dwordx4 v3, s[24:27], s57 offen offset:0  lds\n\t" \
        "v_mfma_f32_16x16x32_bf16 v[66:69], a[12:15], v[82:85], v[66:69]\n\t" \
        "ds_read_b64_tr_b16 a[116:117], v146 offset:64\n\t" \
        "v_cvt_pk_bf16_f32 v88, v122, v123\n\t" \
        "v_and_b32 v167, v153, v26\n\t" \
        "v_mfma_f32_16x16x32_bf16 v[46:49], a[16:19], v[78:81], v[46:49]\n\t" \
        "ds_read_b64_tr_b16 a[118:119], v147 offset:64\n\t" \
        "ds_read_b64_tr_b16 a[120:121], v144 offset:64\n\t" \
        "v_cvt_pk_bf16_f32 v89, v124, v125\n\t" \
        "v_cvt_pk_bf16_f32 v90, v126, v127\n\t" \
        "v_mfma_f32_16x16x32_bf16 v[70:73], a[16:19], v[82:85], v[70:73]\n\t" \
        "ds_read_b64_tr_b16 a[122:123], v145 offset:64\n\t" \
        "v_and_b32 v168, v153, v27\n\t" \
        "v_mfma_f32_16x16x32_bf16 v[50:53], a[20:23], v[78:81], v[50:53]\n\t" \
        "ds_read_b64_tr_b16 a[124:125], v148 offset:64\n\t" \
        "ds_read_b64_tr_b16 a[126:127], v149 offset:64\n\t" \
        "v_cvt_pk_bf16_f32 v91, v128, v129\n\t" \
        "v_cvt_pk_bf16_f32 v92, v130, v131\n\t" \
        "v_mfma_f32_16x16x32_bf16 v[74:77], a[20:23], v[82:85], v[74:77]\n\t" \
        "ds_read_b64_tr_b16 a[48:49], v6 offset:24576\n\t" \
        "v_and_b32 v169, v153, v28\n\t" \
        "v_cvt_pk_bf16_f32 v93, v132, v133\n\t" \
        "v_mfma_f32_16x16x32_bf16 v[118:121], a[96:99], v[162:165], 0\n\t" \
        "buffer_load_dwordx4 v4, s[24:27], s57 offen offset:1024  lds\n\t" \
        "v_mfma_f32_16x16x32_bf16 v[122:125], a[100:103], v[162:165], 0\n\t" \
        "ds_read_b64_tr_b16 a[50:51], v6 offset:27648\n\t" \
        "ds_read_b64_tr_b16 a[52:53], v7 offset:24608\n\t" \
        "v_mfma_f32_16x16x32_bf16 v[126:129], a[104:107], v[166:169], 0\n\t" \
        "ds_read_b64_tr_b16 a[54:55], v7 offset:27680\n\t" \
        "v_cvt_pk_bf16_f32 v94, v102, v103\n\t" \
        "v_mfma_f32_16x16x32_bf16 v[130:133], a[108:111], v[166:169], 0\n\t" \
        "ds_read_b64_tr_b16 a[56:57], v6 offset:24640\n\t" \
        "v_cvt_pk_bf16_f32 v95, v104, v105\n\t" \
        "v_mfma_f32_16x16x32_bf16 v[30:33], a[24:27], v[86:89], v[30:33]\n\t" \
        "ds_read_b64_tr_b16 a[58:59], v6 offset:27712\n\t" \
        "ds_read_b64_tr_b16 a[60:61], v7 offset:24672\n\t" \
        "v_mfma_f32_16x16x32_bf16 v[54:57], a[24:27], v[90:93], v[54:57]\n\t" \
        "ds_read_b64_tr_b16 a[62:63], v7 offset:27744\n\t" \
        "v_cvt_pk_bf16_f32 v96, v106, v107\n\t" \
        "s_waitcnt lgkmcnt(11)\n\t" \
        "v_mfma_f32_16x16x32_bf16 v[34:37], a[28:31], v[86:89], v[34:37]\n\t" \
        "buffer_load_dwordx4 v5, s[24:27], s57 offen offset:2048  lds\n\t" \
        "v_mfma_f32_16x16x32_bf16 v[58:61], a[28:31], v[90:93], v[58:61]\n\t" \
        "ds_read_b64_tr_b16 a[64:65], v6 offset:24704\n\t" \
        "ds_read_b64_tr_b16 a[66:67], v6 offset:27776\n\t" \
        "v_mfma_f32_16x16x32_bf16 v[38:41], a[32:35], v[86:89], v[38:41]\n\t" \
        "ds_read_b64_tr_b16 a[68:69], v7 offset:24736\n\t" \
        "v_cvt_pk_bf16_f32 v97, v108, v109\n\t" \
        "v_mfma_f32_16x16x32_bf16 v[62:65], a[32:35], v[90:93], v[62:65]\n\t" \
        "ds_read_b64_tr_b16 a[70:71], v7 offset:27808\n\t" \
        "v_cvt_pk_bf16_f32 v98, v110, v111\n\t" \
        "v_mfma_f32_16x16x32_bf16 v[42:45], a[36:39], v[86:89], v[42:45]\n\t" \
        "ds_read_b64_tr_b16 a[128:129], v142 offset:128\n\t" \
        "ds_read_b64_tr_b16 a[130:131], v143 offset:128\n\t" \
        "v_mfma_f32_16x16x32_bf16 v[66:69], a[36:39], v[90:93], v[66:69]\n\t" \
        "ds_read_b64_tr_b16 a[132:133], v146 offset:128\n\t" \
        "v_xor_b32 v134, v170, v11\n\t" \
        "v_mfma_f32_16x16x32_bf16 v[46:49], a[40:43], v[86:89], v[46:49]\n\t" \
        "ds_read_b64_tr_b16 a[134:135], v147 offset:128\n\t" \
        "ds_read_b64_tr_b16 a[136:137], v144 offset:128\n\t" \
        "v_mfma_f32_16x16x32_bf16 v[70:73], a[40:43], v[90:93], v[70:73]\n\t" \
        "ds_read_b64_tr_b16 a[138:139], v145 offset:128\n\t" \
        "v_cvt_pk_bf16_f32 v99, v112, v113\n\t" \
        "v_mfma_f32_16x16x32_bf16 v[50:53], a[44:47], v[86:89], v[50:53]\n\t" \
        "ds_read_b64_tr_b16 a[140:141], v148 offset:128\n\t" \
        "v_cvt_pk_bf16_f32 v100, v114, v115\n\t" \
        "v_mfma_f32_16x16x32_bf16 v[74:77], a[44:47], v[90:93], v[74:77]\n\t" \
        "ds_read_b64_tr_b16 a[142:143], v149 offset:128\n\t" \
        "ds_read_b64_tr_b16 a[72:73], v6 offset:30720\n\t" \
        "v_xor_b32 v135, v171, v11\n\t" \
        "v_mfma_f32_16x16x32_bf16 v[102:105], a[112:115], v[162:165], 0\n\t" \
        "ds_read_b64_tr_b16 a[74:75], v6 offset:33792\n\t" \
        "v_cvt_pk_bf16_f32 v101, v116, v117\n\t" \
        "v_mfma_f32_16x16x32_bf16 v[106:109], a[116:119], v[162:165], 0\n\t" \
        "ds_read_b64_tr_b16 a[76:77], v7 offset:30752\n\t" \
        "ds_read_b64 v[150:151], v10 offset:768\n\t" \
        "v_xor_b32 v136, v172, v11\n\t" \
        "v_xor_b32 v138, v170, v12\n\t" \
        "s_waitcnt lgkmcnt(13)\n\t" \
        "v_mfma_f32_16x16x32_bf16 v[110:113], a[120:123], v[166:169], 0\n\t" \
        "ds_read_b64_tr_b16 a[78:79], v7 offset:33824\n\t" \
        "v_xor_b32 v137, v173, v11\n\t" \
        "v_xor_b32 v139, v171, v12\n\t" \
        "v_mfma_f32_16x16x32_bf16 v[114:117], a[124:127], v[166:169], 0\n\t" \
        "ds_read_b64_tr_b16 a[80:81], v6 offset:30784\n\t" \
        "ds_read_b64_tr_b16 a[82:83], v6 offset:33856\n\t" \
        "v_xor_b32 v140, v172, v12\n\t" \
        "v_mfma_f32_16x16x32_bf16 v[30:33], a[48:51], v[94:97], v[30:33]\n\t" \
        "ds_read_b64_tr_b16 a[84:85], v7 offset:30816\n\t" \
        "v_xor_b32 v141, v173, v12\n\t" \
        "v_mfma_f32_16x16x32_bf16 v[54:57], a[48:51], v[98:101], v[54:57]\n\t" \
        "ds_read_b64_tr_b16 a[86:87], v7 offset:33888\n\t" \
        "v_cvt_pk_bf16_f32 v78, v118, v119\n\t" \
        "v_mfma_f32_16x16x32_bf16 v[34:37], a[52:55], v[94:97], v[34:37]\n\t" \
        "ds_read_b64_tr_b16 a[88:89], v6 offset:30848\n\t" \
        "ds_read_b64_tr_b16 a[90:91], v6 offset:33920\n\t" \
        "v_mfma_f32_16x16x32_bf16 v[58:61], a[52:55], v[98:101], v[58:61]\n\t" \
        "s_waitcnt vmcnt(6)\n\t" \
        "s_barrier\n\t" \
        "ds_read_b64_tr_b16 a[92:93], v7 offset:30880\n\t" \
        "v_cvt_pk_bf16_f32 v79, v120, v121\n\t" \
        "v_mfma_f32_16x16x32_bf16 v[38:41], a[56:59], v[94:97], v[38:41]\n\t" \
        "ds_read_b64_tr_b16 a[94:95], v7 offset:33952\n\t" \
        "ds_read_b64_tr_b16 a[96:97], v134 offset:0\n\t" \
        "v_mfma_f32_16x16x32_bf16 v[62:65], a[56:59], v[98:101], v[62:65]\n\t" \
        "ds_read_b64_tr_b16 a[98:99], v135 offset:0\n\t" \
        "v_cvt_pk_bf16_f32 v80, v122, v123\n\t" \
        "v_mfma_f32_16x16x32_bf16 v[42:45], a[60:63], v[94:97], v[42:45]\n\t" \
        "s_add_u32 m0, s39, 12288\n\t" \
        "s_add_u32 s57, s40, 135168\n\t" \
        "buffer_load_dwordx4 v3, s[24:27], s57 offen offset:0  lds\n\t" \
        "v_mfma_f32_16x16x32_bf16 v[66:69], a[60:63], v[98:101], v[66:69]\n\t" \
        "ds_read_b64_tr_b16 a[100:101], v138 offset:0\n\t" \
        "v_cvt_pk_bf16_f32 v81, v124, v125\n\t" \
        "v_mfma_f32_16x16x32_bf16 v[46:49], a[64:67], v[94:97], v[46:49]\n\t" \
        "ds_read_b64_tr_b16 a[102:103], v139 offset:0\n\t" \
        "ds_read_b64_tr_b16 a[104:105], v136 offset:0\n\t" \
        "v_mfma_f32_16x16x32_bf16 v[70:73], a[64:67], v[98:101], v[70:73]\n\t" \
        "ds_read_b64_tr_b16 a[106:107], v137 offset:0\n\t" \
        "v_cvt_pk_bf16_f32 v82, v126, v127\n\t" \
        "s_waitcnt lgkmcnt(11)\n\t" \
        "v_mfma_f32_16x16x32_bf16 v[50:53], a[68:71], v[94:97], v[50:53]\n\t" \
        "ds_read_b64_tr_b16 a[108:109], v140 offset:0\n\t" \
        "ds_read_b64_tr_b16 a[110:111], v141 offset:0\n\t" \
        "v_cvt_pk_bf16_f32 v83, v128, v129\n\t" \
        "v_mfma_f32_16x16x32_bf16 v[74:77], a[68:71], v[98:101], v[74:77]\n\t" \
        "ds_read_b64_tr_b16 a[0:1], v6 offset:36864\n\t" \
        "v_cvt_pk_bf16_f32 v84, v130, v131\n\t" \
        "v_cvt_pk_bf16_f32 v85, v132, v133\n\t" \
        "v_mfma_f32_16x16x32_bf16 v[118:121], a[128:131], v[162:165], 0\n\t" \
        "buffer_load_dwordx4 v4, s[24:27], s57 offen offset:1024  lds\n\t" \
        "v_mfma_f32_16x16x32_bf16 v[122:125], a[132:135], v[162:165], 0\n\t" \
        "ds_read_b64_tr_b16 a[2:3], v6 offset:39936\n\t" \
        "ds_read_b64_tr_b16 a[4:5], v7 offset:36896\n\t" \
        "v_mfma_f32_16x16x32_bf16 v[126:129], a[136:139], v[166:169], 0\n\t" \
        "ds_read_b64_tr_b16 a[6:7], v7 offset:39968\n\t" \
        "v_cvt_pk_bf16_f32 v86, v102, v103\n\t" \
        "v_mfma_f32_16x16x32_bf16 v[130:133], a[140:143], v[166:169], 0\n\t" \
        "ds_read_b64_tr_b16 a[8:9], v6 offset:36928\n\t" \
        "v_and_b32 v154, v150, v25\n\t" \
        "v_mfma_f32_16x16x32_bf16 v[30:33], a[72:75], v[78:81], v[30:33]\n\t" \
        "ds_read_b64_tr_b16 a[10:11], v6 offset:40000\n\t" \
        "ds_read_b64_tr_b16 a[12:13], v7 offset:36960\n\t" \
        "v_mfma_f32_16x16x32_bf16 v[54:57], a[72:75], v[82:85], v[54:57]\n\t" \
        "ds_read_b64_tr_b16 a[14:15], v7 offset:40032\n\t" \
        "v_and_b32 v155, v150, v26\n\t" \
        "v_mfma_f32_16x16x32_bf16 v[34:37], a[76:79], v[78:81], v[34:37]\n\t" \
        "buffer_load_dwordx4 v5, s[24:27], s57 offen offset:2048  lds\n\t" \
        "v_mfma_f32_16x16x32_bf16 v[58:61], a[76:79], v[82:85], v[58:61]\n\t" \
        "ds_read_b64_tr_b16 a[16:17], v6 offset:36992\n\t" \
        "ds_read_b128 v[174:177], v9 offset:896\n\t" \
        "v_mfma_f32_16x16x32_bf16 v[38:41], a[80:83], v[78:81], v[38:41]\n\t" \
        "ds_read_b64_tr_b16 a[18:19], v6 offset:40064\n\t" \
        "v_and_b32 v156, v150, v27\n\t" \
        "v_mfma_f32_16x16x32_bf16 v[62:65], a[80:83], v[82:85], v[62:65]\n\t" \
        "ds_read_b64_tr_b16 a[20:21], v7 offset:37024\n\t" \
        "v_and_b32 v157, v150, v28\n\t" \
        "s_waitcnt lgkmcnt(11)\n\t" \
        "v_mfma_f32_16x16x32_bf16 v[42:45], a[84:87], v[78:81], v[42:45]\n\t" \
        "ds_read_b64_tr_b16 a[22:23], v7 offset:40096\n\t" \
        "ds_read_b64_tr_b16 a[112:113], v134 offset:64\n\t" \
        "v_and_b32 v158, v151, v25\n\t" \
        "v_mfma_f32_16x16x32_bf16 v[66:69], a[84:87], v[82:85], v[66:69]\n\t" \
        "ds_read_b64_tr_b16 a[114:115], v135 offset:64\n\t" \
        "v_cvt_pk_bf16_f32 v87, v104, v105\n\t" \
        "v_mfma_f32_16x16x32_bf16 v[46:49], a[88:91], v[78:81], v[46:49]\n\t" \
        "ds_read_b64_tr_b16 a[116:117], v138 offset:64\n\t" \
        "ds_read_b64_tr_b16 a[118:119], v139 offset:64\n\t" \
        "v_cvt_pk_bf16_f32 v88, v106, v107\n\t" \
        "v_and_b32 v159, v151, v26\n\t" \
        "v_mfma_f32_16x16x32_bf16 v[70:73], a[88:91], v[82:85], v[70:73]\n\t" \
        "ds_read_b64_tr_b16 a[120:121], v136 offset:64\n\t" \
        "v_cvt_pk_bf16_f32 v89, v108, v109\n\t" \
        "v_cvt_pk_bf16_f32 v90, v110, v111\n\t" \
        "v_mfma_f32_16x16x32_bf16 v[50:53], a[92:95], v[78:81], v[50:53]\n\t" \
        "ds_read_b64_tr_b16 a[122:123], v137 offset:64\n\t" \
        "ds_read_b64_tr_b16 a[124:125], v140 offset:64\n\t" \
        "v_and_b32 v160, v151, v27\n\t" \
        "v_mfma_f32_16x16x32_bf16 v[74:77], a[92:95], v[82:85], v[74:77]\n\t" \
        "ds_read_b64_tr_b16 a[126:127], v141 offset:64\n\t" \
        "v_cvt_pk_bf16_f32 v91, v112, v113\n\t" \
        "v_cvt_pk_bf16_f32 v92, v114, v115\n\t" \
        "v_mfma_f32_16x16x32_bf16 v[102:105], a[96:99], v[154:157], 0\n\t" \
        "ds_read_b64_tr_b16 a[24:25], v6 offset:43008\n\t" \
        "v_and_b32 v161, v151, v28\n\t" \
        "v_cvt_pk_bf16_f32 v93, v116, v117\n\t" \
        "v_mfma_f32_16x16x32_bf16 v[106:109], a[100:103], v[154:157], 0\n\t" \
        "ds_read_b64_tr_b16 a[26:27], v6 offset:46080\n\t" \
        "ds_read_b64_tr_b16 a[28:29], v7 offset:43040\n\t" \
        "v_mfma_f32_16x16x32_bf16 v[110:113], a[104:107], v[158:161], 0\n\t" \
        "ds_read_b64_tr_b16 a[30:31], v7 offset:46112\n\t" \
        "v_cvt_pk_bf16_f32 v94, v118, v119\n\t" \
        "v_mfma_f32_16x16x32_bf16 v[114:117], a[108:111], v[158:161], 0\n\t" \
        "ds_read_b64_tr_b16 a[32:33], v6 offset:43072\n\t" \
        "ds_read_b64_tr_b16 a[34:35], v6 offset:46144\n\t" \
        "s_waitcnt lgkmcnt(13)\n\t" \
        "v_mfma_f32_16x16x32_bf16 v[30:33], a[0:3], v[86:89], v[30:33]\n\t" \
        "ds_read_b64_tr_b16 a[36:37], v7 offset:43104\n\t" \
        "v_cvt_pk_bf16_f32 v95, v120, v121\n\t" \
        "v_mfma_f32_16x16x32_bf16 v[54:57], a[0:3], v[90:93], v[54:57]\n\t" \
        "ds_read_b64_tr_b16 a[38:39], v7 offset:46176\n\t" \
        "ds_read_b64_tr_b16 a[40:41], v6 offset:43136\n\t" \
        "v_mfma_f32_16x16x32_bf16 v[34:37], a[4:7], v[86:89], v[34:37]\n\t" \
        "ds_read_b64_tr_b16 a[42:43], v6 offset:46208\n\t" \
        "v_cvt_pk_bf16_f32 v96, v122, v123\n\t" \
        "v_mfma_f32_16x16x32_bf16 v[58:61], a[4:7], v[90:93], v[58:61]\n\t" \
        "s_waitcnt vmcnt(6)\n\t" \
        "s_barrier\n\t" \
        "ds_read_b64_tr_b16 a[44:45], v7 offset:43168\n\t" \
        "v_cvt_pk_bf16_f32 v97, v124, v125\n\t" \
        "v_mfma_f32_16x16x32_bf16 v[38:41], a[8:11], v[86:89], v[38:41]\n\t" \
        "ds_read_b64_tr_b16 a[46:47], v7 offset:46240\n\t" \
        "ds_read_b64_tr_b16 a[128:129], v134 offset:128\n\t" \
        "v_mfma_f32_16x16x32_bf16 v[62:65], a[8:11], v[90:93], v[62:65]\n\t" \
        "ds_read_b64_tr_b16 a[130:131], v135 offset:128\n\t" \
        "v_cvt_pk_bf16_f32 v98, v126, v127\n\t" \
        "v_mfma_f32_16x16x32_bf16 v[42:45], a[12:15], v[86:89], v[42:45]\n\t" \
        "s_add_u32 m0, s39, 24576\n\t" \
        "s_add_u32 s57, s40, 147456\n\t" \
        "buffer_load_dwordx4 v3, s[24:27], s57 offen offset:0  lds\n\t" \
        "v_mfma_f32_16x16x32_bf16 v[66:69], a[12:15], v[90:93], v[66:69]\n\t" \
        "ds_read_b64_tr_b16 a[132:133], v138 offset:128\n\t" \
        "ds_read_b64_tr_b16 a[134:135], v139 offset:128\n\t" \
        "v_mfma_f32_16x16x32_bf16 v[46:49], a[16:19], v[86:89], v[46:49]\n\t" \
        "ds_read_b64_tr_b16 a[136:137], v136 offset:128\n\t" \
        "v_xor_b32 v142, v174, v11\n\t" \
        "v_mfma_f32_16x16x32_bf16 v[70:73], a[16:19], v[90:93], v[70:73]\n\t" \
        "ds_read_b64_tr_b16 a[138:139], v137 offset:128\n\t" \
        "ds_read_b64_tr_b16 a[140:141], v140 offset:128\n\t" \
        "v_cvt_pk_bf16_f32 v99, v128, v129\n\t" \
        "v_mfma_f32_16x16x32_bf16 v[50:53], a[20:23], v[86:89], v[50:53]\n\t" \
        "ds_read_b64_tr_b16 a[142:143], v141 offset:128\n\t" \
        "v_cvt_pk_bf16_f32 v100, v130, v131\n\t" \
        "v_xor_b32 v143, v175, v11\n\t" \
        "v_mfma_f32_16x16x32_bf16 v[74:77], a[20:23], v[90:93], v[74:77]\n\t" \
        "ds_read_b64_tr_b16 a[48:49], v6 offset:49152\n\t" \
        "v_cvt_pk_bf16_f32 v101, v132, v133\n\t" \
        "s_waitcnt lgkmcnt(11)\n\t" \
        "v_mfma_f32_16x16x32_bf16 v[118:121], a[112:115], v[154:157], 0\n\t" \
        "buffer_load_dwordx4 v4, s[24:27], s57 offen offset:1024  lds\n\t" \
        "v_mfma_f32_16x16x32_bf16 v[122:125], a[116:119], v[154:157], 0\n\t" \
        "ds_read_b64_tr_b16 a[50:51], v6 offset:52224\n\t" \
        "ds_read_b64_tr_b16 a[52:53], v7 offset:49184\n\t" \
        "v_xor_b32 v144, v176, v11\n\t" \
        "v_xor_b32 v146, v174, v12\n\t" \
        "v_mfma_f32_16x16x32_bf16 v[126:129], a[120:123], v[158:161], 0\n\t" \
        "ds_read_b64 v[152:153], v10 offset:896\n\t" \
        "v_xor_b32 v145, v177, v11\n\t" \
        "v_xor_b32 v147, v175, v12\n\t" \
        "v_mfma_f32_16x16x32_bf16 v[130:133], a[124:127], v[158:161], 0\n\t" \
        "ds_read_b64_tr_b16 a[54:55], v7 offset:52256\n\t" \
        "ds_read_b64_tr_b16 a[56:57], v6 offset:49216\n\t" \
        "v_xor_b32 v148, v176, v12\n\t" \
        "v_mfma_f32_16x16x32_bf16 v[30:33], a[24:27], v[94:97], v[30:33]\n\t" \
        "ds_read_b64_tr_b16 a[58:59], v6 offset:52288\n\t" \
        "v_xor_b32 v149, v177, v12\n\t" \
        "v_mfma_f32_16x16x32_bf16 v[54:57], a[24:27], v[98:101], v[54:57]\n\t" \
        "ds_read_b64_tr_b16 a[60:61], v7 offset:49248\n\t" \
        "ds_read_b64_tr_b16 a[62:63], v7 offset:52320\n\t" \
        "v_mfma_f32_16x16x32_bf16 v[34:37], a[28:31], v[94:97], v[34:37]\n\t" \
        "buffer_load_dwordx4 v5, s[24:27], s57 offen offset:2048  lds\n\t" \
        "v_mfma_f32_16x16x32_bf16 v[58:61], a[28:31], v[98:101], v[58:61]\n\t" \
        "ds_read_b64_tr_b16 a[64:65], v6 offset:49280\n\t" \
        "v_cvt_pk_bf16_f32 v78, v102, v103\n\t" \
        "v_mfma_f32_16x16x32_bf16 v[38:41], a[32:35], v[94:97], v[38:41]\n\t" \
        "ds_read_b64_tr_b16 a[66:67], v6 offset:52352\n\t" \
        "v_cvt_pk_bf16_f32 v79, v104, v105\n\t" \
        "v_mfma_f32_16x16x32_bf16 v[62:65], a[32:35], v[98:101], v[62:65]\n\t" \
        "ds_read_b64_tr_b16 a[68:69], v7 offset:49312\n\t" \
        "ds_read_b64_tr_b16 a[70:71], v7 offset:52384\n\t" \
        "v_mfma_f32_16x16x32_bf16 v[42:45], a[36:39], v[94:97], v[42:45]\n\t" \
        "ds_read_b64_tr_b16 a[96:97], v142 offset:0\n\t" \
        "v_cvt_pk_bf16_f32 v80, v106, v107\n\t" \
        "v_mfma_f32_16x16x32_bf16 v[66:69], a[36:39], v[98:101], v[66:69]\n\t" \
        "ds_read_b64_tr_b16 a[98:99], v143 offset:0\n\t" \
        "ds_read_b64_tr_b16 a[100:101], v146 offset:0\n\t" \
        "v_mfma_f32_16x16x32_bf16 v[46:49], a[40:43], v[94:97], v[46:49]\n\t" \
        "ds_read_b64_tr_b16 a[102:103], v147 offset:0\n\t" \
        "v_cvt_pk_bf16_f32 v81, v108, v109\n\t" \
        "v_mfma_f32_16x16x32_bf16 v[70:73], a[40:43], v[98:101], v[70:73]\n\t" \
        "ds_read_b64_tr_b16 a[104:105], v144 offset:0\n\t" \
        "ds_read_b64_tr_b16 a[106:107], v145 offset:0\n\t" \
        "s_waitcnt lgkmcnt(12)\n\t" \
        "v_mfma_f32_16x16x32_bf16 v[50:53], a[44:47], v[94:97], v[50:53]\n\t" \
        "ds_read_b64_tr_b16 a[108:109], v148 offset:0\n\t" \
        "v_cvt_pk_bf16_f32 v82, v110, v111\n\t" \
        "v_mfma_f32_16x16x32_bf16 v[74:77], a[44:47], v[98:101], v[74:77]\n\t" \
        "ds_read_b64_tr_b16 a[110:111], v149 offset:0\n\t" \
        "ds_read_b64_tr_b16 a[72:73], v6 offset:55296\n\t" \
        "v_cvt_pk_bf16_f32 v83, v112, v113\n\t" \
        "v_cvt_pk_bf16_f32 v84, v114, v115\n\t" \
        "v_mfma_f32_16x16x32_bf16 v[102:105], a[128:131], v[154:157], 0\n\t" \
        "ds_read_b64_tr_b16 a[74:75], v6 offset:58368\n\t" \
        "v_cvt_pk_bf16_f32 v85, v116, v117\n\t" \
        "v_mfma_f32_16x16x32_bf16 v[106:109], a[132:135], v[154:157], 0\n\t" \
        "ds_read_b64_tr_b16 a[76:77], v7 offset:55328\n\t" \
        "v_cvt_pk_bf16_f32 v86, v118, v119\n\t" \
        "v_mfma_f32_16x16x32_bf16 v[110:113], a[136:139], v[158:161], 0\n\t" \
        "ds_read_b64_tr_b16 a[78:79], v7 offset:58400\n\t" \
        "ds_read_b64_tr_b16 a[80:81], v6 offset:55360\n\t" \
        "v_mfma_f32_16x16x32_bf16 v[114:117], a[140:143], v[158:161], 0\n\t" \
        "ds_read_b64_tr_b16 a[82:83], v6 offset:58432\n\t" \
        "v_cvt_pk_bf16_f32 v87, v120, v121\n\t" \
        "v_mfma_f32_16x16x32_bf16 v[30:33], a[48:51], v[78:81], v[30:33]\n\t" \
        "ds_read_b64_tr_b16 a[84:85], v7 offset:55392\n\t" \
        "ds_read_b64_tr_b16 a[86:87], v7 offset:58464\n\t" \
        "v_mfma_f32_16x16x32_bf16 v[54:57], a[48:51], v[82:85], v[54:57]\n\t" \
        "ds_read_b64_tr_b16 a[88:89], v6 offset:55424\n\t" \
        "v_and_b32 v162, v152, v25\n\t" \
        "v_mfma_f32_16x16x32_bf16 v[34:37], a[52:55], v[78:81], v[34:37]\n\t" \
        "ds_read_b128 v[170:173], v9 offset:1024\n\t" \
        "ds_read_b64_tr_b16 a[90:91], v6 offset:58496\n\t" \
        "v_mfma_f32_16x16x32_bf16 v[58:61], a[52:55], v[82:85], v[58:61]\n\t" \
        "s_waitcnt vmcnt(6)\n\t" \
        "s_barrier\n\t" \
        "ds_read_b64_tr_b16 a[92:93], v7 offset:55456\n\t" \
        "v_and_b32 v163, v152, v26\n\t" \
        "v_mfma_f32_16x16x32_bf16 v[38:41], a[56:59], v[78:81], v[38:41]\n\t" \
        "ds_read_b64_tr_b16 a[94:95], v7 offset:58528\n\t" \
        "v_and_b32 v164, v152, v27\n\t" \
        "v_and_b32 v165, v152, v28\n\t" \
        "v_mfma_f32_16x16x32_bf16 v[62:65], a[56:59], v[82:85], v[62:65]\n\t" \
        "ds_read_b64_tr_b16 a[112:113], v142 offset:64\n\t" \
        "ds_read_b64_tr_b16 a[114:115], v143 offset:64\n\t" \
        "v_and_b32 v166, v153, v25\n\t" \
        "s_waitcnt lgkmcnt(13)\n\t" \
        "v_mfma_f32_16x16x32_bf16 v[42:45], a[60:63], v[78:81], v[42:45]\n\t" \
        "s_add_u32 m0, s39, 36864\n\t" \
        "s_add_u32 s57, s40, 159744\n\t" \
        "buffer_load_dwordx4 v3, s[24:27], s57 offen offset:0  lds\n\t" \
        "v_mfma_f32_16x16x32_bf16 v[66:69], a[60:63], v[82:85], v[66:69]\n\t" \
        "ds_read_b64_tr_b16 a[116:117], v146 offset:64\n\t" \
        "v_cvt_pk_bf16_f32 v88, v122, v123\n\t" \
        "v_and_b32 v167, v153, v26\n\t" \
        "v_mfma_f32_16x16x32_bf16 v[46:49], a[64:67], v[78:81], v[46:49]\n\t" \
        "ds_read_b64_tr_b16 a[118:119], v147 offset:64\n\t" \
        "ds_read_b64_tr_b16 a[120:121], v144 offset:64\n\t" \
        "v_cvt_pk_bf16_f32 v89, v124, v125\n\t" \
        "v_cvt_pk_bf16_f32 v90, v126, v127\n\t" \
        "v_mfma_f32_16x16x32_bf16 v[70:73], a[64:67], v[82:85], v[70:73]\n\t" \
        "ds_read_b64_tr_b16 a[122:123], v145 offset:64\n\t" \
        "v_and_b32 v168, v153, v27\n\t" \
        "v_mfma_f32_16x16x32_bf16 v[50:53], a[68:71], v[78:81], v[50:53]\n\t" \
        "ds_read_b64_tr_b16 a[124:125], v148 offset:64\n\t" \
        "ds_read_b64_tr_b16 a[126:127], v149 offset:64\n\t" \
        "v_cvt_pk_bf16_f32 v91, v128, v129\n\t" \
        "v_cvt_pk_bf16_f32 v92, v130, v131\n\t" \
        "v_mfma_f32_16x16x32_bf16 v[74:77], a[68:71], v[82:85], v[74:77]\n\t" \
        "ds_read_b64_tr_b16 a[0:1], v6 offset:0\n\t" \
        "v_and_b32 v169, v153, v28\n\t" \
        "v_cvt_pk_bf16_f32 v93, v132, v133\n\t" \
        "v_mfma_f32_16x16x32_bf16 v[118:121], a[96:99], v[162:165], 0\n\t" \
        "buffer_load_dwordx4 v4, s[24:27], s57 offen offset:1024  lds\n\t" \
        "v_mfma_f32_16x16x32_bf16 v[122:125], a[100:103], v[162:165], 0\n\t" \
        "ds_read_b64_tr_b16 a[2:3], v6 offset:3072\n\t" \
        "v_cvt_pk_bf16_f32 v94, v102, v103\n\t" \
        "v_mfma_f32_16x16x32_bf16 v[126:129], a[104:107], v[166:169], 0\n\t" \
        "ds_read_b64_tr_b16 a[4:5], v7 offset:32\n\t" \
        "ds_read_b64_tr_b16 a[6:7], v7 offset:3104\n\t" \
        "v_mfma_f32_16x16x32_bf16 v[130:133], a[108:111], v[166:169], 0\n\t" \
        "ds_read_b64_tr_b16 a[8:9], v6 offset:64\n\t" \
        "v_cvt_pk_bf16_f32 v95, v104, v105\n\t" \
        "v_mfma_f32_16x16x32_bf16 v[30:33], a[72:75], v[86:89], v[30:33]\n\t" \
        "ds_read_b64_tr_b16 a[10:11], v6 offset:3136\n\t" \
        "v_cvt_pk_bf16_f32 v96, v106, v107\n\t" \
        "v_mfma_f32_16x16x32_bf16 v[54:57], a[72:75], v[90:93], v[54:57]\n\t" \
        "ds_read_b64_tr_b16 a[12:13], v7 offset:96\n\t" \
        "ds_read_b64_tr_b16 a[14:15], v7 offset:3168\n\t" \
        "s_waitcnt lgkmcnt(11)\n\t" \
        "v_mfma_f32_16x16x32_bf16 v[34:37], a[76:79], v[86:89], v[34:37]\n\t" \
        "buffer_load_dwordx4 v5, s[24:27], s57 offen offset:2048  lds\n\t" \
        "v_mfma_f32_16x16x32_bf16 v[58:61], a[76:79], v[90:93], v[58:61]\n\t" \
        "ds_read_b64_tr_b16 a[16:17], v6 offset:128\n\t" \
        "v_cvt_pk_bf16_f32 v97, v108, v109\n\t" \
        "v_mfma_f32_16x16x32_bf16 v[38:41], a[80:83], v[86:89], v[38:41]\n\t" \
        "ds_read_b64_tr_b16 a[18:19], v6 offset:3200\n\t" \
        "v_cvt_pk_bf16_f32 v98, v110, v111\n\t" \
        "v_mfma_f32_16x16x32_bf16 v[62:65], a[80:83], v[90:93], v[62:65]\n\t" \
        "ds_read_b64_tr_b16 a[20:21], v7 offset:160\n\t" \
        "ds_read_b64_tr_b16 a[22:23], v7 offset:3232\n\t" \
        "v_mfma_f32_16x16x32_bf16 v[42:45], a[84:87], v[86:89], v[42:45]\n\t" \
        "ds_read_b64_tr_b16 a[128:129], v142 offset:128\n\t" \
        "v_xor_b32 v134, v170, v11\n\t" \
        "v_mfma_f32_16x16x32_bf16 v[66:69], a[84:87], v[90:93], v[66:69]\n\t" \
        "ds_read_b64_tr_b16 a[130:131], v143 offset:128\n\t" \
        "v_cvt_pk_bf16_f32 v99, v112, v113\n\t" \
        "v_mfma_f32_16x16x32_bf16 v[46:49], a[88:91], v[86:89], v[46:49]\n\t" \
        "ds_read_b64_tr_b16 a[132:133], v146 offset:128\n\t" \
        "ds_read_b64_tr_b16 a[134:135], v147 offset:128\n\t" \
        "v_mfma_f32_16x16x32_bf16 v[70:73], a[88:91], v[90:93], v[70:73]\n\t" \
        "ds_read_b64_tr_b16 a[136:137], v144 offset:128\n\t" \
        "v_cvt_pk_bf16_f32 v100, v114, v115\n\t" \
        "v_mfma_f32_16x16x32_bf16 v[50:53], a[92:95], v[86:89], v[50:53]\n\t" \
        "ds_read_b64_tr_b16 a[138:139], v145 offset:128\n\t" \
        "v_xor_b32 v135, v171, v11\n\t" \
        "v_mfma_f32_16x16x32_bf16 v[74:77], a[92:95], v[90:93], v[74:77]\n\t" \
        "ds_read_b64_tr_b16 a[140:141], v148 offset:128\n\t" \
        "ds_read_b64_tr_b16 a[142:143], v149 offset:128\n\t" \
        "v_mfma_f32_16x16x32_bf16 v[102:105], a[112:115], v[162:165], 0\n\t" \
        "ds_read_b64_tr_b16 a[24:25], v6 offset:6144\n\t" \
        "v_cvt_pk_bf16_f32 v101, v116, v117\n\t" \
        "v_mfma_f32_16x16x32_bf16 v[106:109], a[116:119], v[162:165], 0\n\t" \
        "ds_read_b64_tr_b16 a[26:27], v6 offset:9216\n\t" \
        "v_xor_b32 v136, v172, v11\n\t" \
        "v_xor_b32 v138, v170, v12\n\t" \
        "s_waitcnt lgkmcnt(12)\n\t" \
        "v_mfma_f32_16x16x32_bf16 v[110:113], a[120:123], v[166:169], 0\n\t" \
        "ds_read_b64_tr_b16 a[28:29], v7 offset:6176\n\t" \
        "ds_read_b64 v[150:151], v10 offset:1024\n\t" \
        "v_xor_b32 v137, v173, v11\n\t" \
        "v_xor_b32 v139, v171, v12\n\t" \
        "v_mfma_f32_16x16x32_bf16 v[114:117], a[124:127], v[166:169], 0\n\t" \
        "ds_read_b64_tr_b16 a[30:31], v7 offset:9248\n\t" \
        "v_xor_b32 v140, v172, v12\n\t" \
        "v_mfma_f32_16x16x32_bf16 v[30:33], a[0:3], v[94:97], v[30:33]\n\t" \
        "ds_read_b64_tr_b16 a[32:33], v6 offset:6208\n\t" \
        "ds_read_b64_tr_b16 a[34:35], v6 offset:9280\n\t" \
        "v_xor_b32 v141, v173, v12\n\t" \
        "v_mfma_f32_16x16x32_bf16 v[54:57], a[0:3], v[98:101], v[54:57]\n\t" \
        "ds_read_b64_tr_b16 a[36:37], v7 offset:6240\n\t" \
        "v_cvt_pk_bf16_f32 v78, v118, v119\n\t" \
        "v_mfma_f32_16x16x32_bf16 v[34:37], a[4:7], v[94:97], v[34:37]\n\t" \
        "ds_read_b64_tr_b16 a[38:39], v7 offset:9312\n\t" \
        "v_cvt_pk_bf16_f32 v79, v120, v121\n\t" \
        "v_mfma_f32_16x16x32_bf16 v[58:61], a[4:7], v[98:101], v[58:61]\n\t" \
        "s_waitcnt vmcnt(6)\n\t" \
        "s_barrier\n\t" \
        "ds_read_b64_tr_b16 a[40:41], v6 offset:6272\n\t" \
        "ds_read_b64_tr_b16 a[42:43], v6 offset:9344\n\t" \
        "v_mfma_f32_16x16x32_bf16 v[38:41], a[8:11], v[94:97], v[38:41]\n\t" \
        "ds_read_b64_tr_b16 a[44:45], v7 offset:6304\n\t" \
        "v_cvt_pk_bf16_f32 v80, v122, v123\n\t" \
        "v_mfma_f32_16x16x32_bf16 v[62:65], a[8:11], v[98:101], v[62:65]\n\t" \
        "ds_read_b64_tr_b16 a[46:47], v7 offset:9376\n\t" \
        "v_cvt_pk_bf16_f32 v81, v124, v125\n\t" \
        "v_mfma_f32_16x16x32_bf16 v[42:45], a[12:15], v[94:97], v[42:45]\n\t" \
        "ds_read_b64_tr_b16 a[96:97], v134 offset:0\n\t" \
        "ds_read_b64_tr_b16 a[98:99], v135 offset:0\n\t" \
        "v_mfma_f32_16x16x32_bf16 v[66:69], a[12:15], v[98:101], v[66:69]\n\t" \
        "ds_read_b64_tr_b16 a[100:101], v138 offset:0\n\t" \
        "v_cvt_pk_bf16_f32 v82, v126, v127\n\t" \
        "v_mfma_f32_16x16x32_bf16 v[46:49], a[16:19], v[94:97], v[46:49]\n\t" \
        "ds_read_b64_tr_b16 a[102:103], v139 offset:0\n\t" \
        "ds_read_b64_tr_b16 a[104:105], v136 offset:0\n\t" \
        "v_mfma_f32_16x16x32_bf16 v[70:73], a[16:19], v[98:101], v[70:73]\n\t" \
        "ds_read_b64_tr_b16 a[106:107], v137 offset:0\n\t" \
        "v_cvt_pk_bf16_f32 v83, v128, v129\n\t" \
        "s_waitcnt lgkmcnt(12)\n\t" \
        "v_mfma_f32_16x16x32_bf16 v[50:53], a[20:23], v[94:97], v[50:53]\n\t" \
        "ds_read_b64_tr_b16 a[108:109], v140 offset:0\n\t" \
        "v_cvt_pk_bf16_f32 v84, v130, v131\n\t" \
        "v_mfma_f32_16x16x32_bf16 v[74:77], a[20:23], v[98:101], v[74:77]\n\t" \
        "ds_read_b64_tr_b16 a[110:111], v141 offset:0\n\t" \
        "ds_read_b64_tr_b16 a[48:49], v6 offset:12288\n\t" \
        "v_mfma_f32_16x16x32_bf16 v[118:121], a[128:131], v[162:165], 0\n\t" \
        "ds_read_b64_tr_b16 a[50:51], v6 offset:15360\n\t" \
        "v_cvt_pk_bf16_f32 v85, v132, v133\n\t" \
        "v_mfma_f32_16x16x32_bf16 v[122:125], a[132:135], v[162:165], 0\n\t" \
        "ds_read_b64_tr_b16 a[52:53], v7 offset:12320\n\t" \
        "ds_read_b64_tr_b16 a[54:55], v7 offset:15392\n\t" \
        "v_mfma_f32_16x16x32_bf16 v[126:129], a[136:139], v[166:169], 0\n\t" \
        "ds_read_b64_tr_b16 a[56:57], v6 offset:12352\n\t" \
        "v_cvt_pk_bf16_f32 v86, v102, v103\n\t" \
        "v_mfma_f32_16x16x32_bf16 v[130:133], a[140:143], v[166:169], 0\n\t" \
        "ds_read_b64_tr_b16 a[58:59], v6 offset:15424\n\t" \
        "ds_read_b64_tr_b16 a[60:61], v7 offset:12384\n\t" \
        "v_mfma_f32_16x16x32_bf16 v[30:33], a[24:27], v[78:81], v[30:33]\n\t" \
        "ds_read_b64_tr_b16 a[62:63], v7 offset:15456\n\t" \
        "v_and_b32 v154, v150, v25\n\t" \
        "v_mfma_f32_16x16x32_bf16 v[54:57], a[24:27], v[82:85], v[54:57]\n\t" \
        "ds_read_b64_tr_b16 a[64:65], v6 offset:12416\n\t" \
        "ds_read_b64_tr_b16 a[66:67], v6 offset:15488\n\t" \
        "v_mfma_f32_16x16x32_bf16 v[34:37], a[28:31], v[78:81], v[34:37]\n\t" \
        "ds_read_b64_tr_b16 a[68:69], v7 offset:12448\n\t" \
        "v_and_b32 v155, v150, v26\n\t" \
        "v_mfma_f32_16x16x32_bf16 v[58:61], a[28:31], v[82:85], v[58:61]\n\t" \
        "ds_read_b64_tr_b16 a[70:71], v7 offset:15520\n\t" \
        "ds_read_b64_tr_b16 a[112:113], v134 offset:64\n\t" \
        "v_mfma_f32_16x16x32_bf16 v[38:41], a[32:35], v[78:81], v[38:41]\n\t" \
        "ds_read_b64_tr_b16 a[114:115], v135 offset:64\n\t" \
        "v_and_b32 v156, v150, v27\n\t" \
        "v_mfma_f32_16x16x32_bf16 v[62:65], a[32:35], v[82:85], v[62:65]\n\t" \
        "ds_read_b64_tr_b16 a[116:117], v138 offset:64\n\t" \
        "ds_read_b64_tr_b16 a[118:119], v139 offset:64\n\t" \
        "s_waitcnt lgkmcnt(14)\n\t" \
        "v_mfma_f32_16x16x32_bf16 v[42:45], a[36:39], v[78:81], v[42:45]\n\t" \
        "ds_read_b64_tr_b16 a[120:121], v136 offset:64\n\t" \
        "v_and_b32 v157, v150, v28\n\t" \
        "v_and_b32 v158, v151, v25\n\t" \
        "v_mfma_f32_16x16x32_bf16 v[66:69], a[36:39], v[82:85], v[66:69]\n\t" \
        "ds_read_b64_tr_b16 a[122:123], v137 offset:64\n\t" \
        "ds_read_b64_tr_b16 a[124:125], v140 offset:64\n\t" \
        "v_cvt_pk_bf16_f32 v87, v104, v105\n\t" \
        "v_mfma_f32_16x16x32_bf16 v[46:49], a[40:43], v[78:81], v[46:49]\n\t" \
        "ds_read_b64_tr_b16 a[126:127], v141 offset:64\n\t" \
        "v_cvt_pk_bf16_f32 v88, v106, v107\n\t" \
        "v_and_b32 v159, v151, v26\n\t" \
        "v_mfma_f32_16x16x32_bf16 v[70:73], a[40:43], v[82:85], v[70:73]\n\t" \
        "ds_read_b64_tr_b16 a[72:73], v6 offset:18432\n\t" \
        "ds_read_b64_tr_b16 a[74:75], v6 offset:21504\n\t" \
        "v_cvt_pk_bf16_f32 v89, v108, v109\n\t" \
        "v_cvt_pk_bf16_f32 v90, v110, v111\n\t" \
        "v_mfma_f32_16x16x32_bf16 v[50:53], a[44:47], v[78:81], v[50:53]\n\t" \
        "ds_read_b64_tr_b16 a[76:77], v7 offset:18464\n\t" \
        "v_and_b32 v160, v151, v27\n\t" \
        "v_mfma_f32_16x16x32_bf16 v[74:77], a[44:47], v[82:85], v[74:77]\n\t" \
        "ds_read_b64_tr_b16 a[78:79], v7 offset:21536\n\t" \
        "ds_read_b64_tr_b16 a[80:81], v6 offset:18496\n\t" \
        "v_cvt_pk_bf16_f32 v91, v112, v113\n\t" \
        "v_cvt_pk_bf16_f32 v92, v114, v115\n\t" \
        "v_mfma_f32_16x16x32_bf16 v[102:105], a[96:99], v[154:157], 0\n\t" \
        "ds_read_b64_tr_b16 a[82:83], v6 offset:21568\n\t" \
        "v_and_b32 v161, v151, v28\n\t" \
        "v_cvt_pk_bf16_f32 v93, v116, v117\n\t" \
        "v_mfma_f32_16x16x32_bf16 v[106:109], a[100:103], v[154:157], 0\n\t" \
        "ds_read_b64_tr_b16 a[84:85], v7 offset:18528\n\t" \
        "ds_read_b64_tr_b16 a[86:87], v7 offset:21600\n\t" \
        "v_mfma_f32_16x16x32_bf16 v[110:113], a[104:107], v[158:161], 0\n\t" \
        "ds_read_b64_tr_b16 a[88:89], v6 offset:18560\n\t" \
        "v_cvt_pk_bf16_f32 v94, v118, v119\n\t" \
        "v_mfma_f32_16x16x32_bf16 v[114:117], a[108:111], v[158:161], 0\n\t" \
        "ds_read_b64_tr_b16 a[90:91], v6 offset:21632\n\t" \
        "ds_read_b64_tr_b16 a[92:93], v7 offset:18592\n\t" \
        "v_mfma_f32_16x16x32_bf16 v[30:33], a[48:51], v[86:89], v[30:33]\n\t" \
        "ds_read_b64_tr_b16 a[94:95], v7 offset:21664\n\t" \
        "v_cvt_pk_bf16_f32 v95, v120, v121\n\t" \
        "v_mfma_f32_16x16x32_bf16 v[54:57], a[48:51], v[90:93], v[54:57]\n\t" \
        "ds_read_b64_tr_b16 a[128:129], v134 offset:128\n\t" \
        "v_cvt_pk_bf16_f32 v96, v122, v123\n\t" \
        "s_waitcnt lgkmcnt(13)\n\t" \
        "v_mfma_f32_16x16x32_bf16 v[34:37], a[52:55], v[86:89], v[34:37]\n\t" \
        "ds_read_b64_tr_b16 a[130:131], v135 offset:128\n\t" \
        "ds_read_b64_tr_b16 a[132:133], v138 offset:128\n\t" \
        "v_mfma_f32_16x16x32_bf16 v[58:61], a[52:55], v[90:93], v[58:61]\n\t" \
        "s_waitcnt vmcnt(3)\n\t" \
        "s_barrier\n\t" \
        "ds_read_b64_tr_b16 a[134:135], v139 offset:128\n\t" \
        "v_cvt_pk_bf16_f32 v97, v124, v125\n\t" \
        "v_mfma_f32_16x16x32_bf16 v[38:41], a[56:59], v[86:89], v[38:41]\n\t" \
        "ds_read_b64_tr_b16 a[136:137], v136 offset:128\n\t" \
        "ds_read_b64_tr_b16 a[138:139], v137 offset:128\n\t" \
        "v_mfma_f32_16x16x32_bf16 v[62:65], a[56:59], v[90:93], v[62:65]\n\t" \
        "ds_read_b64_tr_b16 a[140:141], v140 offset:128\n\t" \
        "v_cvt_pk_bf16_f32 v98, v126, v127\n\t" \
        "v_mfma_f32_16x16x32_bf16 v[42:45], a[60:63], v[86:89], v[42:45]\n\t" \
        "ds_read_b64_tr_b16 a[142:143], v141 offset:128\n\t" \
        "ds_read_b64_tr_b16 a[0:1], v6 offset:24576\n\t" \
        "v_mfma_f32_16x16x32_bf16 v[66:69], a[60:63], v[90:93], v[66:69]\n\t" \
        "ds_read_b64_tr_b16 a[2:3], v6 offset:27648\n\t" \
        "v_cvt_pk_bf16_f32 v99, v128, v129\n\t" \
        "v_mfma_f32_16x16x32_bf16 v[46:49], a[64:67], v[86:89], v[46:49]\n\t" \
        "ds_read_b64_tr_b16 a[4:5], v7 offset:24608\n\t" \
        "ds_read_b64_tr_b16 a[6:7], v7 offset:27680\n\t" \
        "v_mfma_f32_16x16x32_bf16 v[70:73], a[64:67], v[90:93], v[70:73]\n\t" \
        "ds_read_b64_tr_b16 a[8:9], v6 offset:24640\n\t" \
        "v_cvt_pk_bf16_f32 v100, v130, v131\n\t" \
        "v_mfma_f32_16x16x32_bf16 v[50:53], a[68:71], v[86:89], v[50:53]\n\t" \
        "ds_read_b64_tr_b16 a[10:11], v6 offset:27712\n\t" \
        "ds_read_b64_tr_b16 a[12:13], v7 offset:24672\n\t" \
        "v_mfma_f32_16x16x32_bf16 v[74:77], a[68:71], v[90:93], v[74:77]\n\t" \
        "ds_read_b64_tr_b16 a[14:15], v7 offset:27744\n\t" \
        "v_cvt_pk_bf16_f32 v101, v132, v133\n\t" \
        "v_mfma_f32_16x16x32_bf16 v[118:121], a[112:115], v[154:157], 0\n\t" \
        "ds_read_b64_tr_b16 a[16:17], v6 offset:24704\n\t" \
        "ds_read_b64_tr_b16 a[18:19], v6 offset:27776\n\t" \
        "v_mfma_f32_16x16x32_bf16 v[122:125], a[116:119], v[154:157], 0\n\t" \
        "ds_read_b64_tr_b16 a[20:21], v7 offset:24736\n\t" \
        "v_cvt_pk_bf16_f32 v78, v102, v103\n\t" \
        "v_mfma_f32_16x16x32_bf16 v[126:129], a[120:123], v[158:161], 0\n\t" \
        "ds_read_b64_tr_b16 a[22:23], v7 offset:27808\n\t" \
        "ds_read_b64_tr_b16 a[24:25], v6 offset:30720\n\t" \
        "v_mfma_f32_16x16x32_bf16 v[130:133], a[124:127], v[158:161], 0\n\t" \
        "s_waitcnt vmcnt(0) lgkmcnt(15)\n\t" \
        "s_barrier\n\t" \
        "ds_read_b64_tr_b16 a[26:27], v6 offset:33792\n\t" \
        "v_cvt_pk_bf16_f32 v79, v104, v105\n\t" \
        "v_mfma_f32_16x16x32_bf16 v[30:33], a[72:75], v[94:97], v[30:33]\n\t" \
        "ds_read_b64_tr_b16 a[28:29], v7 offset:30752\n\t" \
        "ds_read_b64_tr_b16 a[30:31], v7 offset:33824\n\t" \
        "v_mfma_f32_16x16x32_bf16 v[54:57], a[72:75], v[98:101], v[54:57]\n\t" \
        "ds_read_b64_tr_b16 a[32:33], v6 offset:30784\n\t" \
        "v_cvt_pk_bf16_f32 v80, v106, v107\n\t" \
        "v_mfma_f32_16x16x32_bf16 v[34:37], a[76:79], v[94:97], v[34:37]\n\t" \
        "ds_read_b64_tr_b16 a[34:35], v6 offset:33856\n\t" \
        "ds_read_b64_tr_b16 a[36:37], v7 offset:30816\n\t" \
        "v_mfma_f32_16x16x32_bf16 v[58:61], a[76:79], v[98:101], v[58:61]\n\t" \
        "ds_read_b64_tr_b16 a[38:39], v7 offset:33888\n\t" \
        "v_cvt_pk_bf16_f32 v81, v108, v109\n\t" \
        "v_mfma_f32_16x16x32_bf16 v[38:41], a[80:83], v[94:97], v[38:41]\n\t" \
        "ds_read_b64_tr_b16 a[40:41], v6 offset:30848\n\t" \
        "ds_read_b64_tr_b16 a[42:43], v6 offset:33920\n\t" \
        "v_mfma_f32_16x16x32_bf16 v[62:65], a[80:83], v[98:101], v[62:65]\n\t" \
        "ds_read_b64_tr_b16 a[44:45], v7 offset:30880\n\t" \
        "v_cvt_pk_bf16_f32 v82, v110, v111\n\t" \
        "v_mfma_f32_16x16x32_bf16 v[42:45], a[84:87], v[94:97], v[42:45]\n\t" \
        "ds_read_b64_tr_b16 a[46:47], v7 offset:33952\n\t" \
        "ds_read_b64_tr_b16 a[48:49], v6 offset:36864\n\t" \
        "v_mfma_f32_16x16x32_bf16 v[66:69], a[84:87], v[98:101], v[66:69]\n\t" \
        "ds_read_b64_tr_b16 a[50:51], v6 offset:39936\n\t" \
        "v_cvt_pk_bf16_f32 v83, v112, v113\n\t" \
        "v_mfma_f32_16x16x32_bf16 v[46:49], a[88:91], v[94:97], v[46:49]\n\t" \
        "ds_read_b64_tr_b16 a[52:53], v7 offset:36896\n\t" \
        "ds_read_b64_tr_b16 a[54:55], v7 offset:39968\n\t" \
        "v_mfma_f32_16x16x32_bf16 v[70:73], a[88:91], v[98:101], v[70:73]\n\t" \
        "ds_read_b64_tr_b16 a[56:57], v6 offset:36928\n\t" \
        "v_cvt_pk_bf16_f32 v84, v114, v115\n\t" \
        "v_mfma_f32_16x16x32_bf16 v[50:53], a[92:95], v[94:97], v[50:53]\n\t" \
        "ds_read_b64_tr_b16 a[58:59], v6 offset:40000\n\t" \
        "v_cvt_pk_bf16_f32 v85, v116, v117\n\t" \
        "v_mfma_f32_16x16x32_bf16 v[74:77], a[92:95], v[98:101], v[74:77]\n\t" \
        "ds_read_b64_tr_b16 a[60:61], v7 offset:36960\n\t" \
        "ds_read_b64_tr_b16 a[62:63], v7 offset:40032\n\t" \
        "v_mfma_f32_16x16x32_bf16 v[102:105], a[128:131], v[154:157], 0\n\t" \
        "ds_read_b64_tr_b16 a[64:65], v6 offset:36992\n\t" \
        "ds_read_b64_tr_b16 a[66:67], v6 offset:40064\n\t" \
        "v_mfma_f32_16x16x32_bf16 v[106:109], a[132:135], v[154:157], 0\n\t" \
        "ds_read_b64_tr_b16 a[68:69], v7 offset:37024\n\t" \
        "v_cvt_pk_bf16_f32 v86, v118, v119\n\t" \
        "v_mfma_f32_16x16x32_bf16 v[110:113], a[136:139], v[158:161], 0\n\t" \
        "ds_read_b64_tr_b16 a[70:71], v7 offset:40096\n\t" \
        "v_cvt_pk_bf16_f32 v88, v122, v123\n\t" \
        "s_waitcnt lgkmcnt(13)\n\t" \
        "v_mfma_f32_16x16x32_bf16 v[114:117], a[140:143], v[158:161], 0\n\t" \
        "v_cvt_pk_bf16_f32 v87, v120, v121\n\t" \
        "v_cvt_pk_bf16_f32 v89, v124, v125\n\t" \
        "v_mfma_f32_16x16x32_bf16 v[30:33], a[0:3], v[78:81], v[30:33]\n\t" \
        "v_cvt_pk_bf16_f32 v94, v102, v103\n\t" \
        "v_cvt_pk_bf16_f32 v95, v104, v105\n\t" \
        "v_mfma_f32_16x16x32_bf16 v[34:37], a[4:7], v[78:81], v[34:37]\n\t" \
        "v_cvt_pk_bf16_f32 v96, v106, v107\n\t" \
        "v_cvt_pk_bf16_f32 v97, v108, v109\n\t" \
        "v_mfma_f32_16x16x32_bf16 v[38:41], a[8:11], v[78:81], v[38:41]\n\t" \
        "v_cvt_pk_bf16_f32 v90, v126, v127\n\t" \
        "v_cvt_pk_bf16_f32 v92, v130, v131\n\t" \
        "v_mfma_f32_16x16x32_bf16 v[42:45], a[12:15], v[78:81], v[42:45]\n\t" \
        "v_cvt_pk_bf16_f32 v91, v128, v129\n\t" \
        "v_cvt_pk_bf16_f32 v93, v132, v133\n\t" \
        "v_mfma_f32_16x16x32_bf16 v[46:49], a[16:19], v[78:81], v[46:49]\n\t" \
        "v_cvt_pk_bf16_f32 v98, v110, v111\n\t" \
        "v_cvt_pk_bf16_f32 v100, v114, v115\n\t" \
        "v_mfma_f32_16x16x32_bf16 v[50:53], a[20:23], v[78:81], v[50:53]\n\t" \
        "v_cvt_pk_bf16_f32 v99, v112, v113\n\t" \
        "v_cvt_pk_bf16_f32 v101, v116, v117\n\t" \
        "v_mfma_f32_16x16x32_bf16 v[30:33], a[24:27], v[86:89], v[30:33]\n\t" \
        "v_mfma_f32_16x16x32_bf16 v[34:37], a[28:31], v[86:89], v[34:37]\n\t" \
        "v_mfma_f32_16x16x32_bf16 v[38:41], a[32:35], v[86:89], v[38:41]\n\t" \
        "v_mfma_f32_16x16x32_bf16 v[42:45], a[36:39], v[86:89], v[42:45]\n\t" \
        "v_mfma_f32_16x16x32_bf16 v[46:49], a[40:43], v[86:89], v[46:49]\n\t" \
        "s_waitcnt lgkmcnt(0)\n\t" \
        "v_mfma_f32_16x16x32_bf16 v[50:53], a[44:47], v[86:89], v[50:53]\n\t" \
        "v_mfma_f32_16x16x32_bf16 v[30:33], a[48:51], v[94:97], v[30:33]\n\t" \
        "v_mfma_f32_16x16x32_bf16 v[34:37], a[52:55], v[94:97], v[34:37]\n\t" \
        "v_mfma_f32_16x16x32_bf16 v[38:41], a[56:59], v[94:97], v[38:41]\n\t" \
        "v_mfma_f32_16x16x32_bf16 v[42:45], a[60:63], v[94:97], v[42:45]\n\t" \
        "v_mfma_f32_16x16x32_bf16 v[46:49], a[64:67], v[94:97], v[46:49]\n\t" \
        "v_mfma_f32_16x16x32_bf16 v[50:53], a[68:71], v[94:97], v[50:53]\n\t" \
        "s_nop 4\n\t" \
        "v_cvt_pk_bf16_f32 v30, v30, v31\n\t" \
        "v_mfma_f32_16x16x32_bf16 v[54:57], a[0:3], v[82:85], v[54:57]\n\t" \
        "v_cvt_pk_bf16_f32 v31, v32, v33\n\t" \
        "v_cvt_pk_bf16_f32 v32, v34, v35\n\t" \
        "v_mfma_f32_16x16x32_bf16 v[58:61], a[4:7], v[82:85], v[58:61]\n\t" \
        "v_cvt_pk_bf16_f32 v33, v36, v37\n\t" \
        "v_mfma_f32_16x16x32_bf16 v[62:65], a[8:11], v[82:85], v[62:65]\n\t" \
        "v_permlane16_swap_b32 v30, v32\n\t" \
        "v_mfma_f32_16x16x32_bf16 v[66:69], a[12:15], v[82:85], v[66:69]\n\t" \
        "v_permlane16_swap_b32 v31, v33\n\t" \
        "v_mfma_f32_16x16x32_bf16 v[70:73], a[16:19], v[82:85], v[70:73]\n\t" \
        "v_mfma_f32_16x16x32_bf16 v[74:77], a[20:23], v[82:85], v[74:77]\n\t" \
        "buffer_store_dwordx4 v[30:33], v8, s[28:31], s59 offen offset:0 sc1\n\t" \
        "v_mfma_f32_16x16x32_bf16 v[54:57], a[24:27], v[90:93], v[54:57]\n\t" \
        "v_cvt_pk_bf16_f32 v38, v38, v39\n\t" \
        "v_mfma_f32_16x16x32_bf16 v[58:61], a[28:31], v[90:93], v[58:61]\n\t" \
        "v_cvt_pk_bf16_f32 v39, v40, v41\n\t" \
        "v_cvt_pk_bf16_f32 v40, v42, v43\n\t" \
        "v_mfma_f32_16x16x32_bf16 v[62:65], a[32:35], v[90:93], v[62:65]\n\t" \
        "v_cvt_pk_bf16_f32 v41, v44, v45\n\t" \
        "v_mfma_f32_16x16x32_bf16 v[66:69], a[36:39], v[90:93], v[66:69]\n\t" \
        "v_permlane16_swap_b32 v38, v40\n\t" \
        "v_mfma_f32_16x16x32_bf16 v[70:73], a[40:43], v[90:93], v[70:73]\n\t" \
        "v_permlane16_swap_b32 v39, v41\n\t" \
        "v_mfma_f32_16x16x32_bf16 v[74:77], a[44:47], v[90:93], v[74:77]\n\t" \
        "v_mfma_f32_16x16x32_bf16 v[54:57], a[48:51], v[98:101], v[54:57]\n\t" \
        "buffer_store_dwordx4 v[38:41], v8, s[28:31], s59 offen offset:64 sc1\n\t" \
        "v_mfma_f32_16x16x32_bf16 v[58:61], a[52:55], v[98:101], v[58:61]\n\t" \
        "v_cvt_pk_bf16_f32 v46, v46, v47\n\t" \
        "v_mfma_f32_16x16x32_bf16 v[62:65], a[56:59], v[98:101], v[62:65]\n\t" \
        "v_cvt_pk_bf16_f32 v47, v48, v49\n\t" \
        "v_cvt_pk_bf16_f32 v48, v50, v51\n\t" \
        "v_mfma_f32_16x16x32_bf16 v[66:69], a[60:63], v[98:101], v[66:69]\n\t" \
        "v_cvt_pk_bf16_f32 v49, v52, v53\n\t" \
        "v_mfma_f32_16x16x32_bf16 v[70:73], a[64:67], v[98:101], v[70:73]\n\t" \
        "v_permlane16_swap_b32 v46, v48\n\t" \
        "v_mfma_f32_16x16x32_bf16 v[74:77], a[68:71], v[98:101], v[74:77]\n\t" \
        "v_permlane16_swap_b32 v47, v49\n\t" \
        "buffer_store_dwordx4 v[46:49], v8, s[28:31], s59 offen offset:128 sc1\n\t" \
        "v_cvt_pk_bf16_f32 v54, v54, v55\n\t" \
        "v_cvt_pk_bf16_f32 v55, v56, v57\n\t" \
        "v_cvt_pk_bf16_f32 v56, v58, v59\n\t" \
        "v_cvt_pk_bf16_f32 v57, v60, v61\n\t" \
        "s_nop 0\n\t" \
        "v_permlane16_swap_b32 v54, v56\n\t" \
        "v_permlane16_swap_b32 v55, v57\n\t" \
        "buffer_store_dwordx4 v[54:57], v8, s[28:31], s60 offen offset:0 sc1\n\t" \
        "v_cvt_pk_bf16_f32 v62, v62, v63\n\t" \
        "v_cvt_pk_bf16_f32 v63, v64, v65\n\t" \
        "v_cvt_pk_bf16_f32 v64, v66, v67\n\t" \
        "v_cvt_pk_bf16_f32 v65, v68, v69\n\t" \
        "s_nop 0\n\t" \
        "v_permlane16_swap_b32 v62, v64\n\t" \
        "v_permlane16_swap_b32 v63, v65\n\t" \
        "buffer_store_dwordx4 v[62:65], v8, s[28:31], s60 offen offset:64 sc1\n\t" \
        "v_cvt_pk_bf16_f32 v70, v70, v71\n\t" \
        "v_cvt_pk_bf16_f32 v71, v72, v73\n\t" \
        "v_cvt_pk_bf16_f32 v72, v74, v75\n\t" \
        "v_cvt_pk_bf16_f32 v73, v76, v77\n\t" \
        "s_nop 0\n\t" \
        "v_permlane16_swap_b32 v70, v72\n\t" \
        "v_permlane16_swap_b32 v71, v73\n\t" \
        "buffer_store_dwordx4 v[70:73], v8, s[28:31], s60 offen offset:128 sc1\n\t" \
        "s_endpgm\n\t" \
        ".LH4_start%=:\n\t" \
        "s_mov_b32 s62, 0\n\t" \
        "s_mov_b32 s63, -1\n\t" \
        "v_cmp_lt_u32 vcc, 47, v2\n\t" \
        "v_lshlrev_b32 v162, 4, v2\n\t" \
        "v_xor_b32 v163, 32, v162\n\t" \
        "v_cndmask_b32 v3, v162, v163, vcc\n\t" \
        "v_cndmask_b32 v4, v163, v162, s[62:63]\n\t" \
        "v_cmp_lt_u32 vcc, 15, v2\n\t" \
        "s_nop 1\n\t" \
        "v_cndmask_b32 v5, v162, v163, vcc\n\t" \
        "s_mul_i32 s40, s35, 0xc00\n\t" \
        "s_add_u32 s39, s40, 0xba40\n\t" \
        "s_mov_b32 s42, 0xffff0000\n\t" \
        "s_mov_b32 s43, 0xc0\n\t" \
        "s_mov_b32 s44, 0xf80\n\t" \
        "s_mov_b32 s45, 0x435f435f\n\t" \
        "v_add_u32 v162, s32, v18\n\t" \
        "v_add_u32 v163, s33, v19\n\t" \
        "v_add_u32 v162, -1, v162\n\t" \
        "v_add_u32 v163, -1, v163\n\t" \
        "v_cndmask_b32 v161, 0, 1.0, s[62:63]\n\t" \
        "v_cvt_f32_i32 v55, v162\n\t" \
        "v_cvt_f32_i32 v62, v163\n\t" \
        "v_add_f32 v56, v55, v161\n\t" \
        "v_add_f32 v59, v62, v161\n\t" \
        "v_add_f32 v57, 1.0, v55\n\t" \
        "v_add_f32 v62, 2.0, v62\n\t" \
        "v_add_f32 v58, 2.0, v55\n\t" \
        "v_add_f32 v61, 1.0, v59\n\t" \
        "v_fma_f32 v60, v161, -2.0, v62\n\t" \
        "v_bfe_u32 v160, v2, 4, 1\n\t" \
        "v_lshrrev_b32 v162, 2, v19\n\t" \
        "v_and_b32 v163, 1, v19\n\t" \
        "v_lshlrev_b32 v162, 5, v162\n\t" \
        "v_lshl_add_u32 v162, v163, 4, v162\n\t" \
        "v_and_b32 v163, 2, v19\n\t" \
        "v_lshl_add_u32 v162, v160, 3, v162\n\t" \
        "v_lshlrev_b32 v161, 2, v2\n\t" \
        "v_lshl_add_u32 v162, v163, 1, v162\n\t" \
        "v_and_b32 v161, 0x80, v161\n\t" \
        "s_mul_i32 s55, s35, 2560\n\t" \
        "s_add_u32 s55, s55, 109120\n\t" \
        "v_add3_u32 v63, v162, v161, s55\n\t" \
        "s_mul_i32 s56, s35, 1280\n\t" \
        "s_add_u32 s56, s56, 119360\n\t" \
        "v_lshl_add_u32 v10, v19, 3, s56\n\t" \
        "v_lshl_add_u32 v162, v160, 2, v161\n\t" \
        "v_add_u32 v64, v10, v162\n\t" \
        "s_mul_i32 s55, s32, 0xf80\n\t" \
        "s_mul_i32 s56, s33, 0xc0\n\t" \
        "s_add_u32 s55, s55, s56\n\t" \
        "s_sub_u32 s55, 0x1f40, s55\n\t" \
        "v_mov_b32 v65, s55\n\t" \
        "s_mov_b32 s64, 0xffff0000\n\t" \
        "s_mov_b32 s65, s64\n\t" \
        "s_sub_i32 s55, s32, 2\n\t" \
        "s_sub_i32 s56, s33, 2\n\t" \
        "s_max_i32 s55, s55, 0\n\t" \
        "s_max_i32 s56, s56, 0\n\t" \
        "v_cvt_f32_u32 v162, s55\n\t" \
        "v_cvt_f32_u32 v163, s56\n\t" \
        "s_add_u32 s55, s32, 5\n\t" \
        "s_add_u32 s56, s33, 17\n\t" \
        "v_cvt_pk_bf16_f32 v14, v162, v163\n\t" \
        "v_cvt_f32_u32 v162, s55\n\t" \
        "v_cvt_f32_u32 v163, s56\n\t" \
        "v_cvt_pk_bf16_f32 v15, v162, v163\n\t" \
        "s_waitcnt vmcnt(2)\n\t" \
        "s_mov_b32 s57, s36\n\t" \
        "s_add_u32 m0, s38, 0x1f00\n\t" \
        "s_nop 0\n\t" \
        "buffer_load_dwordx4 v13, s[16:19], s57 offen nt lds\n\t" \
        "v_cndmask_b32 v140, v40, v41, s[62:63]\n\t" \
        "v_lshlrev_b32 v141, 16, v140\n\t" \
        "v_and_b32 v142, s42, v140\n\t" \
        "v_add_f32 v141, v55, v141\n\t" \
        "v_add_f32 v142, v59, v142\n\t" \
        "v_cvt_pk_bf16_f32 v50, v141, v142\n\t" \
        "v_pk_max_i16 v50, v50, 0\n\t" \
        "v_pk_min_i16 v50, v50, s45\n\t" \
        "v_lshlrev_b32 v141, 16, v50\n\t" \
        "v_and_b32 v142, s42, v50\n\t" \
        "v_pk_min_i16 v66, v50, v50\n\t" \
        "v_fract_f32 v143, v141\n\t" \
        "v_fract_f32 v144, v142\n\t" \
        "v_cvt_u32_f32 v145, v141\n\t" \
        "v_cvt_u32_f32 v146, v142\n\t" \
        "s_add_u32 s57, s36, 0xa800\n\t" \
        "s_add_u32 m0, s38, 0x2e80\n\t" \
        "s_nop 0\n\t" \
        "buffer_load_dwordx4 v13, s[16:19], s57 offen nt lds\n\t" \
        "v_cmp_neq_f32 s[10:11], 0, v143\n\t" \
        "v_cmp_neq_f32 vcc, 0, v144\n\t" \
        "v_add_u32 v146, 2, v146\n\t" \
        "v_pk_max_i16 v67, v50, v50\n\t" \
        "s_and_b64 s[10:11], s[10:11], s[64:65]\n\t" \
        "v_mad_u32_u24 v148, v146, s43, v65\n\t" \
        "v_bfe_u32 v147, v146, 2, 1\n\t" \
        "v_sub_f32 v141, 1.0, v143\n\t" \
        "v_addc_co_u32 v145, s[4:5], 0, v145, s[10:11]\n\t" \
        "v_sub_f32 v142, 1.0, v144\n\t" \
        "v_add_u32 v146, 1, v146\n\t" \
        "v_mad_u32_u24 v148, v145, s44, v148\n\t" \
        "v_cvt_pk_bf16_f32 v152, v141, v142\n\t" \
        "v_lshl_add_u32 v149, v147, 5, v148\n\t" \
        "v_bfe_u32 v147, v146, 2, 1\n\t" \
        "s_add_u32 s57, s36, 0x15000\n\t" \
        "s_add_u32 m0, s38, 0x3e00\n\t" \
        "s_nop 0\n\t" \
        "buffer_load_dwordx4 v13, s[16:19], s57 offen nt lds\n\t" \
        "v_add_u32 v148, s43, v148\n\t" \
        "v_lshlrev_b32 v141, 16, v152\n\t" \
        "v_lshl_add_u32 v148, v147, 5, v148\n\t" \
        "v_and_b32 v152, s42, v152\n\t" \
        "v_cndmask_b32 v141, v141, v143, s[64:65]\n\t" \
        "v_cndmask_b32 v150, v149, v148, vcc\n\t" \
        "v_mul_f32 v152, v141, v152\n\t" \
        "v_mul_f32 v144, v141, v144\n\t" \
        "ds_write2st64_b32 v63, v149, v150 offset0:0 offset1:5\n\t" \
        "v_cvt_pk_bf16_f32 v151, v152, v144\n\t" \
        "ds_write_b32 v64, v151 offset:0\n\t" \
        "v_cndmask_b32 v140, v42, v43, s[62:63]\n\t" \
        "v_lshlrev_b32 v141, 16, v140\n\t" \
        "v_and_b32 v142, s42, v140\n\t" \
        "v_add_f32 v141, v56, v141\n\t" \
        "s_add_u32 s57, s36, 0x1f800\n\t" \
        "s_add_u32 m0, s38, 0x4d80\n\t" \
        "s_nop 0\n\t" \
        "buffer_load_dwordx4 v13, s[16:19], s57 offen nt lds\n\t" \
        "v_add_f32 v142, v60, v142\n\t" \
        "v_cvt_pk_bf16_f32 v51, v141, v142\n\t" \
        "v_pk_max_i16 v51, v51, 0\n\t" \
        "v_pk_min_i16 v51, v51, s45\n\t" \
        "v_lshlrev_b32 v141, 16, v51\n\t" \
        "v_and_b32 v142, s42, v51\n\t" \
        "v_pk_min_i16 v66, v66, v51\n\t" \
        "v_fract_f32 v143, v141\n\t" \
        "v_fract_f32 v144, v142\n\t" \
        "v_cvt_u32_f32 v145, v141\n\t" \
        "v_cvt_u32_f32 v146, v142\n\t" \
        "v_cmp_neq_f32 s[10:11], 0, v143\n\t" \
        "v_cmp_neq_f32 vcc, 0, v144\n\t" \
        "v_add_u32 v146, 2, v146\n\t" \
        "v_pk_max_i16 v67, v67, v51\n\t" \
        "s_add_u32 s57, s36, 0x2a000\n\t" \
        "s_min_u32 s57, s57, 0x925800\n\t" \
        "s_add_u32 m0, s38, 0x5d00\n\t" \
        "s_nop 0\n\t" \
        "buffer_load_dwordx4 v13, s[16:19], s57 offen nt lds\n\t" \
        "s_and_b64 s[10:11], s[10:11], s[64:65]\n\t" \
        "v_mad_u32_u24 v148, v146, s43, v65\n\t" \
        "v_bfe_u32 v147, v146, 2, 1\n\t" \
        "v_sub_f32 v141, 1.0, v143\n\t" \
        "v_addc_co_u32 v145, s[4:5], 0, v145, s[10:11]\n\t" \
        "v_sub_f32 v142, 1.0, v144\n\t" \
        "v_add_u32 v146, 1, v146\n\t" \
        "v_mad_u32_u24 v148, v145, s44, v148\n\t" \
        "v_cvt_pk_bf16_f32 v152, v141, v142\n\t" \
        "v_lshl_add_u32 v149, v147, 5, v148\n\t" \
        "v_bfe_u32 v147, v146, 2, 1\n\t" \
        "v_add_u32 v148, s43, v148\n\t" \
        "v_lshlrev_b32 v141, 16, v152\n\t" \
        "v_lshl_add_u32 v148, v147, 5, v148\n\t" \
        "v_and_b32 v152, s42, v152\n\t" \
        "s_add_u32 s57, s36, 0x34800\n\t" \
        "s_min_u32 s57, s57, 0x925800\n\t" \
        "s_add_u32 m0, s38, 0x6c80\n\t" \
        "s_nop 0\n\t" \
        "buffer_load_dwordx4 v13, s[16:19], s57 offen nt lds\n\t" \
        "v_cndmask_b32 v141, v141, v143, s[64:65]\n\t" \
        "v_cndmask_b32 v150, v149, v148, vcc\n\t" \
        "v_mul_f32 v152, v141, v152\n\t" \
        "v_mul_f32 v144, v141, v144\n\t" \
        "ds_write2st64_b32 v63, v149, v150 offset0:1 offset1:6\n\t" \
        "v_cvt_pk_bf16_f32 v151, v152, v144\n\t" \
        "ds_write_b32 v64, v151 offset:256\n\t" \
        "v_cndmask_b32 v140, v44, v45, s[62:63]\n\t" \
        "v_lshlrev_b32 v141, 16, v140\n\t" \
        "v_and_b32 v142, s42, v140\n\t" \
        "v_add_f32 v141, v57, v141\n\t" \
        "v_add_f32 v142, v61, v142\n\t" \
        "v_cvt_pk_bf16_f32 v52, v141, v142\n\t" \
        "v_pk_max_i16 v52, v52, 0\n\t" \
        "v_pk_min_i16 v52, v52, s45\n\t" \
        "s_mov_b32 s57, s40\n\t" \
        "s_mov_b32 m0, s39\n\t" \
        "s_nop 0\n\t" \
        "buffer_load_dwordx4 v3, s[24:27], s57 offen offset:0  lds\n\t" \
        "v_lshlrev_b32 v141, 16, v52\n\t" \
        "v_and_b32 v142, s42, v52\n\t" \
        "v_pk_min_i16 v66, v66, v52\n\t" \
        "v_fract_f32 v143, v141\n\t" \
        "v_fract_f32 v144, v142\n\t" \
        "v_cvt_u32_f32 v145, v141\n\t" \
        "v_cvt_u32_f32 v146, v142\n\t" \
        "v_cmp_neq_f32 s[10:11], 0, v143\n\t" \
        "v_cmp_neq_f32 vcc, 0, v144\n\t" \
        "v_add_u32 v146, 2, v146\n\t" \
        "v_pk_max_i16 v67, v67, v52\n\t" \
        "s_and_b64 s[10:11], s[10:11], s[64:65]\n\t" \
        "v_mad_u32_u24 v148, v146, s43, v65\n\t" \
        "v_bfe_u32 v147, v146, 2, 1\n\t" \
        "v_sub_f32 v141, 1.0, v143\n\t" \
        "buffer_load_dwordx4 v4, s[24:27], s57 offen offset:1024  lds\n\t" \
        "v_addc_co_u32 v145, s[4:5], 0, v145, s[10:11]\n\t" \
        "v_sub_f32 v142, 1.0, v144\n\t" \
        "v_add_u32 v146, 1, v146\n\t" \
        "v_mad_u32_u24 v148, v145, s44, v148\n\t" \
        "v_cvt_pk_bf16_f32 v152, v141, v142\n\t" \
        "v_lshl_add_u32 v149, v147, 5, v148\n\t" \
        "v_bfe_u32 v147, v146, 2, 1\n\t" \
        "v_add_u32 v148, s43, v148\n\t" \
        "v_lshlrev_b32 v141, 16, v152\n\t" \
        "v_lshl_add_u32 v148, v147, 5, v148\n\t" \
        "v_and_b32 v152, s42, v152\n\t" \
        "v_cndmask_b32 v141, v141, v143, s[64:65]\n\t" \
        "v_cndmask_b32 v150, v149, v148, vcc\n\t" \
        "v_mul_f32 v152, v141, v152\n\t" \
        "v_mul_f32 v144, v141, v144\n\t" \
        "buffer_load_dwordx4 v5, s[24:27], s57 offen offset:2048  lds\n\t" \
        "ds_write2st64_b32 v63, v149, v150 offset0:2 offset1:7\n\t" \
        "v_cvt_pk_bf16_f32 v151, v152, v144\n\t" \
        "ds_write_b32 v64, v151 offset:512\n\t" \
        "v_cndmask_b32 v140, v46, v47, s[62:63]\n\t" \
        "v_lshlrev_b32 v141, 16, v140\n\t" \
        "v_and_b32 v142, s42, v140\n\t" \
        "v_add_f32 v141, v58, v141\n\t" \
        "v_add_f32 v142, v59, v142\n\t" \
        "v_cvt_pk_bf16_f32 v53, v141, v142\n\t" \
        "v_pk_max_i16 v53, v53, 0\n\t" \
        "v_pk_min_i16 v53, v53, s45\n\t" \
        "v_lshlrev_b32 v141, 16, v53\n\t" \
        "v_and_b32 v142, s42, v53\n\t" \
        "v_pk_min_i16 v66, v66, v53\n\t" \
        "v_fract_f32 v143, v141\n\t" \
        "s_add_u32 s57, s40, 0x3000\n\t" \
        "s_add_u32 m0, s39, 0x3000\n\t" \
        "s_nop 0\n\t" \
        "buffer_load_dwordx4 v3, s[24:27], s57 offen offset:0  lds\n\t" \
        "v_fract_f32 v144, v142\n\t" \
        "v_cvt_u32_f32 v145, v141\n\t" \
        "v_cvt_u32_f32 v146, v142\n\t" \
        "v_cmp_neq_f32 s[10:11], 0, v143\n\t" \
        "v_cmp_neq_f32 vcc, 0, v144\n\t" \
        "v_add_u32 v146, 2, v146\n\t" \
        "v_pk_max_i16 v67, v67, v53\n\t" \
        "s_and_b64 s[10:11], s[10:11], s[64:65]\n\t" \
        "v_mad_u32_u24 v148, v146, s43, v65\n\t" \
        "v_bfe_u32 v147, v146, 2, 1\n\t" \
        "v_sub_f32 v141, 1.0, v143\n\t" \
        "v_addc_co_u32 v145, s[4:5], 0, v145, s[10:11]\n\t" \
        "v_sub_f32 v142, 1.0, v144\n\t" \
        "v_add_u32 v146, 1, v146\n\t" \
        "v_mad_u32_u24 v148, v145, s44, v148\n\t" \
        "buffer_load_dwordx4 v4, s[24:27], s57 offen offset:1024  lds\n\t" \
        "v_cvt_pk_bf16_f32 v152, v141, v142\n\t" \
        "v_lshl_add_u32 v149, v147, 5, v148\n\t" \
        "v_bfe_u32 v147, v146, 2, 1\n\t" \
        "v_add_u32 v148, s43, v148\n\t" \
        "v_lshlrev_b32 v141, 16, v152\n\t" \
        "v_lshl_add_u32 v148, v147, 5, v148\n\t" \
        "v_and_b32 v152, s42, v152\n\t" \
        "v_cndmask_b32 v141, v141, v143, s[64:65]\n\t" \
        "v_cndmask_b32 v150, v149, v148, vcc\n\t" \
        "v_mul_f32 v152, v141, v152\n\t" \
        "v_mul_f32 v144, v141, v144\n\t" \
        "ds_write2st64_b32 v63, v149, v150 offset0:3 offset1:8\n\t" \
        "v_cvt_pk_bf16_f32 v151, v152, v144\n\t" \
        "ds_write_b32 v64, v151 offset:768\n\t" \
        "v_lshlrev_b32 v141, 16, v48\n\t" \
        "buffer_load_dwordx4 v5, s[24:27], s57 offen offset:2048  lds\n\t" \
        "v_and_b32 v142, s42, v48\n\t" \
        "v_add_f32 v141, v58, v141\n\t" \
        "v_add_f32 v142, v62, v142\n\t" \
        "v_cvt_pk_bf16_f32 v54, v141, v142\n\t" \
        "v_pk_max_i16 v54, v54, 0\n\t" \
        "v_pk_min_i16 v54, v54, s45\n\t" \
        "v_lshlrev_b32 v141, 16, v54\n\t" \
        "v_and_b32 v142, s42, v54\n\t" \
        "v_pk_min_i16 v66, v66, v54\n\t" \
        "v_fract_f32 v143, v141\n\t" \
        "v_fract_f32 v144, v142\n\t" \
        "v_cvt_u32_f32 v145, v141\n\t" \
        "v_cvt_u32_f32 v146, v142\n\t" \
        "v_cmp_neq_f32 s[10:11], 0, v143\n\t" \
        "v_cmp_neq_f32 vcc, 0, v144\n\t" \
        "s_add_u32 s57, s40, 0x6000\n\t" \
        "s_add_u32 m0, s39, 0x6000\n\t" \
        "s_nop 0\n\t" \
        "buffer_load_dwordx4 v3, s[24:27], s57 offen offset:0  lds\n\t" \
        "v_add_u32 v146, 2, v146\n\t" \
        "v_pk_max_i16 v67, v67, v54\n\t" \
        "s_and_b64 s[10:11], s[10:11], s[64:65]\n\t" \
        "v_mad_u32_u24 v148, v146, s43, v65\n\t" \
        "v_bfe_u32 v147, v146, 2, 1\n\t" \
        "v_sub_f32 v141, 1.0, v143\n\t" \
        "v_addc_co_u32 v145, s[4:5], 0, v145, s[10:11]\n\t" \
        "v_sub_f32 v142, 1.0, v144\n\t" \
        "v_add_u32 v146, 1, v146\n\t" \
        "v_mad_u32_u24 v148, v145, s44, v148\n\t" \
        "v_cvt_pk_bf16_f32 v152, v141, v142\n\t" \
        "v_lshl_add_u32 v149, v147, 5, v148\n\t" \
        "v_bfe_u32 v147, v146, 2, 1\n\t" \
        "v_add_u32 v148, s43, v148\n\t" \
        "v_lshlrev_b32 v141, 16, v152\n\t" \
        "buffer_load_dwordx4 v4, s[24:27], s57 offen offset:1024  lds\n\t" \
        "v_lshl_add_u32 v148, v147, 5, v148\n\t" \
        "v_and_b32 v152, s42, v152\n\t" \
        "v_cndmask_b32 v141, v141, v143, s[64:65]\n\t" \
        "v_cndmask_b32 v150, v149, v148, vcc\n\t" \
        "v_mul_f32 v152, v141, v152\n\t" \
        "v_mul_f32 v144, v141, v144\n\t" \
        "ds_write2st64_b32 v63, v149, v150 offset0:4 offset1:9\n\t" \
        "v_cvt_pk_bf16_f32 v151, v152, v144\n\t" \
        "ds_write_b32 v64, v151 offset:1024\n\t" \
        "v_pk_max_i16 v140, v66, v14\n\t" \
        "v_pk_min_i16 v141, v67, v15\n\t" \
        "v_cmp_ne_u32 s[10:11], v140, v66\n\t" \
        "v_cmp_ne_u32 vcc, v141, v67\n\t" \
        "s_nop 0\n\t" \
        "s_or_b64 s[10:11], s[10:11], vcc\n\t" \
        "s_cselect_b32 s54, 1, 0\n\t" \
        "buffer_load_dwordx4 v5, s[24:27], s57 offen offset:2048  lds\n\t" \
        "s_lshl_b32 s55, s35, 2\n\t" \
        "s_cmp_lg_u32 s54, 0\n\t" \
        "v_mov_b32 v162, s55\n\t" \
        "s_cselect_b32 s55, 0x10000, 0\n\t" \
        "v_mov_b32 v163, s55\n\t" \
        "ds_write_b32 v162, v163\n\t" \
        "v_bfe_u32 v162, v2, 2, 1\n\t" \
        "v_lshrrev_b32 v163, 3, v2\n\t" \
        "s_mul_i32 s55, s35, 2560\n\t" \
        "s_add_u32 s55, s55, 109120\n\t" \
        "v_mul_u32_u24 v162, 0x500, v162\n\t" \
        "v_lshlrev_b32 v163, 4, v163\n\t" \
        "v_add3_u32 v9, v162, v163, s55\n\t" \
        "v_lshrrev_b32 v164, 4, v2\n\t" \
        "v_bfe_u32 v162, v2, 2, 2\n\t" \
        "v_and_b32 v163, 3, v2\n\t" \
        "v_lshl_add_u32 v162, v164, 2, v162\n\t" \
        "v_lshlrev_b32 v11, 3, v163\n\t" \
        "v_and_b32 v165, 1, v164\n\t" \
        "v_mul_u32_u24 v162, 0xc0, v162\n\t" \
        "v_xor_b32 v12, 32, v11\n\t" \
        "v_lshl_add_u32 v162, v165, 5, v162\n\t" \
        "v_lshlrev_b32 v163, 6, v165\n\t" \
        "s_mov_b32 s55, 0xba40\n\t" \
        "v_add3_u32 v6, v162, v11, s55\n\t" \
        "v_sub_u32 v7, v6, v163\n\t" \
        "v_mul_u32_u24 v162, 0xc0, v19\n\t" \
        "v_lshrrev_b32 v164, 1, v164\n\t" \
        "v_lshl_add_u32 v162, v165, 5, v162\n\t" \
        "v_lshl_add_u32 v8, v164, 4, v162\n\t" \
        "s_add_u32 s55, s32, s35\n\t" \
        "s_mul_i32 s56, s33, 0xc0\n\t" \
        "s_mul_i32 s55, s55, 0xa800\n\t" \
        "s_add_u32 s59, s55, s56\n\t" \
        "s_add_u32 s60, s59, 0x2a000\n\t" \
        "v_lshrrev_b32 v166, 4, v2\n\t" \
        "v_bfe_u32 v168, v2, 2, 2\n\t" \
        "v_and_b32 v167, 3, v2\n\t" \
        "v_cmp_eq_u32 vcc, v166, v168\n\t" \
        "v_lshlrev_b32 v167, v167, 1\n\t" \
        "s_nop 0\n\t" \
        "v_cndmask_b32 v167, 0, v167, vcc\n\t" \
        "v_bfe_i32 v25, v167, 0, 1\n\t" \
        "v_bfe_i32 v26, v167, 1, 1\n\t" \
        "v_bfe_i32 v27, v167, 2, 1\n\t" \
        "v_bfe_i32 v28, v167, 3, 1\n\t" \
        "v_mov_b32 v163, 0\n\t" \
        "s_waitcnt vmcnt(6) lgkmcnt(0)\n\t" \
        "s_barrier\n\t" \
        "ds_read_b128 v[152:155], v163\n\t" \
        "s_waitcnt lgkmcnt(0)\n\t" \
        "v_add_u32 v152, v152, v154\n\t" \
        "v_add_u32 v153, v153, v155\n\t" \
        "v_max_u32 v152, v152, v153\n\t" \
        "s_nop 0\n\t" \
        "v_readfirstlane_b32 s55, v152\n\t" \
        "s_nop 0\n\t" \
        "s_cmp_gt_u32 s55, 56\n\t" \
        "s_cbranch_scc1 .Lfail%=\n\t" \
        "ds_read_b128 v[170:173], v9 offset:0\n\t" \
        "ds_read_b64 v[150:151], v10 offset:0\n\t" \
        "s_waitcnt lgkmcnt(1)\n\t" \
        "v_xor_b32 v134, v170, v11\n\t" \
        "v_xor_b32 v135, v171, v11\n\t" \
        "v_xor_b32 v136, v172, v11\n\t" \
        "v_xor_b32 v138, v170, v12\n\t" \
        "ds_read_b128 v[174:177], v9 offset:128\n\t" \
        "v_xor_b32 v137, v173, v11\n\t" \
        "v_xor_b32 v139, v171, v12\n\t" \
        "v_xor_b32 v140, v172, v12\n\t" \
        "v_xor_b32 v141, v173, v12\n\t" \
        "ds_read_b64_tr_b16 a[0:1], v6 offset:0\n\t" \
        "ds_read_b64_tr_b16 a[2:3], v6 offset:3072\n\t" \
        "ds_read_b64_tr_b16 a[4:5], v7 offset:32\n\t" \
        "ds_read_b64_tr_b16 a[6:7], v7 offset:3104\n\t" \
        "ds_read_b64_tr_b16 a[8:9], v6 offset:64\n\t" \
        "ds_read_b64_tr_b16 a[10:11], v6 offset:3136\n\t" \
        "ds_read_b64_tr_b16 a[96:97], v134 offset:0\n\t" \
        "ds_read_b64_tr_b16 a[98:99], v135 offset:0\n\t" \
        "ds_read_b64_tr_b16 a[100:101], v138 offset:0\n\t" \
        "ds_read_b64_tr_b16 a[102:103], v139 offset:0\n\t" \
        "ds_read_b64_tr_b16 a[104:105], v136 offset:0\n\t" \
        "ds_read_b64_tr_b16 a[106:107], v137 offset:0\n\t" \
        "ds_read_b64_tr_b16 a[108:109], v140 offset:0\n\t" \
        "ds_read_b64_tr_b16 a[110:111], v141 offset:0\n\t" \
        "ds_read_b64_tr_b16 a[112:113], v134 offset:64\n\t" \
        "ds_read_b64_tr_b16 a[114:115], v135 offset:64\n\t" \
        "ds_read_b64_tr_b16 a[116:117], v138 offset:64\n\t" \
        "ds_read_b64_tr_b16 a[118:119], v139 offset:64\n\t" \
        "ds_read_b64_tr_b16 a[120:121], v136 offset:64\n\t" \
        "ds_read_b64_tr_b16 a[122:123], v137 offset:64\n\t" \
        "ds_read_b64_tr_b16 a[124:125], v140 offset:64\n\t" \
        "ds_read_b64_tr_b16 a[126:127], v141 offset:64\n\t" \
        "ds_read_b64_tr_b16 a[128:129], v134 offset:128\n\t" \
        "ds_read_b64_tr_b16 a[130:131], v135 offset:128\n\t" \
        "ds_read_b64_tr_b16 a[132:133], v138 offset:128\n\t" \
        "ds_read_b64_tr_b16 a[134:135], v139 offset:128\n\t" \
        "ds_read_b64_tr_b16 a[136:137], v136 offset:128\n\t" \
        "s_waitcnt lgkmcnt(15)\n\t" \
        "v_and_b32 v154, v150, v25\n\t" \
        "ds_read_b64_tr_b16 a[138:139], v137 offset:128\n\t" \
        "ds_read_b64_tr_b16 a[140:141], v140 offset:128\n\t" \
        "v_xor_b32 v142, v174, v11\n\t" \
        "v_and_b32 v155, v150, v26\n\t" \
        "ds_read_b64_tr_b16 a[142:143], v141 offset:128\n\t" \
        "v_xor_b32 v143, v175, v11\n\t" \
        "v_and_b32 v156, v150, v27\n\t" \
        "v_xor_b32 v144, v176, v11\n\t" \
        "v_xor_b32 v146, v174, v12\n\t" \
        "v_and_b32 v157, v150, v28\n\t" \
        "v_xor_b32 v145, v177, v11\n\t" \
        "v_xor_b32 v147, v175, v12\n\t" \
        "v_xor_b32 v148, v176, v12\n\t" \
        "ds_read_b64 v[152:153], v10 offset:128\n\t" \
        "v_xor_b32 v149, v177, v12\n\t" \
        "v_and_b32 v158, v151, v25\n\t" \
        "v_and_b32 v159, v151, v26\n\t" \
        "ds_read_b64_tr_b16 a[12:13], v7 offset:96\n\t" \
        "v_and_b32 v160, v151, v27\n\t" \
        "ds_read_b64_tr_b16 a[14:15], v7 offset:3168\n\t" \
        "v_mfma_f32_16x16x32_bf16 v[102:105], a[96:99], v[154:157], 0\n\t" \
        "ds_read_b64_tr_b16 a[16:17], v6 offset:128\n\t" \
        "ds_read_b64_tr_b16 a[18:19], v6 offset:3200\n\t" \
        "v_and_b32 v161, v151, v28\n\t" \
        "v_mfma_f32_16x16x32_bf16 v[106:109], a[100:103], v[154:157], 0\n\t" \
        "s_add_u32 m0, s39, 36864\n\t" \
        "s_add_u32 s57, s40, 36864\n\t" \
        "buffer_load_dwordx4 v3, s[24:27], s57 offen offset:0  lds\n\t" \
        "v_mfma_f32_16x16x32_bf16 v[110:113], a[104:107], v[158:161], 0\n\t" \
        "ds_read_b64_tr_b16 a[20:21], v7 offset:160\n\t" \
        "s_waitcnt lgkmcnt(5)\n\t" \
        "v_and_b32 v162, v152, v25\n\t" \
        "v_mfma_f32_16x16x32_bf16 v[114:117], a[108:111], v[158:161], 0\n\t" \
        "ds_read_b64_tr_b16 a[22:23], v7 offset:3232\n\t" \
        "ds_read_b64_tr_b16 a[96:97], v142 offset:0\n\t" \
        "v_and_b32 v163, v152, v26\n\t" \
        "v_and_b32 v164, v152, v27\n\t" \
        "v_and_b32 v165, v152, v28\n\t" \
        "v_and_b32 v166, v153, v25\n\t" \
        "v_and_b32 v167, v153, v26\n\t" \
        "v_and_b32 v168, v153, v27\n\t" \
        "v_and_b32 v169, v153, v28\n\t" \
        "v_mfma_f32_16x16x32_bf16 v[118:121], a[112:115], v[154:157], 0\n\t" \
        "ds_read_b64_tr_b16 a[98:99], v143 offset:0\n\t" \
        "ds_read_b64_tr_b16 a[100:101], v146 offset:0\n\t" \
        "v_cvt_pk_bf16_f32 v78, v102, v103\n\t" \
        "v_cvt_pk_bf16_f32 v79, v104, v105\n\t" \
        "v_mfma_f32_16x16x32_bf16 v[122:125], a[116:119], v[154:157], 0\n\t" \
        "ds_read_b64_tr_b16 a[102:103], v147 offset:0\n\t" \
        "ds_read_b64_tr_b16 a[104:105], v144 offset:0\n\t" \
        "v_cvt_pk_bf16_f32 v80, v106, v107\n\t" \
        "v_cvt_pk_bf16_f32 v81, v108, v109\n\t" \
        "v_mfma_f32_16x16x32_bf16 v[126:129], a[120:123], v[158:161], 0\n\t" \
        "buffer_load_dwordx4 v4, s[24:27], s57 offen offset:1024  lds\n\t" \
        "v_cvt_pk_bf16_f32 v82, v110, v111\n\t" \
        "v_mfma_f32_16x16x32_bf16 v[130:133], a[124:127], v[158:161], 0\n\t" \
        "ds_read_b64_tr_b16 a[106:107], v145 offset:0\n\t" \
        "ds_read_b64_tr_b16 a[108:109], v148 offset:0\n\t" \
        "v_cvt_pk_bf16_f32 v83, v112, v113\n\t" \
        "v_cvt_pk_bf16_f32 v84, v114, v115\n\t" \
        "v_mfma_f32_16x16x32_bf16 v[102:105], a[128:131], v[154:157], 0\n\t" \
        "ds_read_b64_tr_b16 a[110:111], v149 offset:0\n\t" \
        "ds_read_b64_tr_b16 a[24:25], v6 offset:6144\n\t" \
        "v_cvt_pk_bf16_f32 v85, v116, v117\n\t" \
        "v_mfma_f32_16x16x32_bf16 v[106:109], a[132:135], v[154:157], 0\n\t" \
        "ds_read_b64_tr_b16 a[26:27], v6 offset:9216\n\t" \
        "ds_read_b64_tr_b16 a[28:29], v7 offset:6176\n\t" \
        "v_mfma_f32_16x16x32_bf16 v[110:113], a[136:139], v[158:161], 0\n\t" \
        "ds_read_b64_tr_b16 a[30:31], v7 offset:9248\n\t" \
        "ds_read_b64_tr_b16 a[32:33], v6 offset:6208\n\t" \
        "v_mfma_f32_16x16x32_bf16 v[114:117], a[140:143], v[158:161], 0\n\t" \
        "buffer_load_dwordx4 v5, s[24:27], s57 offen offset:2048  lds\n\t" \
        "v_mfma_f32_16x16x32_bf16 v[30:33], a[0:3], v[78:81], 0\n\t" \
        "ds_read_b64_tr_b16 a[34:35], v6 offset:9280\n\t" \
        "v_cvt_pk_bf16_f32 v86, v118, v119\n\t" \
        "v_mfma_f32_16x16x32_bf16 v[54:57], a[0:3], v[82:85], 0\n\t" \
        "ds_read_b64_tr_b16 a[36:37], v7 offset:6240\n\t" \
        "ds_read_b64_tr_b16 a[38:39], v7 offset:9312\n\t" \
        "v_mfma_f32_16x16x32_bf16 v[34:37], a[4:7], v[78:81], 0\n\t" \
        "ds_read_b64_tr_b16 a[40:41], v6 offset:6272\n\t" \
        "v_cvt_pk_bf16_f32 v87, v120, v121\n\t" \
        "v_mfma_f32_16x16x32_bf16 v[58:61], a[4:7], v[82:85], 0\n\t" \
        "s_waitcnt vmcnt(6)\n\t" \
        "s_barrier\n\t" \
        "ds_read_b128 v[170:173], v9 offset:256\n\t" \
        "ds_read_b64_tr_b16 a[42:43], v6 offset:9344\n\t" \
        "v_mfma_f32_16x16x32_bf16 v[38:41], a[8:11], v[78:81], 0\n\t" \
        "s_add_u32 m0, s39, 49152\n\t" \
        "s_add_u32 s57, s40, 49152\n\t" \
        "buffer_load_dwordx4 v3, s[24:27], s57 offen offset:0  lds\n\t" \
        "v_mfma_f32_16x16x32_bf16 v[62:65], a[8:11], v[82:85], 0\n\t" \
        "ds_read_b64_tr_b16 a[44:45], v7 offset:6304\n\t" \
        "ds_read_b64_tr_b16 a[46:47], v7 offset:9376\n\t" \
        "s_waitcnt lgkmcnt(12)\n\t" \
        "v_mfma_f32_16x16x32_bf16 v[42:45], a[12:15], v[78:81], 0\n\t" \
        "ds_read_b64_tr_b16 a[112:113], v142 offset:64\n\t" \
        "v_cvt_pk_bf16_f32 v88, v122, v123\n\t" \
        "v_mfma_f32_16x16x32_bf16 v[66:69], a[12:15], v[82:85], 0\n\t" \
        "ds_read_b64_tr_b16 a[114:115], v143 offset:64\n\t" \
        "ds_read_b64_tr_b16 a[116:117], v146 offset:64\n\t" \
        "v_mfma_f32_16x16x32_bf16 v[46:49], a[16:19], v[78:81], 0\n\t" \
        "ds_read_b64_tr_b16 a[118:119], v147 offset:64\n\t" \
        "ds_read_b64_tr_b16 a[120:121], v144 offset:64\n\t" \
        "v_mfma_f32_16x16x32_bf16 v[70:73], a[16:19], v[82:85], 0\n\t" \
        "buffer_load_dwordx4 v4, s[24:27], s57 offen offset:1024  lds\n\t" \
        "v_mfma_f32_16x16x32_bf16 v[50:53], a[20:23], v[78:81], 0\n\t" \
        "ds_read_b64_tr_b16 a[122:123], v145 offset:64\n\t" \
        "v_cvt_pk_bf16_f32 v89, v124, v125\n\t" \
        "v_cvt_pk_bf16_f32 v90, v126, v127\n\t" \
        "v_mfma_f32_16x16x32_bf16 v[74:77], a[20:23], v[82:85], 0\n\t" \
        "ds_read_b64_tr_b16 a[124:125], v148 offset:64\n\t" \
        "ds_read_b64_tr_b16 a[126:127], v149 offset:64\n\t" \
        "v_cvt_pk_bf16_f32 v91, v128, v129\n\t" \
        "v_cvt_pk_bf16_f32 v92, v130, v131\n\t" \
        "v_mfma_f32_16x16x32_bf16 v[118:121], a[96:99], v[162:165], 0\n\t" \
        "ds_read_b64_tr_b16 a[48:49], v6 offset:12288\n\t" \
        "v_cvt_pk_bf16_f32 v93, v132, v133\n\t" \
        "v_mfma_f32_16x16x32_bf16 v[122:125], a[100:103], v[162:165], 0\n\t" \
        "ds_read_b64_tr_b16 a[50:51], v6 offset:15360\n\t" \
        "ds_read_b64_tr_b16 a[52:53], v7 offset:12320\n\t" \
        "v_mfma_f32_16x16x32_bf16 v[126:129], a[104:107], v[166:169], 0\n\t" \
        "buffer_load_dwordx4 v5, s[24:27], s57 offen offset:2048  lds\n\t" \
        "v_mfma_f32_16x16x32_bf16 v[130:133], a[108:111], v[166:169], 0\n\t" \
        "ds_read_b64_tr_b16 a[54:55], v7 offset:15392\n\t" \
        "ds_read_b64_tr_b16 a[56:57], v6 offset:12352\n\t" \
        "s_waitcnt lgkmcnt(12)\n\t" \
        "v_mfma_f32_16x16x32_bf16 v[30:33], a[24:27], v[86:89], v[30:33]\n\t" \
        "ds_read_b64_tr_b16 a[58:59], v6 offset:15424\n\t" \
        "v_cvt_pk_bf16_f32 v94, v102, v103\n\t" \
        "v_mfma_f32_16x16x32_bf16 v[54:57], a[24:27], v[90:93], v[54:57]\n\t" \
        "ds_read_b64_tr_b16 a[60:61], v7 offset:12384\n\t" \
        "v_cvt_pk_bf16_f32 v95, v104, v105\n\t" \
        "v_mfma_f32_16x16x32_bf16 v[34:37], a[28:31], v[86:89], v[34:37]\n\t" \
        "ds_read_b64_tr_b16 a[62:63], v7 offset:15456\n\t" \
        "ds_read_b64_tr_b16 a[64:65], v6 offset:12416\n\t" \
        "v_mfma_f32_16x16x32_bf16 v[58:61], a[28:31], v[90:93], v[58:61]\n\t" \
        "ds_read_b64_tr_b16 a[66:67], v6 offset:15488\n\t" \
        "v_cvt_pk_bf16_f32 v96, v106, v107\n\t" \
        "v_mfma_f32_16x16x32_bf16 v[38:41], a[32:35], v[86:89], v[38:41]\n\t" \
        "ds_read_b64_tr_b16 a[68:69], v7 offset:12448\n\t" \
        "ds_read_b64_tr_b16 a[70:71], v7 offset:15520\n\t" \
        "v_mfma_f32_16x16x32_bf16 v[62:65], a[32:35], v[90:93], v[62:65]\n\t" \
        "ds_read_b64_tr_b16 a[128:129], v142 offset:128\n\t" \
        "v_cvt_pk_bf16_f32 v97, v108, v109\n\t" \
        "v_mfma_f32_16x16x32_bf16 v[42:45], a[36:39], v[86:89], v[42:45]\n\t" \
        "ds_read_b64_tr_b16 a[130:131], v143 offset:128\n\t" \
        "v_cvt_pk_bf16_f32 v98, v110, v111\n\t" \
        "v_mfma_f32_16x16x32_bf16 v[66:69], a[36:39], v[90:93], v[66:69]\n\t" \
        "ds_read_b64_tr_b16 a[132:133], v146 offset:128\n\t" \
        "ds_read_b64_tr_b16 a[134:135], v147 offset:128\n\t" \
        "v_mfma_f32_16x16x32_bf16 v[46:49], a[40:43], v[86:89], v[46:49]\n\t" \
        "ds_read_b64_tr_b16 a[136:137], v144 offset:128\n\t" \
        "v_xor_b32 v134, v170, v11\n\t" \
        "v_mfma_f32_16x16x32_bf16 v[70:73], a[40:43], v[90:93], v[70:73]\n\t" \
        "ds_read_b64_tr_b16 a[138:139], v145 offset:128\n\t" \
        "v_cvt_pk_bf16_f32 v99, v112, v113\n\t" \
        "v_mfma_f32_16x16x32_bf16 v[50:53], a[44:47], v[86:89], v[50:53]\n\t" \
        "ds_read_b64_tr_b16 a[140:141], v148 offset:128\n\t" \
        "ds_read_b64_tr_b16 a[142:143], v149 offset:128\n\t" \
        "v_mfma_f32_16x16x32_bf16 v[74:77], a[44:47], v[90:93], v[74:77]\n\t" \
        "ds_read_b64_tr_b16 a[72:73], v6 offset:18432\n\t" \
        "v_cvt_pk_bf16_f32 v100, v114, v115\n\t" \
        "v_xor_b32 v135, v171, v11\n\t" \
        "s_waitcnt lgkmcnt(12)\n\t" \
        "v_mfma_f32_16x16x32_bf16 v[102:105], a[112:115], v[162:165], 0\n\t" \
        "ds_read_b64_tr_b16 a[74:75], v6 offset:21504\n\t" \
        "ds_read_b64_tr_b16 a[76:77], v7 offset:18464\n\t" \
        "v_cvt_pk_bf16_f32 v101, v116, v117\n\t" \
        "v_mfma_f32_16x16x32_bf16 v[106:109], a[116:119], v[162:165], 0\n\t" \
        "ds_read_b64 v[150:151], v10 offset:256\n\t" \
        "v_xor_b32 v136, v172, v11\n\t" \
        "v_xor_b32 v138, v170, v12\n\t" \
        "v_mfma_f32_16x16x32_bf16 v[110:113], a[120:123], v[166:169], 0\n\t" \
        "ds_read_b64_tr_b16 a[78:79], v7 offset:21536\n\t" \
        "ds_read_b64_tr_b16 a[80:81], v6 offset:18496\n\t" \
        "v_xor_b32 v137, v173, v11\n\t" \
        "v_xor_b32 v139, v171, v12\n\t" \
        "v_mfma_f32_16x16x32_bf16 v[114:117], a[124:127], v[166:169], 0\n\t" \
        "ds_read_b64_tr_b16 a[82:83], v6 offset:21568\n\t" \
        "v_xor_b32 v140, v172, v12\n\t" \
        "v_mfma_f32_16x16x32_bf16 v[30:33], a[48:51], v[94:97], v[30:33]\n\t" \
        "ds_read_b64_tr_b16 a[84:85], v7 offset:18528\n\t" \
        "v_xor_b32 v141, v173, v12\n\t" \
        "v_mfma_f32_16x16x32_bf16 v[54:57], a[48:51], v[98:101], v[54:57]\n\t" \
        "ds_read_b64_tr_b16 a[86:87], v7 offset:21600\n\t" \
        "ds_read_b64_tr_b16 a[88:89], v6 offset:18560\n\t" \
        "v_mfma_f32_16x16x32_bf16 v[34:37], a[52:55], v[94:97], v[34:37]\n\t" \
        "ds_read_b64_tr_b16 a[90:91], v6 offset:21632\n\t" \
        "v_cvt_pk_bf16_f32 v78, v118, v119\n\t" \
        "v_mfma_f32_16x16x32_bf16 v[58:61], a[52:55], v[98:101], v[58:61]\n\t" \
        "s_waitcnt vmcnt(6)\n\t" \
        "s_barrier\n\t" \
        "ds_read_b64_tr_b16 a[92:93], v7 offset:18592\n\t" \
        "ds_read_b64_tr_b16 a[94:95], v7 offset:21664\n\t" \
        "v_mfma_f32_16x16x32_bf16 v[38:41], a[56:59], v[94:97], v[38:41]\n\t" \
        "ds_read_b64_tr_b16 a[96:97], v134 offset:0\n\t" \
        "v_cvt_pk_bf16_f32 v79, v120, v121\n\t" \
        "v_mfma_f32_16x16x32_bf16 v[62:65], a[56:59], v[98:101], v[62:65]\n\t" \
        "ds_read_b64_tr_b16 a[98:99], v135 offset:0\n\t" \
        "v_cvt_pk_bf16_f32 v80, v122, v123\n\t" \
        "v_mfma_f32_16x16x32_bf16 v[42:45], a[60:63], v[94:97], v[42:45]\n\t" \
        "s_add_u32 m0, s39, 0\n\t" \
        "s_add_u32 s57, s40, 61440\n\t" \
        "buffer_load_dwordx4 v3, s[24:27], s57 offen offset:0  lds\n\t" \
        "v_mfma_f32_16x16x32_bf16 v[66:69], a[60:63], v[98:101], v[66:69]\n\t" \
        "ds_read_b64_tr_b16 a[100:101], v138 offset:0\n\t" \
        "ds_read_b64_tr_b16 a[102:103], v139 offset:0\n\t" \
        "s_waitcnt lgkmcnt(11)\n\t" \
        "v_mfma_f32_16x16x32_bf16 v[46:49], a[64:67], v[94:97], v[46:49]\n\t" \
        "ds_read_b64_tr_b16 a[104:105], v136 offset:0\n\t" \
        "v_cvt_pk_bf16_f32 v81, v124, v125\n\t" \
        "v_mfma_f32_16x16x32_bf16 v[70:73], a[64:67], v[98:101], v[70:73]\n\t" \
        "ds_read_b64_tr_b16 a[106:107], v137 offset:0\n\t" \
        "ds_read_b64_tr_b16 a[108:109], v140 offset:0\n\t" \
        "v_mfma_f32_16x16x32_bf16 v[50:53], a[68:71], v[94:97], v[50:53]\n\t" \
        "ds_read_b64_tr_b16 a[110:111], v141 offset:0\n\t" \
        "v_cvt_pk_bf16_f32 v82, v126, v127\n\t" \
        "v_cvt_pk_bf16_f32 v83, v128, v129\n\t" \
        "v_mfma_f32_16x16x32_bf16 v[74:77], a[68:71], v[98:101], v[74:77]\n\t" \
        "ds_read_b64_tr_b16 a[0:1], v6 offset:24576\n\t" \
        "v_cvt_pk_bf16_f32 v84, v130, v131\n\t" \
        "v_cvt_pk_bf16_f32 v85, v132, v133\n\t" \
        "v_mfma_f32_16x16x32_bf16 v[118:121], a[128:131], v[162:165], 0\n\t" \
        "buffer_load_dwordx4 v4, s[24:27], s57 offen offset:1024  lds\n\t" \
        "v_mfma_f32_16x16x32_bf16 v[122:125], a[132:135], v[162:165], 0\n\t" \
        "ds_read_b64_tr_b16 a[2:3], v6 offset:27648\n\t" \
        "ds_read_b64_tr_b16 a[4:5], v7 offset:24608\n\t" \
        "v_mfma_f32_16x16x32_bf16 v[126:129], a[136:139], v[166:169], 0\n\t" \
        "ds_read_b64_tr_b16 a[6:7], v7 offset:27680\n\t" \
        "v_cvt_pk_bf16_f32 v86, v102, v103\n\t" \
        "v_mfma_f32_16x16x32_bf16 v[130:133], a[140:143], v[166:169], 0\n\t" \
        "ds_read_b64_tr_b16 a[8:9], v6 offset:24640\n\t" \
        "ds_read_b64_tr_b16 a[10:11], v6 offset:27712\n\t" \
        "v_mfma_f32_16x16x32_bf16 v[30:33], a[72:75], v[78:81], v[30:33]\n\t" \
        "ds_read_b64_tr_b16 a[12:13], v7 offset:24672\n\t" \
        "v_and_b32 v154, v150, v25\n\t" \
        "v_mfma_f32_16x16x32_bf16 v[54:57], a[72:75], v[82:85], v[54:57]\n\t" \
        "ds_read_b64_tr_b16 a[14:15], v7 offset:27744\n\t" \
        "v_and_b32 v155, v150, v26\n\t" \
        "v_mfma_f32_16x16x32_bf16 v[34:37], a[76:79], v[78:81], v[34:37]\n\t" \
        "buffer_load_dwordx4 v5, s[24:27], s57 offen offset:2048  lds\n\t" \
        "v_mfma_f32_16x16x32_bf16 v[58:61], a[76:79], v[82:85], v[58:61]\n\t" \
        "ds_read_b64_tr_b16 a[16:17], v6 offset:24704\n\t" \
        "ds_read_b128 v[174:177], v9 offset:384\n\t" \
        "s_waitcnt lgkmcnt(10)\n\t" \
        "v_mfma_f32_16x16x32_bf16 v[38:41], a[80:83], v[78:81], v[38:41]\n\t" \
        "ds_read_b64_tr_b16 a[18:19], v6 offset:27776\n\t" \
        "v_and_b32 v156, v150, v27\n\t" \
        "v_mfma_f32_16x16x32_bf16 v[62:65], a[80:83], v[82:85], v[62:65]\n\t" \
        "ds_read_b64_tr_b16 a[20:21], v7 offset:24736\n\t" \
        "ds_read_b64_tr_b16 a[22:23], v7 offset:27808\n\t" \
        "v_mfma_f32_16x16x32_bf16 v[42:45], a[84:87], v[78:81], v[42:45]\n\t" \
        "ds_read_b64_tr_b16 a[112:113], v134 offset:64\n\t" \
        "v_and_b32 v157, v150, v28\n\t" \
        "v_and_b32 v158, v151, v25\n\t" \
        "v_mfma_f32_16x16x32_bf16 v[66:69], a[84:87], v[82:85], v[66:69]\n\t" \
        "ds_read_b64_tr_b16 a[114:115], v135 offset:64\n\t" \
        "v_cvt_pk_bf16_f32 v87, v104, v105\n\t" \
        "v_mfma_f32_16x16x32_bf16 v[46:49], a[88:91], v[78:81], v[46:49]\n\t" \
        "ds_read_b64_tr_b16 a[116:117], v138 offset:64\n\t" \
        "ds_read_b64_tr_b16 a[118:119], v139 offset:64\n\t" \
        "v_cvt_pk_bf16_f32 v88, v106, v107\n\t" \
        "v_and_b32 v159, v151, v26\n\t" \
        "v_mfma_f32_16x16x32_bf16 v[70:73], a[88:91], v[82:85], v[70:73]\n\t" \
        "ds_read_b64_tr_b16 a[120:121], v136 offset:64\n\t" \
        "v_cvt_pk_bf16_f32 v89, v108, v109\n\t" \
        "v_cvt_pk_bf16_f32 v90, v110, v111\n\t" \
        "v_mfma_f32_16x16x32_bf16 v[50:53], a[92:95], v[78:81], v[50:53]\n\t" \
        "ds_read_b64_tr_b16 a[122:123], v137 offset:64\n\t" \
        "ds_read_b64_tr_b16 a[124:125], v140 offset:64\n\t" \
        "v_and_b32 v160, v151, v27\n\t" \
        "v_mfma_f32_16x16x32_bf16 v[74:77], a[92:95], v[82:85], v[74:77]\n\t" \
        "ds_read_b64_tr_b16 a[126:127], v141 offset:64\n\t" \
        "v_cvt_pk_bf16_f32 v91, v112, v113\n\t" \
        "v_cvt_pk_bf16_f32 v92, v114, v115\n\t" \
        "v_mfma_f32_16x16x32_bf16 v[102:105], a[96:99], v[154:157], 0\n\t" \
        "ds_read_b64_tr_b16 a[24:25], v6 offset:30720\n\t" \
        "v_and_b32 v161, v151, v28\n\t" \
        "v_cvt_pk_bf16_f32 v93, v116, v117\n\t" \
        "v_mfma_f32_16x16x32_bf16 v[106:109], a[100:103], v[154:157], 0\n\t" \
        "ds_read_b64_tr_b16 a[26:27], v6 offset:33792\n\t" \
        "ds_read_b64_tr_b16 a[28:29], v7 offset:30752\n\t" \
        "v_mfma_f32_16x16x32_bf16 v[110:113], a[104:107], v[158:161], 0\n\t" \
        "ds_read_b64_tr_b16 a[30:31], v7 offset:33824\n\t" \
        "v_cvt_pk_bf16_f32 v94, v118, v119\n\t" \
        "v_mfma_f32_16x16x32_bf16 v[114:117], a[108:111], v[158:161], 0\n\t" \
        "ds_read_b64_tr_b16 a[32:33], v6 offset:30784\n\t" \
        "ds_read_b64_tr_b16 a[34:35], v6 offset:33856\n\t" \
        "s_waitcnt lgkmcnt(13)\n\t" \
        "v_mfma_f32_16x16x32_bf16 v[30:33], a[0:3], v[86:89], v[30:33]\n\t" \
        "ds_read_b64_tr_b16 a[36:37], v7 offset:30816\n\t" \
        "v_cvt_pk_bf16_f32 v95, v120, v121\n\t" \
        "v_mfma_f32_16x16x32_bf16 v[54:57], a[0:3], v[90:93], v[54:57]\n\t" \
        "ds_read_b64_tr_b16 a[38:39], v7 offset:33888\n\t" \
        "ds_read_b64_tr_b16 a[40:41], v6 offset:30848\n\t" \
        "v_mfma_f32_16x16x32_bf16 v[34:37], a[4:7], v[86:89], v[34:37]\n\t" \
        "ds_read_b64_tr_b16 a[42:43], v6 offset:33920\n\t" \
        "v_cvt_pk_bf16_f32 v96, v122, v123\n\t" \
        "v_mfma_f32_16x16x32_bf16 v[58:61], a[4:7], v[90:93], v[58:61]\n\t" \
        "s_waitcnt vmcnt(6)\n\t" \
        "s_barrier\n\t" \
        "ds_read_b64_tr_b16 a[44:45], v7 offset:30880\n\t" \
        "v_cvt_pk_bf16_f32 v97, v124, v125\n\t" \
        "v_mfma_f32_16x16x32_bf16 v[38:41], a[8:11], v[86:89], v[38:41]\n\t" \
        "ds_read_b64_tr_b16 a[46:47], v7 offset:33952\n\t" \
        "ds_read_b64_tr_b16 a[128:129], v134 offset:128\n\t" \
        "v_mfma_f32_16x16x32_bf16 v[62:65], a[8:11], v[90:93], v[62:65]\n\t" \
        "ds_read_b64_tr_b16 a[130:131], v135 offset:128\n\t" \
        "v_cvt_pk_bf16_f32 v98, v126, v127\n\t" \
        "v_mfma_f32_16x16x32_bf16 v[42:45], a[12:15], v[86:89], v[42:45]\n\t" \
        "s_add_u32 m0, s39, 12288\n\t" \
        "s_add_u32 s57, s40, 73728\n\t" \
        "buffer_load_dwordx4 v3, s[24:27], s57 offen offset:0  lds\n\t" \
        "v_mfma_f32_16x16x32_bf16 v[66:69], a[12:15], v[90:93], v[66:69]\n\t" \
        "ds_read_b64_tr_b16 a[132:133], v138 offset:128\n\t" \
        "ds_read_b64_tr_b16 a[134:135], v139 offset:128\n\t" \
        "v_mfma_f32_16x16x32_bf16 v[46:49], a[16:19], v[86:89], v[46:49]\n\t" \
        "ds_read_b64_tr_b16 a[136:137], v136 offset:128\n\t" \
        "v_xor_b32 v142, v174, v11\n\t" \
        "v_mfma_f32_16x16x32_bf16 v[70:73], a[16:19], v[90:93], v[70:73]\n\t" \
        "ds_read_b64_tr_b16 a[138:139], v137 offset:128\n\t" \
        "ds_read_b64_tr_b16 a[140:141], v140 offset:128\n\t" \
        "v_cvt_pk_bf16_f32 v99, v128, v129\n\t" \
        "v_mfma_f32_16x16x32_bf16 v[50:53], a[20:23], v[86:89], v[50:53]\n\t" \
        "ds_read_b64_tr_b16 a[142:143], v141 offset:128\n\t" \
        "v_cvt_pk_bf16_f32 v100, v130, v131\n\t" \
        "v_xor_b32 v143, v175, v11\n\t" \
        "v_mfma_f32_16x16x32_bf16 v[74:77], a[20:23], v[90:93], v[74:77]\n\t" \
        "ds_read_b64_tr_b16 a[48:49], v6 offset:36864\n\t" \
        "v_cvt_pk_bf16_f32 v101, v132, v133\n\t" \
        "s_waitcnt lgkmcnt(11)\n\t" \
        "v_mfma_f32_16x16x32_bf16 v[118:121], a[112:115], v[154:157], 0\n\t" \
        "buffer_load_dwordx4 v4, s[24:27], s57 offen offset:1024  lds\n\t" \
        "v_mfma_f32_16x16x32_bf16 v[122:125], a[116:119], v[154:157], 0\n\t" \
        "ds_read_b64_tr_b16 a[50:51], v6 offset:39936\n\t" \
        "ds_read_b64_tr_b16 a[52:53], v7 offset:36896\n\t" \
        "v_xor_b32 v144, v176, v11\n\t" \
        "v_xor_b32 v146, v174, v12\n\t" \
        "v_mfma_f32_16x16x32_bf16 v[126:129], a[120:123], v[158:161], 0\n\t" \
        "ds_read_b64 v[152:153], v10 offset:384\n\t" \
        "v_xor_b32 v145, v177, v11\n\t" \
        "v_xor_b32 v147, v175, v12\n\t" \
        "v_mfma_f32_16x16x32_bf16 v[130:133], a[124:127], v[158:161], 0\n\t" \
        "ds_read_b64_tr_b16 a[54:55], v7 offset:39968\n\t" \
        "ds_read_b64_tr_b16 a[56:57], v6 offset:36928\n\t" \
        "v_xor_b32 v148, v176, v12\n\t" \
        "v_mfma_f32_16x16x32_bf16 v[30:33], a[24:27], v[94:97], v[30:33]\n\t" \
        "ds_read_b64_tr_b16 a[58:59], v6 offset:40000\n\t" \
        "v_xor_b32 v149, v177, v12\n\t" \
        "v_mfma_f32_16x16x32_bf16 v[54:57], a[24:27], v[98:101], v[54:57]\n\t" \
        "ds_read_b64_tr_b16 a[60:61], v7 offset:36960\n\t" \
        "ds_read_b64_tr_b16 a[62:63], v7 offset:40032\n\t" \
        "v_mfma_f32_16x16x32_bf16 v[34:37], a[28:31], v[94:97], v[34:37]\n\t" \
        "buffer_load_dwordx4 v5, s[24:27], s57 offen offset:2048  lds\n\t" \
        "v_mfma_f32_16x16x32_bf16 v[58:61], a[28:31], v[98:101], v[58:61]\n\t" \
        "ds_read_b64_tr_b16 a[64:65], v6 offset:36992\n\t" \
        "v_cvt_pk_bf16_f32 v78, v102, v103\n\t" \
        "v_mfma_f32_16x16x32_bf16 v[38:41], a[32:35], v[94:97], v[38:41]\n\t" \
        "ds_read_b64_tr_b16 a[66:67], v6 offset:40064\n\t" \
        "v_cvt_pk_bf16_f32 v79, v104, v105\n\t" \
        "v_mfma_f32_16x16x32_bf16 v[62:65], a[32:35], v[98:101], v[62:65]\n\t" \
        "ds_read_b64_tr_b16 a[68:69], v7 offset:37024\n\t" \
        "ds_read_b64_tr_b16 a[70:71], v7 offset:40096\n\t" \
        "v_mfma_f32_16x16x32_bf16 v[42:45], a[36:39], v[94:97], v[42:45]\n\t" \
        "ds_read_b64_tr_b16 a[96:97], v142 offset:0\n\t" \
        "v_cvt_pk_bf16_f32 v80, v106, v107\n\t" \
        "v_mfma_f32_16x16x32_bf16 v[66:69], a[36:39], v[98:101], v[66:69]\n\t" \
        "ds_read_b64_tr_b16 a[98:99], v143 offset:0\n\t" \
        "ds_read_b64_tr_b16 a[100:101], v146 offset:0\n\t" \
        "v_mfma_f32_16x16x32_bf16 v[46:49], a[40:43], v[94:97], v[46:49]\n\t" \
        "ds_read_b64_tr_b16 a[102:103], v147 offset:0\n\t" \
        "v_cvt_pk_bf16_f32 v81, v108, v109\n\t" \
        "v_mfma_f32_16x16x32_bf16 v[70:73], a[40:43], v[98:101], v[70:73]\n\t" \
        "ds_read_b64_tr_b16 a[104:105], v144 offset:0\n\t" \
        "ds_read_b64_tr_b16 a[106:107], v145 offset:0\n\t" \
        "s_waitcnt lgkmcnt(12)\n\t" \
        "v_mfma_f32_16x16x32_bf16 v[50:53], a[44:47], v[94:97], v[50:53]\n\t" \
        "ds_read_b64_tr_b16 a[108:109], v148 offset:0\n\t" \
        "v_cvt_pk_bf16_f32 v82, v110, v111\n\t" \
        "v_mfma_f32_16x16x32_bf16 v[74:77], a[44:47], v[98:101], v[74:77]\n\t" \
        "ds_read_b64_tr_b16 a[110:111], v149 offset:0\n\t" \
        "v_cvt_pk_bf16_f32 v83, v112, v113\n\t" \
        "v_cvt_pk_bf16_f32 v84, v114, v115\n\t" \
        "v_mfma_f32_16x16x32_bf16 v[102:105], a[128:131], v[154:157], 0\n\t" \
        "ds_read_b64_tr_b16 a[72:73], v6 offset:43008\n\t" \
        "ds_read_b64_tr_b16 a[74:75], v6 offset:46080\n\t" \
        "v_cvt_pk_bf16_f32 v85, v116, v117\n\t" \
        "v_mfma_f32_16x16x32_bf16 v[106:109], a[132:135], v[154:157], 0\n\t" \
        "ds_read_b64_tr_b16 a[76:77], v7 offset:43040\n\t" \
        "v_cvt_pk_bf16_f32 v86, v118, v119\n\t" \
        "v_mfma_f32_16x16x32_bf16 v[110:113], a[136:139], v[158:161], 0\n\t" \
        "ds_read_b64_tr_b16 a[78:79], v7 offset:46112\n\t" \
        "ds_read_b64_tr_b16 a[80:81], v6 offset:43072\n\t" \
        "v_mfma_f32_16x16x32_bf16 v[114:117], a[140:143], v[158:161], 0\n\t" \
        "ds_read_b64_tr_b16 a[82:83], v6 offset:46144\n\t" \
        "v_cvt_pk_bf16_f32 v87, v120, v121\n\t" \
        "v_mfma_f32_16x16x32_bf16 v[30:33], a[48:51], v[78:81], v[30:33]\n\t" \
        "ds_read_b64_tr_b16 a[84:85], v7 offset:43104\n\t" \
        "ds_read_b64_tr_b16 a[86:87], v7 offset:46176\n\t" \
        "v_mfma_f32_16x16x32_bf16 v[54:57], a[48:51], v[82:85], v[54:57]\n\t" \
        "ds_read_b64_tr_b16 a[88:89], v6 offset:43136\n\t" \
        "v_and_b32 v162, v152, v25\n\t" \
        "v_mfma_f32_16x16x32_bf16 v[34:37], a[52:55], v[78:81], v[34:37]\n\t" \
        "ds_read_b128 v[170:173], v9 offset:512\n\t" \
        "ds_read_b64_tr_b16 a[90:91], v6 offset:46208\n\t" \
        "v_mfma_f32_16x16x32_bf16 v[58:61], a[52:55], v[82:85], v[58:61]\n\t" \
        "s_waitcnt vmcnt(6)\n\t" \
        "s_barrier\n\t" \
        "ds_read_b64_tr_b16 a[92:93], v7 offset:43168\n\t" \
        "v_and_b32 v163, v152, v26\n\t" \
        "v_mfma_f32_16x16x32_bf16 v[38:41], a[56:59], v[78:81], v[38:41]\n\t" \
        "ds_read_b64_tr_b16 a[94:95], v7 offset:46240\n\t" \
        "v_and_b32 v164, v152, v27\n\t" \
        "v_and_b32 v165, v152, v28\n\t" \
        "v_mfma_f32_16x16x32_bf16 v[62:65], a[56:59], v[82:85], v[62:65]\n\t" \
        "ds_read_b64_tr_b16 a[112:113], v142 offset:64\n\t" \
        "ds_read_b64_tr_b16 a[114:115], v143 offset:64\n\t" \
        "v_and_b32 v166, v153, v25\n\t" \
        "s_waitcnt lgkmcnt(13)\n\t" \
        "v_mfma_f32_16x16x32_bf16 v[42:45], a[60:63], v[78:81], v[42:45]\n\t" \
        "s_add_u32 m0, s39, 24576\n\t" \
        "s_add_u32 s57, s40, 86016\n\t" \
        "buffer_load_dwordx4 v3, s[24:27], s57 offen offset:0  lds\n\t" \
        "v_mfma_f32_16x16x32_bf16 v[66:69], a[60:63], v[82:85], v[66:69]\n\t" \
        "ds_read_b64_tr_b16 a[116:117], v146 offset:64\n\t" \
        "v_cvt_pk_bf16_f32 v88, v122, v123\n\t" \
        "v_and_b32 v167, v153, v26\n\t" \
        "v_mfma_f32_16x16x32_bf16 v[46:49], a[64:67], v[78:81], v[46:49]\n\t" \
        "ds_read_b64_tr_b16 a[118:119], v147 offset:64\n\t" \
        "ds_read_b64_tr_b16 a[120:121], v144 offset:64\n\t" \
        "v_cvt_pk_bf16_f32 v89, v124, v125\n\t" \
        "v_cvt_pk_bf16_f32 v90, v126, v127\n\t" \
        "v_mfma_f32_16x16x32_bf16 v[70:73], a[64:67], v[82:85], v[70:73]\n\t" \
        "ds_read_b64_tr_b16 a[122:123], v145 offset:64\n\t" \
        "v_and_b32 v168, v153, v27\n\t" \
        "v_mfma_f32_16x16x32_bf16 v[50:53], a[68:71], v[78:81], v[50:53]\n\t" \
        "ds_read_b64_tr_b16 a[124:125], v148 offset:64\n\t" \
        "ds_read_b64_tr_b16 a[126:127], v149 offset:64\n\t" \
        "v_cvt_pk_bf16_f32 v91, v128, v129\n\t" \
        "v_cvt_pk_bf16_f32 v92, v130, v131\n\t" \
        "v_mfma_f32_16x16x32_bf16 v[74:77], a[68:71], v[82:85], v[74:77]\n\t" \
        "ds_read_b64_tr_b16 a[0:1], v6 offset:49152\n\t" \
        "v_and_b32 v169, v153, v28\n\t" \
        "v_cvt_pk_bf16_f32 v93, v132, v133\n\t" \
        "v_mfma_f32_16x16x32_bf16 v[118:121], a[96:99], v[162:165], 0\n\t" \
        "buffer_load_dwordx4 v4, s[24:27], s57 offen offset:1024  lds\n\t" \
        "v_mfma_f32_16x16x32_bf16 v[122:125], a[100:103], v[162:165], 0\n\t" \
        "ds_read_b64_tr_b16 a[2:3], v6 offset:52224\n\t" \
        "ds_read_b64_tr_b16 a[4:5], v7 offset:49184\n\t" \
        "v_mfma_f32_16x16x32_bf16 v[126:129], a[104:107], v[166:169], 0\n\t" \
        "ds_read_b64_tr_b16 a[6:7], v7 offset:52256\n\t" \
        "v_cvt_pk_bf16_f32 v94, v102, v103\n\t" \
        "v_mfma_f32_16x16x32_bf16 v[130:133], a[108:111], v[166:169], 0\n\t" \
        "ds_read_b64_tr_b16 a[8:9], v6 offset:49216\n\t" \
        "v_cvt_pk_bf16_f32 v95, v104, v105\n\t" \
        "v_mfma_f32_16x16x32_bf16 v[30:33], a[72:75], v[86:89], v[30:33]\n\t" \
        "ds_read_b64_tr_b16 a[10:11], v6 offset:52288\n\t" \
        "ds_read_b64_tr_b16 a[12:13], v7 offset:49248\n\t" \
        "v_mfma_f32_16x16x32_bf16 v[54:57], a[72:75], v[90:93], v[54:57]\n\t" \
        "ds_read_b64_tr_b16 a[14:15], v7 offset:52320\n\t" \
        "v_cvt_pk_bf16_f32 v96, v106, v107\n\t" \
        "s_waitcnt lgkmcnt(11)\n\t" \
        "v_mfma_f32_16x16x32_bf16 v[34:37], a[76:79], v[86:89], v[34:37]\n\t" \
        "buffer_load_dwordx4 v5, s[24:27], s57 offen offset:2048  lds\n\t" \
        "v_mfma_f32_16x16x32_bf16 v[58:61], a[76:79], v[90:93], v[58:61]\n\t" \
        "ds_read_b64_tr_b16 a[16:17], v6 offset:49280\n\t" \
        "ds_read_b64_tr_b16 a[18:19], v6 offset:52352\n\t" \
        "v_mfma_f32_16x16x32_bf16 v[38:41], a[80:83], v[86:89], v[38:41]\n\t" \
        "ds_read_b64_tr_b16 a[20:21], v7 offset:49312\n\t" \
        "v_cvt_pk_bf16_f32 v97, v108, v109\n\t" \
        "v_mfma_f32_16x16x32_bf16 v[62:65], a[80:83], v[90:93], v[62:65]\n\t" \
        "ds_read_b64_tr_b16 a[22:23], v7 offset:52384\n\t" \
        "v_cvt_pk_bf16_f32 v98, v110, v111\n\t" \
        "v_mfma_f32_16x16x32_bf16 v[42:45], a[84:87], v[86:89], v[42:45]\n\t" \
        "ds_read_b64_tr_b16 a[128:129], v142 offset:128\n\t" \
        "ds_read_b64_tr_b16 a[130:131], v143 offset:128\n\t" \
        "v_mfma_f32_16x16x32_bf16 v[66:69], a[84:87], v[90:93], v[66:69]\n\t" \
        "ds_read_b64_tr_b16 a[132:133], v146 offset:128\n\t" \
        "v_xor_b32 v134, v170, v11\n\t" \
        "v_mfma_f32_16x16x32_bf16 v[46:49], a[88:91], v[86:89], v[46:49]\n\t" \
        "ds_read_b64_tr_b16 a[134:135], v147 offset:128\n\t" \
        "ds_read_b64_tr_b16 a[136:137], v144 offset:128\n\t" \
        "v_mfma_f32_16x16x32_bf16 v[70:73], a[88:91], v[90:93], v[70:73]\n\t" \
        "ds_read_b64_tr_b16 a[138:139], v145 offset:128\n\t" \
        "v_cvt_pk_bf16_f32 v99, v112, v113\n\t" \
        "v_mfma_f32_16x16x32_bf16 v[50:53], a[92:95], v[86:89], v[50:53]\n\t" \
        "ds_read_b64_tr_b16 a[140:141], v148 offset:128\n\t" \
        "v_cvt_pk_bf16_f32 v100, v114, v115\n\t" \
        "v_mfma_f32_16x16x32_bf16 v[74:77], a[92:95], v[90:93], v[74:77]\n\t" \
        "ds_read_b64_tr_b16 a[142:143], v149 offset:128\n\t" \
        "ds_read_b64_tr_b16 a[24:25], v6 offset:55296\n\t" \
        "v_xor_b32 v135, v171, v11\n\t" \
        "v_mfma_f32_16x16x32_bf16 v[102:105], a[112:115], v[162:165], 0\n\t" \
        "ds_read_b64_tr_b16 a[26:27], v6 offset:58368\n\t" \
        "v_cvt_pk_bf16_f32 v101, v116, v117\n\t" \
        "v_mfma_f32_16x16x32_bf16 v[106:109], a[116:119], v[162:165], 0\n\t" \
        "ds_read_b64_tr_b16 a[28:29], v7 offset:55328\n\t" \
        "ds_read_b64 v[150:151], v10 offset:512\n\t" \
        "v_xor_b32 v136, v172, v11\n\t" \
        "v_xor_b32 v138, v170, v12\n\t" \
        "s_waitcnt lgkmcnt(13)\n\t" \
        "v_mfma_f32_16x16x32_bf16 v[110:113], a[120:123], v[166:169], 0\n\t" \
        "ds_read_b64_tr_b16 a[30:31], v7 offset:58400\n\t" \
        "v_xor_b32 v137, v173, v11\n\t" \
        "v_xor_b32 v139, v171, v12\n\t" \
        "v_mfma_f32_16x16x32_bf16 v[114:117], a[124:127], v[166:169], 0\n\t" \
        "ds_read_b64_tr_b16 a[32:33], v6 offset:55360\n\t" \
        "ds_read_b64_tr_b16 a[34:35], v6 offset:58432\n\t" \
        "v_xor_b32 v140, v172, v12\n\t" \
        "v_mfma_f32_16x16x32_bf16 v[30:33], a[0:3], v[94:97], v[30:33]\n\t" \
        "ds_read_b64_tr_b16 a[36:37], v7 offset:55392\n\t" \
        "v_xor_b32 v141, v173, v12\n\t" \
        "v_mfma_f32_16x16x32_bf16 v[54:57], a[0:3], v[98:101], v[54:57]\n\t" \
        "ds_read_b64_tr_b16 a[38:39], v7 offset:58464\n\t" \
        "v_cvt_pk_bf16_f32 v78, v118, v119\n\t" \
        "v_mfma_f32_16x16x32_bf16 v[34:37], a[4:7], v[94:97], v[34:37]\n\t" \
        "ds_read_b64_tr_b16 a[40:41], v6 offset:55424\n\t" \
        "ds_read_b64_tr_b16 a[42:43], v6 offset:58496\n\t" \
        "v_mfma_f32_16x16x32_bf16 v[58:61], a[4:7], v[98:101], v[58:61]\n\t" \
        "s_waitcnt vmcnt(6)\n\t" \
        "s_barrier\n\t" \
        "ds_read_b64_tr_b16 a[44:45], v7 offset:55456\n\t" \
        "v_cvt_pk_bf16_f32 v79, v120, v121\n\t" \
        "v_mfma_f32_16x16x32_bf16 v[38:41], a[8:11], v[94:97], v[38:41]\n\t" \
        "ds_read_b64_tr_b16 a[46:47], v7 offset:58528\n\t" \
        "ds_read_b64_tr_b16 a[96:97], v134 offset:0\n\t" \
        "v_mfma_f32_16x16x32_bf16 v[62:65], a[8:11], v[98:101], v[62:65]\n\t" \
        "ds_read_b64_tr_b16 a[98:99], v135 offset:0\n\t" \
        "v_cvt_pk_bf16_f32 v80, v122, v123\n\t" \
        "v_mfma_f32_16x16x32_bf16 v[42:45], a[12:15], v[94:97], v[42:45]\n\t" \
        "s_add_u32 m0, s39, 36864\n\t" \
        "s_add_u32 s57, s40, 98304\n\t" \
        "buffer_load_dwordx4 v3, s[24:27], s57 offen offset:0  lds\n\t" \
        "v_mfma_f32_16x16x32_bf16 v[66:69], a[12:15], v[98:101], v[66:69]\n\t" \
        "ds_read_b64_tr_b16 a[100:101], v138 offset:0\n\t" \
        "v_cvt_pk_bf16_f32 v81, v124, v125\n\t" \
        "v_mfma_f32_16x16x32_bf16 v[46:49], a[16:19], v[94:97], v[46:49]\n\t" \
        "ds_read_b64_tr_b16 a[102:103], v139 offset:0\n\t" \
        "ds_read_b64_tr_b16 a[104:105], v136 offset:0\n\t" \
        "v_mfma_f32_16x16x32_bf16 v[70:73], a[16:19], v[98:101], v[70:73]\n\t" \
        "ds_read_b64_tr_b16 a[106:107], v137 offset:0\n\t" \
        "v_cvt_pk_bf16_f32 v82, v126, v127\n\t" \
        "s_waitcnt lgkmcnt(11)\n\t" \
        "v_mfma_f32_16x16x32_bf16 v[50:53], a[20:23], v[94:97], v[50:53]\n\t" \
        "ds_read_b64_tr_b16 a[108:109], v140 offset:0\n\t" \
        "ds_read_b64_tr_b16 a[110:111], v141 offset:0\n\t" \
        "v_cvt_pk_bf16_f32 v83, v128, v129\n\t" \
        "v_mfma_f32_16x16x32_bf16 v[74:77], a[20:23], v[98:101], v[74:77]\n\t" \
        "ds_read_b64_tr_b16 a[48:49], v6 offset:0\n\t" \
        "v_cvt_pk_bf16_f32 v84, v130, v131\n\t" \
        "v_cvt_pk_bf16_f32 v85, v132, v133\n\t" \
        "v_mfma_f32_16x16x32_bf16 v[118:121], a[128:131], v[162:165], 0\n\t" \
        "buffer_load_dwordx4 v4, s[24:27], s57 offen offset:1024  lds\n\t" \
        "v_mfma_f32_16x16x32_bf16 v[122:125], a[132:135], v[162:165], 0\n\t" \
        "ds_read_b64_tr_b16 a[50:51], v6 offset:3072\n\t" \
        "ds_read_b64_tr_b16 a[52:53], v7 offset:32\n\t" \
        "v_mfma_f32_16x16x32_bf16 v[126:129], a[136:139], v[166:169], 0\n\t" \
        "ds_read_b64_tr_b16 a[54:55], v7 offset:3104\n\t" \
        "v_cvt_pk_bf16_f32 v86, v102, v103\n\t" \
        "v_mfma_f32_16x16x32_bf16 v[130:133], a[140:143], v[166:169], 0\n\t" \
        "ds_read_b64_tr_b16 a[56:57], v6 offset:64\n\t" \
        "v_and_b32 v154, v150, v25\n\t" \
        "v_mfma_f32_16x16x32_bf16 v[30:33], a[24:27], v[78:81], v[30:33]\n\t" \
        "ds_read_b64_tr_b16 a[58:59], v6 offset:3136\n\t" \
        "ds_read_b64_tr_b16 a[60:61], v7 offset:96\n\t" \
        "v_mfma_f32_16x16x32_bf16 v[54:57], a[24:27], v[82:85], v[54:57]\n\t" \
        "ds_read_b64_tr_b16 a[62:63], v7 offset:3168\n\t" \
        "v_and_b32 v155, v150, v26\n\t" \
        "v_mfma_f32_16x16x32_bf16 v[34:37], a[28:31], v[78:81], v[34:37]\n\t" \
        "buffer_load_dwordx4 v5, s[24:27], s57 offen offset:2048  lds\n\t" \
        "v_mfma_f32_16x16x32_bf16 v[58:61], a[28:31], v[82:85], v[58:61]\n\t" \
        "ds_read_b64_tr_b16 a[64:65], v6 offset:128\n\t" \
        "ds_read_b128 v[174:177], v9 offset:640\n\t" \
        "v_mfma_f32_16x16x32_bf16 v[38:41], a[32:35], v[78:81], v[38:41]\n\t" \
        "ds_read_b64_tr_b16 a[66:67], v6 offset:3200\n\t" \
        "v_and_b32 v156, v150, v27\n\t" \
        "v_mfma_f32_16x16x32_bf16 v[62:65], a[32:35], v[82:85], v[62:65]\n\t" \
        "ds_read_b64_tr_b16 a[68:69], v7 offset:160\n\t" \
        "ds_read_b64_tr_b16 a[70:71], v7 offset:3232\n\t" \
        "s_waitcnt lgkmcnt(12)\n\t" \
        "v_mfma_f32_16x16x32_bf16 v[42:45], a[36:39], v[78:81], v[42:45]\n\t" \
        "ds_read_b64_tr_b16 a[112:113], v134 offset:64\n\t" \
        "v_and_b32 v157, v150, v28\n\t" \
        "v_and_b32 v158, v151, v25\n\t" \
        "v_mfma_f32_16x16x32_bf16 v[66:69], a[36:39], v[82:85], v[66:69]\n\t" \
        "ds_read_b64_tr_b16 a[114:115], v135 offset:64\n\t" \
        "v_cvt_pk_bf16_f32 v87, v104, v105\n\t" \
        "v_mfma_f32_16x16x32_bf16 v[46:49], a[40:43], v[78:81], v[46:49]\n\t" \
        "ds_read_b64_tr_b16 a[116:117], v138 offset:64\n\t" \
        "ds_read_b64_tr_b16 a[118:119], v139 offset:64\n\t" \
        "v_cvt_pk_bf16_f32 v88, v106, v107\n\t" \
        "v_and_b32 v159, v151, v26\n\t" \
        "v_mfma_f32_16x16x32_bf16 v[70:73], a[40:43], v[82:85], v[70:73]\n\t" \
        "ds_read_b64_tr_b16 a[120:121], v136 offset:64\n\t" \
        "v_cvt_pk_bf16_f32 v89, v108, v109\n\t" \
        "v_cvt_pk_bf16_f32 v90, v110, v111\n\t" \
        "v_mfma_f32_16x16x32_bf16 v[50:53], a[44:47], v[78:81], v[50:53]\n\t" \
        "ds_read_b64_tr_b16 a[122:123], v137 offset:64\n\t" \
        "ds_read_b64_tr_b16 a[124:125], v140 offset:64\n\t" \
        "v_and_b32 v160, v151, v27\n\t" \
        "v_mfma_f32_16x16x32_bf16 v[74:77], a[44:47], v[82:85], v[74:77]\n\t" \
        "ds_read_b64_tr_b16 a[126:127], v141 offset:64\n\t" \
        "v_cvt_pk_bf16_f32 v91, v112, v113\n\t" \
        "v_cvt_pk_bf16_f32 v92, v114, v115\n\t" \
        "v_mfma_f32_16x16x32_bf16 v[102:105], a[96:99], v[154:157], 0\n\t" \
        "ds_read_b64_tr_b16 a[72:73], v6 offset:6144\n\t" \
        "v_and_b32 v161, v151, v28\n\t" \
        "v_cvt_pk_bf16_f32 v93, v116, v117\n\t" \
        "v_mfma_f32_16x16x32_bf16 v[106:109], a[100:103], v[154:157], 0\n\t" \
        "ds_read_b64_tr_b16 a[74:75], v6 offset:9216\n\t" \
        "ds_read_b64_tr_b16 a[76:77], v7 offset:6176\n\t" \
        "v_mfma_f32_16x16x32_bf16 v[110:113], a[104:107], v[158:161], 0\n\t" \
        "ds_read_b64_tr_b16 a[78:79], v7 offset:9248\n\t" \
        "v_cvt_pk_bf16_f32 v94, v118, v119\n\t" \
        "v_mfma_f32_16x16x32_bf16 v[114:117], a[108:111], v[158:161], 0\n\t" \
        "ds_read_b64_tr_b16 a[80:81], v6 offset:6208\n\t" \
        "ds_read_b64_tr_b16 a[82:83], v6 offset:9280\n\t" \
        "s_waitcnt lgkmcnt(13)\n\t" \
        "v_mfma_f32_16x16x32_bf16 v[30:33], a[48:51], v[86:89], v[30:33]\n\t" \
        "ds_read_b64_tr_b16 a[84:85], v7 offset:6240\n\t" \
        "v_cvt_pk_bf16_f32 v95, v120, v121\n\t" \
        "v_mfma_f32_16x16x32_bf16 v[54:57], a[48:51], v[90:93], v[54:57]\n\t" \
        "ds_read_b64_tr_b16 a[86:87], v7 offset:9312\n\t" \
        "ds_read_b64_tr_b16 a[88:89], v6 offset:6272\n\t" \
        "v_mfma_f32_16x16x32_bf16 v[34:37], a[52:55], v[86:89], v[34:37]\n\t" \
        "ds_read_b64_tr_b16 a[90:91], v6 offset:9344\n\t" \
        "v_cvt_pk_bf16_f32 v96, v122, v123\n\t" \
        "v_mfma_f32_16x16x32_bf16 v[58:61], a[52:55], v[90:93], v[58:61]\n\t" \
        "s_waitcnt vmcnt(6)\n\t" \
        "s_barrier\n\t" \
        "ds_read_b64_tr_b16 a[92:93], v7 offset:6304\n\t" \
        "v_cvt_pk_bf16_f32 v97, v124, v125\n\t" \
        "v_mfma_f32_16x16x32_bf16 v[38:41], a[56:59], v[86:89], v[38:41]\n\t" \
        "ds_read_b64_tr_b16 a[94:95], v7 offset:9376\n\t" \
        "ds_read_b64_tr_b16 a[128:129], v134 offset:128\n\t" \
        "v_mfma_f32_16x16x32_bf16 v[62:65], a[56:59], v[90:93], v[62:65]\n\t" \
        "ds_read_b64_tr_b16 a[130:131], v135 offset:128\n\t" \
        "v_cvt_pk_bf16_f32 v98, v126, v127\n\t" \
        "v_mfma_f32_16x16x32_bf16 v[42:45], a[60:63], v[86:89], v[42:45]\n\t" \
        "s_add_u32 m0, s39, 49152\n\t" \
        "s_add_u32 s57, s40, 110592\n\t" \
        "buffer_load_dwordx4 v3, s[24:27], s57 offen offset:0  lds\n\t" \
        "v_mfma_f32_16x16x32_bf16 v[66:69], a[60:63], v[90:93], v[66:69]\n\t" \
        "ds_read_b64_tr_b16 a[132:133], v138 offset:128\n\t" \
        "ds_read_b64_tr_b16 a[134:135], v139 offset:128\n\t" \
        "v_mfma_f32_16x16x32_bf16 v[46:49], a[64:67], v[86:89], v[46:49]\n\t" \
        "ds_read_b64_tr_b16 a[136:137], v136 offset:128\n\t" \
        "v_xor_b32 v142, v174, v11\n\t" \
        "v_mfma_f32_16x16x32_bf16 v[70:73], a[64:67], v[90:93], v[70:73]\n\t" \
        "ds_read_b64_tr_b16 a[138:139], v137 offset:128\n\t" \
        "ds_read_b64_tr_b16 a[140:141], v140 offset:128\n\t" \
        "v_cvt_pk_bf16_f32 v99, v128, v129\n\t" \
        "v_mfma_f32_16x16x32_bf16 v[50:53], a[68:71], v[86:89], v[50:53]\n\t" \
        "ds_read_b64_tr_b16 a[142:143], v141 offset:128\n\t" \
        "v_cvt_pk_bf16_f32 v100, v130, v131\n\t" \
        "v_xor_b32 v143, v175, v11\n\t" \
        "v_mfma_f32_16x16x32_bf16 v[74:77], a[68:71], v[90:93], v[74:77]\n\t" \
        "ds_read_b64_tr_b16 a[0:1], v6 offset:12288\n\t" \
        "v_cvt_pk_bf16_f32 v101, v132, v133\n\t" \
        "s_waitcnt lgkmcnt(11)\n\t" \
        "v_mfma_f32_16x16x32_bf16 v[118:121], a[112:115], v[154:157], 0\n\t" \
        "buffer_load_dwordx4 v4, s[24:27], s57 offen offset:1024  lds\n\t" \
        "v_mfma_f32_16x16x32_bf16 v[122:125], a[116:119], v[154:157], 0\n\t" \
        "ds_read_b64_tr_b16 a[2:3], v6 offset:15360\n\t" \
        "ds_read_b64_tr_b16 a[4:5], v7 offset:12320\n\t" \
        "v_xor_b32 v144, v176, v11\n\t" \
        "v_xor_b32 v146, v174, v12\n\t" \
        "v_mfma_f32_16x16x32_bf16 v[126:129], a[120:123], v[158:161], 0\n\t" \
        "ds_read_b64 v[152:153], v10 offset:640\n\t" \
        "v_xor_b32 v145, v177, v11\n\t" \
        "v_xor_b32 v147, v175, v12\n\t" \
        "v_mfma_f32_16x16x32_bf16 v[130:133], a[124:127], v[158:161], 0\n\t" \
        "ds_read_b64_tr_b16 a[6:7], v7 offset:15392\n\t" \
        "ds_read_b64_tr_b16 a[8:9], v6 offset:12352\n\t" \
        "v_xor_b32 v148, v176, v12\n\t" \
        "v_mfma_f32_16x16x32_bf16 v[30:33], a[72:75], v[94:97], v[30:33]\n\t" \
        "ds_read_b64_tr_b16 a[10:11], v6 offset:15424\n\t" \
        "v_xor_b32 v149, v177, v12\n\t" \
        "v_mfma_f32_16x16x32_bf16 v[54:57], a[72:75], v[98:101], v[54:57]\n\t" \
        "ds_read_b64_tr_b16 a[12:13], v7 offset:12384\n\t" \
        "ds_read_b64_tr_b16 a[14:15], v7 offset:15456\n\t" \
        "v_mfma_f32_16x16x32_bf16 v[34:37], a[76:79], v[94:97], v[34:37]\n\t" \
        "buffer_load_dwordx4 v5, s[24:27], s57 offen offset:2048  lds\n\t" \
        "v_mfma_f32_16x16x32_bf16 v[58:61], a[76:79], v[98:101], v[58:61]\n\t" \
        "ds_read_b64_tr_b16 a[16:17], v6 offset:12416\n\t" \
        "v_cvt_pk_bf16_f32 v78, v102, v103\n\t" \
        "v_mfma_f32_16x16x32_bf16 v[38:41], a[80:83], v[94:97], v[38:41]\n\t" \
        "ds_read_b64_tr_b16 a[18:19], v6 offset:15488\n\t" \
        "v_cvt_pk_bf16_f32 v79, v104, v105\n\t" \
        "v_mfma_f32_16x16x32_bf16 v[62:65], a[80:83], v[98:101], v[62:65]\n\t" \
        "ds_read_b64_tr_b16 a[20:21], v7 offset:12448\n\t" \
        "ds_read_b64_tr_b16 a[22:23], v7 offset:15520\n\t" \
        "v_mfma_f32_16x16x32_bf16 v[42:45], a[84:87], v[94:97], v[42:45]\n\t" \
        "ds_read_b64_tr_b16 a[96:97], v142 offset:0\n\t" \
        "v_cvt_pk_bf16_f32 v80, v106, v107\n\t" \
        "v_mfma_f32_16x16x32_bf16 v[66:69], a[84:87], v[98:101], v[66:69]\n\t" \
        "ds_read_b64_tr_b16 a[98:99], v143 offset:0\n\t" \
        "ds_read_b64_tr_b16 a[100:101], v146 offset:0\n\t" \
        "v_mfma_f32_16x16x32_bf16 v[46:49], a[88:91], v[94:97], v[46:49]\n\t" \
        "ds_read_b64_tr_b16 a[102:103], v147 offset:0\n\t" \
        "v_cvt_pk_bf16_f32 v81, v108, v109\n\t" \
        "v_mfma_f32_16x16x32_bf16 v[70:73], a[88:91], v[98:101], v[70:73]\n\t" \
        "ds_read_b64_tr_b16 a[104:105], v144 offset:0\n\t" \
        "ds_read_b64_tr_b16 a[106:107], v145 offset:0\n\t" \
        "s_waitcnt lgkmcnt(12)\n\t" \
        "v_mfma_f32_16x16x32_bf16 v[50:53], a[92:95], v[94:97], v[50:53]\n\t" \
        "ds_read_b64_tr_b16 a[108:109], v148 offset:0\n\t" \
        "v_cvt_pk_bf16_f32 v82, v110, v111\n\t" \
        "v_mfma_f32_16x16x32_bf16 v[74:77], a[92:95], v[98:101], v[74:77]\n\t" \
        "ds_read_b64_tr_b16 a[110:111], v149 offset:0\n\t" \
        "v_cvt_pk_bf16_f32 v83, v112, v113\n\t" \
        "v_cvt_pk_bf16_f32 v84, v114, v115\n\t" \
        "v_mfma_f32_16x16x32_bf16 v[102:105], a[128:131], v[154:157], 0\n\t" \
        "ds_read_b64_tr_b16 a[24:25], v6 offset:18432\n\t" \
        "ds_read_b64_tr_b16 a[26:27], v6 offset:21504\n\t" \
        "v_cvt_pk_bf16_f32 v85, v116, v117\n\t" \
        "v_mfma_f32_16x16x32_bf16 v[106:109], a[132:135], v[154:157], 0\n\t" \
        "ds_read_b64_tr_b16 a[28:29], v7 offset:18464\n\t" \
        "v_cvt_pk_bf16_f32 v86, v118, v119\n\t" \
        "v_mfma_f32_16x16x32_bf16 v[110:113], a[136:139], v[158:161], 0\n\t" \
        "ds_read_b64_tr_b16 a[30:31], v7 offset:21536\n\t" \
        "ds_read_b64_tr_b16 a[32:33], v6 offset:18496\n\t" \
        "v_mfma_f32_16x16x32_bf16 v[114:117], a[140:143], v[158:161], 0\n\t" \
        "ds_read_b64_tr_b16 a[34:35], v6 offset:21568\n\t" \
        "v_cvt_pk_bf16_f32 v87, v120, v121\n\t" \
        "v_mfma_f32_16x16x32_bf16 v[30:33], a[0:3], v[78:81], v[30:33]\n\t" \
        "ds_read_b64_tr_b16 a[36:37], v7 offset:18528\n\t" \
        "ds_read_b64_tr_b16 a[38:39], v7 offset:21600\n\t" \
        "v_mfma_f32_16x16x32_bf16 v[54:57], a[0:3], v[82:85], v[54:57]\n\t" \
        "ds_read_b64_tr_b16 a[40:41], v6 offset:18560\n\t" \
        "v_and_b32 v162, v152, v25\n\t" \
        "v_mfma_f32_16x16x32_bf16 v[34:37], a[4:7], v[78:81], v[34:37]\n\t" \
        "ds_read_b128 v[170:173], v9 offset:768\n\t" \
        "ds_read_b64_tr_b16 a[42:43], v6 offset:21632\n\t" \
        "v_mfma_f32_16x16x32_bf16 v[58:61], a[4:7], v[82:85], v[58:61]\n\t" \
        "s_waitcnt vmcnt(6)\n\t" \
        "s_barrier\n\t" \
        "ds_read_b64_tr_b16 a[44:45], v7 offset:18592\n\t" \
        "v_and_b32 v163, v152, v26\n\t" \
        "v_mfma_f32_16x16x32_bf16 v[38:41], a[8:11], v[78:81], v[38:41]\n\t" \
        "ds_read_b64_tr_b16 a[46:47], v7 offset:21664\n\t" \
        "v_and_b32 v164, v152, v27\n\t" \
        "v_and_b32 v165, v152, v28\n\t" \
        "v_mfma_f32_16x16x32_bf16 v[62:65], a[8:11], v[82:85], v[62:65]\n\t" \
        "ds_read_b64_tr_b16 a[112:113], v142 offset:64\n\t" \
        "ds_read_b64_tr_b16 a[114:115], v143 offset:64\n\t" \
        "v_and_b32 v166, v153, v25\n\t" \
        "s_waitcnt lgkmcnt(13)\n\t" \
        "v_mfma_f32_16x16x32_bf16 v[42:45], a[12:15], v[78:81], v[42:45]\n\t" \
        "s_add_u32 m0, s39, 0\n\t" \
        "s_add_u32 s57, s40, 122880\n\t" \
        "buffer_load_dwordx4 v3, s[24:27], s57 offen offset:0  lds\n\t" \
        "v_mfma_f32_16x16x32_bf16 v[66:69], a[12:15], v[82:85], v[66:69]\n\t" \
        "ds_read_b64_tr_b16 a[116:117], v146 offset:64\n\t" \
        "v_cvt_pk_bf16_f32 v88, v122, v123\n\t" \
        "v_and_b32 v167, v153, v26\n\t" \
        "v_mfma_f32_16x16x32_bf16 v[46:49], a[16:19], v[78:81], v[46:49]\n\t" \
        "ds_read_b64_tr_b16 a[118:119], v147 offset:64\n\t" \
        "ds_read_b64_tr_b16 a[120:121], v144 offset:64\n\t" \
        "v_cvt_pk_bf16_f32 v89, v124, v125\n\t" \
        "v_cvt_pk_bf16_f32 v90, v126, v127\n\t" \
        "v_mfma_f32_16x16x32_bf16 v[70:73], a[16:19], v[82:85], v[70:73]\n\t" \
        "ds_read_b64_tr_b16 a[122:123], v145 offset:64\n\t" \
        "v_and_b32 v168, v153, v27\n\t" \
        "v_mfma_f32_16x16x32_bf16 v[50:53], a[20:23], v[78:81], v[50:53]\n\t" \
        "ds_read_b64_tr_b16 a[124:125], v148 offset:64\n\t" \
        "ds_read_b64_tr_b16 a[126:127], v149 offset:64\n\t" \
        "v_cvt_pk_bf16_f32 v91, v128, v129\n\t" \
        "v_cvt_pk_bf16_f32 v92, v130, v131\n\t" \
        "v_mfma_f32_16x16x32_bf16 v[74:77], a[20:23], v[82:85], v[74:77]\n\t" \
        "ds_read_b64_tr_b16 a[48:49], v6 offset:24576\n\t" \
        "v_and_b32 v169, v153, v28\n\t" \
        "v_cvt_pk_bf16_f32 v93, v132, v133\n\t" \
        "v_mfma_f32_16x16x32_bf16 v[118:121], a[96:99], v[162:165], 0\n\t" \
        "buffer_load_dwordx4 v4, s[24:27], s57 offen offset:1024  lds\n\t" \
        "v_mfma_f32_16x16x32_bf16 v[122:125], a[100:103], v[162:165], 0\n\t" \
        "ds_read_b64_tr_b16 a[50:51], v6 offset:27648\n\t" \
        "ds_read_b64_tr_b16 a[52:53], v7 offset:24608\n\t" \
        "v_mfma_f32_16x16x32_bf16 v[126:129], a[104:107], v[166:169], 0\n\t" \
        "ds_read_b64_tr_b16 a[54:55], v7 offset:27680\n\t" \
        "v_cvt_pk_bf16_f32 v94, v102, v103\n\t" \
        "v_mfma_f32_16x16x32_bf16 v[130:133], a[108:111], v[166:169], 0\n\t" \
        "ds_read_b64_tr_b16 a[56:57], v6 offset:24640\n\t" \
        "v_cvt_pk_bf16_f32 v95, v104, v105\n\t" \
        "v_mfma_f32_16x16x32_bf16 v[30:33], a[24:27], v[86:89], v[30:33]\n\t" \
        "ds_read_b64_tr_b16 a[58:59], v6 offset:27712\n\t" \
        "ds_read_b64_tr_b16 a[60:61], v7 offset:24672\n\t" \
        "v_mfma_f32_16x16x32_bf16 v[54:57], a[24:27], v[90:93], v[54:57]\n\t" \
        "ds_read_b64_tr_b16 a[62:63], v7 offset:27744\n\t" \
        "v_cvt_pk_bf16_f32 v96, v106, v107\n\t" \
        "s_waitcnt lgkmcnt(11)\n\t" \
        "v_mfma_f32_16x16x32_bf16 v[34:37], a[28:31], v[86:89], v[34:37]\n\t" \
        "buffer_load_dwordx4 v5, s[24:27], s57 offen offset:2048  lds\n\t" \
        "v_mfma_f32_16x16x32_bf16 v[58:61], a[28:31], v[90:93], v[58:61]\n\t" \
        "ds_read_b64_tr_b16 a[64:65], v6 offset:24704\n\t" \
        "ds_read_b64_tr_b16 a[66:67], v6 offset:27776\n\t" \
        "v_mfma_f32_16x16x32_bf16 v[38:41], a[32:35], v[86:89], v[38:41]\n\t" \
        "ds_read_b64_tr_b16 a[68:69], v7 offset:24736\n\t" \
        "v_cvt_pk_bf16_f32 v97, v108, v109\n\t" \
        "v_mfma_f32_16x16x32_bf16 v[62:65], a[32:35], v[90:93], v[62:65]\n\t" \
        "ds_read_b64_tr_b16 a[70:71], v7 offset:27808\n\t" \
        "v_cvt_pk_bf16_f32 v98, v110, v111\n\t" \
        "v_mfma_f32_16x16x32_bf16 v[42:45], a[36:39], v[86:89], v[42:45]\n\t" \
        "ds_read_b64_tr_b16 a[128:129], v142 offset:128\n\t" \
        "ds_read_b64_tr_b16 a[130:131], v143 offset:128\n\t" \
        "v_mfma_f32_16x16x32_bf16 v[66:69], a[36:39], v[90:93], v[66:69]\n\t" \
        "ds_read_b64_tr_b16 a[132:133], v146 offset:128\n\t" \
        "v_xor_b32 v134, v170, v11\n\t" \
        "v_mfma_f32_16x16x32_bf16 v[46:49], a[40:43], v[86:89], v[46:49]\n\t" \
        "ds_read_b64_tr_b16 a[134:135], v147 offset:128\n\t" \
        "ds_read_b64_tr_b16 a[136:137], v144 offset:128\n\t" \
        "v_mfma_f32_16x16x32_bf16 v[70:73], a[40:43], v[90:93], v[70:73]\n\t" \
        "ds_read_b64_tr_b16 a[138:139], v145 offset:128\n\t" \
        "v_cvt_pk_bf16_f32 v99, v112, v113\n\t" \
        "v_mfma_f32_16x16x32_bf16 v[50:53], a[44:47], v[86:89], v[50:53]\n\t" \
        "ds_read_b64_tr_b16 a[140:141], v148 offset:128\n\t" \
        "v_cvt_pk_bf16_f32 v100, v114, v115\n\t" \
        "v_mfma_f32_16x16x32_bf16 v[74:77], a[44:47], v[90:93], v[74:77]\n\t" \
        "ds_read_b64_tr_b16 a[142:143], v149 offset:128\n\t" \
        "ds_read_b64_tr_b16 a[72:73], v6 offset:30720\n\t" \
        "v_xor_b32 v135, v171, v11\n\t" \
        "v_mfma_f32_16x16x32_bf16 v[102:105], a[112:115], v[162:165], 0\n\t" \
        "ds_read_b64_tr_b16 a[74:75], v6 offset:33792\n\t" \
        "v_cvt_pk_bf16_f32 v101, v116, v117\n\t" \
        "v_mfma_f32_16x16x32_bf16 v[106:109], a[116:119], v[162:165], 0\n\t" \
        "ds_read_b64_tr_b16 a[76:77], v7 offset:30752\n\t" \
        "ds_read_b64 v[150:151], v10 offset:768\n\t" \
        "v_xor_b32 v136, v172, v11\n\t" \
        "v_xor_b32 v138, v170, v12\n\t" \
        "s_waitcnt lgkmcnt(13)\n\t" \
        "v_mfma_f32_16x16x32_bf16 v[110:113], a[120:123], v[166:169], 0\n\t" \
        "ds_read_b64_tr_b16 a[78:79], v7 offset:33824\n\t" \
        "v_xor_b32 v137, v173, v11\n\t" \
        "v_xor_b32 v139, v171, v12\n\t" \
        "v_mfma_f32_16x16x32_bf16 v[114:117], a[124:127], v[166:169], 0\n\t" \
        "ds_read_b64_tr_b16 a[80:81], v6 offset:30784\n\t" \
        "ds_read_b64_tr_b16 a[82:83], v6 offset:33856\n\t" \
        "v_xor_b32 v140, v172, v12\n\t" \
        "v_mfma_f32_16x16x32_bf16 v[30:33], a[48:51], v[94:97], v[30:33]\n\t" \
        "ds_read_b64_tr_b16 a[84:85], v7 offset:30816\n\t" \
        "v_xor_b32 v141, v173, v12\n\t" \
        "v_mfma_f32_16x16x32_bf16 v[54:57], a[48:51], v[98:101], v[54:57]\n\t" \
        "ds_read_b64_tr_b16 a[86:87], v7 offset:33888\n\t" \
        "v_cvt_pk_bf16_f32 v78, v118, v119\n\t" \
        "v_mfma_f32_16x16x32_bf16 v[34:37], a[52:55], v[94:97], v[34:37]\n\t" \
        "ds_read_b64_tr_b16 a[88:89], v6 offset:30848\n\t" \
        "ds_read_b64_tr_b16 a[90:91], v6 offset:33920\n\t" \
        "v_mfma_f32_16x16x32_bf16 v[58:61], a[52:55], v[98:101], v[58:61]\n\t" \
        "s_waitcnt vmcnt(6)\n\t" \
        "s_barrier\n\t" \
        "ds_read_b64_tr_b16 a[92:93], v7 offset:30880\n\t" \
        "v_cvt_pk_bf16_f32 v79, v120, v121\n\t" \
        "v_mfma_f32_16x16x32_bf16 v[38:41], a[56:59], v[94:97], v[38:41]\n\t" \
        "ds_read_b64_tr_b16 a[94:95], v7 offset:33952\n\t" \
        "ds_read_b64_tr_b16 a[96:97], v134 offset:0\n\t" \
        "v_mfma_f32_16x16x32_bf16 v[62:65], a[56:59], v[98:101], v[62:65]\n\t" \
        "ds_read_b64_tr_b16 a[98:99], v135 offset:0\n\t" \
        "v_cvt_pk_bf16_f32 v80, v122, v123\n\t" \
        "v_mfma_f32_16x16x32_bf16 v[42:45], a[60:63], v[94:97], v[42:45]\n\t" \
        "s_add_u32 m0, s39, 12288\n\t" \
        "s_add_u32 s57, s40, 135168\n\t" \
        "buffer_load_dwordx4 v3, s[24:27], s57 offen offset:0  lds\n\t" \
        "v_mfma_f32_16x16x32_bf16 v[66:69], a[60:63], v[98:101], v[66:69]\n\t" \
        "ds_read_b64_tr_b16 a[100:101], v138 offset:0\n\t" \
        "v_cvt_pk_bf16_f32 v81, v124, v125\n\t" \
        "v_mfma_f32_16x16x32_bf16 v[46:49], a[64:67], v[94:97], v[46:49]\n\t" \
        "ds_read_b64_tr_b16 a[102:103], v139 offset:0\n\t" \
        "ds_read_b64_tr_b16 a[104:105], v136 offset:0\n\t" \
        "v_mfma_f32_16x16x32_bf16 v[70:73], a[64:67], v[98:101], v[70:73]\n\t" \
        "ds_read_b64_tr_b16 a[106:107], v137 offset:0\n\t" \
        "v_cvt_pk_bf16_f32 v82, v126, v127\n\t" \
        "s_waitcnt lgkmcnt(11)\n\t" \
        "v_mfma_f32_16x16x32_bf16 v[50:53], a[68:71], v[94:97], v[50:53]\n\t" \
        "ds_read_b64_tr_b16 a[108:109], v140 offset:0\n\t" \
        "ds_read_b64_tr_b16 a[110:111], v141 offset:0\n\t" \
        "v_cvt_pk_bf16_f32 v83, v128, v129\n\t" \
        "v_mfma_f32_16x16x32_bf16 v[74:77], a[68:71], v[98:101], v[74:77]\n\t" \
        "ds_read_b64_tr_b16 a[0:1], v6 offset:36864\n\t" \
        "v_cvt_pk_bf16_f32 v84, v130, v131\n\t" \
        "v_cvt_pk_bf16_f32 v85, v132, v133\n\t" \
        "v_mfma_f32_16x16x32_bf16 v[118:121], a[128:131], v[162:165], 0\n\t" \
        "buffer_load_dwordx4 v4, s[24:27], s57 offen offset:1024  lds\n\t" \
        "v_mfma_f32_16x16x32_bf16 v[122:125], a[132:135], v[162:165], 0\n\t" \
        "ds_read_b64_tr_b16 a[2:3], v6 offset:39936\n\t" \
        "ds_read_b64_tr_b16 a[4:5], v7 offset:36896\n\t" \
        "v_mfma_f32_16x16x32_bf16 v[126:129], a[136:139], v[166:169], 0\n\t" \
        "ds_read_b64_tr_b16 a[6:7], v7 offset:39968\n\t" \
        "v_cvt_pk_bf16_f32 v86, v102, v103\n\t" \
        "v_mfma_f32_16x16x32_bf16 v[130:133], a[140:143], v[166:169], 0\n\t" \
        "ds_read_b64_tr_b16 a[8:9], v6 offset:36928\n\t" \
        "v_and_b32 v154, v150, v25\n\t" \
        "v_mfma_f32_16x16x32_bf16 v[30:33], a[72:75], v[78:81], v[30:33]\n\t" \
        "ds_read_b64_tr_b16 a[10:11], v6 offset:40000\n\t" \
        "ds_read_b64_tr_b16 a[12:13], v7 offset:36960\n\t" \
        "v_mfma_f32_16x16x32_bf16 v[54:57], a[72:75], v[82:85], v[54:57]\n\t" \
        "ds_read_b64_tr_b16 a[14:15], v7 offset:40032\n\t" \
        "v_and_b32 v155, v150, v26\n\t" \
        "v_mfma_f32_16x16x32_bf16 v[34:37], a[76:79], v[78:81], v[34:37]\n\t" \
        "buffer_load_dwordx4 v5, s[24:27], s57 offen offset:2048  lds\n\t" \
        "v_mfma_f32_16x16x32_bf16 v[58:61], a[76:79], v[82:85], v[58:61]\n\t" \
        "ds_read_b64_tr_b16 a[16:17], v6 offset:36992\n\t" \
        "ds_read_b128 v[174:177], v9 offset:896\n\t" \
        "v_mfma_f32_16x16x32_bf16 v[38:41], a[80:83], v[78:81], v[38:41]\n\t" \
        "ds_read_b64_tr_b16 a[18:19], v6 offset:40064\n\t" \
        "v_and_b32 v156, v150, v27\n\t" \
        "v_mfma_f32_16x16x32_bf16 v[62:65], a[80:83], v[82:85], v[62:65]\n\t" \
        "ds_read_b64_tr_b16 a[20:21], v7 offset:37024\n\t" \
        "v_and_b32 v157, v150, v28\n\t" \
        "s_waitcnt lgkmcnt(11)\n\t" \
        "v_mfma_f32_16x16x32_bf16 v[42:45], a[84:87], v[78:81], v[42:45]\n\t" \
        "ds_read_b64_tr_b16 a[22:23], v7 offset:40096\n\t" \
        "ds_read_b64_tr_b16 a[112:113], v134 offset:64\n\t" \
        "v_and_b32 v158, v151, v25\n\t" \
        "v_mfma_f32_16x16x32_bf16 v[66:69], a[84:87], v[82:85], v[66:69]\n\t" \
        "ds_read_b64_tr_b16 a[114:115], v135 offset:64\n\t" \
        "v_cvt_pk_bf16_f32 v87, v104, v105\n\t" \
        "v_mfma_f32_16x16x32_bf16 v[46:49], a[88:91], v[78:81], v[46:49]\n\t" \
        "ds_read_b64_tr_b16 a[116:117], v138 offset:64\n\t" \
        "ds_read_b64_tr_b16 a[118:119], v139 offset:64\n\t" \
        "v_cvt_pk_bf16_f32 v88, v106, v107\n\t" \
        "v_and_b32 v159, v151, v26\n\t" \
        "v_mfma_f32_16x16x32_bf16 v[70:73], a[88:91], v[82:85], v[70:73]\n\t" \
        "ds_read_b64_tr_b16 a[120:121], v136 offset:64\n\t" \
        "v_cvt_pk_bf16_f32 v89, v108, v109\n\t" \
        "v_cvt_pk_bf16_f32 v90, v110, v111\n\t" \
        "v_mfma_f32_16x16x32_bf16 v[50:53], a[92:95], v[78:81], v[50:53]\n\t" \
        "ds_read_b64_tr_b16 a[122:123], v137 offset:64\n\t" \
        "ds_read_b64_tr_b16 a[124:125], v140 offset:64\n\t" \
        "v_and_b32 v160, v151, v27\n\t" \
        "v_mfma_f32_16x16x32_bf16 v[74:77], a[92:95], v[82:85], v[74:77]\n\t" \
        "ds_read_b64_tr_b16 a[126:127], v141 offset:64\n\t" \
        "v_cvt_pk_bf16_f32 v91, v112, v113\n\t" \
        "v_cvt_pk_bf16_f32 v92, v114, v115\n\t" \
        "v_mfma_f32_16x16x32_bf16 v[102:105], a[96:99], v[154:157], 0\n\t" \
        "ds_read_b64_tr_b16 a[24:25], v6 offset:43008\n\t" \
        "v_and_b32 v161, v151, v28\n\t" \
        "v_cvt_pk_bf16_f32 v93, v116, v117\n\t" \
        "v_mfma_f32_16x16x32_bf16 v[106:109], a[100:103], v[154:157], 0\n\t" \
        "ds_read_b64_tr_b16 a[26:27], v6 offset:46080\n\t" \
        "ds_read_b64_tr_b16 a[28:29], v7 offset:43040\n\t" \
        "v_mfma_f32_16x16x32_bf16 v[110:113], a[104:107], v[158:161], 0\n\t" \
        "ds_read_b64_tr_b16 a[30:31], v7 offset:46112\n\t" \
        "v_cvt_pk_bf16_f32 v94, v118, v119\n\t" \
        "v_mfma_f32_16x16x32_bf16 v[114:117], a[108:111], v[158:161], 0\n\t" \
        "ds_read_b64_tr_b16 a[32:33], v6 offset:43072\n\t" \
        "ds_read_b64_tr_b16 a[34:35], v6 offset:46144\n\t" \
        "s_waitcnt lgkmcnt(13)\n\t" \
        "v_mfma_f32_16x16x32_bf16 v[30:33], a[0:3], v[86:89], v[30:33]\n\t" \
        "ds_read_b64_tr_b16 a[36:37], v7 offset:43104\n\t" \
        "v_cvt_pk_bf16_f32 v95, v120, v121\n\t" \
        "v_mfma_f32_16x16x32_bf16 v[54:57], a[0:3], v[90:93], v[54:57]\n\t" \
        "ds_read_b64_tr_b16 a[38:39], v7 offset:46176\n\t" \
        "ds_read_b64_tr_b16 a[40:41], v6 offset:43136\n\t" \
        "v_mfma_f32_16x16x32_bf16 v[34:37], a[4:7], v[86:89], v[34:37]\n\t" \
        "ds_read_b64_tr_b16 a[42:43], v6 offset:46208\n\t" \
        "v_cvt_pk_bf16_f32 v96, v122, v123\n\t" \
        "v_mfma_f32_16x16x32_bf16 v[58:61], a[4:7], v[90:93], v[58:61]\n\t" \
        "s_waitcnt vmcnt(6)\n\t" \
        "s_barrier\n\t" \
        "ds_read_b64_tr_b16 a[44:45], v7 offset:43168\n\t" \
        "v_cvt_pk_bf16_f32 v97, v124, v125\n\t" \
        "v_mfma_f32_16x16x32_bf16 v[38:41], a[8:11], v[86:89], v[38:41]\n\t" \
        "ds_read_b64_tr_b16 a[46:47], v7 offset:46240\n\t" \
        "ds_read_b64_tr_b16 a[128:129], v134 offset:128\n\t" \
        "v_mfma_f32_16x16x32_bf16 v[62:65], a[8:11], v[90:93], v[62:65]\n\t" \
        "ds_read_b64_tr_b16 a[130:131], v135 offset:128\n\t" \
        "v_cvt_pk_bf16_f32 v98, v126, v127\n\t" \
        "v_mfma_f32_16x16x32_bf16 v[42:45], a[12:15], v[86:89], v[42:45]\n\t" \
        "s_add_u32 m0, s39, 24576\n\t" \
        "s_add_u32 s57, s40, 147456\n\t" \
        "buffer_load_dwordx4 v3, s[24:27], s57 offen offset:0  lds\n\t" \
        "v_mfma_f32_16x16x32_bf16 v[66:69], a[12:15], v[90:93], v[66:69]\n\t" \
        "ds_read_b64_tr_b16 a[132:133], v138 offset:128\n\t" \
        "ds_read_b64_tr_b16 a[134:135], v139 offset:128\n\t" \
        "v_mfma_f32_16x16x32_bf16 v[46:49], a[16:19], v[86:89], v[46:49]\n\t" \
        "ds_read_b64_tr_b16 a[136:137], v136 offset:128\n\t" \
        "v_xor_b32 v142, v174, v11\n\t" \
        "v_mfma_f32_16x16x32_bf16 v[70:73], a[16:19], v[90:93], v[70:73]\n\t" \
        "ds_read_b64_tr_b16 a[138:139], v137 offset:128\n\t" \
        "ds_read_b64_tr_b16 a[140:141], v140 offset:128\n\t" \
        "v_cvt_pk_bf16_f32 v99, v128, v129\n\t" \
        "v_mfma_f32_16x16x32_bf16 v[50:53], a[20:23], v[86:89], v[50:53]\n\t" \
        "ds_read_b64_tr_b16 a[142:143], v141 offset:128\n\t" \
        "v_cvt_pk_bf16_f32 v100, v130, v131\n\t" \
        "v_xor_b32 v143, v175, v11\n\t" \
        "v_mfma_f32_16x16x32_bf16 v[74:77], a[20:23], v[90:93], v[74:77]\n\t" \
        "ds_read_b64_tr_b16 a[48:49], v6 offset:49152\n\t" \
        "v_cvt_pk_bf16_f32 v101, v132, v133\n\t" \
        "s_waitcnt lgkmcnt(11)\n\t" \
        "v_mfma_f32_16x16x32_bf16 v[118:121], a[112:115], v[154:157], 0\n\t" \
        "buffer_load_dwordx4 v4, s[24:27], s57 offen offset:1024  lds\n\t" \
        "v_mfma_f32_16x16x32_bf16 v[122:125], a[116:119], v[154:157], 0\n\t" \
        "ds_read_b64_tr_b16 a[50:51], v6 offset:52224\n\t" \
        "ds_read_b64_tr_b16 a[52:53], v7 offset:49184\n\t" \
        "v_xor_b32 v144, v176, v11\n\t" \
        "v_xor_b32 v146, v174, v12\n\t" \
        "v_mfma_f32_16x16x32_bf16 v[126:129], a[120:123], v[158:161], 0\n\t" \
        "ds_read_b64 v[152:153], v10 offset:896\n\t" \
        "v_xor_b32 v145, v177, v11\n\t" \
        "v_xor_b32 v147, v175, v12\n\t" \
        "v_mfma_f32_16x16x32_bf16 v[130:133], a[124:127], v[158:161], 0\n\t" \
        "ds_read_b64_tr_b16 a[54:55], v7 offset:52256\n\t" \
        "ds_read_b64_tr_b16 a[56:57], v6 offset:49216\n\t" \
        "v_xor_b32 v148, v176, v12\n\t" \
        "v_mfma_f32_16x16x32_bf16 v[30:33], a[24:27], v[94:97], v[30:33]\n\t" \
        "ds_read_b64_tr_b16 a[58:59], v6 offset:52288\n\t" \
        "v_xor_b32 v149, v177, v12\n\t" \
        "v_mfma_f32_16x16x32_bf16 v[54:57], a[24:27], v[98:101], v[54:57]\n\t" \
        "ds_read_b64_tr_b16 a[60:61], v7 offset:49248\n\t" \
        "ds_read_b64_tr_b16 a[62:63], v7 offset:52320\n\t" \
        "v_mfma_f32_16x16x32_bf16 v[34:37], a[28:31], v[94:97], v[34:37]\n\t" \
        "buffer_load_dwordx4 v5, s[24:27], s57 offen offset:2048  lds\n\t" \
        "v_mfma_f32_16x16x32_bf16 v[58:61], a[28:31], v[98:101], v[58:61]\n\t" \
        "ds_read_b64_tr_b16 a[64:65], v6 offset:49280\n\t" \
        "v_cvt_pk_bf16_f32 v78, v102, v103\n\t" \
        "v_mfma_f32_16x16x32_bf16 v[38:41], a[32:35], v[94:97], v[38:41]\n\t" \
        "ds_read_b64_tr_b16 a[66:67], v6 offset:52352\n\t" \
        "v_cvt_pk_bf16_f32 v79, v104, v105\n\t" \
        "v_mfma_f32_16x16x32_bf16 v[62:65], a[32:35], v[98:101], v[62:65]\n\t" \
        "ds_read_b64_tr_b16 a[68:69], v7 offset:49312\n\t" \
        "ds_read_b64_tr_b16 a[70:71], v7 offset:52384\n\t" \
        "v_mfma_f32_16x16x32_bf16 v[42:45], a[36:39], v[94:97], v[42:45]\n\t" \
        "ds_read_b64_tr_b16 a[96:97], v142 offset:0\n\t" \
        "v_cvt_pk_bf16_f32 v80, v106, v107\n\t" \
        "v_mfma_f32_16x16x32_bf16 v[66:69], a[36:39], v[98:101], v[66:69]\n\t" \
        "ds_read_b64_tr_b16 a[98:99], v143 offset:0\n\t" \
        "ds_read_b64_tr_b16 a[100:101], v146 offset:0\n\t" \
        "v_mfma_f32_16x16x32_bf16 v[46:49], a[40:43], v[94:97], v[46:49]\n\t" \
        "ds_read_b64_tr_b16 a[102:103], v147 offset:0\n\t" \
        "v_cvt_pk_bf16_f32 v81, v108, v109\n\t" \
        "v_mfma_f32_16x16x32_bf16 v[70:73], a[40:43], v[98:101], v[70:73]\n\t" \
        "ds_read_b64_tr_b16 a[104:105], v144 offset:0\n\t" \
        "ds_read_b64_tr_b16 a[106:107], v145 offset:0\n\t" \
        "s_waitcnt lgkmcnt(12)\n\t" \
        "v_mfma_f32_16x16x32_bf16 v[50:53], a[44:47], v[94:97], v[50:53]\n\t" \
        "ds_read_b64_tr_b16 a[108:109], v148 offset:0\n\t" \
        "v_cvt_pk_bf16_f32 v82, v110, v111\n\t" \
        "v_mfma_f32_16x16x32_bf16 v[74:77], a[44:47], v[98:101], v[74:77]\n\t" \
        "ds_read_b64_tr_b16 a[110:111], v149 offset:0\n\t" \
        "ds_read_b64_tr_b16 a[72:73], v6 offset:55296\n\t" \
        "v_cvt_pk_bf16_f32 v83, v112, v113\n\t" \
        "v_cvt_pk_bf16_f32 v84, v114, v115\n\t" \
        "v_mfma_f32_16x16x32_bf16 v[102:105], a[128:131], v[154:157], 0\n\t" \
        "ds_read_b64_tr_b16 a[74:75], v6 offset:58368\n\t" \
        "v_cvt_pk_bf16_f32 v85, v116, v117\n\t" \
        "v_mfma_f32_16x16x32_bf16 v[106:109], a[132:135], v[154:157], 0\n\t" \
        "ds_read_b64_tr_b16 a[76:77], v7 offset:55328\n\t" \
        "v_cvt_pk_bf16_f32 v86, v118, v119\n\t" \
        "v_mfma_f32_16x16x32_bf16 v[110:113], a[136:139], v[158:161], 0\n\t" \
        "ds_read_b64_tr_b16 a[78:79], v7 offset:58400\n\t" \
        "ds_read_b64_tr_b16 a[80:81], v6 offset:55360\n\t" \
        "v_mfma_f32_16x16x32_bf16 v[114:117], a[140:143], v[158:161], 0\n\t" \
        "ds_read_b64_tr_b16 a[82:83], v6 offset:58432\n\t" \
        "v_cvt_pk_bf16_f32 v87, v120, v121\n\t" \
        "v_mfma_f32_16x16x32_bf16 v[30:33], a[48:51], v[78:81], v[30:33]\n\t" \
        "ds_read_b64_tr_b16 a[84:85], v7 offset:55392\n\t" \
        "ds_read_b64_tr_b16 a[86:87], v7 offset:58464\n\t" \
        "v_mfma_f32_16x16x32_bf16 v[54:57], a[48:51], v[82:85], v[54:57]\n\t" \
        "ds_read_b64_tr_b16 a[88:89], v6 offset:55424\n\t" \
        "v_and_b32 v162, v152, v25\n\t" \
        "v_mfma_f32_16x16x32_bf16 v[34:37], a[52:55], v[78:81], v[34:37]\n\t" \
        "ds_read_b128 v[170:173], v9 offset:1024\n\t" \
        "ds_read_b64_tr_b16 a[90:91], v6 offset:58496\n\t" \
        "v_mfma_f32_16x16x32_bf16 v[58:61], a[52:55], v[82:85], v[58:61]\n\t" \
        "s_waitcnt vmcnt(6)\n\t" \
        "s_barrier\n\t" \
        "ds_read_b64_tr_b16 a[92:93], v7 offset:55456\n\t" \
        "v_and_b32 v163, v152, v26\n\t" \
        "v_mfma_f32_16x16x32_bf16 v[38:41], a[56:59], v[78:81], v[38:41]\n\t" \
        "ds_read_b64_tr_b16 a[94:95], v7 offset:58528\n\t" \
        "v_and_b32 v164, v152, v27\n\t" \
        "v_and_b32 v165, v152, v28\n\t" \
        "v_mfma_f32_16x16x32_bf16 v[62:65], a[56:59], v[82:85], v[62:65]\n\t" \
        "ds_read_b64_tr_b16 a[112:113], v142 offset:64\n\t" \
        "ds_read_b64_tr_b16 a[114:115], v143 offset:64\n\t" \
        "v_and_b32 v166, v153, v25\n\t" \
        "s_waitcnt lgkmcnt(13)\n\t" \
        "v_mfma_f32_16x16x32_bf16 v[42:45], a[60:63], v[78:81], v[42:45]\n\t" \
        "s_add_u32 m0, s39, 36864\n\t" \
        "s_add_u32 s57, s40, 159744\n\t" \
        "buffer_load_dwordx4 v3, s[24:27], s57 offen offset:0  lds\n\t" \
        "v_mfma_f32_16x16x32_bf16 v[66:69], a[60:63], v[82:85], v[66:69]\n\t" \
        "ds_read_b64_tr_b16 a[116:117], v146 offset:64\n\t" \
        "v_cvt_pk_bf16_f32 v88, v122, v123\n\t" \
        "v_and_b32 v167, v153, v26\n\t" \
        "v_mfma_f32_16x16x32_bf16 v[46:49], a[64:67], v[78:81], v[46:49]\n\t" \
        "ds_read_b64_tr_b16 a[118:119], v147 offset:64\n\t" \
        "ds_read_b64_tr_b16 a[120:121], v144 offset:64\n\t" \
        "v_cvt_pk_bf16_f32 v89, v124, v125\n\t" \
        "v_cvt_pk_bf16_f32 v90, v126, v127\n\t" \
        "v_mfma_f32_16x16x32_bf16 v[70:73], a[64:67], v[82:85], v[70:73]\n\t" \
        "ds_read_b64_tr_b16 a[122:123], v145 offset:64\n\t" \
        "v_and_b32 v168, v153, v27\n\t" \
        "v_mfma_f32_16x16x32_bf16 v[50:53], a[68:71], v[78:81], v[50:53]\n\t" \
        "ds_read_b64_tr_b16 a[124:125], v148 offset:64\n\t" \
        "ds_read_b64_tr_b16 a[126:127], v149 offset:64\n\t" \
        "v_cvt_pk_bf16_f32 v91, v128, v129\n\t" \
        "v_cvt_pk_bf16_f32 v92, v130, v131\n\t" \
        "v_mfma_f32_16x16x32_bf16 v[74:77], a[68:71], v[82:85], v[74:77]\n\t" \
        "ds_read_b64_tr_b16 a[0:1], v6 offset:0\n\t" \
        "v_and_b32 v169, v153, v28\n\t" \
        "v_cvt_pk_bf16_f32 v93, v132, v133\n\t" \
        "v_mfma_f32_16x16x32_bf16 v[118:121], a[96:99], v[162:165], 0\n\t" \
        "buffer_load_dwordx4 v4, s[24:27], s57 offen offset:1024  lds\n\t" \
        "v_mfma_f32_16x16x32_bf16 v[122:125], a[100:103], v[162:165], 0\n\t" \
        "ds_read_b64_tr_b16 a[2:3], v6 offset:3072\n\t" \
        "v_cvt_pk_bf16_f32 v94, v102, v103\n\t" \
        "v_mfma_f32_16x16x32_bf16 v[126:129], a[104:107], v[166:169], 0\n\t" \
        "ds_read_b64_tr_b16 a[4:5], v7 offset:32\n\t" \
        "ds_read_b64_tr_b16 a[6:7], v7 offset:3104\n\t" \
        "v_mfma_f32_16x16x32_bf16 v[130:133], a[108:111], v[166:169], 0\n\t" \
        "ds_read_b64_tr_b16 a[8:9], v6 offset:64\n\t" \
        "v_cvt_pk_bf16_f32 v95, v104, v105\n\t" \
        "v_mfma_f32_16x16x32_bf16 v[30:33], a[72:75], v[86:89], v[30:33]\n\t" \
        "ds_read_b64_tr_b16 a[10:11], v6 offset:3136\n\t" \
        "v_cvt_pk_bf16_f32 v96, v106, v107\n\t" \
        "v_mfma_f32_16x16x32_bf16 v[54:57], a[72:75], v[90:93], v[54:57]\n\t" \
        "ds_read_b64_tr_b16 a[12:13], v7 offset:96\n\t" \
        "ds_read_b64_tr_b16 a[14:15], v7 offset:3168\n\t" \
        "s_waitcnt lgkmcnt(11)\n\t" \
        "v_mfma_f32_16x16x32_bf16 v[34:37], a[76:79], v[86:89], v[34:37]\n\t" \
        "buffer_load_dwordx4 v5, s[24:27], s57 offen offset:2048  lds\n\t" \
        "v_mfma_f32_16x16x32_bf16 v[58:61], a[76:79], v[90:93], v[58:61]\n\t" \
        "ds_read_b64_tr_b16 a[16:17], v6 offset:128\n\t" \
        "v_cvt_pk_bf16_f32 v97, v108, v109\n\t" \
        "v_mfma_f32_16x16x32_bf16 v[38:41], a[80:83], v[86:89], v[38:41]\n\t" \
        "ds_read_b64_tr_b16 a[18:19], v6 offset:3200\n\t" \
        "v_cvt_pk_bf16_f32 v98, v110, v111\n\t" \
        "v_mfma_f32_16x16x32_bf16 v[62:65], a[80:83], v[90:93], v[62:65]\n\t" \
        "ds_read_b64_tr_b16 a[20:21], v7 offset:160\n\t" \
        "ds_read_b64_tr_b16 a[22:23], v7 offset:3232\n\t" \
        "v_mfma_f32_16x16x32_bf16 v[42:45], a[84:87], v[86:89], v[42:45]\n\t" \
        "ds_read_b64_tr_b16 a[128:129], v142 offset:128\n\t" \
        "v_xor_b32 v134, v170, v11\n\t" \
        "v_mfma_f32_16x16x32_bf16 v[66:69], a[84:87], v[90:93], v[66:69]\n\t" \
        "ds_read_b64_tr_b16 a[130:131], v143 offset:128\n\t" \
        "v_cvt_pk_bf16_f32 v99, v112, v113\n\t" \
        "v_mfma_f32_16x16x32_bf16 v[46:49], a[88:91], v[86:89], v[46:49]\n\t" \
        "ds_read_b64_tr_b16 a[132:133], v146 offset:128\n\t" \
        "ds_read_b64_tr_b16 a[134:135], v147 offset:128\n\t" \
        "v_mfma_f32_16x16x32_bf16 v[70:73], a[88:91], v[90:93], v[70:73]\n\t" \
        "ds_read_b64_tr_b16 a[136:137], v144 offset:128\n\t" \
        "v_cvt_pk_bf16_f32 v100, v114, v115\n\t" \
        "v_mfma_f32_16x16x32_bf16 v[50:53], a[92:95], v[86:89], v[50:53]\n\t" \
        "ds_read_b64_tr_b16 a[138:139], v145 offset:128\n\t" \
        "v_xor_b32 v135, v171, v11\n\t" \
        "v_mfma_f32_16x16x32_bf16 v[74:77], a[92:95], v[90:93], v[74:77]\n\t" \
        "ds_read_b64_tr_b16 a[140:141], v148 offset:128\n\t" \
        "ds_read_b64_tr_b16 a[142:143], v149 offset:128\n\t" \
        "v_mfma_f32_16x16x32_bf16 v[102:105], a[112:115], v[162:165], 0\n\t" \
        "ds_read_b64_tr_b16 a[24:25], v6 offset:6144\n\t" \
        "v_cvt_pk_bf16_f32 v101, v116, v117\n\t" \
        "v_mfma_f32_16x16x32_bf16 v[106:109], a[116:119], v[162:165], 0\n\t" \
        "ds_read_b64_tr_b16 a[26:27], v6 offset:9216\n\t" \
        "v_xor_b32 v136, v172, v11\n\t" \
        "v_xor_b32 v138, v170, v12\n\t" \
        "s_waitcnt lgkmcnt(12)\n\t" \
        "v_mfma_f32_16x16x32_bf16 v[110:113], a[120:123], v[166:169], 0\n\t" \
        "ds_read_b64_tr_b16 a[28:29], v7 offset:6176\n\t" \
        "ds_read_b64 v[150:151], v10 offset:1024\n\t" \
        "v_xor_b32 v137, v173, v11\n\t" \
        "v_xor_b32 v139, v171, v12\n\t" \
        "v_mfma_f32_16x16x32_bf16 v[114:117], a[124:127], v[166:169], 0\n\t" \
        "ds_read_b64_tr_b16 a[30:31], v7 offset:9248\n\t" \
        "v_xor_b32 v140, v172, v12\n\t" \
        "v_mfma_f32_16x16x32_bf16 v[30:33], a[0:3], v[94:97], v[30:33]\n\t" \
        "ds_read_b64_tr_b16 a[32:33], v6 offset:6208\n\t" \
        "ds_read_b64_tr_b16 a[34:35], v6 offset:9280\n\t" \
        "v_xor_b32 v141, v173, v12\n\t" \
        "v_mfma_f32_16x16x32_bf16 v[54:57], a[0:3], v[98:101], v[54:57]\n\t" \
        "ds_read_b64_tr_b16 a[36:37], v7 offset:6240\n\t" \
        "v_cvt_pk_bf16_f32 v78, v118, v119\n\t" \
        "v_mfma_f32_16x16x32_bf16 v[34:37], a[4:7], v[94:97], v[34:37]\n\t" \
        "ds_read_b64_tr_b16 a[38:39], v7 offset:9312\n\t" \
        "v_cvt_pk_bf16_f32 v79, v120, v121\n\t" \
        "v_mfma_f32_16x16x32_bf16 v[58:61], a[4:7], v[98:101], v[58:61]\n\t" \
        "s_waitcnt vmcnt(6)\n\t" \
        "s_barrier\n\t" \
        "ds_read_b64_tr_b16 a[40:41], v6 offset:6272\n\t" \
        "ds_read_b64_tr_b16 a[42:43], v6 offset:9344\n\t" \
        "v_mfma_f32_16x16x32_bf16 v[38:41], a[8:11], v[94:97], v[38:41]\n\t" \
        "ds_read_b64_tr_b16 a[44:45], v7 offset:6304\n\t" \
        "v_cvt_pk_bf16_f32 v80, v122, v123\n\t" \
        "v_mfma_f32_16x16x32_bf16 v[62:65], a[8:11], v[98:101], v[62:65]\n\t" \
        "ds_read_b64_tr_b16 a[46:47], v7 offset:9376\n\t" \
        "v_cvt_pk_bf16_f32 v81, v124, v125\n\t" \
        "v_mfma_f32_16x16x32_bf16 v[42:45], a[12:15], v[94:97], v[42:45]\n\t" \
        "ds_read_b64_tr_b16 a[96:97], v134 offset:0\n\t" \
        "ds_read_b64_tr_b16 a[98:99], v135 offset:0\n\t" \
        "v_mfma_f32_16x16x32_bf16 v[66:69], a[12:15], v[98:101], v[66:69]\n\t" \
        "ds_read_b64_tr_b16 a[100:101], v138 offset:0\n\t" \
        "v_cvt_pk_bf16_f32 v82, v126, v127\n\t" \
        "v_mfma_f32_16x16x32_bf16 v[46:49], a[16:19], v[94:97], v[46:49]\n\t" \
        "ds_read_b64_tr_b16 a[102:103], v139 offset:0\n\t" \
        "ds_read_b64_tr_b16 a[104:105], v136 offset:0\n\t" \
        "v_mfma_f32_16x16x32_bf16 v[70:73], a[16:19], v[98:101], v[70:73]\n\t" \
        "ds_read_b64_tr_b16 a[106:107], v137 offset:0\n\t" \
        "v_cvt_pk_bf16_f32 v83, v128, v129\n\t" \
        "s_waitcnt lgkmcnt(12)\n\t" \
        "v_mfma_f32_16x16x32_bf16 v[50:53], a[20:23], v[94:97], v[50:53]\n\t" \
        "ds_read_b64_tr_b16 a[108:109], v140 offset:0\n\t" \
        "v_cvt_pk_bf16_f32 v84, v130, v131\n\t" \
        "v_mfma_f32_16x16x32_bf16 v[74:77], a[20:23], v[98:101], v[74:77]\n\t" \
        "ds_read_b64_tr_b16 a[110:111], v141 offset:0\n\t" \
        "ds_read_b64_tr_b16 a[48:49], v6 offset:12288\n\t" \
        "v_mfma_f32_16x16x32_bf16 v[118:121], a[128:131], v[162:165], 0\n\t" \
        "ds_read_b64_tr_b16 a[50:51], v6 offset:15360\n\t" \
        "v_cvt_pk_bf16_f32 v85, v132, v133\n\t" \
        "v_mfma_f32_16x16x32_bf16 v[122:125], a[132:135], v[162:165], 0\n\t" \
        "ds_read_b64_tr_b16 a[52:53], v7 offset:12320\n\t" \
        "ds_read_b64_tr_b16 a[54:55], v7 offset:15392\n\t" \
        "v_mfma_f32_16x16x32_bf16 v[126:129], a[136:139], v[166:169], 0\n\t" \
        "ds_read_b64_tr_b16 a[56:57], v6 offset:12352\n\t" \
        "v_cvt_pk_bf16_f32 v86, v102, v103\n\t" \
        "v_mfma_f32_16x16x32_bf16 v[130:133], a[140:143], v[166:169], 0\n\t" \
        "ds_read_b64_tr_b16 a[58:59], v6 offset:15424\n\t" \
        "ds_read_b64_tr_b16 a[60:61], v7 offset:12384\n\t" \
        "v_mfma_f32_16x16x32_bf16 v[30:33], a[24:27], v[78:81], v[30:33]\n\t" \
        "ds_read_b64_tr_b16 a[62:63], v7 offset:15456\n\t" \
        "v_and_b32 v154, v150, v25\n\t" \
        "v_mfma_f32_16x16x32_bf16 v[54:57], a[24:27], v[82:85], v[54:57]\n\t" \
        "ds_read_b64_tr_b16 a[64:65], v6 offset:12416\n\t" \
        "ds_read_b64_tr_b16 a[66:67], v6 offset:15488\n\t" \
        "v_mfma_f32_16x16x32_bf16 v[34:37], a[28:31], v[78:81], v[34:37]\n\t" \
        "ds_read_b64_tr_b16 a[68:69], v7 offset:12448\n\t" \
        "v_and_b32 v155, v150, v26\n\t" \
        "v_mfma_f32_16x16x32_bf16 v[58:61], a[28:31], v[82:85], v[58:61]\n\t" \
        "ds_read_b64_tr_b16 a[70:71], v7 offset:15520\n\t" \
        "ds_read_b64_tr_b16 a[112:113], v134 offset:64\n\t" \
        "v_mfma_f32_16x16x32_bf16 v[38:41], a[32:35], v[78:81], v[38:41]\n\t" \
        "ds_read_b64_tr_b16 a[114:115], v135 offset:64\n\t" \
        "v_and_b32 v156, v150, v27\n\t" \
        "v_mfma_f32_16x16x32_bf16 v[62:65], a[32:35], v[82:85], v[62:65]\n\t" \
        "ds_read_b64_tr_b16 a[116:117], v138 offset:64\n\t" \
        "ds_read_b64_tr_b16 a[118:119], v139 offset:64\n\t" \
        "s_waitcnt lgkmcnt(14)\n\t" \
        "v_mfma_f32_16x16x32_bf16 v[42:45], a[36:39], v[78:81], v[42:45]\n\t" \
        "ds_read_b64_tr_b16 a[120:121], v136 offset:64\n\t" \
        "v_and_b32 v157, v150, v28\n\t" \
        "v_and_b32 v158, v151, v25\n\t" \
        "v_mfma_f32_16x16x32_bf16 v[66:69], a[36:39], v[82:85], v[66:69]\n\t" \
        "ds_read_b64_tr_b16 a[122:123], v137 offset:64\n\t" \
        "ds_read_b64_tr_b16 a[124:125], v140 offset:64\n\t" \
        "v_cvt_pk_bf16_f32 v87, v104, v105\n\t" \
        "v_mfma_f32_16x16x32_bf16 v[46:49], a[40:43], v[78:81], v[46:49]\n\t" \
        "ds_read_b64_tr_b16 a[126:127], v141 offset:64\n\t" \
        "v_cvt_pk_bf16_f32 v88, v106, v107\n\t" \
        "v_and_b32 v159, v151, v26\n\t" \
        "v_mfma_f32_16x16x32_bf16 v[70:73], a[40:43], v[82:85], v[70:73]\n\t" \
        "ds_read_b64_tr_b16 a[72:73], v6 offset:18432\n\t" \
        "ds_read_b64_tr_b16 a[74:75], v6 offset:21504\n\t" \
        "v_cvt_pk_bf16_f32 v89, v108, v109\n\t" \
        "v_cvt_pk_bf16_f32 v90, v110, v111\n\t" \
        "v_mfma_f32_16x16x32_bf16 v[50:53], a[44:47], v[78:81], v[50:53]\n\t" \
        "ds_read_b64_tr_b16 a[76:77], v7 offset:18464\n\t" \
        "v_and_b32 v160, v151, v27\n\t" \
        "v_mfma_f32_16x16x32_bf16 v[74:77], a[44:47], v[82:85], v[74:77]\n\t" \
        "ds_read_b64_tr_b16 a[78:79], v7 offset:21536\n\t" \
        "ds_read_b64_tr_b16 a[80:81], v6 offset:18496\n\t" \
        "v_cvt_pk_bf16_f32 v91, v112, v113\n\t" \
        "v_cvt_pk_bf16_f32 v92, v114, v115\n\t" \
        "v_mfma_f32_16x16x32_bf16 v[102:105], a[96:99], v[154:157], 0\n\t" \
        "ds_read_b64_tr_b16 a[82:83], v6 offset:21568\n\t" \
        "v_and_b32 v161, v151, v28\n\t" \
        "v_cvt_pk_bf16_f32 v93, v116, v117\n\t" \
        "v_mfma_f32_16x16x32_bf16 v[106:109], a[100:103], v[154:157], 0\n\t" \
        "ds_read_b64_tr_b16 a[84:85], v7 offset:18528\n\t" \
        "ds_read_b64_tr_b16 a[86:87], v7 offset:21600\n\t" \
        "v_mfma_f32_16x16x32_bf16 v[110:113], a[104:107], v[158:161], 0\n\t" \
        "ds_read_b64_tr_b16 a[88:89], v6 offset:18560\n\t" \
        "v_cvt_pk_bf16_f32 v94, v118, v119\n\t" \
        "v_mfma_f32_16x16x32_bf16 v[114:117], a[108:111], v[158:161], 0\n\t" \
        "ds_read_b64_tr_b16 a[90:91], v6 offset:21632\n\t" \
        "ds_read_b64_tr_b16 a[92:93], v7 offset:18592\n\t" \
        "v_mfma_f32_16x16x32_bf16 v[30:33], a[48:51], v[86:89], v[30:33]\n\t" \
        "ds_read_b64_tr_b16 a[94:95], v7 offset:21664\n\t" \
        "v_cvt_pk_bf16_f32 v95, v120, v121\n\t" \
        "v_mfma_f32_16x16x32_bf16 v[54:57], a[48:51], v[90:93], v[54:57]\n\t" \
        "ds_read_b64_tr_b16 a[128:129], v134 offset:128\n\t" \
        "v_cvt_pk_bf16_f32 v96, v122, v123\n\t" \
        "s_waitcnt lgkmcnt(13)\n\t" \
        "v_mfma_f32_16x16x32_bf16 v[34:37], a[52:55], v[86:89], v[34:37]\n\t" \
        "ds_read_b64_tr_b16 a[130:131], v135 offset:128\n\t" \
        "ds_read_b64_tr_b16 a[132:133], v138 offset:128\n\t" \
        "v_mfma_f32_16x16x32_bf16 v[58:61], a[52:55], v[90:93], v[58:61]\n\t" \
        "s_waitcnt vmcnt(3)\n\t" \
        "s_barrier\n\t" \
        "ds_read_b64_tr_b16 a[134:135], v139 offset:128\n\t" \
        "v_cvt_pk_bf16_f32 v97, v124, v125\n\t" \
        "v_mfma_f32_16x16x32_bf16 v[38:41], a[56:59], v[86:89], v[38:41]\n\t" \
        "ds_read_b64_tr_b16 a[136:137], v136 offset:128\n\t" \
        "ds_read_b64_tr_b16 a[138:139], v137 offset:128\n\t" \
        "v_mfma_f32_16x16x32_bf16 v[62:65], a[56:59], v[90:93], v[62:65]\n\t" \
        "ds_read_b64_tr_b16 a[140:141], v140 offset:128\n\t" \
        "v_cvt_pk_bf16_f32 v98, v126, v127\n\t" \
        "v_mfma_f32_16x16x32_bf16 v[42:45], a[60:63], v[86:89], v[42:45]\n\t" \
        "ds_read_b64_tr_b16 a[142:143], v141 offset:128\n\t" \
        "ds_read_b64_tr_b16 a[0:1], v6 offset:24576\n\t" \
        "v_mfma_f32_16x16x32_bf16 v[66:69], a[60:63], v[90:93], v[66:69]\n\t" \
        "ds_read_b64_tr_b16 a[2:3], v6 offset:27648\n\t" \
        "v_cvt_pk_bf16_f32 v99, v128, v129\n\t" \
        "v_mfma_f32_16x16x32_bf16 v[46:49], a[64:67], v[86:89], v[46:49]\n\t" \
        "ds_read_b64_tr_b16 a[4:5], v7 offset:24608\n\t" \
        "ds_read_b64_tr_b16 a[6:7], v7 offset:27680\n\t" \
        "v_mfma_f32_16x16x32_bf16 v[70:73], a[64:67], v[90:93], v[70:73]\n\t" \
        "ds_read_b64_tr_b16 a[8:9], v6 offset:24640\n\t" \
        "v_cvt_pk_bf16_f32 v100, v130, v131\n\t" \
        "v_mfma_f32_16x16x32_bf16 v[50:53], a[68:71], v[86:89], v[50:53]\n\t" \
        "ds_read_b64_tr_b16 a[10:11], v6 offset:27712\n\t" \
        "ds_read_b64_tr_b16 a[12:13], v7 offset:24672\n\t" \
        "v_mfma_f32_16x16x32_bf16 v[74:77], a[68:71], v[90:93], v[74:77]\n\t" \
        "ds_read_b64_tr_b16 a[14:15], v7 offset:27744\n\t" \
        "v_cvt_pk_bf16_f32 v101, v132, v133\n\t" \
        "v_mfma_f32_16x16x32_bf16 v[118:121], a[112:115], v[154:157], 0\n\t" \
        "ds_read_b64_tr_b16 a[16:17], v6 offset:24704\n\t" \
        "ds_read_b64_tr_b16 a[18:19], v6 offset:27776\n\t" \
        "v_mfma_f32_16x16x32_bf16 v[122:125], a[116:119], v[154:157], 0\n\t" \
        "ds_read_b64_tr_b16 a[20:21], v7 offset:24736\n\t" \
        "v_cvt_pk_bf16_f32 v78, v102, v103\n\t" \
        "v_mfma_f32_16x16x32_bf16 v[126:129], a[120:123], v[158:161], 0\n\t" \
        "ds_read_b64_tr_b16 a[22:23], v7 offset:27808\n\t" \
        "ds_read_b64_tr_b16 a[24:25], v6 offset:30720\n\t" \
        "v_mfma_f32_16x16x32_bf16 v[130:133], a[124:127], v[158:161], 0\n\t" \
        "s_waitcnt vmcnt(0) lgkmcnt(15)\n\t" \
        "s_barrier\n\t" \
        "ds_read_b64_tr_b16 a[26:27], v6 offset:33792\n\t" \
        "v_cvt_pk_bf16_f32 v79, v104, v105\n\t" \
        "v_mfma_f32_16x16x32_bf16 v[30:33], a[72:75], v[94:97], v[30:33]\n\t" \
        "ds_read_b64_tr_b16 a[28:29], v7 offset:30752\n\t" \
        "ds_read_b64_tr_b16 a[30:31], v7 offset:33824\n\t" \
        "v_mfma_f32_16x16x32_bf16 v[54:57], a[72:75], v[98:101], v[54:57]\n\t" \
        "ds_read_b64_tr_b16 a[32:33], v6 offset:30784\n\t" \
        "v_cvt_pk_bf16_f32 v80, v106, v107\n\t" \
        "v_mfma_f32_16x16x32_bf16 v[34:37], a[76:79], v[94:97], v[34:37]\n\t" \
        "ds_read_b64_tr_b16 a[34:35], v6 offset:33856\n\t" \
        "ds_read_b64_tr_b16 a[36:37], v7 offset:30816\n\t" \
        "v_mfma_f32_16x16x32_bf16 v[58:61], a[76:79], v[98:101], v[58:61]\n\t" \
        "ds_read_b64_tr_b16 a[38:39], v7 offset:33888\n\t" \
        "v_cvt_pk_bf16_f32 v81, v108, v109\n\t" \
        "v_mfma_f32_16x16x32_bf16 v[38:41], a[80:83], v[94:97], v[38:41]\n\t" \
        "ds_read_b64_tr_b16 a[40:41], v6 offset:30848\n\t" \
        "ds_read_b64_tr_b16 a[42:43], v6 offset:33920\n\t" \
        "v_mfma_f32_16x16x32_bf16 v[62:65], a[80:83], v[98:101], v[62:65]\n\t" \
        "ds_read_b64_tr_b16 a[44:45], v7 offset:30880\n\t" \
        "v_cvt_pk_bf16_f32 v82, v110, v111\n\t" \
        "v_mfma_f32_16x16x32_bf16 v[42:45], a[84:87], v[94:97], v[42:45]\n\t" \
        "ds_read_b64_tr_b16 a[46:47], v7 offset:33952\n\t" \
        "ds_read_b64_tr_b16 a[48:49], v6 offset:36864\n\t" \
        "v_mfma_f32_16x16x32_bf16 v[66:69], a[84:87], v[98:101], v[66:69]\n\t" \
        "ds_read_b64_tr_b16 a[50:51], v6 offset:39936\n\t" \
        "v_cvt_pk_bf16_f32 v83, v112, v113\n\t" \
        "v_mfma_f32_16x16x32_bf16 v[46:49], a[88:91], v[94:97], v[46:49]\n\t" \
        "ds_read_b64_tr_b16 a[52:53], v7 offset:36896\n\t" \
        "ds_read_b64_tr_b16 a[54:55], v7 offset:39968\n\t" \
        "v_mfma_f32_16x16x32_bf16 v[70:73], a[88:91], v[98:101], v[70:73]\n\t" \
        "ds_read_b64_tr_b16 a[56:57], v6 offset:36928\n\t" \
        "v_cvt_pk_bf16_f32 v84, v114, v115\n\t" \
        "v_mfma_f32_16x16x32_bf16 v[50:53], a[92:95], v[94:97], v[50:53]\n\t" \
        "ds_read_b64_tr_b16 a[58:59], v6 offset:40000\n\t" \
        "v_cvt_pk_bf16_f32 v85, v116, v117\n\t" \
        "v_mfma_f32_16x16x32_bf16 v[74:77], a[92:95], v[98:101], v[74:77]\n\t" \
        "ds_read_b64_tr_b16 a[60:61], v7 offset:36960\n\t" \
        "ds_read_b64_tr_b16 a[62:63], v7 offset:40032\n\t" \
        "v_mfma_f32_16x16x32_bf16 v[102:105], a[128:131], v[154:157], 0\n\t" \
        "ds_read_b64_tr_b16 a[64:65], v6 offset:36992\n\t" \
        "ds_read_b64_tr_b16 a[66:67], v6 offset:40064\n\t" \
        "v_mfma_f32_16x16x32_bf16 v[106:109], a[132:135], v[154:157], 0\n\t" \
        "ds_read_b64_tr_b16 a[68:69], v7 offset:37024\n\t" \
        "v_cvt_pk_bf16_f32 v86, v118, v119\n\t" \
        "v_mfma_f32_16x16x32_bf16 v[110:113], a[136:139], v[158:161], 0\n\t" \
        "ds_read_b64_tr_b16 a[70:71], v7 offset:40096\n\t" \
        "v_cvt_pk_bf16_f32 v88, v122, v123\n\t" \
        "s_waitcnt lgkmcnt(13)\n\t" \
        "v_mfma_f32_16x16x32_bf16 v[114:117], a[140:143], v[158:161], 0\n\t" \
        "v_cvt_pk_bf16_f32 v87, v120, v121\n\t" \
        "v_cvt_pk_bf16_f32 v89, v124, v125\n\t" \
        "v_mfma_f32_16x16x32_bf16 v[30:33], a[0:3], v[78:81], v[30:33]\n\t" \
        "v_cvt_pk_bf16_f32 v94, v102, v103\n\t" \
        "v_cvt_pk_bf16_f32 v95, v104, v105\n\t" \
        "v_mfma_f32_16x16x32_bf16 v[34:37], a[4:7], v[78:81], v[34:37]\n\t" \
        "v_cvt_pk_bf16_f32 v96, v106, v107\n\t" \
        "v_cvt_pk_bf16_f32 v97, v108, v109\n\t" \
        "v_mfma_f32_16x16x32_bf16 v[38:41], a[8:11], v[78:81], v[38:41]\n\t" \
        "v_cvt_pk_bf16_f32 v90, v126, v127\n\t" \
        "v_cvt_pk_bf16_f32 v92, v130, v131\n\t" \
        "v_mfma_f32_16x16x32_bf16 v[42:45], a[12:15], v[78:81], v[42:45]\n\t" \
        "v_cvt_pk_bf16_f32 v91, v128, v129\n\t" \
        "v_cvt_pk_bf16_f32 v93, v132, v133\n\t" \
        "v_mfma_f32_16x16x32_bf16 v[46:49], a[16:19], v[78:81], v[46:49]\n\t" \
        "v_cvt_pk_bf16_f32 v98, v110, v111\n\t" \
        "v_cvt_pk_bf16_f32 v100, v114, v115\n\t" \
        "v_mfma_f32_16x16x32_bf16 v[50:53], a[20:23], v[78:81], v[50:53]\n\t" \
        "v_cvt_pk_bf16_f32 v99, v112, v113\n\t" \
        "v_cvt_pk_bf16_f32 v101, v116, v117\n\t" \
        "v_mfma_f32_16x16x32_bf16 v[30:33], a[24:27], v[86:89], v[30:33]\n\t" \
        "v_mfma_f32_16x16x32_bf16 v[34:37], a[28:31], v[86:89], v[34:37]\n\t" \
        "v_mfma_f32_16x16x32_bf16 v[38:41], a[32:35], v[86:89], v[38:41]\n\t" \
        "v_mfma_f32_16x16x32_bf16 v[42:45], a[36:39], v[86:89], v[42:45]\n\t" \
        "v_mfma_f32_16x16x32_bf16 v[46:49], a[40:43], v[86:89], v[46:49]\n\t" \
        "s_waitcnt lgkmcnt(0)\n\t" \
        "v_mfma_f32_16x16x32_bf16 v[50:53], a[44:47], v[86:89], v[50:53]\n\t" \
        "v_mfma_f32_16x16x32_bf16 v[30:33], a[48:51], v[94:97], v[30:33]\n\t" \
        "v_mfma_f32_16x16x32_bf16 v[34:37], a[52:55], v[94:97], v[34:37]\n\t" \
        "v_mfma_f32_16x16x32_bf16 v[38:41], a[56:59], v[94:97], v[38:41]\n\t" \
        "v_mfma_f32_16x16x32_bf16 v[42:45], a[60:63], v[94:97], v[42:45]\n\t" \
        "v_mfma_f32_16x16x32_bf16 v[46:49], a[64:67], v[94:97], v[46:49]\n\t" \
        "v_mfma_f32_16x16x32_bf16 v[50:53], a[68:71], v[94:97], v[50:53]\n\t" \
        "v_mfma_f32_16x16x32_bf16 v[54:57], a[0:3], v[82:85], v[54:57]\n\t" \
        "v_mfma_f32_16x16x32_bf16 v[58:61], a[4:7], v[82:85], v[58:61]\n\t" \
        "v_mfma_f32_16x16x32_bf16 v[62:65], a[8:11], v[82:85], v[62:65]\n\t" \
        "v_mfma_f32_16x16x32_bf16 v[66:69], a[12:15], v[82:85], v[66:69]\n\t" \
        "v_mfma_f32_16x16x32_bf16 v[70:73], a[16:19], v[82:85], v[70:73]\n\t" \
        "v_mfma_f32_16x16x32_bf16 v[74:77], a[20:23], v[82:85], v[74:77]\n\t" \
        "v_mfma_f32_16x16x32_bf16 v[54:57], a[24:27], v[90:93], v[54:57]\n\t" \
        "v_mfma_f32_16x16x32_bf16 v[58:61], a[28:31], v[90:93], v[58:61]\n\t" \
        "v_mfma_f32_16x16x32_bf16 v[62:65], a[32:35], v[90:93], v[62:65]\n\t" \
        "v_mfma_f32_16x16x32_bf16 v[66:69], a[36:39], v[90:93], v[66:69]\n\t" \
        "v_mfma_f32_16x16x32_bf16 v[70:73], a[40:43], v[90:93], v[70:73]\n\t" \
        "v_mfma_f32_16x16x32_bf16 v[74:77], a[44:47], v[90:93], v[74:77]\n\t" \
        "v_mfma_f32_16x16x32_bf16 v[54:57], a[48:51], v[98:101], v[54:57]\n\t" \
        "v_mfma_f32_16x16x32_bf16 v[58:61], a[52:55], v[98:101], v[58:61]\n\t" \
        "v_mfma_f32_16x16x32_bf16 v[62:65], a[56:59], v[98:101], v[62:65]\n\t" \
        "v_mfma_f32_16x16x32_bf16 v[66:69], a[60:63], v[98:101], v[66:69]\n\t" \
        "v_mfma_f32_16x16x32_bf16 v[70:73], a[64:67], v[98:101], v[70:73]\n\t" \
        "v_mfma_f32_16x16x32_bf16 v[74:77], a[68:71], v[98:101], v[74:77]\n\t" \
        "s_nop 4\n\t" \
        "v_add_f32 v30, v30, v54\n\t" \
        "v_add_f32 v31, v31, v55\n\t" \
        "v_add_f32 v32, v32, v56\n\t" \
        "v_add_f32 v33, v33, v57\n\t" \
        "v_add_f32 v34, v34, v58\n\t" \
        "v_add_f32 v35, v35, v59\n\t" \
        "v_add_f32 v36, v36, v60\n\t" \
        "v_add_f32 v37, v37, v61\n\t" \
        "v_add_f32 v38, v38, v62\n\t" \
        "v_add_f32 v39, v39, v63\n\t" \
        "v_add_f32 v40, v40, v64\n\t" \
        "v_add_f32 v41, v41, v65\n\t" \
        "v_add_f32 v42, v42, v66\n\t" \
        "v_add_f32 v43, v43, v67\n\t" \
        "v_add_f32 v44, v44, v68\n\t" \
        "v_add_f32 v45, v45, v69\n\t" \
        "v_add_f32 v46, v46, v70\n\t" \
        "v_add_f32 v47, v47, v71\n\t" \
        "v_add_f32 v48, v48, v72\n\t" \
        "v_add_f32 v49, v49, v73\n\t" \
        "v_add_f32 v50, v50, v74\n\t" \
        "v_add_f32 v51, v51, v75\n\t" \
        "v_add_f32 v52, v52, v76\n\t" \
        "v_add_f32 v53, v53, v77\n\t" \
        "v_cvt_pk_bf16_f32 v30, v30, v31\n\t" \
        "v_cvt_pk_bf16_f32 v31, v32, v33\n\t" \
        "v_cvt_pk_bf16_f32 v32, v34, v35\n\t" \
        "v_cvt_pk_bf16_f32 v33, v36, v37\n\t" \
        "s_nop 0\n\t" \
        "v_permlane16_swap_b32 v30, v32\n\t" \
        "v_permlane16_swap_b32 v31, v33\n\t" \
        "buffer_store_dwordx4 v[30:33], v8, s[28:31], s59 offen offset:0 sc1\n\t" \
        "v_cvt_pk_bf16_f32 v38, v38, v39\n\t" \
        "v_cvt_pk_bf16_f32 v39, v40, v41\n\t" \
        "v_cvt_pk_bf16_f32 v40, v42, v43\n\t" \
        "v_cvt_pk_bf16_f32 v41, v44, v45\n\t" \
        "s_nop 0\n\t" \
        "v_permlane16_swap_b32 v38, v40\n\t" \
        "v_permlane16_swap_b32 v39, v41\n\t" \
        "buffer_store_dwordx4 v[38:41], v8, s[28:31], s59 offen offset:64 sc1\n\t" \
        "v_cvt_pk_bf16_f32 v46, v46, v47\n\t" \
        "v_cvt_pk_bf16_f32 v47, v48, v49\n\t" \
        "v_cvt_pk_bf16_f32 v48, v50, v51\n\t" \
        "v_cvt_pk_bf16_f32 v49, v52, v53\n\t" \
        "s_nop 0\n\t" \
        "v_permlane16_swap_b32 v46, v48\n\t" \
        "v_permlane16_swap_b32 v47, v49\n\t" \
        "buffer_store_dwordx4 v[46:49], v8, s[28:31], s59 offen offset:128 sc1\n\t" \
        "s_endpgm\n\t" \
        ".LTeprod%=:\n\t" \
        "s_waitcnt lgkmcnt(10)\n\t" \
        "v_mfma_f32_16x16x32_bf16 v[88:91], v[64:67], v[112:115], 0\n\t" \
        "v_lshlrev_b32 v116, 5, v120\n\t" \
        "v_bitop3_b32 v116, v116, s52, v23 bitop3:0x6a\n\t" \
        "v_xor_b32 v117, 8, v116\n\t" \
        "v_lshlrev_b32 v118, 5, v121\n\t" \
        "v_bitop3_b32 v118, v118, s52, v23 bitop3:0x6a\n\t" \
        "v_xor_b32 v119, 8, v118\n\t" \
        "ds_read_u16 v120, v20 offset:128\n\t" \
        "ds_read_u16 v121, v20 offset:136\n\t" \
        "ds_read_b64_tr_b16 v[64:65], v116 offset:0\n\t" \
        "ds_read_b64_tr_b16 v[66:67], v118 offset:0\n\t" \
        "s_waitcnt lgkmcnt(12)\n\t" \
        "v_mfma_f32_16x16x32_bf16 v[92:95], v[68:71], v[112:115], 0\n\t" \
        "ds_read_b64_tr_b16 v[68:69], v117 offset:0\n\t" \
        "ds_read_b64_tr_b16 v[70:71], v119 offset:0\n\t" \
        "s_waitcnt lgkmcnt(12)\n\t" \
        "v_mfma_f32_16x16x32_bf16 v[96:99], v[72:75], v[112:115], 0\n\t" \
        "ds_read_b64_tr_b16 v[72:73], v116 offset:64\n\t" \
        "ds_read_b64_tr_b16 v[74:75], v118 offset:64\n\t" \
        "s_waitcnt lgkmcnt(12)\n\t" \
        "v_mfma_f32_16x16x32_bf16 v[100:103], v[76:79], v[112:115], 0\n\t" \
        "ds_read_b64_tr_b16 v[76:77], v117 offset:64\n\t" \
        "ds_read_b64_tr_b16 v[78:79], v119 offset:64\n\t" \
        "s_waitcnt lgkmcnt(12)\n\t" \
        "v_mfma_f32_16x16x32_bf16 v[104:107], v[80:83], v[112:115], 0\n\t" \
        "ds_read_b64_tr_b16 v[80:81], v116 offset:128\n\t" \
        "ds_read_b64_tr_b16 v[82:83], v118 offset:128\n\t" \
        "s_waitcnt lgkmcnt(12)\n\t" \
        "v_mfma_f32_16x16x32_bf16 v[108:111], v[84:87], v[112:115], 0\n\t" \
        "ds_read_b64_tr_b16 v[84:85], v117 offset:128\n\t" \
        "ds_read_b64_tr_b16 v[86:87], v119 offset:128\n\t" \
        "v_and_b32 v112, v122, v25\n\t" \
        "v_and_b32 v113, v122, v26\n\t" \
        "v_and_b32 v114, v122, v27\n\t" \
        "v_and_b32 v115, v122, v28\n\t" \
        "ds_read_b32 v122, v21 offset:256\n\t" \
        "s_and_b32 s55, s50, 0xffff\n\t" \
        "s_cbranch_scc1 .LTetwo1%=\n\t" \
        ".LTeback1%=:\n\t" \
        "s_sub_u32 s55, s46, s47\n\t" \
        "v_xor_b32 v156, 8, v22\n\t" \
        "v_cmp_gt_i32 vcc, s55, v24\n\t" \
        "s_mov_b64 exec, vcc\n\t" \
        "v_cvt_pk_bf16_f32 v88, v88, v89\n\t" \
        "v_cvt_pk_bf16_f32 v89, v90, v91\n\t" \
        "ds_write_b64 v22, v[88:89] offset:0\n\t" \
        "v_cvt_pk_bf16_f32 v92, v92, v93\n\t" \
        "v_cvt_pk_bf16_f32 v93, v94, v95\n\t" \
        "ds_write_b64 v156, v[92:93] offset:0\n\t" \
        "v_cvt_pk_bf16_f32 v96, v96, v97\n\t" \
        "v_cvt_pk_bf16_f32 v97, v98, v99\n\t" \
        "ds_write_b64 v22, v[96:97] offset:64\n\t" \
        "v_cvt_pk_bf16_f32 v100, v100, v101\n\t" \
        "v_cvt_pk_bf16_f32 v101, v102, v103\n\t" \
        "ds_write_b64 v156, v[100:101] offset:64\n\t" \
        "v_cvt_pk_bf16_f32 v104, v104, v105\n\t" \
        "v_cvt_pk_bf16_f32 v105, v106, v107\n\t" \
        "ds_write_b64 v22, v[104:105] offset:128\n\t" \
        "v_cvt_pk_bf16_f32 v108, v108, v109\n\t" \
        "v_cvt_pk_bf16_f32 v109, v110, v111\n\t" \
        "ds_write_b64 v156, v[108:109] offset:128\n\t" \
        "s_mov_b64 exec, -1\n\t" \
        "s_lshl_b32 s55, s53, 4\n\t" \
        "s_add_u32 s47, s47, 16\n\t" \
        "v_add_u32 v22, s55, v22\n\t" \
        "s_lshr_b64 s[50:51], s[50:51], 16\n\t" \
        "v_add_u32 v20, 64, v20\n\t" \
        "v_add_u32 v21, 0x80, v21\n\t" \
        "s_add_u32 s55, s47, 16\n\t" \
        "s_cmp_lt_u32 s55, s46\n\t" \
        "s_cbranch_scc1 .LTeprod%=\n\t" \
        "s_branch .LTelast%=\n\t" \
        ".LTetwo1%=:\n\t" \
        "s_nop 4\n\t" \
        "ds_read_u16 v156, v20\n\t" \
        "ds_read_u16 v158, v20 offset:8\n\t" \
        "ds_read_b32 v127, v21 offset:4\n\t" \
        "s_waitcnt lgkmcnt(0)\n\t" \
        "v_lshrrev_b32 v124, 15, v156\n\t" \
        "v_lshlrev_b32 v156, 5, v156\n\t" \
        "v_bitop3_b32 v156, v156, s52, v23 bitop3:0x6a\n\t" \
        "v_mul_u32_u24 v124, 3968, v124\n\t" \
        "v_add_u32 v156, v156, v124\n\t" \
        "v_xor_b32 v157, 8, v156\n\t" \
        "v_lshrrev_b32 v125, 15, v158\n\t" \
        "v_lshlrev_b32 v158, 5, v158\n\t" \
        "v_bitop3_b32 v158, v158, s52, v23 bitop3:0x6a\n\t" \
        "v_mul_u32_u24 v125, 3968, v125\n\t" \
        "v_add_u32 v158, v158, v125\n\t" \
        "v_xor_b32 v159, 8, v158\n\t" \
        "ds_read_b64_tr_b16 v[128:129], v156 offset:0\n\t" \
        "ds_read_b64_tr_b16 v[130:131], v158 offset:0\n\t" \
        "ds_read_b64_tr_b16 v[132:133], v157 offset:0\n\t" \
        "ds_read_b64_tr_b16 v[134:135], v159 offset:0\n\t" \
        "ds_read_b64_tr_b16 v[136:137], v156 offset:64\n\t" \
        "ds_read_b64_tr_b16 v[138:139], v158 offset:64\n\t" \
        "ds_read_b64_tr_b16 v[140:141], v157 offset:64\n\t" \
        "ds_read_b64_tr_b16 v[142:143], v159 offset:64\n\t" \
        "ds_read_b64_tr_b16 v[144:145], v156 offset:128\n\t" \
        "ds_read_b64_tr_b16 v[146:147], v158 offset:128\n\t" \
        "ds_read_b64_tr_b16 v[148:149], v157 offset:128\n\t" \
        "ds_read_b64_tr_b16 v[150:151], v159 offset:128\n\t" \
        "v_and_b32 v124, v127, v25\n\t" \
        "v_and_b32 v125, v127, v26\n\t" \
        "v_and_b32 v126, v127, v27\n\t" \
        "v_and_b32 v127, v127, v28\n\t" \
        "s_nop 1\n\t" \
        "s_waitcnt lgkmcnt(10)\n\t" \
        "v_mfma_f32_16x16x32_bf16 v[88:91], v[128:131], v[124:127], v[88:91]\n\t" \
        "s_waitcnt lgkmcnt(8)\n\t" \
        "v_mfma_f32_16x16x32_bf16 v[92:95], v[132:135], v[124:127], v[92:95]\n\t" \
        "s_waitcnt lgkmcnt(6)\n\t" \
        "v_mfma_f32_16x16x32_bf16 v[96:99], v[136:139], v[124:127], v[96:99]\n\t" \
        "s_waitcnt lgkmcnt(4)\n\t" \
        "v_mfma_f32_16x16x32_bf16 v[100:103], v[140:143], v[124:127], v[100:103]\n\t" \
        "s_waitcnt lgkmcnt(2)\n\t" \
        "v_mfma_f32_16x16x32_bf16 v[104:107], v[144:147], v[124:127], v[104:107]\n\t" \
        "s_waitcnt lgkmcnt(0)\n\t" \
        "v_mfma_f32_16x16x32_bf16 v[108:111], v[148:151], v[124:127], v[108:111]\n\t" \
        "s_nop 7\n\t" \
        "s_branch .LTeback1%=\n\t" \
        ".LTetwo2%=:\n\t" \
        "s_nop 4\n\t" \
        "ds_read_u16 v156, v20\n\t" \
        "ds_read_u16 v158, v20 offset:8\n\t" \
        "ds_read_b32 v127, v21 offset:4\n\t" \
        "s_waitcnt lgkmcnt(0)\n\t" \
        "v_lshrrev_b32 v124, 15, v156\n\t" \
        "v_lshlrev_b32 v156, 5, v156\n\t" \
        "v_bitop3_b32 v156, v156, s52, v23 bitop3:0x6a\n\t" \
        "v_mul_u32_u24 v124, 3968, v124\n\t" \
        "v_add_u32 v156, v156, v124\n\t" \
        "v_xor_b32 v157, 8, v156\n\t" \
        "v_lshrrev_b32 v125, 15, v158\n\t" \
        "v_lshlrev_b32 v158, 5, v158\n\t" \
        "v_bitop3_b32 v158, v158, s52, v23 bitop3:0x6a\n\t" \
        "v_mul_u32_u24 v125, 3968, v125\n\t" \
        "v_add_u32 v158, v158, v125\n\t" \
        "v_xor_b32 v159, 8, v158\n\t" \
        "ds_read_b64_tr_b16 v[128:129], v156 offset:0\n\t" \
        "ds_read_b64_tr_b16 v[130:131], v158 offset:0\n\t" \
        "ds_read_b64_tr_b16 v[132:133], v157 offset:0\n\t" \
        "ds_read_b64_tr_b16 v[134:135], v159 offset:0\n\t" \
        "ds_read_b64_tr_b16 v[136:137], v156 offset:64\n\t" \
        "ds_read_b64_tr_b16 v[138:139], v158 offset:64\n\t" \
        "ds_read_b64_tr_b16 v[140:141], v157 offset:64\n\t" \
        "ds_read_b64_tr_b16 v[142:143], v159 offset:64\n\t" \
        "ds_read_b64_tr_b16 v[144:145], v156 offset:128\n\t" \
        "ds_read_b64_tr_b16 v[146:147], v158 offset:128\n\t" \
        "ds_read_b64_tr_b16 v[148:149], v157 offset:128\n\t" \
        "ds_read_b64_tr_b16 v[150:151], v159 offset:128\n\t" \
        "v_and_b32 v124, v127, v25\n\t" \
        "v_and_b32 v125, v127, v26\n\t" \
        "v_and_b32 v126, v127, v27\n\t" \
        "v_and_b32 v127, v127, v28\n\t" \
        "s_nop 1\n\t" \
        "s_waitcnt lgkmcnt(10)\n\t" \
        "v_mfma_f32_16x16x32_bf16 v[88:91], v[128:131], v[124:127], v[88:91]\n\t" \
        "s_waitcnt lgkmcnt(8)\n\t" \
        "v_mfma_f32_16x16x32_bf16 v[92:95], v[132:135], v[124:127], v[92:95]\n\t" \
        "s_waitcnt lgkmcnt(6)\n\t" \
        "v_mfma_f32_16x16x32_bf16 v[96:99], v[136:139], v[124:127], v[96:99]\n\t" \
        "s_waitcnt lgkmcnt(4)\n\t" \
        "v_mfma_f32_16x16x32_bf16 v[100:103], v[140:143], v[124:127], v[100:103]\n\t" \
        "s_waitcnt lgkmcnt(2)\n\t" \
        "v_mfma_f32_16x16x32_bf16 v[104:107], v[144:147], v[124:127], v[104:107]\n\t" \
        "s_waitcnt lgkmcnt(0)\n\t" \
        "v_mfma_f32_16x16x32_bf16 v[108:111], v[148:151], v[124:127], v[108:111]\n\t" \
        "s_nop 7\n\t" \
        "s_branch .LTeback2%=\n\t" \
        ".LT_again%=:\n\t" \
        "s_waitcnt vmcnt(0) lgkmcnt(0)\n\t" \
        "s_barrier\n\t" \
        "s_add_u32 s32, s32, s3\n\t" \
        "s_mov_b32 s3, 0\n\t" \
        "s_mov_b32 s11, 3\n\t" \
        "s_branch .LT_entry%=\n\t" \
        ".LR_abort%=:\n\t" \
        "s_waitcnt vmcnt(0) lgkmcnt(0)\n\t" \
        "s_barrier\n\t" \
        "s_nop 7\n\t" \
        "s_nop 7\n\t" \
        "s_sub_u32 s3, s34, 12\n\t" \
        "s_mov_b32 s34, 12\n\t" \
        "s_mov_b32 s11, 3\n\t" \
        "s_branch .LT_entry%=\n\t" \
        ".LH_ent%=:\n\t" \
        "s_nop 4\n\t" \
        "v_bfe_u32 v162, v2, 2, 1\n\t" \
        "v_lshrrev_b32 v163, 3, v2\n\t" \
        "s_mul_i32 s55, s35, 2560\n\t" \
        "s_add_u32 s55, s55, 109120\n\t" \
        "v_mul_u32_u24 v162, 0x500, v162\n\t" \
        "v_lshlrev_b32 v163, 4, v163\n\t" \
        "v_add3_u32 v9, v162, v163, s55\n\t" \
        "v_lshrrev_b32 v164, 4, v2\n\t" \
        "v_bfe_u32 v162, v2, 2, 2\n\t" \
        "v_and_b32 v163, 3, v2\n\t" \
        "v_lshl_add_u32 v162, v164, 2, v162\n\t" \
        "v_lshlrev_b32 v11, 3, v163\n\t" \
        "v_and_b32 v165, 1, v164\n\t" \
        "v_mul_u32_u24 v162, 0xc0, v162\n\t" \
        "v_xor_b32 v12, 32, v11\n\t" \
        "v_lshl_add_u32 v162, v165, 5, v162\n\t" \
        "v_lshlrev_b32 v163, 6, v165\n\t" \
        "s_mov_b32 s55, 0xba40\n\t" \
        "v_add3_u32 v6, v162, v11, s55\n\t" \
        "v_sub_u32 v7, v6, v163\n\t" \
        "v_mul_u32_u24 v162, 0xc0, v19\n\t" \
        "v_lshrrev_b32 v164, 1, v164\n\t" \
        "v_lshl_add_u32 v162, v165, 5, v162\n\t" \
        "v_lshl_add_u32 v8, v164, 4, v162\n\t" \
        "s_add_u32 s55, s32, s35\n\t" \
        "s_mul_i32 s56, s33, 0xc0\n\t" \
        "s_mul_i32 s55, s55, 0xa800\n\t" \
        "s_add_u32 s59, s55, s56\n\t" \
        "s_add_u32 s60, s59, 0x2a000\n\t" \
        "v_lshrrev_b32 v166, 4, v2\n\t" \
        "v_bfe_u32 v168, v2, 2, 2\n\t" \
        "v_and_b32 v167, 3, v2\n\t" \
        "v_cmp_eq_u32 vcc, v166, v168\n\t" \
        "v_lshlrev_b32 v167, v167, 1\n\t" \
        "s_nop 0\n\t" \
        "v_cndmask_b32 v167, 0, v167, vcc\n\t" \
        "v_bfe_i32 v25, v167, 0, 1\n\t" \
        "v_bfe_i32 v26, v167, 1, 1\n\t" \
        "v_bfe_i32 v27, v167, 2, 1\n\t" \
        "v_bfe_i32 v28, v167, 3, 1\n\t" \
        "s_lshl_b32 s48, s35, 10\n\t" \
        "s_and_b32 s55, s35, 1\n\t" \
        "s_add_u32 s48, s48, 0x1e640\n\t" \
        "s_mul_i32 s55, s55, 0x2a00\n\t" \
        "s_cmp_lt_u32 s35, 2\n\t" \
        "s_cselect_b32 s49, 0, 0x2940\n\t" \
        "s_cselect_b32 s53, 1, -1\n\t" \
        "s_add_u32 s49, s49, s55\n\t" \
        "s_mul_i32 s53, s53, 192\n\t" \
        "s_mov_b32 s52, 0x3ffe0\n\t" \
        "s_add_u32 s49, s49, 0x1f640\n\t" \
        "v_lshrrev_b32 v166, 4, v2\n\t" \
        "v_and_b32 v24, 15, v2\n\t" \
        "v_bfe_u32 v167, v2, 3, 1\n\t" \
        "v_lshl_add_u32 v167, v166, 2, v167\n\t" \
        "v_lshrrev_b32 v168, 1, v2\n\t" \
        "v_and_b32 v168, 2, v168\n\t" \
        "v_lshl_add_u32 v167, v167, 2, v168\n\t" \
        "s_add_u32 s55, s48, 512\n\t" \
        "v_add_u32 v20, s55, v167\n\t" \
        "v_lshl_add_u32 v21, v24, 3, s48\n\t" \
        "v_bfe_u32 v167, v2, 1, 1\n\t" \
        "v_xor_b32 v167, v167, v166\n\t" \
        "v_lshrrev_b32 v168, 2, v24\n\t" \
        "v_and_b32 v169, 2, v168\n\t" \
        "v_xor_b32 v167, v167, v169\n\t" \
        "v_and_b32 v169, 4, v2\n\t" \
        "v_lshlrev_b32 v167, 4, v167\n\t" \
        "v_lshl_add_u32 v167, v169, 1, v167\n\t" \
        "v_mul_i32_i24 v22, s53, v24\n\t" \
        "v_add3_u32 v22, v22, v167, s49\n\t" \
        "v_and_b32 v167, 3, v2\n\t" \
        "v_and_b32 v169, 1, v166\n\t" \
        "v_lshlrev_b32 v169, 3, v169\n\t" \
        "v_lshl_or_b32 v23, v167, 4, v169\n\t" \
        "s_mov_b32 s45, s48\n\t" \
        "v_mbcnt_lo_u32_b32 v140, s4, 0\n\t" \
        "v_mbcnt_hi_u32_b32 v140, s5, v140\n\t" \
        "s_bcnt1_i32_b64 s55, s[4:5]\n\t" \
        "v_add_u32 v141, 0, v63\n\t" \
        "v_lshl_add_u32 v140, v140, 3, s45\n\t" \
        "s_lshl3_add_u32 s45, s55, s45\n\t" \
        "s_mov_b64 exec, s[4:5]\n\t" \
        "ds_write2_b32 v140, v50, v141 offset1:1\n\t" \
        "s_mov_b64 exec, -1\n\t" \
        "v_mbcnt_lo_u32_b32 v140, s6, 0\n\t" \
        "v_mbcnt_hi_u32_b32 v140, s7, v140\n\t" \
        "s_bcnt1_i32_b64 s55, s[6:7]\n\t" \
        "v_add_u32 v141, 256, v63\n\t" \
        "v_lshl_add_u32 v140, v140, 3, s45\n\t" \
        "s_lshl3_add_u32 s45, s55, s45\n\t" \
        "s_mov_b64 exec, s[6:7]\n\t" \
        "ds_write2_b32 v140, v51, v141 offset1:1\n\t" \
        "s_mov_b64 exec, -1\n\t" \
        "v_mbcnt_lo_u32_b32 v140, s8, 0\n\t" \
        "v_mbcnt_hi_u32_b32 v140, s9, v140\n\t" \
        "s_bcnt1_i32_b64 s55, s[8:9]\n\t" \
        "v_add_u32 v141, 512, v63\n\t" \
        "v_lshl_add_u32 v140, v140, 3, s45\n\t" \
        "s_lshl3_add_u32 s45, s55, s45\n\t" \
        "s_mov_b64 exec, s[8:9]\n\t" \
        "ds_write2_b32 v140, v52, v141 offset1:1\n\t" \
        "s_mov_b64 exec, -1\n\t" \
        "v_mbcnt_lo_u32_b32 v140, s12, 0\n\t" \
        "v_mbcnt_hi_u32_b32 v140, s13, v140\n\t" \
        "s_bcnt1_i32_b64 s55, s[12:13]\n\t" \
        "v_add_u32 v141, 768, v63\n\t" \
        "v_lshl_add_u32 v140, v140, 3, s45\n\t" \
        "s_lshl3_add_u32 s45, s55, s45\n\t" \
        "s_mov_b64 exec, s[12:13]\n\t" \
        "ds_write2_b32 v140, v53, v141 offset1:1\n\t" \
        "s_mov_b64 exec, -1\n\t" \
        "v_mbcnt_lo_u32_b32 v140, s14, 0\n\t" \
        "v_mbcnt_hi_u32_b32 v140, s15, v140\n\t" \
        "s_bcnt1_i32_b64 s55, s[14:15]\n\t" \
        "v_add_u32 v141, 1024, v63\n\t" \
        "v_lshl_add_u32 v140, v140, 3, s45\n\t" \
        "s_lshl3_add_u32 s45, s55, s45\n\t" \
        "s_mov_b64 exec, s[14:15]\n\t" \
        "ds_write2_b32 v140, v54, v141 offset1:1\n\t" \
        "s_mov_b64 exec, -1\n\t" \
        "s_sub_u32 s46, s45, s48\n\t" \
        "s_lshr_b32 s46, s46, 3\n\t" \
        "s_cmp_gt_u32 s46, 64\n\t" \
        "s_cselect_b32 s54, 1, s54\n\t" \
        "s_cselect_b32 s46, 64, s46\n\t" \
        "v_mul_i32_i24 v30, s53, v2\n\t" \
        "v_lshlrev_b32 v166, 3, v2\n\t" \
        "v_lshlrev_b32 v167, 2, v2\n\t" \
        "v_add_u32 v30, s49, v30\n\t" \
        "v_and_b32 v166, 16, v166\n\t" \
        "v_and_b32 v167, 32, v167\n\t" \
        "v_lshl_add_u32 v31, v2, 3, s48\n\t" \
        "v_or3_b32 v30, v30, v166, v167\n\t" \
        "v_lshlrev_b32 v166, 2, v2\n\t" \
        "s_add_u32 s55, s48, 512\n\t" \
        "v_add_u32 v32, s55, v166\n\t" \
        "v_cvt_f32_u32 v34, s32\n\t" \
        "v_cvt_f32_u32 v35, s33\n\t" \
        "v_mov_b32 v36, 124\n\t" \
        "v_add_f32 v34, -2.0, v34\n\t" \
        "v_add_f32 v35, -2.0, v35\n\t" \
        "v_cvt_f32_u32 v33, 11\n\t" \
        "v_mov_b32 v37, 0x05040100\n\t" \
        "v_mov_b32 v38, 0x80008000\n\t" \
        "v_mov_b32 v39, 0x435f435f\n\t" \
        "ds_read_b64 v[68:69], v31\n\t" \
        "v_cmp_gt_u32 s[12:13], s46, v2\n\t" \
        "s_mov_b32 s14, 0x2\n\t" \
        "s_mov_b32 s15, 0x8\n\t" \
        "v_mov_b32 v83, 0x41980000\n\t" \
        "v_mov_b32 v29, 0x20002\n\t" \
        "s_waitcnt lgkmcnt(0)\n\t" \
        "v_lshlrev_b32 v70, 16, v68\n\t" \
        "v_and_b32 v71, 0xffff0000, v68\n\t" \
        "s_mov_b64 exec, s[12:13]\n\t" \
        "ds_write_b32 v69, v30\n\t" \
        "ds_write_b32 v69, v30 offset:1280\n\t" \
        "s_mov_b64 exec, -1\n\t" \
        "v_fract_f32 v72, v70\n\t" \
        "v_fract_f32 v73, v71\n\t" \
        "v_sub_f32 v70, v70, v34\n\t" \
        "v_sub_f32 v71, v71, v35\n\t" \
        "v_sub_f32 v74, 1.0, v72\n\t" \
        "v_sub_f32 v75, 1.0, v73\n\t" \
        "v_med3_f32 v78, v70, 0, v33\n\t" \
        "v_cvt_pk_bf16_f32 v74, v74, v75\n\t" \
        "v_med3_f32 v79, v71, 0, v83\n\t" \
        "v_cmp_neq_f32 s[4:5], v78, v70\n\t" \
        "v_and_b32 v75, 0xffff0000, v74\n\t" \
        "v_cmp_neq_f32 s[6:7], v79, v71\n\t" \
        "v_lshlrev_b32 v74, 16, v74\n\t" \
        "v_cvt_u32_f32 v78, v78\n\t" \
        "v_cvt_u32_f32 v79, v79\n\t" \
        "s_or_b64 s[4:5], s[4:5], s[6:7]\n\t" \
        "v_mul_f32 v76, v74, v75\n\t" \
        "v_mul_f32 v74, v74, v73\n\t" \
        "s_and_b64 s[4:5], s[4:5], s[12:13]\n\t" \
        "s_cselect_b32 s54, 1, s54\n\t" \
        "v_mul_f32 v77, v72, v75\n\t" \
        "v_mul_f32 v75, v72, v73\n\t" \
        "v_cmp_neq_f32 s[4:5], 0, v73\n\t" \
        "v_cmp_neq_f32 s[6:7], 0, v72\n\t" \
        "v_cvt_pk_bf16_f32 v76, v76, v74\n\t" \
        "v_cvt_pk_bf16_f32 v77, v77, v75\n\t" \
        "v_mul_u32_u24 v80, 124, v78\n\t" \
        "v_add_u32 v83, 1, v79\n\t" \
        "v_mad_u32_u24 v80, v79, 6, v80\n\t" \
        "v_bfe_u32 v84, v79, 2, 1\n\t" \
        "v_bfe_u32 v83, v83, 2, 1\n\t" \
        "s_and_b64 s[50:51], s[4:5], s[6:7]\n\t" \
        "v_add3_u32 v81, v80, v84, s14\n\t" \
        "v_add3_u32 v82, v80, v83, s15\n\t" \
        "s_and_b64 s[50:51], s[50:51], s[12:13]\n\t" \
        "v_cndmask_b32 v83, 0, v36, s[6:7]\n\t" \
        "v_perm_b32 v74, v77, v76, v37\n\t" \
        "v_add_u32 v83, v81, v83\n\t" \
        "v_cndmask_b32 v68, v74, v76, s[4:5]\n\t" \
        "v_cndmask_b32 v83, v83, v82, s[4:5]\n\t" \
        "v_cndmask_b32 v75, 0, v38, s[50:51]\n\t" \
        "v_lshl_or_b32 v81, v83, 16, v81\n\t" \
        "v_cndmask_b32 v69, 0, v77, s[50:51]\n\t" \
        "v_or_b32 v81, v81, v75\n\t" \
        "ds_write_b64 v31, v[68:69]\n\t" \
        "v_cndmask_b32 v81, v29, v81, s[12:13]\n\t" \
        "ds_write_b32 v32, v81\n\t" \
        "s_lshl_b32 s55, s35, 2\n\t" \
        "s_cmp_lg_u32 s54, 0\n\t" \
        "v_mov_b32 v162, s55\n\t" \
        "s_cselect_b32 s55, 0x10000, s46\n\t" \
        "v_mov_b32 v163, s55\n\t" \
        "ds_write_b32 v162, v163\n\t" \
        "ds_read_u16 v120, v20\n\t" \
        "ds_read_u16 v121, v20 offset:8\n\t" \
        "ds_read_b32 v122, v21\n\t" \
        "v_mov_b32 v156, 0\n\t" \
        "s_mov_b32 s47, 0\n\t" \
        "s_waitcnt lgkmcnt(0)\n\t" \
        "v_lshlrev_b32 v116, 5, v120\n\t" \
        "v_bitop3_b32 v116, v116, s52, v23 bitop3:0x6a\n\t" \
        "v_xor_b32 v117, 8, v116\n\t" \
        "v_lshlrev_b32 v118, 5, v121\n\t" \
        "v_bitop3_b32 v118, v118, s52, v23 bitop3:0x6a\n\t" \
        "v_xor_b32 v119, 8, v118\n\t" \
        "v_and_b32 v112, v122, v25\n\t" \
        "v_and_b32 v113, v122, v26\n\t" \
        "v_and_b32 v114, v122, v27\n\t" \
        "v_and_b32 v115, v122, v28\n\t" \
        "ds_read_u16 v120, v20 offset:64\n\t" \
        "ds_read_u16 v121, v20 offset:72\n\t" \
        "ds_read_b32 v122, v21 offset:128\n\t" \
        "s_waitcnt vmcnt(6) lgkmcnt(0)\n\t" \
        "s_barrier\n\t" \
        "ds_read_b128 v[152:155], v156\n\t" \
        "ds_read_b64_tr_b16 v[64:65], v116 offset:0\n\t" \
        "ds_read_b64_tr_b16 v[66:67], v118 offset:0\n\t" \
        "ds_read_b64_tr_b16 v[68:69], v117 offset:0\n\t" \
        "ds_read_b64_tr_b16 v[70:71], v119 offset:0\n\t" \
        "ds_read_b64_tr_b16 v[72:73], v116 offset:64\n\t" \
        "ds_read_b64_tr_b16 v[74:75], v118 offset:64\n\t" \
        "ds_read_b64_tr_b16 v[76:77], v117 offset:64\n\t" \
        "ds_read_b64_tr_b16 v[78:79], v119 offset:64\n\t" \
        "ds_read_b64_tr_b16 v[80:81], v116 offset:128\n\t" \
        "ds_read_b64_tr_b16 v[82:83], v118 offset:128\n\t" \
        "ds_read_b64_tr_b16 v[84:85], v117 offset:128\n\t" \
        "ds_read_b64_tr_b16 v[86:87], v119 offset:128\n\t" \
        "s_waitcnt lgkmcnt(12)\n\t" \
        "v_add_u32 v152, v152, v154\n\t" \
        "v_add_u32 v153, v153, v155\n\t" \
        "v_max_u32 v152, v152, v153\n\t" \
        "s_nop 0\n\t" \
        "v_readfirstlane_b32 s55, v152\n\t" \
        "s_nop 0\n\t" \
        "s_cmp_gt_u32 s55, 56\n\t" \
        "s_cbranch_scc1 .Lfail%=\n\t" \
        "s_cmp_gt_u32 s46, 16\n\t" \
        "s_cbranch_scc1 .LHeprod%=\n\t" \
        ".LHelast%=:\n\t" \
        "s_waitcnt lgkmcnt(10)\n\t" \
        "v_mfma_f32_16x16x32_bf16 v[88:91], v[64:67], v[112:115], 0\n\t" \
        "s_waitcnt lgkmcnt(8)\n\t" \
        "v_mfma_f32_16x16x32_bf16 v[92:95], v[68:71], v[112:115], 0\n\t" \
        "s_waitcnt lgkmcnt(6)\n\t" \
        "v_mfma_f32_16x16x32_bf16 v[96:99], v[72:75], v[112:115], 0\n\t" \
        "s_waitcnt lgkmcnt(4)\n\t" \
        "v_mfma_f32_16x16x32_bf16 v[100:103], v[76:79], v[112:115], 0\n\t" \
        "s_waitcnt lgkmcnt(2)\n\t" \
        "v_mfma_f32_16x16x32_bf16 v[104:107], v[80:83], v[112:115], 0\n\t" \
        "s_waitcnt lgkmcnt(0)\n\t" \
        "v_mfma_f32_16x16x32_bf16 v[108:111], v[84:87], v[112:115], 0\n\t" \
        "s_and_b32 s55, s50, 0xffff\n\t" \
        "s_cbranch_scc1 .LHetwo2%=\n\t" \
        ".LHeback2%=:\n\t" \
        "s_sub_u32 s55, s46, s47\n\t" \
        "v_xor_b32 v156, 8, v22\n\t" \
        "v_cmp_gt_i32 vcc, s55, v24\n\t" \
        "s_mov_b64 exec, vcc\n\t" \
        "v_cvt_pk_bf16_f32 v88, v88, v89\n\t" \
        "v_cvt_pk_bf16_f32 v89, v90, v91\n\t" \
        "ds_write_b64 v22, v[88:89] offset:0\n\t" \
        "v_cvt_pk_bf16_f32 v92, v92, v93\n\t" \
        "v_cvt_pk_bf16_f32 v93, v94, v95\n\t" \
        "ds_write_b64 v156, v[92:93] offset:0\n\t" \
        "v_cvt_pk_bf16_f32 v96, v96, v97\n\t" \
        "v_cvt_pk_bf16_f32 v97, v98, v99\n\t" \
        "ds_write_b64 v22, v[96:97] offset:64\n\t" \
        "v_cvt_pk_bf16_f32 v100, v100, v101\n\t" \
        "v_cvt_pk_bf16_f32 v101, v102, v103\n\t" \
        "ds_write_b64 v156, v[100:101] offset:64\n\t" \
        "v_cvt_pk_bf16_f32 v104, v104, v105\n\t" \
        "v_cvt_pk_bf16_f32 v105, v106, v107\n\t" \
        "ds_write_b64 v22, v[104:105] offset:128\n\t" \
        "v_cvt_pk_bf16_f32 v108, v108, v109\n\t" \
        "v_cvt_pk_bf16_f32 v109, v110, v111\n\t" \
        "ds_write_b64 v156, v[108:109] offset:128\n\t" \
        "s_mov_b64 exec, -1\n\t" \
        "s_waitcnt lgkmcnt(0)\n\t" \
        "s_nop 7\n\t" \
        "s_branch .LH_join%=\n\t" \
        ".LHeprod%=:\n\t" \
        "s_waitcnt lgkmcnt(10)\n\t" \
        "v_mfma_f32_16x16x32_bf16 v[88:91], v[64:67], v[112:115], 0\n\t" \
        "v_lshlrev_b32 v116, 5, v120\n\t" \
        "v_bitop3_b32 v116, v116, s52, v23 bitop3:0x6a\n\t" \
        "v_xor_b32 v117, 8, v116\n\t" \
        "v_lshlrev_b32 v118, 5, v121\n\t" \
        "v_bitop3_b32 v118, v118, s52, v23 bitop3:0x6a\n\t" \
        "v_xor_b32 v119, 8, v118\n\t" \
        "ds_read_u16 v120, v20 offset:128\n\t" \
        "ds_read_u16 v121, v20 offset:136\n\t" \
        "ds_read_b64_tr_b16 v[64:65], v116 offset:0\n\t" \
        "ds_read_b64_tr_b16 v[66:67], v118 offset:0\n\t" \
        "s_waitcnt lgkmcnt(12)\n\t" \
        "v_mfma_f32_16x16x32_bf16 v[92:95], v[68:71], v[112:115], 0\n\t" \
        "ds_read_b64_tr_b16 v[68:69], v117 offset:0\n\t" \
        "ds_read_b64_tr_b16 v[70:71], v119 offset:0\n\t" \
        "s_waitcnt lgkmcnt(12)\n\t" \
        "v_mfma_f32_16x16x32_bf16 v[96:99], v[72:75], v[112:115], 0\n\t" \
        "ds_read_b64_tr_b16 v[72:73], v116 offset:64\n\t" \
        "ds_read_b64_tr_b16 v[74:75], v118 offset:64\n\t" \
        "s_waitcnt lgkmcnt(12)\n\t" \
        "v_mfma_f32_16x16x32_bf16 v[100:103], v[76:79], v[112:115], 0\n\t" \
        "ds_read_b64_tr_b16 v[76:77], v117 offset:64\n\t" \
        "ds_read_b64_tr_b16 v[78:79], v119 offset:64\n\t" \
        "s_waitcnt lgkmcnt(12)\n\t" \
        "v_mfma_f32_16x16x32_bf16 v[104:107], v[80:83], v[112:115], 0\n\t" \
        "ds_read_b64_tr_b16 v[80:81], v116 offset:128\n\t" \
        "ds_read_b64_tr_b16 v[82:83], v118 offset:128\n\t" \
        "s_waitcnt lgkmcnt(12)\n\t" \
        "v_mfma_f32_16x16x32_bf16 v[108:111], v[84:87], v[112:115], 0\n\t" \
        "ds_read_b64_tr_b16 v[84:85], v117 offset:128\n\t" \
        "ds_read_b64_tr_b16 v[86:87], v119 offset:128\n\t" \
        "v_and_b32 v112, v122, v25\n\t" \
        "v_and_b32 v113, v122, v26\n\t" \
        "v_and_b32 v114, v122, v27\n\t" \
        "v_and_b32 v115, v122, v28\n\t" \
        "ds_read_b32 v122, v21 offset:256\n\t" \
        "s_and_b32 s55, s50, 0xffff\n\t" \
        "s_cbranch_scc1 .LHetwo1%=\n\t" \
        ".LHeback1%=:\n\t" \
        "s_sub_u32 s55, s46, s47\n\t" \
        "v_xor_b32 v156, 8, v22\n\t" \
        "v_cmp_gt_i32 vcc, s55, v24\n\t" \
        "s_mov_b64 exec, vcc\n\t" \
        "v_cvt_pk_bf16_f32 v88, v88, v89\n\t" \
        "v_cvt_pk_bf16_f32 v89, v90, v91\n\t" \
        "ds_write_b64 v22, v[88:89] offset:0\n\t" \
        "v_cvt_pk_bf16_f32 v92, v92, v93\n\t" \
        "v_cvt_pk_bf16_f32 v93, v94, v95\n\t" \
        "ds_write_b64 v156, v[92:93] offset:0\n\t" \
        "v_cvt_pk_bf16_f32 v96, v96, v97\n\t" \
        "v_cvt_pk_bf16_f32 v97, v98, v99\n\t" \
        "ds_write_b64 v22, v[96:97] offset:64\n\t" \
        "v_cvt_pk_bf16_f32 v100, v100, v101\n\t" \
        "v_cvt_pk_bf16_f32 v101, v102, v103\n\t" \
        "ds_write_b64 v156, v[100:101] offset:64\n\t" \
        "v_cvt_pk_bf16_f32 v104, v104, v105\n\t" \
        "v_cvt_pk_bf16_f32 v105, v106, v107\n\t" \
        "ds_write_b64 v22, v[104:105] offset:128\n\t" \
        "v_cvt_pk_bf16_f32 v108, v108, v109\n\t" \
        "v_cvt_pk_bf16_f32 v109, v110, v111\n\t" \
        "ds_write_b64 v156, v[108:109] offset:128\n\t" \
        "s_mov_b64 exec, -1\n\t" \
        "s_lshl_b32 s55, s53, 4\n\t" \
        "s_add_u32 s47, s47, 16\n\t" \
        "v_add_u32 v22, s55, v22\n\t" \
        "s_lshr_b64 s[50:51], s[50:51], 16\n\t" \
        "v_add_u32 v20, 64, v20\n\t" \
        "v_add_u32 v21, 0x80, v21\n\t" \
        "s_add_u32 s55, s47, 16\n\t" \
        "s_cmp_lt_u32 s55, s46\n\t" \
        "s_cbranch_scc1 .LHeprod%=\n\t" \
        "s_branch .LHelast%=\n\t" \
        ".LHetwo1%=:\n\t" \
        "s_nop 4\n\t" \
        "ds_read_u16 v156, v20\n\t" \
        "ds_read_u16 v158, v20 offset:8\n\t" \
        "ds_read_b32 v127, v21 offset:4\n\t" \
        "s_waitcnt lgkmcnt(0)\n\t" \
        "v_lshrrev_b32 v124, 15, v156\n\t" \
        "v_lshlrev_b32 v156, 5, v156\n\t" \
        "v_bitop3_b32 v156, v156, s52, v23 bitop3:0x6a\n\t" \
        "v_mul_u32_u24 v124, 3968, v124\n\t" \
        "v_add_u32 v156, v156, v124\n\t" \
        "v_xor_b32 v157, 8, v156\n\t" \
        "v_lshrrev_b32 v125, 15, v158\n\t" \
        "v_lshlrev_b32 v158, 5, v158\n\t" \
        "v_bitop3_b32 v158, v158, s52, v23 bitop3:0x6a\n\t" \
        "v_mul_u32_u24 v125, 3968, v125\n\t" \
        "v_add_u32 v158, v158, v125\n\t" \
        "v_xor_b32 v159, 8, v158\n\t" \
        "ds_read_b64_tr_b16 v[128:129], v156 offset:0\n\t" \
        "ds_read_b64_tr_b16 v[130:131], v158 offset:0\n\t" \
        "ds_read_b64_tr_b16 v[132:133], v157 offset:0\n\t" \
        "ds_read_b64_tr_b16 v[134:135], v159 offset:0\n\t" \
        "ds_read_b64_tr_b16 v[136:137], v156 offset:64\n\t" \
        "ds_read_b64_tr_b16 v[138:139], v158 offset:64\n\t" \
        "ds_read_b64_tr_b16 v[140:141], v157 offset:64\n\t" \
        "ds_read_b64_tr_b16 v[142:143], v159 offset:64\n\t" \
        "ds_read_b64_tr_b16 v[144:145], v156 offset:128\n\t" \
        "ds_read_b64_tr_b16 v[146:147], v158 offset:128\n\t" \
        "ds_read_b64_tr_b16 v[148:149], v157 offset:128\n\t" \
        "ds_read_b64_tr_b16 v[150:151], v159 offset:128\n\t" \
        "v_and_b32 v124, v127, v25\n\t" \
        "v_and_b32 v125, v127, v26\n\t" \
        "v_and_b32 v126, v127, v27\n\t" \
        "v_and_b32 v127, v127, v28\n\t" \
        "s_nop 1\n\t" \
        "s_waitcnt lgkmcnt(10)\n\t" \
        "v_mfma_f32_16x16x32_bf16 v[88:91], v[128:131], v[124:127], v[88:91]\n\t" \
        "s_waitcnt lgkmcnt(8)\n\t" \
        "v_mfma_f32_16x16x32_bf16 v[92:95], v[132:135], v[124:127], v[92:95]\n\t" \
        "s_waitcnt lgkmcnt(6)\n\t" \
        "v_mfma_f32_16x16x32_bf16 v[96:99], v[136:139], v[124:127], v[96:99]\n\t" \
        "s_waitcnt lgkmcnt(4)\n\t" \
        "v_mfma_f32_16x16x32_bf16 v[100:103], v[140:143], v[124:127], v[100:103]\n\t" \
        "s_waitcnt lgkmcnt(2)\n\t" \
        "v_mfma_f32_16x16x32_bf16 v[104:107], v[144:147], v[124:127], v[104:107]\n\t" \
        "s_waitcnt lgkmcnt(0)\n\t" \
        "v_mfma_f32_16x16x32_bf16 v[108:111], v[148:151], v[124:127], v[108:111]\n\t" \
        "s_nop 7\n\t" \
        "s_branch .LHeback1%=\n\t" \
        ".LHetwo2%=:\n\t" \
        "s_nop 4\n\t" \
        "ds_read_u16 v156, v20\n\t" \
        "ds_read_u16 v158, v20 offset:8\n\t" \
        "ds_read_b32 v127, v21 offset:4\n\t" \
        "s_waitcnt lgkmcnt(0)\n\t" \
        "v_lshrrev_b32 v124, 15, v156\n\t" \
        "v_lshlrev_b32 v156, 5, v156\n\t" \
        "v_bitop3_b32 v156, v156, s52, v23 bitop3:0x6a\n\t" \
        "v_mul_u32_u24 v124, 3968, v124\n\t" \
        "v_add_u32 v156, v156, v124\n\t" \
        "v_xor_b32 v157, 8, v156\n\t" \
        "v_lshrrev_b32 v125, 15, v158\n\t" \
        "v_lshlrev_b32 v158, 5, v158\n\t" \
        "v_bitop3_b32 v158, v158, s52, v23 bitop3:0x6a\n\t" \
        "v_mul_u32_u24 v125, 3968, v125\n\t" \
        "v_add_u32 v158, v158, v125\n\t" \
        "v_xor_b32 v159, 8, v158\n\t" \
        "ds_read_b64_tr_b16 v[128:129], v156 offset:0\n\t" \
        "ds_read_b64_tr_b16 v[130:131], v158 offset:0\n\t" \
        "ds_read_b64_tr_b16 v[132:133], v157 offset:0\n\t" \
        "ds_read_b64_tr_b16 v[134:135], v159 offset:0\n\t" \
        "ds_read_b64_tr_b16 v[136:137], v156 offset:64\n\t" \
        "ds_read_b64_tr_b16 v[138:139], v158 offset:64\n\t" \
        "ds_read_b64_tr_b16 v[140:141], v157 offset:64\n\t" \
        "ds_read_b64_tr_b16 v[142:143], v159 offset:64\n\t" \
        "ds_read_b64_tr_b16 v[144:145], v156 offset:128\n\t" \
        "ds_read_b64_tr_b16 v[146:147], v158 offset:128\n\t" \
        "ds_read_b64_tr_b16 v[148:149], v157 offset:128\n\t" \
        "ds_read_b64_tr_b16 v[150:151], v159 offset:128\n\t" \
        "v_and_b32 v124, v127, v25\n\t" \
        "v_and_b32 v125, v127, v26\n\t" \
        "v_and_b32 v126, v127, v27\n\t" \
        "v_and_b32 v127, v127, v28\n\t" \
        "s_nop 1\n\t" \
        "s_waitcnt lgkmcnt(10)\n\t" \
        "v_mfma_f32_16x16x32_bf16 v[88:91], v[128:131], v[124:127], v[88:91]\n\t" \
        "s_waitcnt lgkmcnt(8)\n\t" \
        "v_mfma_f32_16x16x32_bf16 v[92:95], v[132:135], v[124:127], v[92:95]\n\t" \
        "s_waitcnt lgkmcnt(6)\n\t" \
        "v_mfma_f32_16x16x32_bf16 v[96:99], v[136:139], v[124:127], v[96:99]\n\t" \
        "s_waitcnt lgkmcnt(4)\n\t" \
        "v_mfma_f32_16x16x32_bf16 v[100:103], v[140:143], v[124:127], v[100:103]\n\t" \
        "s_waitcnt lgkmcnt(2)\n\t" \
        "v_mfma_f32_16x16x32_bf16 v[104:107], v[144:147], v[124:127], v[104:107]\n\t" \
        "s_waitcnt lgkmcnt(0)\n\t" \
        "v_mfma_f32_16x16x32_bf16 v[108:111], v[148:151], v[124:127], v[108:111]\n\t" \
        "s_nop 7\n\t" \
        "s_branch .LHeback2%=\n\t" \
        ".Lpf1%=:\n\t" \
        ".Lfail%=:\n\t" \
        "s_waitcnt vmcnt(0) lgkmcnt(0)\n\t" \
        "s_nop 7\n\t" \
        "s_nop 7\n\t" \
        "s_add_u32 s34, s34, s3\n\t" \
        "s_mov_b32 s41, 1\n\t" \
        ".Lexit%=:\n\t"
#define DCN_ASM_CLOBBERS "s3", "s4", "s5", "s6", "s7", "s8", "s9", "s10", "s11", "s12", "s13", "s14", "s15", "s36", "s37", "s38", "s39", "s40", "s42", "s43", "s44", "s45", "s46", "s47", "s48", "s49", "s50", "s51", "s52", "s53", "s54", "s55", "s56", "s57", "s58", "s59", "s60", "s61", "s62", "s63", "s64", "s65", "s66", "s67", "s68", "s69", "s70", "s71", "s72", "s73", "s74", "s75", "s76", "s77", "s78", "s79", "s80", "s81", "s82", "s83", "s84", "s85", "s86", "s87", "s88", "s89", "s90", "s91", "s92", "s93", "s94", "s95", "s96", "s97", "s98", "s99", "v2", "v3", "v4", "v5", "v6", "v7", "v8", "v9", "v10", "v11", "v12", "v13", "v14", "v15", "v16", "v17", "v18", "v19", "v20", "v21", "v22", "v23", "v24", "v25", "v26", "v27", "v28", "v29", "v30", "v31", "v32", "v33", "v34", "v35", "v36", "v37", "v38", "v39", "v40", "v41", "v42", "v43", "v44", "v45", "v46", "v47", "v48", "v49", "v50", "v51", "v52", "v53", "v54", "v55", "v56", "v57", "v58", "v59", "v60", "v61", "v62", "v63", "v64", "v65", "v66", "v67", "v68", "v69", "v70", "v71", "v72", "v73", "v74", "v75", "v76", "v77", "v78", "v79", "v80", "v81", "v82", "v83", "v84", "v85", "v86", "v87", "v88", "v89", "v90", "v91", "v92", "v93", "v94", "v95", "v96", "v97", "v98", "v99", "v100", "v101", "v102", "v103", "v104", "v105", "v106", "v107", "v108", "v109", "v110", "v111", "v112", "v113", "v114", "v115", "v116", "v117", "v118", "v119", "v120", "v121", "v122", "v123", "v124", "v125", "v126", "v127", "v128", "v129", "v130", "v131", "v132", "v133", "v134", "v135", "v136", "v137", "v138", "v139", "v140", "v141", "v142", "v143", "v144", "v145", "v146", "v147", "v148", "v149", "v150", "v151", "v152", "v153", "v154", "v155", "v156", "v157", "v158", "v159", "v160", "v161", "v162", "v163", "v164", "v165", "v166", "v167", "v168", "v169", "v170", "v171", "v172", "v173", "v174", "v175", "v176", "v177", "v178", "v179", "v180", "v181", "v182", "v183", "v184", "v185", "v186", "v187", "v188", "v189", "v190", "v191", "v192", "v193", "v194", "v195", "v196", "v197", "v198", "v199", "v200", "v201", "v202", "v203", "v204", "v205", "v206", "v207", "v208", "v209", "v210", "v211", "v212", "v213", "v214", "v215", "v216", "v217", "v218", "v219", "v220", "v221", "v222", "v223", "v224", "v225", "v226", "v227", "v228", "v229", "v230", "v231", "v232", "v233", "v234", "v235", "v236", "v237", "v238", "v239", "v240", "v241", "v242", "v243", "v244", "v245", "v246", "v247", "v248", "v249", "v250", "v251", "v252", "v253", "v254", "v255", "a0", "a1", "a2", "a3", "a4", "a5", "a6", "a7", "a8", "a9", "a10", "a11", "a12", "a13", "a14", "a15", "a16", "a17", "a18", "a19", "a20", "a21", "a22", "a23", "a24", "a25", "a26", "a27", "a28", "a29", "a30", "a31", "a32", "a33", "a34", "a35", "a36", "a37", "a38", "a39", "a40", "a41", "a42", "a43", "a44", "a45", "a46", "a47", "a48", "a49", "a50", "a51", "a52", "a53", "a54", "a55", "a56", "a57", "a58", "a59", "a60", "a61", "a62", "a63", "a64", "a65", "a66", "a67", "a68", "a69", "a70", "a71", "a72", "a73", "a74", "a75", "a76", "a77", "a78", "a79", "a80", "a81", "a82", "a83", "a84", "a85", "a86", "a87", "a88", "a89", "a90", "a91", "a92", "a93", "a94", "a95", "a96", "a97", "a98", "a99", "a100", "a101", "a102", "a103", "a104", "a105", "a106", "a107", "a108", "a109", "a110", "a111", "a112", "a113", "a114", "a115", "a116", "a117", "a118", "a119", "a120", "a121", "a122", "a123", "a124", "a125", "a126", "a127", "a128", "a129", "a130", "a131", "a132", "a133", "a134", "a135", "a136", "a137", "a138", "a139", "a140", "a141", "a142", "a143", "a144", "a145", "a146", "a147", "a148", "a149", "a150", "a151", "a152", "a153", "a154", "a155", "a156", "a157", "a158", "a159", "a160", "a161", "a162", "a163", "a164", "a165", "a166", "a167", "a168", "a169", "a170", "a171", "a172", "a173", "a174", "a175", "a176", "a177", "a178", "a179", "a180", "a181", "a182", "a183", "a184", "a185", "a186", "a187", "a188", "a189", "a190", "a191", "a192", "a193", "a194", "a195", "a196", "a197", "a198", "a199", "a200", "a201", "a202", "a203", "a204", "a205", "a206", "a207", "a208", "a209", "a210", "a211", "a212", "a213", "a214", "a215", "a216", "a217", "a218", "a219", "a220", "a221", "a222", "a223", "a224", "a225", "a226", "a227", "a228", "a229", "a230", "a231", "a232", "a233", "a234", "a235", "a236", "a237", "a238", "a239", "a240", "a241", "a242", "a243", "a244", "a245", "a246", "a247", "a248", "a249", "a250", "a251", "a252", "a253", "a254", "a255"
#define NWG 255
#define CYC 1

__device__ __forceinline__ float bf_lo(unsigned u) { return __uint_as_float(u << 16); }
__device__ __forceinline__ float bf_hi(unsigned u) { return __uint_as_float(u & 0xffff0000u); }
__device__ __forceinline__ unsigned cvt_pk(float lo, float hi) {
    bf16x2 r = {(__bf16)lo, (__bf16)hi};
    unsigned u;
    __builtin_memcpy(&u, &r, 4);
    return u;
}
__device__ __forceinline__ f32x4 mfma16(u32x4 a, u32x4 b, f32x4 c) {
    bf16x8 A, B;
    __builtin_memcpy(&A, &a, 16);
    __builtin_memcpy(&B, &b, 16);
    return __builtin_amdgcn_mfma_f32_16x16x32_bf16(A, B, c, 0, 0, 0);
}
__device__ __forceinline__ u32x2 tr_read(unsigned a) {
    s16x4 v = __builtin_amdgcn_ds_read_tr16_b64_v4i16((lds_s16x4*)(uintptr_t)a);
    u32x2 u;
    __builtin_memcpy(&u, &v, 8);
    return u;
}
__device__ __forceinline__ unsigned clip_pk(unsigned v) {
    typedef __attribute__((ext_vector_type(2))) short i16x2;
    i16x2 r;
    __builtin_memcpy(&r, &v, 4);
    r = __builtin_elementwise_min(__builtin_elementwise_max(r, (i16x2){0, 0}), (i16x2){0x435f, 0x435f});
    __builtin_memcpy(&v, &r, 4);
    return v;
}

__device__ __forceinline__ void slow_tile(const unsigned short* x, const unsigned short* off, const unsigned short* wgt, unsigned short* out,
                                          const int wv, const unsigned lane, const int y0, const int x0, const int nrows, const unsigned lb) {
    const unsigned px = lane & 15, q = lane >> 4;
#pragma unroll 1
    for (int r0 = 0; r0 < nrows; r0 += 4) {
        const int row = y0 + min(r0 + wv, nrows - 1), col = x0 + px;
        f32x4 acc[6];
#pragma unroll
        for (int i = 0; i < 6; ++i) acc[i] = (f32x4){0.f, 0.f, 0.f, 0.f};
#pragma unroll 1
        for (int t = 0; t < 9; ++t) {
            asm volatile("s_waitcnt vmcnt(0) lgkmcnt(0)\n\ts_barrier" ::: "memory");
            for (unsigned d = wv * 64 + lane; d < WTAP / 16; d += NTHREADS)
                *(lds_u32x4*)(uintptr_t)(lb + d * 16) = *(const glb_u32x4*)((uintptr_t)wgt + t * WTAP + d * 16);
            const unsigned od = *(const glb_u32*)((uintptr_t)off + (row * IMG + col) * 36 + t * 4);
            const unsigned pc = clip_pk(cvt_pk((float)(row + t / 3 - 1) + bf_lo(od), (float)(col + t % 3 - 1) + bf_hi(od)));
            const float py = bf_lo(pc), pxf = bf_hi(pc);
            const int gy = (int)py, gx = (int)pxf;
            const float wy = py - (float)gy, wx = pxf - (float)gx;
            const unsigned ab = cvt_pk(1.f - wy, 1.f - wx);
            const float a = bf_lo(ab), b = bf_hi(ab);
            const unsigned w01 = cvt_pk(a * b, a * wx), w23 = cvt_pk(wy * b, wy * wx);
            const unsigned o00 = (gy * IMG + gx) * (CH * 2) + q * 16;
            const unsigned dx = gx < IMG - 1 ? CH * 2 : 0, dy = gy < IMG - 1 ? ROWB : 0;
            const float wc[4] = {bf_lo(w01), bf_hi(w01), bf_lo(w23), bf_hi(w23)};
            u32x4 bfr[3];
#pragma unroll
            for (int j = 0; j < 3; ++j) {
                float vv[8];
#pragma unroll
                for (int c = 0; c < 4; ++c) {
                    const u32x4 gv = *(const glb_u32x4*)((uintptr_t)x + (o00 + ((c & 1) ? dx : 0u) + ((c & 2) ? dy : 0u) + j * 64));
#pragma unroll
                    for (int k = 0; k < 4; ++k) {
                        vv[2 * k] = c ? fmaf(wc[c], bf_lo(gv[k]), vv[2 * k]) : wc[0] * bf_lo(gv[k]);
                        vv[2 * k + 1] = c ? fmaf(wc[c], bf_hi(gv[k]), vv[2 * k + 1]) : wc[0] * bf_hi(gv[k]);
                    }
                }
                bfr[j] = (u32x4){cvt_pk(vv[0], vv[1]), cvt_pk(vv[2], vv[3]), cvt_pk(vv[4], vv[5]), cvt_pk(vv[6], vv[7])};
            }
            asm volatile("s_waitcnt vmcnt(0) lgkmcnt(0)\n\ts_barrier" ::: "memory");
            const unsigned wa = lb + (8 * q + ((lane >> 2) & 3)) * 192 + (lane & 3) * 8;
#pragma unroll
            for (int j = 0; j < 3; ++j)
#pragma unroll
                for (int i = 0; i < 6; ++i) {
                    const u32x2 lo = tr_read(wa + j * 6144 + i * 32), hi = tr_read(wa + j * 6144 + i * 32 + 768);
                    acc[i] = mfma16((u32x4){lo[0], lo[1], hi[0], hi[1]}, bfr[j], acc[i]);
                }
        }
        if (r0 + wv < nrows) {
#pragma unroll
            for (int i = 0; i < 6; ++i)
                *(glb_u32x2*)((uintptr_t)out + (row * IMG + col) * (CH * 2) + i * 32 + q * 8) =
                    (u32x2){cvt_pk(acc[i][0], acc[i][1]), cvt_pk(acc[i][2], acc[i][3])};
        }
    }
}

__global__ __launch_bounds__(NTHREADS, 1) void dcn_kernel(const unsigned short* x_, const unsigned short* off_,
                                                          const unsigned short* wgt_, unsigned short* out_) {
    __shared__ __attribute__((aligned(64))) unsigned char lds[LDS_BYTES];
    u32x4 xd, od, wd, odsc;
    unsigned y0, x0, rows, wv, status, stv;
    asm volatile(DCN_ASM_TEXT
                 : "={s[16:19]}"(xd), "={s[20:23]}"(od), "={s[24:27]}"(wd), "={s[28:31]}"(odsc),
                   "={s32}"(y0), "={s33}"(x0), "={s34}"(rows), "={s35}"(wv), "={s41}"(status), "={v1}"(stv)
                 : "{s[0:1]}"(__builtin_amdgcn_kernarg_segment_ptr()), "{s2}"(blockIdx.x), "{v0}"(threadIdx.x)
                 : "memory", "scc", "vcc", DCN_ASM_CLOBBERS);
    unsigned lane;
    asm volatile("v_mbcnt_lo_u32_b32 %0, -1, 0\n\tv_mbcnt_hi_u32_b32 %0, -1, %0" : "=v"(lane));
    slow_tile((const unsigned short*)(((unsigned long long)xd[1] << 32) | xd[0]), (const unsigned short*)(((unsigned long long)od[1] << 32) | od[0]),
              (const unsigned short*)(((unsigned long long)wd[1] << 32) | wd[0]), (unsigned short*)(((unsigned long long)odsc[1] << 32) | odsc[0]),
              wv, lane, y0, x0, rows, (unsigned)(uintptr_t)(__attribute__((address_space(3))) unsigned char*)lds);
}

extern "C" void kernel_launch(void* const* d_in, const int* in_sizes, int n_in,
                              void* d_out, int out_size, void* d_ws, size_t ws_size,
                              hipStream_t stream) {
    dcn_kernel<<<NWG, NTHREADS, 0, stream>>>((const unsigned short*)d_in[0], (const unsigned short*)d_in[1],
                                             (const unsigned short*)d_in[2], (unsigned short*)d_out);
}
```
